```python
import torch

def _softmax(x, axis=-1):
    return torch.softmax(x, dim=axis)

def _gelu(x):
    return torch.nn.functional.gelu(x, approximate='tanh')

def _silu(x):
    return torch.nn.functional.silu(x)

def _sigmoid(x):
    return torch.sigmoid(x)

def _layernorm(x, axis=-1, eps=1e-05):
    m = x.mean(dim=axis, keepdim=True)
    v = x.var(dim=axis, keepdim=True, unbiased=False)
    return (x - m) / torch.sqrt(v + eps)

def _scatter_add(tgt, idx, src, axis=0):
    return tgt.clone().index_add_(axis, idx, src)

def _segment_sum(src, seg, n):
    out = torch.zeros((n,) + src.shape[1:], dtype=src.dtype)
    return out.index_add_(0, seg, src)

def _pad(x, pw):
    flat = tuple((v for pair in reversed(pw) for v in pair))
    return torch.nn.functional.pad(x, flat)
kernel_name = 'concat_heads__ssm_conv_s4d__memory_aug_recurrent_mem_tr__prompt'

def setup_inputs():

    _w = lambda a: (lambda t: t.to(torch.bfloat16) if t.is_floating_point() else t)(torch.as_tensor(a))

    def _A_setup():
        Bsz, L, D, N = (1, 2048, 448, 256)
        return {'u': _w((lambda *s: torch.randn(*s, dtype=torch.bfloat16) * 0.05)(Bsz, L, D)), 'log_A_real': _w((lambda *s: torch.randn(*s, dtype=torch.bfloat16) * 6.4)(D, N) * 0.5), 'A_imag': _w((lambda *s: torch.randn(*s, dtype=torch.bfloat16) * 0.05)(D, N) * 3.14), 'C_real': _w((lambda *s: torch.randn(*s, dtype=torch.bfloat16) * 0.2)(D, N) / N ** 0.5), 'C_imag': _w((lambda *s: torch.randn(*s, dtype=torch.bfloat16) * 0.2)(D, N) / N ** 0.5), 'log_dt': _w((lambda *s: torch.randn(*s, dtype=torch.bfloat16) * 0.05)(D) * 0.5 - 4.0), 'D_skip': _w((lambda *s: torch.randn(*s, dtype=torch.bfloat16) * 0.05)(D))}
    _A_in = _A_setup()

    def _B_setup():
        B, S, Dm = (1, 2048, 448)
        H, Dh = (16, 256)
        M, seg = (8, 32)
        seg = min(seg, S)
        S = S // seg * seg
        return {'x': _w((lambda *s: torch.randn(*s, dtype=torch.bfloat16) * 0.05)(B, S, Dm)), 'mem0': _w((lambda *s: torch.randn(*s, dtype=torch.bfloat16) * 0.4)(B, M, Dm)), 'Wq': _w((lambda *s: torch.randn(*s, dtype=torch.bfloat16) * 6.4)(Dm, H, Dh) * Dm ** (-0.5)), 'Wk': _w((lambda *s: torch.randn(*s, dtype=torch.bfloat16) * 6.4)(Dm, H, Dh) * Dm ** (-0.5)), 'Wv': _w((lambda *s: torch.randn(*s, dtype=torch.bfloat16) * 0.4)(Dm, H, Dh) * Dm ** (-0.5)), 'Wo': _w((lambda *s: torch.randn(*s, dtype=torch.bfloat16) * 0.4)(H, Dh, Dm) * (H * Dh) ** (-0.5)), 'seg_len': seg}
    _B_in = _B_setup()
    return {'A_u': _A_in['u'], 'A_log_A_real': _A_in['log_A_real'], 'A_A_imag': _A_in['A_imag'], 'A_C_real': _A_in['C_real'], 'A_C_imag': _A_in['C_imag'], 'A_log_dt': _A_in['log_dt'], 'A_D_skip': _A_in['D_skip'], 'B_x': _B_in['x'], 'B_mem0': _B_in['mem0'], 'B_Wq': _B_in['Wq'], 'B_Wk': _B_in['Wk'], 'B_Wv': _B_in['Wv'], 'B_Wo': _B_in['Wo'], 'B_seg_len': _B_in['seg_len']}

def reference(A_u, A_log_A_real, A_A_imag, A_C_real, A_C_imag, A_log_dt, A_D_skip, B_x, B_mem0, B_Wq, B_Wk, B_Wv, B_Wo, B_seg_len):

    def _A_ref(u, log_A_real, A_imag, C_real, C_imag, log_dt, D_skip):
        L = u.shape[1]
        dt = torch.exp(log_dt)
        A = -torch.exp(log_A_real) + 1j * A_imag
        C = (C_real + 1j * C_imag) * dt[:, None]
        pos = (lambda x, dt: x.to(dt))(torch.arange(L), log_dt.dtype)
        P = torch.exp((A * dt[:, None])[:, :, None] * pos[None, None, :])
        K = 2.0 * torch.einsum('dn,dnl->dl', C, P).real
        L2 = 2 * L
        u_t = (lambda x, *d: x.permute(*d))(u, 0, 2, 1)
        Kf = (lambda x, *a, **k: torch.fft.rfft(x.float(), *a, **k))(K, L2)
        Uf = (lambda x, *a, **k: torch.fft.rfft(x.float(), *a, **k))(u_t, L2)
        y = (lambda x, *a, **k: torch.fft.irfft(x, *a, **k).to(torch.bfloat16))(Uf * Kf[None, :, :], L2)[:, :, :L]
        y = (lambda x, *d: x.permute(*d))(y, 0, 2, 1)
        return y + u * D_skip[None, None, :]

    def _B_ref(x, mem0, Wq, Wk, Wv, Wo, seg_len):
        B, S, Dm = x.shape
        H, Dh = (Wq.shape[1], Wq.shape[2])
        M = mem0.shape[1]
        scale = Dh ** (-0.5)
        n_seg = S // seg_len
        L = M + seg_len
        idx = torch.arange(L)
        mask = (idx[:, None] < M) | (idx[:, None] >= idx[None, :])
        mem = mem0
        outs = []
        for s in range(n_seg):
            xs = x[:, s * seg_len:(s + 1) * seg_len, :].clone()
            h = torch.concatenate([mem, xs], axis=1)
            q = torch.einsum('btd,dhe->bhte', h, Wq)
            k = torch.einsum('btd,dhe->bhte', h, Wk)
            v = torch.einsum('btd,dhe->bhte', h, Wv)
            scores = torch.einsum('bhqe,bhke->bhqk', q, k) * scale
            scores = torch.where(mask[None, None], scores, -1000000000.0)
            p = _softmax(scores, axis=-1)
            o = torch.einsum('bhqk,bhke->bhqe', p, v)
            o = torch.einsum('bhte,hed->btd', o, Wo)
            mem = o[:, :M, :].clone()
            outs.append(o[:, M:, :])
        return torch.concatenate(outs, axis=1)
    a = _A_ref(A_u, A_log_A_real, A_A_imag, A_C_real, A_C_imag, A_log_dt, A_D_skip)
    b = _B_ref(B_x, B_mem0, B_Wq, B_Wk, B_Wv, B_Wo, B_seg_len)
    return torch.concatenate([a, b], axis=-1)
```

```cpp
#include <hip/hip_runtime.h>
#include <stdint.h>
typedef unsigned short u16;
typedef unsigned long long u64;
typedef __attribute__((ext_vector_type(8))) __bf16 bf8;
typedef __attribute__((ext_vector_type(16))) __bf16 bf16v;
typedef __attribute__((ext_vector_type(2))) __bf16 bf2;
typedef __attribute__((ext_vector_type(2))) float f2;
typedef __attribute__((ext_vector_type(4))) float f4;
typedef __attribute__((ext_vector_type(4))) unsigned u4;
typedef __attribute__((ext_vector_type(2))) unsigned u2;
typedef __attribute__((ext_vector_type(8))) unsigned u8v;
typedef __attribute__((ext_vector_type(4))) short s4;
#define LAS __attribute__((address_space(3)))
#define DI __device__ __forceinline__
#define SB() __builtin_amdgcn_sched_barrier(0)

DI unsigned pk2(float a, float b) { f2 v = {a, b}; return __builtin_bit_cast(unsigned, __builtin_convertvector(v, bf2)); }
DI float bfr(float a) { return __uint_as_float(pk2(a, a) & 0xffff0000u); }
DI unsigned san(unsigned x) { return x < 0xFFFFFFFEu ? x : 0xFFFFFFFEu; }
DI void bar() { asm volatile("s_waitcnt lgkmcnt(0)\n\ts_barrier" ::: "memory"); SB(); }
DI float x32max(float a) { auto r = __builtin_amdgcn_permlane32_swap(__float_as_uint(a), __float_as_uint(a), false, false); return fmaxf(__uint_as_float(r[0]), __uint_as_float(r[1])); }
DI float x32add(float a) { auto r = __builtin_amdgcn_permlane32_swap(__float_as_uint(a), __float_as_uint(a), false, false); return __uint_as_float(r[0]) + __uint_as_float(r[1]); }
DI u2 trread(const char LAS* p) { return __builtin_bit_cast(u2, __builtin_amdgcn_ds_read_tr16_b64_v4i16((LAS s4*)p)); }
DI u4 mksrd(const void* p, unsigned bytes) { u64 x = (u64)p; return (u4){(unsigned)x, (unsigned)(x >> 32) & 0xffffu, bytes, 0x00020000u}; }
DI u4 ld128(u4 srd, int voff, int soff) { u4 v; asm volatile("buffer_load_dwordx4 %0, %1, %2, %3 offen sc1" : "=v"(v) : "v"(voff), "s"(srd), "s"(soff) : "memory"); return v; }
DI unsigned ld32(u4 srd, int voff, int soff) { unsigned v; asm volatile("buffer_load_dword %0, %1, %2, %3 offen sc1" : "=v"(v) : "v"(voff), "s"(srd), "s"(soff) : "memory"); return v; }
DI void st128(u4 v, u4 srd, int voff, int soff) { asm volatile("buffer_store_dwordx4 %0, %1, %2, %3 offen sc1\n\ts_nop 1" :: "v"(v), "v"(voff), "s"(srd), "s"(soff) : "memory"); }
DI void st32(unsigned v, u4 srd, int voff, int soff) { asm volatile("buffer_store_dword %0, %1, %2, %3 offen sc1" :: "v"(v), "v"(voff), "s"(srd), "s"(soff) : "memory"); }
#define PIN(x) asm volatile("" : "+v"(x))
#define WAITV(n, ...) asm volatile("s_waitcnt vmcnt(" #n ")" : __VA_ARGS__ :: "memory")

#define NSEG 64
#define O_MEM 0
#define O_SMM (2 * 32768)
#define O_MZ (4 * 32768)
#define O_MEMX 16384
#define RXW(n) ((n) < 7 ? (5 + (n)) * 32768 : 16384 + ((n) - 5) * 32768)
#define O_PROBE (15 * 32768)
#define O_FL (4 << 20)
#define O_PAR O_FL
#define O_RDY (O_FL + 512)
#define O_FPA (O_FL + 1024)
#define O_FE (O_FL + 2048)
#define O_FW (O_FL + 4096)
#define O_FT (O_FW + 16384)
#define O_REC (O_FT + 8192)
#define O_CNT (O_REC + 16384)
#define O_EPOCH (O_CNT + 32768)
#define O_TICK (O_EPOCH + 128)
#define HB_SZ (5 << 20)
#define SPIN_MAX (1u << 17)
#define LOG2E 1.4426950408889634f
#define FF 0xFFFFFFFFu
#define OOB 0x7f000000

struct Args {
    const u16 *Au, *AlogA, *Aim, *ACr, *ACi, *Alogdt, *ADsk, *X, *mem0, *Wq, *Wk, *Wv, *Wo;
    u16* out;
    char* HB;
    u16 *WvoF, *WqkF, *GF, *UF, *EX, *UM, *TF, *PA; float *MZX;
    unsigned* TS;
};
typedef const Args __attribute__((address_space(4))) CArgs;
DI CArgs& get_args() { CArgs* p = (CArgs*)__builtin_amdgcn_kernarg_segment_ptr(); asm volatile("" : "+s"(p)); return *p; }
extern __shared__ __attribute__((aligned(16))) char smem_[];
DI unsigned now32() { u64 t; asm volatile("s_memtime %0\n\ts_waitcnt lgkmcnt(0)" : "=s"(t) :: "memory"); return (unsigned)t; }
DI unsigned rt_now() { u64 t; asm volatile("s_memrealtime %0\n\ts_waitcnt lgkmcnt(0)" : "=s"(t) :: "memory"); return (unsigned)t; }
#define REC_A 1260
#define REC_B 1045
#define LAT_R 1160u
#define OFF_P 60
#define OFF_X 20
#define TOL_C -100
#define ASM_ROLES \
".p2align 6\n" \
"fk_role_S0:\n" \
"ds_read_b32 v200, v255 offset:0\n" \
"ds_read_b32 v201, v255 offset:256\n" \
"ds_read_b32 v202, v255 offset:512\n" \
"ds_read_b32 v203, v255 offset:768\n" \
"ds_read_b32 v204, v255 offset:1024\n" \
"ds_read_b32 v205, v255 offset:1280\n" \
"ds_read_b32 v206, v255 offset:1536\n" \
"ds_read_b32 v207, v255 offset:1792\n" \
"ds_read_b32 v208, v255 offset:2048\n" \
"ds_read_b32 v209, v255 offset:2304\n" \
"ds_read_b32 v210, v255 offset:2560\n" \
"ds_read_b32 v211, v255 offset:2816\n" \
"ds_read_b32 v212, v255 offset:3072\n" \
"ds_read_b32 v213, v255 offset:3328\n" \
"ds_read_b32 v214, v255 offset:3584\n" \
"ds_read_b32 v215, v255 offset:3840\n" \
"ds_read_b32 v254, v255 offset:7936\n" \
"s_waitcnt lgkmcnt(0)\n" \
"v_readlane_b32 s4, v254, 0\n" \
"v_readlane_b32 s5, v254, 1\n" \
"v_readlane_b32 s6, v254, 2\n" \
"v_readlane_b32 s7, v254, 3\n" \
"v_readlane_b32 s8, v254, 4\n" \
"v_readlane_b32 s9, v254, 5\n" \
"v_readlane_b32 s10, v254, 6\n" \
"v_readlane_b32 s11, v254, 7\n" \
"v_readlane_b32 s12, v254, 8\n" \
"v_readlane_b32 s13, v254, 9\n" \
"v_readlane_b32 s14, v254, 10\n" \
"v_readlane_b32 s15, v254, 11\n" \
"v_readlane_b32 s16, v254, 12\n" \
"v_readlane_b32 s17, v254, 13\n" \
"v_readlane_b32 s18, v254, 14\n" \
"v_readlane_b32 s19, v254, 15\n" \
"v_readlane_b32 s20, v254, 16\n" \
"v_readlane_b32 s21, v254, 17\n" \
"v_readlane_b32 s22, v254, 18\n" \
"v_readlane_b32 s23, v254, 19\n" \
"v_readlane_b32 s24, v254, 20\n" \
"v_readlane_b32 s25, v254, 21\n" \
"v_readlane_b32 s26, v254, 22\n" \
"v_readlane_b32 s27, v254, 23\n" \
"v_readlane_b32 s28, v254, 24\n" \
"v_readlane_b32 s29, v254, 25\n" \
"v_readlane_b32 s30, v254, 26\n" \
"v_readlane_b32 s31, v254, 27\n" \
"v_readlane_b32 s32, v254, 28\n" \
"v_readlane_b32 s33, v254, 29\n" \
"v_readlane_b32 s34, v254, 30\n" \
"v_readlane_b32 s35, v254, 31\n" \
"v_readlane_b32 s36, v254, 32\n" \
"v_readlane_b32 s37, v254, 33\n" \
"v_readlane_b32 s38, v254, 34\n" \
"v_readlane_b32 s39, v254, 35\n" \
"s_mov_b32 s40, 0\n" \
"s_mov_b32 s51, 0\n" \
"s_nop 0\n" \
"v_mov_b32 v252, s38\n" \
"s_nop 4\n" \
"s_add_u32 s34, s33, 1\n" \
"s_add_u32 s36, s33, 2\n" \
"s_add_u32 s43, s30, s33\n" \
"s_mov_b32 s50, 0\n" \
".LS0_w1l:\n" \
"buffer_load_dword v240, v204, s[4:7], 0 offen sc1\n" \
"s_waitcnt vmcnt(0)\n" \
".LS0_w1c:\n" \
"v_subrev_u32 v241, s43, v240\n" \
"v_cmp_gt_u32 vcc, 0x100, v241\n" \
"s_cmp_eq_u64 vcc, -1\n" \
"s_cbranch_scc1 .LS0_w1d\n" \
"s_sleep 2\n" \
"s_add_u32 s50, s50, 1\n" \
"s_cmp_lt_u32 s50, 0x20000\n" \
"s_cbranch_scc1 .LS0_w1l\n" \
"s_branch .LS0_end\n" \
".LS0_w1d:\n" \
"v_readlane_b32 s43, v202, 0\n" \
"s_nop 4\n" \
"buffer_load_dwordx4 a[0:3], v201, s[8:11], s43 offen sc1\n" \
"buffer_load_dwordx4 a[4:7], v201, s[8:11], s43 offen offset:1024 sc1\n" \
"v_readlane_b32 s43, v202, 1\n" \
"s_nop 4\n" \
"buffer_load_dwordx4 a[8:11], v201, s[8:11], s43 offen sc1\n" \
"buffer_load_dwordx4 a[12:15], v201, s[8:11], s43 offen offset:1024 sc1\n" \
"v_readlane_b32 s43, v202, 2\n" \
"s_nop 4\n" \
"buffer_load_dwordx4 a[16:19], v201, s[8:11], s43 offen sc1\n" \
"buffer_load_dwordx4 a[20:23], v201, s[8:11], s43 offen offset:1024 sc1\n" \
"v_readlane_b32 s43, v202, 3\n" \
"s_nop 4\n" \
"buffer_load_dwordx4 a[24:27], v201, s[8:11], s43 offen sc1\n" \
"buffer_load_dwordx4 a[28:31], v201, s[8:11], s43 offen offset:1024 sc1\n" \
"v_readlane_b32 s43, v202, 4\n" \
"s_nop 4\n" \
"buffer_load_dwordx4 a[32:35], v201, s[8:11], s43 offen sc1\n" \
"buffer_load_dwordx4 a[36:39], v201, s[8:11], s43 offen offset:1024 sc1\n" \
"v_readlane_b32 s43, v202, 5\n" \
"s_nop 4\n" \
"buffer_load_dwordx4 a[40:43], v201, s[8:11], s43 offen sc1\n" \
"buffer_load_dwordx4 a[44:47], v201, s[8:11], s43 offen offset:1024 sc1\n" \
"v_readlane_b32 s43, v202, 6\n" \
"s_nop 4\n" \
"buffer_load_dwordx4 a[48:51], v201, s[8:11], s43 offen sc1\n" \
"buffer_load_dwordx4 a[52:55], v201, s[8:11], s43 offen offset:1024 sc1\n" \
"v_readlane_b32 s43, v202, 7\n" \
"s_nop 4\n" \
"buffer_load_dwordx4 a[56:59], v201, s[8:11], s43 offen sc1\n" \
"buffer_load_dwordx4 a[60:63], v201, s[8:11], s43 offen offset:1024 sc1\n" \
"s_add_u32 s43, s30, s34\n" \
"s_mov_b32 s50, 0\n" \
".LS0_w2l:\n" \
"buffer_load_dword v240, v204, s[4:7], 0 offen sc1\n" \
"s_waitcnt vmcnt(0)\n" \
".LS0_w2c:\n" \
"v_subrev_u32 v241, s43, v240\n" \
"v_cmp_gt_u32 vcc, 0x100, v241\n" \
"s_cmp_eq_u64 vcc, -1\n" \
"s_cbranch_scc1 .LS0_w2d\n" \
"s_sleep 2\n" \
"s_add_u32 s50, s50, 1\n" \
"s_cmp_lt_u32 s50, 0x20000\n" \
"s_cbranch_scc1 .LS0_w2l\n" \
"s_branch .LS0_end\n" \
".LS0_w2d:\n" \
"v_readlane_b32 s43, v202, 8\n" \
"s_nop 4\n" \
"buffer_load_dwordx4 a[64:67], v201, s[8:11], s43 offen sc1\n" \
"buffer_load_dwordx4 a[68:71], v201, s[8:11], s43 offen offset:1024 sc1\n" \
"v_readlane_b32 s43, v202, 9\n" \
"s_nop 4\n" \
"buffer_load_dwordx4 a[72:75], v201, s[8:11], s43 offen sc1\n" \
"buffer_load_dwordx4 a[76:79], v201, s[8:11], s43 offen offset:1024 sc1\n" \
"v_readlane_b32 s43, v202, 10\n" \
"s_nop 4\n" \
"buffer_load_dwordx4 a[80:83], v201, s[8:11], s43 offen sc1\n" \
"buffer_load_dwordx4 a[84:87], v201, s[8:11], s43 offen offset:1024 sc1\n" \
"v_readlane_b32 s43, v202, 12\n" \
"s_nop 4\n" \
"buffer_load_dwordx4 a[96:99], v201, s[8:11], s43 offen sc1\n" \
"buffer_load_dwordx4 a[100:103], v201, s[8:11], s43 offen offset:1024 sc1\n" \
"v_readlane_b32 s43, v202, 14\n" \
"s_nop 4\n" \
"buffer_load_dwordx4 a[112:115], v201, s[8:11], s43 offen sc1\n" \
"buffer_load_dwordx4 a[116:119], v201, s[8:11], s43 offen offset:1024 sc1\n" \
"v_readlane_b32 s43, v202, 19\n" \
"s_nop 4\n" \
"buffer_load_dwordx4 a[152:155], v201, s[8:11], s43 offen sc1\n" \
"buffer_load_dwordx4 a[156:159], v201, s[8:11], s43 offen offset:1024 sc1\n" \
"v_readlane_b32 s43, v202, 22\n" \
"s_nop 4\n" \
"buffer_load_dwordx4 a[176:179], v201, s[8:11], s43 offen sc1\n" \
"buffer_load_dwordx4 a[180:183], v201, s[8:11], s43 offen offset:1024 sc1\n" \
"v_readlane_b32 s43, v202, 25\n" \
"s_nop 4\n" \
"buffer_load_dwordx4 a[200:203], v201, s[8:11], s43 offen sc1\n" \
"buffer_load_dwordx4 a[204:207], v201, s[8:11], s43 offen offset:1024 sc1\n" \
"s_add_u32 s43, s30, s36\n" \
"s_mov_b32 s50, 0\n" \
".LS0_w3l:\n" \
"buffer_load_dword v240, v204, s[4:7], 0 offen sc1\n" \
"s_waitcnt vmcnt(0)\n" \
".LS0_w3c:\n" \
"v_subrev_u32 v241, s43, v240\n" \
"v_cmp_gt_u32 vcc, 0x100, v241\n" \
"s_cmp_eq_u64 vcc, -1\n" \
"s_cbranch_scc1 .LS0_w3d\n" \
"s_sleep 2\n" \
"s_add_u32 s50, s50, 1\n" \
"s_cmp_lt_u32 s50, 0x20000\n" \
"s_cbranch_scc1 .LS0_w3l\n" \
"s_branch .LS0_end\n" \
".LS0_w3d:\n" \
"v_readlane_b32 s43, v202, 11\n" \
"s_nop 4\n" \
"buffer_load_dwordx4 a[88:91], v201, s[8:11], s43 offen sc1\n" \
"buffer_load_dwordx4 a[92:95], v201, s[8:11], s43 offen offset:1024 sc1\n" \
"v_readlane_b32 s43, v202, 13\n" \
"s_nop 4\n" \
"buffer_load_dwordx4 a[104:107], v201, s[8:11], s43 offen sc1\n" \
"buffer_load_dwordx4 a[108:111], v201, s[8:11], s43 offen offset:1024 sc1\n" \
"v_readlane_b32 s43, v202, 15\n" \
"s_nop 4\n" \
"buffer_load_dwordx4 a[120:123], v201, s[8:11], s43 offen sc1\n" \
"buffer_load_dwordx4 a[124:127], v201, s[8:11], s43 offen offset:1024 sc1\n" \
"v_readlane_b32 s43, v202, 16\n" \
"s_nop 4\n" \
"buffer_load_dwordx4 a[128:131], v201, s[8:11], s43 offen sc1\n" \
"buffer_load_dwordx4 a[132:135], v201, s[8:11], s43 offen offset:1024 sc1\n" \
"v_readlane_b32 s43, v202, 17\n" \
"s_nop 4\n" \
"buffer_load_dwordx4 a[136:139], v201, s[8:11], s43 offen sc1\n" \
"buffer_load_dwordx4 a[140:143], v201, s[8:11], s43 offen offset:1024 sc1\n" \
"v_readlane_b32 s43, v202, 20\n" \
"s_nop 4\n" \
"buffer_load_dwordx4 a[160:163], v201, s[8:11], s43 offen sc1\n" \
"buffer_load_dwordx4 a[164:167], v201, s[8:11], s43 offen offset:1024 sc1\n" \
"v_readlane_b32 s43, v202, 23\n" \
"s_nop 4\n" \
"buffer_load_dwordx4 a[184:187], v201, s[8:11], s43 offen sc1\n" \
"buffer_load_dwordx4 a[188:191], v201, s[8:11], s43 offen offset:1024 sc1\n" \
"v_readlane_b32 s43, v202, 26\n" \
"s_nop 4\n" \
"buffer_load_dwordx4 a[208:211], v201, s[8:11], s43 offen sc1\n" \
"buffer_load_dwordx4 a[212:215], v201, s[8:11], s43 offen offset:1024 sc1\n" \
"s_add_u32 s43, s30, 4\n" \
"s_mov_b32 s50, 0\n" \
".LS0_w4l:\n" \
"buffer_load_dword v240, v204, s[4:7], 0 offen sc1\n" \
"s_waitcnt vmcnt(0)\n" \
".LS0_w4c:\n" \
"v_subrev_u32 v241, s43, v240\n" \
"v_cmp_gt_u32 vcc, 0x100, v241\n" \
"s_cmp_eq_u64 vcc, -1\n" \
"s_cbranch_scc1 .LS0_w4d\n" \
"s_sleep 2\n" \
"s_add_u32 s50, s50, 1\n" \
"s_cmp_lt_u32 s50, 0x20000\n" \
"s_cbranch_scc1 .LS0_w4l\n" \
"s_branch .LS0_end\n" \
".LS0_w4d:\n" \
"v_readlane_b32 s43, v202, 18\n" \
"s_nop 4\n" \
"buffer_load_dwordx4 a[144:147], v201, s[8:11], s43 offen sc1\n" \
"buffer_load_dwordx4 a[148:151], v201, s[8:11], s43 offen offset:1024 sc1\n" \
"v_readlane_b32 s43, v202, 21\n" \
"s_nop 4\n" \
"buffer_load_dwordx4 a[168:171], v201, s[8:11], s43 offen sc1\n" \
"buffer_load_dwordx4 a[172:175], v201, s[8:11], s43 offen offset:1024 sc1\n" \
"v_readlane_b32 s43, v202, 24\n" \
"s_nop 4\n" \
"buffer_load_dwordx4 a[192:195], v201, s[8:11], s43 offen sc1\n" \
"buffer_load_dwordx4 a[196:199], v201, s[8:11], s43 offen offset:1024 sc1\n" \
"v_readlane_b32 s43, v202, 27\n" \
"s_nop 4\n" \
"buffer_load_dwordx4 a[216:219], v201, s[8:11], s43 offen sc1\n" \
"buffer_load_dwordx4 a[220:223], v201, s[8:11], s43 offen offset:1024 sc1\n" \
"v_mov_b32 v188, -1\n" \
"s_add_u32 s43, s30, 1\n" \
"s_mov_b32 s50, 0\n" \
".LS0_w5l:\n" \
"buffer_load_dword v240, v205, s[4:7], 0 offen sc1\n" \
"s_waitcnt vmcnt(0)\n" \
".LS0_w5c:\n" \
"v_subrev_u32 v241, s43, v240\n" \
"v_cmp_gt_u32 vcc, 0x100, v241\n" \
"s_cmp_eq_u64 vcc, -1\n" \
"s_cbranch_scc1 .LS0_w5d\n" \
"s_sleep 2\n" \
"s_add_u32 s50, s50, 1\n" \
"s_cmp_lt_u32 s50, 0x20000\n" \
"s_cbranch_scc1 .LS0_w5l\n" \
"s_branch .LS0_end\n" \
".LS0_w5d:\n" \
"s_add_u32 s43, s30, 1\n" \
"s_mov_b32 s50, 0\n" \
".LS0_w6l:\n" \
"buffer_load_dword v240, v206, s[4:7], 0 offen sc1\n" \
"s_waitcnt vmcnt(0)\n" \
".LS0_w6c:\n" \
"v_subrev_u32 v241, s43, v240\n" \
"v_cmp_gt_u32 vcc, 0x100, v241\n" \
"s_cmp_eq_u64 vcc, -1\n" \
"s_cbranch_scc1 .LS0_w6d\n" \
"s_sleep 2\n" \
"s_add_u32 s50, s50, 1\n" \
"s_cmp_lt_u32 s50, 0x20000\n" \
"s_cbranch_scc1 .LS0_w6l\n" \
"s_branch .LS0_end\n" \
".LS0_w6d:\n" \
"s_barrier\n" \
"ds_read_b96 v[248:250], v252\n" \
"s_waitcnt lgkmcnt(0)\n" \
"v_readfirstlane_b32 s44, v248\n" \
"v_readfirstlane_b32 s45, v249\n" \
"v_readfirstlane_b32 s46, v250\n" \
"s_mul_i32 s42, s25, s46\n" \
"s_lshr_b32 s42, s42, 12\n" \
".LS0_loop:\n" \
"s_add_u32 s43, s40, 0\n" \
"s_and_b32 s43, s43, 3\n" \
"s_bitcmp1_b32 s43, 0\n" \
"s_cselect_b32 s47, s32, s31\n" \
"s_lshl_b32 s43, s43, 19\n" \
"s_add_u32 s47, s47, s43\n" \
"s_sub_u32 s43, s24, s44\n" \
"s_max_i32 s43, s43, 0xfffd0000\n" \
"s_min_i32 s43, s43, 0x4000\n" \
"s_mul_i32 s43, s43, s46\n" \
"s_ashr_i32 s43, s43, 12\n" \
"s_add_u32 s41, s45, s43\n" \
"s_branch .LS0_a1\n" \
".LS0_retry:\n" \
"s_add_u32 s51, s51, 1\n" \
"s_cmp_gt_u32 s51, 0x20000\n" \
"s_cbranch_scc1 .LS0_end\n" \
".LS0_a0:\n" \
"s_add_u32 s41, s41, s42\n" \
"s_add_u32 s24, s24, s25\n" \
"s_xor_b32 s31, s31, s29\n" \
"s_xor_b32 s32, s32, s29\n" \
"s_xor_b32 s47, s47, s29\n" \
"s_xor_b32 s48, s48, s29\n" \
"s_add_u32 s39, s39, 1\n" \
".p2align 6\n" \
".LS0_a1:\n" \
"s_memtime vcc\n" \
"s_waitcnt lgkmcnt(0)\n" \
"s_sub_u32 s43, s41, vcc_lo\n" \
"s_cmp_gt_i32 s43, 128\n" \
"s_cbranch_scc1 .LS0_a1\n" \
"s_cmp_lt_i32 s43, s37\n" \
"s_cbranch_scc1 .LS0_a0\n" \
"s_max_i32 s43, s43, 0\n" \
"s_and_b32 s43, s43, -4\n" \
"s_getpc_b64 vcc\n" \
".LS0_a3:\n" \
"s_sub_u32 s43, .LS0_a4-.LS0_a3, s43\n" \
"s_add_u32 vcc_lo, vcc_lo, s43\n" \
"s_addc_u32 vcc_hi, vcc_hi, 0\n" \
"s_setpc_b64 vcc\n" \
".rept 32\n" \
"s_nop 0\n" \
".endr\n" \
".LS0_a4:\n" \
"buffer_load_dwordx4 v[0:3], v208, s[4:7], s47 offen sc1\n" \
"s_nop 9\n" \
"buffer_load_dwordx4 v[4:7], v208, s[4:7], s47 offen offset:1024 sc1\n" \
"s_nop 9\n" \
"buffer_load_dwordx4 v[8:11], v208, s[4:7], s47 offen offset:2048 sc1\n" \
"s_nop 9\n" \
"buffer_load_dwordx4 v[12:15], v210, s[4:7], s47 offen sc1\n" \
"s_add_u32 s35, s35, 1\n" \
"v_mov_b32 v28, 0\n" \
"v_mov_b32 v29, 0\n" \
"v_mov_b32 v30, 0\n" \
"v_mov_b32 v31, 0\n" \
"v_mov_b32 v32, 0\n" \
"v_mov_b32 v33, 0\n" \
"v_mov_b32 v34, 0\n" \
"v_mov_b32 v35, 0\n" \
"v_mov_b32 v36, 0\n" \
"v_mov_b32 v37, 0\n" \
"v_mov_b32 v38, 0\n" \
"v_mov_b32 v39, 0\n" \
"v_mov_b32 v40, 0\n" \
"v_mov_b32 v41, 0\n" \
"v_mov_b32 v42, 0\n" \
"v_mov_b32 v43, 0\n" \
"v_mov_b32 v44, 0\n" \
"v_mov_b32 v45, 0\n" \
"v_mov_b32 v46, 0\n" \
"v_mov_b32 v47, 0\n" \
"v_mov_b32 v48, 0\n" \
"v_mov_b32 v49, 0\n" \
"v_mov_b32 v50, 0\n" \
"v_mov_b32 v51, 0\n" \
"v_mov_b32 v52, 0\n" \
"v_mov_b32 v53, 0\n" \
"v_mov_b32 v54, 0\n" \
"v_mov_b32 v55, 0\n" \
"v_mov_b32 v56, 0\n" \
"v_mov_b32 v57, 0\n" \
"v_mov_b32 v58, 0\n" \
"v_mov_b32 v59, 0\n" \
"s_waitcnt vmcnt(3)\n" \
"v_smfmac_f32_16x16x64_bf16 v[28:31], v[0:3], a[0:7], v200\n" \
"v_smfmac_f32_16x16x64_bf16 v[32:35], v[0:3], a[8:15], v200\n" \
"v_smfmac_f32_16x16x64_bf16 v[36:39], v[0:3], a[16:23], v200\n" \
"v_smfmac_f32_16x16x64_bf16 v[40:43], v[0:3], a[24:31], v200\n" \
"v_smfmac_f32_16x16x64_bf16 v[44:47], v[0:3], a[32:39], v200\n" \
"v_smfmac_f32_16x16x64_bf16 v[48:51], v[0:3], a[40:47], v200\n" \
"v_smfmac_f32_16x16x64_bf16 v[52:55], v[0:3], a[48:55], v200\n" \
"v_smfmac_f32_16x16x64_bf16 v[56:59], v[0:3], a[56:63], v200\n" \
"s_waitcnt vmcnt(2)\n" \
"v_smfmac_f32_16x16x64_bf16 v[28:31], v[4:7], a[64:71], v200\n" \
"v_smfmac_f32_16x16x64_bf16 v[32:35], v[4:7], a[72:79], v200\n" \
"v_smfmac_f32_16x16x64_bf16 v[36:39], v[4:7], a[80:87], v200\n" \
"s_waitcnt vmcnt(1)\n" \
"v_smfmac_f32_16x16x64_bf16 v[28:31], v[8:11], a[88:95], v200\n" \
"s_nop 2\n" \
"v_smfmac_f32_16x16x64_bf16 v[40:43], v[4:7], a[96:103], v200\n" \
"s_nop 2\n" \
"v_smfmac_f32_16x16x64_bf16 v[32:35], v[8:11], a[104:111], v200\n" \
"s_nop 0\n" \
"v_cvt_pk_bf16_f32 v128, v28, v29\n" \
"v_cvt_pk_bf16_f32 v129, v30, v31\n" \
"v_smfmac_f32_16x16x64_bf16 v[44:47], v[4:7], a[112:119], v200\n" \
"ds_write_b64 v211, v[128:129] offset:0\n" \
"s_nop 1\n" \
"v_smfmac_f32_16x16x64_bf16 v[36:39], v[8:11], a[120:127], v200\n" \
"s_nop 0\n" \
"v_cvt_pk_bf16_f32 v130, v32, v33\n" \
"v_cvt_pk_bf16_f32 v131, v34, v35\n" \
"v_smfmac_f32_16x16x64_bf16 v[44:47], v[8:11], a[128:135], v200\n" \
"ds_write_b64 v211, v[130:131] offset:384\n" \
"s_nop 1\n" \
"v_smfmac_f32_16x16x64_bf16 v[40:43], v[8:11], a[136:143], v200\n" \
"s_waitcnt vmcnt(0)\n" \
"v_cvt_pk_bf16_f32 v132, v36, v37\n" \
"v_cvt_pk_bf16_f32 v133, v38, v39\n" \
"v_smfmac_f32_16x16x64_bf16 v[44:47], v[12:15], a[144:151], v200\n" \
"ds_write_b64 v211, v[132:133] offset:2560\n" \
"s_nop 1\n" \
"v_smfmac_f32_16x16x64_bf16 v[48:51], v[4:7], a[152:159], v200\n" \
"s_nop 0\n" \
"v_cvt_pk_bf16_f32 v134, v40, v41\n" \
"v_cvt_pk_bf16_f32 v135, v42, v43\n" \
"v_smfmac_f32_16x16x64_bf16 v[48:51], v[8:11], a[160:167], v200\n" \
"ds_write_b64 v211, v[134:135] offset:4736\n" \
"v_cvt_pk_bf16_f32 v136, v44, v45\n" \
"v_cvt_pk_bf16_f32 v137, v46, v47\n" \
"v_smfmac_f32_16x16x64_bf16 v[48:51], v[12:15], a[168:175], v200\n" \
"ds_write_b64 v211, v[136:137] offset:9216\n" \
"s_nop 1\n" \
"v_smfmac_f32_16x16x64_bf16 v[52:55], v[4:7], a[176:183], v200\n" \
"s_nop 2\n" \
"v_smfmac_f32_16x16x64_bf16 v[52:55], v[8:11], a[184:191], v200\n" \
"s_nop 0\n" \
"v_cvt_pk_bf16_f32 v138, v48, v49\n" \
"v_cvt_pk_bf16_f32 v139, v50, v51\n" \
"v_smfmac_f32_16x16x64_bf16 v[52:55], v[12:15], a[192:199], v200\n" \
"ds_write_b64 v211, v[138:139] offset:9600\n" \
"s_waitcnt lgkmcnt(2)\n" \
"s_barrier\n" \
"v_smfmac_f32_16x16x64_bf16 v[56:59], v[4:7], a[200:207], v200\n" \
"s_nop 2\n" \
"v_smfmac_f32_16x16x64_bf16 v[56:59], v[8:11], a[208:215], v200\n" \
"s_nop 2\n" \
"v_smfmac_f32_16x16x64_bf16 v[56:59], v[12:15], a[216:223], v200\n" \
"v_cvt_pk_bf16_f32 v140, v52, v53\n" \
"v_cvt_pk_bf16_f32 v141, v54, v55\n" \
"s_nop 1\n" \
"ds_write_b64 v211, v[140:141] offset:11776\n" \
"s_nop 2\n" \
"v_cvt_pk_bf16_f32 v142, v56, v57\n" \
"v_cvt_pk_bf16_f32 v143, v58, v59\n" \
"ds_write_b64 v211, v[142:143] offset:13952\n" \
"s_waitcnt lgkmcnt(2)\n" \
"s_barrier\n" \
"s_waitcnt lgkmcnt(1)\n" \
"s_barrier\n" \
"s_waitcnt lgkmcnt(0)\n" \
"s_barrier\n" \
"s_nop 3\n" \
"ds_read_b32 v64, v252 offset:20\n" \
"s_waitcnt lgkmcnt(0)\n" \
"v_readfirstlane_b32 s43, v64\n" \
"s_cmp_eq_u32 s43, s35\n" \
"s_cbranch_scc1 .LS0_retry\n" \
"s_nop 15\n" \
"v_max3_u32 v64, v0, v0, v1\n" \
"v_max3_u32 v64, v64, v2, v3\n" \
"v_max3_u32 v64, v64, v4, v5\n" \
"v_max3_u32 v64, v64, v6, v7\n" \
"v_max3_u32 v64, v64, v8, v9\n" \
"v_max3_u32 v64, v64, v10, v11\n" \
"v_max3_u32 v64, v64, v12, v13\n" \
"v_max3_u32 v64, v64, v14, v15\n" \
"v_cmp_eq_u32 vcc, -1, v64\n" \
"s_cbranch_vccnz .LS0_veto\n" \
".LS0_back:\n" \
"s_waitcnt lgkmcnt(0)\n" \
"s_barrier\n" \
"ds_read_b32 v64, v252 offset:16\n" \
"s_waitcnt lgkmcnt(0)\n" \
"v_readfirstlane_b32 s43, v64\n" \
"s_cmp_eq_u32 s43, s35\n" \
"s_cbranch_scc1 .LS0_retry\n" \
"s_waitcnt lgkmcnt(0)\n" \
"s_barrier\n" \
"ds_read_b64 v[246:247], v252 offset:24\n" \
"ds_read_b96 v[248:250], v252\n" \
"s_waitcnt lgkmcnt(0)\n" \
"v_readfirstlane_b32 s44, v248\n" \
"v_readfirstlane_b32 s45, v249\n" \
"v_readfirstlane_b32 s46, v250\n" \
"v_readfirstlane_b32 s26, v246\n" \
"v_readfirstlane_b32 s27, v247\n" \
"s_mul_i32 s42, s25, s46\n" \
"s_lshr_b32 s42, s42, 12\n" \
"s_add_u32 s40, s40, 1\n" \
"s_add_u32 s24, s24, s26\n" \
"s_add_u32 s24, s24, s27\n" \
"s_cmp_lt_u32 s40, 63\n" \
"s_cbranch_scc1 .LS0_loop\n" \
".LS0_end:\n" \
"s_endpgm\n" \
".LS0_veto:\n" \
"v_mov_b32 v64, s35\n" \
"ds_write_b32 v252, v64 offset:16\n" \
"s_branch .LS0_back\n" \
".p2align 6\n" \
"fk_role_S1:\n" \
"ds_read_b32 v200, v255 offset:0\n" \
"ds_read_b32 v201, v255 offset:256\n" \
"ds_read_b32 v202, v255 offset:512\n" \
"ds_read_b32 v203, v255 offset:768\n" \
"ds_read_b32 v204, v255 offset:1024\n" \
"ds_read_b32 v205, v255 offset:1280\n" \
"ds_read_b32 v206, v255 offset:1536\n" \
"ds_read_b32 v207, v255 offset:1792\n" \
"ds_read_b32 v208, v255 offset:2048\n" \
"ds_read_b32 v209, v255 offset:2304\n" \
"ds_read_b32 v210, v255 offset:2560\n" \
"ds_read_b32 v211, v255 offset:2816\n" \
"ds_read_b32 v212, v255 offset:3072\n" \
"ds_read_b32 v213, v255 offset:3328\n" \
"ds_read_b32 v214, v255 offset:3584\n" \
"ds_read_b32 v215, v255 offset:3840\n" \
"ds_read_b32 v254, v255 offset:7936\n" \
"s_waitcnt lgkmcnt(0)\n" \
"v_readlane_b32 s4, v254, 0\n" \
"v_readlane_b32 s5, v254, 1\n" \
"v_readlane_b32 s6, v254, 2\n" \
"v_readlane_b32 s7, v254, 3\n" \
"v_readlane_b32 s8, v254, 4\n" \
"v_readlane_b32 s9, v254, 5\n" \
"v_readlane_b32 s10, v254, 6\n" \
"v_readlane_b32 s11, v254, 7\n" \
"v_readlane_b32 s12, v254, 8\n" \
"v_readlane_b32 s13, v254, 9\n" \
"v_readlane_b32 s14, v254, 10\n" \
"v_readlane_b32 s15, v254, 11\n" \
"v_readlane_b32 s16, v254, 12\n" \
"v_readlane_b32 s17, v254, 13\n" \
"v_readlane_b32 s18, v254, 14\n" \
"v_readlane_b32 s19, v254, 15\n" \
"v_readlane_b32 s20, v254, 16\n" \
"v_readlane_b32 s21, v254, 17\n" \
"v_readlane_b32 s22, v254, 18\n" \
"v_readlane_b32 s23, v254, 19\n" \
"v_readlane_b32 s24, v254, 20\n" \
"v_readlane_b32 s25, v254, 21\n" \
"v_readlane_b32 s26, v254, 22\n" \
"v_readlane_b32 s27, v254, 23\n" \
"v_readlane_b32 s28, v254, 24\n" \
"v_readlane_b32 s29, v254, 25\n" \
"v_readlane_b32 s30, v254, 26\n" \
"v_readlane_b32 s31, v254, 27\n" \
"v_readlane_b32 s32, v254, 28\n" \
"v_readlane_b32 s33, v254, 29\n" \
"v_readlane_b32 s34, v254, 30\n" \
"v_readlane_b32 s35, v254, 31\n" \
"v_readlane_b32 s36, v254, 32\n" \
"v_readlane_b32 s37, v254, 33\n" \
"v_readlane_b32 s38, v254, 34\n" \
"v_readlane_b32 s39, v254, 35\n" \
"s_mov_b32 s40, 0\n" \
"s_mov_b32 s51, 0\n" \
"s_nop 0\n" \
"v_mov_b32 v252, s38\n" \
"s_nop 4\n" \
"s_add_u32 s34, s33, 1\n" \
"s_add_u32 s36, s33, 2\n" \
"s_add_u32 s43, s30, s33\n" \
"s_mov_b32 s50, 0\n" \
".LS1_w7l:\n" \
"buffer_load_dword v240, v204, s[4:7], 0 offen sc1\n" \
"s_waitcnt vmcnt(0)\n" \
".LS1_w7c:\n" \
"v_subrev_u32 v241, s43, v240\n" \
"v_cmp_gt_u32 vcc, 0x100, v241\n" \
"s_cmp_eq_u64 vcc, -1\n" \
"s_cbranch_scc1 .LS1_w7d\n" \
"s_sleep 2\n" \
"s_add_u32 s50, s50, 1\n" \
"s_cmp_lt_u32 s50, 0x20000\n" \
"s_cbranch_scc1 .LS1_w7l\n" \
"s_branch .LS1_end\n" \
".LS1_w7d:\n" \
"v_readlane_b32 s43, v202, 11\n" \
"s_nop 4\n" \
"buffer_load_dwordx4 a[88:91], v201, s[8:11], s43 offen sc1\n" \
"buffer_load_dwordx4 a[92:95], v201, s[8:11], s43 offen offset:1024 sc1\n" \
"v_readlane_b32 s43, v202, 13\n" \
"s_nop 4\n" \
"buffer_load_dwordx4 a[104:107], v201, s[8:11], s43 offen sc1\n" \
"buffer_load_dwordx4 a[108:111], v201, s[8:11], s43 offen offset:1024 sc1\n" \
"v_readlane_b32 s43, v202, 15\n" \
"s_nop 4\n" \
"buffer_load_dwordx4 a[120:123], v201, s[8:11], s43 offen sc1\n" \
"buffer_load_dwordx4 a[124:127], v201, s[8:11], s43 offen offset:1024 sc1\n" \
"v_readlane_b32 s43, v202, 16\n" \
"s_nop 4\n" \
"buffer_load_dwordx4 a[128:131], v201, s[8:11], s43 offen sc1\n" \
"buffer_load_dwordx4 a[132:135], v201, s[8:11], s43 offen offset:1024 sc1\n" \
"v_readlane_b32 s43, v202, 17\n" \
"s_nop 4\n" \
"buffer_load_dwordx4 a[136:139], v201, s[8:11], s43 offen sc1\n" \
"buffer_load_dwordx4 a[140:143], v201, s[8:11], s43 offen offset:1024 sc1\n" \
"v_readlane_b32 s43, v202, 20\n" \
"s_nop 4\n" \
"buffer_load_dwordx4 a[160:163], v201, s[8:11], s43 offen sc1\n" \
"buffer_load_dwordx4 a[164:167], v201, s[8:11], s43 offen offset:1024 sc1\n" \
"v_readlane_b32 s43, v202, 23\n" \
"s_nop 4\n" \
"buffer_load_dwordx4 a[184:187], v201, s[8:11], s43 offen sc1\n" \
"buffer_load_dwordx4 a[188:191], v201, s[8:11], s43 offen offset:1024 sc1\n" \
"v_readlane_b32 s43, v202, 26\n" \
"s_nop 4\n" \
"buffer_load_dwordx4 a[208:211], v201, s[8:11], s43 offen sc1\n" \
"buffer_load_dwordx4 a[212:215], v201, s[8:11], s43 offen offset:1024 sc1\n" \
"s_add_u32 s43, s30, s34\n" \
"s_mov_b32 s50, 0\n" \
".LS1_w8l:\n" \
"buffer_load_dword v240, v204, s[4:7], 0 offen sc1\n" \
"s_waitcnt vmcnt(0)\n" \
".LS1_w8c:\n" \
"v_subrev_u32 v241, s43, v240\n" \
"v_cmp_gt_u32 vcc, 0x100, v241\n" \
"s_cmp_eq_u64 vcc, -1\n" \
"s_cbranch_scc1 .LS1_w8d\n" \
"s_sleep 2\n" \
"s_add_u32 s50, s50, 1\n" \
"s_cmp_lt_u32 s50, 0x20000\n" \
"s_cbranch_scc1 .LS1_w8l\n" \
"s_branch .LS1_end\n" \
".LS1_w8d:\n" \
"v_readlane_b32 s43, v202, 0\n" \
"s_nop 4\n" \
"buffer_load_dwordx4 a[0:3], v201, s[8:11], s43 offen sc1\n" \
"buffer_load_dwordx4 a[4:7], v201, s[8:11], s43 offen offset:1024 sc1\n" \
"v_readlane_b32 s43, v202, 1\n" \
"s_nop 4\n" \
"buffer_load_dwordx4 a[8:11], v201, s[8:11], s43 offen sc1\n" \
"buffer_load_dwordx4 a[12:15], v201, s[8:11], s43 offen offset:1024 sc1\n" \
"v_readlane_b32 s43, v202, 2\n" \
"s_nop 4\n" \
"buffer_load_dwordx4 a[16:19], v201, s[8:11], s43 offen sc1\n" \
"buffer_load_dwordx4 a[20:23], v201, s[8:11], s43 offen offset:1024 sc1\n" \
"v_readlane_b32 s43, v202, 3\n" \
"s_nop 4\n" \
"buffer_load_dwordx4 a[24:27], v201, s[8:11], s43 offen sc1\n" \
"buffer_load_dwordx4 a[28:31], v201, s[8:11], s43 offen offset:1024 sc1\n" \
"v_readlane_b32 s43, v202, 4\n" \
"s_nop 4\n" \
"buffer_load_dwordx4 a[32:35], v201, s[8:11], s43 offen sc1\n" \
"buffer_load_dwordx4 a[36:39], v201, s[8:11], s43 offen offset:1024 sc1\n" \
"v_readlane_b32 s43, v202, 5\n" \
"s_nop 4\n" \
"buffer_load_dwordx4 a[40:43], v201, s[8:11], s43 offen sc1\n" \
"buffer_load_dwordx4 a[44:47], v201, s[8:11], s43 offen offset:1024 sc1\n" \
"v_readlane_b32 s43, v202, 6\n" \
"s_nop 4\n" \
"buffer_load_dwordx4 a[48:51], v201, s[8:11], s43 offen sc1\n" \
"buffer_load_dwordx4 a[52:55], v201, s[8:11], s43 offen offset:1024 sc1\n" \
"v_readlane_b32 s43, v202, 7\n" \
"s_nop 4\n" \
"buffer_load_dwordx4 a[56:59], v201, s[8:11], s43 offen sc1\n" \
"buffer_load_dwordx4 a[60:63], v201, s[8:11], s43 offen offset:1024 sc1\n" \
"s_add_u32 s43, s30, s36\n" \
"s_mov_b32 s50, 0\n" \
".LS1_w9l:\n" \
"buffer_load_dword v240, v204, s[4:7], 0 offen sc1\n" \
"s_waitcnt vmcnt(0)\n" \
".LS1_w9c:\n" \
"v_subrev_u32 v241, s43, v240\n" \
"v_cmp_gt_u32 vcc, 0x100, v241\n" \
"s_cmp_eq_u64 vcc, -1\n" \
"s_cbranch_scc1 .LS1_w9d\n" \
"s_sleep 2\n" \
"s_add_u32 s50, s50, 1\n" \
"s_cmp_lt_u32 s50, 0x20000\n" \
"s_cbranch_scc1 .LS1_w9l\n" \
"s_branch .LS1_end\n" \
".LS1_w9d:\n" \
"v_readlane_b32 s43, v202, 8\n" \
"s_nop 4\n" \
"buffer_load_dwordx4 a[64:67], v201, s[8:11], s43 offen sc1\n" \
"buffer_load_dwordx4 a[68:71], v201, s[8:11], s43 offen offset:1024 sc1\n" \
"v_readlane_b32 s43, v202, 9\n" \
"s_nop 4\n" \
"buffer_load_dwordx4 a[72:75], v201, s[8:11], s43 offen sc1\n" \
"buffer_load_dwordx4 a[76:79], v201, s[8:11], s43 offen offset:1024 sc1\n" \
"v_readlane_b32 s43, v202, 10\n" \
"s_nop 4\n" \
"buffer_load_dwordx4 a[80:83], v201, s[8:11], s43 offen sc1\n" \
"buffer_load_dwordx4 a[84:87], v201, s[8:11], s43 offen offset:1024 sc1\n" \
"v_readlane_b32 s43, v202, 12\n" \
"s_nop 4\n" \
"buffer_load_dwordx4 a[96:99], v201, s[8:11], s43 offen sc1\n" \
"buffer_load_dwordx4 a[100:103], v201, s[8:11], s43 offen offset:1024 sc1\n" \
"v_readlane_b32 s43, v202, 14\n" \
"s_nop 4\n" \
"buffer_load_dwordx4 a[112:115], v201, s[8:11], s43 offen sc1\n" \
"buffer_load_dwordx4 a[116:119], v201, s[8:11], s43 offen offset:1024 sc1\n" \
"v_readlane_b32 s43, v202, 19\n" \
"s_nop 4\n" \
"buffer_load_dwordx4 a[152:155], v201, s[8:11], s43 offen sc1\n" \
"buffer_load_dwordx4 a[156:159], v201, s[8:11], s43 offen offset:1024 sc1\n" \
"v_readlane_b32 s43, v202, 22\n" \
"s_nop 4\n" \
"buffer_load_dwordx4 a[176:179], v201, s[8:11], s43 offen sc1\n" \
"buffer_load_dwordx4 a[180:183], v201, s[8:11], s43 offen offset:1024 sc1\n" \
"v_readlane_b32 s43, v202, 25\n" \
"s_nop 4\n" \
"buffer_load_dwordx4 a[200:203], v201, s[8:11], s43 offen sc1\n" \
"buffer_load_dwordx4 a[204:207], v201, s[8:11], s43 offen offset:1024 sc1\n" \
"s_add_u32 s43, s30, 4\n" \
"s_mov_b32 s50, 0\n" \
".LS1_w10l:\n" \
"buffer_load_dword v240, v204, s[4:7], 0 offen sc1\n" \
"s_waitcnt vmcnt(0)\n" \
".LS1_w10c:\n" \
"v_subrev_u32 v241, s43, v240\n" \
"v_cmp_gt_u32 vcc, 0x100, v241\n" \
"s_cmp_eq_u64 vcc, -1\n" \
"s_cbranch_scc1 .LS1_w10d\n" \
"s_sleep 2\n" \
"s_add_u32 s50, s50, 1\n" \
"s_cmp_lt_u32 s50, 0x20000\n" \
"s_cbranch_scc1 .LS1_w10l\n" \
"s_branch .LS1_end\n" \
".LS1_w10d:\n" \
"v_readlane_b32 s43, v202, 18\n" \
"s_nop 4\n" \
"buffer_load_dwordx4 a[144:147], v201, s[8:11], s43 offen sc1\n" \
"buffer_load_dwordx4 a[148:151], v201, s[8:11], s43 offen offset:1024 sc1\n" \
"v_readlane_b32 s43, v202, 21\n" \
"s_nop 4\n" \
"buffer_load_dwordx4 a[168:171], v201, s[8:11], s43 offen sc1\n" \
"buffer_load_dwordx4 a[172:175], v201, s[8:11], s43 offen offset:1024 sc1\n" \
"v_readlane_b32 s43, v202, 24\n" \
"s_nop 4\n" \
"buffer_load_dwordx4 a[192:195], v201, s[8:11], s43 offen sc1\n" \
"buffer_load_dwordx4 a[196:199], v201, s[8:11], s43 offen offset:1024 sc1\n" \
"v_readlane_b32 s43, v202, 27\n" \
"s_nop 4\n" \
"buffer_load_dwordx4 a[216:219], v201, s[8:11], s43 offen sc1\n" \
"buffer_load_dwordx4 a[220:223], v201, s[8:11], s43 offen offset:1024 sc1\n" \
"v_mov_b32 v188, -1\n" \
"s_add_u32 s43, s30, 1\n" \
"s_mov_b32 s50, 0\n" \
".LS1_w11l:\n" \
"buffer_load_dword v240, v205, s[4:7], 0 offen sc1\n" \
"s_waitcnt vmcnt(0)\n" \
".LS1_w11c:\n" \
"v_subrev_u32 v241, s43, v240\n" \
"v_cmp_gt_u32 vcc, 0x100, v241\n" \
"s_cmp_eq_u64 vcc, -1\n" \
"s_cbranch_scc1 .LS1_w11d\n" \
"s_sleep 2\n" \
"s_add_u32 s50, s50, 1\n" \
"s_cmp_lt_u32 s50, 0x20000\n" \
"s_cbranch_scc1 .LS1_w11l\n" \
"s_branch .LS1_end\n" \
".LS1_w11d:\n" \
"s_add_u32 s43, s30, 1\n" \
"s_mov_b32 s50, 0\n" \
".LS1_w12l:\n" \
"buffer_load_dword v240, v206, s[4:7], 0 offen sc1\n" \
"s_waitcnt vmcnt(0)\n" \
".LS1_w12c:\n" \
"v_subrev_u32 v241, s43, v240\n" \
"v_cmp_gt_u32 vcc, 0x100, v241\n" \
"s_cmp_eq_u64 vcc, -1\n" \
"s_cbranch_scc1 .LS1_w12d\n" \
"s_sleep 2\n" \
"s_add_u32 s50, s50, 1\n" \
"s_cmp_lt_u32 s50, 0x20000\n" \
"s_cbranch_scc1 .LS1_w12l\n" \
"s_branch .LS1_end\n" \
".LS1_w12d:\n" \
"s_barrier\n" \
"ds_read_b96 v[248:250], v252\n" \
"s_waitcnt lgkmcnt(0)\n" \
"v_readfirstlane_b32 s44, v248\n" \
"v_readfirstlane_b32 s45, v249\n" \
"v_readfirstlane_b32 s46, v250\n" \
"s_mul_i32 s42, s25, s46\n" \
"s_lshr_b32 s42, s42, 12\n" \
".LS1_loop:\n" \
"s_add_u32 s43, s40, 0\n" \
"s_and_b32 s43, s43, 3\n" \
"s_bitcmp1_b32 s43, 0\n" \
"s_cselect_b32 s47, s32, s31\n" \
"s_lshl_b32 s43, s43, 19\n" \
"s_add_u32 s47, s47, s43\n" \
"s_sub_u32 s43, s24, s44\n" \
"s_max_i32 s43, s43, 0xfffd0000\n" \
"s_min_i32 s43, s43, 0x4000\n" \
"s_mul_i32 s43, s43, s46\n" \
"s_ashr_i32 s43, s43, 12\n" \
"s_add_u32 s41, s45, s43\n" \
"s_add_u32 s41, s41, 4\n" \
"s_branch .LS1_a1\n" \
".LS1_retry:\n" \
"s_add_u32 s51, s51, 1\n" \
"s_cmp_gt_u32 s51, 0x20000\n" \
"s_cbranch_scc1 .LS1_end\n" \
".LS1_a0:\n" \
"s_add_u32 s41, s41, s42\n" \
"s_add_u32 s24, s24, s25\n" \
"s_xor_b32 s31, s31, s29\n" \
"s_xor_b32 s32, s32, s29\n" \
"s_xor_b32 s47, s47, s29\n" \
"s_xor_b32 s48, s48, s29\n" \
"s_add_u32 s39, s39, 1\n" \
".p2align 6\n" \
".LS1_a1:\n" \
"s_memtime vcc\n" \
"s_waitcnt lgkmcnt(0)\n" \
"s_sub_u32 s43, s41, vcc_lo\n" \
"s_cmp_gt_i32 s43, 128\n" \
"s_cbranch_scc1 .LS1_a1\n" \
"s_cmp_lt_i32 s43, s37\n" \
"s_cbranch_scc1 .LS1_a0\n" \
"s_max_i32 s43, s43, 0\n" \
"s_and_b32 s43, s43, -4\n" \
"s_getpc_b64 vcc\n" \
".LS1_a3:\n" \
"s_sub_u32 s43, .LS1_a4-.LS1_a3, s43\n" \
"s_add_u32 vcc_lo, vcc_lo, s43\n" \
"s_addc_u32 vcc_hi, vcc_hi, 0\n" \
"s_setpc_b64 vcc\n" \
".rept 32\n" \
"s_nop 0\n" \
".endr\n" \
".LS1_a4:\n" \
"buffer_load_dwordx4 v[0:3], v208, s[4:7], s47 offen offset:1024 sc1\n" \
"s_nop 9\n" \
"buffer_load_dwordx4 v[4:7], v208, s[4:7], s47 offen offset:2048 sc1\n" \
"s_nop 9\n" \
"buffer_load_dwordx4 v[8:11], v208, s[4:7], s47 offen sc1\n" \
"s_nop 9\n" \
"buffer_load_dwordx4 v[12:15], v210, s[4:7], s47 offen sc1\n" \
"s_add_u32 s35, s35, 1\n" \
"v_mov_b32 v28, 0\n" \
"v_mov_b32 v29, 0\n" \
"v_mov_b32 v30, 0\n" \
"v_mov_b32 v31, 0\n" \
"v_mov_b32 v32, 0\n" \
"v_mov_b32 v33, 0\n" \
"v_mov_b32 v34, 0\n" \
"v_mov_b32 v35, 0\n" \
"v_mov_b32 v36, 0\n" \
"v_mov_b32 v37, 0\n" \
"v_mov_b32 v38, 0\n" \
"v_mov_b32 v39, 0\n" \
"v_mov_b32 v40, 0\n" \
"v_mov_b32 v41, 0\n" \
"v_mov_b32 v42, 0\n" \
"v_mov_b32 v43, 0\n" \
"v_mov_b32 v44, 0\n" \
"v_mov_b32 v45, 0\n" \
"v_mov_b32 v46, 0\n" \
"v_mov_b32 v47, 0\n" \
"v_mov_b32 v48, 0\n" \
"v_mov_b32 v49, 0\n" \
"v_mov_b32 v50, 0\n" \
"v_mov_b32 v51, 0\n" \
"v_mov_b32 v52, 0\n" \
"v_mov_b32 v53, 0\n" \
"v_mov_b32 v54, 0\n" \
"v_mov_b32 v55, 0\n" \
"v_mov_b32 v56, 0\n" \
"v_mov_b32 v57, 0\n" \
"v_mov_b32 v58, 0\n" \
"v_mov_b32 v59, 0\n" \
"s_waitcnt vmcnt(3)\n" \
"v_smfmac_f32_16x16x64_bf16 v[28:31], v[0:3], a[0:7], v200\n" \
"v_smfmac_f32_16x16x64_bf16 v[32:35], v[0:3], a[8:15], v200\n" \
"v_smfmac_f32_16x16x64_bf16 v[36:39], v[0:3], a[16:23], v200\n" \
"v_smfmac_f32_16x16x64_bf16 v[40:43], v[0:3], a[24:31], v200\n" \
"v_smfmac_f32_16x16x64_bf16 v[44:47], v[0:3], a[32:39], v200\n" \
"v_smfmac_f32_16x16x64_bf16 v[48:51], v[0:3], a[40:47], v200\n" \
"v_smfmac_f32_16x16x64_bf16 v[52:55], v[0:3], a[48:55], v200\n" \
"v_smfmac_f32_16x16x64_bf16 v[56:59], v[0:3], a[56:63], v200\n" \
"s_waitcnt vmcnt(2)\n" \
"v_smfmac_f32_16x16x64_bf16 v[28:31], v[4:7], a[64:71], v200\n" \
"v_smfmac_f32_16x16x64_bf16 v[32:35], v[4:7], a[72:79], v200\n" \
"v_smfmac_f32_16x16x64_bf16 v[36:39], v[4:7], a[80:87], v200\n" \
"s_waitcnt vmcnt(1)\n" \
"v_smfmac_f32_16x16x64_bf16 v[28:31], v[8:11], a[88:95], v200\n" \
"s_nop 2\n" \
"v_smfmac_f32_16x16x64_bf16 v[40:43], v[4:7], a[96:103], v200\n" \
"s_nop 2\n" \
"v_smfmac_f32_16x16x64_bf16 v[32:35], v[8:11], a[104:111], v200\n" \
"s_nop 0\n" \
"v_cvt_pk_bf16_f32 v128, v28, v29\n" \
"v_cvt_pk_bf16_f32 v129, v30, v31\n" \
"v_smfmac_f32_16x16x64_bf16 v[44:47], v[4:7], a[112:119], v200\n" \
"ds_write_b64 v211, v[128:129] offset:128\n" \
"s_nop 1\n" \
"v_smfmac_f32_16x16x64_bf16 v[36:39], v[8:11], a[120:127], v200\n" \
"s_nop 0\n" \
"v_cvt_pk_bf16_f32 v130, v32, v33\n" \
"v_cvt_pk_bf16_f32 v131, v34, v35\n" \
"v_smfmac_f32_16x16x64_bf16 v[44:47], v[8:11], a[128:135], v200\n" \
"ds_write_b64 v211, v[130:131] offset:2304\n" \
"s_nop 1\n" \
"v_smfmac_f32_16x16x64_bf16 v[40:43], v[8:11], a[136:143], v200\n" \
"s_waitcnt vmcnt(0)\n" \
"v_cvt_pk_bf16_f32 v132, v36, v37\n" \
"v_cvt_pk_bf16_f32 v133, v38, v39\n" \
"v_smfmac_f32_16x16x64_bf16 v[44:47], v[12:15], a[144:151], v200\n" \
"ds_write_b64 v211, v[132:133] offset:2688\n" \
"s_nop 1\n" \
"v_smfmac_f32_16x16x64_bf16 v[48:51], v[4:7], a[152:159], v200\n" \
"s_nop 0\n" \
"v_cvt_pk_bf16_f32 v134, v40, v41\n" \
"v_cvt_pk_bf16_f32 v135, v42, v43\n" \
"v_smfmac_f32_16x16x64_bf16 v[48:51], v[8:11], a[160:167], v200\n" \
"ds_write_b64 v211, v[134:135] offset:4864\n" \
"v_cvt_pk_bf16_f32 v136, v44, v45\n" \
"v_cvt_pk_bf16_f32 v137, v46, v47\n" \
"v_smfmac_f32_16x16x64_bf16 v[48:51], v[12:15], a[168:175], v200\n" \
"ds_write_b64 v211, v[136:137] offset:9344\n" \
"s_nop 1\n" \
"v_smfmac_f32_16x16x64_bf16 v[52:55], v[4:7], a[176:183], v200\n" \
"s_nop 2\n" \
"v_smfmac_f32_16x16x64_bf16 v[52:55], v[8:11], a[184:191], v200\n" \
"s_nop 0\n" \
"v_cvt_pk_bf16_f32 v138, v48, v49\n" \
"v_cvt_pk_bf16_f32 v139, v50, v51\n" \
"v_smfmac_f32_16x16x64_bf16 v[52:55], v[12:15], a[192:199], v200\n" \
"ds_write_b64 v211, v[138:139] offset:11520\n" \
"s_waitcnt lgkmcnt(2)\n" \
"s_barrier\n" \
"v_smfmac_f32_16x16x64_bf16 v[56:59], v[4:7], a[200:207], v200\n" \
"s_nop 2\n" \
"v_smfmac_f32_16x16x64_bf16 v[56:59], v[8:11], a[208:215], v200\n" \
"s_nop 2\n" \
"v_smfmac_f32_16x16x64_bf16 v[56:59], v[12:15], a[216:223], v200\n" \
"v_cvt_pk_bf16_f32 v140, v52, v53\n" \
"v_cvt_pk_bf16_f32 v141, v54, v55\n" \
"s_nop 1\n" \
"ds_write_b64 v211, v[140:141] offset:11904\n" \
"s_nop 2\n" \
"v_cvt_pk_bf16_f32 v142, v56, v57\n" \
"v_cvt_pk_bf16_f32 v143, v58, v59\n" \
"ds_write_b64 v211, v[142:143] offset:14080\n" \
"s_waitcnt lgkmcnt(2)\n" \
"s_barrier\n" \
"s_waitcnt lgkmcnt(1)\n" \
"s_barrier\n" \
"s_waitcnt lgkmcnt(0)\n" \
"s_barrier\n" \
"s_nop 3\n" \
"ds_read_b32 v64, v252 offset:20\n" \
"s_waitcnt lgkmcnt(0)\n" \
"v_readfirstlane_b32 s43, v64\n" \
"s_cmp_eq_u32 s43, s35\n" \
"s_cbranch_scc1 .LS1_retry\n" \
"s_nop 15\n" \
"v_max3_u32 v64, v0, v0, v1\n" \
"v_max3_u32 v64, v64, v2, v3\n" \
"v_max3_u32 v64, v64, v4, v5\n" \
"v_max3_u32 v64, v64, v6, v7\n" \
"v_max3_u32 v64, v64, v8, v9\n" \
"v_max3_u32 v64, v64, v10, v11\n" \
"v_max3_u32 v64, v64, v12, v13\n" \
"v_max3_u32 v64, v64, v14, v15\n" \
"v_cmp_eq_u32 vcc, -1, v64\n" \
"s_cbranch_vccnz .LS1_veto\n" \
".LS1_back:\n" \
"s_waitcnt lgkmcnt(0)\n" \
"s_barrier\n" \
"ds_read_b32 v64, v252 offset:16\n" \
"s_waitcnt lgkmcnt(0)\n" \
"v_readfirstlane_b32 s43, v64\n" \
"s_cmp_eq_u32 s43, s35\n" \
"s_cbranch_scc1 .LS1_retry\n" \
"s_waitcnt lgkmcnt(0)\n" \
"s_barrier\n" \
"ds_read_b64 v[246:247], v252 offset:24\n" \
"ds_read_b96 v[248:250], v252\n" \
"s_waitcnt lgkmcnt(0)\n" \
"v_readfirstlane_b32 s44, v248\n" \
"v_readfirstlane_b32 s45, v249\n" \
"v_readfirstlane_b32 s46, v250\n" \
"v_readfirstlane_b32 s26, v246\n" \
"v_readfirstlane_b32 s27, v247\n" \
"s_mul_i32 s42, s25, s46\n" \
"s_lshr_b32 s42, s42, 12\n" \
"s_add_u32 s40, s40, 1\n" \
"s_add_u32 s24, s24, s26\n" \
"s_add_u32 s24, s24, s27\n" \
"s_cmp_lt_u32 s40, 63\n" \
"s_cbranch_scc1 .LS1_loop\n" \
".LS1_end:\n" \
"s_endpgm\n" \
".LS1_veto:\n" \
"v_mov_b32 v64, s35\n" \
"ds_write_b32 v252, v64 offset:16\n" \
"s_branch .LS1_back\n" \
".p2align 6\n" \
"fk_role_S2:\n" \
"ds_read_b32 v200, v255 offset:0\n" \
"ds_read_b32 v201, v255 offset:256\n" \
"ds_read_b32 v202, v255 offset:512\n" \
"ds_read_b32 v203, v255 offset:768\n" \
"ds_read_b32 v204, v255 offset:1024\n" \
"ds_read_b32 v205, v255 offset:1280\n" \
"ds_read_b32 v206, v255 offset:1536\n" \
"ds_read_b32 v207, v255 offset:1792\n" \
"ds_read_b32 v208, v255 offset:2048\n" \
"ds_read_b32 v209, v255 offset:2304\n" \
"ds_read_b32 v210, v255 offset:2560\n" \
"ds_read_b32 v211, v255 offset:2816\n" \
"ds_read_b32 v212, v255 offset:3072\n" \
"ds_read_b32 v213, v255 offset:3328\n" \
"ds_read_b32 v214, v255 offset:3584\n" \
"ds_read_b32 v215, v255 offset:3840\n" \
"ds_read_b32 v254, v255 offset:7936\n" \
"s_waitcnt lgkmcnt(0)\n" \
"v_readlane_b32 s4, v254, 0\n" \
"v_readlane_b32 s5, v254, 1\n" \
"v_readlane_b32 s6, v254, 2\n" \
"v_readlane_b32 s7, v254, 3\n" \
"v_readlane_b32 s8, v254, 4\n" \
"v_readlane_b32 s9, v254, 5\n" \
"v_readlane_b32 s10, v254, 6\n" \
"v_readlane_b32 s11, v254, 7\n" \
"v_readlane_b32 s12, v254, 8\n" \
"v_readlane_b32 s13, v254, 9\n" \
"v_readlane_b32 s14, v254, 10\n" \
"v_readlane_b32 s15, v254, 11\n" \
"v_readlane_b32 s16, v254, 12\n" \
"v_readlane_b32 s17, v254, 13\n" \
"v_readlane_b32 s18, v254, 14\n" \
"v_readlane_b32 s19, v254, 15\n" \
"v_readlane_b32 s20, v254, 16\n" \
"v_readlane_b32 s21, v254, 17\n" \
"v_readlane_b32 s22, v254, 18\n" \
"v_readlane_b32 s23, v254, 19\n" \
"v_readlane_b32 s24, v254, 20\n" \
"v_readlane_b32 s25, v254, 21\n" \
"v_readlane_b32 s26, v254, 22\n" \
"v_readlane_b32 s27, v254, 23\n" \
"v_readlane_b32 s28, v254, 24\n" \
"v_readlane_b32 s29, v254, 25\n" \
"v_readlane_b32 s30, v254, 26\n" \
"v_readlane_b32 s31, v254, 27\n" \
"v_readlane_b32 s32, v254, 28\n" \
"v_readlane_b32 s33, v254, 29\n" \
"v_readlane_b32 s34, v254, 30\n" \
"v_readlane_b32 s35, v254, 31\n" \
"v_readlane_b32 s36, v254, 32\n" \
"v_readlane_b32 s37, v254, 33\n" \
"v_readlane_b32 s38, v254, 34\n" \
"v_readlane_b32 s39, v254, 35\n" \
"s_mov_b32 s40, 0\n" \
"s_mov_b32 s51, 0\n" \
"s_nop 0\n" \
"v_mov_b32 v252, s38\n" \
"s_nop 4\n" \
"s_add_u32 s34, s33, 1\n" \
"s_add_u32 s36, s33, 2\n" \
"s_add_u32 s43, s30, s33\n" \
"s_mov_b32 s50, 0\n" \
".LS2_w13l:\n" \
"buffer_load_dword v240, v204, s[4:7], 0 offen sc1\n" \
"s_waitcnt vmcnt(0)\n" \
".LS2_w13c:\n" \
"v_subrev_u32 v241, s43, v240\n" \
"v_cmp_gt_u32 vcc, 0x100, v241\n" \
"s_cmp_eq_u64 vcc, -1\n" \
"s_cbranch_scc1 .LS2_w13d\n" \
"s_sleep 2\n" \
"s_add_u32 s50, s50, 1\n" \
"s_cmp_lt_u32 s50, 0x20000\n" \
"s_cbranch_scc1 .LS2_w13l\n" \
"s_branch .LS2_end\n" \
".LS2_w13d:\n" \
"v_readlane_b32 s43, v202, 8\n" \
"s_nop 4\n" \
"buffer_load_dwordx4 a[64:67], v201, s[8:11], s43 offen sc1\n" \
"buffer_load_dwordx4 a[68:71], v201, s[8:11], s43 offen offset:1024 sc1\n" \
"v_readlane_b32 s43, v202, 9\n" \
"s_nop 4\n" \
"buffer_load_dwordx4 a[72:75], v201, s[8:11], s43 offen sc1\n" \
"buffer_load_dwordx4 a[76:79], v201, s[8:11], s43 offen offset:1024 sc1\n" \
"v_readlane_b32 s43, v202, 10\n" \
"s_nop 4\n" \
"buffer_load_dwordx4 a[80:83], v201, s[8:11], s43 offen sc1\n" \
"buffer_load_dwordx4 a[84:87], v201, s[8:11], s43 offen offset:1024 sc1\n" \
"v_readlane_b32 s43, v202, 12\n" \
"s_nop 4\n" \
"buffer_load_dwordx4 a[96:99], v201, s[8:11], s43 offen sc1\n" \
"buffer_load_dwordx4 a[100:103], v201, s[8:11], s43 offen offset:1024 sc1\n" \
"v_readlane_b32 s43, v202, 14\n" \
"s_nop 4\n" \
"buffer_load_dwordx4 a[112:115], v201, s[8:11], s43 offen sc1\n" \
"buffer_load_dwordx4 a[116:119], v201, s[8:11], s43 offen offset:1024 sc1\n" \
"v_readlane_b32 s43, v202, 19\n" \
"s_nop 4\n" \
"buffer_load_dwordx4 a[152:155], v201, s[8:11], s43 offen sc1\n" \
"buffer_load_dwordx4 a[156:159], v201, s[8:11], s43 offen offset:1024 sc1\n" \
"v_readlane_b32 s43, v202, 22\n" \
"s_nop 4\n" \
"buffer_load_dwordx4 a[176:179], v201, s[8:11], s43 offen sc1\n" \
"buffer_load_dwordx4 a[180:183], v201, s[8:11], s43 offen offset:1024 sc1\n" \
"v_readlane_b32 s43, v202, 25\n" \
"s_nop 4\n" \
"buffer_load_dwordx4 a[200:203], v201, s[8:11], s43 offen sc1\n" \
"buffer_load_dwordx4 a[204:207], v201, s[8:11], s43 offen offset:1024 sc1\n" \
"s_add_u32 s43, s30, s34\n" \
"s_mov_b32 s50, 0\n" \
".LS2_w14l:\n" \
"buffer_load_dword v240, v204, s[4:7], 0 offen sc1\n" \
"s_waitcnt vmcnt(0)\n" \
".LS2_w14c:\n" \
"v_subrev_u32 v241, s43, v240\n" \
"v_cmp_gt_u32 vcc, 0x100, v241\n" \
"s_cmp_eq_u64 vcc, -1\n" \
"s_cbranch_scc1 .LS2_w14d\n" \
"s_sleep 2\n" \
"s_add_u32 s50, s50, 1\n" \
"s_cmp_lt_u32 s50, 0x20000\n" \
"s_cbranch_scc1 .LS2_w14l\n" \
"s_branch .LS2_end\n" \
".LS2_w14d:\n" \
"v_readlane_b32 s43, v202, 11\n" \
"s_nop 4\n" \
"buffer_load_dwordx4 a[88:91], v201, s[8:11], s43 offen sc1\n" \
"buffer_load_dwordx4 a[92:95], v201, s[8:11], s43 offen offset:1024 sc1\n" \
"v_readlane_b32 s43, v202, 13\n" \
"s_nop 4\n" \
"buffer_load_dwordx4 a[104:107], v201, s[8:11], s43 offen sc1\n" \
"buffer_load_dwordx4 a[108:111], v201, s[8:11], s43 offen offset:1024 sc1\n" \
"v_readlane_b32 s43, v202, 15\n" \
"s_nop 4\n" \
"buffer_load_dwordx4 a[120:123], v201, s[8:11], s43 offen sc1\n" \
"buffer_load_dwordx4 a[124:127], v201, s[8:11], s43 offen offset:1024 sc1\n" \
"v_readlane_b32 s43, v202, 16\n" \
"s_nop 4\n" \
"buffer_load_dwordx4 a[128:131], v201, s[8:11], s43 offen sc1\n" \
"buffer_load_dwordx4 a[132:135], v201, s[8:11], s43 offen offset:1024 sc1\n" \
"v_readlane_b32 s43, v202, 17\n" \
"s_nop 4\n" \
"buffer_load_dwordx4 a[136:139], v201, s[8:11], s43 offen sc1\n" \
"buffer_load_dwordx4 a[140:143], v201, s[8:11], s43 offen offset:1024 sc1\n" \
"v_readlane_b32 s43, v202, 20\n" \
"s_nop 4\n" \
"buffer_load_dwordx4 a[160:163], v201, s[8:11], s43 offen sc1\n" \
"buffer_load_dwordx4 a[164:167], v201, s[8:11], s43 offen offset:1024 sc1\n" \
"v_readlane_b32 s43, v202, 23\n" \
"s_nop 4\n" \
"buffer_load_dwordx4 a[184:187], v201, s[8:11], s43 offen sc1\n" \
"buffer_load_dwordx4 a[188:191], v201, s[8:11], s43 offen offset:1024 sc1\n" \
"v_readlane_b32 s43, v202, 26\n" \
"s_nop 4\n" \
"buffer_load_dwordx4 a[208:211], v201, s[8:11], s43 offen sc1\n" \
"buffer_load_dwordx4 a[212:215], v201, s[8:11], s43 offen offset:1024 sc1\n" \
"s_add_u32 s43, s30, s36\n" \
"s_mov_b32 s50, 0\n" \
".LS2_w15l:\n" \
"buffer_load_dword v240, v204, s[4:7], 0 offen sc1\n" \
"s_waitcnt vmcnt(0)\n" \
".LS2_w15c:\n" \
"v_subrev_u32 v241, s43, v240\n" \
"v_cmp_gt_u32 vcc, 0x100, v241\n" \
"s_cmp_eq_u64 vcc, -1\n" \
"s_cbranch_scc1 .LS2_w15d\n" \
"s_sleep 2\n" \
"s_add_u32 s50, s50, 1\n" \
"s_cmp_lt_u32 s50, 0x20000\n" \
"s_cbranch_scc1 .LS2_w15l\n" \
"s_branch .LS2_end\n" \
".LS2_w15d:\n" \
"v_readlane_b32 s43, v202, 0\n" \
"s_nop 4\n" \
"buffer_load_dwordx4 a[0:3], v201, s[8:11], s43 offen sc1\n" \
"buffer_load_dwordx4 a[4:7], v201, s[8:11], s43 offen offset:1024 sc1\n" \
"v_readlane_b32 s43, v202, 1\n" \
"s_nop 4\n" \
"buffer_load_dwordx4 a[8:11], v201, s[8:11], s43 offen sc1\n" \
"buffer_load_dwordx4 a[12:15], v201, s[8:11], s43 offen offset:1024 sc1\n" \
"v_readlane_b32 s43, v202, 2\n" \
"s_nop 4\n" \
"buffer_load_dwordx4 a[16:19], v201, s[8:11], s43 offen sc1\n" \
"buffer_load_dwordx4 a[20:23], v201, s[8:11], s43 offen offset:1024 sc1\n" \
"v_readlane_b32 s43, v202, 3\n" \
"s_nop 4\n" \
"buffer_load_dwordx4 a[24:27], v201, s[8:11], s43 offen sc1\n" \
"buffer_load_dwordx4 a[28:31], v201, s[8:11], s43 offen offset:1024 sc1\n" \
"v_readlane_b32 s43, v202, 4\n" \
"s_nop 4\n" \
"buffer_load_dwordx4 a[32:35], v201, s[8:11], s43 offen sc1\n" \
"buffer_load_dwordx4 a[36:39], v201, s[8:11], s43 offen offset:1024 sc1\n" \
"v_readlane_b32 s43, v202, 5\n" \
"s_nop 4\n" \
"buffer_load_dwordx4 a[40:43], v201, s[8:11], s43 offen sc1\n" \
"buffer_load_dwordx4 a[44:47], v201, s[8:11], s43 offen offset:1024 sc1\n" \
"v_readlane_b32 s43, v202, 6\n" \
"s_nop 4\n" \
"buffer_load_dwordx4 a[48:51], v201, s[8:11], s43 offen sc1\n" \
"buffer_load_dwordx4 a[52:55], v201, s[8:11], s43 offen offset:1024 sc1\n" \
"v_readlane_b32 s43, v202, 7\n" \
"s_nop 4\n" \
"buffer_load_dwordx4 a[56:59], v201, s[8:11], s43 offen sc1\n" \
"buffer_load_dwordx4 a[60:63], v201, s[8:11], s43 offen offset:1024 sc1\n" \
"s_add_u32 s43, s30, 4\n" \
"s_mov_b32 s50, 0\n" \
".LS2_w16l:\n" \
"buffer_load_dword v240, v204, s[4:7], 0 offen sc1\n" \
"s_waitcnt vmcnt(0)\n" \
".LS2_w16c:\n" \
"v_subrev_u32 v241, s43, v240\n" \
"v_cmp_gt_u32 vcc, 0x100, v241\n" \
"s_cmp_eq_u64 vcc, -1\n" \
"s_cbranch_scc1 .LS2_w16d\n" \
"s_sleep 2\n" \
"s_add_u32 s50, s50, 1\n" \
"s_cmp_lt_u32 s50, 0x20000\n" \
"s_cbranch_scc1 .LS2_w16l\n" \
"s_branch .LS2_end\n" \
".LS2_w16d:\n" \
"v_readlane_b32 s43, v202, 18\n" \
"s_nop 4\n" \
"buffer_load_dwordx4 a[144:147], v201, s[8:11], s43 offen sc1\n" \
"buffer_load_dwordx4 a[148:151], v201, s[8:11], s43 offen offset:1024 sc1\n" \
"v_readlane_b32 s43, v202, 21\n" \
"s_nop 4\n" \
"buffer_load_dwordx4 a[168:171], v201, s[8:11], s43 offen sc1\n" \
"buffer_load_dwordx4 a[172:175], v201, s[8:11], s43 offen offset:1024 sc1\n" \
"v_readlane_b32 s43, v202, 24\n" \
"s_nop 4\n" \
"buffer_load_dwordx4 a[192:195], v201, s[8:11], s43 offen sc1\n" \
"buffer_load_dwordx4 a[196:199], v201, s[8:11], s43 offen offset:1024 sc1\n" \
"v_readlane_b32 s43, v202, 27\n" \
"s_nop 4\n" \
"buffer_load_dwordx4 a[216:219], v201, s[8:11], s43 offen sc1\n" \
"buffer_load_dwordx4 a[220:223], v201, s[8:11], s43 offen offset:1024 sc1\n" \
"v_mov_b32 v188, -1\n" \
"s_add_u32 s43, s30, 1\n" \
"s_mov_b32 s50, 0\n" \
".LS2_w17l:\n" \
"buffer_load_dword v240, v205, s[4:7], 0 offen sc1\n" \
"s_waitcnt vmcnt(0)\n" \
".LS2_w17c:\n" \
"v_subrev_u32 v241, s43, v240\n" \
"v_cmp_gt_u32 vcc, 0x100, v241\n" \
"s_cmp_eq_u64 vcc, -1\n" \
"s_cbranch_scc1 .LS2_w17d\n" \
"s_sleep 2\n" \
"s_add_u32 s50, s50, 1\n" \
"s_cmp_lt_u32 s50, 0x20000\n" \
"s_cbranch_scc1 .LS2_w17l\n" \
"s_branch .LS2_end\n" \
".LS2_w17d:\n" \
"s_add_u32 s43, s30, 1\n" \
"s_mov_b32 s50, 0\n" \
".LS2_w18l:\n" \
"buffer_load_dword v240, v206, s[4:7], 0 offen sc1\n" \
"s_waitcnt vmcnt(0)\n" \
".LS2_w18c:\n" \
"v_subrev_u32 v241, s43, v240\n" \
"v_cmp_gt_u32 vcc, 0x100, v241\n" \
"s_cmp_eq_u64 vcc, -1\n" \
"s_cbranch_scc1 .LS2_w18d\n" \
"s_sleep 2\n" \
"s_add_u32 s50, s50, 1\n" \
"s_cmp_lt_u32 s50, 0x20000\n" \
"s_cbranch_scc1 .LS2_w18l\n" \
"s_branch .LS2_end\n" \
".LS2_w18d:\n" \
"ds_read_b96 v[248:250], v252\n" \
"s_waitcnt lgkmcnt(0)\n" \
"v_readfirstlane_b32 s44, v248\n" \
"v_readfirstlane_b32 s45, v249\n" \
"v_readfirstlane_b32 s46, v250\n" \
"s_mul_i32 s42, s25, s46\n" \
"s_lshr_b32 s42, s42, 12\n" \
".p2align 3\n" \
"s_memtime s[54:55]\n" \
"s_memrealtime s[56:57]\n" \
"s_memtime s[58:59]\n" \
"s_memrealtime s[60:61]\n" \
"s_nop 0\n" \
"s_nop 0\n" \
"s_memtime s[62:63]\n" \
"s_memrealtime s[64:65]\n" \
"s_memtime s[66:67]\n" \
"s_memrealtime s[68:69]\n" \
"s_memtime s[70:71]\n" \
"s_memrealtime s[72:73]\n" \
"s_memtime s[74:75]\n" \
"s_memrealtime s[76:77]\n" \
"s_nop 0\n" \
"s_nop 0\n" \
"s_memtime s[78:79]\n" \
"s_memrealtime s[80:81]\n" \
"s_memtime s[82:83]\n" \
"s_memrealtime s[84:85]\n" \
"s_memtime s[86:87]\n" \
"s_memrealtime s[88:89]\n" \
"s_memtime s[90:91]\n" \
"s_memrealtime s[92:93]\n" \
"s_nop 0\n" \
"s_nop 0\n" \
"s_memtime s[94:95]\n" \
"s_memrealtime s[96:97]\n" \
"s_memtime s[98:99]\n" \
"s_mov_b32 s52, 0\n" \
"s_mov_b32 s53, 4096\n" \
"s_waitcnt lgkmcnt(0)\n" \
"s_cmp_lg_u32 s96, s56\n" \
"s_cselect_b32 s52, s90, s52\n" \
"s_cselect_b32 s53, s98, s53\n" \
"s_cmp_lg_u32 s92, s56\n" \
"s_cselect_b32 s52, s86, s52\n" \
"s_cselect_b32 s53, s94, s53\n" \
"s_cmp_lg_u32 s88, s56\n" \
"s_cselect_b32 s52, s82, s52\n" \
"s_cselect_b32 s53, s90, s53\n" \
"s_cmp_lg_u32 s84, s56\n" \
"s_cselect_b32 s52, s78, s52\n" \
"s_cselect_b32 s53, s86, s53\n" \
"s_cmp_lg_u32 s80, s56\n" \
"s_cselect_b32 s52, s74, s52\n" \
"s_cselect_b32 s53, s82, s53\n" \
"s_cmp_lg_u32 s76, s56\n" \
"s_cselect_b32 s52, s70, s52\n" \
"s_cselect_b32 s53, s78, s53\n" \
"s_cmp_lg_u32 s72, s56\n" \
"s_cselect_b32 s52, s66, s52\n" \
"s_cselect_b32 s53, s74, s53\n" \
"s_cmp_lg_u32 s68, s56\n" \
"s_cselect_b32 s52, s62, s52\n" \
"s_cselect_b32 s53, s70, s53\n" \
"s_cmp_lg_u32 s64, s56\n" \
"s_cselect_b32 s52, s58, s52\n" \
"s_cselect_b32 s53, s66, s53\n" \
"s_cmp_lg_u32 s60, s56\n" \
"s_cselect_b32 s52, s54, s52\n" \
"s_cselect_b32 s53, s62, s53\n" \
"s_sub_u32 s53, s53, s52\n" \
"s_lshr_b32 s55, s53, 1\n" \
"s_add_u32 s52, s52, s55\n" \
"s_add_u32 s55, s56, 4\n" \
"s_lshl_b32 s55, s55, 4\n" \
"s_cmp_le_u32 s53, 40\n" \
"s_cselect_b32 s44, s55, s44\n" \
"s_cselect_b32 s45, s52, s45\n" \
"v_mov_b32 v248, s44\n" \
"v_mov_b32 v249, s45\n" \
"v_mov_b32 v250, s46\n" \
"s_mov_b64 exec, 1\n" \
"ds_write_b96 v252, v[248:250]\n" \
"s_mov_b64 exec, -1\n" \
".p2align 3\n" \
"s_memtime s[54:55]\n" \
"s_memrealtime s[56:57]\n" \
"s_memtime s[58:59]\n" \
"s_memrealtime s[60:61]\n" \
"s_nop 0\n" \
"s_nop 0\n" \
"s_memtime s[62:63]\n" \
"s_memrealtime s[64:65]\n" \
"s_memtime s[66:67]\n" \
"s_memrealtime s[68:69]\n" \
"s_memtime s[70:71]\n" \
"s_memrealtime s[72:73]\n" \
"s_memtime s[74:75]\n" \
"s_memrealtime s[76:77]\n" \
"s_nop 0\n" \
"s_nop 0\n" \
"s_memtime s[78:79]\n" \
"s_memrealtime s[80:81]\n" \
"s_memtime s[82:83]\n" \
"s_memrealtime s[84:85]\n" \
"s_memtime s[86:87]\n" \
"s_memrealtime s[88:89]\n" \
"s_memtime s[90:91]\n" \
"s_memrealtime s[92:93]\n" \
"s_nop 0\n" \
"s_nop 0\n" \
"s_memtime s[94:95]\n" \
"s_memrealtime s[96:97]\n" \
"s_memtime s[98:99]\n" \
"s_mov_b32 s52, 0\n" \
"s_mov_b32 s53, 4096\n" \
"s_waitcnt lgkmcnt(0)\n" \
"s_cmp_lg_u32 s96, s56\n" \
"s_cselect_b32 s52, s90, s52\n" \
"s_cselect_b32 s53, s98, s53\n" \
"s_cmp_lg_u32 s92, s56\n" \
"s_cselect_b32 s52, s86, s52\n" \
"s_cselect_b32 s53, s94, s53\n" \
"s_cmp_lg_u32 s88, s56\n" \
"s_cselect_b32 s52, s82, s52\n" \
"s_cselect_b32 s53, s90, s53\n" \
"s_cmp_lg_u32 s84, s56\n" \
"s_cselect_b32 s52, s78, s52\n" \
"s_cselect_b32 s53, s86, s53\n" \
"s_cmp_lg_u32 s80, s56\n" \
"s_cselect_b32 s52, s74, s52\n" \
"s_cselect_b32 s53, s82, s53\n" \
"s_cmp_lg_u32 s76, s56\n" \
"s_cselect_b32 s52, s70, s52\n" \
"s_cselect_b32 s53, s78, s53\n" \
"s_cmp_lg_u32 s72, s56\n" \
"s_cselect_b32 s52, s66, s52\n" \
"s_cselect_b32 s53, s74, s53\n" \
"s_cmp_lg_u32 s68, s56\n" \
"s_cselect_b32 s52, s62, s52\n" \
"s_cselect_b32 s53, s70, s53\n" \
"s_cmp_lg_u32 s64, s56\n" \
"s_cselect_b32 s52, s58, s52\n" \
"s_cselect_b32 s53, s66, s53\n" \
"s_cmp_lg_u32 s60, s56\n" \
"s_cselect_b32 s52, s54, s52\n" \
"s_cselect_b32 s53, s62, s53\n" \
"s_sub_u32 s53, s53, s52\n" \
"s_lshr_b32 s55, s53, 1\n" \
"s_add_u32 s52, s52, s55\n" \
"s_add_u32 s55, s56, 4\n" \
"s_lshl_b32 s55, s55, 4\n" \
"s_cmp_le_u32 s53, 40\n" \
"s_cselect_b32 s44, s55, s44\n" \
"s_cselect_b32 s45, s52, s45\n" \
"v_mov_b32 v248, s44\n" \
"v_mov_b32 v249, s45\n" \
"v_mov_b32 v250, s46\n" \
"s_mov_b64 exec, 1\n" \
"ds_write_b96 v252, v[248:250]\n" \
"s_mov_b64 exec, -1\n" \
"s_waitcnt lgkmcnt(0)\n" \
"s_barrier\n" \
"ds_read_b96 v[248:250], v252\n" \
"s_waitcnt lgkmcnt(0)\n" \
"v_readfirstlane_b32 s44, v248\n" \
"v_readfirstlane_b32 s45, v249\n" \
"v_readfirstlane_b32 s46, v250\n" \
"s_mul_i32 s42, s25, s46\n" \
"s_lshr_b32 s42, s42, 12\n" \
".LS2_loop:\n" \
"s_add_u32 s43, s40, 0\n" \
"s_and_b32 s43, s43, 3\n" \
"s_bitcmp1_b32 s43, 0\n" \
"s_cselect_b32 s47, s32, s31\n" \
"s_lshl_b32 s43, s43, 19\n" \
"s_add_u32 s47, s47, s43\n" \
"s_sub_u32 s43, s24, s44\n" \
"s_max_i32 s43, s43, 0xfffd0000\n" \
"s_min_i32 s43, s43, 0x4000\n" \
"s_mul_i32 s43, s43, s46\n" \
"s_ashr_i32 s43, s43, 12\n" \
"s_add_u32 s41, s45, s43\n" \
"s_add_u32 s41, s41, 8\n" \
"s_branch .LS2_a1\n" \
".LS2_retry:\n" \
"s_add_u32 s51, s51, 1\n" \
"s_cmp_gt_u32 s51, 0x20000\n" \
"s_cbranch_scc1 .LS2_end\n" \
".LS2_a0:\n" \
"s_add_u32 s41, s41, s42\n" \
"s_add_u32 s24, s24, s25\n" \
"s_xor_b32 s31, s31, s29\n" \
"s_xor_b32 s32, s32, s29\n" \
"s_xor_b32 s47, s47, s29\n" \
"s_xor_b32 s48, s48, s29\n" \
"s_add_u32 s39, s39, 1\n" \
".p2align 6\n" \
".LS2_a1:\n" \
"s_memtime vcc\n" \
"s_waitcnt lgkmcnt(0)\n" \
"s_sub_u32 s43, s41, vcc_lo\n" \
"s_cmp_gt_i32 s43, 128\n" \
"s_cbranch_scc1 .LS2_a1\n" \
"s_cmp_lt_i32 s43, s37\n" \
"s_cbranch_scc1 .LS2_a0\n" \
"s_max_i32 s43, s43, 0\n" \
"s_and_b32 s43, s43, -4\n" \
"s_getpc_b64 vcc\n" \
".LS2_a3:\n" \
"s_sub_u32 s43, .LS2_a4-.LS2_a3, s43\n" \
"s_add_u32 vcc_lo, vcc_lo, s43\n" \
"s_addc_u32 vcc_hi, vcc_hi, 0\n" \
"s_setpc_b64 vcc\n" \
".rept 32\n" \
"s_nop 0\n" \
".endr\n" \
".LS2_a4:\n" \
"buffer_load_dwordx4 v[0:3], v208, s[4:7], s47 offen offset:2048 sc1\n" \
"s_nop 9\n" \
"buffer_load_dwordx4 v[4:7], v208, s[4:7], s47 offen sc1\n" \
"s_nop 9\n" \
"buffer_load_dwordx4 v[8:11], v208, s[4:7], s47 offen offset:1024 sc1\n" \
"s_nop 9\n" \
"buffer_load_dwordx4 v[12:15], v210, s[4:7], s47 offen sc1\n" \
"s_add_u32 s35, s35, 1\n" \
"v_mov_b32 v28, 0\n" \
"v_mov_b32 v29, 0\n" \
"v_mov_b32 v30, 0\n" \
"v_mov_b32 v31, 0\n" \
"v_mov_b32 v32, 0\n" \
"v_mov_b32 v33, 0\n" \
"v_mov_b32 v34, 0\n" \
"v_mov_b32 v35, 0\n" \
"v_mov_b32 v36, 0\n" \
"v_mov_b32 v37, 0\n" \
"v_mov_b32 v38, 0\n" \
"v_mov_b32 v39, 0\n" \
"v_mov_b32 v40, 0\n" \
"v_mov_b32 v41, 0\n" \
"v_mov_b32 v42, 0\n" \
"v_mov_b32 v43, 0\n" \
"v_mov_b32 v44, 0\n" \
"v_mov_b32 v45, 0\n" \
"v_mov_b32 v46, 0\n" \
"v_mov_b32 v47, 0\n" \
"v_mov_b32 v48, 0\n" \
"v_mov_b32 v49, 0\n" \
"v_mov_b32 v50, 0\n" \
"v_mov_b32 v51, 0\n" \
"v_mov_b32 v52, 0\n" \
"v_mov_b32 v53, 0\n" \
"v_mov_b32 v54, 0\n" \
"v_mov_b32 v55, 0\n" \
"v_mov_b32 v56, 0\n" \
"v_mov_b32 v57, 0\n" \
"v_mov_b32 v58, 0\n" \
"v_mov_b32 v59, 0\n" \
"s_waitcnt vmcnt(3)\n" \
"v_smfmac_f32_16x16x64_bf16 v[28:31], v[0:3], a[0:7], v200\n" \
"v_smfmac_f32_16x16x64_bf16 v[32:35], v[0:3], a[8:15], v200\n" \
"v_smfmac_f32_16x16x64_bf16 v[36:39], v[0:3], a[16:23], v200\n" \
"v_smfmac_f32_16x16x64_bf16 v[40:43], v[0:3], a[24:31], v200\n" \
"v_smfmac_f32_16x16x64_bf16 v[44:47], v[0:3], a[32:39], v200\n" \
"v_smfmac_f32_16x16x64_bf16 v[48:51], v[0:3], a[40:47], v200\n" \
"v_smfmac_f32_16x16x64_bf16 v[52:55], v[0:3], a[48:55], v200\n" \
"v_smfmac_f32_16x16x64_bf16 v[56:59], v[0:3], a[56:63], v200\n" \
"s_waitcnt vmcnt(2)\n" \
"v_smfmac_f32_16x16x64_bf16 v[28:31], v[4:7], a[64:71], v200\n" \
"v_smfmac_f32_16x16x64_bf16 v[32:35], v[4:7], a[72:79], v200\n" \
"v_smfmac_f32_16x16x64_bf16 v[36:39], v[4:7], a[80:87], v200\n" \
"s_waitcnt vmcnt(1)\n" \
"v_smfmac_f32_16x16x64_bf16 v[28:31], v[8:11], a[88:95], v200\n" \
"s_nop 2\n" \
"v_smfmac_f32_16x16x64_bf16 v[40:43], v[4:7], a[96:103], v200\n" \
"s_nop 2\n" \
"v_smfmac_f32_16x16x64_bf16 v[32:35], v[8:11], a[104:111], v200\n" \
"s_nop 0\n" \
"v_cvt_pk_bf16_f32 v128, v28, v29\n" \
"v_cvt_pk_bf16_f32 v129, v30, v31\n" \
"v_smfmac_f32_16x16x64_bf16 v[44:47], v[4:7], a[112:119], v200\n" \
"ds_write_b64 v211, v[128:129] offset:256\n" \
"s_nop 1\n" \
"v_smfmac_f32_16x16x64_bf16 v[36:39], v[8:11], a[120:127], v200\n" \
"s_nop 0\n" \
"v_cvt_pk_bf16_f32 v130, v32, v33\n" \
"v_cvt_pk_bf16_f32 v131, v34, v35\n" \
"v_smfmac_f32_16x16x64_bf16 v[44:47], v[8:11], a[128:135], v200\n" \
"ds_write_b64 v211, v[130:131] offset:2432\n" \
"s_nop 1\n" \
"v_smfmac_f32_16x16x64_bf16 v[40:43], v[8:11], a[136:143], v200\n" \
"s_waitcnt vmcnt(0)\n" \
"v_cvt_pk_bf16_f32 v132, v36, v37\n" \
"v_cvt_pk_bf16_f32 v133, v38, v39\n" \
"v_smfmac_f32_16x16x64_bf16 v[44:47], v[12:15], a[144:151], v200\n" \
"ds_write_b64 v211, v[132:133] offset:4608\n" \
"s_nop 1\n" \
"v_smfmac_f32_16x16x64_bf16 v[48:51], v[4:7], a[152:159], v200\n" \
"s_nop 0\n" \
"v_cvt_pk_bf16_f32 v134, v40, v41\n" \
"v_cvt_pk_bf16_f32 v135, v42, v43\n" \
"v_smfmac_f32_16x16x64_bf16 v[48:51], v[8:11], a[160:167], v200\n" \
"ds_write_b64 v211, v[134:135] offset:4992\n" \
"v_cvt_pk_bf16_f32 v136, v44, v45\n" \
"v_cvt_pk_bf16_f32 v137, v46, v47\n" \
"v_smfmac_f32_16x16x64_bf16 v[48:51], v[12:15], a[168:175], v200\n" \
"ds_write_b64 v211, v[136:137] offset:9472\n" \
"s_nop 1\n" \
"v_smfmac_f32_16x16x64_bf16 v[52:55], v[4:7], a[176:183], v200\n" \
"s_nop 2\n" \
"v_smfmac_f32_16x16x64_bf16 v[52:55], v[8:11], a[184:191], v200\n" \
"s_nop 0\n" \
"v_cvt_pk_bf16_f32 v138, v48, v49\n" \
"v_cvt_pk_bf16_f32 v139, v50, v51\n" \
"v_smfmac_f32_16x16x64_bf16 v[52:55], v[12:15], a[192:199], v200\n" \
"ds_write_b64 v211, v[138:139] offset:11648\n" \
"s_waitcnt lgkmcnt(2)\n" \
"s_barrier\n" \
"v_smfmac_f32_16x16x64_bf16 v[56:59], v[4:7], a[200:207], v200\n" \
"s_nop 2\n" \
"v_smfmac_f32_16x16x64_bf16 v[56:59], v[8:11], a[208:215], v200\n" \
"s_nop 2\n" \
"v_smfmac_f32_16x16x64_bf16 v[56:59], v[12:15], a[216:223], v200\n" \
"v_cvt_pk_bf16_f32 v140, v52, v53\n" \
"v_cvt_pk_bf16_f32 v141, v54, v55\n" \
"s_nop 1\n" \
"ds_write_b64 v211, v[140:141] offset:13824\n" \
"s_nop 2\n" \
"v_cvt_pk_bf16_f32 v142, v56, v57\n" \
"v_cvt_pk_bf16_f32 v143, v58, v59\n" \
"ds_write_b64 v211, v[142:143] offset:14208\n" \
"s_waitcnt lgkmcnt(2)\n" \
"s_barrier\n" \
"s_waitcnt lgkmcnt(1)\n" \
"s_barrier\n" \
"s_waitcnt lgkmcnt(0)\n" \
"s_barrier\n" \
"s_nop 3\n" \
"ds_read_b32 v64, v252 offset:20\n" \
"s_waitcnt lgkmcnt(0)\n" \
"v_readfirstlane_b32 s43, v64\n" \
"s_cmp_eq_u32 s43, s35\n" \
"s_cbranch_scc1 .LS2_retry\n" \
"s_nop 15\n" \
"v_max3_u32 v64, v0, v0, v1\n" \
"v_max3_u32 v64, v64, v2, v3\n" \
"v_max3_u32 v64, v64, v4, v5\n" \
"v_max3_u32 v64, v64, v6, v7\n" \
"v_max3_u32 v64, v64, v8, v9\n" \
"v_max3_u32 v64, v64, v10, v11\n" \
"v_max3_u32 v64, v64, v12, v13\n" \
"v_max3_u32 v64, v64, v14, v15\n" \
"v_cmp_eq_u32 vcc, -1, v64\n" \
"s_cbranch_vccnz .LS2_veto\n" \
".LS2_back:\n" \
"s_waitcnt lgkmcnt(0)\n" \
"s_barrier\n" \
"ds_read_b32 v64, v252 offset:16\n" \
"s_waitcnt lgkmcnt(0)\n" \
"v_readfirstlane_b32 s43, v64\n" \
"s_cmp_eq_u32 s43, s35\n" \
"s_cbranch_scc1 .LS2_retry\n" \
".p2align 3\n" \
"s_memtime s[54:55]\n" \
"s_memrealtime s[56:57]\n" \
"s_memtime s[58:59]\n" \
"s_memrealtime s[60:61]\n" \
"s_nop 0\n" \
"s_nop 0\n" \
"s_memtime s[62:63]\n" \
"s_memrealtime s[64:65]\n" \
"s_memtime s[66:67]\n" \
"s_memrealtime s[68:69]\n" \
"s_memtime s[70:71]\n" \
"s_memrealtime s[72:73]\n" \
"s_memtime s[74:75]\n" \
"s_memrealtime s[76:77]\n" \
"s_nop 0\n" \
"s_nop 0\n" \
"s_memtime s[78:79]\n" \
"s_memrealtime s[80:81]\n" \
"s_memtime s[82:83]\n" \
"s_memrealtime s[84:85]\n" \
"s_memtime s[86:87]\n" \
"s_memrealtime s[88:89]\n" \
"s_memtime s[90:91]\n" \
"s_memrealtime s[92:93]\n" \
"s_nop 0\n" \
"s_nop 0\n" \
"s_memtime s[94:95]\n" \
"s_memrealtime s[96:97]\n" \
"s_memtime s[98:99]\n" \
"s_mov_b32 s52, 0\n" \
"s_mov_b32 s53, 4096\n" \
"s_waitcnt lgkmcnt(0)\n" \
"s_cmp_lg_u32 s96, s56\n" \
"s_cselect_b32 s52, s90, s52\n" \
"s_cselect_b32 s53, s98, s53\n" \
"s_cmp_lg_u32 s92, s56\n" \
"s_cselect_b32 s52, s86, s52\n" \
"s_cselect_b32 s53, s94, s53\n" \
"s_cmp_lg_u32 s88, s56\n" \
"s_cselect_b32 s52, s82, s52\n" \
"s_cselect_b32 s53, s90, s53\n" \
"s_cmp_lg_u32 s84, s56\n" \
"s_cselect_b32 s52, s78, s52\n" \
"s_cselect_b32 s53, s86, s53\n" \
"s_cmp_lg_u32 s80, s56\n" \
"s_cselect_b32 s52, s74, s52\n" \
"s_cselect_b32 s53, s82, s53\n" \
"s_cmp_lg_u32 s76, s56\n" \
"s_cselect_b32 s52, s70, s52\n" \
"s_cselect_b32 s53, s78, s53\n" \
"s_cmp_lg_u32 s72, s56\n" \
"s_cselect_b32 s52, s66, s52\n" \
"s_cselect_b32 s53, s74, s53\n" \
"s_cmp_lg_u32 s68, s56\n" \
"s_cselect_b32 s52, s62, s52\n" \
"s_cselect_b32 s53, s70, s53\n" \
"s_cmp_lg_u32 s64, s56\n" \
"s_cselect_b32 s52, s58, s52\n" \
"s_cselect_b32 s53, s66, s53\n" \
"s_cmp_lg_u32 s60, s56\n" \
"s_cselect_b32 s52, s54, s52\n" \
"s_cselect_b32 s53, s62, s53\n" \
"s_sub_u32 s53, s53, s52\n" \
"s_lshr_b32 s55, s53, 1\n" \
"s_add_u32 s52, s52, s55\n" \
"s_add_u32 s55, s56, 4\n" \
"s_lshl_b32 s55, s55, 4\n" \
"s_sub_u32 s59, s55, s44\n" \
"s_cmp_lt_u32 s59, 2600\n" \
"s_cselect_b32 s63, 1, 3\n" \
"s_mul_i32 s59, s59, s46\n" \
"s_ashr_i32 s59, s59, 12\n" \
"s_add_u32 s59, s59, s45\n" \
"s_sub_u32 s59, s52, s59\n" \
"s_max_i32 s59, s59, -96\n" \
"s_min_i32 s59, s59, 96\n" \
"s_ashr_i32 s63, s59, s63\n" \
"s_add_u32 s63, s63, s46\n" \
"s_max_i32 s63, s63, 0x800\n" \
"s_min_i32 s63, s63, 0x2000\n" \
"s_cmp_le_u32 s53, 40\n" \
"s_cselect_b32 s44, s55, s44\n" \
"s_cselect_b32 s45, s52, s45\n" \
"s_cselect_b32 s46, s63, s46\n" \
"v_mov_b32 v248, s44\n" \
"v_mov_b32 v249, s45\n" \
"v_mov_b32 v250, s46\n" \
"s_mov_b64 exec, 1\n" \
"ds_write_b96 v252, v[248:250]\n" \
"s_mov_b64 exec, -1\n" \
"s_waitcnt lgkmcnt(0)\n" \
"s_barrier\n" \
"ds_read_b64 v[246:247], v252 offset:24\n" \
"ds_read_b96 v[248:250], v252\n" \
"s_waitcnt lgkmcnt(0)\n" \
"v_readfirstlane_b32 s44, v248\n" \
"v_readfirstlane_b32 s45, v249\n" \
"v_readfirstlane_b32 s46, v250\n" \
"v_readfirstlane_b32 s26, v246\n" \
"v_readfirstlane_b32 s27, v247\n" \
"s_mul_i32 s42, s25, s46\n" \
"s_lshr_b32 s42, s42, 12\n" \
"s_add_u32 s40, s40, 1\n" \
"s_add_u32 s24, s24, s26\n" \
"s_add_u32 s24, s24, s27\n" \
"s_cmp_lt_u32 s40, 63\n" \
"s_cbranch_scc1 .LS2_loop\n" \
".LS2_end:\n" \
"s_endpgm\n" \
".LS2_veto:\n" \
"v_mov_b32 v64, s35\n" \
"ds_write_b32 v252, v64 offset:16\n" \
"s_branch .LS2_back\n" \
".p2align 6\n" \
"fk_role_S3:\n" \
"ds_read_b32 v200, v255 offset:0\n" \
"ds_read_b32 v201, v255 offset:256\n" \
"ds_read_b32 v202, v255 offset:512\n" \
"ds_read_b32 v203, v255 offset:768\n" \
"ds_read_b32 v204, v255 offset:1024\n" \
"ds_read_b32 v205, v255 offset:1280\n" \
"ds_read_b32 v206, v255 offset:1536\n" \
"ds_read_b32 v207, v255 offset:1792\n" \
"ds_read_b32 v208, v255 offset:2048\n" \
"ds_read_b32 v209, v255 offset:2304\n" \
"ds_read_b32 v210, v255 offset:2560\n" \
"ds_read_b32 v211, v255 offset:2816\n" \
"ds_read_b32 v212, v255 offset:3072\n" \
"ds_read_b32 v213, v255 offset:3328\n" \
"ds_read_b32 v214, v255 offset:3584\n" \
"ds_read_b32 v215, v255 offset:3840\n" \
"ds_read_b32 v254, v255 offset:7936\n" \
"s_waitcnt lgkmcnt(0)\n" \
"v_readlane_b32 s4, v254, 0\n" \
"v_readlane_b32 s5, v254, 1\n" \
"v_readlane_b32 s6, v254, 2\n" \
"v_readlane_b32 s7, v254, 3\n" \
"v_readlane_b32 s8, v254, 4\n" \
"v_readlane_b32 s9, v254, 5\n" \
"v_readlane_b32 s10, v254, 6\n" \
"v_readlane_b32 s11, v254, 7\n" \
"v_readlane_b32 s12, v254, 8\n" \
"v_readlane_b32 s13, v254, 9\n" \
"v_readlane_b32 s14, v254, 10\n" \
"v_readlane_b32 s15, v254, 11\n" \
"v_readlane_b32 s16, v254, 12\n" \
"v_readlane_b32 s17, v254, 13\n" \
"v_readlane_b32 s18, v254, 14\n" \
"v_readlane_b32 s19, v254, 15\n" \
"v_readlane_b32 s20, v254, 16\n" \
"v_readlane_b32 s21, v254, 17\n" \
"v_readlane_b32 s22, v254, 18\n" \
"v_readlane_b32 s23, v254, 19\n" \
"v_readlane_b32 s24, v254, 20\n" \
"v_readlane_b32 s25, v254, 21\n" \
"v_readlane_b32 s26, v254, 22\n" \
"v_readlane_b32 s27, v254, 23\n" \
"v_readlane_b32 s28, v254, 24\n" \
"v_readlane_b32 s29, v254, 25\n" \
"v_readlane_b32 s30, v254, 26\n" \
"v_readlane_b32 s31, v254, 27\n" \
"v_readlane_b32 s32, v254, 28\n" \
"v_readlane_b32 s33, v254, 29\n" \
"v_readlane_b32 s34, v254, 30\n" \
"v_readlane_b32 s35, v254, 31\n" \
"v_readlane_b32 s36, v254, 32\n" \
"v_readlane_b32 s37, v254, 33\n" \
"v_readlane_b32 s38, v254, 34\n" \
"v_readlane_b32 s39, v254, 35\n" \
"s_mov_b32 s40, 0\n" \
"s_mov_b32 s51, 0\n" \
"s_nop 0\n" \
"v_mov_b32 v252, s38\n" \
"s_nop 4\n" \
"s_add_u32 s34, s33, 1\n" \
"s_add_u32 s36, s33, 2\n" \
"s_add_u32 s43, s30, s33\n" \
"s_mov_b32 s50, 0\n" \
".LS3_w19l:\n" \
"buffer_load_dword v240, v204, s[4:7], 0 offen sc1\n" \
"s_waitcnt vmcnt(0)\n" \
".LS3_w19c:\n" \
"v_subrev_u32 v241, s43, v240\n" \
"v_cmp_gt_u32 vcc, 0x100, v241\n" \
"s_cmp_eq_u64 vcc, -1\n" \
"s_cbranch_scc1 .LS3_w19d\n" \
"s_sleep 2\n" \
"s_add_u32 s50, s50, 1\n" \
"s_cmp_lt_u32 s50, 0x20000\n" \
"s_cbranch_scc1 .LS3_w19l\n" \
"s_branch .LS3_end\n" \
".LS3_w19d:\n" \
"v_readlane_b32 s43, v202, 2\n" \
"s_nop 4\n" \
"buffer_load_dwordx4 a[16:19], v201, s[8:11], s43 offen sc1\n" \
"buffer_load_dwordx4 a[20:23], v201, s[8:11], s43 offen offset:1024 sc1\n" \
"v_readlane_b32 s43, v202, 3\n" \
"s_nop 4\n" \
"buffer_load_dwordx4 a[24:27], v201, s[8:11], s43 offen sc1\n" \
"buffer_load_dwordx4 a[28:31], v201, s[8:11], s43 offen offset:1024 sc1\n" \
"v_readlane_b32 s43, v202, 4\n" \
"s_nop 4\n" \
"buffer_load_dwordx4 a[32:35], v201, s[8:11], s43 offen sc1\n" \
"buffer_load_dwordx4 a[36:39], v201, s[8:11], s43 offen offset:1024 sc1\n" \
"v_readlane_b32 s43, v202, 5\n" \
"s_nop 4\n" \
"buffer_load_dwordx4 a[40:43], v201, s[8:11], s43 offen sc1\n" \
"buffer_load_dwordx4 a[44:47], v201, s[8:11], s43 offen offset:1024 sc1\n" \
"s_add_u32 s43, s30, s34\n" \
"s_mov_b32 s50, 0\n" \
".LS3_w20l:\n" \
"buffer_load_dword v240, v204, s[4:7], 0 offen sc1\n" \
"s_waitcnt vmcnt(0)\n" \
".LS3_w20c:\n" \
"v_subrev_u32 v241, s43, v240\n" \
"v_cmp_gt_u32 vcc, 0x100, v241\n" \
"s_cmp_eq_u64 vcc, -1\n" \
"s_cbranch_scc1 .LS3_w20d\n" \
"s_sleep 2\n" \
"s_add_u32 s50, s50, 1\n" \
"s_cmp_lt_u32 s50, 0x20000\n" \
"s_cbranch_scc1 .LS3_w20l\n" \
"s_branch .LS3_end\n" \
".LS3_w20d:\n" \
"v_readlane_b32 s43, v202, 6\n" \
"s_nop 4\n" \
"buffer_load_dwordx4 a[48:51], v201, s[8:11], s43 offen sc1\n" \
"buffer_load_dwordx4 a[52:55], v201, s[8:11], s43 offen offset:1024 sc1\n" \
"v_readlane_b32 s43, v202, 7\n" \
"s_nop 4\n" \
"buffer_load_dwordx4 a[56:59], v201, s[8:11], s43 offen sc1\n" \
"buffer_load_dwordx4 a[60:63], v201, s[8:11], s43 offen offset:1024 sc1\n" \
"v_readlane_b32 s43, v202, 8\n" \
"s_nop 4\n" \
"buffer_load_dwordx4 a[64:67], v201, s[8:11], s43 offen sc1\n" \
"buffer_load_dwordx4 a[68:71], v201, s[8:11], s43 offen offset:1024 sc1\n" \
"v_readlane_b32 s43, v202, 9\n" \
"s_nop 4\n" \
"buffer_load_dwordx4 a[72:75], v201, s[8:11], s43 offen sc1\n" \
"buffer_load_dwordx4 a[76:79], v201, s[8:11], s43 offen offset:1024 sc1\n" \
"s_add_u32 s43, s30, s36\n" \
"s_mov_b32 s50, 0\n" \
".LS3_w21l:\n" \
"buffer_load_dword v240, v204, s[4:7], 0 offen sc1\n" \
"s_waitcnt vmcnt(0)\n" \
".LS3_w21c:\n" \
"v_subrev_u32 v241, s43, v240\n" \
"v_cmp_gt_u32 vcc, 0x100, v241\n" \
"s_cmp_eq_u64 vcc, -1\n" \
"s_cbranch_scc1 .LS3_w21d\n" \
"s_sleep 2\n" \
"s_add_u32 s50, s50, 1\n" \
"s_cmp_lt_u32 s50, 0x20000\n" \
"s_cbranch_scc1 .LS3_w21l\n" \
"s_branch .LS3_end\n" \
".LS3_w21d:\n" \
"v_readlane_b32 s43, v202, 10\n" \
"s_nop 4\n" \
"buffer_load_dwordx4 a[80:83], v201, s[8:11], s43 offen sc1\n" \
"buffer_load_dwordx4 a[84:87], v201, s[8:11], s43 offen offset:1024 sc1\n" \
"v_readlane_b32 s43, v202, 11\n" \
"s_nop 4\n" \
"buffer_load_dwordx4 a[88:91], v201, s[8:11], s43 offen sc1\n" \
"buffer_load_dwordx4 a[92:95], v201, s[8:11], s43 offen offset:1024 sc1\n" \
"v_readlane_b32 s43, v202, 12\n" \
"s_nop 4\n" \
"buffer_load_dwordx4 a[96:99], v201, s[8:11], s43 offen sc1\n" \
"buffer_load_dwordx4 a[100:103], v201, s[8:11], s43 offen offset:1024 sc1\n" \
"v_readlane_b32 s43, v202, 13\n" \
"s_nop 4\n" \
"buffer_load_dwordx4 a[104:107], v201, s[8:11], s43 offen sc1\n" \
"buffer_load_dwordx4 a[108:111], v201, s[8:11], s43 offen offset:1024 sc1\n" \
"s_add_u32 s43, s30, 4\n" \
"s_mov_b32 s50, 0\n" \
".LS3_w22l:\n" \
"buffer_load_dword v240, v204, s[4:7], 0 offen sc1\n" \
"s_waitcnt vmcnt(0)\n" \
".LS3_w22c:\n" \
"v_subrev_u32 v241, s43, v240\n" \
"v_cmp_gt_u32 vcc, 0x100, v241\n" \
"s_cmp_eq_u64 vcc, -1\n" \
"s_cbranch_scc1 .LS3_w22d\n" \
"s_sleep 2\n" \
"s_add_u32 s50, s50, 1\n" \
"s_cmp_lt_u32 s50, 0x20000\n" \
"s_cbranch_scc1 .LS3_w22l\n" \
"s_branch .LS3_end\n" \
".LS3_w22d:\n" \
"v_readlane_b32 s43, v202, 0\n" \
"s_nop 4\n" \
"buffer_load_dwordx4 a[0:3], v201, s[8:11], s43 offen sc1\n" \
"buffer_load_dwordx4 a[4:7], v201, s[8:11], s43 offen offset:1024 sc1\n" \
"v_readlane_b32 s43, v202, 1\n" \
"s_nop 4\n" \
"buffer_load_dwordx4 a[8:11], v201, s[8:11], s43 offen sc1\n" \
"buffer_load_dwordx4 a[12:15], v201, s[8:11], s43 offen offset:1024 sc1\n" \
"v_mov_b32 v188, -1\n" \
"s_add_u32 s43, s30, 1\n" \
"s_mov_b32 s50, 0\n" \
".LS3_w23l:\n" \
"buffer_load_dword v240, v205, s[4:7], 0 offen sc1\n" \
"s_waitcnt vmcnt(0)\n" \
".LS3_w23c:\n" \
"v_subrev_u32 v241, s43, v240\n" \
"v_cmp_gt_u32 vcc, 0x100, v241\n" \
"s_cmp_eq_u64 vcc, -1\n" \
"s_cbranch_scc1 .LS3_w23d\n" \
"s_sleep 2\n" \
"s_add_u32 s50, s50, 1\n" \
"s_cmp_lt_u32 s50, 0x20000\n" \
"s_cbranch_scc1 .LS3_w23l\n" \
"s_branch .LS3_end\n" \
".LS3_w23d:\n" \
"s_add_u32 s43, s30, 1\n" \
"s_mov_b32 s50, 0\n" \
".LS3_w24l:\n" \
"buffer_load_dword v240, v206, s[4:7], 0 offen sc1\n" \
"s_waitcnt vmcnt(0)\n" \
".LS3_w24c:\n" \
"v_subrev_u32 v241, s43, v240\n" \
"v_cmp_gt_u32 vcc, 0x100, v241\n" \
"s_cmp_eq_u64 vcc, -1\n" \
"s_cbranch_scc1 .LS3_w24d\n" \
"s_sleep 2\n" \
"s_add_u32 s50, s50, 1\n" \
"s_cmp_lt_u32 s50, 0x20000\n" \
"s_cbranch_scc1 .LS3_w24l\n" \
"s_branch .LS3_end\n" \
".LS3_w24d:\n" \
"s_barrier\n" \
"ds_read_b96 v[248:250], v252\n" \
"s_waitcnt lgkmcnt(0)\n" \
"v_readfirstlane_b32 s44, v248\n" \
"v_readfirstlane_b32 s45, v249\n" \
"v_readfirstlane_b32 s46, v250\n" \
"s_mul_i32 s42, s25, s46\n" \
"s_lshr_b32 s42, s42, 12\n" \
".LS3_loop:\n" \
"s_add_u32 s43, s40, 0\n" \
"s_and_b32 s43, s43, 3\n" \
"s_bitcmp1_b32 s43, 0\n" \
"s_cselect_b32 s47, s32, s31\n" \
"s_lshl_b32 s43, s43, 19\n" \
"s_add_u32 s47, s47, s43\n" \
"s_add_u32 s43, s40, 2\n" \
"s_and_b32 s43, s43, 3\n" \
"s_bitcmp1_b32 s43, 0\n" \
"s_cselect_b32 s49, s32, s31\n" \
"s_lshl_b32 s43, s43, 19\n" \
"s_add_u32 s49, s49, s43\n" \
"s_sub_u32 s43, s24, s44\n" \
"s_max_i32 s43, s43, 0xfffd0000\n" \
"s_min_i32 s43, s43, 0x4000\n" \
"s_mul_i32 s43, s43, s46\n" \
"s_ashr_i32 s43, s43, 12\n" \
"s_add_u32 s41, s45, s43\n" \
"s_add_u32 s41, s41, 12\n" \
"s_branch .LS3_a1\n" \
".LS3_retry:\n" \
"s_add_u32 s51, s51, 1\n" \
"s_cmp_gt_u32 s51, 0x20000\n" \
"s_cbranch_scc1 .LS3_end\n" \
".LS3_a0:\n" \
"s_add_u32 s41, s41, s42\n" \
"s_add_u32 s24, s24, s25\n" \
"s_xor_b32 s31, s31, s29\n" \
"s_xor_b32 s32, s32, s29\n" \
"s_xor_b32 s47, s47, s29\n" \
"s_xor_b32 s48, s48, s29\n" \
"s_add_u32 s39, s39, 1\n" \
"buffer_store_dword v188, v213, s[4:7], s47 offen sc1\n" \
"buffer_store_dword v188, v215, s[4:7], s47 offen sc1\n" \
".p2align 6\n" \
".LS3_a1:\n" \
"s_memtime vcc\n" \
"s_waitcnt lgkmcnt(0)\n" \
"s_sub_u32 s43, s41, vcc_lo\n" \
"s_cmp_gt_i32 s43, 128\n" \
"s_cbranch_scc1 .LS3_a1\n" \
"s_cmp_lt_i32 s43, s37\n" \
"s_cbranch_scc1 .LS3_a0\n" \
"s_max_i32 s43, s43, 0\n" \
"s_and_b32 s43, s43, -4\n" \
"s_getpc_b64 vcc\n" \
".LS3_a3:\n" \
"s_sub_u32 s43, .LS3_a4-.LS3_a3, s43\n" \
"s_add_u32 vcc_lo, vcc_lo, s43\n" \
"s_addc_u32 vcc_hi, vcc_hi, 0\n" \
"s_setpc_b64 vcc\n" \
".rept 32\n" \
"s_nop 0\n" \
".endr\n" \
".LS3_a4:\n" \
"buffer_load_dwordx4 v[12:15], v210, s[4:7], s47 offen sc1\n" \
"s_nop 9\n" \
"buffer_load_dwordx4 v[0:3], v208, s[4:7], s47 offen sc1\n" \
"s_nop 9\n" \
"buffer_load_dwordx4 v[4:7], v208, s[4:7], s47 offen offset:1024 sc1\n" \
"s_nop 9\n" \
"buffer_load_dwordx4 v[8:11], v208, s[4:7], s47 offen offset:2048 sc1\n" \
"s_nop 9\n" \
"buffer_load_dwordx4 v[16:19], v209, s[4:7], s47 offen sc1\n" \
"s_nop 3\n" \
"buffer_load_dwordx4 v[20:23], v209, s[4:7], s47 offen offset:1024 sc1\n" \
"s_nop 3\n" \
"buffer_load_dwordx4 v[24:27], v209, s[4:7], s47 offen offset:2048 sc1\n" \
"s_lshl_b32 s43, s40, 7\n" \
"buffer_load_dwordx4 v[236:239], v207, s[4:7], s43 offen sc1\n" \
"s_add_u32 s35, s35, 1\n" \
"v_mov_b32 v28, 0\n" \
"v_mov_b32 v29, 0\n" \
"v_mov_b32 v30, 0\n" \
"v_mov_b32 v31, 0\n" \
"v_mov_b32 v32, 0\n" \
"v_mov_b32 v33, 0\n" \
"v_mov_b32 v34, 0\n" \
"v_mov_b32 v35, 0\n" \
"v_mov_b32 v36, 0\n" \
"v_mov_b32 v37, 0\n" \
"v_mov_b32 v38, 0\n" \
"v_mov_b32 v39, 0\n" \
"v_mov_b32 v40, 0\n" \
"v_mov_b32 v41, 0\n" \
"v_mov_b32 v42, 0\n" \
"v_mov_b32 v43, 0\n" \
"v_mov_b32 v68, 0\n" \
"v_mov_b32 v69, 0\n" \
"v_mov_b32 v70, 0\n" \
"v_mov_b32 v71, 0\n" \
"s_waitcnt vmcnt(7)\n" \
"v_smfmac_f32_16x16x64_bf16 v[36:39], v[12:15], a[0:7], v200\n" \
"v_smfmac_f32_16x16x64_bf16 v[40:43], v[12:15], a[8:15], v200\n" \
"v_max3_u32 v64, v12, v12, v13\n" \
"v_max3_u32 v64, v64, v14, v15\n" \
"s_waitcnt vmcnt(6)\n" \
"v_smfmac_f32_16x16x64_bf16 v[28:31], v[0:3], a[16:23], v200\n" \
"v_smfmac_f32_16x16x64_bf16 v[32:35], v[0:3], a[24:31], v200\n" \
"v_smfmac_f32_16x16x64_bf16 v[36:39], v[0:3], a[32:39], v200\n" \
"v_smfmac_f32_16x16x64_bf16 v[40:43], v[0:3], a[40:47], v200\n" \
"v_max3_u32 v64, v64, v0, v1\n" \
"v_max3_u32 v64, v64, v2, v3\n" \
"s_waitcnt vmcnt(5)\n" \
"v_smfmac_f32_16x16x64_bf16 v[28:31], v[4:7], a[48:55], v200\n" \
"v_smfmac_f32_16x16x64_bf16 v[32:35], v[4:7], a[56:63], v200\n" \
"v_smfmac_f32_16x16x64_bf16 v[36:39], v[4:7], a[64:71], v200\n" \
"v_smfmac_f32_16x16x64_bf16 v[40:43], v[4:7], a[72:79], v200\n" \
"v_max3_u32 v64, v64, v4, v5\n" \
"v_max3_u32 v64, v64, v6, v7\n" \
"s_waitcnt vmcnt(4)\n" \
"v_smfmac_f32_16x16x64_bf16 v[28:31], v[8:11], a[80:87], v200\n" \
"v_smfmac_f32_16x16x64_bf16 v[32:35], v[8:11], a[88:95], v200\n" \
"v_smfmac_f32_16x16x64_bf16 v[36:39], v[8:11], a[96:103], v200\n" \
"v_smfmac_f32_16x16x64_bf16 v[40:43], v[8:11], a[104:111], v200\n" \
"v_max3_u32 v64, v64, v8, v9\n" \
"v_max3_u32 v64, v64, v10, v11\n" \
"s_nop 2\n" \
"v_cvt_pk_bf16_f32 v128, v28, v29\n" \
"v_cvt_pk_bf16_f32 v129, v30, v31\n" \
"v_cvt_pk_bf16_f32 v130, v32, v33\n" \
"v_cvt_pk_bf16_f32 v131, v34, v35\n" \
"ds_write_b64 v211, v[128:129] offset:0\n" \
"v_cvt_pk_bf16_f32 v132, v36, v37\n" \
"v_cvt_pk_bf16_f32 v133, v38, v39\n" \
"ds_write_b64 v211, v[130:131] offset:128\n" \
"v_cvt_pk_bf16_f32 v134, v40, v41\n" \
"v_cvt_pk_bf16_f32 v135, v42, v43\n" \
"ds_write_b64 v214, v[132:133] offset:0\n" \
"ds_write_b64 v214, v[134:135] offset:128\n" \
"ds_read_b64_tr_b16 v[96:97], v212 offset:6912\n" \
"ds_read_b64_tr_b16 v[98:99], v212 offset:6928\n" \
"ds_read_b64_tr_b16 v[100:101], v212 offset:7168\n" \
"ds_read_b64_tr_b16 v[102:103], v212 offset:7184\n" \
"s_waitcnt vmcnt(1)\n" \
"v_max3_u32 v64, v64, v16, v17\n" \
"v_max3_u32 v64, v64, v18, v19\n" \
"v_max3_u32 v64, v64, v20, v21\n" \
"v_max3_u32 v64, v64, v22, v23\n" \
"v_max3_u32 v64, v64, v24, v25\n" \
"v_max3_u32 v64, v64, v26, v27\n" \
"v_cmp_eq_u32 vcc, -1, v64\n" \
"s_barrier\n" \
"ds_read_b64_tr_b16 v[72:73], v212 offset:0\n" \
"ds_read_b64_tr_b16 v[74:75], v212 offset:16\n" \
"ds_read_b64_tr_b16 v[76:77], v212 offset:256\n" \
"ds_read_b64_tr_b16 v[78:79], v212 offset:272\n" \
"ds_read_b64_tr_b16 v[80:81], v212 offset:2304\n" \
"ds_read_b64_tr_b16 v[82:83], v212 offset:2320\n" \
"ds_read_b64_tr_b16 v[84:85], v212 offset:2560\n" \
"ds_read_b64_tr_b16 v[86:87], v212 offset:2576\n" \
"s_cbranch_vccnz .LS3_veto\n" \
"s_waitcnt lgkmcnt(8)\n" \
"v_smfmac_f32_16x16x64_bf16 v[68:71], v[12:15], v[96:103], v200\n" \
"ds_read_b64_tr_b16 v[88:89], v212 offset:4608\n" \
"ds_read_b64_tr_b16 v[90:91], v212 offset:4624\n" \
"ds_read_b64_tr_b16 v[92:93], v212 offset:4864\n" \
"ds_read_b64_tr_b16 v[94:95], v212 offset:4880\n" \
"s_waitcnt lgkmcnt(8)\n" \
"v_smfmac_f32_16x16x64_bf16 v[68:71], v[0:3], v[72:79], v200\n" \
"s_waitcnt lgkmcnt(4)\n" \
"v_smfmac_f32_16x16x64_bf16 v[68:71], v[4:7], v[80:87], v200\n" \
"s_waitcnt lgkmcnt(0)\n" \
"v_smfmac_f32_16x16x64_bf16 v[68:71], v[8:11], v[88:95], v200\n" \
"s_barrier\n" \
"ds_read_b64_tr_b16 v[104:105], v212 offset:9216\n" \
"ds_read_b64_tr_b16 v[106:107], v212 offset:9232\n" \
"ds_read_b64_tr_b16 v[108:109], v212 offset:9472\n" \
"ds_read_b64_tr_b16 v[110:111], v212 offset:9488\n" \
"s_barrier\n" \
"ds_read_b64_tr_b16 v[112:113], v212 offset:11520\n" \
"ds_read_b64_tr_b16 v[114:115], v212 offset:11536\n" \
"ds_read_b64_tr_b16 v[116:117], v212 offset:11776\n" \
"ds_read_b64_tr_b16 v[118:119], v212 offset:11792\n" \
"s_barrier\n" \
"ds_read_b64_tr_b16 v[120:121], v212 offset:13824\n" \
"ds_read_b64_tr_b16 v[122:123], v212 offset:13840\n" \
"ds_read_b64_tr_b16 v[124:125], v212 offset:14080\n" \
"ds_read_b64_tr_b16 v[126:127], v212 offset:14096\n" \
"s_waitcnt lgkmcnt(8)\n" \
"v_smfmac_f32_16x16x64_bf16 v[68:71], v[16:19], v[104:111], v200\n" \
"s_waitcnt lgkmcnt(4)\n" \
"v_smfmac_f32_16x16x64_bf16 v[68:71], v[20:23], v[112:119], v200\n" \
"s_waitcnt lgkmcnt(0)\n" \
"v_smfmac_f32_16x16x64_bf16 v[68:71], v[24:27], v[120:127], v200\n" \
"s_nop 7\n" \
"v_add_f32_dpp v128, v68, v68 row_shl:4 row_mask:0xf bank_mask:0x5\n" \
"v_add_f32_dpp v129, v70, v70 row_shl:4 row_mask:0xf bank_mask:0x5\n" \
"v_add_f32_dpp v128, v69, v69 row_shr:4 row_mask:0xf bank_mask:0xa\n" \
"v_add_f32_dpp v129, v71, v71 row_shr:4 row_mask:0xf bank_mask:0xa\n" \
"s_nop 1\n" \
"v_permlane32_swap_b32 v128, v129\n" \
"v_add_f32 v128, v128, v129\n" \
"buffer_store_dword v128, v213, s[4:7], s47 offen sc1\n" \
"buffer_store_dword v128, v215, s[4:7], s47 offen sc1\n" \
"s_xor_b32 s43, s47, s29\n" \
"buffer_store_dword v128, v213, s[4:7], s43 offen sc1\n" \
"buffer_store_dword v128, v215, s[4:7], s43 offen sc1\n" \
"s_waitcnt lgkmcnt(0)\n" \
"s_barrier\n" \
"ds_read_b32 v64, v252 offset:16\n" \
"s_waitcnt lgkmcnt(0)\n" \
"v_readfirstlane_b32 s43, v64\n" \
"s_cmp_eq_u32 s43, s35\n" \
"s_cbranch_scc1 .LS3_retry\n" \
"v_cmp_u_f32 vcc, v128, v128\n" \
"s_cbranch_vccnz .LS3_nan\n" \
".LS3_nanr:\n" \
"s_waitcnt vmcnt(4)\n" \
"v_max_u32 v236, v236, v238\n" \
"v_max_u32 v237, v237, v239\n" \
"v_and_b32 v236, 0xfff, v236\n" \
"v_and_b32 v237, 0xfff, v237\n" \
"s_mov_b64 exec, 1\n" \
"ds_write_b64 v252, v[236:237] offset:24\n" \
"s_mov_b64 exec, -1\n" \
"buffer_store_dword v188, v213, s[4:7], s49 offen sc1\n" \
"buffer_store_dword v188, v215, s[4:7], s49 offen sc1\n" \
"s_xor_b32 s43, s49, s29\n" \
"buffer_store_dword v188, v213, s[4:7], s43 offen sc1\n" \
"buffer_store_dword v188, v215, s[4:7], s43 offen sc1\n" \
"s_waitcnt lgkmcnt(0)\n" \
"s_barrier\n" \
"ds_read_b64 v[246:247], v252 offset:24\n" \
"ds_read_b96 v[248:250], v252\n" \
"s_waitcnt lgkmcnt(0)\n" \
"v_readfirstlane_b32 s44, v248\n" \
"v_readfirstlane_b32 s45, v249\n" \
"v_readfirstlane_b32 s46, v250\n" \
"v_readfirstlane_b32 s26, v246\n" \
"v_readfirstlane_b32 s27, v247\n" \
"s_mul_i32 s42, s25, s46\n" \
"s_lshr_b32 s42, s42, 12\n" \
"s_add_u32 s40, s40, 1\n" \
"s_add_u32 s24, s24, s26\n" \
"s_add_u32 s24, s24, s27\n" \
"s_cmp_lt_u32 s40, 63\n" \
"s_cbranch_scc1 .LS3_loop\n" \
".LS3_end:\n" \
"s_endpgm\n" \
".LS3_nan:\n" \
"v_cmp_u_f32 vcc, v128, v128\n" \
"v_mov_b32 v129, 0x7fc00001\n" \
"s_nop 1\n" \
"v_cndmask_b32 v128, v128, v129, vcc\n" \
"buffer_store_dword v128, v213, s[4:7], s47 offen sc1\n" \
"buffer_store_dword v128, v215, s[4:7], s47 offen sc1\n" \
"s_xor_b32 s43, s47, s29\n" \
"buffer_store_dword v128, v213, s[4:7], s43 offen sc1\n" \
"buffer_store_dword v128, v215, s[4:7], s43 offen sc1\n" \
"s_branch .LS3_nanr\n" \
".LS3_veto:\n" \
"v_mov_b32 v64, s35\n" \
"ds_write_b32 v252, v64 offset:20\n" \
"s_barrier\n" \
"s_barrier\n" \
"s_waitcnt lgkmcnt(0)\n" \
"s_barrier\n" \
"s_branch .LS3_retry\n" \
".p2align 6\n" \
"fk_role_P0:\n" \
"ds_read_b32 v200, v255 offset:0\n" \
"ds_read_b32 v201, v255 offset:256\n" \
"ds_read_b32 v202, v255 offset:512\n" \
"ds_read_b32 v203, v255 offset:768\n" \
"ds_read_b32 v204, v255 offset:1024\n" \
"ds_read_b32 v205, v255 offset:1280\n" \
"ds_read_b32 v206, v255 offset:1536\n" \
"ds_read_b32 v207, v255 offset:1792\n" \
"ds_read_b32 v208, v255 offset:2048\n" \
"ds_read_b32 v209, v255 offset:2304\n" \
"ds_read_b32 v210, v255 offset:2560\n" \
"ds_read_b32 v211, v255 offset:2816\n" \
"ds_read_b32 v212, v255 offset:3072\n" \
"ds_read_b32 v213, v255 offset:3328\n" \
"ds_read_b32 v214, v255 offset:3584\n" \
"ds_read_b32 v215, v255 offset:3840\n" \
"ds_read_b32 v216, v255 offset:4096\n" \
"ds_read_b32 v217, v255 offset:4352\n" \
"ds_read_b32 v218, v255 offset:4608\n" \
"ds_read_b32 v219, v255 offset:4864\n" \
"ds_read_b32 v220, v255 offset:5120\n" \
"ds_read_b32 v221, v255 offset:5376\n" \
"ds_read_b32 v222, v255 offset:5632\n" \
"ds_read_b32 v223, v255 offset:5888\n" \
"ds_read_b32 v254, v255 offset:7936\n" \
"s_waitcnt lgkmcnt(0)\n" \
"v_readlane_b32 s4, v254, 0\n" \
"v_readlane_b32 s5, v254, 1\n" \
"v_readlane_b32 s6, v254, 2\n" \
"v_readlane_b32 s7, v254, 3\n" \
"v_readlane_b32 s8, v254, 4\n" \
"v_readlane_b32 s9, v254, 5\n" \
"v_readlane_b32 s10, v254, 6\n" \
"v_readlane_b32 s11, v254, 7\n" \
"v_readlane_b32 s12, v254, 8\n" \
"v_readlane_b32 s13, v254, 9\n" \
"v_readlane_b32 s14, v254, 10\n" \
"v_readlane_b32 s15, v254, 11\n" \
"v_readlane_b32 s16, v254, 12\n" \
"v_readlane_b32 s17, v254, 13\n" \
"v_readlane_b32 s18, v254, 14\n" \
"v_readlane_b32 s19, v254, 15\n" \
"v_readlane_b32 s20, v254, 16\n" \
"v_readlane_b32 s21, v254, 17\n" \
"v_readlane_b32 s22, v254, 18\n" \
"v_readlane_b32 s23, v254, 19\n" \
"v_readlane_b32 s24, v254, 20\n" \
"v_readlane_b32 s25, v254, 21\n" \
"v_readlane_b32 s26, v254, 22\n" \
"v_readlane_b32 s27, v254, 23\n" \
"v_readlane_b32 s28, v254, 24\n" \
"v_readlane_b32 s29, v254, 25\n" \
"v_readlane_b32 s30, v254, 26\n" \
"v_readlane_b32 s31, v254, 27\n" \
"v_readlane_b32 s32, v254, 28\n" \
"v_readlane_b32 s33, v254, 29\n" \
"v_readlane_b32 s34, v254, 30\n" \
"v_readlane_b32 s35, v254, 31\n" \
"v_readlane_b32 s36, v254, 32\n" \
"v_readlane_b32 s37, v254, 33\n" \
"v_readlane_b32 s38, v254, 34\n" \
"v_readlane_b32 s39, v254, 35\n" \
"s_mov_b32 s40, 0\n" \
"s_mov_b32 s51, 0\n" \
"s_nop 0\n" \
"v_mov_b32 v252, s38\n" \
"s_nop 4\n" \
"s_add_u32 s43, s30, 1\n" \
"s_mov_b32 s50, 0\n" \
".LP0_w25l:\n" \
"buffer_load_dword v240, v204, s[4:7], 0 offen sc1\n" \
"s_waitcnt vmcnt(0)\n" \
".LP0_w25c:\n" \
"v_subrev_u32 v241, s43, v240\n" \
"v_cmp_gt_u32 vcc, 0x100, v241\n" \
"s_cmp_eq_u64 vcc, -1\n" \
"s_cbranch_scc1 .LP0_w25d\n" \
"s_sleep 2\n" \
"s_add_u32 s50, s50, 1\n" \
"s_cmp_lt_u32 s50, 0x20000\n" \
"s_cbranch_scc1 .LP0_w25l\n" \
"s_branch .LP0_end\n" \
".LP0_w25d:\n" \
"v_readlane_b32 s43, v202, 0\n" \
"s_nop 4\n" \
"buffer_load_dwordx4 a[0:3], v201, s[8:11], s43 offen sc1\n" \
"buffer_load_dwordx4 a[4:7], v201, s[8:11], s43 offen offset:1024 sc1\n" \
"v_readlane_b32 s43, v202, 7\n" \
"s_nop 4\n" \
"buffer_load_dwordx4 a[56:59], v201, s[8:11], s43 offen sc1\n" \
"buffer_load_dwordx4 a[60:63], v201, s[8:11], s43 offen offset:1024 sc1\n" \
"v_readlane_b32 s43, v202, 14\n" \
"s_nop 4\n" \
"buffer_load_dwordx4 a[112:115], v201, s[8:11], s43 offen sc1\n" \
"buffer_load_dwordx4 a[116:119], v201, s[8:11], s43 offen offset:1024 sc1\n" \
"v_readlane_b32 s43, v202, 21\n" \
"s_nop 4\n" \
"buffer_load_dwordx4 a[168:171], v201, s[8:11], s43 offen sc1\n" \
"buffer_load_dwordx4 a[172:175], v201, s[8:11], s43 offen offset:1024 sc1\n" \
"s_add_u32 s43, s30, 2\n" \
"s_mov_b32 s50, 0\n" \
".LP0_w26l:\n" \
"buffer_load_dword v240, v204, s[4:7], 0 offen sc1\n" \
"s_waitcnt vmcnt(0)\n" \
".LP0_w26c:\n" \
"v_subrev_u32 v241, s43, v240\n" \
"v_cmp_gt_u32 vcc, 0x100, v241\n" \
"s_cmp_eq_u64 vcc, -1\n" \
"s_cbranch_scc1 .LP0_w26d\n" \
"s_sleep 2\n" \
"s_add_u32 s50, s50, 1\n" \
"s_cmp_lt_u32 s50, 0x20000\n" \
"s_cbranch_scc1 .LP0_w26l\n" \
"s_branch .LP0_end\n" \
".LP0_w26d:\n" \
"v_readlane_b32 s43, v202, 1\n" \
"s_nop 4\n" \
"buffer_load_dwordx4 a[8:11], v201, s[8:11], s43 offen sc1\n" \
"buffer_load_dwordx4 a[12:15], v201, s[8:11], s43 offen offset:1024 sc1\n" \
"v_readlane_b32 s43, v202, 8\n" \
"s_nop 4\n" \
"buffer_load_dwordx4 a[64:67], v201, s[8:11], s43 offen sc1\n" \
"buffer_load_dwordx4 a[68:71], v201, s[8:11], s43 offen offset:1024 sc1\n" \
"v_readlane_b32 s43, v202, 15\n" \
"s_nop 4\n" \
"buffer_load_dwordx4 a[120:123], v201, s[8:11], s43 offen sc1\n" \
"buffer_load_dwordx4 a[124:127], v201, s[8:11], s43 offen offset:1024 sc1\n" \
"v_readlane_b32 s43, v202, 22\n" \
"s_nop 4\n" \
"buffer_load_dwordx4 a[176:179], v201, s[8:11], s43 offen sc1\n" \
"buffer_load_dwordx4 a[180:183], v201, s[8:11], s43 offen offset:1024 sc1\n" \
"s_add_u32 s43, s30, 3\n" \
"s_mov_b32 s50, 0\n" \
".LP0_w27l:\n" \
"buffer_load_dword v240, v204, s[4:7], 0 offen sc1\n" \
"s_waitcnt vmcnt(0)\n" \
".LP0_w27c:\n" \
"v_subrev_u32 v241, s43, v240\n" \
"v_cmp_gt_u32 vcc, 0x100, v241\n" \
"s_cmp_eq_u64 vcc, -1\n" \
"s_cbranch_scc1 .LP0_w27d\n" \
"s_sleep 2\n" \
"s_add_u32 s50, s50, 1\n" \
"s_cmp_lt_u32 s50, 0x20000\n" \
"s_cbranch_scc1 .LP0_w27l\n" \
"s_branch .LP0_end\n" \
".LP0_w27d:\n" \
"v_readlane_b32 s43, v202, 2\n" \
"s_nop 4\n" \
"buffer_load_dwordx4 a[16:19], v201, s[8:11], s43 offen sc1\n" \
"buffer_load_dwordx4 a[20:23], v201, s[8:11], s43 offen offset:1024 sc1\n" \
"v_readlane_b32 s43, v202, 9\n" \
"s_nop 4\n" \
"buffer_load_dwordx4 a[72:75], v201, s[8:11], s43 offen sc1\n" \
"buffer_load_dwordx4 a[76:79], v201, s[8:11], s43 offen offset:1024 sc1\n" \
"v_readlane_b32 s43, v202, 16\n" \
"s_nop 4\n" \
"buffer_load_dwordx4 a[128:131], v201, s[8:11], s43 offen sc1\n" \
"buffer_load_dwordx4 a[132:135], v201, s[8:11], s43 offen offset:1024 sc1\n" \
"v_readlane_b32 s43, v202, 23\n" \
"s_nop 4\n" \
"buffer_load_dwordx4 a[184:187], v201, s[8:11], s43 offen sc1\n" \
"buffer_load_dwordx4 a[188:191], v201, s[8:11], s43 offen offset:1024 sc1\n" \
"s_add_u32 s43, s30, 4\n" \
"s_mov_b32 s50, 0\n" \
".LP0_w28l:\n" \
"buffer_load_dword v240, v204, s[4:7], 0 offen sc1\n" \
"s_waitcnt vmcnt(0)\n" \
".LP0_w28c:\n" \
"v_subrev_u32 v241, s43, v240\n" \
"v_cmp_gt_u32 vcc, 0x100, v241\n" \
"s_cmp_eq_u64 vcc, -1\n" \
"s_cbranch_scc1 .LP0_w28d\n" \
"s_sleep 2\n" \
"s_add_u32 s50, s50, 1\n" \
"s_cmp_lt_u32 s50, 0x20000\n" \
"s_cbranch_scc1 .LP0_w28l\n" \
"s_branch .LP0_end\n" \
".LP0_w28d:\n" \
"v_readlane_b32 s43, v202, 3\n" \
"s_nop 4\n" \
"buffer_load_dwordx4 a[24:27], v201, s[8:11], s43 offen sc1\n" \
"buffer_load_dwordx4 a[28:31], v201, s[8:11], s43 offen offset:1024 sc1\n" \
"v_readlane_b32 s43, v202, 10\n" \
"s_nop 4\n" \
"buffer_load_dwordx4 a[80:83], v201, s[8:11], s43 offen sc1\n" \
"buffer_load_dwordx4 a[84:87], v201, s[8:11], s43 offen offset:1024 sc1\n" \
"v_readlane_b32 s43, v202, 17\n" \
"s_nop 4\n" \
"buffer_load_dwordx4 a[136:139], v201, s[8:11], s43 offen sc1\n" \
"buffer_load_dwordx4 a[140:143], v201, s[8:11], s43 offen offset:1024 sc1\n" \
"v_readlane_b32 s43, v202, 24\n" \
"s_nop 4\n" \
"buffer_load_dwordx4 a[192:195], v201, s[8:11], s43 offen sc1\n" \
"buffer_load_dwordx4 a[196:199], v201, s[8:11], s43 offen offset:1024 sc1\n" \
"s_add_u32 s43, s30, 5\n" \
"s_mov_b32 s50, 0\n" \
".LP0_w29l:\n" \
"buffer_load_dword v240, v204, s[4:7], 0 offen sc1\n" \
"s_waitcnt vmcnt(0)\n" \
".LP0_w29c:\n" \
"v_subrev_u32 v241, s43, v240\n" \
"v_cmp_gt_u32 vcc, 0x100, v241\n" \
"s_cmp_eq_u64 vcc, -1\n" \
"s_cbranch_scc1 .LP0_w29d\n" \
"s_sleep 2\n" \
"s_add_u32 s50, s50, 1\n" \
"s_cmp_lt_u32 s50, 0x20000\n" \
"s_cbranch_scc1 .LP0_w29l\n" \
"s_branch .LP0_end\n" \
".LP0_w29d:\n" \
"v_readlane_b32 s43, v202, 4\n" \
"s_nop 4\n" \
"buffer_load_dwordx4 a[32:35], v201, s[8:11], s43 offen sc1\n" \
"buffer_load_dwordx4 a[36:39], v201, s[8:11], s43 offen offset:1024 sc1\n" \
"v_readlane_b32 s43, v202, 11\n" \
"s_nop 4\n" \
"buffer_load_dwordx4 a[88:91], v201, s[8:11], s43 offen sc1\n" \
"buffer_load_dwordx4 a[92:95], v201, s[8:11], s43 offen offset:1024 sc1\n" \
"v_readlane_b32 s43, v202, 18\n" \
"s_nop 4\n" \
"buffer_load_dwordx4 a[144:147], v201, s[8:11], s43 offen sc1\n" \
"buffer_load_dwordx4 a[148:151], v201, s[8:11], s43 offen offset:1024 sc1\n" \
"v_readlane_b32 s43, v202, 25\n" \
"s_nop 4\n" \
"buffer_load_dwordx4 a[200:203], v201, s[8:11], s43 offen sc1\n" \
"buffer_load_dwordx4 a[204:207], v201, s[8:11], s43 offen offset:1024 sc1\n" \
"s_add_u32 s43, s30, 6\n" \
"s_mov_b32 s50, 0\n" \
".LP0_w30l:\n" \
"buffer_load_dword v240, v204, s[4:7], 0 offen sc1\n" \
"s_waitcnt vmcnt(0)\n" \
".LP0_w30c:\n" \
"v_subrev_u32 v241, s43, v240\n" \
"v_cmp_gt_u32 vcc, 0x100, v241\n" \
"s_cmp_eq_u64 vcc, -1\n" \
"s_cbranch_scc1 .LP0_w30d\n" \
"s_sleep 2\n" \
"s_add_u32 s50, s50, 1\n" \
"s_cmp_lt_u32 s50, 0x20000\n" \
"s_cbranch_scc1 .LP0_w30l\n" \
"s_branch .LP0_end\n" \
".LP0_w30d:\n" \
"v_readlane_b32 s43, v202, 5\n" \
"s_nop 4\n" \
"buffer_load_dwordx4 a[40:43], v201, s[8:11], s43 offen sc1\n" \
"buffer_load_dwordx4 a[44:47], v201, s[8:11], s43 offen offset:1024 sc1\n" \
"v_readlane_b32 s43, v202, 12\n" \
"s_nop 4\n" \
"buffer_load_dwordx4 a[96:99], v201, s[8:11], s43 offen sc1\n" \
"buffer_load_dwordx4 a[100:103], v201, s[8:11], s43 offen offset:1024 sc1\n" \
"v_readlane_b32 s43, v202, 19\n" \
"s_nop 4\n" \
"buffer_load_dwordx4 a[152:155], v201, s[8:11], s43 offen sc1\n" \
"buffer_load_dwordx4 a[156:159], v201, s[8:11], s43 offen offset:1024 sc1\n" \
"v_readlane_b32 s43, v202, 26\n" \
"s_nop 4\n" \
"buffer_load_dwordx4 a[208:211], v201, s[8:11], s43 offen sc1\n" \
"buffer_load_dwordx4 a[212:215], v201, s[8:11], s43 offen offset:1024 sc1\n" \
"s_add_u32 s43, s30, 7\n" \
"s_mov_b32 s50, 0\n" \
".LP0_w31l:\n" \
"buffer_load_dword v240, v204, s[4:7], 0 offen sc1\n" \
"s_waitcnt vmcnt(0)\n" \
".LP0_w31c:\n" \
"v_subrev_u32 v241, s43, v240\n" \
"v_cmp_gt_u32 vcc, 0x100, v241\n" \
"s_cmp_eq_u64 vcc, -1\n" \
"s_cbranch_scc1 .LP0_w31d\n" \
"s_sleep 2\n" \
"s_add_u32 s50, s50, 1\n" \
"s_cmp_lt_u32 s50, 0x20000\n" \
"s_cbranch_scc1 .LP0_w31l\n" \
"s_branch .LP0_end\n" \
".LP0_w31d:\n" \
"v_readlane_b32 s43, v202, 6\n" \
"s_nop 4\n" \
"buffer_load_dwordx4 a[48:51], v201, s[8:11], s43 offen sc1\n" \
"buffer_load_dwordx4 a[52:55], v201, s[8:11], s43 offen offset:1024 sc1\n" \
"v_readlane_b32 s43, v202, 13\n" \
"s_nop 4\n" \
"buffer_load_dwordx4 a[104:107], v201, s[8:11], s43 offen sc1\n" \
"buffer_load_dwordx4 a[108:111], v201, s[8:11], s43 offen offset:1024 sc1\n" \
"v_readlane_b32 s43, v202, 20\n" \
"s_nop 4\n" \
"buffer_load_dwordx4 a[160:163], v201, s[8:11], s43 offen sc1\n" \
"buffer_load_dwordx4 a[164:167], v201, s[8:11], s43 offen offset:1024 sc1\n" \
"v_readlane_b32 s43, v202, 27\n" \
"s_nop 4\n" \
"buffer_load_dwordx4 a[216:219], v201, s[8:11], s43 offen sc1\n" \
"buffer_load_dwordx4 a[220:223], v201, s[8:11], s43 offen offset:1024 sc1\n" \
"v_mov_b32 v112, -1\n" \
"v_mov_b32 v113, -1\n" \
"v_mov_b32 v114, -1\n" \
"v_mov_b32 v115, -1\n" \
"v_mov_b32 v109, 0x7fc00001\n" \
"s_add_u32 s43, s30, 1\n" \
"s_mov_b32 s50, 0\n" \
".LP0_w32l:\n" \
"buffer_load_dword v240, v205, s[4:7], 0 offen sc1\n" \
"s_waitcnt vmcnt(0)\n" \
".LP0_w32c:\n" \
"v_subrev_u32 v241, s43, v240\n" \
"v_cmp_gt_u32 vcc, 0x100, v241\n" \
"s_cmp_eq_u64 vcc, -1\n" \
"s_cbranch_scc1 .LP0_w32d\n" \
"s_sleep 2\n" \
"s_add_u32 s50, s50, 1\n" \
"s_cmp_lt_u32 s50, 0x20000\n" \
"s_cbranch_scc1 .LP0_w32l\n" \
"s_branch .LP0_end\n" \
".LP0_w32d:\n" \
"s_add_u32 s43, s30, 1\n" \
"s_mov_b32 s50, 0\n" \
".LP0_w33l:\n" \
"buffer_load_dword v240, v206, s[4:7], 0 offen sc1\n" \
"s_waitcnt vmcnt(0)\n" \
".LP0_w33c:\n" \
"v_subrev_u32 v241, s43, v240\n" \
"v_cmp_gt_u32 vcc, 0x100, v241\n" \
"s_cmp_eq_u64 vcc, -1\n" \
"s_cbranch_scc1 .LP0_w33d\n" \
"s_sleep 2\n" \
"s_add_u32 s50, s50, 1\n" \
"s_cmp_lt_u32 s50, 0x20000\n" \
"s_cbranch_scc1 .LP0_w33l\n" \
"s_branch .LP0_end\n" \
".LP0_w33d:\n" \
"s_add_u32 s43, s40, 0\n" \
"s_and_b32 s43, s43, 3\n" \
"s_bitcmp1_b32 s43, 0\n" \
"s_cselect_b32 s47, s32, s31\n" \
"s_lshl_b32 s43, s43, 19\n" \
"s_add_u32 s47, s47, s43\n" \
"buffer_store_dword v219, v213, s[4:7], s47 offen sc1\n" \
"s_xor_b32 s43, s47, s29\n" \
"buffer_store_dword v219, v213, s[4:7], s43 offen sc1\n" \
"s_barrier\n" \
"ds_read_b96 v[248:250], v252\n" \
"s_waitcnt lgkmcnt(0)\n" \
"v_readfirstlane_b32 s44, v248\n" \
"v_readfirstlane_b32 s45, v249\n" \
"v_readfirstlane_b32 s46, v250\n" \
"s_mul_i32 s42, s25, s46\n" \
"s_lshr_b32 s42, s42, 12\n" \
"s_add_u32 s43, s40, 1\n" \
"s_and_b32 s43, s43, 3\n" \
"s_bitcmp1_b32 s43, 0\n" \
"s_cselect_b32 s48, s32, s31\n" \
"s_lshl_b32 s43, s43, 19\n" \
"s_add_u32 s48, s48, s43\n" \
"s_add_u32 s43, s40, 2\n" \
"s_and_b32 s43, s43, 3\n" \
"s_bitcmp1_b32 s43, 0\n" \
"s_cselect_b32 s49, s32, s31\n" \
"s_lshl_b32 s43, s43, 19\n" \
"s_add_u32 s49, s49, s43\n" \
"s_sub_u32 s43, s24, s44\n" \
"s_add_u32 s43, s43, s28\n" \
"s_max_i32 s43, s43, 0xfffd0000\n" \
"s_min_i32 s43, s43, 0x4000\n" \
"s_mul_i32 s43, s43, s46\n" \
"s_ashr_i32 s43, s43, 12\n" \
"s_add_u32 s41, s45, s43\n" \
"s_lshr_b32 s35, s42, 1\n" \
"s_sub_u32 s35, 0, s35\n" \
".LP0_loop:\n" \
"s_branch .LP0_m1\n" \
".LP0_retry1:\n" \
"s_add_u32 s51, s51, 1\n" \
"s_cmp_gt_u32 s51, 0x20000\n" \
"s_cbranch_scc1 .LP0_end\n" \
".LP0_m0:\n" \
"s_add_u32 s41, s41, s42\n" \
"s_add_u32 s24, s24, s25\n" \
"s_xor_b32 s31, s31, s29\n" \
"s_xor_b32 s32, s32, s29\n" \
"s_xor_b32 s47, s47, s29\n" \
"s_xor_b32 s48, s48, s29\n" \
"s_add_u32 s39, s39, 1\n" \
"buffer_store_dwordx4 v[112:115], v223, s[4:7], s48 offen sc1\n" \
"s_bitcmp1_b32 s31, 21\n" \
"s_cselect_b32 s43, s29, 0\n" \
"v_mov_b32 v116, s43\n" \
"ds_write_b32 v252, v116 offset:16\n" \
".LP0_m1:\n" \
"s_memtime vcc\n" \
"s_waitcnt lgkmcnt(0)\n" \
"s_sub_u32 s43, s41, vcc_lo\n" \
"s_cmp_gt_i32 s43, 0\n" \
"s_cbranch_scc1 .LP0_m1\n" \
"s_cmp_lt_i32 s43, s35\n" \
"s_cbranch_scc1 .LP0_m0\n" \
"buffer_load_dwordx4 v[0:3], v208, s[4:7], s47 offen sc1\n" \
"buffer_load_dwordx4 v[4:7], v208, s[4:7], s47 offen offset:1024 sc1\n" \
"s_nop 15\n" \
"s_nop 13\n" \
"s_mul_i32 s50, s40, 114688\n" \
"buffer_store_dwordx4 v[68:71], v218, s[12:15], s50 offen sc1\n" \
"buffer_store_dword v112, v213, s[4:7], s49 offen sc1\n" \
"s_xor_b32 s43, s49, s29\n" \
"buffer_store_dword v112, v213, s[4:7], s43 offen sc1\n" \
"s_add_u32 s43, s30, s40\n" \
"s_add_u32 s43, s43, -1\n" \
"v_mov_b32 v241, s43\n" \
"buffer_store_dword v241, v221, s[4:7], 0 offen sc1\n" \
"s_bitcmp1_b32 s31, 21\n" \
"s_cselect_b32 s43, s29, 0\n" \
"v_mov_b32 v116, s43\n" \
"ds_write_b32 v252, v116 offset:16\n" \
"v_mov_b32 v28, 0\n" \
"v_mov_b32 v29, 0\n" \
"v_mov_b32 v30, 0\n" \
"v_mov_b32 v31, 0\n" \
"v_mov_b32 v32, 0\n" \
"v_mov_b32 v33, 0\n" \
"v_mov_b32 v34, 0\n" \
"v_mov_b32 v35, 0\n" \
"v_mov_b32 v36, 0\n" \
"v_mov_b32 v37, 0\n" \
"v_mov_b32 v38, 0\n" \
"v_mov_b32 v39, 0\n" \
"v_mov_b32 v40, 0\n" \
"v_mov_b32 v41, 0\n" \
"v_mov_b32 v42, 0\n" \
"v_mov_b32 v43, 0\n" \
"s_waitcnt vmcnt(5)\n" \
"ds_write_b128 v220, v[0:3] offset:0\n" \
"s_waitcnt vmcnt(4)\n" \
"ds_write_b128 v220, v[4:7] offset:1024\n" \
"v_max3_u32 v44, v0, v0, v1\n" \
"v_max3_u32 v44, v44, v2, v3\n" \
"v_max3_u32 v44, v44, v4, v5\n" \
"v_max3_u32 v44, v44, v6, v7\n" \
"v_cmp_eq_u32 vcc, -1, v44\n" \
"v_smfmac_f32_16x16x64_bf16 v[28:31], v[0:3], a[0:7], v200\n" \
"v_smfmac_f32_16x16x64_bf16 v[32:35], v[0:3], a[56:63], v200\n" \
"s_cbranch_vccnz .LP0_retry1\n" \
"s_waitcnt lgkmcnt(0)\n" \
"s_barrier\n" \
"ds_read_b128 v[8:11], v220 offset:2048\n" \
"ds_read_b128 v[12:15], v220 offset:3072\n" \
"ds_read_b128 v[16:19], v220 offset:4096\n" \
"ds_read_b128 v[20:23], v220 offset:5120\n" \
"ds_read_b128 v[24:27], v220 offset:6144\n" \
"ds_read_b64 v[246:247], v252 offset:24\n" \
"ds_read_b96 v[248:250], v252\n" \
"v_smfmac_f32_16x16x64_bf16 v[36:39], v[0:3], a[112:119], v200\n" \
"v_smfmac_f32_16x16x64_bf16 v[40:43], v[0:3], a[168:175], v200\n" \
"v_smfmac_f32_16x16x64_bf16 v[28:31], v[4:7], a[8:15], v200\n" \
"v_smfmac_f32_16x16x64_bf16 v[32:35], v[4:7], a[64:71], v200\n" \
"v_smfmac_f32_16x16x64_bf16 v[36:39], v[4:7], a[120:127], v200\n" \
"v_smfmac_f32_16x16x64_bf16 v[40:43], v[4:7], a[176:183], v200\n" \
"s_waitcnt lgkmcnt(6)\n" \
"v_smfmac_f32_16x16x64_bf16 v[28:31], v[8:11], a[16:23], v200\n" \
"v_smfmac_f32_16x16x64_bf16 v[32:35], v[8:11], a[72:79], v200\n" \
"v_smfmac_f32_16x16x64_bf16 v[36:39], v[8:11], a[128:135], v200\n" \
"v_smfmac_f32_16x16x64_bf16 v[40:43], v[8:11], a[184:191], v200\n" \
"s_waitcnt lgkmcnt(0)\n" \
"v_readfirstlane_b32 s44, v248\n" \
"v_readfirstlane_b32 s45, v249\n" \
"v_smfmac_f32_16x16x64_bf16 v[28:31], v[12:15], a[24:31], v200\n" \
"v_readfirstlane_b32 s46, v250\n" \
"v_readfirstlane_b32 s26, v246\n" \
"v_readfirstlane_b32 s27, v247\n" \
"v_smfmac_f32_16x16x64_bf16 v[32:35], v[12:15], a[80:87], v200\n" \
"s_mul_i32 s42, s25, s46\n" \
"s_lshr_b32 s42, s42, 12\n" \
"s_add_u32 s24, s24, s26\n" \
"v_smfmac_f32_16x16x64_bf16 v[36:39], v[12:15], a[136:143], v200\n" \
"s_sub_u32 s43, s24, s44\n" \
"s_max_i32 s43, s43, 0xfffd0000\n" \
"s_min_i32 s43, s43, 0x4000\n" \
"v_smfmac_f32_16x16x64_bf16 v[40:43], v[12:15], a[192:199], v200\n" \
"s_mul_i32 s43, s43, s46\n" \
"s_ashr_i32 s43, s43, 12\n" \
"s_add_u32 s41, s45, s43\n" \
"s_cmp_eq_u32 s40, 63\n" \
"s_cbranch_scc1 .LP0_last\n" \
"s_branch .LP0_z1\n" \
".LP0_z0:\n" \
"s_add_u32 s41, s41, s42\n" \
"s_add_u32 s24, s24, s25\n" \
"s_xor_b32 s31, s31, s29\n" \
"s_xor_b32 s32, s32, s29\n" \
"s_xor_b32 s47, s47, s29\n" \
"s_xor_b32 s48, s48, s29\n" \
"s_add_u32 s39, s39, 1\n" \
"buffer_store_dwordx4 v[112:115], v223, s[4:7], s48 offen sc1\n" \
"s_bitcmp1_b32 s31, 21\n" \
"s_cselect_b32 s43, s29, 0\n" \
"v_mov_b32 v116, s43\n" \
"ds_write_b32 v252, v116 offset:16\n" \
".LP0_z1:\n" \
"s_memtime vcc\n" \
"s_waitcnt lgkmcnt(0)\n" \
"s_sub_u32 s43, s41, vcc_lo\n" \
"s_cmp_gt_i32 s43, 200\n" \
"s_cbranch_scc1 .LP0_z1\n" \
"s_cmp_lt_i32 s43, s37\n" \
"s_cbranch_scc1 .LP0_z0\n" \
"buffer_load_dwordx2 v[64:65], v211, s[4:7], s47 offen sc1\n" \
".p2align 6\n" \
".LP0_b1:\n" \
"s_memtime vcc\n" \
"s_waitcnt lgkmcnt(0)\n" \
"s_sub_u32 s43, s41, vcc_lo\n" \
"s_cmp_gt_i32 s43, 128\n" \
"s_cbranch_scc1 .LP0_b1\n" \
"s_cmp_lt_i32 s43, s37\n" \
"s_cbranch_scc1 .LP0_z0\n" \
"s_max_i32 s43, s43, 0\n" \
"s_and_b32 s43, s43, -4\n" \
"s_getpc_b64 vcc\n" \
".LP0_b3:\n" \
"s_sub_u32 s43, .LP0_b4-.LP0_b3, s43\n" \
"s_add_u32 vcc_lo, vcc_lo, s43\n" \
"s_addc_u32 vcc_hi, vcc_hi, 0\n" \
"s_setpc_b64 vcc\n" \
".rept 32\n" \
"s_nop 0\n" \
".endr\n" \
".LP0_b4:\n" \
"buffer_load_dwordx4 v[56:59], v210, s[4:7], s47 offen sc1\n" \
"buffer_load_dwordx4 v[60:63], v210, s[4:7], s47 offen offset:256 sc1\n" \
"v_smfmac_f32_16x16x64_bf16 v[28:31], v[16:19], a[32:39], v200\n" \
"v_smfmac_f32_16x16x64_bf16 v[32:35], v[16:19], a[88:95], v200\n" \
"v_smfmac_f32_16x16x64_bf16 v[36:39], v[16:19], a[144:151], v200\n" \
"v_smfmac_f32_16x16x64_bf16 v[40:43], v[16:19], a[200:207], v200\n" \
"v_smfmac_f32_16x16x64_bf16 v[28:31], v[20:23], a[40:47], v200\n" \
"v_smfmac_f32_16x16x64_bf16 v[32:35], v[20:23], a[96:103], v200\n" \
"buffer_load_dwordx4 v[50:53], v212, s[4:7], s47 offen sc1\n" \
"v_smfmac_f32_16x16x64_bf16 v[36:39], v[20:23], a[152:159], v200\n" \
"v_smfmac_f32_16x16x64_bf16 v[40:43], v[20:23], a[208:215], v200\n" \
"v_smfmac_f32_16x16x64_bf16 v[28:31], v[24:27], a[48:55], v200\n" \
"v_smfmac_f32_16x16x64_bf16 v[32:35], v[24:27], a[104:111], v200\n" \
"v_smfmac_f32_16x16x64_bf16 v[36:39], v[24:27], a[160:167], v200\n" \
"v_smfmac_f32_16x16x64_bf16 v[40:43], v[24:27], a[216:223], v200\n" \
"s_nop 2\n" \
"v_mov_b32 v245, v245\n" \
"v_mov_b32 v245, v245\n" \
"v_mov_b32 v245, v245\n" \
"v_permlane32_swap_b32 v28, v32\n" \
"v_permlane32_swap_b32 v29, v33\n" \
"v_permlane32_swap_b32 v30, v34\n" \
"v_permlane32_swap_b32 v31, v35\n" \
"v_add_f32 v28, v28, v32\n" \
"v_add_f32 v29, v29, v33\n" \
"v_add_f32 v30, v30, v34\n" \
"v_add_f32 v31, v31, v35\n" \
"v_cvt_pk_bf16_f32 v68, v28, v29\n" \
"v_cvt_pk_bf16_f32 v69, v30, v31\n" \
"v_permlane32_swap_b32 v36, v40\n" \
"v_permlane32_swap_b32 v37, v41\n" \
"v_permlane32_swap_b32 v38, v42\n" \
"v_permlane32_swap_b32 v39, v43\n" \
"v_add_f32 v36, v36, v40\n" \
"v_add_f32 v37, v37, v41\n" \
"v_add_f32 v38, v38, v42\n" \
"v_add_f32 v39, v39, v43\n" \
"v_cvt_pk_bf16_f32 v70, v36, v37\n" \
"v_cvt_pk_bf16_f32 v71, v38, v39\n" \
"v_mov_b32 v48, v68\n" \
"v_mov_b32 v49, v69\n" \
"v_mov_b32 v54, v70\n" \
"v_mov_b32 v55, v71\n" \
"s_nop 1\n" \
"v_permlane16_swap_b32 v48, v54\n" \
"v_permlane16_swap_b32 v49, v55\n" \
".LP0_wait:\n" \
"s_waitcnt vmcnt(1)\n" \
"v_pk_add_f32 v[72:73], v[56:57], v[60:61]\n" \
"v_pk_add_f32 v[74:75], v[58:59], v[62:63]\n" \
"v_max3_f32 v77, v72, v73, v64\n" \
"v_max3_f32 v77, v77, v74, v75\n" \
"s_nop 1\n" \
"v_max_f32_dpp v76, v77, v77 row_ror:8 row_mask:0xf bank_mask:0xf\n" \
"v_pk_add_f32 v[72:73], v[72:73], v[76:77] op_sel_hi:[1,0] neg_lo:[0,1] neg_hi:[0,1]\n" \
"v_pk_add_f32 v[74:75], v[74:75], v[76:77] op_sel_hi:[1,0] neg_lo:[0,1] neg_hi:[0,1]\n" \
"v_sub_f32 v78, v64, v76\n" \
"v_exp_f32 v72, v72\n" \
"v_exp_f32 v73, v73\n" \
"v_exp_f32 v74, v74\n" \
"v_exp_f32 v75, v75\n" \
"v_exp_f32 v78, v78\n" \
"v_pk_add_f32 v[80:81], v[72:73], v[74:75]\n" \
"v_fma_f32 v80, v78, v65, v80\n" \
"v_add_f32 v81, v80, v81\n" \
"s_nop 1\n" \
"v_add_f32_dpp v80, v81, v81 row_ror:8 row_mask:0xf bank_mask:0xf\n" \
"v_rcp_f32 v80, v80\n" \
"s_nop 0\n" \
"v_mul_f32 v78, v78, v80\n" \
"v_pk_mul_f32 v[72:73], v[72:73], v[80:81] op_sel_hi:[1,0]\n" \
"v_pk_mul_f32 v[74:75], v[74:75], v[80:81] op_sel_hi:[1,0]\n" \
"v_cvt_pk_bf16_f32 v78, v78, v78\n" \
"v_cvt_pk_bf16_f32 v82, v72, v73\n" \
"v_cvt_pk_bf16_f32 v83, v74, v75\n" \
"v_and_b32 v84, v78, v214\n" \
"v_and_b32 v85, v78, v215\n" \
"v_cvt_pk_bf16_f32 v86, v72, v73\n" \
"v_cvt_pk_bf16_f32 v87, v74, v75\n" \
"s_waitcnt vmcnt(0)\n" \
"v_mfma_f32_16x16x32_bf16 v[88:91], v[48:51], v[82:85], 0\n" \
"v_mfma_f32_16x16x32_bf16 v[92:95], v[52:55], v[84:87], 0\n" \
"s_nop 2\n" \
"v_mov_b32 v245, v245\n" \
"s_nop 2\n" \
"v_mov_b32 v245, v245\n" \
"v_add_f32_dpp v96, v92, v88 row_ror:8 row_mask:0xf bank_mask:0x3\n" \
"v_add_f32_dpp v97, v93, v89 row_ror:8 row_mask:0xf bank_mask:0x3\n" \
"v_add_f32_dpp v96, v90, v94 row_ror:8 row_mask:0xf bank_mask:0xc\n" \
"v_add_f32_dpp v97, v91, v95 row_ror:8 row_mask:0xf bank_mask:0xc\n" \
"ds_write_b64 v216, v[96:97]\n" \
"v_cmp_u_f32 vcc, v96, v97\n" \
"s_bitcmp1_b32 s31, 21\n" \
"s_cselect_b32 s18, s29, 0\n" \
"s_xor_b32 s18, s18, s48\n" \
"v_mov_b32 v245, v245\n" \
"s_xor_b32 s19, s18, s29\n" \
"s_nop 0\n" \
"s_nop 0\n" \
"v_mov_b32 v245, v245\n" \
"s_nop 0\n" \
"s_nop 0\n" \
"s_nop 0\n" \
"v_mov_b32 v245, v245\n" \
"s_cbranch_vccnz .LP0_cls\n" \
".LP0_clsr:\n" \
"s_waitcnt lgkmcnt(0)\n" \
"s_barrier\n" \
"ds_read_b64 v[100:101], v217 offset:0\n" \
"ds_read_b64 v[102:103], v217 offset:512\n" \
"ds_read_b32 v116, v252 offset:16\n" \
"ds_read_b64 v[104:105], v217 offset:1024\n" \
"ds_read_b64 v[106:107], v217 offset:1536\n" \
"v_mov_b32 v245, v245\n" \
"v_mov_b32 v245, v245\n" \
"s_add_u32 s40, s40, 1\n" \
"s_add_u32 s24, s24, s27\n" \
"s_mov_b32 s47, s48\n" \
"v_mov_b32 v245, v245\n" \
"s_waitcnt lgkmcnt(2)\n" \
"v_xad_u32 v117, v116, s18, v213\n" \
"v_pk_add_f32 v[100:101], v[100:101], v[102:103]\n" \
"s_waitcnt lgkmcnt(0)\n" \
"v_pk_add_f32 v[104:105], v[104:105], v[106:107]\n" \
"v_pk_add_f32 v[100:101], v[100:101], v[104:105]\n" \
"v_cvt_pk_bf16_f32 v96, v100, v101\n" \
"buffer_store_dword v96, v117, s[4:7], 0 offen sc1\n" \
"s_add_u32 s43, s40, 1\n" \
"s_and_b32 s43, s43, 3\n" \
"s_bitcmp1_b32 s43, 0\n" \
"s_cselect_b32 s48, s32, s31\n" \
"s_lshl_b32 s43, s43, 19\n" \
"s_add_u32 s48, s48, s43\n" \
"s_add_u32 s43, s40, 2\n" \
"s_and_b32 s43, s43, 3\n" \
"s_bitcmp1_b32 s43, 0\n" \
"s_cselect_b32 s49, s32, s31\n" \
"s_lshl_b32 s43, s43, 19\n" \
"s_add_u32 s49, s49, s43\n" \
"s_sub_u32 s43, s24, s44\n" \
"s_add_u32 s43, s43, s28\n" \
"s_max_i32 s43, s43, 0xfffd0000\n" \
"s_min_i32 s43, s43, 0x4000\n" \
"s_mul_i32 s43, s43, s46\n" \
"s_ashr_i32 s43, s43, 12\n" \
"s_add_u32 s41, s45, s43\n" \
"s_lshr_b32 s35, s42, 1\n" \
"s_sub_u32 s35, 0, s35\n" \
"v_xad_u32 v116, v116, s19, v213\n" \
"buffer_store_dword v96, v116, s[4:7], 0 offen sc1\n" \
"s_branch .LP0_loop\n" \
"s_branch .LP0_y1\n" \
".LP0_retry2:\n" \
"s_add_u32 s51, s51, 1\n" \
"s_cmp_gt_u32 s51, 0x20000\n" \
"s_cbranch_scc1 .LP0_end\n" \
".LP0_y0:\n" \
"s_add_u32 s41, s41, s42\n" \
"s_add_u32 s24, s24, s25\n" \
"s_xor_b32 s31, s31, s29\n" \
"s_xor_b32 s32, s32, s29\n" \
"s_xor_b32 s47, s47, s29\n" \
"s_xor_b32 s48, s48, s29\n" \
"s_add_u32 s39, s39, 1\n" \
"buffer_store_dwordx4 v[112:115], v223, s[4:7], s48 offen sc1\n" \
"s_bitcmp1_b32 s31, 21\n" \
"s_cselect_b32 s43, s29, 0\n" \
"v_mov_b32 v116, s43\n" \
"ds_write_b32 v252, v116 offset:16\n" \
".LP0_y1:\n" \
"s_memtime vcc\n" \
"s_waitcnt lgkmcnt(0)\n" \
"s_sub_u32 s43, s41, vcc_lo\n" \
"s_cmp_gt_i32 s43, 200\n" \
"s_cbranch_scc1 .LP0_y1\n" \
"s_cmp_lt_i32 s43, s37\n" \
"s_cbranch_scc1 .LP0_y0\n" \
"buffer_load_dwordx2 v[64:65], v211, s[4:7], s47 offen sc1\n" \
".p2align 6\n" \
".LP0_c1:\n" \
"s_memtime vcc\n" \
"s_waitcnt lgkmcnt(0)\n" \
"s_sub_u32 s43, s41, vcc_lo\n" \
"s_cmp_gt_i32 s43, 128\n" \
"s_cbranch_scc1 .LP0_c1\n" \
"s_cmp_lt_i32 s43, s37\n" \
"s_cbranch_scc1 .LP0_y0\n" \
"s_max_i32 s43, s43, 0\n" \
"s_and_b32 s43, s43, -4\n" \
"s_getpc_b64 vcc\n" \
".LP0_c3:\n" \
"s_sub_u32 s43, .LP0_c4-.LP0_c3, s43\n" \
"s_add_u32 vcc_lo, vcc_lo, s43\n" \
"s_addc_u32 vcc_hi, vcc_hi, 0\n" \
"s_setpc_b64 vcc\n" \
".rept 32\n" \
"s_nop 0\n" \
".endr\n" \
".LP0_c4:\n" \
"buffer_load_dwordx4 v[56:59], v210, s[4:7], s47 offen sc1\n" \
"buffer_load_dwordx4 v[60:63], v210, s[4:7], s47 offen offset:256 sc1\n" \
"s_nop 2\n" \
"v_mov_b32 v245, v245\n" \
"s_nop 2\n" \
"v_mov_b32 v245, v245\n" \
"s_nop 2\n" \
"v_mov_b32 v245, v245\n" \
"s_nop 2\n" \
"v_mov_b32 v245, v245\n" \
"s_nop 2\n" \
"v_mov_b32 v245, v245\n" \
"s_nop 2\n" \
"v_mov_b32 v245, v245\n" \
"buffer_load_dwordx4 v[50:53], v212, s[4:7], s47 offen sc1\n" \
"s_nop 2\n" \
"v_mov_b32 v245, v245\n" \
"s_nop 2\n" \
"v_mov_b32 v245, v245\n" \
"s_nop 2\n" \
"v_mov_b32 v245, v245\n" \
"s_nop 2\n" \
"v_mov_b32 v245, v245\n" \
"s_nop 2\n" \
"v_mov_b32 v245, v245\n" \
"s_nop 2\n" \
"v_mov_b32 v245, v245\n" \
"s_nop 2\n" \
"v_mov_b32 v245, v245\n" \
"s_nop 2\n" \
"v_mov_b32 v245, v245\n" \
"s_nop 2\n" \
"v_mov_b32 v245, v245\n" \
"s_nop 2\n" \
"v_mov_b32 v245, v245\n" \
"s_nop 2\n" \
"v_mov_b32 v245, v245\n" \
"s_nop 2\n" \
"v_mov_b32 v245, v245\n" \
"s_nop 2\n" \
"v_mov_b32 v245, v245\n" \
"s_nop 2\n" \
"v_mov_b32 v245, v245\n" \
"s_nop 2\n" \
"v_mov_b32 v245, v245\n" \
"s_nop 2\n" \
"v_mov_b32 v245, v245\n" \
"s_nop 2\n" \
"v_mov_b32 v245, v245\n" \
"s_branch .LP0_wait\n" \
".LP0_cls:\n" \
"s_waitcnt vmcnt(0)\n" \
"v_max3_u32 v44, v64, v64, v65\n" \
"v_max3_u32 v44, v44, v50, v51\n" \
"v_max3_u32 v44, v44, v52, v53\n" \
"v_cmp_eq_u32 vcc, -1, v44\n" \
"s_cbranch_vccnz .LP0_retry2\n" \
"v_cmp_u_f32 vcc, v56, v56\n" \
"v_cmp_ne_u32 s[2:3], v56, v109\n" \
"s_and_b64 vcc, vcc, s[2:3]\n" \
"s_cbranch_vccnz .LP0_retry2\n" \
"v_cmp_u_f32 vcc, v57, v57\n" \
"v_cmp_ne_u32 s[2:3], v57, v109\n" \
"s_and_b64 vcc, vcc, s[2:3]\n" \
"s_cbranch_vccnz .LP0_retry2\n" \
"v_cmp_u_f32 vcc, v58, v58\n" \
"v_cmp_ne_u32 s[2:3], v58, v109\n" \
"s_and_b64 vcc, vcc, s[2:3]\n" \
"s_cbranch_vccnz .LP0_retry2\n" \
"v_cmp_u_f32 vcc, v59, v59\n" \
"v_cmp_ne_u32 s[2:3], v59, v109\n" \
"s_and_b64 vcc, vcc, s[2:3]\n" \
"s_cbranch_vccnz .LP0_retry2\n" \
"v_cmp_u_f32 vcc, v60, v60\n" \
"v_cmp_ne_u32 s[2:3], v60, v109\n" \
"s_and_b64 vcc, vcc, s[2:3]\n" \
"s_cbranch_vccnz .LP0_retry2\n" \
"v_cmp_u_f32 vcc, v61, v61\n" \
"v_cmp_ne_u32 s[2:3], v61, v109\n" \
"s_and_b64 vcc, vcc, s[2:3]\n" \
"s_cbranch_vccnz .LP0_retry2\n" \
"v_cmp_u_f32 vcc, v62, v62\n" \
"v_cmp_ne_u32 s[2:3], v62, v109\n" \
"s_and_b64 vcc, vcc, s[2:3]\n" \
"s_cbranch_vccnz .LP0_retry2\n" \
"v_cmp_u_f32 vcc, v63, v63\n" \
"v_cmp_ne_u32 s[2:3], v63, v109\n" \
"s_and_b64 vcc, vcc, s[2:3]\n" \
"s_cbranch_vccnz .LP0_retry2\n" \
"s_branch .LP0_clsr\n" \
".LP0_last:\n" \
"v_smfmac_f32_16x16x64_bf16 v[28:31], v[16:19], a[32:39], v200\n" \
"v_smfmac_f32_16x16x64_bf16 v[32:35], v[16:19], a[88:95], v200\n" \
"v_smfmac_f32_16x16x64_bf16 v[36:39], v[16:19], a[144:151], v200\n" \
"v_smfmac_f32_16x16x64_bf16 v[40:43], v[16:19], a[200:207], v200\n" \
"v_smfmac_f32_16x16x64_bf16 v[28:31], v[20:23], a[40:47], v200\n" \
"v_smfmac_f32_16x16x64_bf16 v[32:35], v[20:23], a[96:103], v200\n" \
"v_smfmac_f32_16x16x64_bf16 v[36:39], v[20:23], a[152:159], v200\n" \
"v_smfmac_f32_16x16x64_bf16 v[40:43], v[20:23], a[208:215], v200\n" \
"v_smfmac_f32_16x16x64_bf16 v[28:31], v[24:27], a[48:55], v200\n" \
"v_smfmac_f32_16x16x64_bf16 v[32:35], v[24:27], a[104:111], v200\n" \
"v_smfmac_f32_16x16x64_bf16 v[36:39], v[24:27], a[160:167], v200\n" \
"v_smfmac_f32_16x16x64_bf16 v[40:43], v[24:27], a[216:223], v200\n" \
"s_waitcnt vmcnt(0)\n" \
"s_nop 7\n" \
"v_permlane32_swap_b32 v28, v32\n" \
"v_permlane32_swap_b32 v29, v33\n" \
"v_permlane32_swap_b32 v30, v34\n" \
"v_permlane32_swap_b32 v31, v35\n" \
"v_add_f32 v28, v28, v32\n" \
"v_add_f32 v29, v29, v33\n" \
"v_add_f32 v30, v30, v34\n" \
"v_add_f32 v31, v31, v35\n" \
"v_cvt_pk_bf16_f32 v68, v28, v29\n" \
"v_cvt_pk_bf16_f32 v69, v30, v31\n" \
"v_permlane32_swap_b32 v36, v40\n" \
"v_permlane32_swap_b32 v37, v41\n" \
"v_permlane32_swap_b32 v38, v42\n" \
"v_permlane32_swap_b32 v39, v43\n" \
"v_add_f32 v36, v36, v40\n" \
"v_add_f32 v37, v37, v41\n" \
"v_add_f32 v38, v38, v42\n" \
"v_add_f32 v39, v39, v43\n" \
"v_cvt_pk_bf16_f32 v70, v36, v37\n" \
"v_cvt_pk_bf16_f32 v71, v38, v39\n" \
"s_mov_b32 s50, 7340032\n" \
"buffer_store_dwordx4 v[68:71], v218, s[12:15], s50 offen sc1\n" \
"s_waitcnt vmcnt(0)\n" \
"s_barrier\n" \
"s_add_u32 s43, s30, s40\n" \
"s_add_u32 s43, s43, 1\n" \
"v_mov_b32 v241, s43\n" \
"buffer_store_dword v241, v221, s[4:7], 0 offen sc1\n" \
".LP0_end:\n" \
"s_endpgm\n" \
".p2align 6\n" \
"fk_role_P1:\n" \
"ds_read_b32 v200, v255 offset:0\n" \
"ds_read_b32 v201, v255 offset:256\n" \
"ds_read_b32 v202, v255 offset:512\n" \
"ds_read_b32 v203, v255 offset:768\n" \
"ds_read_b32 v204, v255 offset:1024\n" \
"ds_read_b32 v205, v255 offset:1280\n" \
"ds_read_b32 v206, v255 offset:1536\n" \
"ds_read_b32 v207, v255 offset:1792\n" \
"ds_read_b32 v208, v255 offset:2048\n" \
"ds_read_b32 v209, v255 offset:2304\n" \
"ds_read_b32 v210, v255 offset:2560\n" \
"ds_read_b32 v211, v255 offset:2816\n" \
"ds_read_b32 v212, v255 offset:3072\n" \
"ds_read_b32 v213, v255 offset:3328\n" \
"ds_read_b32 v214, v255 offset:3584\n" \
"ds_read_b32 v215, v255 offset:3840\n" \
"ds_read_b32 v216, v255 offset:4096\n" \
"ds_read_b32 v217, v255 offset:4352\n" \
"ds_read_b32 v218, v255 offset:4608\n" \
"ds_read_b32 v219, v255 offset:4864\n" \
"ds_read_b32 v220, v255 offset:5120\n" \
"ds_read_b32 v221, v255 offset:5376\n" \
"ds_read_b32 v222, v255 offset:5632\n" \
"ds_read_b32 v223, v255 offset:5888\n" \
"ds_read_b32 v254, v255 offset:7936\n" \
"s_waitcnt lgkmcnt(0)\n" \
"v_readlane_b32 s4, v254, 0\n" \
"v_readlane_b32 s5, v254, 1\n" \
"v_readlane_b32 s6, v254, 2\n" \
"v_readlane_b32 s7, v254, 3\n" \
"v_readlane_b32 s8, v254, 4\n" \
"v_readlane_b32 s9, v254, 5\n" \
"v_readlane_b32 s10, v254, 6\n" \
"v_readlane_b32 s11, v254, 7\n" \
"v_readlane_b32 s12, v254, 8\n" \
"v_readlane_b32 s13, v254, 9\n" \
"v_readlane_b32 s14, v254, 10\n" \
"v_readlane_b32 s15, v254, 11\n" \
"v_readlane_b32 s16, v254, 12\n" \
"v_readlane_b32 s17, v254, 13\n" \
"v_readlane_b32 s18, v254, 14\n" \
"v_readlane_b32 s19, v254, 15\n" \
"v_readlane_b32 s20, v254, 16\n" \
"v_readlane_b32 s21, v254, 17\n" \
"v_readlane_b32 s22, v254, 18\n" \
"v_readlane_b32 s23, v254, 19\n" \
"v_readlane_b32 s24, v254, 20\n" \
"v_readlane_b32 s25, v254, 21\n" \
"v_readlane_b32 s26, v254, 22\n" \
"v_readlane_b32 s27, v254, 23\n" \
"v_readlane_b32 s28, v254, 24\n" \
"v_readlane_b32 s29, v254, 25\n" \
"v_readlane_b32 s30, v254, 26\n" \
"v_readlane_b32 s31, v254, 27\n" \
"v_readlane_b32 s32, v254, 28\n" \
"v_readlane_b32 s33, v254, 29\n" \
"v_readlane_b32 s34, v254, 30\n" \
"v_readlane_b32 s35, v254, 31\n" \
"v_readlane_b32 s36, v254, 32\n" \
"v_readlane_b32 s37, v254, 33\n" \
"v_readlane_b32 s38, v254, 34\n" \
"v_readlane_b32 s39, v254, 35\n" \
"s_mov_b32 s40, 0\n" \
"s_mov_b32 s51, 0\n" \
"s_nop 0\n" \
"v_mov_b32 v252, s38\n" \
"s_nop 4\n" \
"s_add_u32 s43, s30, 1\n" \
"s_mov_b32 s50, 0\n" \
".LP1_w34l:\n" \
"buffer_load_dword v240, v204, s[4:7], 0 offen sc1\n" \
"s_waitcnt vmcnt(0)\n" \
".LP1_w34c:\n" \
"v_subrev_u32 v241, s43, v240\n" \
"v_cmp_gt_u32 vcc, 0x100, v241\n" \
"s_cmp_eq_u64 vcc, -1\n" \
"s_cbranch_scc1 .LP1_w34d\n" \
"s_sleep 2\n" \
"s_add_u32 s50, s50, 1\n" \
"s_cmp_lt_u32 s50, 0x20000\n" \
"s_cbranch_scc1 .LP1_w34l\n" \
"s_branch .LP1_end\n" \
".LP1_w34d:\n" \
"v_readlane_b32 s43, v202, 4\n" \
"s_nop 4\n" \
"buffer_load_dwordx4 a[32:35], v201, s[8:11], s43 offen sc1\n" \
"buffer_load_dwordx4 a[36:39], v201, s[8:11], s43 offen offset:1024 sc1\n" \
"v_readlane_b32 s43, v202, 11\n" \
"s_nop 4\n" \
"buffer_load_dwordx4 a[88:91], v201, s[8:11], s43 offen sc1\n" \
"buffer_load_dwordx4 a[92:95], v201, s[8:11], s43 offen offset:1024 sc1\n" \
"v_readlane_b32 s43, v202, 18\n" \
"s_nop 4\n" \
"buffer_load_dwordx4 a[144:147], v201, s[8:11], s43 offen sc1\n" \
"buffer_load_dwordx4 a[148:151], v201, s[8:11], s43 offen offset:1024 sc1\n" \
"v_readlane_b32 s43, v202, 25\n" \
"s_nop 4\n" \
"buffer_load_dwordx4 a[200:203], v201, s[8:11], s43 offen sc1\n" \
"buffer_load_dwordx4 a[204:207], v201, s[8:11], s43 offen offset:1024 sc1\n" \
"s_add_u32 s43, s30, 2\n" \
"s_mov_b32 s50, 0\n" \
".LP1_w35l:\n" \
"buffer_load_dword v240, v204, s[4:7], 0 offen sc1\n" \
"s_waitcnt vmcnt(0)\n" \
".LP1_w35c:\n" \
"v_subrev_u32 v241, s43, v240\n" \
"v_cmp_gt_u32 vcc, 0x100, v241\n" \
"s_cmp_eq_u64 vcc, -1\n" \
"s_cbranch_scc1 .LP1_w35d\n" \
"s_sleep 2\n" \
"s_add_u32 s50, s50, 1\n" \
"s_cmp_lt_u32 s50, 0x20000\n" \
"s_cbranch_scc1 .LP1_w35l\n" \
"s_branch .LP1_end\n" \
".LP1_w35d:\n" \
"v_readlane_b32 s43, v202, 5\n" \
"s_nop 4\n" \
"buffer_load_dwordx4 a[40:43], v201, s[8:11], s43 offen sc1\n" \
"buffer_load_dwordx4 a[44:47], v201, s[8:11], s43 offen offset:1024 sc1\n" \
"v_readlane_b32 s43, v202, 12\n" \
"s_nop 4\n" \
"buffer_load_dwordx4 a[96:99], v201, s[8:11], s43 offen sc1\n" \
"buffer_load_dwordx4 a[100:103], v201, s[8:11], s43 offen offset:1024 sc1\n" \
"v_readlane_b32 s43, v202, 19\n" \
"s_nop 4\n" \
"buffer_load_dwordx4 a[152:155], v201, s[8:11], s43 offen sc1\n" \
"buffer_load_dwordx4 a[156:159], v201, s[8:11], s43 offen offset:1024 sc1\n" \
"v_readlane_b32 s43, v202, 26\n" \
"s_nop 4\n" \
"buffer_load_dwordx4 a[208:211], v201, s[8:11], s43 offen sc1\n" \
"buffer_load_dwordx4 a[212:215], v201, s[8:11], s43 offen offset:1024 sc1\n" \
"s_add_u32 s43, s30, 3\n" \
"s_mov_b32 s50, 0\n" \
".LP1_w36l:\n" \
"buffer_load_dword v240, v204, s[4:7], 0 offen sc1\n" \
"s_waitcnt vmcnt(0)\n" \
".LP1_w36c:\n" \
"v_subrev_u32 v241, s43, v240\n" \
"v_cmp_gt_u32 vcc, 0x100, v241\n" \
"s_cmp_eq_u64 vcc, -1\n" \
"s_cbranch_scc1 .LP1_w36d\n" \
"s_sleep 2\n" \
"s_add_u32 s50, s50, 1\n" \
"s_cmp_lt_u32 s50, 0x20000\n" \
"s_cbranch_scc1 .LP1_w36l\n" \
"s_branch .LP1_end\n" \
".LP1_w36d:\n" \
"v_readlane_b32 s43, v202, 0\n" \
"s_nop 4\n" \
"buffer_load_dwordx4 a[0:3], v201, s[8:11], s43 offen sc1\n" \
"buffer_load_dwordx4 a[4:7], v201, s[8:11], s43 offen offset:1024 sc1\n" \
"v_readlane_b32 s43, v202, 7\n" \
"s_nop 4\n" \
"buffer_load_dwordx4 a[56:59], v201, s[8:11], s43 offen sc1\n" \
"buffer_load_dwordx4 a[60:63], v201, s[8:11], s43 offen offset:1024 sc1\n" \
"v_readlane_b32 s43, v202, 14\n" \
"s_nop 4\n" \
"buffer_load_dwordx4 a[112:115], v201, s[8:11], s43 offen sc1\n" \
"buffer_load_dwordx4 a[116:119], v201, s[8:11], s43 offen offset:1024 sc1\n" \
"v_readlane_b32 s43, v202, 21\n" \
"s_nop 4\n" \
"buffer_load_dwordx4 a[168:171], v201, s[8:11], s43 offen sc1\n" \
"buffer_load_dwordx4 a[172:175], v201, s[8:11], s43 offen offset:1024 sc1\n" \
"s_add_u32 s43, s30, 4\n" \
"s_mov_b32 s50, 0\n" \
".LP1_w37l:\n" \
"buffer_load_dword v240, v204, s[4:7], 0 offen sc1\n" \
"s_waitcnt vmcnt(0)\n" \
".LP1_w37c:\n" \
"v_subrev_u32 v241, s43, v240\n" \
"v_cmp_gt_u32 vcc, 0x100, v241\n" \
"s_cmp_eq_u64 vcc, -1\n" \
"s_cbranch_scc1 .LP1_w37d\n" \
"s_sleep 2\n" \
"s_add_u32 s50, s50, 1\n" \
"s_cmp_lt_u32 s50, 0x20000\n" \
"s_cbranch_scc1 .LP1_w37l\n" \
"s_branch .LP1_end\n" \
".LP1_w37d:\n" \
"v_readlane_b32 s43, v202, 6\n" \
"s_nop 4\n" \
"buffer_load_dwordx4 a[48:51], v201, s[8:11], s43 offen sc1\n" \
"buffer_load_dwordx4 a[52:55], v201, s[8:11], s43 offen offset:1024 sc1\n" \
"v_readlane_b32 s43, v202, 13\n" \
"s_nop 4\n" \
"buffer_load_dwordx4 a[104:107], v201, s[8:11], s43 offen sc1\n" \
"buffer_load_dwordx4 a[108:111], v201, s[8:11], s43 offen offset:1024 sc1\n" \
"v_readlane_b32 s43, v202, 20\n" \
"s_nop 4\n" \
"buffer_load_dwordx4 a[160:163], v201, s[8:11], s43 offen sc1\n" \
"buffer_load_dwordx4 a[164:167], v201, s[8:11], s43 offen offset:1024 sc1\n" \
"v_readlane_b32 s43, v202, 27\n" \
"s_nop 4\n" \
"buffer_load_dwordx4 a[216:219], v201, s[8:11], s43 offen sc1\n" \
"buffer_load_dwordx4 a[220:223], v201, s[8:11], s43 offen offset:1024 sc1\n" \
"s_add_u32 s43, s30, 5\n" \
"s_mov_b32 s50, 0\n" \
".LP1_w38l:\n" \
"buffer_load_dword v240, v204, s[4:7], 0 offen sc1\n" \
"s_waitcnt vmcnt(0)\n" \
".LP1_w38c:\n" \
"v_subrev_u32 v241, s43, v240\n" \
"v_cmp_gt_u32 vcc, 0x100, v241\n" \
"s_cmp_eq_u64 vcc, -1\n" \
"s_cbranch_scc1 .LP1_w38d\n" \
"s_sleep 2\n" \
"s_add_u32 s50, s50, 1\n" \
"s_cmp_lt_u32 s50, 0x20000\n" \
"s_cbranch_scc1 .LP1_w38l\n" \
"s_branch .LP1_end\n" \
".LP1_w38d:\n" \
"v_readlane_b32 s43, v202, 1\n" \
"s_nop 4\n" \
"buffer_load_dwordx4 a[8:11], v201, s[8:11], s43 offen sc1\n" \
"buffer_load_dwordx4 a[12:15], v201, s[8:11], s43 offen offset:1024 sc1\n" \
"v_readlane_b32 s43, v202, 8\n" \
"s_nop 4\n" \
"buffer_load_dwordx4 a[64:67], v201, s[8:11], s43 offen sc1\n" \
"buffer_load_dwordx4 a[68:71], v201, s[8:11], s43 offen offset:1024 sc1\n" \
"v_readlane_b32 s43, v202, 15\n" \
"s_nop 4\n" \
"buffer_load_dwordx4 a[120:123], v201, s[8:11], s43 offen sc1\n" \
"buffer_load_dwordx4 a[124:127], v201, s[8:11], s43 offen offset:1024 sc1\n" \
"v_readlane_b32 s43, v202, 22\n" \
"s_nop 4\n" \
"buffer_load_dwordx4 a[176:179], v201, s[8:11], s43 offen sc1\n" \
"buffer_load_dwordx4 a[180:183], v201, s[8:11], s43 offen offset:1024 sc1\n" \
"s_add_u32 s43, s30, 6\n" \
"s_mov_b32 s50, 0\n" \
".LP1_w39l:\n" \
"buffer_load_dword v240, v204, s[4:7], 0 offen sc1\n" \
"s_waitcnt vmcnt(0)\n" \
".LP1_w39c:\n" \
"v_subrev_u32 v241, s43, v240\n" \
"v_cmp_gt_u32 vcc, 0x100, v241\n" \
"s_cmp_eq_u64 vcc, -1\n" \
"s_cbranch_scc1 .LP1_w39d\n" \
"s_sleep 2\n" \
"s_add_u32 s50, s50, 1\n" \
"s_cmp_lt_u32 s50, 0x20000\n" \
"s_cbranch_scc1 .LP1_w39l\n" \
"s_branch .LP1_end\n" \
".LP1_w39d:\n" \
"v_readlane_b32 s43, v202, 2\n" \
"s_nop 4\n" \
"buffer_load_dwordx4 a[16:19], v201, s[8:11], s43 offen sc1\n" \
"buffer_load_dwordx4 a[20:23], v201, s[8:11], s43 offen offset:1024 sc1\n" \
"v_readlane_b32 s43, v202, 9\n" \
"s_nop 4\n" \
"buffer_load_dwordx4 a[72:75], v201, s[8:11], s43 offen sc1\n" \
"buffer_load_dwordx4 a[76:79], v201, s[8:11], s43 offen offset:1024 sc1\n" \
"v_readlane_b32 s43, v202, 16\n" \
"s_nop 4\n" \
"buffer_load_dwordx4 a[128:131], v201, s[8:11], s43 offen sc1\n" \
"buffer_load_dwordx4 a[132:135], v201, s[8:11], s43 offen offset:1024 sc1\n" \
"v_readlane_b32 s43, v202, 23\n" \
"s_nop 4\n" \
"buffer_load_dwordx4 a[184:187], v201, s[8:11], s43 offen sc1\n" \
"buffer_load_dwordx4 a[188:191], v201, s[8:11], s43 offen offset:1024 sc1\n" \
"s_add_u32 s43, s30, 7\n" \
"s_mov_b32 s50, 0\n" \
".LP1_w40l:\n" \
"buffer_load_dword v240, v204, s[4:7], 0 offen sc1\n" \
"s_waitcnt vmcnt(0)\n" \
".LP1_w40c:\n" \
"v_subrev_u32 v241, s43, v240\n" \
"v_cmp_gt_u32 vcc, 0x100, v241\n" \
"s_cmp_eq_u64 vcc, -1\n" \
"s_cbranch_scc1 .LP1_w40d\n" \
"s_sleep 2\n" \
"s_add_u32 s50, s50, 1\n" \
"s_cmp_lt_u32 s50, 0x20000\n" \
"s_cbranch_scc1 .LP1_w40l\n" \
"s_branch .LP1_end\n" \
".LP1_w40d:\n" \
"v_readlane_b32 s43, v202, 3\n" \
"s_nop 4\n" \
"buffer_load_dwordx4 a[24:27], v201, s[8:11], s43 offen sc1\n" \
"buffer_load_dwordx4 a[28:31], v201, s[8:11], s43 offen offset:1024 sc1\n" \
"v_readlane_b32 s43, v202, 10\n" \
"s_nop 4\n" \
"buffer_load_dwordx4 a[80:83], v201, s[8:11], s43 offen sc1\n" \
"buffer_load_dwordx4 a[84:87], v201, s[8:11], s43 offen offset:1024 sc1\n" \
"v_readlane_b32 s43, v202, 17\n" \
"s_nop 4\n" \
"buffer_load_dwordx4 a[136:139], v201, s[8:11], s43 offen sc1\n" \
"buffer_load_dwordx4 a[140:143], v201, s[8:11], s43 offen offset:1024 sc1\n" \
"v_readlane_b32 s43, v202, 24\n" \
"s_nop 4\n" \
"buffer_load_dwordx4 a[192:195], v201, s[8:11], s43 offen sc1\n" \
"buffer_load_dwordx4 a[196:199], v201, s[8:11], s43 offen offset:1024 sc1\n" \
"v_mov_b32 v112, -1\n" \
"v_mov_b32 v113, -1\n" \
"v_mov_b32 v114, -1\n" \
"v_mov_b32 v115, -1\n" \
"v_mov_b32 v109, 0x7fc00001\n" \
"s_add_u32 s43, s30, 1\n" \
"s_mov_b32 s50, 0\n" \
".LP1_w41l:\n" \
"buffer_load_dword v240, v205, s[4:7], 0 offen sc1\n" \
"s_waitcnt vmcnt(0)\n" \
".LP1_w41c:\n" \
"v_subrev_u32 v241, s43, v240\n" \
"v_cmp_gt_u32 vcc, 0x100, v241\n" \
"s_cmp_eq_u64 vcc, -1\n" \
"s_cbranch_scc1 .LP1_w41d\n" \
"s_sleep 2\n" \
"s_add_u32 s50, s50, 1\n" \
"s_cmp_lt_u32 s50, 0x20000\n" \
"s_cbranch_scc1 .LP1_w41l\n" \
"s_branch .LP1_end\n" \
".LP1_w41d:\n" \
"s_add_u32 s43, s30, 1\n" \
"s_mov_b32 s50, 0\n" \
".LP1_w42l:\n" \
"buffer_load_dword v240, v206, s[4:7], 0 offen sc1\n" \
"s_waitcnt vmcnt(0)\n" \
".LP1_w42c:\n" \
"v_subrev_u32 v241, s43, v240\n" \
"v_cmp_gt_u32 vcc, 0x100, v241\n" \
"s_cmp_eq_u64 vcc, -1\n" \
"s_cbranch_scc1 .LP1_w42d\n" \
"s_sleep 2\n" \
"s_add_u32 s50, s50, 1\n" \
"s_cmp_lt_u32 s50, 0x20000\n" \
"s_cbranch_scc1 .LP1_w42l\n" \
"s_branch .LP1_end\n" \
".LP1_w42d:\n" \
"s_add_u32 s43, s40, 0\n" \
"s_and_b32 s43, s43, 3\n" \
"s_bitcmp1_b32 s43, 0\n" \
"s_cselect_b32 s47, s32, s31\n" \
"s_lshl_b32 s43, s43, 19\n" \
"s_add_u32 s47, s47, s43\n" \
"buffer_store_dword v219, v213, s[4:7], s47 offen sc1\n" \
"s_xor_b32 s43, s47, s29\n" \
"buffer_store_dword v219, v213, s[4:7], s43 offen sc1\n" \
"s_lshl_b32 s43, s40, 7\n" \
"buffer_load_dwordx4 v[236:239], v207, s[4:7], s43 offen sc1\n" \
"s_waitcnt vmcnt(0)\n" \
"s_barrier\n" \
"ds_read_b96 v[248:250], v252\n" \
"s_waitcnt lgkmcnt(0)\n" \
"v_readfirstlane_b32 s44, v248\n" \
"v_readfirstlane_b32 s45, v249\n" \
"v_readfirstlane_b32 s46, v250\n" \
"s_mul_i32 s42, s25, s46\n" \
"s_lshr_b32 s42, s42, 12\n" \
"s_add_u32 s43, s40, 1\n" \
"s_and_b32 s43, s43, 3\n" \
"s_bitcmp1_b32 s43, 0\n" \
"s_cselect_b32 s48, s32, s31\n" \
"s_lshl_b32 s43, s43, 19\n" \
"s_add_u32 s48, s48, s43\n" \
"s_add_u32 s43, s40, 2\n" \
"s_and_b32 s43, s43, 3\n" \
"s_bitcmp1_b32 s43, 0\n" \
"s_cselect_b32 s49, s32, s31\n" \
"s_lshl_b32 s43, s43, 19\n" \
"s_add_u32 s49, s49, s43\n" \
"s_sub_u32 s43, s24, s44\n" \
"s_add_u32 s43, s43, s28\n" \
"s_max_i32 s43, s43, 0xfffd0000\n" \
"s_min_i32 s43, s43, 0x4000\n" \
"s_mul_i32 s43, s43, s46\n" \
"s_ashr_i32 s43, s43, 12\n" \
"s_add_u32 s41, s45, s43\n" \
"s_lshr_b32 s35, s42, 1\n" \
"s_sub_u32 s35, 0, s35\n" \
"v_max_u32 v236, v236, v238\n" \
"v_max_u32 v237, v237, v239\n" \
"v_and_b32 v236, 0xfff, v236\n" \
"v_and_b32 v237, 0xfff, v237\n" \
"s_mov_b64 exec, 1\n" \
"ds_write_b64 v252, v[236:237] offset:24\n" \
"s_mov_b64 exec, -1\n" \
".LP1_loop:\n" \
"s_branch .LP1_m1\n" \
".LP1_retry1:\n" \
"s_add_u32 s51, s51, 1\n" \
"s_cmp_gt_u32 s51, 0x20000\n" \
"s_cbranch_scc1 .LP1_end\n" \
".LP1_m0:\n" \
"s_add_u32 s41, s41, s42\n" \
"s_add_u32 s24, s24, s25\n" \
"s_xor_b32 s31, s31, s29\n" \
"s_xor_b32 s32, s32, s29\n" \
"s_xor_b32 s47, s47, s29\n" \
"s_xor_b32 s48, s48, s29\n" \
"s_add_u32 s39, s39, 1\n" \
"buffer_store_dwordx4 v[112:115], v223, s[4:7], s48 offen sc1\n" \
"s_bitcmp1_b32 s31, 21\n" \
"s_cselect_b32 s43, s29, 0\n" \
"v_mov_b32 v116, s43\n" \
"ds_write_b32 v252, v116 offset:16\n" \
".LP1_m1:\n" \
"s_memtime vcc\n" \
"s_waitcnt lgkmcnt(0)\n" \
"s_sub_u32 s43, s41, vcc_lo\n" \
"s_cmp_gt_i32 s43, 0\n" \
"s_cbranch_scc1 .LP1_m1\n" \
"s_cmp_lt_i32 s43, s35\n" \
"s_cbranch_scc1 .LP1_m0\n" \
"buffer_load_dwordx4 v[0:3], v208, s[4:7], s47 offen offset:2048 sc1\n" \
"buffer_load_dwordx4 v[4:7], v209, s[4:7], s47 offen sc1\n" \
"s_nop 15\n" \
"s_nop 13\n" \
"s_mul_i32 s50, s40, 114688\n" \
"buffer_store_dwordx4 v[68:71], v218, s[12:15], s50 offen sc1\n" \
"buffer_store_dword v112, v213, s[4:7], s49 offen sc1\n" \
"s_xor_b32 s43, s49, s29\n" \
"buffer_store_dword v112, v213, s[4:7], s43 offen sc1\n" \
"s_lshl_b32 s43, s40, 7\n" \
"buffer_load_dwordx4 v[236:239], v207, s[4:7], s43 offen offset:128 sc1\n" \
"v_mov_b32 v28, 0\n" \
"v_mov_b32 v29, 0\n" \
"v_mov_b32 v30, 0\n" \
"v_mov_b32 v31, 0\n" \
"v_mov_b32 v32, 0\n" \
"v_mov_b32 v33, 0\n" \
"v_mov_b32 v34, 0\n" \
"v_mov_b32 v35, 0\n" \
"v_mov_b32 v36, 0\n" \
"v_mov_b32 v37, 0\n" \
"v_mov_b32 v38, 0\n" \
"v_mov_b32 v39, 0\n" \
"v_mov_b32 v40, 0\n" \
"v_mov_b32 v41, 0\n" \
"v_mov_b32 v42, 0\n" \
"v_mov_b32 v43, 0\n" \
"s_waitcnt vmcnt(5)\n" \
"ds_write_b128 v220, v[0:3] offset:2048\n" \
"s_waitcnt vmcnt(4)\n" \
"ds_write_b128 v220, v[4:7] offset:4096\n" \
"v_max3_u32 v44, v0, v0, v1\n" \
"v_max3_u32 v44, v44, v2, v3\n" \
"v_max3_u32 v44, v44, v4, v5\n" \
"v_max3_u32 v44, v44, v6, v7\n" \
"v_cmp_eq_u32 vcc, -1, v44\n" \
"v_smfmac_f32_16x16x64_bf16 v[28:31], v[0:3], a[0:7], v200\n" \
"v_smfmac_f32_16x16x64_bf16 v[32:35], v[0:3], a[56:63], v200\n" \
"s_cbranch_vccnz .LP1_retry1\n" \
"s_waitcnt lgkmcnt(0)\n" \
"s_barrier\n" \
"ds_read_b128 v[8:11], v220 offset:5120\n" \
"ds_read_b128 v[12:15], v220 offset:6144\n" \
"ds_read_b128 v[16:19], v220 offset:0\n" \
"ds_read_b128 v[20:23], v220 offset:1024\n" \
"ds_read_b128 v[24:27], v220 offset:3072\n" \
"ds_read_b64 v[246:247], v252 offset:24\n" \
"ds_read_b96 v[248:250], v252\n" \
"v_smfmac_f32_16x16x64_bf16 v[36:39], v[0:3], a[112:119], v200\n" \
"v_smfmac_f32_16x16x64_bf16 v[40:43], v[0:3], a[168:175], v200\n" \
"v_smfmac_f32_16x16x64_bf16 v[28:31], v[4:7], a[8:15], v200\n" \
"v_smfmac_f32_16x16x64_bf16 v[32:35], v[4:7], a[64:71], v200\n" \
"v_smfmac_f32_16x16x64_bf16 v[36:39], v[4:7], a[120:127], v200\n" \
"v_smfmac_f32_16x16x64_bf16 v[40:43], v[4:7], a[176:183], v200\n" \
"s_waitcnt lgkmcnt(6)\n" \
"v_smfmac_f32_16x16x64_bf16 v[28:31], v[8:11], a[16:23], v200\n" \
"v_smfmac_f32_16x16x64_bf16 v[32:35], v[8:11], a[72:79], v200\n" \
"v_smfmac_f32_16x16x64_bf16 v[36:39], v[8:11], a[128:135], v200\n" \
"v_smfmac_f32_16x16x64_bf16 v[40:43], v[8:11], a[184:191], v200\n" \
"s_waitcnt lgkmcnt(0)\n" \
"v_readfirstlane_b32 s44, v248\n" \
"v_readfirstlane_b32 s45, v249\n" \
"v_smfmac_f32_16x16x64_bf16 v[28:31], v[12:15], a[24:31], v200\n" \
"v_readfirstlane_b32 s46, v250\n" \
"v_readfirstlane_b32 s26, v246\n" \
"v_readfirstlane_b32 s27, v247\n" \
"v_smfmac_f32_16x16x64_bf16 v[32:35], v[12:15], a[80:87], v200\n" \
"s_mul_i32 s42, s25, s46\n" \
"s_lshr_b32 s42, s42, 12\n" \
"s_add_u32 s24, s24, s26\n" \
"v_smfmac_f32_16x16x64_bf16 v[36:39], v[12:15], a[136:143], v200\n" \
"s_sub_u32 s43, s24, s44\n" \
"s_max_i32 s43, s43, 0xfffd0000\n" \
"s_min_i32 s43, s43, 0x4000\n" \
"v_smfmac_f32_16x16x64_bf16 v[40:43], v[12:15], a[192:199], v200\n" \
"s_mul_i32 s43, s43, s46\n" \
"s_ashr_i32 s43, s43, 12\n" \
"s_add_u32 s41, s45, s43\n" \
"s_cmp_eq_u32 s40, 63\n" \
"s_cbranch_scc1 .LP1_last\n" \
"s_branch .LP1_z1\n" \
".LP1_z0:\n" \
"s_add_u32 s41, s41, s42\n" \
"s_add_u32 s24, s24, s25\n" \
"s_xor_b32 s31, s31, s29\n" \
"s_xor_b32 s32, s32, s29\n" \
"s_xor_b32 s47, s47, s29\n" \
"s_xor_b32 s48, s48, s29\n" \
"s_add_u32 s39, s39, 1\n" \
"buffer_store_dwordx4 v[112:115], v223, s[4:7], s48 offen sc1\n" \
"s_bitcmp1_b32 s31, 21\n" \
"s_cselect_b32 s43, s29, 0\n" \
"v_mov_b32 v116, s43\n" \
"ds_write_b32 v252, v116 offset:16\n" \
".LP1_z1:\n" \
"s_memtime vcc\n" \
"s_waitcnt lgkmcnt(0)\n" \
"s_sub_u32 s43, s41, vcc_lo\n" \
"s_cmp_gt_i32 s43, 200\n" \
"s_cbranch_scc1 .LP1_z1\n" \
"s_cmp_lt_i32 s43, s37\n" \
"s_cbranch_scc1 .LP1_z0\n" \
"buffer_load_dwordx2 v[64:65], v211, s[4:7], s47 offen sc1\n" \
".p2align 6\n" \
".LP1_b1:\n" \
"s_memtime vcc\n" \
"s_waitcnt lgkmcnt(0)\n" \
"s_sub_u32 s43, s41, vcc_lo\n" \
"s_cmp_gt_i32 s43, 128\n" \
"s_cbranch_scc1 .LP1_b1\n" \
"s_cmp_lt_i32 s43, s37\n" \
"s_cbranch_scc1 .LP1_z0\n" \
"s_max_i32 s43, s43, 0\n" \
"s_and_b32 s43, s43, -4\n" \
"s_getpc_b64 vcc\n" \
".LP1_b3:\n" \
"s_sub_u32 s43, .LP1_b4-.LP1_b3, s43\n" \
"s_add_u32 vcc_lo, vcc_lo, s43\n" \
"s_addc_u32 vcc_hi, vcc_hi, 0\n" \
"s_setpc_b64 vcc\n" \
".rept 32\n" \
"s_nop 0\n" \
".endr\n" \
".LP1_b4:\n" \
"buffer_load_dwordx4 v[56:59], v210, s[4:7], s47 offen sc1\n" \
"buffer_load_dwordx4 v[60:63], v210, s[4:7], s47 offen offset:256 sc1\n" \
"v_smfmac_f32_16x16x64_bf16 v[28:31], v[16:19], a[32:39], v200\n" \
"v_smfmac_f32_16x16x64_bf16 v[32:35], v[16:19], a[88:95], v200\n" \
"v_smfmac_f32_16x16x64_bf16 v[36:39], v[16:19], a[144:151], v200\n" \
"v_smfmac_f32_16x16x64_bf16 v[40:43], v[16:19], a[200:207], v200\n" \
"v_smfmac_f32_16x16x64_bf16 v[28:31], v[20:23], a[40:47], v200\n" \
"v_smfmac_f32_16x16x64_bf16 v[32:35], v[20:23], a[96:103], v200\n" \
"buffer_load_dwordx4 v[50:53], v212, s[4:7], s47 offen sc1\n" \
"v_smfmac_f32_16x16x64_bf16 v[36:39], v[20:23], a[152:159], v200\n" \
"v_smfmac_f32_16x16x64_bf16 v[40:43], v[20:23], a[208:215], v200\n" \
"v_smfmac_f32_16x16x64_bf16 v[28:31], v[24:27], a[48:55], v200\n" \
"v_smfmac_f32_16x16x64_bf16 v[32:35], v[24:27], a[104:111], v200\n" \
"v_smfmac_f32_16x16x64_bf16 v[36:39], v[24:27], a[160:167], v200\n" \
"v_smfmac_f32_16x16x64_bf16 v[40:43], v[24:27], a[216:223], v200\n" \
"s_nop 2\n" \
"v_mov_b32 v245, v245\n" \
"v_mov_b32 v245, v245\n" \
"v_mov_b32 v245, v245\n" \
"v_permlane32_swap_b32 v28, v32\n" \
"v_permlane32_swap_b32 v29, v33\n" \
"v_permlane32_swap_b32 v30, v34\n" \
"v_permlane32_swap_b32 v31, v35\n" \
"v_add_f32 v28, v28, v32\n" \
"v_add_f32 v29, v29, v33\n" \
"v_add_f32 v30, v30, v34\n" \
"v_add_f32 v31, v31, v35\n" \
"v_cvt_pk_bf16_f32 v68, v28, v29\n" \
"v_cvt_pk_bf16_f32 v69, v30, v31\n" \
"v_permlane32_swap_b32 v36, v40\n" \
"v_permlane32_swap_b32 v37, v41\n" \
"v_permlane32_swap_b32 v38, v42\n" \
"v_permlane32_swap_b32 v39, v43\n" \
"v_add_f32 v36, v36, v40\n" \
"v_add_f32 v37, v37, v41\n" \
"v_add_f32 v38, v38, v42\n" \
"v_add_f32 v39, v39, v43\n" \
"v_cvt_pk_bf16_f32 v70, v36, v37\n" \
"v_cvt_pk_bf16_f32 v71, v38, v39\n" \
"v_mov_b32 v48, v68\n" \
"v_mov_b32 v49, v69\n" \
"v_mov_b32 v54, v70\n" \
"v_mov_b32 v55, v71\n" \
"s_nop 1\n" \
"v_permlane16_swap_b32 v48, v54\n" \
"v_permlane16_swap_b32 v49, v55\n" \
".LP1_wait:\n" \
"s_waitcnt vmcnt(1)\n" \
"v_pk_add_f32 v[72:73], v[56:57], v[60:61]\n" \
"v_pk_add_f32 v[74:75], v[58:59], v[62:63]\n" \
"v_max3_f32 v77, v72, v73, v64\n" \
"v_max3_f32 v77, v77, v74, v75\n" \
"s_nop 1\n" \
"v_max_f32_dpp v76, v77, v77 row_ror:8 row_mask:0xf bank_mask:0xf\n" \
"v_pk_add_f32 v[72:73], v[72:73], v[76:77] op_sel_hi:[1,0] neg_lo:[0,1] neg_hi:[0,1]\n" \
"v_pk_add_f32 v[74:75], v[74:75], v[76:77] op_sel_hi:[1,0] neg_lo:[0,1] neg_hi:[0,1]\n" \
"v_sub_f32 v78, v64, v76\n" \
"v_exp_f32 v72, v72\n" \
"v_exp_f32 v73, v73\n" \
"v_exp_f32 v74, v74\n" \
"v_exp_f32 v75, v75\n" \
"v_exp_f32 v78, v78\n" \
"v_pk_add_f32 v[80:81], v[72:73], v[74:75]\n" \
"v_fma_f32 v80, v78, v65, v80\n" \
"v_add_f32 v81, v80, v81\n" \
"s_nop 1\n" \
"v_add_f32_dpp v80, v81, v81 row_ror:8 row_mask:0xf bank_mask:0xf\n" \
"v_rcp_f32 v80, v80\n" \
"s_nop 0\n" \
"v_mul_f32 v78, v78, v80\n" \
"v_pk_mul_f32 v[72:73], v[72:73], v[80:81] op_sel_hi:[1,0]\n" \
"v_pk_mul_f32 v[74:75], v[74:75], v[80:81] op_sel_hi:[1,0]\n" \
"v_cvt_pk_bf16_f32 v78, v78, v78\n" \
"v_cvt_pk_bf16_f32 v82, v72, v73\n" \
"v_cvt_pk_bf16_f32 v83, v74, v75\n" \
"v_and_b32 v84, v78, v214\n" \
"v_and_b32 v85, v78, v215\n" \
"v_cvt_pk_bf16_f32 v86, v72, v73\n" \
"v_cvt_pk_bf16_f32 v87, v74, v75\n" \
"s_waitcnt vmcnt(0)\n" \
"v_mfma_f32_16x16x32_bf16 v[88:91], v[48:51], v[82:85], 0\n" \
"v_mfma_f32_16x16x32_bf16 v[92:95], v[52:55], v[84:87], 0\n" \
"s_nop 2\n" \
"v_mov_b32 v245, v245\n" \
"s_nop 2\n" \
"v_mov_b32 v245, v245\n" \
"v_add_f32_dpp v96, v92, v88 row_ror:8 row_mask:0xf bank_mask:0x3\n" \
"v_add_f32_dpp v97, v93, v89 row_ror:8 row_mask:0xf bank_mask:0x3\n" \
"v_add_f32_dpp v96, v90, v94 row_ror:8 row_mask:0xf bank_mask:0xc\n" \
"v_add_f32_dpp v97, v91, v95 row_ror:8 row_mask:0xf bank_mask:0xc\n" \
"ds_write_b64 v216, v[96:97]\n" \
"v_cmp_u_f32 vcc, v96, v97\n" \
"s_bitcmp1_b32 s31, 21\n" \
"s_cselect_b32 s18, s29, 0\n" \
"s_xor_b32 s18, s18, s48\n" \
"v_mov_b32 v245, v245\n" \
"s_xor_b32 s19, s18, s29\n" \
"s_nop 0\n" \
"s_nop 0\n" \
"v_mov_b32 v245, v245\n" \
"s_nop 0\n" \
"s_nop 0\n" \
"s_nop 0\n" \
"v_mov_b32 v245, v245\n" \
"s_cbranch_vccnz .LP1_cls\n" \
".LP1_clsr:\n" \
"s_waitcnt lgkmcnt(0)\n" \
"s_barrier\n" \
"ds_read_b64 v[100:101], v217 offset:0\n" \
"ds_read_b64 v[102:103], v217 offset:512\n" \
"ds_read_b32 v116, v252 offset:16\n" \
"ds_read_b64 v[104:105], v217 offset:1024\n" \
"ds_read_b64 v[106:107], v217 offset:1536\n" \
"v_mov_b32 v245, v245\n" \
"v_mov_b32 v245, v245\n" \
"s_add_u32 s40, s40, 1\n" \
"s_add_u32 s24, s24, s27\n" \
"s_mov_b32 s47, s48\n" \
"v_mov_b32 v245, v245\n" \
"s_waitcnt lgkmcnt(2)\n" \
"v_xad_u32 v117, v116, s18, v213\n" \
"v_pk_add_f32 v[100:101], v[100:101], v[102:103]\n" \
"s_waitcnt lgkmcnt(0)\n" \
"v_pk_add_f32 v[104:105], v[104:105], v[106:107]\n" \
"v_pk_add_f32 v[100:101], v[100:101], v[104:105]\n" \
"v_cvt_pk_bf16_f32 v96, v100, v101\n" \
"s_add_u32 s43, s40, 1\n" \
"s_and_b32 s43, s43, 3\n" \
"s_bitcmp1_b32 s43, 0\n" \
"s_cselect_b32 s48, s32, s31\n" \
"s_lshl_b32 s43, s43, 19\n" \
"s_add_u32 s48, s48, s43\n" \
"s_add_u32 s43, s40, 2\n" \
"s_and_b32 s43, s43, 3\n" \
"buffer_store_dword v96, v117, s[4:7], 0 offen sc1\n" \
"s_bitcmp1_b32 s43, 0\n" \
"s_cselect_b32 s49, s32, s31\n" \
"s_lshl_b32 s43, s43, 19\n" \
"s_add_u32 s49, s49, s43\n" \
"s_sub_u32 s43, s24, s44\n" \
"s_add_u32 s43, s43, s28\n" \
"s_max_i32 s43, s43, 0xfffd0000\n" \
"s_min_i32 s43, s43, 0x4000\n" \
"s_mul_i32 s43, s43, s46\n" \
"s_ashr_i32 s43, s43, 12\n" \
"s_add_u32 s41, s45, s43\n" \
"s_lshr_b32 s35, s42, 1\n" \
"s_sub_u32 s35, 0, s35\n" \
"v_max_u32 v236, v236, v238\n" \
"v_max_u32 v237, v237, v239\n" \
"v_and_b32 v236, 0xfff, v236\n" \
"v_and_b32 v237, 0xfff, v237\n" \
"s_mov_b64 exec, 1\n" \
"ds_write_b64 v252, v[236:237] offset:24\n" \
"s_mov_b64 exec, -1\n" \
"v_xad_u32 v116, v116, s19, v213\n" \
"buffer_store_dword v96, v116, s[4:7], 0 offen sc1\n" \
"s_branch .LP1_loop\n" \
"s_branch .LP1_y1\n" \
".LP1_retry2:\n" \
"s_add_u32 s51, s51, 1\n" \
"s_cmp_gt_u32 s51, 0x20000\n" \
"s_cbranch_scc1 .LP1_end\n" \
".LP1_y0:\n" \
"s_add_u32 s41, s41, s42\n" \
"s_add_u32 s24, s24, s25\n" \
"s_xor_b32 s31, s31, s29\n" \
"s_xor_b32 s32, s32, s29\n" \
"s_xor_b32 s47, s47, s29\n" \
"s_xor_b32 s48, s48, s29\n" \
"s_add_u32 s39, s39, 1\n" \
"buffer_store_dwordx4 v[112:115], v223, s[4:7], s48 offen sc1\n" \
"s_bitcmp1_b32 s31, 21\n" \
"s_cselect_b32 s43, s29, 0\n" \
"v_mov_b32 v116, s43\n" \
"ds_write_b32 v252, v116 offset:16\n" \
".LP1_y1:\n" \
"s_memtime vcc\n" \
"s_waitcnt lgkmcnt(0)\n" \
"s_sub_u32 s43, s41, vcc_lo\n" \
"s_cmp_gt_i32 s43, 200\n" \
"s_cbranch_scc1 .LP1_y1\n" \
"s_cmp_lt_i32 s43, s37\n" \
"s_cbranch_scc1 .LP1_y0\n" \
"buffer_load_dwordx2 v[64:65], v211, s[4:7], s47 offen sc1\n" \
".p2align 6\n" \
".LP1_c1:\n" \
"s_memtime vcc\n" \
"s_waitcnt lgkmcnt(0)\n" \
"s_sub_u32 s43, s41, vcc_lo\n" \
"s_cmp_gt_i32 s43, 128\n" \
"s_cbranch_scc1 .LP1_c1\n" \
"s_cmp_lt_i32 s43, s37\n" \
"s_cbranch_scc1 .LP1_y0\n" \
"s_max_i32 s43, s43, 0\n" \
"s_and_b32 s43, s43, -4\n" \
"s_getpc_b64 vcc\n" \
".LP1_c3:\n" \
"s_sub_u32 s43, .LP1_c4-.LP1_c3, s43\n" \
"s_add_u32 vcc_lo, vcc_lo, s43\n" \
"s_addc_u32 vcc_hi, vcc_hi, 0\n" \
"s_setpc_b64 vcc\n" \
".rept 32\n" \
"s_nop 0\n" \
".endr\n" \
".LP1_c4:\n" \
"buffer_load_dwordx4 v[56:59], v210, s[4:7], s47 offen sc1\n" \
"buffer_load_dwordx4 v[60:63], v210, s[4:7], s47 offen offset:256 sc1\n" \
"s_nop 2\n" \
"v_mov_b32 v245, v245\n" \
"s_nop 2\n" \
"v_mov_b32 v245, v245\n" \
"s_nop 2\n" \
"v_mov_b32 v245, v245\n" \
"s_nop 2\n" \
"v_mov_b32 v245, v245\n" \
"s_nop 2\n" \
"v_mov_b32 v245, v245\n" \
"s_nop 2\n" \
"v_mov_b32 v245, v245\n" \
"buffer_load_dwordx4 v[50:53], v212, s[4:7], s47 offen sc1\n" \
"s_nop 2\n" \
"v_mov_b32 v245, v245\n" \
"s_nop 2\n" \
"v_mov_b32 v245, v245\n" \
"s_nop 2\n" \
"v_mov_b32 v245, v245\n" \
"s_nop 2\n" \
"v_mov_b32 v245, v245\n" \
"s_nop 2\n" \
"v_mov_b32 v245, v245\n" \
"s_nop 2\n" \
"v_mov_b32 v245, v245\n" \
"s_nop 2\n" \
"v_mov_b32 v245, v245\n" \
"s_nop 2\n" \
"v_mov_b32 v245, v245\n" \
"s_nop 2\n" \
"v_mov_b32 v245, v245\n" \
"s_nop 2\n" \
"v_mov_b32 v245, v245\n" \
"s_nop 2\n" \
"v_mov_b32 v245, v245\n" \
"s_nop 2\n" \
"v_mov_b32 v245, v245\n" \
"s_nop 2\n" \
"v_mov_b32 v245, v245\n" \
"s_nop 2\n" \
"v_mov_b32 v245, v245\n" \
"s_nop 2\n" \
"v_mov_b32 v245, v245\n" \
"s_nop 2\n" \
"v_mov_b32 v245, v245\n" \
"s_nop 2\n" \
"v_mov_b32 v245, v245\n" \
"s_branch .LP1_wait\n" \
".LP1_cls:\n" \
"s_waitcnt vmcnt(0)\n" \
"v_max3_u32 v44, v64, v64, v65\n" \
"v_max3_u32 v44, v44, v50, v51\n" \
"v_max3_u32 v44, v44, v52, v53\n" \
"v_cmp_eq_u32 vcc, -1, v44\n" \
"s_cbranch_vccnz .LP1_retry2\n" \
"v_cmp_u_f32 vcc, v56, v56\n" \
"v_cmp_ne_u32 s[2:3], v56, v109\n" \
"s_and_b64 vcc, vcc, s[2:3]\n" \
"s_cbranch_vccnz .LP1_retry2\n" \
"v_cmp_u_f32 vcc, v57, v57\n" \
"v_cmp_ne_u32 s[2:3], v57, v109\n" \
"s_and_b64 vcc, vcc, s[2:3]\n" \
"s_cbranch_vccnz .LP1_retry2\n" \
"v_cmp_u_f32 vcc, v58, v58\n" \
"v_cmp_ne_u32 s[2:3], v58, v109\n" \
"s_and_b64 vcc, vcc, s[2:3]\n" \
"s_cbranch_vccnz .LP1_retry2\n" \
"v_cmp_u_f32 vcc, v59, v59\n" \
"v_cmp_ne_u32 s[2:3], v59, v109\n" \
"s_and_b64 vcc, vcc, s[2:3]\n" \
"s_cbranch_vccnz .LP1_retry2\n" \
"v_cmp_u_f32 vcc, v60, v60\n" \
"v_cmp_ne_u32 s[2:3], v60, v109\n" \
"s_and_b64 vcc, vcc, s[2:3]\n" \
"s_cbranch_vccnz .LP1_retry2\n" \
"v_cmp_u_f32 vcc, v61, v61\n" \
"v_cmp_ne_u32 s[2:3], v61, v109\n" \
"s_and_b64 vcc, vcc, s[2:3]\n" \
"s_cbranch_vccnz .LP1_retry2\n" \
"v_cmp_u_f32 vcc, v62, v62\n" \
"v_cmp_ne_u32 s[2:3], v62, v109\n" \
"s_and_b64 vcc, vcc, s[2:3]\n" \
"s_cbranch_vccnz .LP1_retry2\n" \
"v_cmp_u_f32 vcc, v63, v63\n" \
"v_cmp_ne_u32 s[2:3], v63, v109\n" \
"s_and_b64 vcc, vcc, s[2:3]\n" \
"s_cbranch_vccnz .LP1_retry2\n" \
"s_branch .LP1_clsr\n" \
".LP1_last:\n" \
"v_smfmac_f32_16x16x64_bf16 v[28:31], v[16:19], a[32:39], v200\n" \
"v_smfmac_f32_16x16x64_bf16 v[32:35], v[16:19], a[88:95], v200\n" \
"v_smfmac_f32_16x16x64_bf16 v[36:39], v[16:19], a[144:151], v200\n" \
"v_smfmac_f32_16x16x64_bf16 v[40:43], v[16:19], a[200:207], v200\n" \
"v_smfmac_f32_16x16x64_bf16 v[28:31], v[20:23], a[40:47], v200\n" \
"v_smfmac_f32_16x16x64_bf16 v[32:35], v[20:23], a[96:103], v200\n" \
"v_smfmac_f32_16x16x64_bf16 v[36:39], v[20:23], a[152:159], v200\n" \
"v_smfmac_f32_16x16x64_bf16 v[40:43], v[20:23], a[208:215], v200\n" \
"v_smfmac_f32_16x16x64_bf16 v[28:31], v[24:27], a[48:55], v200\n" \
"v_smfmac_f32_16x16x64_bf16 v[32:35], v[24:27], a[104:111], v200\n" \
"v_smfmac_f32_16x16x64_bf16 v[36:39], v[24:27], a[160:167], v200\n" \
"v_smfmac_f32_16x16x64_bf16 v[40:43], v[24:27], a[216:223], v200\n" \
"s_waitcnt vmcnt(0)\n" \
"s_nop 7\n" \
"v_permlane32_swap_b32 v28, v32\n" \
"v_permlane32_swap_b32 v29, v33\n" \
"v_permlane32_swap_b32 v30, v34\n" \
"v_permlane32_swap_b32 v31, v35\n" \
"v_add_f32 v28, v28, v32\n" \
"v_add_f32 v29, v29, v33\n" \
"v_add_f32 v30, v30, v34\n" \
"v_add_f32 v31, v31, v35\n" \
"v_cvt_pk_bf16_f32 v68, v28, v29\n" \
"v_cvt_pk_bf16_f32 v69, v30, v31\n" \
"v_permlane32_swap_b32 v36, v40\n" \
"v_permlane32_swap_b32 v37, v41\n" \
"v_permlane32_swap_b32 v38, v42\n" \
"v_permlane32_swap_b32 v39, v43\n" \
"v_add_f32 v36, v36, v40\n" \
"v_add_f32 v37, v37, v41\n" \
"v_add_f32 v38, v38, v42\n" \
"v_add_f32 v39, v39, v43\n" \
"v_cvt_pk_bf16_f32 v70, v36, v37\n" \
"v_cvt_pk_bf16_f32 v71, v38, v39\n" \
"s_mov_b32 s50, 7340032\n" \
"buffer_store_dwordx4 v[68:71], v218, s[12:15], s50 offen sc1\n" \
"s_waitcnt vmcnt(0)\n" \
"s_barrier\n" \
"v_mov_b32 v241, s30\n" \
"buffer_store_dword v241, v222, s[4:7], 0 offen sc1\n" \
".LP1_end:\n" \
"s_endpgm\n" \
".p2align 6\n" \
"fk_role_P2:\n" \
"ds_read_b32 v200, v255 offset:0\n" \
"ds_read_b32 v201, v255 offset:256\n" \
"ds_read_b32 v202, v255 offset:512\n" \
"ds_read_b32 v203, v255 offset:768\n" \
"ds_read_b32 v204, v255 offset:1024\n" \
"ds_read_b32 v205, v255 offset:1280\n" \
"ds_read_b32 v206, v255 offset:1536\n" \
"ds_read_b32 v207, v255 offset:1792\n" \
"ds_read_b32 v208, v255 offset:2048\n" \
"ds_read_b32 v209, v255 offset:2304\n" \
"ds_read_b32 v210, v255 offset:2560\n" \
"ds_read_b32 v211, v255 offset:2816\n" \
"ds_read_b32 v212, v255 offset:3072\n" \
"ds_read_b32 v213, v255 offset:3328\n" \
"ds_read_b32 v214, v255 offset:3584\n" \
"ds_read_b32 v215, v255 offset:3840\n" \
"ds_read_b32 v216, v255 offset:4096\n" \
"ds_read_b32 v217, v255 offset:4352\n" \
"ds_read_b32 v218, v255 offset:4608\n" \
"ds_read_b32 v219, v255 offset:4864\n" \
"ds_read_b32 v220, v255 offset:5120\n" \
"ds_read_b32 v221, v255 offset:5376\n" \
"ds_read_b32 v222, v255 offset:5632\n" \
"ds_read_b32 v223, v255 offset:5888\n" \
"ds_read_b32 v254, v255 offset:7936\n" \
"s_waitcnt lgkmcnt(0)\n" \
"v_readlane_b32 s4, v254, 0\n" \
"v_readlane_b32 s5, v254, 1\n" \
"v_readlane_b32 s6, v254, 2\n" \
"v_readlane_b32 s7, v254, 3\n" \
"v_readlane_b32 s8, v254, 4\n" \
"v_readlane_b32 s9, v254, 5\n" \
"v_readlane_b32 s10, v254, 6\n" \
"v_readlane_b32 s11, v254, 7\n" \
"v_readlane_b32 s12, v254, 8\n" \
"v_readlane_b32 s13, v254, 9\n" \
"v_readlane_b32 s14, v254, 10\n" \
"v_readlane_b32 s15, v254, 11\n" \
"v_readlane_b32 s16, v254, 12\n" \
"v_readlane_b32 s17, v254, 13\n" \
"v_readlane_b32 s18, v254, 14\n" \
"v_readlane_b32 s19, v254, 15\n" \
"v_readlane_b32 s20, v254, 16\n" \
"v_readlane_b32 s21, v254, 17\n" \
"v_readlane_b32 s22, v254, 18\n" \
"v_readlane_b32 s23, v254, 19\n" \
"v_readlane_b32 s24, v254, 20\n" \
"v_readlane_b32 s25, v254, 21\n" \
"v_readlane_b32 s26, v254, 22\n" \
"v_readlane_b32 s27, v254, 23\n" \
"v_readlane_b32 s28, v254, 24\n" \
"v_readlane_b32 s29, v254, 25\n" \
"v_readlane_b32 s30, v254, 26\n" \
"v_readlane_b32 s31, v254, 27\n" \
"v_readlane_b32 s32, v254, 28\n" \
"v_readlane_b32 s33, v254, 29\n" \
"v_readlane_b32 s34, v254, 30\n" \
"v_readlane_b32 s35, v254, 31\n" \
"v_readlane_b32 s36, v254, 32\n" \
"v_readlane_b32 s37, v254, 33\n" \
"v_readlane_b32 s38, v254, 34\n" \
"v_readlane_b32 s39, v254, 35\n" \
"s_mov_b32 s40, 0\n" \
"s_mov_b32 s51, 0\n" \
"s_nop 0\n" \
"v_mov_b32 v252, s38\n" \
"s_nop 4\n" \
"s_add_u32 s43, s30, 1\n" \
"s_mov_b32 s50, 0\n" \
".LP2_w43l:\n" \
"buffer_load_dword v240, v204, s[4:7], 0 offen sc1\n" \
"s_waitcnt vmcnt(0)\n" \
".LP2_w43c:\n" \
"v_subrev_u32 v241, s43, v240\n" \
"v_cmp_gt_u32 vcc, 0x100, v241\n" \
"s_cmp_eq_u64 vcc, -1\n" \
"s_cbranch_scc1 .LP2_w43d\n" \
"s_sleep 2\n" \
"s_add_u32 s50, s50, 1\n" \
"s_cmp_lt_u32 s50, 0x20000\n" \
"s_cbranch_scc1 .LP2_w43l\n" \
"s_branch .LP2_end\n" \
".LP2_w43d:\n" \
"v_readlane_b32 s43, v202, 2\n" \
"s_nop 4\n" \
"buffer_load_dwordx4 a[16:19], v201, s[8:11], s43 offen sc1\n" \
"buffer_load_dwordx4 a[20:23], v201, s[8:11], s43 offen offset:1024 sc1\n" \
"v_readlane_b32 s43, v202, 9\n" \
"s_nop 4\n" \
"buffer_load_dwordx4 a[72:75], v201, s[8:11], s43 offen sc1\n" \
"buffer_load_dwordx4 a[76:79], v201, s[8:11], s43 offen offset:1024 sc1\n" \
"v_readlane_b32 s43, v202, 16\n" \
"s_nop 4\n" \
"buffer_load_dwordx4 a[128:131], v201, s[8:11], s43 offen sc1\n" \
"buffer_load_dwordx4 a[132:135], v201, s[8:11], s43 offen offset:1024 sc1\n" \
"v_readlane_b32 s43, v202, 23\n" \
"s_nop 4\n" \
"buffer_load_dwordx4 a[184:187], v201, s[8:11], s43 offen sc1\n" \
"buffer_load_dwordx4 a[188:191], v201, s[8:11], s43 offen offset:1024 sc1\n" \
"s_add_u32 s43, s30, 2\n" \
"s_mov_b32 s50, 0\n" \
".LP2_w44l:\n" \
"buffer_load_dword v240, v204, s[4:7], 0 offen sc1\n" \
"s_waitcnt vmcnt(0)\n" \
".LP2_w44c:\n" \
"v_subrev_u32 v241, s43, v240\n" \
"v_cmp_gt_u32 vcc, 0x100, v241\n" \
"s_cmp_eq_u64 vcc, -1\n" \
"s_cbranch_scc1 .LP2_w44d\n" \
"s_sleep 2\n" \
"s_add_u32 s50, s50, 1\n" \
"s_cmp_lt_u32 s50, 0x20000\n" \
"s_cbranch_scc1 .LP2_w44l\n" \
"s_branch .LP2_end\n" \
".LP2_w44d:\n" \
"v_readlane_b32 s43, v202, 3\n" \
"s_nop 4\n" \
"buffer_load_dwordx4 a[24:27], v201, s[8:11], s43 offen sc1\n" \
"buffer_load_dwordx4 a[28:31], v201, s[8:11], s43 offen offset:1024 sc1\n" \
"v_readlane_b32 s43, v202, 10\n" \
"s_nop 4\n" \
"buffer_load_dwordx4 a[80:83], v201, s[8:11], s43 offen sc1\n" \
"buffer_load_dwordx4 a[84:87], v201, s[8:11], s43 offen offset:1024 sc1\n" \
"v_readlane_b32 s43, v202, 17\n" \
"s_nop 4\n" \
"buffer_load_dwordx4 a[136:139], v201, s[8:11], s43 offen sc1\n" \
"buffer_load_dwordx4 a[140:143], v201, s[8:11], s43 offen offset:1024 sc1\n" \
"v_readlane_b32 s43, v202, 24\n" \
"s_nop 4\n" \
"buffer_load_dwordx4 a[192:195], v201, s[8:11], s43 offen sc1\n" \
"buffer_load_dwordx4 a[196:199], v201, s[8:11], s43 offen offset:1024 sc1\n" \
"s_add_u32 s43, s30, 3\n" \
"s_mov_b32 s50, 0\n" \
".LP2_w45l:\n" \
"buffer_load_dword v240, v204, s[4:7], 0 offen sc1\n" \
"s_waitcnt vmcnt(0)\n" \
".LP2_w45c:\n" \
"v_subrev_u32 v241, s43, v240\n" \
"v_cmp_gt_u32 vcc, 0x100, v241\n" \
"s_cmp_eq_u64 vcc, -1\n" \
"s_cbranch_scc1 .LP2_w45d\n" \
"s_sleep 2\n" \
"s_add_u32 s50, s50, 1\n" \
"s_cmp_lt_u32 s50, 0x20000\n" \
"s_cbranch_scc1 .LP2_w45l\n" \
"s_branch .LP2_end\n" \
".LP2_w45d:\n" \
"v_readlane_b32 s43, v202, 4\n" \
"s_nop 4\n" \
"buffer_load_dwordx4 a[32:35], v201, s[8:11], s43 offen sc1\n" \
"buffer_load_dwordx4 a[36:39], v201, s[8:11], s43 offen offset:1024 sc1\n" \
"v_readlane_b32 s43, v202, 11\n" \
"s_nop 4\n" \
"buffer_load_dwordx4 a[88:91], v201, s[8:11], s43 offen sc1\n" \
"buffer_load_dwordx4 a[92:95], v201, s[8:11], s43 offen offset:1024 sc1\n" \
"v_readlane_b32 s43, v202, 18\n" \
"s_nop 4\n" \
"buffer_load_dwordx4 a[144:147], v201, s[8:11], s43 offen sc1\n" \
"buffer_load_dwordx4 a[148:151], v201, s[8:11], s43 offen offset:1024 sc1\n" \
"v_readlane_b32 s43, v202, 25\n" \
"s_nop 4\n" \
"buffer_load_dwordx4 a[200:203], v201, s[8:11], s43 offen sc1\n" \
"buffer_load_dwordx4 a[204:207], v201, s[8:11], s43 offen offset:1024 sc1\n" \
"s_add_u32 s43, s30, 4\n" \
"s_mov_b32 s50, 0\n" \
".LP2_w46l:\n" \
"buffer_load_dword v240, v204, s[4:7], 0 offen sc1\n" \
"s_waitcnt vmcnt(0)\n" \
".LP2_w46c:\n" \
"v_subrev_u32 v241, s43, v240\n" \
"v_cmp_gt_u32 vcc, 0x100, v241\n" \
"s_cmp_eq_u64 vcc, -1\n" \
"s_cbranch_scc1 .LP2_w46d\n" \
"s_sleep 2\n" \
"s_add_u32 s50, s50, 1\n" \
"s_cmp_lt_u32 s50, 0x20000\n" \
"s_cbranch_scc1 .LP2_w46l\n" \
"s_branch .LP2_end\n" \
".LP2_w46d:\n" \
"v_readlane_b32 s43, v202, 5\n" \
"s_nop 4\n" \
"buffer_load_dwordx4 a[40:43], v201, s[8:11], s43 offen sc1\n" \
"buffer_load_dwordx4 a[44:47], v201, s[8:11], s43 offen offset:1024 sc1\n" \
"v_readlane_b32 s43, v202, 12\n" \
"s_nop 4\n" \
"buffer_load_dwordx4 a[96:99], v201, s[8:11], s43 offen sc1\n" \
"buffer_load_dwordx4 a[100:103], v201, s[8:11], s43 offen offset:1024 sc1\n" \
"v_readlane_b32 s43, v202, 19\n" \
"s_nop 4\n" \
"buffer_load_dwordx4 a[152:155], v201, s[8:11], s43 offen sc1\n" \
"buffer_load_dwordx4 a[156:159], v201, s[8:11], s43 offen offset:1024 sc1\n" \
"v_readlane_b32 s43, v202, 26\n" \
"s_nop 4\n" \
"buffer_load_dwordx4 a[208:211], v201, s[8:11], s43 offen sc1\n" \
"buffer_load_dwordx4 a[212:215], v201, s[8:11], s43 offen offset:1024 sc1\n" \
"s_add_u32 s43, s30, 5\n" \
"s_mov_b32 s50, 0\n" \
".LP2_w47l:\n" \
"buffer_load_dword v240, v204, s[4:7], 0 offen sc1\n" \
"s_waitcnt vmcnt(0)\n" \
".LP2_w47c:\n" \
"v_subrev_u32 v241, s43, v240\n" \
"v_cmp_gt_u32 vcc, 0x100, v241\n" \
"s_cmp_eq_u64 vcc, -1\n" \
"s_cbranch_scc1 .LP2_w47d\n" \
"s_sleep 2\n" \
"s_add_u32 s50, s50, 1\n" \
"s_cmp_lt_u32 s50, 0x20000\n" \
"s_cbranch_scc1 .LP2_w47l\n" \
"s_branch .LP2_end\n" \
".LP2_w47d:\n" \
"v_readlane_b32 s43, v202, 6\n" \
"s_nop 4\n" \
"buffer_load_dwordx4 a[48:51], v201, s[8:11], s43 offen sc1\n" \
"buffer_load_dwordx4 a[52:55], v201, s[8:11], s43 offen offset:1024 sc1\n" \
"v_readlane_b32 s43, v202, 13\n" \
"s_nop 4\n" \
"buffer_load_dwordx4 a[104:107], v201, s[8:11], s43 offen sc1\n" \
"buffer_load_dwordx4 a[108:111], v201, s[8:11], s43 offen offset:1024 sc1\n" \
"v_readlane_b32 s43, v202, 20\n" \
"s_nop 4\n" \
"buffer_load_dwordx4 a[160:163], v201, s[8:11], s43 offen sc1\n" \
"buffer_load_dwordx4 a[164:167], v201, s[8:11], s43 offen offset:1024 sc1\n" \
"v_readlane_b32 s43, v202, 27\n" \
"s_nop 4\n" \
"buffer_load_dwordx4 a[216:219], v201, s[8:11], s43 offen sc1\n" \
"buffer_load_dwordx4 a[220:223], v201, s[8:11], s43 offen offset:1024 sc1\n" \
"s_add_u32 s43, s30, 6\n" \
"s_mov_b32 s50, 0\n" \
".LP2_w48l:\n" \
"buffer_load_dword v240, v204, s[4:7], 0 offen sc1\n" \
"s_waitcnt vmcnt(0)\n" \
".LP2_w48c:\n" \
"v_subrev_u32 v241, s43, v240\n" \
"v_cmp_gt_u32 vcc, 0x100, v241\n" \
"s_cmp_eq_u64 vcc, -1\n" \
"s_cbranch_scc1 .LP2_w48d\n" \
"s_sleep 2\n" \
"s_add_u32 s50, s50, 1\n" \
"s_cmp_lt_u32 s50, 0x20000\n" \
"s_cbranch_scc1 .LP2_w48l\n" \
"s_branch .LP2_end\n" \
".LP2_w48d:\n" \
"v_readlane_b32 s43, v202, 0\n" \
"s_nop 4\n" \
"buffer_load_dwordx4 a[0:3], v201, s[8:11], s43 offen sc1\n" \
"buffer_load_dwordx4 a[4:7], v201, s[8:11], s43 offen offset:1024 sc1\n" \
"v_readlane_b32 s43, v202, 7\n" \
"s_nop 4\n" \
"buffer_load_dwordx4 a[56:59], v201, s[8:11], s43 offen sc1\n" \
"buffer_load_dwordx4 a[60:63], v201, s[8:11], s43 offen offset:1024 sc1\n" \
"v_readlane_b32 s43, v202, 14\n" \
"s_nop 4\n" \
"buffer_load_dwordx4 a[112:115], v201, s[8:11], s43 offen sc1\n" \
"buffer_load_dwordx4 a[116:119], v201, s[8:11], s43 offen offset:1024 sc1\n" \
"v_readlane_b32 s43, v202, 21\n" \
"s_nop 4\n" \
"buffer_load_dwordx4 a[168:171], v201, s[8:11], s43 offen sc1\n" \
"buffer_load_dwordx4 a[172:175], v201, s[8:11], s43 offen offset:1024 sc1\n" \
"s_add_u32 s43, s30, 7\n" \
"s_mov_b32 s50, 0\n" \
".LP2_w49l:\n" \
"buffer_load_dword v240, v204, s[4:7], 0 offen sc1\n" \
"s_waitcnt vmcnt(0)\n" \
".LP2_w49c:\n" \
"v_subrev_u32 v241, s43, v240\n" \
"v_cmp_gt_u32 vcc, 0x100, v241\n" \
"s_cmp_eq_u64 vcc, -1\n" \
"s_cbranch_scc1 .LP2_w49d\n" \
"s_sleep 2\n" \
"s_add_u32 s50, s50, 1\n" \
"s_cmp_lt_u32 s50, 0x20000\n" \
"s_cbranch_scc1 .LP2_w49l\n" \
"s_branch .LP2_end\n" \
".LP2_w49d:\n" \
"v_readlane_b32 s43, v202, 1\n" \
"s_nop 4\n" \
"buffer_load_dwordx4 a[8:11], v201, s[8:11], s43 offen sc1\n" \
"buffer_load_dwordx4 a[12:15], v201, s[8:11], s43 offen offset:1024 sc1\n" \
"v_readlane_b32 s43, v202, 8\n" \
"s_nop 4\n" \
"buffer_load_dwordx4 a[64:67], v201, s[8:11], s43 offen sc1\n" \
"buffer_load_dwordx4 a[68:71], v201, s[8:11], s43 offen offset:1024 sc1\n" \
"v_readlane_b32 s43, v202, 15\n" \
"s_nop 4\n" \
"buffer_load_dwordx4 a[120:123], v201, s[8:11], s43 offen sc1\n" \
"buffer_load_dwordx4 a[124:127], v201, s[8:11], s43 offen offset:1024 sc1\n" \
"v_readlane_b32 s43, v202, 22\n" \
"s_nop 4\n" \
"buffer_load_dwordx4 a[176:179], v201, s[8:11], s43 offen sc1\n" \
"buffer_load_dwordx4 a[180:183], v201, s[8:11], s43 offen offset:1024 sc1\n" \
"v_mov_b32 v112, -1\n" \
"v_mov_b32 v113, -1\n" \
"v_mov_b32 v114, -1\n" \
"v_mov_b32 v115, -1\n" \
"v_mov_b32 v109, 0x7fc00001\n" \
"s_add_u32 s43, s30, 1\n" \
"s_mov_b32 s50, 0\n" \
".LP2_w50l:\n" \
"buffer_load_dword v240, v205, s[4:7], 0 offen sc1\n" \
"s_waitcnt vmcnt(0)\n" \
".LP2_w50c:\n" \
"v_subrev_u32 v241, s43, v240\n" \
"v_cmp_gt_u32 vcc, 0x100, v241\n" \
"s_cmp_eq_u64 vcc, -1\n" \
"s_cbranch_scc1 .LP2_w50d\n" \
"s_sleep 2\n" \
"s_add_u32 s50, s50, 1\n" \
"s_cmp_lt_u32 s50, 0x20000\n" \
"s_cbranch_scc1 .LP2_w50l\n" \
"s_branch .LP2_end\n" \
".LP2_w50d:\n" \
"s_add_u32 s43, s30, 1\n" \
"s_mov_b32 s50, 0\n" \
".LP2_w51l:\n" \
"buffer_load_dword v240, v206, s[4:7], 0 offen sc1\n" \
"s_waitcnt vmcnt(0)\n" \
".LP2_w51c:\n" \
"v_subrev_u32 v241, s43, v240\n" \
"v_cmp_gt_u32 vcc, 0x100, v241\n" \
"s_cmp_eq_u64 vcc, -1\n" \
"s_cbranch_scc1 .LP2_w51d\n" \
"s_sleep 2\n" \
"s_add_u32 s50, s50, 1\n" \
"s_cmp_lt_u32 s50, 0x20000\n" \
"s_cbranch_scc1 .LP2_w51l\n" \
"s_branch .LP2_end\n" \
".LP2_w51d:\n" \
"s_add_u32 s43, s40, 0\n" \
"s_and_b32 s43, s43, 3\n" \
"s_bitcmp1_b32 s43, 0\n" \
"s_cselect_b32 s47, s32, s31\n" \
"s_lshl_b32 s43, s43, 19\n" \
"s_add_u32 s47, s47, s43\n" \
"buffer_store_dword v219, v213, s[4:7], s47 offen sc1\n" \
"s_xor_b32 s43, s47, s29\n" \
"buffer_store_dword v219, v213, s[4:7], s43 offen sc1\n" \
"s_barrier\n" \
"ds_read_b96 v[248:250], v252\n" \
"s_waitcnt lgkmcnt(0)\n" \
"v_readfirstlane_b32 s44, v248\n" \
"v_readfirstlane_b32 s45, v249\n" \
"v_readfirstlane_b32 s46, v250\n" \
"s_mul_i32 s42, s25, s46\n" \
"s_lshr_b32 s42, s42, 12\n" \
"s_add_u32 s43, s40, 1\n" \
"s_and_b32 s43, s43, 3\n" \
"s_bitcmp1_b32 s43, 0\n" \
"s_cselect_b32 s48, s32, s31\n" \
"s_lshl_b32 s43, s43, 19\n" \
"s_add_u32 s48, s48, s43\n" \
"s_add_u32 s43, s40, 2\n" \
"s_and_b32 s43, s43, 3\n" \
"s_bitcmp1_b32 s43, 0\n" \
"s_cselect_b32 s49, s32, s31\n" \
"s_lshl_b32 s43, s43, 19\n" \
"s_add_u32 s49, s49, s43\n" \
"s_sub_u32 s43, s24, s44\n" \
"s_add_u32 s43, s43, s28\n" \
"s_max_i32 s43, s43, 0xfffd0000\n" \
"s_min_i32 s43, s43, 0x4000\n" \
"s_mul_i32 s43, s43, s46\n" \
"s_ashr_i32 s43, s43, 12\n" \
"s_add_u32 s41, s45, s43\n" \
"s_lshr_b32 s35, s42, 1\n" \
"s_sub_u32 s35, 0, s35\n" \
".LP2_loop:\n" \
"s_branch .LP2_m1\n" \
".LP2_retry1:\n" \
"s_add_u32 s51, s51, 1\n" \
"s_cmp_gt_u32 s51, 0x20000\n" \
"s_cbranch_scc1 .LP2_end\n" \
".LP2_m0:\n" \
"s_add_u32 s41, s41, s42\n" \
"s_add_u32 s24, s24, s25\n" \
"s_xor_b32 s31, s31, s29\n" \
"s_xor_b32 s32, s32, s29\n" \
"s_xor_b32 s47, s47, s29\n" \
"s_xor_b32 s48, s48, s29\n" \
"s_add_u32 s39, s39, 1\n" \
"buffer_store_dwordx4 v[112:115], v223, s[4:7], s48 offen sc1\n" \
"s_bitcmp1_b32 s31, 21\n" \
"s_cselect_b32 s43, s29, 0\n" \
"v_mov_b32 v116, s43\n" \
"ds_write_b32 v252, v116 offset:16\n" \
".LP2_m1:\n" \
"s_memtime vcc\n" \
"s_waitcnt lgkmcnt(0)\n" \
"s_sub_u32 s43, s41, vcc_lo\n" \
"s_cmp_gt_i32 s43, 0\n" \
"s_cbranch_scc1 .LP2_m1\n" \
"s_cmp_lt_i32 s43, s35\n" \
"s_cbranch_scc1 .LP2_m0\n" \
"buffer_load_dwordx4 v[0:3], v209, s[4:7], s47 offen offset:1024 sc1\n" \
"buffer_load_dwordx4 v[4:7], v209, s[4:7], s47 offen offset:2048 sc1\n" \
"s_cmp_lg_u32 s39, s34\n" \
"s_cselect_b32 s43, 4, 0\n" \
"s_cmp_lt_u32 s40, 6\n" \
"s_cselect_b32 s43, 0, s43\n" \
"s_add_u32 s33, s33, s43\n" \
"s_min_u32 s33, s33, 40\n" \
"s_mov_b32 s34, s39\n" \
"s_cmp_lt_u32 s40, 4\n" \
"s_cselect_b32 s50, 60, 0\n" \
"s_add_u32 s50, s50, s33\n" \
"s_sub_u32 s43, 6042, s46\n" \
"s_mul_i32 s36, s43, 150\n" \
"s_ashr_i32 s36, s36, 10\n" \
"s_add_u32 s36, s36, s50\n" \
"s_add_u32 s36, s36, 1060\n" \
"v_mov_b32 v110, s36\n" \
"s_mul_i32 s36, s43, 110\n" \
"s_ashr_i32 s36, s36, 10\n" \
"s_add_u32 s36, s36, s50\n" \
"s_add_u32 s36, s36, 845\n" \
"v_mov_b32 v111, s36\n" \
"s_nop 15\n" \
"s_nop 13\n" \
"s_mul_i32 s50, s40, 114688\n" \
"buffer_store_dwordx4 v[68:71], v218, s[12:15], s50 offen sc1\n" \
"buffer_store_dword v112, v213, s[4:7], s49 offen sc1\n" \
"s_xor_b32 s43, s49, s29\n" \
"buffer_store_dword v112, v213, s[4:7], s43 offen sc1\n" \
"s_lshl_b32 s43, s40, 7\n" \
"buffer_store_dwordx2 v[110:111], v222, s[4:7], s43 offen offset:384 sc1\n" \
"v_mov_b32 v28, 0\n" \
"v_mov_b32 v29, 0\n" \
"v_mov_b32 v30, 0\n" \
"v_mov_b32 v31, 0\n" \
"v_mov_b32 v32, 0\n" \
"v_mov_b32 v33, 0\n" \
"v_mov_b32 v34, 0\n" \
"v_mov_b32 v35, 0\n" \
"v_mov_b32 v36, 0\n" \
"v_mov_b32 v37, 0\n" \
"v_mov_b32 v38, 0\n" \
"v_mov_b32 v39, 0\n" \
"v_mov_b32 v40, 0\n" \
"v_mov_b32 v41, 0\n" \
"v_mov_b32 v42, 0\n" \
"v_mov_b32 v43, 0\n" \
"s_waitcnt vmcnt(5)\n" \
"ds_write_b128 v220, v[0:3] offset:5120\n" \
"s_waitcnt vmcnt(4)\n" \
"ds_write_b128 v220, v[4:7] offset:6144\n" \
"v_max3_u32 v44, v0, v0, v1\n" \
"v_max3_u32 v44, v44, v2, v3\n" \
"v_max3_u32 v44, v44, v4, v5\n" \
"v_max3_u32 v44, v44, v6, v7\n" \
"v_cmp_eq_u32 vcc, -1, v44\n" \
"v_smfmac_f32_16x16x64_bf16 v[28:31], v[0:3], a[0:7], v200\n" \
"v_smfmac_f32_16x16x64_bf16 v[32:35], v[0:3], a[56:63], v200\n" \
"s_cbranch_vccnz .LP2_retry1\n" \
"s_waitcnt lgkmcnt(0)\n" \
"s_barrier\n" \
"ds_read_b128 v[8:11], v220 offset:0\n" \
"ds_read_b128 v[12:15], v220 offset:1024\n" \
"ds_read_b128 v[16:19], v220 offset:2048\n" \
"ds_read_b128 v[20:23], v220 offset:3072\n" \
"ds_read_b128 v[24:27], v220 offset:4096\n" \
"ds_read_b64 v[246:247], v252 offset:24\n" \
"ds_read_b96 v[248:250], v252\n" \
"v_smfmac_f32_16x16x64_bf16 v[36:39], v[0:3], a[112:119], v200\n" \
"v_smfmac_f32_16x16x64_bf16 v[40:43], v[0:3], a[168:175], v200\n" \
"v_smfmac_f32_16x16x64_bf16 v[28:31], v[4:7], a[8:15], v200\n" \
"v_smfmac_f32_16x16x64_bf16 v[32:35], v[4:7], a[64:71], v200\n" \
"v_smfmac_f32_16x16x64_bf16 v[36:39], v[4:7], a[120:127], v200\n" \
"v_smfmac_f32_16x16x64_bf16 v[40:43], v[4:7], a[176:183], v200\n" \
"s_waitcnt lgkmcnt(6)\n" \
"v_smfmac_f32_16x16x64_bf16 v[28:31], v[8:11], a[16:23], v200\n" \
"v_smfmac_f32_16x16x64_bf16 v[32:35], v[8:11], a[72:79], v200\n" \
"v_smfmac_f32_16x16x64_bf16 v[36:39], v[8:11], a[128:135], v200\n" \
"v_smfmac_f32_16x16x64_bf16 v[40:43], v[8:11], a[184:191], v200\n" \
"s_waitcnt lgkmcnt(0)\n" \
"v_readfirstlane_b32 s44, v248\n" \
"v_readfirstlane_b32 s45, v249\n" \
"v_smfmac_f32_16x16x64_bf16 v[28:31], v[12:15], a[24:31], v200\n" \
"v_readfirstlane_b32 s46, v250\n" \
"v_readfirstlane_b32 s26, v246\n" \
"v_readfirstlane_b32 s27, v247\n" \
"v_smfmac_f32_16x16x64_bf16 v[32:35], v[12:15], a[80:87], v200\n" \
"s_mul_i32 s42, s25, s46\n" \
"s_lshr_b32 s42, s42, 12\n" \
"s_add_u32 s24, s24, s26\n" \
"v_smfmac_f32_16x16x64_bf16 v[36:39], v[12:15], a[136:143], v200\n" \
"s_sub_u32 s43, s24, s44\n" \
"s_max_i32 s43, s43, 0xfffd0000\n" \
"s_min_i32 s43, s43, 0x4000\n" \
"v_smfmac_f32_16x16x64_bf16 v[40:43], v[12:15], a[192:199], v200\n" \
"s_mul_i32 s43, s43, s46\n" \
"s_ashr_i32 s43, s43, 12\n" \
"s_add_u32 s41, s45, s43\n" \
"s_cmp_eq_u32 s40, 63\n" \
"s_cbranch_scc1 .LP2_last\n" \
"s_branch .LP2_z1\n" \
".LP2_z0:\n" \
"s_add_u32 s41, s41, s42\n" \
"s_add_u32 s24, s24, s25\n" \
"s_xor_b32 s31, s31, s29\n" \
"s_xor_b32 s32, s32, s29\n" \
"s_xor_b32 s47, s47, s29\n" \
"s_xor_b32 s48, s48, s29\n" \
"s_add_u32 s39, s39, 1\n" \
"buffer_store_dwordx4 v[112:115], v223, s[4:7], s48 offen sc1\n" \
"s_bitcmp1_b32 s31, 21\n" \
"s_cselect_b32 s43, s29, 0\n" \
"v_mov_b32 v116, s43\n" \
"ds_write_b32 v252, v116 offset:16\n" \
".LP2_z1:\n" \
"s_memtime vcc\n" \
"s_waitcnt lgkmcnt(0)\n" \
"s_sub_u32 s43, s41, vcc_lo\n" \
"s_cmp_gt_i32 s43, 200\n" \
"s_cbranch_scc1 .LP2_z1\n" \
"s_cmp_lt_i32 s43, s37\n" \
"s_cbranch_scc1 .LP2_z0\n" \
"buffer_load_dwordx2 v[64:65], v211, s[4:7], s47 offen sc1\n" \
".p2align 6\n" \
".LP2_b1:\n" \
"s_memtime vcc\n" \
"s_waitcnt lgkmcnt(0)\n" \
"s_sub_u32 s43, s41, vcc_lo\n" \
"s_cmp_gt_i32 s43, 128\n" \
"s_cbranch_scc1 .LP2_b1\n" \
"s_cmp_lt_i32 s43, s37\n" \
"s_cbranch_scc1 .LP2_z0\n" \
"s_max_i32 s43, s43, 0\n" \
"s_and_b32 s43, s43, -4\n" \
"s_getpc_b64 vcc\n" \
".LP2_b3:\n" \
"s_sub_u32 s43, .LP2_b4-.LP2_b3, s43\n" \
"s_add_u32 vcc_lo, vcc_lo, s43\n" \
"s_addc_u32 vcc_hi, vcc_hi, 0\n" \
"s_setpc_b64 vcc\n" \
".rept 32\n" \
"s_nop 0\n" \
".endr\n" \
".LP2_b4:\n" \
"buffer_load_dwordx4 v[56:59], v210, s[4:7], s47 offen sc1\n" \
"buffer_load_dwordx4 v[60:63], v210, s[4:7], s47 offen offset:256 sc1\n" \
"v_smfmac_f32_16x16x64_bf16 v[28:31], v[16:19], a[32:39], v200\n" \
"v_smfmac_f32_16x16x64_bf16 v[32:35], v[16:19], a[88:95], v200\n" \
"v_smfmac_f32_16x16x64_bf16 v[36:39], v[16:19], a[144:151], v200\n" \
"v_smfmac_f32_16x16x64_bf16 v[40:43], v[16:19], a[200:207], v200\n" \
"v_smfmac_f32_16x16x64_bf16 v[28:31], v[20:23], a[40:47], v200\n" \
"v_smfmac_f32_16x16x64_bf16 v[32:35], v[20:23], a[96:103], v200\n" \
"buffer_load_dwordx4 v[50:53], v212, s[4:7], s47 offen sc1\n" \
"v_smfmac_f32_16x16x64_bf16 v[36:39], v[20:23], a[152:159], v200\n" \
"v_smfmac_f32_16x16x64_bf16 v[40:43], v[20:23], a[208:215], v200\n" \
"v_smfmac_f32_16x16x64_bf16 v[28:31], v[24:27], a[48:55], v200\n" \
"v_smfmac_f32_16x16x64_bf16 v[32:35], v[24:27], a[104:111], v200\n" \
"v_smfmac_f32_16x16x64_bf16 v[36:39], v[24:27], a[160:167], v200\n" \
"v_smfmac_f32_16x16x64_bf16 v[40:43], v[24:27], a[216:223], v200\n" \
"s_nop 2\n" \
"v_mov_b32 v245, v245\n" \
"v_mov_b32 v245, v245\n" \
"v_mov_b32 v245, v245\n" \
"v_permlane32_swap_b32 v28, v32\n" \
"v_permlane32_swap_b32 v29, v33\n" \
"v_permlane32_swap_b32 v30, v34\n" \
"v_permlane32_swap_b32 v31, v35\n" \
"v_add_f32 v28, v28, v32\n" \
"v_add_f32 v29, v29, v33\n" \
"v_add_f32 v30, v30, v34\n" \
"v_add_f32 v31, v31, v35\n" \
"v_cvt_pk_bf16_f32 v68, v28, v29\n" \
"v_cvt_pk_bf16_f32 v69, v30, v31\n" \
"v_permlane32_swap_b32 v36, v40\n" \
"v_permlane32_swap_b32 v37, v41\n" \
"v_permlane32_swap_b32 v38, v42\n" \
"v_permlane32_swap_b32 v39, v43\n" \
"v_add_f32 v36, v36, v40\n" \
"v_add_f32 v37, v37, v41\n" \
"v_add_f32 v38, v38, v42\n" \
"v_add_f32 v39, v39, v43\n" \
"v_cvt_pk_bf16_f32 v70, v36, v37\n" \
"v_cvt_pk_bf16_f32 v71, v38, v39\n" \
"v_mov_b32 v48, v68\n" \
"v_mov_b32 v49, v69\n" \
"v_mov_b32 v54, v70\n" \
"v_mov_b32 v55, v71\n" \
"s_nop 1\n" \
"v_permlane16_swap_b32 v48, v54\n" \
"v_permlane16_swap_b32 v49, v55\n" \
".LP2_wait:\n" \
"s_waitcnt vmcnt(1)\n" \
"v_pk_add_f32 v[72:73], v[56:57], v[60:61]\n" \
"v_pk_add_f32 v[74:75], v[58:59], v[62:63]\n" \
"v_max3_f32 v77, v72, v73, v64\n" \
"v_max3_f32 v77, v77, v74, v75\n" \
"s_nop 1\n" \
"v_max_f32_dpp v76, v77, v77 row_ror:8 row_mask:0xf bank_mask:0xf\n" \
"v_pk_add_f32 v[72:73], v[72:73], v[76:77] op_sel_hi:[1,0] neg_lo:[0,1] neg_hi:[0,1]\n" \
"v_pk_add_f32 v[74:75], v[74:75], v[76:77] op_sel_hi:[1,0] neg_lo:[0,1] neg_hi:[0,1]\n" \
"v_sub_f32 v78, v64, v76\n" \
"v_exp_f32 v72, v72\n" \
"v_exp_f32 v73, v73\n" \
"v_exp_f32 v74, v74\n" \
"v_exp_f32 v75, v75\n" \
"v_exp_f32 v78, v78\n" \
"v_pk_add_f32 v[80:81], v[72:73], v[74:75]\n" \
"v_fma_f32 v80, v78, v65, v80\n" \
"v_add_f32 v81, v80, v81\n" \
"s_nop 1\n" \
"v_add_f32_dpp v80, v81, v81 row_ror:8 row_mask:0xf bank_mask:0xf\n" \
"v_rcp_f32 v80, v80\n" \
"s_nop 0\n" \
"v_mul_f32 v78, v78, v80\n" \
"v_pk_mul_f32 v[72:73], v[72:73], v[80:81] op_sel_hi:[1,0]\n" \
"v_pk_mul_f32 v[74:75], v[74:75], v[80:81] op_sel_hi:[1,0]\n" \
"v_cvt_pk_bf16_f32 v78, v78, v78\n" \
"v_cvt_pk_bf16_f32 v82, v72, v73\n" \
"v_cvt_pk_bf16_f32 v83, v74, v75\n" \
"v_and_b32 v84, v78, v214\n" \
"v_and_b32 v85, v78, v215\n" \
"v_cvt_pk_bf16_f32 v86, v72, v73\n" \
"v_cvt_pk_bf16_f32 v87, v74, v75\n" \
"s_waitcnt vmcnt(0)\n" \
"v_mfma_f32_16x16x32_bf16 v[88:91], v[48:51], v[82:85], 0\n" \
"v_mfma_f32_16x16x32_bf16 v[92:95], v[52:55], v[84:87], 0\n" \
"s_nop 2\n" \
"v_mov_b32 v245, v245\n" \
"s_nop 2\n" \
"v_mov_b32 v245, v245\n" \
"v_add_f32_dpp v96, v92, v88 row_ror:8 row_mask:0xf bank_mask:0x3\n" \
"v_add_f32_dpp v97, v93, v89 row_ror:8 row_mask:0xf bank_mask:0x3\n" \
"v_add_f32_dpp v96, v90, v94 row_ror:8 row_mask:0xf bank_mask:0xc\n" \
"v_add_f32_dpp v97, v91, v95 row_ror:8 row_mask:0xf bank_mask:0xc\n" \
"ds_write_b64 v216, v[96:97]\n" \
"v_cmp_u_f32 vcc, v96, v97\n" \
"s_bitcmp1_b32 s31, 21\n" \
"s_cselect_b32 s18, s29, 0\n" \
"s_xor_b32 s18, s18, s48\n" \
"v_mov_b32 v245, v245\n" \
"s_xor_b32 s19, s18, s29\n" \
"s_nop 0\n" \
"s_nop 0\n" \
"v_mov_b32 v245, v245\n" \
"s_nop 0\n" \
"s_nop 0\n" \
"s_nop 0\n" \
"v_mov_b32 v245, v245\n" \
"s_cbranch_vccnz .LP2_cls\n" \
".LP2_clsr:\n" \
"s_waitcnt lgkmcnt(0)\n" \
"s_barrier\n" \
"ds_read_b64 v[100:101], v217 offset:0\n" \
"ds_read_b64 v[102:103], v217 offset:512\n" \
"ds_read_b32 v116, v252 offset:16\n" \
"ds_read_b64 v[104:105], v217 offset:1024\n" \
"ds_read_b64 v[106:107], v217 offset:1536\n" \
"v_mov_b32 v245, v245\n" \
"v_mov_b32 v245, v245\n" \
"s_add_u32 s40, s40, 1\n" \
"s_add_u32 s24, s24, s27\n" \
"s_mov_b32 s47, s48\n" \
"v_mov_b32 v245, v245\n" \
"s_waitcnt lgkmcnt(2)\n" \
"v_xad_u32 v117, v116, s18, v213\n" \
"v_pk_add_f32 v[100:101], v[100:101], v[102:103]\n" \
"s_waitcnt lgkmcnt(0)\n" \
"v_pk_add_f32 v[104:105], v[104:105], v[106:107]\n" \
"v_pk_add_f32 v[100:101], v[100:101], v[104:105]\n" \
"v_cvt_pk_bf16_f32 v96, v100, v101\n" \
"s_add_u32 s43, s40, 1\n" \
"s_and_b32 s43, s43, 3\n" \
"s_bitcmp1_b32 s43, 0\n" \
"s_cselect_b32 s48, s32, s31\n" \
"s_lshl_b32 s43, s43, 19\n" \
"s_add_u32 s48, s48, s43\n" \
"s_add_u32 s43, s40, 2\n" \
"s_and_b32 s43, s43, 3\n" \
"buffer_store_dword v96, v117, s[4:7], 0 offen sc1\n" \
"s_bitcmp1_b32 s43, 0\n" \
"s_cselect_b32 s49, s32, s31\n" \
"s_lshl_b32 s43, s43, 19\n" \
"s_add_u32 s49, s49, s43\n" \
"s_sub_u32 s43, s24, s44\n" \
"s_add_u32 s43, s43, s28\n" \
"s_max_i32 s43, s43, 0xfffd0000\n" \
"s_min_i32 s43, s43, 0x4000\n" \
"s_mul_i32 s43, s43, s46\n" \
"s_ashr_i32 s43, s43, 12\n" \
"s_add_u32 s41, s45, s43\n" \
"s_lshr_b32 s35, s42, 1\n" \
"s_sub_u32 s35, 0, s35\n" \
"s_nop 6\n" \
"v_xad_u32 v116, v116, s19, v213\n" \
"buffer_store_dword v96, v116, s[4:7], 0 offen sc1\n" \
"s_branch .LP2_loop\n" \
"s_branch .LP2_y1\n" \
".LP2_retry2:\n" \
"s_add_u32 s51, s51, 1\n" \
"s_cmp_gt_u32 s51, 0x20000\n" \
"s_cbranch_scc1 .LP2_end\n" \
".LP2_y0:\n" \
"s_add_u32 s41, s41, s42\n" \
"s_add_u32 s24, s24, s25\n" \
"s_xor_b32 s31, s31, s29\n" \
"s_xor_b32 s32, s32, s29\n" \
"s_xor_b32 s47, s47, s29\n" \
"s_xor_b32 s48, s48, s29\n" \
"s_add_u32 s39, s39, 1\n" \
"buffer_store_dwordx4 v[112:115], v223, s[4:7], s48 offen sc1\n" \
"s_bitcmp1_b32 s31, 21\n" \
"s_cselect_b32 s43, s29, 0\n" \
"v_mov_b32 v116, s43\n" \
"ds_write_b32 v252, v116 offset:16\n" \
".LP2_y1:\n" \
"s_memtime vcc\n" \
"s_waitcnt lgkmcnt(0)\n" \
"s_sub_u32 s43, s41, vcc_lo\n" \
"s_cmp_gt_i32 s43, 200\n" \
"s_cbranch_scc1 .LP2_y1\n" \
"s_cmp_lt_i32 s43, s37\n" \
"s_cbranch_scc1 .LP2_y0\n" \
"buffer_load_dwordx2 v[64:65], v211, s[4:7], s47 offen sc1\n" \
".p2align 6\n" \
".LP2_c1:\n" \
"s_memtime vcc\n" \
"s_waitcnt lgkmcnt(0)\n" \
"s_sub_u32 s43, s41, vcc_lo\n" \
"s_cmp_gt_i32 s43, 128\n" \
"s_cbranch_scc1 .LP2_c1\n" \
"s_cmp_lt_i32 s43, s37\n" \
"s_cbranch_scc1 .LP2_y0\n" \
"s_max_i32 s43, s43, 0\n" \
"s_and_b32 s43, s43, -4\n" \
"s_getpc_b64 vcc\n" \
".LP2_c3:\n" \
"s_sub_u32 s43, .LP2_c4-.LP2_c3, s43\n" \
"s_add_u32 vcc_lo, vcc_lo, s43\n" \
"s_addc_u32 vcc_hi, vcc_hi, 0\n" \
"s_setpc_b64 vcc\n" \
".rept 32\n" \
"s_nop 0\n" \
".endr\n" \
".LP2_c4:\n" \
"buffer_load_dwordx4 v[56:59], v210, s[4:7], s47 offen sc1\n" \
"buffer_load_dwordx4 v[60:63], v210, s[4:7], s47 offen offset:256 sc1\n" \
"s_nop 2\n" \
"v_mov_b32 v245, v245\n" \
"s_nop 2\n" \
"v_mov_b32 v245, v245\n" \
"s_nop 2\n" \
"v_mov_b32 v245, v245\n" \
"s_nop 2\n" \
"v_mov_b32 v245, v245\n" \
"s_nop 2\n" \
"v_mov_b32 v245, v245\n" \
"s_nop 2\n" \
"v_mov_b32 v245, v245\n" \
"buffer_load_dwordx4 v[50:53], v212, s[4:7], s47 offen sc1\n" \
"s_nop 2\n" \
"v_mov_b32 v245, v245\n" \
"s_nop 2\n" \
"v_mov_b32 v245, v245\n" \
"s_nop 2\n" \
"v_mov_b32 v245, v245\n" \
"s_nop 2\n" \
"v_mov_b32 v245, v245\n" \
"s_nop 2\n" \
"v_mov_b32 v245, v245\n" \
"s_nop 2\n" \
"v_mov_b32 v245, v245\n" \
"s_nop 2\n" \
"v_mov_b32 v245, v245\n" \
"s_nop 2\n" \
"v_mov_b32 v245, v245\n" \
"s_nop 2\n" \
"v_mov_b32 v245, v245\n" \
"s_nop 2\n" \
"v_mov_b32 v245, v245\n" \
"s_nop 2\n" \
"v_mov_b32 v245, v245\n" \
"s_nop 2\n" \
"v_mov_b32 v245, v245\n" \
"s_nop 2\n" \
"v_mov_b32 v245, v245\n" \
"s_nop 2\n" \
"v_mov_b32 v245, v245\n" \
"s_nop 2\n" \
"v_mov_b32 v245, v245\n" \
"s_nop 2\n" \
"v_mov_b32 v245, v245\n" \
"s_nop 2\n" \
"v_mov_b32 v245, v245\n" \
"s_branch .LP2_wait\n" \
".LP2_cls:\n" \
"s_waitcnt vmcnt(0)\n" \
"v_max3_u32 v44, v64, v64, v65\n" \
"v_max3_u32 v44, v44, v50, v51\n" \
"v_max3_u32 v44, v44, v52, v53\n" \
"v_cmp_eq_u32 vcc, -1, v44\n" \
"s_cbranch_vccnz .LP2_retry2\n" \
"v_cmp_u_f32 vcc, v56, v56\n" \
"v_cmp_ne_u32 s[2:3], v56, v109\n" \
"s_and_b64 vcc, vcc, s[2:3]\n" \
"s_cbranch_vccnz .LP2_retry2\n" \
"v_cmp_u_f32 vcc, v57, v57\n" \
"v_cmp_ne_u32 s[2:3], v57, v109\n" \
"s_and_b64 vcc, vcc, s[2:3]\n" \
"s_cbranch_vccnz .LP2_retry2\n" \
"v_cmp_u_f32 vcc, v58, v58\n" \
"v_cmp_ne_u32 s[2:3], v58, v109\n" \
"s_and_b64 vcc, vcc, s[2:3]\n" \
"s_cbranch_vccnz .LP2_retry2\n" \
"v_cmp_u_f32 vcc, v59, v59\n" \
"v_cmp_ne_u32 s[2:3], v59, v109\n" \
"s_and_b64 vcc, vcc, s[2:3]\n" \
"s_cbranch_vccnz .LP2_retry2\n" \
"v_cmp_u_f32 vcc, v60, v60\n" \
"v_cmp_ne_u32 s[2:3], v60, v109\n" \
"s_and_b64 vcc, vcc, s[2:3]\n" \
"s_cbranch_vccnz .LP2_retry2\n" \
"v_cmp_u_f32 vcc, v61, v61\n" \
"v_cmp_ne_u32 s[2:3], v61, v109\n" \
"s_and_b64 vcc, vcc, s[2:3]\n" \
"s_cbranch_vccnz .LP2_retry2\n" \
"v_cmp_u_f32 vcc, v62, v62\n" \
"v_cmp_ne_u32 s[2:3], v62, v109\n" \
"s_and_b64 vcc, vcc, s[2:3]\n" \
"s_cbranch_vccnz .LP2_retry2\n" \
"v_cmp_u_f32 vcc, v63, v63\n" \
"v_cmp_ne_u32 s[2:3], v63, v109\n" \
"s_and_b64 vcc, vcc, s[2:3]\n" \
"s_cbranch_vccnz .LP2_retry2\n" \
"s_branch .LP2_clsr\n" \
".LP2_last:\n" \
"v_smfmac_f32_16x16x64_bf16 v[28:31], v[16:19], a[32:39], v200\n" \
"v_smfmac_f32_16x16x64_bf16 v[32:35], v[16:19], a[88:95], v200\n" \
"v_smfmac_f32_16x16x64_bf16 v[36:39], v[16:19], a[144:151], v200\n" \
"v_smfmac_f32_16x16x64_bf16 v[40:43], v[16:19], a[200:207], v200\n" \
"v_smfmac_f32_16x16x64_bf16 v[28:31], v[20:23], a[40:47], v200\n" \
"v_smfmac_f32_16x16x64_bf16 v[32:35], v[20:23], a[96:103], v200\n" \
"v_smfmac_f32_16x16x64_bf16 v[36:39], v[20:23], a[152:159], v200\n" \
"v_smfmac_f32_16x16x64_bf16 v[40:43], v[20:23], a[208:215], v200\n" \
"v_smfmac_f32_16x16x64_bf16 v[28:31], v[24:27], a[48:55], v200\n" \
"v_smfmac_f32_16x16x64_bf16 v[32:35], v[24:27], a[104:111], v200\n" \
"v_smfmac_f32_16x16x64_bf16 v[36:39], v[24:27], a[160:167], v200\n" \
"v_smfmac_f32_16x16x64_bf16 v[40:43], v[24:27], a[216:223], v200\n" \
"s_waitcnt vmcnt(0)\n" \
"s_nop 7\n" \
"v_permlane32_swap_b32 v28, v32\n" \
"v_permlane32_swap_b32 v29, v33\n" \
"v_permlane32_swap_b32 v30, v34\n" \
"v_permlane32_swap_b32 v31, v35\n" \
"v_add_f32 v28, v28, v32\n" \
"v_add_f32 v29, v29, v33\n" \
"v_add_f32 v30, v30, v34\n" \
"v_add_f32 v31, v31, v35\n" \
"v_cvt_pk_bf16_f32 v68, v28, v29\n" \
"v_cvt_pk_bf16_f32 v69, v30, v31\n" \
"v_permlane32_swap_b32 v36, v40\n" \
"v_permlane32_swap_b32 v37, v41\n" \
"v_permlane32_swap_b32 v38, v42\n" \
"v_permlane32_swap_b32 v39, v43\n" \
"v_add_f32 v36, v36, v40\n" \
"v_add_f32 v37, v37, v41\n" \
"v_add_f32 v38, v38, v42\n" \
"v_add_f32 v39, v39, v43\n" \
"v_cvt_pk_bf16_f32 v70, v36, v37\n" \
"v_cvt_pk_bf16_f32 v71, v38, v39\n" \
"s_mov_b32 s50, 7340032\n" \
"buffer_store_dwordx4 v[68:71], v218, s[12:15], s50 offen sc1\n" \
"s_waitcnt vmcnt(0)\n" \
"s_barrier\n" \
".LP2_end:\n" \
"s_endpgm\n" \
".p2align 6\n" \
"fk_role_P3:\n" \
"ds_read_b32 v200, v255 offset:0\n" \
"ds_read_b32 v201, v255 offset:256\n" \
"ds_read_b32 v202, v255 offset:512\n" \
"ds_read_b32 v203, v255 offset:768\n" \
"ds_read_b32 v204, v255 offset:1024\n" \
"ds_read_b32 v205, v255 offset:1280\n" \
"ds_read_b32 v206, v255 offset:1536\n" \
"ds_read_b32 v207, v255 offset:1792\n" \
"ds_read_b32 v208, v255 offset:2048\n" \
"ds_read_b32 v209, v255 offset:2304\n" \
"ds_read_b32 v210, v255 offset:2560\n" \
"ds_read_b32 v211, v255 offset:2816\n" \
"ds_read_b32 v212, v255 offset:3072\n" \
"ds_read_b32 v213, v255 offset:3328\n" \
"ds_read_b32 v214, v255 offset:3584\n" \
"ds_read_b32 v215, v255 offset:3840\n" \
"ds_read_b32 v216, v255 offset:4096\n" \
"ds_read_b32 v217, v255 offset:4352\n" \
"ds_read_b32 v218, v255 offset:4608\n" \
"ds_read_b32 v219, v255 offset:4864\n" \
"ds_read_b32 v220, v255 offset:5120\n" \
"ds_read_b32 v221, v255 offset:5376\n" \
"ds_read_b32 v222, v255 offset:5632\n" \
"ds_read_b32 v223, v255 offset:5888\n" \
"ds_read_b32 v254, v255 offset:7936\n" \
"s_waitcnt lgkmcnt(0)\n" \
"v_readlane_b32 s4, v254, 0\n" \
"v_readlane_b32 s5, v254, 1\n" \
"v_readlane_b32 s6, v254, 2\n" \
"v_readlane_b32 s7, v254, 3\n" \
"v_readlane_b32 s8, v254, 4\n" \
"v_readlane_b32 s9, v254, 5\n" \
"v_readlane_b32 s10, v254, 6\n" \
"v_readlane_b32 s11, v254, 7\n" \
"v_readlane_b32 s12, v254, 8\n" \
"v_readlane_b32 s13, v254, 9\n" \
"v_readlane_b32 s14, v254, 10\n" \
"v_readlane_b32 s15, v254, 11\n" \
"v_readlane_b32 s16, v254, 12\n" \
"v_readlane_b32 s17, v254, 13\n" \
"v_readlane_b32 s18, v254, 14\n" \
"v_readlane_b32 s19, v254, 15\n" \
"v_readlane_b32 s20, v254, 16\n" \
"v_readlane_b32 s21, v254, 17\n" \
"v_readlane_b32 s22, v254, 18\n" \
"v_readlane_b32 s23, v254, 19\n" \
"v_readlane_b32 s24, v254, 20\n" \
"v_readlane_b32 s25, v254, 21\n" \
"v_readlane_b32 s26, v254, 22\n" \
"v_readlane_b32 s27, v254, 23\n" \
"v_readlane_b32 s28, v254, 24\n" \
"v_readlane_b32 s29, v254, 25\n" \
"v_readlane_b32 s30, v254, 26\n" \
"v_readlane_b32 s31, v254, 27\n" \
"v_readlane_b32 s32, v254, 28\n" \
"v_readlane_b32 s33, v254, 29\n" \
"v_readlane_b32 s34, v254, 30\n" \
"v_readlane_b32 s35, v254, 31\n" \
"v_readlane_b32 s36, v254, 32\n" \
"v_readlane_b32 s37, v254, 33\n" \
"v_readlane_b32 s38, v254, 34\n" \
"v_readlane_b32 s39, v254, 35\n" \
"s_mov_b32 s40, 0\n" \
"s_mov_b32 s51, 0\n" \
"s_nop 0\n" \
"v_mov_b32 v252, s38\n" \
"s_nop 4\n" \
"s_add_u32 s43, s30, 1\n" \
"s_mov_b32 s50, 0\n" \
".LP3_w52l:\n" \
"buffer_load_dword v240, v204, s[4:7], 0 offen sc1\n" \
"s_waitcnt vmcnt(0)\n" \
".LP3_w52c:\n" \
"v_subrev_u32 v241, s43, v240\n" \
"v_cmp_gt_u32 vcc, 0x100, v241\n" \
"s_cmp_eq_u64 vcc, -1\n" \
"s_cbranch_scc1 .LP3_w52d\n" \
"s_sleep 2\n" \
"s_add_u32 s50, s50, 1\n" \
"s_cmp_lt_u32 s50, 0x20000\n" \
"s_cbranch_scc1 .LP3_w52l\n" \
"s_branch .LP3_end\n" \
".LP3_w52d:\n" \
"v_readlane_b32 s43, v202, 1\n" \
"s_nop 4\n" \
"buffer_load_dwordx4 a[8:11], v201, s[8:11], s43 offen sc1\n" \
"buffer_load_dwordx4 a[12:15], v201, s[8:11], s43 offen offset:1024 sc1\n" \
"v_readlane_b32 s43, v202, 8\n" \
"s_nop 4\n" \
"buffer_load_dwordx4 a[64:67], v201, s[8:11], s43 offen sc1\n" \
"buffer_load_dwordx4 a[68:71], v201, s[8:11], s43 offen offset:1024 sc1\n" \
"v_readlane_b32 s43, v202, 15\n" \
"s_nop 4\n" \
"buffer_load_dwordx4 a[120:123], v201, s[8:11], s43 offen sc1\n" \
"buffer_load_dwordx4 a[124:127], v201, s[8:11], s43 offen offset:1024 sc1\n" \
"v_readlane_b32 s43, v202, 22\n" \
"s_nop 4\n" \
"buffer_load_dwordx4 a[176:179], v201, s[8:11], s43 offen sc1\n" \
"buffer_load_dwordx4 a[180:183], v201, s[8:11], s43 offen offset:1024 sc1\n" \
"s_add_u32 s43, s30, 2\n" \
"s_mov_b32 s50, 0\n" \
".LP3_w53l:\n" \
"buffer_load_dword v240, v204, s[4:7], 0 offen sc1\n" \
"s_waitcnt vmcnt(0)\n" \
".LP3_w53c:\n" \
"v_subrev_u32 v241, s43, v240\n" \
"v_cmp_gt_u32 vcc, 0x100, v241\n" \
"s_cmp_eq_u64 vcc, -1\n" \
"s_cbranch_scc1 .LP3_w53d\n" \
"s_sleep 2\n" \
"s_add_u32 s50, s50, 1\n" \
"s_cmp_lt_u32 s50, 0x20000\n" \
"s_cbranch_scc1 .LP3_w53l\n" \
"s_branch .LP3_end\n" \
".LP3_w53d:\n" \
"v_readlane_b32 s43, v202, 2\n" \
"s_nop 4\n" \
"buffer_load_dwordx4 a[16:19], v201, s[8:11], s43 offen sc1\n" \
"buffer_load_dwordx4 a[20:23], v201, s[8:11], s43 offen offset:1024 sc1\n" \
"v_readlane_b32 s43, v202, 9\n" \
"s_nop 4\n" \
"buffer_load_dwordx4 a[72:75], v201, s[8:11], s43 offen sc1\n" \
"buffer_load_dwordx4 a[76:79], v201, s[8:11], s43 offen offset:1024 sc1\n" \
"v_readlane_b32 s43, v202, 16\n" \
"s_nop 4\n" \
"buffer_load_dwordx4 a[128:131], v201, s[8:11], s43 offen sc1\n" \
"buffer_load_dwordx4 a[132:135], v201, s[8:11], s43 offen offset:1024 sc1\n" \
"v_readlane_b32 s43, v202, 23\n" \
"s_nop 4\n" \
"buffer_load_dwordx4 a[184:187], v201, s[8:11], s43 offen sc1\n" \
"buffer_load_dwordx4 a[188:191], v201, s[8:11], s43 offen offset:1024 sc1\n" \
"s_add_u32 s43, s30, 3\n" \
"s_mov_b32 s50, 0\n" \
".LP3_w54l:\n" \
"buffer_load_dword v240, v204, s[4:7], 0 offen sc1\n" \
"s_waitcnt vmcnt(0)\n" \
".LP3_w54c:\n" \
"v_subrev_u32 v241, s43, v240\n" \
"v_cmp_gt_u32 vcc, 0x100, v241\n" \
"s_cmp_eq_u64 vcc, -1\n" \
"s_cbranch_scc1 .LP3_w54d\n" \
"s_sleep 2\n" \
"s_add_u32 s50, s50, 1\n" \
"s_cmp_lt_u32 s50, 0x20000\n" \
"s_cbranch_scc1 .LP3_w54l\n" \
"s_branch .LP3_end\n" \
".LP3_w54d:\n" \
"v_readlane_b32 s43, v202, 3\n" \
"s_nop 4\n" \
"buffer_load_dwordx4 a[24:27], v201, s[8:11], s43 offen sc1\n" \
"buffer_load_dwordx4 a[28:31], v201, s[8:11], s43 offen offset:1024 sc1\n" \
"v_readlane_b32 s43, v202, 10\n" \
"s_nop 4\n" \
"buffer_load_dwordx4 a[80:83], v201, s[8:11], s43 offen sc1\n" \
"buffer_load_dwordx4 a[84:87], v201, s[8:11], s43 offen offset:1024 sc1\n" \
"v_readlane_b32 s43, v202, 17\n" \
"s_nop 4\n" \
"buffer_load_dwordx4 a[136:139], v201, s[8:11], s43 offen sc1\n" \
"buffer_load_dwordx4 a[140:143], v201, s[8:11], s43 offen offset:1024 sc1\n" \
"v_readlane_b32 s43, v202, 24\n" \
"s_nop 4\n" \
"buffer_load_dwordx4 a[192:195], v201, s[8:11], s43 offen sc1\n" \
"buffer_load_dwordx4 a[196:199], v201, s[8:11], s43 offen offset:1024 sc1\n" \
"s_add_u32 s43, s30, 4\n" \
"s_mov_b32 s50, 0\n" \
".LP3_w55l:\n" \
"buffer_load_dword v240, v204, s[4:7], 0 offen sc1\n" \
"s_waitcnt vmcnt(0)\n" \
".LP3_w55c:\n" \
"v_subrev_u32 v241, s43, v240\n" \
"v_cmp_gt_u32 vcc, 0x100, v241\n" \
"s_cmp_eq_u64 vcc, -1\n" \
"s_cbranch_scc1 .LP3_w55d\n" \
"s_sleep 2\n" \
"s_add_u32 s50, s50, 1\n" \
"s_cmp_lt_u32 s50, 0x20000\n" \
"s_cbranch_scc1 .LP3_w55l\n" \
"s_branch .LP3_end\n" \
".LP3_w55d:\n" \
"v_readlane_b32 s43, v202, 0\n" \
"s_nop 4\n" \
"buffer_load_dwordx4 a[0:3], v201, s[8:11], s43 offen sc1\n" \
"buffer_load_dwordx4 a[4:7], v201, s[8:11], s43 offen offset:1024 sc1\n" \
"v_readlane_b32 s43, v202, 7\n" \
"s_nop 4\n" \
"buffer_load_dwordx4 a[56:59], v201, s[8:11], s43 offen sc1\n" \
"buffer_load_dwordx4 a[60:63], v201, s[8:11], s43 offen offset:1024 sc1\n" \
"v_readlane_b32 s43, v202, 14\n" \
"s_nop 4\n" \
"buffer_load_dwordx4 a[112:115], v201, s[8:11], s43 offen sc1\n" \
"buffer_load_dwordx4 a[116:119], v201, s[8:11], s43 offen offset:1024 sc1\n" \
"v_readlane_b32 s43, v202, 21\n" \
"s_nop 4\n" \
"buffer_load_dwordx4 a[168:171], v201, s[8:11], s43 offen sc1\n" \
"buffer_load_dwordx4 a[172:175], v201, s[8:11], s43 offen offset:1024 sc1\n" \
"s_add_u32 s43, s30, 5\n" \
"s_mov_b32 s50, 0\n" \
".LP3_w56l:\n" \
"buffer_load_dword v240, v204, s[4:7], 0 offen sc1\n" \
"s_waitcnt vmcnt(0)\n" \
".LP3_w56c:\n" \
"v_subrev_u32 v241, s43, v240\n" \
"v_cmp_gt_u32 vcc, 0x100, v241\n" \
"s_cmp_eq_u64 vcc, -1\n" \
"s_cbranch_scc1 .LP3_w56d\n" \
"s_sleep 2\n" \
"s_add_u32 s50, s50, 1\n" \
"s_cmp_lt_u32 s50, 0x20000\n" \
"s_cbranch_scc1 .LP3_w56l\n" \
"s_branch .LP3_end\n" \
".LP3_w56d:\n" \
"v_readlane_b32 s43, v202, 4\n" \
"s_nop 4\n" \
"buffer_load_dwordx4 a[32:35], v201, s[8:11], s43 offen sc1\n" \
"buffer_load_dwordx4 a[36:39], v201, s[8:11], s43 offen offset:1024 sc1\n" \
"v_readlane_b32 s43, v202, 11\n" \
"s_nop 4\n" \
"buffer_load_dwordx4 a[88:91], v201, s[8:11], s43 offen sc1\n" \
"buffer_load_dwordx4 a[92:95], v201, s[8:11], s43 offen offset:1024 sc1\n" \
"v_readlane_b32 s43, v202, 18\n" \
"s_nop 4\n" \
"buffer_load_dwordx4 a[144:147], v201, s[8:11], s43 offen sc1\n" \
"buffer_load_dwordx4 a[148:151], v201, s[8:11], s43 offen offset:1024 sc1\n" \
"v_readlane_b32 s43, v202, 25\n" \
"s_nop 4\n" \
"buffer_load_dwordx4 a[200:203], v201, s[8:11], s43 offen sc1\n" \
"buffer_load_dwordx4 a[204:207], v201, s[8:11], s43 offen offset:1024 sc1\n" \
"s_add_u32 s43, s30, 6\n" \
"s_mov_b32 s50, 0\n" \
".LP3_w57l:\n" \
"buffer_load_dword v240, v204, s[4:7], 0 offen sc1\n" \
"s_waitcnt vmcnt(0)\n" \
".LP3_w57c:\n" \
"v_subrev_u32 v241, s43, v240\n" \
"v_cmp_gt_u32 vcc, 0x100, v241\n" \
"s_cmp_eq_u64 vcc, -1\n" \
"s_cbranch_scc1 .LP3_w57d\n" \
"s_sleep 2\n" \
"s_add_u32 s50, s50, 1\n" \
"s_cmp_lt_u32 s50, 0x20000\n" \
"s_cbranch_scc1 .LP3_w57l\n" \
"s_branch .LP3_end\n" \
".LP3_w57d:\n" \
"v_readlane_b32 s43, v202, 5\n" \
"s_nop 4\n" \
"buffer_load_dwordx4 a[40:43], v201, s[8:11], s43 offen sc1\n" \
"buffer_load_dwordx4 a[44:47], v201, s[8:11], s43 offen offset:1024 sc1\n" \
"v_readlane_b32 s43, v202, 12\n" \
"s_nop 4\n" \
"buffer_load_dwordx4 a[96:99], v201, s[8:11], s43 offen sc1\n" \
"buffer_load_dwordx4 a[100:103], v201, s[8:11], s43 offen offset:1024 sc1\n" \
"v_readlane_b32 s43, v202, 19\n" \
"s_nop 4\n" \
"buffer_load_dwordx4 a[152:155], v201, s[8:11], s43 offen sc1\n" \
"buffer_load_dwordx4 a[156:159], v201, s[8:11], s43 offen offset:1024 sc1\n" \
"v_readlane_b32 s43, v202, 26\n" \
"s_nop 4\n" \
"buffer_load_dwordx4 a[208:211], v201, s[8:11], s43 offen sc1\n" \
"buffer_load_dwordx4 a[212:215], v201, s[8:11], s43 offen offset:1024 sc1\n" \
"s_add_u32 s43, s30, 7\n" \
"s_mov_b32 s50, 0\n" \
".LP3_w58l:\n" \
"buffer_load_dword v240, v204, s[4:7], 0 offen sc1\n" \
"s_waitcnt vmcnt(0)\n" \
".LP3_w58c:\n" \
"v_subrev_u32 v241, s43, v240\n" \
"v_cmp_gt_u32 vcc, 0x100, v241\n" \
"s_cmp_eq_u64 vcc, -1\n" \
"s_cbranch_scc1 .LP3_w58d\n" \
"s_sleep 2\n" \
"s_add_u32 s50, s50, 1\n" \
"s_cmp_lt_u32 s50, 0x20000\n" \
"s_cbranch_scc1 .LP3_w58l\n" \
"s_branch .LP3_end\n" \
".LP3_w58d:\n" \
"v_readlane_b32 s43, v202, 6\n" \
"s_nop 4\n" \
"buffer_load_dwordx4 a[48:51], v201, s[8:11], s43 offen sc1\n" \
"buffer_load_dwordx4 a[52:55], v201, s[8:11], s43 offen offset:1024 sc1\n" \
"v_readlane_b32 s43, v202, 13\n" \
"s_nop 4\n" \
"buffer_load_dwordx4 a[104:107], v201, s[8:11], s43 offen sc1\n" \
"buffer_load_dwordx4 a[108:111], v201, s[8:11], s43 offen offset:1024 sc1\n" \
"v_readlane_b32 s43, v202, 20\n" \
"s_nop 4\n" \
"buffer_load_dwordx4 a[160:163], v201, s[8:11], s43 offen sc1\n" \
"buffer_load_dwordx4 a[164:167], v201, s[8:11], s43 offen offset:1024 sc1\n" \
"v_readlane_b32 s43, v202, 27\n" \
"s_nop 4\n" \
"buffer_load_dwordx4 a[216:219], v201, s[8:11], s43 offen sc1\n" \
"buffer_load_dwordx4 a[220:223], v201, s[8:11], s43 offen offset:1024 sc1\n" \
"v_mov_b32 v112, -1\n" \
"v_mov_b32 v113, -1\n" \
"v_mov_b32 v114, -1\n" \
"v_mov_b32 v115, -1\n" \
"v_mov_b32 v109, 0x7fc00001\n" \
"s_add_u32 s43, s30, 1\n" \
"s_mov_b32 s50, 0\n" \
".LP3_w59l:\n" \
"buffer_load_dword v240, v205, s[4:7], 0 offen sc1\n" \
"s_waitcnt vmcnt(0)\n" \
".LP3_w59c:\n" \
"v_subrev_u32 v241, s43, v240\n" \
"v_cmp_gt_u32 vcc, 0x100, v241\n" \
"s_cmp_eq_u64 vcc, -1\n" \
"s_cbranch_scc1 .LP3_w59d\n" \
"s_sleep 2\n" \
"s_add_u32 s50, s50, 1\n" \
"s_cmp_lt_u32 s50, 0x20000\n" \
"s_cbranch_scc1 .LP3_w59l\n" \
"s_branch .LP3_end\n" \
".LP3_w59d:\n" \
"s_add_u32 s43, s30, 1\n" \
"s_mov_b32 s50, 0\n" \
".LP3_w60l:\n" \
"buffer_load_dword v240, v206, s[4:7], 0 offen sc1\n" \
"s_waitcnt vmcnt(0)\n" \
".LP3_w60c:\n" \
"v_subrev_u32 v241, s43, v240\n" \
"v_cmp_gt_u32 vcc, 0x100, v241\n" \
"s_cmp_eq_u64 vcc, -1\n" \
"s_cbranch_scc1 .LP3_w60d\n" \
"s_sleep 2\n" \
"s_add_u32 s50, s50, 1\n" \
"s_cmp_lt_u32 s50, 0x20000\n" \
"s_cbranch_scc1 .LP3_w60l\n" \
"s_branch .LP3_end\n" \
".LP3_w60d:\n" \
"s_add_u32 s43, s40, 0\n" \
"s_and_b32 s43, s43, 3\n" \
"s_bitcmp1_b32 s43, 0\n" \
"s_cselect_b32 s47, s32, s31\n" \
"s_lshl_b32 s43, s43, 19\n" \
"s_add_u32 s47, s47, s43\n" \
"buffer_store_dword v219, v213, s[4:7], s47 offen sc1\n" \
"s_xor_b32 s43, s47, s29\n" \
"buffer_store_dword v219, v213, s[4:7], s43 offen sc1\n" \
"ds_read_b96 v[248:250], v252\n" \
"s_waitcnt lgkmcnt(0)\n" \
"v_readfirstlane_b32 s44, v248\n" \
"v_readfirstlane_b32 s45, v249\n" \
"v_readfirstlane_b32 s46, v250\n" \
"s_mul_i32 s42, s25, s46\n" \
"s_lshr_b32 s42, s42, 12\n" \
".p2align 3\n" \
"s_memtime s[54:55]\n" \
"s_memrealtime s[56:57]\n" \
"s_memtime s[58:59]\n" \
"s_memrealtime s[60:61]\n" \
"s_nop 0\n" \
"s_nop 0\n" \
"s_memtime s[62:63]\n" \
"s_memrealtime s[64:65]\n" \
"s_memtime s[66:67]\n" \
"s_memrealtime s[68:69]\n" \
"s_memtime s[70:71]\n" \
"s_memrealtime s[72:73]\n" \
"s_memtime s[74:75]\n" \
"s_memrealtime s[76:77]\n" \
"s_nop 0\n" \
"s_nop 0\n" \
"s_memtime s[78:79]\n" \
"s_memrealtime s[80:81]\n" \
"s_memtime s[82:83]\n" \
"s_memrealtime s[84:85]\n" \
"s_memtime s[86:87]\n" \
"s_memrealtime s[88:89]\n" \
"s_memtime s[90:91]\n" \
"s_memrealtime s[92:93]\n" \
"s_nop 0\n" \
"s_nop 0\n" \
"s_memtime s[94:95]\n" \
"s_memrealtime s[96:97]\n" \
"s_memtime s[98:99]\n" \
"s_mov_b32 s52, 0\n" \
"s_mov_b32 s53, 4096\n" \
"s_waitcnt lgkmcnt(0)\n" \
"s_cmp_lg_u32 s96, s56\n" \
"s_cselect_b32 s52, s90, s52\n" \
"s_cselect_b32 s53, s98, s53\n" \
"s_cmp_lg_u32 s92, s56\n" \
"s_cselect_b32 s52, s86, s52\n" \
"s_cselect_b32 s53, s94, s53\n" \
"s_cmp_lg_u32 s88, s56\n" \
"s_cselect_b32 s52, s82, s52\n" \
"s_cselect_b32 s53, s90, s53\n" \
"s_cmp_lg_u32 s84, s56\n" \
"s_cselect_b32 s52, s78, s52\n" \
"s_cselect_b32 s53, s86, s53\n" \
"s_cmp_lg_u32 s80, s56\n" \
"s_cselect_b32 s52, s74, s52\n" \
"s_cselect_b32 s53, s82, s53\n" \
"s_cmp_lg_u32 s76, s56\n" \
"s_cselect_b32 s52, s70, s52\n" \
"s_cselect_b32 s53, s78, s53\n" \
"s_cmp_lg_u32 s72, s56\n" \
"s_cselect_b32 s52, s66, s52\n" \
"s_cselect_b32 s53, s74, s53\n" \
"s_cmp_lg_u32 s68, s56\n" \
"s_cselect_b32 s52, s62, s52\n" \
"s_cselect_b32 s53, s70, s53\n" \
"s_cmp_lg_u32 s64, s56\n" \
"s_cselect_b32 s52, s58, s52\n" \
"s_cselect_b32 s53, s66, s53\n" \
"s_cmp_lg_u32 s60, s56\n" \
"s_cselect_b32 s52, s54, s52\n" \
"s_cselect_b32 s53, s62, s53\n" \
"s_sub_u32 s53, s53, s52\n" \
"s_lshr_b32 s55, s53, 1\n" \
"s_add_u32 s52, s52, s55\n" \
"s_add_u32 s55, s56, 4\n" \
"s_lshl_b32 s55, s55, 4\n" \
"s_cmp_le_u32 s53, 40\n" \
"s_cselect_b32 s44, s55, s44\n" \
"s_cselect_b32 s45, s52, s45\n" \
"v_mov_b32 v248, s44\n" \
"v_mov_b32 v249, s45\n" \
"v_mov_b32 v250, s46\n" \
"s_mov_b64 exec, 1\n" \
"ds_write_b96 v252, v[248:250]\n" \
"s_mov_b64 exec, -1\n" \
".p2align 3\n" \
"s_memtime s[54:55]\n" \
"s_memrealtime s[56:57]\n" \
"s_memtime s[58:59]\n" \
"s_memrealtime s[60:61]\n" \
"s_nop 0\n" \
"s_nop 0\n" \
"s_memtime s[62:63]\n" \
"s_memrealtime s[64:65]\n" \
"s_memtime s[66:67]\n" \
"s_memrealtime s[68:69]\n" \
"s_memtime s[70:71]\n" \
"s_memrealtime s[72:73]\n" \
"s_memtime s[74:75]\n" \
"s_memrealtime s[76:77]\n" \
"s_nop 0\n" \
"s_nop 0\n" \
"s_memtime s[78:79]\n" \
"s_memrealtime s[80:81]\n" \
"s_memtime s[82:83]\n" \
"s_memrealtime s[84:85]\n" \
"s_memtime s[86:87]\n" \
"s_memrealtime s[88:89]\n" \
"s_memtime s[90:91]\n" \
"s_memrealtime s[92:93]\n" \
"s_nop 0\n" \
"s_nop 0\n" \
"s_memtime s[94:95]\n" \
"s_memrealtime s[96:97]\n" \
"s_memtime s[98:99]\n" \
"s_mov_b32 s52, 0\n" \
"s_mov_b32 s53, 4096\n" \
"s_waitcnt lgkmcnt(0)\n" \
"s_cmp_lg_u32 s96, s56\n" \
"s_cselect_b32 s52, s90, s52\n" \
"s_cselect_b32 s53, s98, s53\n" \
"s_cmp_lg_u32 s92, s56\n" \
"s_cselect_b32 s52, s86, s52\n" \
"s_cselect_b32 s53, s94, s53\n" \
"s_cmp_lg_u32 s88, s56\n" \
"s_cselect_b32 s52, s82, s52\n" \
"s_cselect_b32 s53, s90, s53\n" \
"s_cmp_lg_u32 s84, s56\n" \
"s_cselect_b32 s52, s78, s52\n" \
"s_cselect_b32 s53, s86, s53\n" \
"s_cmp_lg_u32 s80, s56\n" \
"s_cselect_b32 s52, s74, s52\n" \
"s_cselect_b32 s53, s82, s53\n" \
"s_cmp_lg_u32 s76, s56\n" \
"s_cselect_b32 s52, s70, s52\n" \
"s_cselect_b32 s53, s78, s53\n" \
"s_cmp_lg_u32 s72, s56\n" \
"s_cselect_b32 s52, s66, s52\n" \
"s_cselect_b32 s53, s74, s53\n" \
"s_cmp_lg_u32 s68, s56\n" \
"s_cselect_b32 s52, s62, s52\n" \
"s_cselect_b32 s53, s70, s53\n" \
"s_cmp_lg_u32 s64, s56\n" \
"s_cselect_b32 s52, s58, s52\n" \
"s_cselect_b32 s53, s66, s53\n" \
"s_cmp_lg_u32 s60, s56\n" \
"s_cselect_b32 s52, s54, s52\n" \
"s_cselect_b32 s53, s62, s53\n" \
"s_sub_u32 s53, s53, s52\n" \
"s_lshr_b32 s55, s53, 1\n" \
"s_add_u32 s52, s52, s55\n" \
"s_add_u32 s55, s56, 4\n" \
"s_lshl_b32 s55, s55, 4\n" \
"s_cmp_le_u32 s53, 40\n" \
"s_cselect_b32 s44, s55, s44\n" \
"s_cselect_b32 s45, s52, s45\n" \
"v_mov_b32 v248, s44\n" \
"v_mov_b32 v249, s45\n" \
"v_mov_b32 v250, s46\n" \
"s_mov_b64 exec, 1\n" \
"ds_write_b96 v252, v[248:250]\n" \
"s_mov_b64 exec, -1\n" \
"s_waitcnt lgkmcnt(0)\n" \
"s_barrier\n" \
"ds_read_b96 v[248:250], v252\n" \
"s_waitcnt lgkmcnt(0)\n" \
"v_readfirstlane_b32 s44, v248\n" \
"v_readfirstlane_b32 s45, v249\n" \
"v_readfirstlane_b32 s46, v250\n" \
"s_mul_i32 s42, s25, s46\n" \
"s_lshr_b32 s42, s42, 12\n" \
"s_add_u32 s43, s40, 1\n" \
"s_and_b32 s43, s43, 3\n" \
"s_bitcmp1_b32 s43, 0\n" \
"s_cselect_b32 s48, s32, s31\n" \
"s_lshl_b32 s43, s43, 19\n" \
"s_add_u32 s48, s48, s43\n" \
"s_add_u32 s43, s40, 2\n" \
"s_and_b32 s43, s43, 3\n" \
"s_bitcmp1_b32 s43, 0\n" \
"s_cselect_b32 s49, s32, s31\n" \
"s_lshl_b32 s43, s43, 19\n" \
"s_add_u32 s49, s49, s43\n" \
"s_sub_u32 s43, s24, s44\n" \
"s_add_u32 s43, s43, s28\n" \
"s_max_i32 s43, s43, 0xfffd0000\n" \
"s_min_i32 s43, s43, 0x4000\n" \
"s_mul_i32 s43, s43, s46\n" \
"s_ashr_i32 s43, s43, 12\n" \
"s_add_u32 s41, s45, s43\n" \
"s_lshr_b32 s35, s42, 1\n" \
"s_sub_u32 s35, 0, s35\n" \
".LP3_loop:\n" \
"s_branch .LP3_m1\n" \
".LP3_retry1:\n" \
"s_add_u32 s51, s51, 1\n" \
"s_cmp_gt_u32 s51, 0x20000\n" \
"s_cbranch_scc1 .LP3_end\n" \
".LP3_m0:\n" \
"s_add_u32 s41, s41, s42\n" \
"s_add_u32 s24, s24, s25\n" \
"s_xor_b32 s31, s31, s29\n" \
"s_xor_b32 s32, s32, s29\n" \
"s_xor_b32 s47, s47, s29\n" \
"s_xor_b32 s48, s48, s29\n" \
"s_add_u32 s39, s39, 1\n" \
"buffer_store_dwordx4 v[112:115], v223, s[4:7], s48 offen sc1\n" \
"s_bitcmp1_b32 s31, 21\n" \
"s_cselect_b32 s43, s29, 0\n" \
"v_mov_b32 v116, s43\n" \
"ds_write_b32 v252, v116 offset:16\n" \
".LP3_m1:\n" \
"s_memtime vcc\n" \
"s_waitcnt lgkmcnt(0)\n" \
"s_sub_u32 s43, s41, vcc_lo\n" \
"s_cmp_gt_i32 s43, 0\n" \
"s_cbranch_scc1 .LP3_m1\n" \
"s_cmp_lt_i32 s43, s35\n" \
"s_cbranch_scc1 .LP3_m0\n" \
"buffer_load_dwordx4 v[0:3], v208, s[4:7], s47 offen offset:3072 sc1\n" \
".p2align 3\n" \
"s_memtime s[54:55]\n" \
"s_memrealtime s[56:57]\n" \
"s_memtime s[58:59]\n" \
"s_memrealtime s[60:61]\n" \
"s_nop 0\n" \
"s_nop 0\n" \
"s_memtime s[62:63]\n" \
"s_memrealtime s[64:65]\n" \
"s_memtime s[66:67]\n" \
"s_memrealtime s[68:69]\n" \
"s_memtime s[70:71]\n" \
"s_memrealtime s[72:73]\n" \
"s_memtime s[74:75]\n" \
"s_memrealtime s[76:77]\n" \
"s_nop 0\n" \
"s_nop 0\n" \
"s_memtime s[78:79]\n" \
"s_memrealtime s[80:81]\n" \
"s_memtime s[82:83]\n" \
"s_memrealtime s[84:85]\n" \
"s_memtime s[86:87]\n" \
"s_memrealtime s[88:89]\n" \
"s_memtime s[90:91]\n" \
"s_memrealtime s[92:93]\n" \
"s_nop 0\n" \
"s_nop 0\n" \
"s_memtime s[94:95]\n" \
"s_memrealtime s[96:97]\n" \
"s_memtime s[98:99]\n" \
"s_mov_b32 s52, 0\n" \
"s_mov_b32 s53, 4096\n" \
"s_waitcnt lgkmcnt(0)\n" \
"s_cmp_lg_u32 s96, s56\n" \
"s_cselect_b32 s52, s90, s52\n" \
"s_cselect_b32 s53, s98, s53\n" \
"s_cmp_lg_u32 s92, s56\n" \
"s_cselect_b32 s52, s86, s52\n" \
"s_cselect_b32 s53, s94, s53\n" \
"s_cmp_lg_u32 s88, s56\n" \
"s_cselect_b32 s52, s82, s52\n" \
"s_cselect_b32 s53, s90, s53\n" \
"s_cmp_lg_u32 s84, s56\n" \
"s_cselect_b32 s52, s78, s52\n" \
"s_cselect_b32 s53, s86, s53\n" \
"s_cmp_lg_u32 s80, s56\n" \
"s_cselect_b32 s52, s74, s52\n" \
"s_cselect_b32 s53, s82, s53\n" \
"s_cmp_lg_u32 s76, s56\n" \
"s_cselect_b32 s52, s70, s52\n" \
"s_cselect_b32 s53, s78, s53\n" \
"s_cmp_lg_u32 s72, s56\n" \
"s_cselect_b32 s52, s66, s52\n" \
"s_cselect_b32 s53, s74, s53\n" \
"s_cmp_lg_u32 s68, s56\n" \
"s_cselect_b32 s52, s62, s52\n" \
"s_cselect_b32 s53, s70, s53\n" \
"s_cmp_lg_u32 s64, s56\n" \
"s_cselect_b32 s52, s58, s52\n" \
"s_cselect_b32 s53, s66, s53\n" \
"s_cmp_lg_u32 s60, s56\n" \
"s_cselect_b32 s52, s54, s52\n" \
"s_cselect_b32 s53, s62, s53\n" \
"s_sub_u32 s53, s53, s52\n" \
"s_lshr_b32 s55, s53, 1\n" \
"s_add_u32 s52, s52, s55\n" \
"s_add_u32 s55, s56, 4\n" \
"s_lshl_b32 s55, s55, 4\n" \
"s_sub_u32 s59, s55, s44\n" \
"s_cmp_lt_u32 s59, 2600\n" \
"s_cselect_b32 s63, 1, 3\n" \
"s_mul_i32 s59, s59, s46\n" \
"s_ashr_i32 s59, s59, 12\n" \
"s_add_u32 s59, s59, s45\n" \
"s_sub_u32 s59, s52, s59\n" \
"s_max_i32 s59, s59, -96\n" \
"s_min_i32 s59, s59, 96\n" \
"s_ashr_i32 s63, s59, s63\n" \
"s_add_u32 s63, s63, s46\n" \
"s_max_i32 s63, s63, 0x800\n" \
"s_min_i32 s63, s63, 0x2000\n" \
"s_cmp_le_u32 s53, 40\n" \
"s_cselect_b32 s44, s55, s44\n" \
"s_cselect_b32 s45, s52, s45\n" \
"s_cselect_b32 s46, s63, s46\n" \
"v_mov_b32 v248, s44\n" \
"v_mov_b32 v249, s45\n" \
"v_mov_b32 v250, s46\n" \
"s_mov_b64 exec, 1\n" \
"ds_write_b96 v252, v[248:250]\n" \
"s_mov_b64 exec, -1\n" \
"v_mov_b32 v28, 0\n" \
"v_mov_b32 v29, 0\n" \
"v_mov_b32 v30, 0\n" \
"v_mov_b32 v31, 0\n" \
"v_mov_b32 v32, 0\n" \
"v_mov_b32 v33, 0\n" \
"v_mov_b32 v34, 0\n" \
"v_mov_b32 v35, 0\n" \
"v_mov_b32 v36, 0\n" \
"v_mov_b32 v37, 0\n" \
"v_mov_b32 v38, 0\n" \
"v_mov_b32 v39, 0\n" \
"v_mov_b32 v40, 0\n" \
"v_mov_b32 v41, 0\n" \
"v_mov_b32 v42, 0\n" \
"v_mov_b32 v43, 0\n" \
"s_waitcnt vmcnt(0)\n" \
"ds_write_b128 v220, v[0:3] offset:3072\n" \
"v_max3_u32 v44, v0, v0, v1\n" \
"v_max3_u32 v44, v44, v2, v3\n" \
"v_cmp_eq_u32 vcc, -1, v44\n" \
"v_smfmac_f32_16x16x64_bf16 v[28:31], v[0:3], a[0:7], v200\n" \
"v_smfmac_f32_16x16x64_bf16 v[32:35], v[0:3], a[56:63], v200\n" \
"s_cbranch_vccnz .LP3_retry1\n" \
"s_waitcnt lgkmcnt(0)\n" \
"s_barrier\n" \
"ds_read_b128 v[4:7], v220 offset:0\n" \
"ds_read_b128 v[8:11], v220 offset:1024\n" \
"ds_read_b128 v[12:15], v220 offset:2048\n" \
"ds_read_b128 v[16:19], v220 offset:4096\n" \
"ds_read_b128 v[20:23], v220 offset:5120\n" \
"ds_read_b128 v[24:27], v220 offset:6144\n" \
"ds_read_b64 v[246:247], v252 offset:24\n" \
"ds_read_b96 v[248:250], v252\n" \
"s_mul_i32 s50, s40, 114688\n" \
"buffer_store_dwordx4 v[68:71], v218, s[12:15], s50 offen sc1\n" \
"buffer_store_dword v112, v213, s[4:7], s49 offen sc1\n" \
"s_xor_b32 s43, s49, s29\n" \
"buffer_store_dword v112, v213, s[4:7], s43 offen sc1\n" \
"v_smfmac_f32_16x16x64_bf16 v[36:39], v[0:3], a[112:119], v200\n" \
"v_smfmac_f32_16x16x64_bf16 v[40:43], v[0:3], a[168:175], v200\n" \
"s_waitcnt lgkmcnt(7)\n" \
"v_smfmac_f32_16x16x64_bf16 v[28:31], v[4:7], a[8:15], v200\n" \
"v_smfmac_f32_16x16x64_bf16 v[32:35], v[4:7], a[64:71], v200\n" \
"v_smfmac_f32_16x16x64_bf16 v[36:39], v[4:7], a[120:127], v200\n" \
"v_smfmac_f32_16x16x64_bf16 v[40:43], v[4:7], a[176:183], v200\n" \
"s_waitcnt lgkmcnt(6)\n" \
"v_smfmac_f32_16x16x64_bf16 v[28:31], v[8:11], a[16:23], v200\n" \
"v_smfmac_f32_16x16x64_bf16 v[32:35], v[8:11], a[72:79], v200\n" \
"v_smfmac_f32_16x16x64_bf16 v[36:39], v[8:11], a[128:135], v200\n" \
"v_smfmac_f32_16x16x64_bf16 v[40:43], v[8:11], a[184:191], v200\n" \
"s_waitcnt lgkmcnt(0)\n" \
"v_readfirstlane_b32 s44, v248\n" \
"v_readfirstlane_b32 s45, v249\n" \
"v_smfmac_f32_16x16x64_bf16 v[28:31], v[12:15], a[24:31], v200\n" \
"v_readfirstlane_b32 s46, v250\n" \
"v_readfirstlane_b32 s26, v246\n" \
"v_readfirstlane_b32 s27, v247\n" \
"v_smfmac_f32_16x16x64_bf16 v[32:35], v[12:15], a[80:87], v200\n" \
"s_mul_i32 s42, s25, s46\n" \
"s_lshr_b32 s42, s42, 12\n" \
"s_add_u32 s24, s24, s26\n" \
"v_smfmac_f32_16x16x64_bf16 v[36:39], v[12:15], a[136:143], v200\n" \
"s_sub_u32 s43, s24, s44\n" \
"s_max_i32 s43, s43, 0xfffd0000\n" \
"s_min_i32 s43, s43, 0x4000\n" \
"v_smfmac_f32_16x16x64_bf16 v[40:43], v[12:15], a[192:199], v200\n" \
"s_mul_i32 s43, s43, s46\n" \
"s_ashr_i32 s43, s43, 12\n" \
"s_add_u32 s41, s45, s43\n" \
"s_cmp_eq_u32 s40, 63\n" \
"s_cbranch_scc1 .LP3_last\n" \
"s_branch .LP3_z1\n" \
".LP3_z0:\n" \
"s_add_u32 s41, s41, s42\n" \
"s_add_u32 s24, s24, s25\n" \
"s_xor_b32 s31, s31, s29\n" \
"s_xor_b32 s32, s32, s29\n" \
"s_xor_b32 s47, s47, s29\n" \
"s_xor_b32 s48, s48, s29\n" \
"s_add_u32 s39, s39, 1\n" \
"buffer_store_dwordx4 v[112:115], v223, s[4:7], s48 offen sc1\n" \
"s_bitcmp1_b32 s31, 21\n" \
"s_cselect_b32 s43, s29, 0\n" \
"v_mov_b32 v116, s43\n" \
"ds_write_b32 v252, v116 offset:16\n" \
".LP3_z1:\n" \
"s_memtime vcc\n" \
"s_waitcnt lgkmcnt(0)\n" \
"s_sub_u32 s43, s41, vcc_lo\n" \
"s_cmp_gt_i32 s43, 200\n" \
"s_cbranch_scc1 .LP3_z1\n" \
"s_cmp_lt_i32 s43, s37\n" \
"s_cbranch_scc1 .LP3_z0\n" \
"buffer_load_dwordx2 v[64:65], v211, s[4:7], s47 offen sc1\n" \
".p2align 6\n" \
".LP3_b1:\n" \
"s_memtime vcc\n" \
"s_waitcnt lgkmcnt(0)\n" \
"s_sub_u32 s43, s41, vcc_lo\n" \
"s_cmp_gt_i32 s43, 128\n" \
"s_cbranch_scc1 .LP3_b1\n" \
"s_cmp_lt_i32 s43, s37\n" \
"s_cbranch_scc1 .LP3_z0\n" \
"s_max_i32 s43, s43, 0\n" \
"s_and_b32 s43, s43, -4\n" \
"s_getpc_b64 vcc\n" \
".LP3_b3:\n" \
"s_sub_u32 s43, .LP3_b4-.LP3_b3, s43\n" \
"s_add_u32 vcc_lo, vcc_lo, s43\n" \
"s_addc_u32 vcc_hi, vcc_hi, 0\n" \
"s_setpc_b64 vcc\n" \
".rept 32\n" \
"s_nop 0\n" \
".endr\n" \
".LP3_b4:\n" \
"buffer_load_dwordx4 v[56:59], v210, s[4:7], s47 offen sc1\n" \
"buffer_load_dwordx4 v[60:63], v210, s[4:7], s47 offen offset:256 sc1\n" \
"v_smfmac_f32_16x16x64_bf16 v[28:31], v[16:19], a[32:39], v200\n" \
"v_smfmac_f32_16x16x64_bf16 v[32:35], v[16:19], a[88:95], v200\n" \
"v_smfmac_f32_16x16x64_bf16 v[36:39], v[16:19], a[144:151], v200\n" \
"v_smfmac_f32_16x16x64_bf16 v[40:43], v[16:19], a[200:207], v200\n" \
"v_smfmac_f32_16x16x64_bf16 v[28:31], v[20:23], a[40:47], v200\n" \
"v_smfmac_f32_16x16x64_bf16 v[32:35], v[20:23], a[96:103], v200\n" \
"buffer_load_dwordx4 v[50:53], v212, s[4:7], s47 offen sc1\n" \
"v_smfmac_f32_16x16x64_bf16 v[36:39], v[20:23], a[152:159], v200\n" \
"v_smfmac_f32_16x16x64_bf16 v[40:43], v[20:23], a[208:215], v200\n" \
"v_smfmac_f32_16x16x64_bf16 v[28:31], v[24:27], a[48:55], v200\n" \
"v_smfmac_f32_16x16x64_bf16 v[32:35], v[24:27], a[104:111], v200\n" \
"v_smfmac_f32_16x16x64_bf16 v[36:39], v[24:27], a[160:167], v200\n" \
"v_smfmac_f32_16x16x64_bf16 v[40:43], v[24:27], a[216:223], v200\n" \
"s_nop 2\n" \
"v_mov_b32 v245, v245\n" \
"v_mov_b32 v245, v245\n" \
"v_mov_b32 v245, v245\n" \
"v_permlane32_swap_b32 v28, v32\n" \
"v_permlane32_swap_b32 v29, v33\n" \
"v_permlane32_swap_b32 v30, v34\n" \
"v_permlane32_swap_b32 v31, v35\n" \
"v_add_f32 v28, v28, v32\n" \
"v_add_f32 v29, v29, v33\n" \
"v_add_f32 v30, v30, v34\n" \
"v_add_f32 v31, v31, v35\n" \
"v_cvt_pk_bf16_f32 v68, v28, v29\n" \
"v_cvt_pk_bf16_f32 v69, v30, v31\n" \
"v_permlane32_swap_b32 v36, v40\n" \
"v_permlane32_swap_b32 v37, v41\n" \
"v_permlane32_swap_b32 v38, v42\n" \
"v_permlane32_swap_b32 v39, v43\n" \
"v_add_f32 v36, v36, v40\n" \
"v_add_f32 v37, v37, v41\n" \
"v_add_f32 v38, v38, v42\n" \
"v_add_f32 v39, v39, v43\n" \
"v_cvt_pk_bf16_f32 v70, v36, v37\n" \
"v_cvt_pk_bf16_f32 v71, v38, v39\n" \
"v_mov_b32 v48, v68\n" \
"v_mov_b32 v49, v69\n" \
"v_mov_b32 v54, v70\n" \
"v_mov_b32 v55, v71\n" \
"s_nop 1\n" \
"v_permlane16_swap_b32 v48, v54\n" \
"v_permlane16_swap_b32 v49, v55\n" \
".LP3_wait:\n" \
"s_waitcnt vmcnt(1)\n" \
"v_pk_add_f32 v[72:73], v[56:57], v[60:61]\n" \
"v_pk_add_f32 v[74:75], v[58:59], v[62:63]\n" \
"v_max3_f32 v77, v72, v73, v64\n" \
"v_max3_f32 v77, v77, v74, v75\n" \
"s_nop 1\n" \
"v_max_f32_dpp v76, v77, v77 row_ror:8 row_mask:0xf bank_mask:0xf\n" \
"v_pk_add_f32 v[72:73], v[72:73], v[76:77] op_sel_hi:[1,0] neg_lo:[0,1] neg_hi:[0,1]\n" \
"v_pk_add_f32 v[74:75], v[74:75], v[76:77] op_sel_hi:[1,0] neg_lo:[0,1] neg_hi:[0,1]\n" \
"v_sub_f32 v78, v64, v76\n" \
"v_exp_f32 v72, v72\n" \
"v_exp_f32 v73, v73\n" \
"v_exp_f32 v74, v74\n" \
"v_exp_f32 v75, v75\n" \
"v_exp_f32 v78, v78\n" \
"v_pk_add_f32 v[80:81], v[72:73], v[74:75]\n" \
"v_fma_f32 v80, v78, v65, v80\n" \
"v_add_f32 v81, v80, v81\n" \
"s_nop 1\n" \
"v_add_f32_dpp v80, v81, v81 row_ror:8 row_mask:0xf bank_mask:0xf\n" \
"v_rcp_f32 v80, v80\n" \
"s_nop 0\n" \
"v_mul_f32 v78, v78, v80\n" \
"v_pk_mul_f32 v[72:73], v[72:73], v[80:81] op_sel_hi:[1,0]\n" \
"v_pk_mul_f32 v[74:75], v[74:75], v[80:81] op_sel_hi:[1,0]\n" \
"v_cvt_pk_bf16_f32 v78, v78, v78\n" \
"v_cvt_pk_bf16_f32 v82, v72, v73\n" \
"v_cvt_pk_bf16_f32 v83, v74, v75\n" \
"v_and_b32 v84, v78, v214\n" \
"v_and_b32 v85, v78, v215\n" \
"v_cvt_pk_bf16_f32 v86, v72, v73\n" \
"v_cvt_pk_bf16_f32 v87, v74, v75\n" \
"s_waitcnt vmcnt(0)\n" \
"v_mfma_f32_16x16x32_bf16 v[88:91], v[48:51], v[82:85], 0\n" \
"v_mfma_f32_16x16x32_bf16 v[92:95], v[52:55], v[84:87], 0\n" \
"s_nop 2\n" \
"v_mov_b32 v245, v245\n" \
"s_nop 2\n" \
"v_mov_b32 v245, v245\n" \
"v_add_f32_dpp v96, v92, v88 row_ror:8 row_mask:0xf bank_mask:0x3\n" \
"v_add_f32_dpp v97, v93, v89 row_ror:8 row_mask:0xf bank_mask:0x3\n" \
"v_add_f32_dpp v96, v90, v94 row_ror:8 row_mask:0xf bank_mask:0xc\n" \
"v_add_f32_dpp v97, v91, v95 row_ror:8 row_mask:0xf bank_mask:0xc\n" \
"ds_write_b64 v216, v[96:97]\n" \
"v_cmp_u_f32 vcc, v96, v97\n" \
"s_bitcmp1_b32 s31, 21\n" \
"s_cselect_b32 s18, s29, 0\n" \
"s_xor_b32 s18, s18, s48\n" \
"v_mov_b32 v245, v245\n" \
"s_xor_b32 s19, s18, s29\n" \
"s_nop 0\n" \
"s_nop 0\n" \
"v_mov_b32 v245, v245\n" \
"s_nop 0\n" \
"s_nop 0\n" \
"s_nop 0\n" \
"v_mov_b32 v245, v245\n" \
"s_cbranch_vccnz .LP3_cls\n" \
".LP3_clsr:\n" \
"s_waitcnt lgkmcnt(0)\n" \
"s_barrier\n" \
"ds_read_b64 v[100:101], v217 offset:0\n" \
"ds_read_b64 v[102:103], v217 offset:512\n" \
"ds_read_b32 v116, v252 offset:16\n" \
"ds_read_b64 v[104:105], v217 offset:1024\n" \
"ds_read_b64 v[106:107], v217 offset:1536\n" \
"v_mov_b32 v245, v245\n" \
"v_mov_b32 v245, v245\n" \
"s_add_u32 s40, s40, 1\n" \
"s_add_u32 s24, s24, s27\n" \
"s_mov_b32 s47, s48\n" \
"v_mov_b32 v245, v245\n" \
"s_waitcnt lgkmcnt(2)\n" \
"v_xad_u32 v117, v116, s18, v213\n" \
"v_pk_add_f32 v[100:101], v[100:101], v[102:103]\n" \
"s_waitcnt lgkmcnt(0)\n" \
"v_pk_add_f32 v[104:105], v[104:105], v[106:107]\n" \
"v_pk_add_f32 v[100:101], v[100:101], v[104:105]\n" \
"v_cvt_pk_bf16_f32 v96, v100, v101\n" \
"buffer_store_dword v96, v117, s[4:7], 0 offen sc1\n" \
"s_add_u32 s43, s40, 1\n" \
"s_and_b32 s43, s43, 3\n" \
"s_bitcmp1_b32 s43, 0\n" \
"s_cselect_b32 s48, s32, s31\n" \
"s_lshl_b32 s43, s43, 19\n" \
"s_add_u32 s48, s48, s43\n" \
"s_add_u32 s43, s40, 2\n" \
"s_and_b32 s43, s43, 3\n" \
"s_bitcmp1_b32 s43, 0\n" \
"s_cselect_b32 s49, s32, s31\n" \
"s_lshl_b32 s43, s43, 19\n" \
"s_add_u32 s49, s49, s43\n" \
"s_sub_u32 s43, s24, s44\n" \
"s_add_u32 s43, s43, s28\n" \
"s_max_i32 s43, s43, 0xfffd0000\n" \
"s_min_i32 s43, s43, 0x4000\n" \
"s_mul_i32 s43, s43, s46\n" \
"s_ashr_i32 s43, s43, 12\n" \
"s_add_u32 s41, s45, s43\n" \
"s_lshr_b32 s35, s42, 1\n" \
"s_sub_u32 s35, 0, s35\n" \
"v_xad_u32 v116, v116, s19, v213\n" \
"buffer_store_dword v96, v116, s[4:7], 0 offen sc1\n" \
"s_branch .LP3_loop\n" \
"s_branch .LP3_y1\n" \
".LP3_retry2:\n" \
"s_add_u32 s51, s51, 1\n" \
"s_cmp_gt_u32 s51, 0x20000\n" \
"s_cbranch_scc1 .LP3_end\n" \
".LP3_y0:\n" \
"s_add_u32 s41, s41, s42\n" \
"s_add_u32 s24, s24, s25\n" \
"s_xor_b32 s31, s31, s29\n" \
"s_xor_b32 s32, s32, s29\n" \
"s_xor_b32 s47, s47, s29\n" \
"s_xor_b32 s48, s48, s29\n" \
"s_add_u32 s39, s39, 1\n" \
"buffer_store_dwordx4 v[112:115], v223, s[4:7], s48 offen sc1\n" \
"s_bitcmp1_b32 s31, 21\n" \
"s_cselect_b32 s43, s29, 0\n" \
"v_mov_b32 v116, s43\n" \
"ds_write_b32 v252, v116 offset:16\n" \
".LP3_y1:\n" \
"s_memtime vcc\n" \
"s_waitcnt lgkmcnt(0)\n" \
"s_sub_u32 s43, s41, vcc_lo\n" \
"s_cmp_gt_i32 s43, 200\n" \
"s_cbranch_scc1 .LP3_y1\n" \
"s_cmp_lt_i32 s43, s37\n" \
"s_cbranch_scc1 .LP3_y0\n" \
"buffer_load_dwordx2 v[64:65], v211, s[4:7], s47 offen sc1\n" \
".p2align 6\n" \
".LP3_c1:\n" \
"s_memtime vcc\n" \
"s_waitcnt lgkmcnt(0)\n" \
"s_sub_u32 s43, s41, vcc_lo\n" \
"s_cmp_gt_i32 s43, 128\n" \
"s_cbranch_scc1 .LP3_c1\n" \
"s_cmp_lt_i32 s43, s37\n" \
"s_cbranch_scc1 .LP3_y0\n" \
"s_max_i32 s43, s43, 0\n" \
"s_and_b32 s43, s43, -4\n" \
"s_getpc_b64 vcc\n" \
".LP3_c3:\n" \
"s_sub_u32 s43, .LP3_c4-.LP3_c3, s43\n" \
"s_add_u32 vcc_lo, vcc_lo, s43\n" \
"s_addc_u32 vcc_hi, vcc_hi, 0\n" \
"s_setpc_b64 vcc\n" \
".rept 32\n" \
"s_nop 0\n" \
".endr\n" \
".LP3_c4:\n" \
"buffer_load_dwordx4 v[56:59], v210, s[4:7], s47 offen sc1\n" \
"buffer_load_dwordx4 v[60:63], v210, s[4:7], s47 offen offset:256 sc1\n" \
"s_nop 2\n" \
"v_mov_b32 v245, v245\n" \
"s_nop 2\n" \
"v_mov_b32 v245, v245\n" \
"s_nop 2\n" \
"v_mov_b32 v245, v245\n" \
"s_nop 2\n" \
"v_mov_b32 v245, v245\n" \
"s_nop 2\n" \
"v_mov_b32 v245, v245\n" \
"s_nop 2\n" \
"v_mov_b32 v245, v245\n" \
"buffer_load_dwordx4 v[50:53], v212, s[4:7], s47 offen sc1\n" \
"s_nop 2\n" \
"v_mov_b32 v245, v245\n" \
"s_nop 2\n" \
"v_mov_b32 v245, v245\n" \
"s_nop 2\n" \
"v_mov_b32 v245, v245\n" \
"s_nop 2\n" \
"v_mov_b32 v245, v245\n" \
"s_nop 2\n" \
"v_mov_b32 v245, v245\n" \
"s_nop 2\n" \
"v_mov_b32 v245, v245\n" \
"s_nop 2\n" \
"v_mov_b32 v245, v245\n" \
"s_nop 2\n" \
"v_mov_b32 v245, v245\n" \
"s_nop 2\n" \
"v_mov_b32 v245, v245\n" \
"s_nop 2\n" \
"v_mov_b32 v245, v245\n" \
"s_nop 2\n" \
"v_mov_b32 v245, v245\n" \
"s_nop 2\n" \
"v_mov_b32 v245, v245\n" \
"s_nop 2\n" \
"v_mov_b32 v245, v245\n" \
"s_nop 2\n" \
"v_mov_b32 v245, v245\n" \
"s_nop 2\n" \
"v_mov_b32 v245, v245\n" \
"s_nop 2\n" \
"v_mov_b32 v245, v245\n" \
"s_nop 2\n" \
"v_mov_b32 v245, v245\n" \
"s_branch .LP3_wait\n" \
".LP3_cls:\n" \
"s_waitcnt vmcnt(0)\n" \
"v_max3_u32 v44, v64, v64, v65\n" \
"v_max3_u32 v44, v44, v50, v51\n" \
"v_max3_u32 v44, v44, v52, v53\n" \
"v_cmp_eq_u32 vcc, -1, v44\n" \
"s_cbranch_vccnz .LP3_retry2\n" \
"v_cmp_u_f32 vcc, v56, v56\n" \
"v_cmp_ne_u32 s[2:3], v56, v109\n" \
"s_and_b64 vcc, vcc, s[2:3]\n" \
"s_cbranch_vccnz .LP3_retry2\n" \
"v_cmp_u_f32 vcc, v57, v57\n" \
"v_cmp_ne_u32 s[2:3], v57, v109\n" \
"s_and_b64 vcc, vcc, s[2:3]\n" \
"s_cbranch_vccnz .LP3_retry2\n" \
"v_cmp_u_f32 vcc, v58, v58\n" \
"v_cmp_ne_u32 s[2:3], v58, v109\n" \
"s_and_b64 vcc, vcc, s[2:3]\n" \
"s_cbranch_vccnz .LP3_retry2\n" \
"v_cmp_u_f32 vcc, v59, v59\n" \
"v_cmp_ne_u32 s[2:3], v59, v109\n" \
"s_and_b64 vcc, vcc, s[2:3]\n" \
"s_cbranch_vccnz .LP3_retry2\n" \
"v_cmp_u_f32 vcc, v60, v60\n" \
"v_cmp_ne_u32 s[2:3], v60, v109\n" \
"s_and_b64 vcc, vcc, s[2:3]\n" \
"s_cbranch_vccnz .LP3_retry2\n" \
"v_cmp_u_f32 vcc, v61, v61\n" \
"v_cmp_ne_u32 s[2:3], v61, v109\n" \
"s_and_b64 vcc, vcc, s[2:3]\n" \
"s_cbranch_vccnz .LP3_retry2\n" \
"v_cmp_u_f32 vcc, v62, v62\n" \
"v_cmp_ne_u32 s[2:3], v62, v109\n" \
"s_and_b64 vcc, vcc, s[2:3]\n" \
"s_cbranch_vccnz .LP3_retry2\n" \
"v_cmp_u_f32 vcc, v63, v63\n" \
"v_cmp_ne_u32 s[2:3], v63, v109\n" \
"s_and_b64 vcc, vcc, s[2:3]\n" \
"s_cbranch_vccnz .LP3_retry2\n" \
"s_branch .LP3_clsr\n" \
".LP3_last:\n" \
"v_smfmac_f32_16x16x64_bf16 v[28:31], v[16:19], a[32:39], v200\n" \
"v_smfmac_f32_16x16x64_bf16 v[32:35], v[16:19], a[88:95], v200\n" \
"v_smfmac_f32_16x16x64_bf16 v[36:39], v[16:19], a[144:151], v200\n" \
"v_smfmac_f32_16x16x64_bf16 v[40:43], v[16:19], a[200:207], v200\n" \
"v_smfmac_f32_16x16x64_bf16 v[28:31], v[20:23], a[40:47], v200\n" \
"v_smfmac_f32_16x16x64_bf16 v[32:35], v[20:23], a[96:103], v200\n" \
"v_smfmac_f32_16x16x64_bf16 v[36:39], v[20:23], a[152:159], v200\n" \
"v_smfmac_f32_16x16x64_bf16 v[40:43], v[20:23], a[208:215], v200\n" \
"v_smfmac_f32_16x16x64_bf16 v[28:31], v[24:27], a[48:55], v200\n" \
"v_smfmac_f32_16x16x64_bf16 v[32:35], v[24:27], a[104:111], v200\n" \
"v_smfmac_f32_16x16x64_bf16 v[36:39], v[24:27], a[160:167], v200\n" \
"v_smfmac_f32_16x16x64_bf16 v[40:43], v[24:27], a[216:223], v200\n" \
"s_waitcnt vmcnt(0)\n" \
"s_nop 7\n" \
"v_permlane32_swap_b32 v28, v32\n" \
"v_permlane32_swap_b32 v29, v33\n" \
"v_permlane32_swap_b32 v30, v34\n" \
"v_permlane32_swap_b32 v31, v35\n" \
"v_add_f32 v28, v28, v32\n" \
"v_add_f32 v29, v29, v33\n" \
"v_add_f32 v30, v30, v34\n" \
"v_add_f32 v31, v31, v35\n" \
"v_cvt_pk_bf16_f32 v68, v28, v29\n" \
"v_cvt_pk_bf16_f32 v69, v30, v31\n" \
"v_permlane32_swap_b32 v36, v40\n" \
"v_permlane32_swap_b32 v37, v41\n" \
"v_permlane32_swap_b32 v38, v42\n" \
"v_permlane32_swap_b32 v39, v43\n" \
"v_add_f32 v36, v36, v40\n" \
"v_add_f32 v37, v37, v41\n" \
"v_add_f32 v38, v38, v42\n" \
"v_add_f32 v39, v39, v43\n" \
"v_cvt_pk_bf16_f32 v70, v36, v37\n" \
"v_cvt_pk_bf16_f32 v71, v38, v39\n" \
"s_mov_b32 s50, 7340032\n" \
"buffer_store_dwordx4 v[68:71], v218, s[12:15], s50 offen sc1\n" \
"s_waitcnt vmcnt(0)\n" \
"s_barrier\n" \
".LP3_end:\n" \
"s_endpgm\n" \
".p2align 6\n" \
"fk_role_X0:\n" \
"ds_read_b32 v200, v255 offset:0\n" \
"ds_read_b32 v201, v255 offset:256\n" \
"ds_read_b32 v202, v255 offset:512\n" \
"ds_read_b32 v203, v255 offset:768\n" \
"ds_read_b32 v204, v255 offset:1024\n" \
"ds_read_b32 v205, v255 offset:1280\n" \
"ds_read_b32 v206, v255 offset:1536\n" \
"ds_read_b32 v207, v255 offset:1792\n" \
"ds_read_b32 v208, v255 offset:2048\n" \
"ds_read_b32 v209, v255 offset:2304\n" \
"ds_read_b32 v210, v255 offset:2560\n" \
"ds_read_b32 v211, v255 offset:2816\n" \
"ds_read_b32 v212, v255 offset:3072\n" \
"ds_read_b32 v213, v255 offset:3328\n" \
"ds_read_b32 v214, v255 offset:3584\n" \
"ds_read_b32 v215, v255 offset:3840\n" \
"ds_read_b32 v216, v255 offset:4096\n" \
"ds_read_b32 v217, v255 offset:4352\n" \
"ds_read_b32 v218, v255 offset:4608\n" \
"ds_read_b32 v219, v255 offset:4864\n" \
"ds_read_b32 v220, v255 offset:5120\n" \
"ds_read_b32 v221, v255 offset:5376\n" \
"ds_read_b32 v254, v255 offset:7936\n" \
"s_waitcnt lgkmcnt(0)\n" \
"v_readlane_b32 s4, v254, 0\n" \
"v_readlane_b32 s5, v254, 1\n" \
"v_readlane_b32 s6, v254, 2\n" \
"v_readlane_b32 s7, v254, 3\n" \
"v_readlane_b32 s8, v254, 4\n" \
"v_readlane_b32 s9, v254, 5\n" \
"v_readlane_b32 s10, v254, 6\n" \
"v_readlane_b32 s11, v254, 7\n" \
"v_readlane_b32 s12, v254, 8\n" \
"v_readlane_b32 s13, v254, 9\n" \
"v_readlane_b32 s14, v254, 10\n" \
"v_readlane_b32 s15, v254, 11\n" \
"v_readlane_b32 s16, v254, 12\n" \
"v_readlane_b32 s17, v254, 13\n" \
"v_readlane_b32 s18, v254, 14\n" \
"v_readlane_b32 s19, v254, 15\n" \
"v_readlane_b32 s20, v254, 16\n" \
"v_readlane_b32 s21, v254, 17\n" \
"v_readlane_b32 s22, v254, 18\n" \
"v_readlane_b32 s23, v254, 19\n" \
"v_readlane_b32 s24, v254, 20\n" \
"v_readlane_b32 s25, v254, 21\n" \
"v_readlane_b32 s26, v254, 22\n" \
"v_readlane_b32 s27, v254, 23\n" \
"v_readlane_b32 s28, v254, 24\n" \
"v_readlane_b32 s29, v254, 25\n" \
"v_readlane_b32 s30, v254, 26\n" \
"v_readlane_b32 s31, v254, 27\n" \
"v_readlane_b32 s32, v254, 28\n" \
"v_readlane_b32 s33, v254, 29\n" \
"v_readlane_b32 s34, v254, 30\n" \
"v_readlane_b32 s35, v254, 31\n" \
"v_readlane_b32 s36, v254, 32\n" \
"v_readlane_b32 s37, v254, 33\n" \
"v_readlane_b32 s38, v254, 34\n" \
"v_readlane_b32 s39, v254, 35\n" \
"s_mov_b32 s40, 0\n" \
"s_mov_b32 s51, 0\n" \
"s_nop 0\n" \
"v_mov_b32 v252, s38\n" \
"s_nop 4\n" \
"v_mov_b32 v100, -1\n" \
"v_mov_b32 v101, -1\n" \
"v_mov_b32 v102, -1\n" \
"v_mov_b32 v103, -1\n" \
"s_mov_b32 s35, -1\n" \
"s_mov_b32 s2, 0\n" \
"s_mov_b32 s3, -1\n" \
"s_add_u32 s43, s30, 1\n" \
"s_mov_b32 s50, 0\n" \
".LX0_w61l:\n" \
"buffer_load_dword v240, v204, s[4:7], 0 offen sc1\n" \
"s_waitcnt vmcnt(0)\n" \
".LX0_w61c:\n" \
"v_subrev_u32 v241, s43, v240\n" \
"v_cmp_gt_u32 vcc, 0x100, v241\n" \
"s_cmp_eq_u64 vcc, -1\n" \
"s_cbranch_scc1 .LX0_w61d\n" \
"s_sleep 2\n" \
"s_add_u32 s50, s50, 1\n" \
"s_cmp_lt_u32 s50, 0x20000\n" \
"s_cbranch_scc1 .LX0_w61l\n" \
"s_branch .LX0_end\n" \
".LX0_w61d:\n" \
"v_readlane_b32 s43, v202, 0\n" \
"s_nop 4\n" \
"buffer_load_dwordx4 a[0:3], v201, s[8:11], s43 offen sc1\n" \
"buffer_load_dwordx4 a[4:7], v201, s[8:11], s43 offen offset:1024 sc1\n" \
"v_readlane_b32 s43, v202, 7\n" \
"s_nop 4\n" \
"buffer_load_dwordx4 a[56:59], v201, s[8:11], s43 offen sc1\n" \
"buffer_load_dwordx4 a[60:63], v201, s[8:11], s43 offen offset:1024 sc1\n" \
"v_readlane_b32 s43, v202, 14\n" \
"s_nop 4\n" \
"buffer_load_dwordx4 a[112:115], v201, s[8:11], s43 offen sc1\n" \
"buffer_load_dwordx4 a[116:119], v201, s[8:11], s43 offen offset:1024 sc1\n" \
"v_readlane_b32 s43, v202, 21\n" \
"s_nop 4\n" \
"buffer_load_dwordx4 a[168:171], v201, s[8:11], s43 offen sc1\n" \
"buffer_load_dwordx4 a[172:175], v201, s[8:11], s43 offen offset:1024 sc1\n" \
"s_add_u32 s43, s30, 2\n" \
"s_mov_b32 s50, 0\n" \
".LX0_w62l:\n" \
"buffer_load_dword v240, v204, s[4:7], 0 offen sc1\n" \
"s_waitcnt vmcnt(0)\n" \
".LX0_w62c:\n" \
"v_subrev_u32 v241, s43, v240\n" \
"v_cmp_gt_u32 vcc, 0x100, v241\n" \
"s_cmp_eq_u64 vcc, -1\n" \
"s_cbranch_scc1 .LX0_w62d\n" \
"s_sleep 2\n" \
"s_add_u32 s50, s50, 1\n" \
"s_cmp_lt_u32 s50, 0x20000\n" \
"s_cbranch_scc1 .LX0_w62l\n" \
"s_branch .LX0_end\n" \
".LX0_w62d:\n" \
"v_readlane_b32 s43, v202, 1\n" \
"s_nop 4\n" \
"buffer_load_dwordx4 a[8:11], v201, s[8:11], s43 offen sc1\n" \
"buffer_load_dwordx4 a[12:15], v201, s[8:11], s43 offen offset:1024 sc1\n" \
"v_readlane_b32 s43, v202, 8\n" \
"s_nop 4\n" \
"buffer_load_dwordx4 a[64:67], v201, s[8:11], s43 offen sc1\n" \
"buffer_load_dwordx4 a[68:71], v201, s[8:11], s43 offen offset:1024 sc1\n" \
"v_readlane_b32 s43, v202, 15\n" \
"s_nop 4\n" \
"buffer_load_dwordx4 a[120:123], v201, s[8:11], s43 offen sc1\n" \
"buffer_load_dwordx4 a[124:127], v201, s[8:11], s43 offen offset:1024 sc1\n" \
"v_readlane_b32 s43, v202, 22\n" \
"s_nop 4\n" \
"buffer_load_dwordx4 a[176:179], v201, s[8:11], s43 offen sc1\n" \
"buffer_load_dwordx4 a[180:183], v201, s[8:11], s43 offen offset:1024 sc1\n" \
"s_add_u32 s43, s30, 3\n" \
"s_mov_b32 s50, 0\n" \
".LX0_w63l:\n" \
"buffer_load_dword v240, v204, s[4:7], 0 offen sc1\n" \
"s_waitcnt vmcnt(0)\n" \
".LX0_w63c:\n" \
"v_subrev_u32 v241, s43, v240\n" \
"v_cmp_gt_u32 vcc, 0x100, v241\n" \
"s_cmp_eq_u64 vcc, -1\n" \
"s_cbranch_scc1 .LX0_w63d\n" \
"s_sleep 2\n" \
"s_add_u32 s50, s50, 1\n" \
"s_cmp_lt_u32 s50, 0x20000\n" \
"s_cbranch_scc1 .LX0_w63l\n" \
"s_branch .LX0_end\n" \
".LX0_w63d:\n" \
"v_readlane_b32 s43, v202, 2\n" \
"s_nop 4\n" \
"buffer_load_dwordx4 a[16:19], v201, s[8:11], s43 offen sc1\n" \
"buffer_load_dwordx4 a[20:23], v201, s[8:11], s43 offen offset:1024 sc1\n" \
"v_readlane_b32 s43, v202, 9\n" \
"s_nop 4\n" \
"buffer_load_dwordx4 a[72:75], v201, s[8:11], s43 offen sc1\n" \
"buffer_load_dwordx4 a[76:79], v201, s[8:11], s43 offen offset:1024 sc1\n" \
"v_readlane_b32 s43, v202, 16\n" \
"s_nop 4\n" \
"buffer_load_dwordx4 a[128:131], v201, s[8:11], s43 offen sc1\n" \
"buffer_load_dwordx4 a[132:135], v201, s[8:11], s43 offen offset:1024 sc1\n" \
"v_readlane_b32 s43, v202, 23\n" \
"s_nop 4\n" \
"buffer_load_dwordx4 a[184:187], v201, s[8:11], s43 offen sc1\n" \
"buffer_load_dwordx4 a[188:191], v201, s[8:11], s43 offen offset:1024 sc1\n" \
"s_add_u32 s43, s30, 4\n" \
"s_mov_b32 s50, 0\n" \
".LX0_w64l:\n" \
"buffer_load_dword v240, v204, s[4:7], 0 offen sc1\n" \
"s_waitcnt vmcnt(0)\n" \
".LX0_w64c:\n" \
"v_subrev_u32 v241, s43, v240\n" \
"v_cmp_gt_u32 vcc, 0x100, v241\n" \
"s_cmp_eq_u64 vcc, -1\n" \
"s_cbranch_scc1 .LX0_w64d\n" \
"s_sleep 2\n" \
"s_add_u32 s50, s50, 1\n" \
"s_cmp_lt_u32 s50, 0x20000\n" \
"s_cbranch_scc1 .LX0_w64l\n" \
"s_branch .LX0_end\n" \
".LX0_w64d:\n" \
"v_readlane_b32 s43, v202, 3\n" \
"s_nop 4\n" \
"buffer_load_dwordx4 a[24:27], v201, s[8:11], s43 offen sc1\n" \
"buffer_load_dwordx4 a[28:31], v201, s[8:11], s43 offen offset:1024 sc1\n" \
"v_readlane_b32 s43, v202, 10\n" \
"s_nop 4\n" \
"buffer_load_dwordx4 a[80:83], v201, s[8:11], s43 offen sc1\n" \
"buffer_load_dwordx4 a[84:87], v201, s[8:11], s43 offen offset:1024 sc1\n" \
"v_readlane_b32 s43, v202, 17\n" \
"s_nop 4\n" \
"buffer_load_dwordx4 a[136:139], v201, s[8:11], s43 offen sc1\n" \
"buffer_load_dwordx4 a[140:143], v201, s[8:11], s43 offen offset:1024 sc1\n" \
"v_readlane_b32 s43, v202, 24\n" \
"s_nop 4\n" \
"buffer_load_dwordx4 a[192:195], v201, s[8:11], s43 offen sc1\n" \
"buffer_load_dwordx4 a[196:199], v201, s[8:11], s43 offen offset:1024 sc1\n" \
"s_add_u32 s43, s30, 5\n" \
"s_mov_b32 s50, 0\n" \
".LX0_w65l:\n" \
"buffer_load_dword v240, v204, s[4:7], 0 offen sc1\n" \
"s_waitcnt vmcnt(0)\n" \
".LX0_w65c:\n" \
"v_subrev_u32 v241, s43, v240\n" \
"v_cmp_gt_u32 vcc, 0x100, v241\n" \
"s_cmp_eq_u64 vcc, -1\n" \
"s_cbranch_scc1 .LX0_w65d\n" \
"s_sleep 2\n" \
"s_add_u32 s50, s50, 1\n" \
"s_cmp_lt_u32 s50, 0x20000\n" \
"s_cbranch_scc1 .LX0_w65l\n" \
"s_branch .LX0_end\n" \
".LX0_w65d:\n" \
"v_readlane_b32 s43, v202, 4\n" \
"s_nop 4\n" \
"buffer_load_dwordx4 a[32:35], v201, s[8:11], s43 offen sc1\n" \
"buffer_load_dwordx4 a[36:39], v201, s[8:11], s43 offen offset:1024 sc1\n" \
"v_readlane_b32 s43, v202, 11\n" \
"s_nop 4\n" \
"buffer_load_dwordx4 a[88:91], v201, s[8:11], s43 offen sc1\n" \
"buffer_load_dwordx4 a[92:95], v201, s[8:11], s43 offen offset:1024 sc1\n" \
"v_readlane_b32 s43, v202, 18\n" \
"s_nop 4\n" \
"buffer_load_dwordx4 a[144:147], v201, s[8:11], s43 offen sc1\n" \
"buffer_load_dwordx4 a[148:151], v201, s[8:11], s43 offen offset:1024 sc1\n" \
"v_readlane_b32 s43, v202, 25\n" \
"s_nop 4\n" \
"buffer_load_dwordx4 a[200:203], v201, s[8:11], s43 offen sc1\n" \
"buffer_load_dwordx4 a[204:207], v201, s[8:11], s43 offen offset:1024 sc1\n" \
"s_add_u32 s43, s30, 6\n" \
"s_mov_b32 s50, 0\n" \
".LX0_w66l:\n" \
"buffer_load_dword v240, v204, s[4:7], 0 offen sc1\n" \
"s_waitcnt vmcnt(0)\n" \
".LX0_w66c:\n" \
"v_subrev_u32 v241, s43, v240\n" \
"v_cmp_gt_u32 vcc, 0x100, v241\n" \
"s_cmp_eq_u64 vcc, -1\n" \
"s_cbranch_scc1 .LX0_w66d\n" \
"s_sleep 2\n" \
"s_add_u32 s50, s50, 1\n" \
"s_cmp_lt_u32 s50, 0x20000\n" \
"s_cbranch_scc1 .LX0_w66l\n" \
"s_branch .LX0_end\n" \
".LX0_w66d:\n" \
"v_readlane_b32 s43, v202, 5\n" \
"s_nop 4\n" \
"buffer_load_dwordx4 a[40:43], v201, s[8:11], s43 offen sc1\n" \
"buffer_load_dwordx4 a[44:47], v201, s[8:11], s43 offen offset:1024 sc1\n" \
"v_readlane_b32 s43, v202, 12\n" \
"s_nop 4\n" \
"buffer_load_dwordx4 a[96:99], v201, s[8:11], s43 offen sc1\n" \
"buffer_load_dwordx4 a[100:103], v201, s[8:11], s43 offen offset:1024 sc1\n" \
"v_readlane_b32 s43, v202, 19\n" \
"s_nop 4\n" \
"buffer_load_dwordx4 a[152:155], v201, s[8:11], s43 offen sc1\n" \
"buffer_load_dwordx4 a[156:159], v201, s[8:11], s43 offen offset:1024 sc1\n" \
"v_readlane_b32 s43, v202, 26\n" \
"s_nop 4\n" \
"buffer_load_dwordx4 a[208:211], v201, s[8:11], s43 offen sc1\n" \
"buffer_load_dwordx4 a[212:215], v201, s[8:11], s43 offen offset:1024 sc1\n" \
"s_add_u32 s43, s30, 7\n" \
"s_mov_b32 s50, 0\n" \
".LX0_w67l:\n" \
"buffer_load_dword v240, v204, s[4:7], 0 offen sc1\n" \
"s_waitcnt vmcnt(0)\n" \
".LX0_w67c:\n" \
"v_subrev_u32 v241, s43, v240\n" \
"v_cmp_gt_u32 vcc, 0x100, v241\n" \
"s_cmp_eq_u64 vcc, -1\n" \
"s_cbranch_scc1 .LX0_w67d\n" \
"s_sleep 2\n" \
"s_add_u32 s50, s50, 1\n" \
"s_cmp_lt_u32 s50, 0x20000\n" \
"s_cbranch_scc1 .LX0_w67l\n" \
"s_branch .LX0_end\n" \
".LX0_w67d:\n" \
"v_readlane_b32 s43, v202, 6\n" \
"s_nop 4\n" \
"buffer_load_dwordx4 a[48:51], v201, s[8:11], s43 offen sc1\n" \
"buffer_load_dwordx4 a[52:55], v201, s[8:11], s43 offen offset:1024 sc1\n" \
"v_readlane_b32 s43, v202, 13\n" \
"s_nop 4\n" \
"buffer_load_dwordx4 a[104:107], v201, s[8:11], s43 offen sc1\n" \
"buffer_load_dwordx4 a[108:111], v201, s[8:11], s43 offen offset:1024 sc1\n" \
"v_readlane_b32 s43, v202, 20\n" \
"s_nop 4\n" \
"buffer_load_dwordx4 a[160:163], v201, s[8:11], s43 offen sc1\n" \
"buffer_load_dwordx4 a[164:167], v201, s[8:11], s43 offen offset:1024 sc1\n" \
"v_readlane_b32 s43, v202, 27\n" \
"s_nop 4\n" \
"buffer_load_dwordx4 a[216:219], v201, s[8:11], s43 offen sc1\n" \
"buffer_load_dwordx4 a[220:223], v201, s[8:11], s43 offen offset:1024 sc1\n" \
"s_add_u32 s43, s30, 9\n" \
"s_mov_b32 s50, 0\n" \
".LX0_w68l:\n" \
"buffer_load_dword v240, v215, s[4:7], 0 offen sc1\n" \
"s_waitcnt vmcnt(0)\n" \
".LX0_w68c:\n" \
"v_subrev_u32 v241, s43, v240\n" \
"v_cmp_gt_u32 vcc, 0x100, v241\n" \
"s_cmp_eq_u64 vcc, -1\n" \
"s_cbranch_scc1 .LX0_w68d\n" \
"s_sleep 2\n" \
"s_add_u32 s50, s50, 1\n" \
"s_cmp_lt_u32 s50, 0x20000\n" \
"s_cbranch_scc1 .LX0_w68l\n" \
"s_branch .LX0_end\n" \
".LX0_w68d:\n" \
"s_add_u32 s50, s40, 0\n" \
"s_mul_i32 s50, s50, 458752\n" \
"s_add_u32 s50, s50, s34\n" \
"buffer_load_dwordx4 v[28:31], v201, s[12:15], s50 offen sc1\n" \
"buffer_load_dwordx4 v[32:35], v201, s[12:15], s50 offen offset:1024 sc1\n" \
"buffer_load_dwordx4 v[36:39], v201, s[12:15], s50 offen offset:2048 sc1\n" \
"buffer_load_dwordx4 v[40:43], v201, s[12:15], s50 offen offset:3072 sc1\n" \
"s_add_u32 s43, s30, 1\n" \
"s_mov_b32 s50, 0\n" \
".LX0_w69l:\n" \
"buffer_load_dword v240, v205, s[4:7], 0 offen sc1\n" \
"s_waitcnt vmcnt(0)\n" \
".LX0_w69c:\n" \
"v_subrev_u32 v241, s43, v240\n" \
"v_cmp_gt_u32 vcc, 0x100, v241\n" \
"s_cmp_eq_u64 vcc, -1\n" \
"s_cbranch_scc1 .LX0_w69d\n" \
"s_sleep 2\n" \
"s_add_u32 s50, s50, 1\n" \
"s_cmp_lt_u32 s50, 0x20000\n" \
"s_cbranch_scc1 .LX0_w69l\n" \
"s_branch .LX0_end\n" \
".LX0_w69d:\n" \
"s_add_u32 s43, s30, 1\n" \
"s_mov_b32 s50, 0\n" \
".LX0_w70l:\n" \
"buffer_load_dword v240, v206, s[4:7], 0 offen sc1\n" \
"s_waitcnt vmcnt(0)\n" \
".LX0_w70c:\n" \
"v_subrev_u32 v241, s43, v240\n" \
"v_cmp_gt_u32 vcc, 0x100, v241\n" \
"s_cmp_eq_u64 vcc, -1\n" \
"s_cbranch_scc1 .LX0_w70d\n" \
"s_sleep 2\n" \
"s_add_u32 s50, s50, 1\n" \
"s_cmp_lt_u32 s50, 0x20000\n" \
"s_cbranch_scc1 .LX0_w70l\n" \
"s_branch .LX0_end\n" \
".LX0_w70d:\n" \
"ds_read_b96 v[248:250], v252\n" \
"s_waitcnt lgkmcnt(0)\n" \
"v_readfirstlane_b32 s44, v248\n" \
"v_readfirstlane_b32 s45, v249\n" \
"v_readfirstlane_b32 s46, v250\n" \
"s_mul_i32 s42, s25, s46\n" \
"s_lshr_b32 s42, s42, 12\n" \
".p2align 3\n" \
"s_memtime s[54:55]\n" \
"s_memrealtime s[56:57]\n" \
"s_memtime s[58:59]\n" \
"s_memrealtime s[60:61]\n" \
"s_nop 0\n" \
"s_nop 0\n" \
"s_memtime s[62:63]\n" \
"s_memrealtime s[64:65]\n" \
"s_memtime s[66:67]\n" \
"s_memrealtime s[68:69]\n" \
"s_memtime s[70:71]\n" \
"s_memrealtime s[72:73]\n" \
"s_memtime s[74:75]\n" \
"s_memrealtime s[76:77]\n" \
"s_nop 0\n" \
"s_nop 0\n" \
"s_memtime s[78:79]\n" \
"s_memrealtime s[80:81]\n" \
"s_memtime s[82:83]\n" \
"s_memrealtime s[84:85]\n" \
"s_memtime s[86:87]\n" \
"s_memrealtime s[88:89]\n" \
"s_memtime s[90:91]\n" \
"s_memrealtime s[92:93]\n" \
"s_nop 0\n" \
"s_nop 0\n" \
"s_memtime s[94:95]\n" \
"s_memrealtime s[96:97]\n" \
"s_memtime s[98:99]\n" \
"s_mov_b32 s52, 0\n" \
"s_mov_b32 s53, 4096\n" \
"s_waitcnt lgkmcnt(0)\n" \
"s_cmp_lg_u32 s96, s56\n" \
"s_cselect_b32 s52, s90, s52\n" \
"s_cselect_b32 s53, s98, s53\n" \
"s_cmp_lg_u32 s92, s56\n" \
"s_cselect_b32 s52, s86, s52\n" \
"s_cselect_b32 s53, s94, s53\n" \
"s_cmp_lg_u32 s88, s56\n" \
"s_cselect_b32 s52, s82, s52\n" \
"s_cselect_b32 s53, s90, s53\n" \
"s_cmp_lg_u32 s84, s56\n" \
"s_cselect_b32 s52, s78, s52\n" \
"s_cselect_b32 s53, s86, s53\n" \
"s_cmp_lg_u32 s80, s56\n" \
"s_cselect_b32 s52, s74, s52\n" \
"s_cselect_b32 s53, s82, s53\n" \
"s_cmp_lg_u32 s76, s56\n" \
"s_cselect_b32 s52, s70, s52\n" \
"s_cselect_b32 s53, s78, s53\n" \
"s_cmp_lg_u32 s72, s56\n" \
"s_cselect_b32 s52, s66, s52\n" \
"s_cselect_b32 s53, s74, s53\n" \
"s_cmp_lg_u32 s68, s56\n" \
"s_cselect_b32 s52, s62, s52\n" \
"s_cselect_b32 s53, s70, s53\n" \
"s_cmp_lg_u32 s64, s56\n" \
"s_cselect_b32 s52, s58, s52\n" \
"s_cselect_b32 s53, s66, s53\n" \
"s_cmp_lg_u32 s60, s56\n" \
"s_cselect_b32 s52, s54, s52\n" \
"s_cselect_b32 s53, s62, s53\n" \
"s_sub_u32 s53, s53, s52\n" \
"s_lshr_b32 s55, s53, 1\n" \
"s_add_u32 s52, s52, s55\n" \
"s_add_u32 s55, s56, 4\n" \
"s_lshl_b32 s55, s55, 4\n" \
"s_cmp_le_u32 s53, 40\n" \
"s_cselect_b32 s44, s55, s44\n" \
"s_cselect_b32 s45, s52, s45\n" \
"v_mov_b32 v248, s44\n" \
"v_mov_b32 v249, s45\n" \
"v_mov_b32 v250, s46\n" \
"s_mov_b64 exec, 1\n" \
"ds_write_b96 v252, v[248:250]\n" \
"s_mov_b64 exec, -1\n" \
".p2align 3\n" \
"s_memtime s[54:55]\n" \
"s_memrealtime s[56:57]\n" \
"s_memtime s[58:59]\n" \
"s_memrealtime s[60:61]\n" \
"s_nop 0\n" \
"s_nop 0\n" \
"s_memtime s[62:63]\n" \
"s_memrealtime s[64:65]\n" \
"s_memtime s[66:67]\n" \
"s_memrealtime s[68:69]\n" \
"s_memtime s[70:71]\n" \
"s_memrealtime s[72:73]\n" \
"s_memtime s[74:75]\n" \
"s_memrealtime s[76:77]\n" \
"s_nop 0\n" \
"s_nop 0\n" \
"s_memtime s[78:79]\n" \
"s_memrealtime s[80:81]\n" \
"s_memtime s[82:83]\n" \
"s_memrealtime s[84:85]\n" \
"s_memtime s[86:87]\n" \
"s_memrealtime s[88:89]\n" \
"s_memtime s[90:91]\n" \
"s_memrealtime s[92:93]\n" \
"s_nop 0\n" \
"s_nop 0\n" \
"s_memtime s[94:95]\n" \
"s_memrealtime s[96:97]\n" \
"s_memtime s[98:99]\n" \
"s_mov_b32 s52, 0\n" \
"s_mov_b32 s53, 4096\n" \
"s_waitcnt lgkmcnt(0)\n" \
"s_cmp_lg_u32 s96, s56\n" \
"s_cselect_b32 s52, s90, s52\n" \
"s_cselect_b32 s53, s98, s53\n" \
"s_cmp_lg_u32 s92, s56\n" \
"s_cselect_b32 s52, s86, s52\n" \
"s_cselect_b32 s53, s94, s53\n" \
"s_cmp_lg_u32 s88, s56\n" \
"s_cselect_b32 s52, s82, s52\n" \
"s_cselect_b32 s53, s90, s53\n" \
"s_cmp_lg_u32 s84, s56\n" \
"s_cselect_b32 s52, s78, s52\n" \
"s_cselect_b32 s53, s86, s53\n" \
"s_cmp_lg_u32 s80, s56\n" \
"s_cselect_b32 s52, s74, s52\n" \
"s_cselect_b32 s53, s82, s53\n" \
"s_cmp_lg_u32 s76, s56\n" \
"s_cselect_b32 s52, s70, s52\n" \
"s_cselect_b32 s53, s78, s53\n" \
"s_cmp_lg_u32 s72, s56\n" \
"s_cselect_b32 s52, s66, s52\n" \
"s_cselect_b32 s53, s74, s53\n" \
"s_cmp_lg_u32 s68, s56\n" \
"s_cselect_b32 s52, s62, s52\n" \
"s_cselect_b32 s53, s70, s53\n" \
"s_cmp_lg_u32 s64, s56\n" \
"s_cselect_b32 s52, s58, s52\n" \
"s_cselect_b32 s53, s66, s53\n" \
"s_cmp_lg_u32 s60, s56\n" \
"s_cselect_b32 s52, s54, s52\n" \
"s_cselect_b32 s53, s62, s53\n" \
"s_sub_u32 s53, s53, s52\n" \
"s_lshr_b32 s55, s53, 1\n" \
"s_add_u32 s52, s52, s55\n" \
"s_add_u32 s55, s56, 4\n" \
"s_lshl_b32 s55, s55, 4\n" \
"s_cmp_le_u32 s53, 40\n" \
"s_cselect_b32 s44, s55, s44\n" \
"s_cselect_b32 s45, s52, s45\n" \
"v_mov_b32 v248, s44\n" \
"v_mov_b32 v249, s45\n" \
"v_mov_b32 v250, s46\n" \
"s_mov_b64 exec, 1\n" \
"ds_write_b96 v252, v[248:250]\n" \
"s_mov_b64 exec, -1\n" \
"s_waitcnt lgkmcnt(0)\n" \
"s_barrier\n" \
".LX0_loop:\n" \
"s_add_u32 s43, s40, 2\n" \
"s_and_b32 s43, s43, 3\n" \
"s_bitcmp1_b32 s43, 0\n" \
"s_cselect_b32 s49, s32, s31\n" \
"s_lshl_b32 s43, s43, 19\n" \
"s_add_u32 s49, s49, s43\n" \
"ds_read_b128 v[0:3], v216 offset:0\n" \
"ds_read_b128 v[4:7], v216 offset:1024\n" \
"ds_read_b128 v[8:11], v216 offset:2048\n" \
"ds_read_b128 v[12:15], v216 offset:3072\n" \
"ds_read_b128 v[16:19], v216 offset:4096\n" \
"ds_read_b128 v[20:23], v216 offset:5120\n" \
"ds_read_b128 v[24:27], v216 offset:6144\n" \
"v_mov_b32 v60, 0\n" \
"v_mov_b32 v61, 0\n" \
"v_mov_b32 v62, 0\n" \
"v_mov_b32 v63, 0\n" \
"v_mov_b32 v64, 0\n" \
"v_mov_b32 v65, 0\n" \
"v_mov_b32 v66, 0\n" \
"v_mov_b32 v67, 0\n" \
"v_mov_b32 v68, 0\n" \
"v_mov_b32 v69, 0\n" \
"v_mov_b32 v70, 0\n" \
"v_mov_b32 v71, 0\n" \
"v_mov_b32 v72, 0\n" \
"v_mov_b32 v73, 0\n" \
"v_mov_b32 v74, 0\n" \
"v_mov_b32 v75, 0\n" \
"s_nop 1\n" \
"s_waitcnt lgkmcnt(6)\n" \
"v_smfmac_f32_16x16x64_bf16 v[60:63], v[0:3], a[0:7], v200\n" \
"v_smfmac_f32_16x16x64_bf16 v[64:67], v[0:3], a[56:63], v200\n" \
"v_smfmac_f32_16x16x64_bf16 v[68:71], v[0:3], a[112:119], v200\n" \
"v_smfmac_f32_16x16x64_bf16 v[72:75], v[0:3], a[168:175], v200\n" \
"s_waitcnt lgkmcnt(5)\n" \
"v_smfmac_f32_16x16x64_bf16 v[60:63], v[4:7], a[8:15], v200\n" \
"v_smfmac_f32_16x16x64_bf16 v[64:67], v[4:7], a[64:71], v200\n" \
"v_smfmac_f32_16x16x64_bf16 v[68:71], v[4:7], a[120:127], v200\n" \
"v_smfmac_f32_16x16x64_bf16 v[72:75], v[4:7], a[176:183], v200\n" \
"s_waitcnt lgkmcnt(4)\n" \
"v_smfmac_f32_16x16x64_bf16 v[60:63], v[8:11], a[16:23], v200\n" \
"v_smfmac_f32_16x16x64_bf16 v[64:67], v[8:11], a[72:79], v200\n" \
"v_smfmac_f32_16x16x64_bf16 v[68:71], v[8:11], a[128:135], v200\n" \
"v_smfmac_f32_16x16x64_bf16 v[72:75], v[8:11], a[184:191], v200\n" \
"s_waitcnt lgkmcnt(3)\n" \
"v_smfmac_f32_16x16x64_bf16 v[60:63], v[12:15], a[24:31], v200\n" \
"v_smfmac_f32_16x16x64_bf16 v[64:67], v[12:15], a[80:87], v200\n" \
"v_smfmac_f32_16x16x64_bf16 v[68:71], v[12:15], a[136:143], v200\n" \
"v_smfmac_f32_16x16x64_bf16 v[72:75], v[12:15], a[192:199], v200\n" \
"s_waitcnt lgkmcnt(2)\n" \
"v_smfmac_f32_16x16x64_bf16 v[60:63], v[16:19], a[32:39], v200\n" \
"v_smfmac_f32_16x16x64_bf16 v[64:67], v[16:19], a[88:95], v200\n" \
"v_smfmac_f32_16x16x64_bf16 v[68:71], v[16:19], a[144:151], v200\n" \
"v_smfmac_f32_16x16x64_bf16 v[72:75], v[16:19], a[200:207], v200\n" \
"s_waitcnt lgkmcnt(1)\n" \
"v_smfmac_f32_16x16x64_bf16 v[60:63], v[20:23], a[40:47], v200\n" \
"v_smfmac_f32_16x16x64_bf16 v[64:67], v[20:23], a[96:103], v200\n" \
"v_smfmac_f32_16x16x64_bf16 v[68:71], v[20:23], a[152:159], v200\n" \
"v_smfmac_f32_16x16x64_bf16 v[72:75], v[20:23], a[208:215], v200\n" \
"s_waitcnt lgkmcnt(0)\n" \
"v_smfmac_f32_16x16x64_bf16 v[60:63], v[24:27], a[48:55], v200\n" \
"v_smfmac_f32_16x16x64_bf16 v[64:67], v[24:27], a[104:111], v200\n" \
"v_smfmac_f32_16x16x64_bf16 v[68:71], v[24:27], a[160:167], v200\n" \
"v_smfmac_f32_16x16x64_bf16 v[72:75], v[24:27], a[216:223], v200\n" \
"s_barrier\n" \
"s_add_u32 s50, s40, 1\n" \
"s_mul_i32 s50, s50, 458752\n" \
"s_add_u32 s50, s50, s34\n" \
"buffer_load_dwordx4 v[44:47], v201, s[12:15], s50 offen sc1\n" \
"buffer_load_dwordx4 v[48:51], v201, s[12:15], s50 offen offset:1024 sc1\n" \
"buffer_load_dwordx4 v[52:55], v201, s[12:15], s50 offen offset:2048 sc1\n" \
"buffer_load_dwordx4 v[56:59], v201, s[12:15], s50 offen offset:3072 sc1\n" \
"s_nop 7\n" \
"v_permlane32_swap_b32 v60, v64\n" \
"v_permlane32_swap_b32 v61, v65\n" \
"v_permlane32_swap_b32 v62, v66\n" \
"v_permlane32_swap_b32 v63, v67\n" \
"v_add_f32 v60, v60, v64\n" \
"v_add_f32 v61, v61, v65\n" \
"v_add_f32 v62, v62, v66\n" \
"v_add_f32 v63, v63, v67\n" \
"v_cvt_pk_bf16_f32 v184, v60, v61\n" \
"v_cvt_pk_bf16_f32 v185, v62, v63\n" \
"ds_write_b64 v217, v[184:185] offset:0\n" \
"v_permlane32_swap_b32 v68, v72\n" \
"v_permlane32_swap_b32 v69, v73\n" \
"v_permlane32_swap_b32 v70, v74\n" \
"v_permlane32_swap_b32 v71, v75\n" \
"v_add_f32 v68, v68, v72\n" \
"v_add_f32 v69, v69, v73\n" \
"v_add_f32 v70, v70, v74\n" \
"v_add_f32 v71, v71, v75\n" \
"v_cvt_pk_bf16_f32 v186, v68, v69\n" \
"v_cvt_pk_bf16_f32 v187, v70, v71\n" \
"ds_write_b64 v217, v[186:187] offset:512\n" \
"ds_read_b64_tr_b16 v[0:1], v218 offset:0\n" \
"ds_read_b64_tr_b16 v[2:3], v218 offset:64\n" \
"s_add_u32 s43, s40, 0\n" \
"s_mul_i32 s43, s43, 114688\n" \
"s_waitcnt lgkmcnt(0)\n" \
"buffer_store_dwordx4 v[0:3], v219, s[16:19], s43 offen sc1\n" \
"s_waitcnt vmcnt(5)\n" \
"s_barrier\n" \
"ds_read_b64_tr_b16 v[92:93], v213\n" \
"ds_read_b64_tr_b16 v[94:95], v213 offset:64\n" \
"ds_read_b32 v84, v252 offset:16\n" \
"v_mov_b32 v245, v245\n" \
"s_nop 2\n" \
"v_mov_b32 v245, v245\n" \
"s_nop 2\n" \
"v_mov_b32 v245, v245\n" \
"s_waitcnt lgkmcnt(1)\n" \
"v_mfma_f32_16x16x32_bf16 v[68:71], v[92:95], v[28:31], 0\n" \
"s_waitcnt lgkmcnt(0)\n" \
"v_xor_b32 v85, s29, v84\n" \
"v_mfma_f32_16x16x32_bf16 v[76:79], v[92:95], v[36:39], 0\n" \
"v_add_u32 v84, v84, v210\n" \
"v_add_u32 v85, v85, v210\n" \
"v_mfma_f32_16x16x32_bf16 v[72:75], v[92:95], v[32:35], 0\n" \
"s_nop 1\n" \
"v_mfma_f32_16x16x32_bf16 v[80:83], v[92:95], v[40:43], 0\n" \
"v_cvt_pk_bf16_f32 v96, v68, v69\n" \
"v_cvt_pk_bf16_f32 v97, v70, v71\n" \
"v_cvt_pk_bf16_f32 v98, v76, v77\n" \
"v_cvt_pk_bf16_f32 v99, v78, v79\n" \
"s_mov_b64 exec, s[2:3]\n" \
"v_cvt_pk_bf16_f32 v96, v72, v73\n" \
"v_cvt_pk_bf16_f32 v97, v74, v75\n" \
"s_nop 0\n" \
"v_cvt_pk_bf16_f32 v98, v80, v81\n" \
"v_cvt_pk_bf16_f32 v99, v82, v83\n" \
"s_mov_b64 exec, -1\n" \
"s_nop 1\n" \
"v_permlane16_swap_b32 v96, v98\n" \
"v_permlane16_swap_b32 v97, v99\n" \
"buffer_store_dwordx4 v[96:99], v84, s[4:7], 0 offen sc1\n" \
"s_barrier\n" \
"buffer_store_dwordx4 v[96:99], v85, s[4:7], 0 offen sc1\n" \
"ds_read_b96 v[248:250], v252\n" \
"s_waitcnt lgkmcnt(0)\n" \
"v_readfirstlane_b32 s44, v248\n" \
"v_readfirstlane_b32 s45, v249\n" \
"v_readfirstlane_b32 s46, v250\n" \
"s_mul_i32 s42, s25, s46\n" \
"s_lshr_b32 s42, s42, 12\n" \
".p2align 3\n" \
"s_memtime s[54:55]\n" \
"s_memrealtime s[56:57]\n" \
"s_memtime s[58:59]\n" \
"s_memrealtime s[60:61]\n" \
"s_nop 0\n" \
"s_nop 0\n" \
"s_memtime s[62:63]\n" \
"s_memrealtime s[64:65]\n" \
"s_memtime s[66:67]\n" \
"s_memrealtime s[68:69]\n" \
"s_memtime s[70:71]\n" \
"s_memrealtime s[72:73]\n" \
"s_memtime s[74:75]\n" \
"s_memrealtime s[76:77]\n" \
"s_nop 0\n" \
"s_nop 0\n" \
"s_memtime s[78:79]\n" \
"s_memrealtime s[80:81]\n" \
"s_memtime s[82:83]\n" \
"s_memrealtime s[84:85]\n" \
"s_memtime s[86:87]\n" \
"s_memrealtime s[88:89]\n" \
"s_memtime s[90:91]\n" \
"s_memrealtime s[92:93]\n" \
"s_nop 0\n" \
"s_nop 0\n" \
"s_memtime s[94:95]\n" \
"s_memrealtime s[96:97]\n" \
"s_memtime s[98:99]\n" \
"s_mov_b32 s52, 0\n" \
"s_mov_b32 s53, 4096\n" \
"s_waitcnt lgkmcnt(0)\n" \
"s_cmp_lg_u32 s96, s56\n" \
"s_cselect_b32 s52, s90, s52\n" \
"s_cselect_b32 s53, s98, s53\n" \
"s_cmp_lg_u32 s92, s56\n" \
"s_cselect_b32 s52, s86, s52\n" \
"s_cselect_b32 s53, s94, s53\n" \
"s_cmp_lg_u32 s88, s56\n" \
"s_cselect_b32 s52, s82, s52\n" \
"s_cselect_b32 s53, s90, s53\n" \
"s_cmp_lg_u32 s84, s56\n" \
"s_cselect_b32 s52, s78, s52\n" \
"s_cselect_b32 s53, s86, s53\n" \
"s_cmp_lg_u32 s80, s56\n" \
"s_cselect_b32 s52, s74, s52\n" \
"s_cselect_b32 s53, s82, s53\n" \
"s_cmp_lg_u32 s76, s56\n" \
"s_cselect_b32 s52, s70, s52\n" \
"s_cselect_b32 s53, s78, s53\n" \
"s_cmp_lg_u32 s72, s56\n" \
"s_cselect_b32 s52, s66, s52\n" \
"s_cselect_b32 s53, s74, s53\n" \
"s_cmp_lg_u32 s68, s56\n" \
"s_cselect_b32 s52, s62, s52\n" \
"s_cselect_b32 s53, s70, s53\n" \
"s_cmp_lg_u32 s64, s56\n" \
"s_cselect_b32 s52, s58, s52\n" \
"s_cselect_b32 s53, s66, s53\n" \
"s_cmp_lg_u32 s60, s56\n" \
"s_cselect_b32 s52, s54, s52\n" \
"s_cselect_b32 s53, s62, s53\n" \
"s_sub_u32 s53, s53, s52\n" \
"s_lshr_b32 s55, s53, 1\n" \
"s_add_u32 s52, s52, s55\n" \
"s_add_u32 s55, s56, 4\n" \
"s_lshl_b32 s55, s55, 4\n" \
"s_sub_u32 s59, s55, s44\n" \
"s_cmp_lt_u32 s59, 2600\n" \
"s_cselect_b32 s63, 1, 3\n" \
"s_mul_i32 s59, s59, s46\n" \
"s_ashr_i32 s59, s59, 12\n" \
"s_add_u32 s59, s59, s45\n" \
"s_sub_u32 s59, s52, s59\n" \
"s_max_i32 s59, s59, -96\n" \
"s_min_i32 s59, s59, 96\n" \
"s_ashr_i32 s63, s59, s63\n" \
"s_add_u32 s63, s63, s46\n" \
"s_max_i32 s63, s63, 0x800\n" \
"s_min_i32 s63, s63, 0x2000\n" \
"s_cmp_le_u32 s53, 40\n" \
"s_cselect_b32 s44, s55, s44\n" \
"s_cselect_b32 s45, s52, s45\n" \
"s_cselect_b32 s46, s63, s46\n" \
"v_mov_b32 v248, s44\n" \
"v_mov_b32 v249, s45\n" \
"v_mov_b32 v250, s46\n" \
"s_mov_b64 exec, 1\n" \
"ds_write_b96 v252, v[248:250]\n" \
"s_mov_b64 exec, -1\n" \
"buffer_store_dwordx4 v[100:103], v214, s[4:7], s49 offen sc1\n" \
"s_xor_b32 s43, s49, s29\n" \
"buffer_store_dwordx4 v[100:103], v214, s[4:7], s43 offen sc1\n" \
"s_waitcnt vmcnt(4)\n" \
"v_mov_b32 v28, v44\n" \
"v_mov_b32 v29, v45\n" \
"v_mov_b32 v30, v46\n" \
"v_mov_b32 v31, v47\n" \
"v_mov_b32 v32, v48\n" \
"v_mov_b32 v33, v49\n" \
"v_mov_b32 v34, v50\n" \
"v_mov_b32 v35, v51\n" \
"v_mov_b32 v36, v52\n" \
"v_mov_b32 v37, v53\n" \
"v_mov_b32 v38, v54\n" \
"v_mov_b32 v39, v55\n" \
"v_mov_b32 v40, v56\n" \
"v_mov_b32 v41, v57\n" \
"v_mov_b32 v42, v58\n" \
"v_mov_b32 v43, v59\n" \
"s_add_u32 s40, s40, 1\n" \
"s_waitcnt lgkmcnt(0)\n" \
"s_barrier\n" \
"s_cmp_lt_u32 s40, 63\n" \
"s_cbranch_scc1 .LX0_loop\n" \
"ds_read_b128 v[0:3], v216 offset:0\n" \
"ds_read_b128 v[4:7], v216 offset:1024\n" \
"ds_read_b128 v[8:11], v216 offset:2048\n" \
"ds_read_b128 v[12:15], v216 offset:3072\n" \
"ds_read_b128 v[16:19], v216 offset:4096\n" \
"ds_read_b128 v[20:23], v216 offset:5120\n" \
"ds_read_b128 v[24:27], v216 offset:6144\n" \
"v_mov_b32 v60, 0\n" \
"v_mov_b32 v61, 0\n" \
"v_mov_b32 v62, 0\n" \
"v_mov_b32 v63, 0\n" \
"v_mov_b32 v64, 0\n" \
"v_mov_b32 v65, 0\n" \
"v_mov_b32 v66, 0\n" \
"v_mov_b32 v67, 0\n" \
"v_mov_b32 v68, 0\n" \
"v_mov_b32 v69, 0\n" \
"v_mov_b32 v70, 0\n" \
"v_mov_b32 v71, 0\n" \
"v_mov_b32 v72, 0\n" \
"v_mov_b32 v73, 0\n" \
"v_mov_b32 v74, 0\n" \
"v_mov_b32 v75, 0\n" \
"s_nop 1\n" \
"s_waitcnt lgkmcnt(6)\n" \
"v_smfmac_f32_16x16x64_bf16 v[60:63], v[0:3], a[0:7], v200\n" \
"v_smfmac_f32_16x16x64_bf16 v[64:67], v[0:3], a[56:63], v200\n" \
"v_smfmac_f32_16x16x64_bf16 v[68:71], v[0:3], a[112:119], v200\n" \
"v_smfmac_f32_16x16x64_bf16 v[72:75], v[0:3], a[168:175], v200\n" \
"s_waitcnt lgkmcnt(5)\n" \
"v_smfmac_f32_16x16x64_bf16 v[60:63], v[4:7], a[8:15], v200\n" \
"v_smfmac_f32_16x16x64_bf16 v[64:67], v[4:7], a[64:71], v200\n" \
"v_smfmac_f32_16x16x64_bf16 v[68:71], v[4:7], a[120:127], v200\n" \
"v_smfmac_f32_16x16x64_bf16 v[72:75], v[4:7], a[176:183], v200\n" \
"s_waitcnt lgkmcnt(4)\n" \
"v_smfmac_f32_16x16x64_bf16 v[60:63], v[8:11], a[16:23], v200\n" \
"v_smfmac_f32_16x16x64_bf16 v[64:67], v[8:11], a[72:79], v200\n" \
"v_smfmac_f32_16x16x64_bf16 v[68:71], v[8:11], a[128:135], v200\n" \
"v_smfmac_f32_16x16x64_bf16 v[72:75], v[8:11], a[184:191], v200\n" \
"s_waitcnt lgkmcnt(3)\n" \
"v_smfmac_f32_16x16x64_bf16 v[60:63], v[12:15], a[24:31], v200\n" \
"v_smfmac_f32_16x16x64_bf16 v[64:67], v[12:15], a[80:87], v200\n" \
"v_smfmac_f32_16x16x64_bf16 v[68:71], v[12:15], a[136:143], v200\n" \
"v_smfmac_f32_16x16x64_bf16 v[72:75], v[12:15], a[192:199], v200\n" \
"s_waitcnt lgkmcnt(2)\n" \
"v_smfmac_f32_16x16x64_bf16 v[60:63], v[16:19], a[32:39], v200\n" \
"v_smfmac_f32_16x16x64_bf16 v[64:67], v[16:19], a[88:95], v200\n" \
"v_smfmac_f32_16x16x64_bf16 v[68:71], v[16:19], a[144:151], v200\n" \
"v_smfmac_f32_16x16x64_bf16 v[72:75], v[16:19], a[200:207], v200\n" \
"s_waitcnt lgkmcnt(1)\n" \
"v_smfmac_f32_16x16x64_bf16 v[60:63], v[20:23], a[40:47], v200\n" \
"v_smfmac_f32_16x16x64_bf16 v[64:67], v[20:23], a[96:103], v200\n" \
"v_smfmac_f32_16x16x64_bf16 v[68:71], v[20:23], a[152:159], v200\n" \
"v_smfmac_f32_16x16x64_bf16 v[72:75], v[20:23], a[208:215], v200\n" \
"s_waitcnt lgkmcnt(0)\n" \
"v_smfmac_f32_16x16x64_bf16 v[60:63], v[24:27], a[48:55], v200\n" \
"v_smfmac_f32_16x16x64_bf16 v[64:67], v[24:27], a[104:111], v200\n" \
"v_smfmac_f32_16x16x64_bf16 v[68:71], v[24:27], a[160:167], v200\n" \
"v_smfmac_f32_16x16x64_bf16 v[72:75], v[24:27], a[216:223], v200\n" \
"s_nop 7\n" \
"v_permlane32_swap_b32 v60, v64\n" \
"v_permlane32_swap_b32 v61, v65\n" \
"v_permlane32_swap_b32 v62, v66\n" \
"v_permlane32_swap_b32 v63, v67\n" \
"v_add_f32 v60, v60, v64\n" \
"v_add_f32 v61, v61, v65\n" \
"v_add_f32 v62, v62, v66\n" \
"v_add_f32 v63, v63, v67\n" \
"v_cvt_pk_bf16_f32 v184, v60, v61\n" \
"v_cvt_pk_bf16_f32 v185, v62, v63\n" \
"ds_write_b64 v217, v[184:185] offset:0\n" \
"v_permlane32_swap_b32 v68, v72\n" \
"v_permlane32_swap_b32 v69, v73\n" \
"v_permlane32_swap_b32 v70, v74\n" \
"v_permlane32_swap_b32 v71, v75\n" \
"v_add_f32 v68, v68, v72\n" \
"v_add_f32 v69, v69, v73\n" \
"v_add_f32 v70, v70, v74\n" \
"v_add_f32 v71, v71, v75\n" \
"v_cvt_pk_bf16_f32 v186, v68, v69\n" \
"v_cvt_pk_bf16_f32 v187, v70, v71\n" \
"ds_write_b64 v217, v[186:187] offset:512\n" \
"ds_read_b64_tr_b16 v[0:1], v218 offset:0\n" \
"ds_read_b64_tr_b16 v[2:3], v218 offset:64\n" \
"s_add_u32 s43, s40, 0\n" \
"s_mul_i32 s43, s43, 114688\n" \
"s_waitcnt lgkmcnt(0)\n" \
"buffer_store_dwordx4 v[0:3], v219, s[16:19], s43 offen sc1\n" \
"s_waitcnt vmcnt(0)\n" \
"s_barrier\n" \
"ds_read_b128 v[0:3], v216 offset:7168\n" \
"ds_read_b128 v[4:7], v216 offset:8192\n" \
"ds_read_b128 v[8:11], v216 offset:9216\n" \
"ds_read_b128 v[12:15], v216 offset:10240\n" \
"ds_read_b128 v[16:19], v216 offset:11264\n" \
"ds_read_b128 v[20:23], v216 offset:12288\n" \
"ds_read_b128 v[24:27], v216 offset:13312\n" \
"v_mov_b32 v60, 0\n" \
"v_mov_b32 v61, 0\n" \
"v_mov_b32 v62, 0\n" \
"v_mov_b32 v63, 0\n" \
"v_mov_b32 v64, 0\n" \
"v_mov_b32 v65, 0\n" \
"v_mov_b32 v66, 0\n" \
"v_mov_b32 v67, 0\n" \
"v_mov_b32 v68, 0\n" \
"v_mov_b32 v69, 0\n" \
"v_mov_b32 v70, 0\n" \
"v_mov_b32 v71, 0\n" \
"v_mov_b32 v72, 0\n" \
"v_mov_b32 v73, 0\n" \
"v_mov_b32 v74, 0\n" \
"v_mov_b32 v75, 0\n" \
"s_nop 1\n" \
"s_waitcnt lgkmcnt(6)\n" \
"v_smfmac_f32_16x16x64_bf16 v[60:63], v[0:3], a[0:7], v200\n" \
"v_smfmac_f32_16x16x64_bf16 v[64:67], v[0:3], a[56:63], v200\n" \
"v_smfmac_f32_16x16x64_bf16 v[68:71], v[0:3], a[112:119], v200\n" \
"v_smfmac_f32_16x16x64_bf16 v[72:75], v[0:3], a[168:175], v200\n" \
"s_waitcnt lgkmcnt(5)\n" \
"v_smfmac_f32_16x16x64_bf16 v[60:63], v[4:7], a[8:15], v200\n" \
"v_smfmac_f32_16x16x64_bf16 v[64:67], v[4:7], a[64:71], v200\n" \
"v_smfmac_f32_16x16x64_bf16 v[68:71], v[4:7], a[120:127], v200\n" \
"v_smfmac_f32_16x16x64_bf16 v[72:75], v[4:7], a[176:183], v200\n" \
"s_waitcnt lgkmcnt(4)\n" \
"v_smfmac_f32_16x16x64_bf16 v[60:63], v[8:11], a[16:23], v200\n" \
"v_smfmac_f32_16x16x64_bf16 v[64:67], v[8:11], a[72:79], v200\n" \
"v_smfmac_f32_16x16x64_bf16 v[68:71], v[8:11], a[128:135], v200\n" \
"v_smfmac_f32_16x16x64_bf16 v[72:75], v[8:11], a[184:191], v200\n" \
"s_waitcnt lgkmcnt(3)\n" \
"v_smfmac_f32_16x16x64_bf16 v[60:63], v[12:15], a[24:31], v200\n" \
"v_smfmac_f32_16x16x64_bf16 v[64:67], v[12:15], a[80:87], v200\n" \
"v_smfmac_f32_16x16x64_bf16 v[68:71], v[12:15], a[136:143], v200\n" \
"v_smfmac_f32_16x16x64_bf16 v[72:75], v[12:15], a[192:199], v200\n" \
"s_waitcnt lgkmcnt(2)\n" \
"v_smfmac_f32_16x16x64_bf16 v[60:63], v[16:19], a[32:39], v200\n" \
"v_smfmac_f32_16x16x64_bf16 v[64:67], v[16:19], a[88:95], v200\n" \
"v_smfmac_f32_16x16x64_bf16 v[68:71], v[16:19], a[144:151], v200\n" \
"v_smfmac_f32_16x16x64_bf16 v[72:75], v[16:19], a[200:207], v200\n" \
"s_waitcnt lgkmcnt(1)\n" \
"v_smfmac_f32_16x16x64_bf16 v[60:63], v[20:23], a[40:47], v200\n" \
"v_smfmac_f32_16x16x64_bf16 v[64:67], v[20:23], a[96:103], v200\n" \
"v_smfmac_f32_16x16x64_bf16 v[68:71], v[20:23], a[152:159], v200\n" \
"v_smfmac_f32_16x16x64_bf16 v[72:75], v[20:23], a[208:215], v200\n" \
"s_waitcnt lgkmcnt(0)\n" \
"v_smfmac_f32_16x16x64_bf16 v[60:63], v[24:27], a[48:55], v200\n" \
"v_smfmac_f32_16x16x64_bf16 v[64:67], v[24:27], a[104:111], v200\n" \
"v_smfmac_f32_16x16x64_bf16 v[68:71], v[24:27], a[160:167], v200\n" \
"v_smfmac_f32_16x16x64_bf16 v[72:75], v[24:27], a[216:223], v200\n" \
"s_nop 7\n" \
"v_permlane32_swap_b32 v60, v64\n" \
"v_permlane32_swap_b32 v61, v65\n" \
"v_permlane32_swap_b32 v62, v66\n" \
"v_permlane32_swap_b32 v63, v67\n" \
"v_add_f32 v60, v60, v64\n" \
"v_add_f32 v61, v61, v65\n" \
"v_add_f32 v62, v62, v66\n" \
"v_add_f32 v63, v63, v67\n" \
"v_cvt_pk_bf16_f32 v184, v60, v61\n" \
"v_cvt_pk_bf16_f32 v185, v62, v63\n" \
"ds_write_b64 v217, v[184:185] offset:0\n" \
"v_permlane32_swap_b32 v68, v72\n" \
"v_permlane32_swap_b32 v69, v73\n" \
"v_permlane32_swap_b32 v70, v74\n" \
"v_permlane32_swap_b32 v71, v75\n" \
"v_add_f32 v68, v68, v72\n" \
"v_add_f32 v69, v69, v73\n" \
"v_add_f32 v70, v70, v74\n" \
"v_add_f32 v71, v71, v75\n" \
"v_cvt_pk_bf16_f32 v186, v68, v69\n" \
"v_cvt_pk_bf16_f32 v187, v70, v71\n" \
"ds_write_b64 v217, v[186:187] offset:512\n" \
"ds_read_b64_tr_b16 v[0:1], v218 offset:0\n" \
"ds_read_b64_tr_b16 v[2:3], v218 offset:64\n" \
"s_add_u32 s43, s40, 1\n" \
"s_mul_i32 s43, s43, 114688\n" \
"s_waitcnt lgkmcnt(0)\n" \
"buffer_store_dwordx4 v[0:3], v219, s[16:19], s43 offen sc1\n" \
"s_waitcnt vmcnt(0)\n" \
"s_barrier\n" \
".LX0_end:\n" \
"s_endpgm\n" \
".p2align 6\n" \
"fk_role_X1:\n" \
"ds_read_b32 v200, v255 offset:0\n" \
"ds_read_b32 v201, v255 offset:256\n" \
"ds_read_b32 v202, v255 offset:512\n" \
"ds_read_b32 v203, v255 offset:768\n" \
"ds_read_b32 v204, v255 offset:1024\n" \
"ds_read_b32 v205, v255 offset:1280\n" \
"ds_read_b32 v206, v255 offset:1536\n" \
"ds_read_b32 v207, v255 offset:1792\n" \
"ds_read_b32 v208, v255 offset:2048\n" \
"ds_read_b32 v209, v255 offset:2304\n" \
"ds_read_b32 v210, v255 offset:2560\n" \
"ds_read_b32 v211, v255 offset:2816\n" \
"ds_read_b32 v212, v255 offset:3072\n" \
"ds_read_b32 v213, v255 offset:3328\n" \
"ds_read_b32 v214, v255 offset:3584\n" \
"ds_read_b32 v215, v255 offset:3840\n" \
"ds_read_b32 v216, v255 offset:4096\n" \
"ds_read_b32 v217, v255 offset:4352\n" \
"ds_read_b32 v218, v255 offset:4608\n" \
"ds_read_b32 v219, v255 offset:4864\n" \
"ds_read_b32 v220, v255 offset:5120\n" \
"ds_read_b32 v221, v255 offset:5376\n" \
"ds_read_b32 v254, v255 offset:7936\n" \
"s_waitcnt lgkmcnt(0)\n" \
"v_readlane_b32 s4, v254, 0\n" \
"v_readlane_b32 s5, v254, 1\n" \
"v_readlane_b32 s6, v254, 2\n" \
"v_readlane_b32 s7, v254, 3\n" \
"v_readlane_b32 s8, v254, 4\n" \
"v_readlane_b32 s9, v254, 5\n" \
"v_readlane_b32 s10, v254, 6\n" \
"v_readlane_b32 s11, v254, 7\n" \
"v_readlane_b32 s12, v254, 8\n" \
"v_readlane_b32 s13, v254, 9\n" \
"v_readlane_b32 s14, v254, 10\n" \
"v_readlane_b32 s15, v254, 11\n" \
"v_readlane_b32 s16, v254, 12\n" \
"v_readlane_b32 s17, v254, 13\n" \
"v_readlane_b32 s18, v254, 14\n" \
"v_readlane_b32 s19, v254, 15\n" \
"v_readlane_b32 s20, v254, 16\n" \
"v_readlane_b32 s21, v254, 17\n" \
"v_readlane_b32 s22, v254, 18\n" \
"v_readlane_b32 s23, v254, 19\n" \
"v_readlane_b32 s24, v254, 20\n" \
"v_readlane_b32 s25, v254, 21\n" \
"v_readlane_b32 s26, v254, 22\n" \
"v_readlane_b32 s27, v254, 23\n" \
"v_readlane_b32 s28, v254, 24\n" \
"v_readlane_b32 s29, v254, 25\n" \
"v_readlane_b32 s30, v254, 26\n" \
"v_readlane_b32 s31, v254, 27\n" \
"v_readlane_b32 s32, v254, 28\n" \
"v_readlane_b32 s33, v254, 29\n" \
"v_readlane_b32 s34, v254, 30\n" \
"v_readlane_b32 s35, v254, 31\n" \
"v_readlane_b32 s36, v254, 32\n" \
"v_readlane_b32 s37, v254, 33\n" \
"v_readlane_b32 s38, v254, 34\n" \
"v_readlane_b32 s39, v254, 35\n" \
"s_mov_b32 s40, 0\n" \
"s_mov_b32 s51, 0\n" \
"s_nop 0\n" \
"v_mov_b32 v252, s38\n" \
"s_nop 4\n" \
"v_mov_b32 v100, -1\n" \
"v_mov_b32 v101, -1\n" \
"v_mov_b32 v102, -1\n" \
"v_mov_b32 v103, -1\n" \
"s_mov_b32 s35, -1\n" \
"s_mov_b32 s2, 0\n" \
"s_mov_b32 s3, -1\n" \
"s_add_u32 s43, s30, 1\n" \
"s_mov_b32 s50, 0\n" \
".LX1_w71l:\n" \
"buffer_load_dword v240, v204, s[4:7], 0 offen sc1\n" \
"s_waitcnt vmcnt(0)\n" \
".LX1_w71c:\n" \
"v_subrev_u32 v241, s43, v240\n" \
"v_cmp_gt_u32 vcc, 0x100, v241\n" \
"s_cmp_eq_u64 vcc, -1\n" \
"s_cbranch_scc1 .LX1_w71d\n" \
"s_sleep 2\n" \
"s_add_u32 s50, s50, 1\n" \
"s_cmp_lt_u32 s50, 0x20000\n" \
"s_cbranch_scc1 .LX1_w71l\n" \
"s_branch .LX1_end\n" \
".LX1_w71d:\n" \
"v_readlane_b32 s43, v202, 0\n" \
"s_nop 4\n" \
"buffer_load_dwordx4 a[0:3], v201, s[8:11], s43 offen sc1\n" \
"buffer_load_dwordx4 a[4:7], v201, s[8:11], s43 offen offset:1024 sc1\n" \
"v_readlane_b32 s43, v202, 7\n" \
"s_nop 4\n" \
"buffer_load_dwordx4 a[56:59], v201, s[8:11], s43 offen sc1\n" \
"buffer_load_dwordx4 a[60:63], v201, s[8:11], s43 offen offset:1024 sc1\n" \
"v_readlane_b32 s43, v202, 14\n" \
"s_nop 4\n" \
"buffer_load_dwordx4 a[112:115], v201, s[8:11], s43 offen sc1\n" \
"buffer_load_dwordx4 a[116:119], v201, s[8:11], s43 offen offset:1024 sc1\n" \
"v_readlane_b32 s43, v202, 21\n" \
"s_nop 4\n" \
"buffer_load_dwordx4 a[168:171], v201, s[8:11], s43 offen sc1\n" \
"buffer_load_dwordx4 a[172:175], v201, s[8:11], s43 offen offset:1024 sc1\n" \
"s_add_u32 s43, s30, 2\n" \
"s_mov_b32 s50, 0\n" \
".LX1_w72l:\n" \
"buffer_load_dword v240, v204, s[4:7], 0 offen sc1\n" \
"s_waitcnt vmcnt(0)\n" \
".LX1_w72c:\n" \
"v_subrev_u32 v241, s43, v240\n" \
"v_cmp_gt_u32 vcc, 0x100, v241\n" \
"s_cmp_eq_u64 vcc, -1\n" \
"s_cbranch_scc1 .LX1_w72d\n" \
"s_sleep 2\n" \
"s_add_u32 s50, s50, 1\n" \
"s_cmp_lt_u32 s50, 0x20000\n" \
"s_cbranch_scc1 .LX1_w72l\n" \
"s_branch .LX1_end\n" \
".LX1_w72d:\n" \
"v_readlane_b32 s43, v202, 1\n" \
"s_nop 4\n" \
"buffer_load_dwordx4 a[8:11], v201, s[8:11], s43 offen sc1\n" \
"buffer_load_dwordx4 a[12:15], v201, s[8:11], s43 offen offset:1024 sc1\n" \
"v_readlane_b32 s43, v202, 8\n" \
"s_nop 4\n" \
"buffer_load_dwordx4 a[64:67], v201, s[8:11], s43 offen sc1\n" \
"buffer_load_dwordx4 a[68:71], v201, s[8:11], s43 offen offset:1024 sc1\n" \
"v_readlane_b32 s43, v202, 15\n" \
"s_nop 4\n" \
"buffer_load_dwordx4 a[120:123], v201, s[8:11], s43 offen sc1\n" \
"buffer_load_dwordx4 a[124:127], v201, s[8:11], s43 offen offset:1024 sc1\n" \
"v_readlane_b32 s43, v202, 22\n" \
"s_nop 4\n" \
"buffer_load_dwordx4 a[176:179], v201, s[8:11], s43 offen sc1\n" \
"buffer_load_dwordx4 a[180:183], v201, s[8:11], s43 offen offset:1024 sc1\n" \
"s_add_u32 s43, s30, 3\n" \
"s_mov_b32 s50, 0\n" \
".LX1_w73l:\n" \
"buffer_load_dword v240, v204, s[4:7], 0 offen sc1\n" \
"s_waitcnt vmcnt(0)\n" \
".LX1_w73c:\n" \
"v_subrev_u32 v241, s43, v240\n" \
"v_cmp_gt_u32 vcc, 0x100, v241\n" \
"s_cmp_eq_u64 vcc, -1\n" \
"s_cbranch_scc1 .LX1_w73d\n" \
"s_sleep 2\n" \
"s_add_u32 s50, s50, 1\n" \
"s_cmp_lt_u32 s50, 0x20000\n" \
"s_cbranch_scc1 .LX1_w73l\n" \
"s_branch .LX1_end\n" \
".LX1_w73d:\n" \
"v_readlane_b32 s43, v202, 2\n" \
"s_nop 4\n" \
"buffer_load_dwordx4 a[16:19], v201, s[8:11], s43 offen sc1\n" \
"buffer_load_dwordx4 a[20:23], v201, s[8:11], s43 offen offset:1024 sc1\n" \
"v_readlane_b32 s43, v202, 9\n" \
"s_nop 4\n" \
"buffer_load_dwordx4 a[72:75], v201, s[8:11], s43 offen sc1\n" \
"buffer_load_dwordx4 a[76:79], v201, s[8:11], s43 offen offset:1024 sc1\n" \
"v_readlane_b32 s43, v202, 16\n" \
"s_nop 4\n" \
"buffer_load_dwordx4 a[128:131], v201, s[8:11], s43 offen sc1\n" \
"buffer_load_dwordx4 a[132:135], v201, s[8:11], s43 offen offset:1024 sc1\n" \
"v_readlane_b32 s43, v202, 23\n" \
"s_nop 4\n" \
"buffer_load_dwordx4 a[184:187], v201, s[8:11], s43 offen sc1\n" \
"buffer_load_dwordx4 a[188:191], v201, s[8:11], s43 offen offset:1024 sc1\n" \
"s_add_u32 s43, s30, 4\n" \
"s_mov_b32 s50, 0\n" \
".LX1_w74l:\n" \
"buffer_load_dword v240, v204, s[4:7], 0 offen sc1\n" \
"s_waitcnt vmcnt(0)\n" \
".LX1_w74c:\n" \
"v_subrev_u32 v241, s43, v240\n" \
"v_cmp_gt_u32 vcc, 0x100, v241\n" \
"s_cmp_eq_u64 vcc, -1\n" \
"s_cbranch_scc1 .LX1_w74d\n" \
"s_sleep 2\n" \
"s_add_u32 s50, s50, 1\n" \
"s_cmp_lt_u32 s50, 0x20000\n" \
"s_cbranch_scc1 .LX1_w74l\n" \
"s_branch .LX1_end\n" \
".LX1_w74d:\n" \
"v_readlane_b32 s43, v202, 3\n" \
"s_nop 4\n" \
"buffer_load_dwordx4 a[24:27], v201, s[8:11], s43 offen sc1\n" \
"buffer_load_dwordx4 a[28:31], v201, s[8:11], s43 offen offset:1024 sc1\n" \
"v_readlane_b32 s43, v202, 10\n" \
"s_nop 4\n" \
"buffer_load_dwordx4 a[80:83], v201, s[8:11], s43 offen sc1\n" \
"buffer_load_dwordx4 a[84:87], v201, s[8:11], s43 offen offset:1024 sc1\n" \
"v_readlane_b32 s43, v202, 17\n" \
"s_nop 4\n" \
"buffer_load_dwordx4 a[136:139], v201, s[8:11], s43 offen sc1\n" \
"buffer_load_dwordx4 a[140:143], v201, s[8:11], s43 offen offset:1024 sc1\n" \
"v_readlane_b32 s43, v202, 24\n" \
"s_nop 4\n" \
"buffer_load_dwordx4 a[192:195], v201, s[8:11], s43 offen sc1\n" \
"buffer_load_dwordx4 a[196:199], v201, s[8:11], s43 offen offset:1024 sc1\n" \
"s_add_u32 s43, s30, 5\n" \
"s_mov_b32 s50, 0\n" \
".LX1_w75l:\n" \
"buffer_load_dword v240, v204, s[4:7], 0 offen sc1\n" \
"s_waitcnt vmcnt(0)\n" \
".LX1_w75c:\n" \
"v_subrev_u32 v241, s43, v240\n" \
"v_cmp_gt_u32 vcc, 0x100, v241\n" \
"s_cmp_eq_u64 vcc, -1\n" \
"s_cbranch_scc1 .LX1_w75d\n" \
"s_sleep 2\n" \
"s_add_u32 s50, s50, 1\n" \
"s_cmp_lt_u32 s50, 0x20000\n" \
"s_cbranch_scc1 .LX1_w75l\n" \
"s_branch .LX1_end\n" \
".LX1_w75d:\n" \
"v_readlane_b32 s43, v202, 4\n" \
"s_nop 4\n" \
"buffer_load_dwordx4 a[32:35], v201, s[8:11], s43 offen sc1\n" \
"buffer_load_dwordx4 a[36:39], v201, s[8:11], s43 offen offset:1024 sc1\n" \
"v_readlane_b32 s43, v202, 11\n" \
"s_nop 4\n" \
"buffer_load_dwordx4 a[88:91], v201, s[8:11], s43 offen sc1\n" \
"buffer_load_dwordx4 a[92:95], v201, s[8:11], s43 offen offset:1024 sc1\n" \
"v_readlane_b32 s43, v202, 18\n" \
"s_nop 4\n" \
"buffer_load_dwordx4 a[144:147], v201, s[8:11], s43 offen sc1\n" \
"buffer_load_dwordx4 a[148:151], v201, s[8:11], s43 offen offset:1024 sc1\n" \
"v_readlane_b32 s43, v202, 25\n" \
"s_nop 4\n" \
"buffer_load_dwordx4 a[200:203], v201, s[8:11], s43 offen sc1\n" \
"buffer_load_dwordx4 a[204:207], v201, s[8:11], s43 offen offset:1024 sc1\n" \
"s_add_u32 s43, s30, 6\n" \
"s_mov_b32 s50, 0\n" \
".LX1_w76l:\n" \
"buffer_load_dword v240, v204, s[4:7], 0 offen sc1\n" \
"s_waitcnt vmcnt(0)\n" \
".LX1_w76c:\n" \
"v_subrev_u32 v241, s43, v240\n" \
"v_cmp_gt_u32 vcc, 0x100, v241\n" \
"s_cmp_eq_u64 vcc, -1\n" \
"s_cbranch_scc1 .LX1_w76d\n" \
"s_sleep 2\n" \
"s_add_u32 s50, s50, 1\n" \
"s_cmp_lt_u32 s50, 0x20000\n" \
"s_cbranch_scc1 .LX1_w76l\n" \
"s_branch .LX1_end\n" \
".LX1_w76d:\n" \
"v_readlane_b32 s43, v202, 5\n" \
"s_nop 4\n" \
"buffer_load_dwordx4 a[40:43], v201, s[8:11], s43 offen sc1\n" \
"buffer_load_dwordx4 a[44:47], v201, s[8:11], s43 offen offset:1024 sc1\n" \
"v_readlane_b32 s43, v202, 12\n" \
"s_nop 4\n" \
"buffer_load_dwordx4 a[96:99], v201, s[8:11], s43 offen sc1\n" \
"buffer_load_dwordx4 a[100:103], v201, s[8:11], s43 offen offset:1024 sc1\n" \
"v_readlane_b32 s43, v202, 19\n" \
"s_nop 4\n" \
"buffer_load_dwordx4 a[152:155], v201, s[8:11], s43 offen sc1\n" \
"buffer_load_dwordx4 a[156:159], v201, s[8:11], s43 offen offset:1024 sc1\n" \
"v_readlane_b32 s43, v202, 26\n" \
"s_nop 4\n" \
"buffer_load_dwordx4 a[208:211], v201, s[8:11], s43 offen sc1\n" \
"buffer_load_dwordx4 a[212:215], v201, s[8:11], s43 offen offset:1024 sc1\n" \
"s_add_u32 s43, s30, 7\n" \
"s_mov_b32 s50, 0\n" \
".LX1_w77l:\n" \
"buffer_load_dword v240, v204, s[4:7], 0 offen sc1\n" \
"s_waitcnt vmcnt(0)\n" \
".LX1_w77c:\n" \
"v_subrev_u32 v241, s43, v240\n" \
"v_cmp_gt_u32 vcc, 0x100, v241\n" \
"s_cmp_eq_u64 vcc, -1\n" \
"s_cbranch_scc1 .LX1_w77d\n" \
"s_sleep 2\n" \
"s_add_u32 s50, s50, 1\n" \
"s_cmp_lt_u32 s50, 0x20000\n" \
"s_cbranch_scc1 .LX1_w77l\n" \
"s_branch .LX1_end\n" \
".LX1_w77d:\n" \
"v_readlane_b32 s43, v202, 6\n" \
"s_nop 4\n" \
"buffer_load_dwordx4 a[48:51], v201, s[8:11], s43 offen sc1\n" \
"buffer_load_dwordx4 a[52:55], v201, s[8:11], s43 offen offset:1024 sc1\n" \
"v_readlane_b32 s43, v202, 13\n" \
"s_nop 4\n" \
"buffer_load_dwordx4 a[104:107], v201, s[8:11], s43 offen sc1\n" \
"buffer_load_dwordx4 a[108:111], v201, s[8:11], s43 offen offset:1024 sc1\n" \
"v_readlane_b32 s43, v202, 20\n" \
"s_nop 4\n" \
"buffer_load_dwordx4 a[160:163], v201, s[8:11], s43 offen sc1\n" \
"buffer_load_dwordx4 a[164:167], v201, s[8:11], s43 offen offset:1024 sc1\n" \
"v_readlane_b32 s43, v202, 27\n" \
"s_nop 4\n" \
"buffer_load_dwordx4 a[216:219], v201, s[8:11], s43 offen sc1\n" \
"buffer_load_dwordx4 a[220:223], v201, s[8:11], s43 offen offset:1024 sc1\n" \
"s_add_u32 s43, s30, 9\n" \
"s_mov_b32 s50, 0\n" \
".LX1_w78l:\n" \
"buffer_load_dword v240, v215, s[4:7], 0 offen sc1\n" \
"s_waitcnt vmcnt(0)\n" \
".LX1_w78c:\n" \
"v_subrev_u32 v241, s43, v240\n" \
"v_cmp_gt_u32 vcc, 0x100, v241\n" \
"s_cmp_eq_u64 vcc, -1\n" \
"s_cbranch_scc1 .LX1_w78d\n" \
"s_sleep 2\n" \
"s_add_u32 s50, s50, 1\n" \
"s_cmp_lt_u32 s50, 0x20000\n" \
"s_cbranch_scc1 .LX1_w78l\n" \
"s_branch .LX1_end\n" \
".LX1_w78d:\n" \
"s_add_u32 s50, s40, 0\n" \
"s_mul_i32 s50, s50, 458752\n" \
"s_add_u32 s50, s50, s34\n" \
"buffer_load_dwordx4 v[28:31], v201, s[12:15], s50 offen sc1\n" \
"buffer_load_dwordx4 v[32:35], v201, s[12:15], s50 offen offset:1024 sc1\n" \
"buffer_load_dwordx4 v[36:39], v201, s[12:15], s50 offen offset:2048 sc1\n" \
"buffer_load_dwordx4 v[40:43], v201, s[12:15], s50 offen offset:3072 sc1\n" \
"s_add_u32 s43, s30, 1\n" \
"s_mov_b32 s50, 0\n" \
".LX1_w79l:\n" \
"buffer_load_dword v240, v205, s[4:7], 0 offen sc1\n" \
"s_waitcnt vmcnt(0)\n" \
".LX1_w79c:\n" \
"v_subrev_u32 v241, s43, v240\n" \
"v_cmp_gt_u32 vcc, 0x100, v241\n" \
"s_cmp_eq_u64 vcc, -1\n" \
"s_cbranch_scc1 .LX1_w79d\n" \
"s_sleep 2\n" \
"s_add_u32 s50, s50, 1\n" \
"s_cmp_lt_u32 s50, 0x20000\n" \
"s_cbranch_scc1 .LX1_w79l\n" \
"s_branch .LX1_end\n" \
".LX1_w79d:\n" \
"s_add_u32 s43, s30, 1\n" \
"s_mov_b32 s50, 0\n" \
".LX1_w80l:\n" \
"buffer_load_dword v240, v206, s[4:7], 0 offen sc1\n" \
"s_waitcnt vmcnt(0)\n" \
".LX1_w80c:\n" \
"v_subrev_u32 v241, s43, v240\n" \
"v_cmp_gt_u32 vcc, 0x100, v241\n" \
"s_cmp_eq_u64 vcc, -1\n" \
"s_cbranch_scc1 .LX1_w80d\n" \
"s_sleep 2\n" \
"s_add_u32 s50, s50, 1\n" \
"s_cmp_lt_u32 s50, 0x20000\n" \
"s_cbranch_scc1 .LX1_w80l\n" \
"s_branch .LX1_end\n" \
".LX1_w80d:\n" \
"s_barrier\n" \
".LX1_loop:\n" \
"s_add_u32 s43, s40, 2\n" \
"s_and_b32 s43, s43, 3\n" \
"s_bitcmp1_b32 s43, 0\n" \
"s_cselect_b32 s49, s32, s31\n" \
"s_lshl_b32 s43, s43, 19\n" \
"s_add_u32 s49, s49, s43\n" \
"s_add_u32 s43, s30, s40\n" \
"s_add_u32 s43, s43, -1\n" \
"v_mov_b32 v241, s43\n" \
"buffer_store_dword v241, v220, s[4:7], 0 offen sc1\n" \
"ds_read_b128 v[0:3], v216 offset:0\n" \
"ds_read_b128 v[4:7], v216 offset:1024\n" \
"ds_read_b128 v[8:11], v216 offset:2048\n" \
"ds_read_b128 v[12:15], v216 offset:3072\n" \
"ds_read_b128 v[16:19], v216 offset:4096\n" \
"ds_read_b128 v[20:23], v216 offset:5120\n" \
"ds_read_b128 v[24:27], v216 offset:6144\n" \
"v_mov_b32 v60, 0\n" \
"v_mov_b32 v61, 0\n" \
"v_mov_b32 v62, 0\n" \
"v_mov_b32 v63, 0\n" \
"v_mov_b32 v64, 0\n" \
"v_mov_b32 v65, 0\n" \
"v_mov_b32 v66, 0\n" \
"v_mov_b32 v67, 0\n" \
"v_mov_b32 v68, 0\n" \
"v_mov_b32 v69, 0\n" \
"v_mov_b32 v70, 0\n" \
"v_mov_b32 v71, 0\n" \
"v_mov_b32 v72, 0\n" \
"v_mov_b32 v73, 0\n" \
"v_mov_b32 v74, 0\n" \
"v_mov_b32 v75, 0\n" \
"s_nop 1\n" \
"s_waitcnt lgkmcnt(6)\n" \
"v_smfmac_f32_16x16x64_bf16 v[60:63], v[0:3], a[0:7], v200\n" \
"v_smfmac_f32_16x16x64_bf16 v[64:67], v[0:3], a[56:63], v200\n" \
"v_smfmac_f32_16x16x64_bf16 v[68:71], v[0:3], a[112:119], v200\n" \
"v_smfmac_f32_16x16x64_bf16 v[72:75], v[0:3], a[168:175], v200\n" \
"s_waitcnt lgkmcnt(5)\n" \
"v_smfmac_f32_16x16x64_bf16 v[60:63], v[4:7], a[8:15], v200\n" \
"v_smfmac_f32_16x16x64_bf16 v[64:67], v[4:7], a[64:71], v200\n" \
"v_smfmac_f32_16x16x64_bf16 v[68:71], v[4:7], a[120:127], v200\n" \
"v_smfmac_f32_16x16x64_bf16 v[72:75], v[4:7], a[176:183], v200\n" \
"s_waitcnt lgkmcnt(4)\n" \
"v_smfmac_f32_16x16x64_bf16 v[60:63], v[8:11], a[16:23], v200\n" \
"v_smfmac_f32_16x16x64_bf16 v[64:67], v[8:11], a[72:79], v200\n" \
"v_smfmac_f32_16x16x64_bf16 v[68:71], v[8:11], a[128:135], v200\n" \
"v_smfmac_f32_16x16x64_bf16 v[72:75], v[8:11], a[184:191], v200\n" \
"s_waitcnt lgkmcnt(3)\n" \
"v_smfmac_f32_16x16x64_bf16 v[60:63], v[12:15], a[24:31], v200\n" \
"v_smfmac_f32_16x16x64_bf16 v[64:67], v[12:15], a[80:87], v200\n" \
"v_smfmac_f32_16x16x64_bf16 v[68:71], v[12:15], a[136:143], v200\n" \
"v_smfmac_f32_16x16x64_bf16 v[72:75], v[12:15], a[192:199], v200\n" \
"s_waitcnt lgkmcnt(2)\n" \
"v_smfmac_f32_16x16x64_bf16 v[60:63], v[16:19], a[32:39], v200\n" \
"v_smfmac_f32_16x16x64_bf16 v[64:67], v[16:19], a[88:95], v200\n" \
"v_smfmac_f32_16x16x64_bf16 v[68:71], v[16:19], a[144:151], v200\n" \
"v_smfmac_f32_16x16x64_bf16 v[72:75], v[16:19], a[200:207], v200\n" \
"s_waitcnt lgkmcnt(1)\n" \
"v_smfmac_f32_16x16x64_bf16 v[60:63], v[20:23], a[40:47], v200\n" \
"v_smfmac_f32_16x16x64_bf16 v[64:67], v[20:23], a[96:103], v200\n" \
"v_smfmac_f32_16x16x64_bf16 v[68:71], v[20:23], a[152:159], v200\n" \
"v_smfmac_f32_16x16x64_bf16 v[72:75], v[20:23], a[208:215], v200\n" \
"s_waitcnt lgkmcnt(0)\n" \
"v_smfmac_f32_16x16x64_bf16 v[60:63], v[24:27], a[48:55], v200\n" \
"v_smfmac_f32_16x16x64_bf16 v[64:67], v[24:27], a[104:111], v200\n" \
"v_smfmac_f32_16x16x64_bf16 v[68:71], v[24:27], a[160:167], v200\n" \
"v_smfmac_f32_16x16x64_bf16 v[72:75], v[24:27], a[216:223], v200\n" \
"s_barrier\n" \
"s_add_u32 s50, s40, 1\n" \
"s_mul_i32 s50, s50, 458752\n" \
"s_add_u32 s50, s50, s34\n" \
"buffer_load_dwordx4 v[44:47], v201, s[12:15], s50 offen sc1\n" \
"buffer_load_dwordx4 v[48:51], v201, s[12:15], s50 offen offset:1024 sc1\n" \
"buffer_load_dwordx4 v[52:55], v201, s[12:15], s50 offen offset:2048 sc1\n" \
"buffer_load_dwordx4 v[56:59], v201, s[12:15], s50 offen offset:3072 sc1\n" \
"s_nop 7\n" \
"v_permlane32_swap_b32 v60, v64\n" \
"v_permlane32_swap_b32 v61, v65\n" \
"v_permlane32_swap_b32 v62, v66\n" \
"v_permlane32_swap_b32 v63, v67\n" \
"v_add_f32 v60, v60, v64\n" \
"v_add_f32 v61, v61, v65\n" \
"v_add_f32 v62, v62, v66\n" \
"v_add_f32 v63, v63, v67\n" \
"v_cvt_pk_bf16_f32 v184, v60, v61\n" \
"v_cvt_pk_bf16_f32 v185, v62, v63\n" \
"ds_write_b64 v217, v[184:185] offset:0\n" \
"v_permlane32_swap_b32 v68, v72\n" \
"v_permlane32_swap_b32 v69, v73\n" \
"v_permlane32_swap_b32 v70, v74\n" \
"v_permlane32_swap_b32 v71, v75\n" \
"v_add_f32 v68, v68, v72\n" \
"v_add_f32 v69, v69, v73\n" \
"v_add_f32 v70, v70, v74\n" \
"v_add_f32 v71, v71, v75\n" \
"v_cvt_pk_bf16_f32 v186, v68, v69\n" \
"v_cvt_pk_bf16_f32 v187, v70, v71\n" \
"ds_write_b64 v217, v[186:187] offset:512\n" \
"ds_read_b64_tr_b16 v[0:1], v218 offset:0\n" \
"ds_read_b64_tr_b16 v[2:3], v218 offset:64\n" \
"s_add_u32 s43, s40, 0\n" \
"s_mul_i32 s43, s43, 114688\n" \
"s_waitcnt lgkmcnt(0)\n" \
"buffer_store_dwordx4 v[0:3], v219, s[16:19], s43 offen sc1\n" \
"s_waitcnt vmcnt(5)\n" \
"s_barrier\n" \
"ds_read_b64_tr_b16 v[92:93], v213\n" \
"ds_read_b64_tr_b16 v[94:95], v213 offset:64\n" \
"ds_read_b32 v84, v252 offset:16\n" \
"v_mov_b32 v245, v245\n" \
"s_nop 2\n" \
"v_mov_b32 v245, v245\n" \
"s_nop 2\n" \
"v_mov_b32 v245, v245\n" \
"s_waitcnt lgkmcnt(1)\n" \
"v_mfma_f32_16x16x32_bf16 v[68:71], v[92:95], v[28:31], 0\n" \
"s_waitcnt lgkmcnt(0)\n" \
"v_xor_b32 v85, s29, v84\n" \
"v_mfma_f32_16x16x32_bf16 v[76:79], v[92:95], v[36:39], 0\n" \
"v_add_u32 v84, v84, v210\n" \
"v_add_u32 v85, v85, v210\n" \
"v_mfma_f32_16x16x32_bf16 v[72:75], v[92:95], v[32:35], 0\n" \
"s_nop 1\n" \
"v_mfma_f32_16x16x32_bf16 v[80:83], v[92:95], v[40:43], 0\n" \
"v_cvt_pk_bf16_f32 v96, v68, v69\n" \
"v_cvt_pk_bf16_f32 v97, v70, v71\n" \
"v_cvt_pk_bf16_f32 v98, v76, v77\n" \
"v_cvt_pk_bf16_f32 v99, v78, v79\n" \
"s_mov_b64 exec, s[2:3]\n" \
"v_cvt_pk_bf16_f32 v96, v72, v73\n" \
"v_cvt_pk_bf16_f32 v97, v74, v75\n" \
"s_nop 0\n" \
"v_cvt_pk_bf16_f32 v98, v80, v81\n" \
"v_cvt_pk_bf16_f32 v99, v82, v83\n" \
"s_mov_b64 exec, -1\n" \
"s_nop 1\n" \
"v_permlane16_swap_b32 v96, v98\n" \
"v_permlane16_swap_b32 v97, v99\n" \
"buffer_store_dwordx4 v[96:99], v84, s[4:7], 0 offen sc1\n" \
"s_barrier\n" \
"buffer_store_dwordx4 v[96:99], v85, s[4:7], 0 offen sc1\n" \
"s_nop 15\n" \
"s_nop 15\n" \
"s_nop 15\n" \
"s_nop 15\n" \
"s_nop 15\n" \
"buffer_store_dwordx4 v[100:103], v214, s[4:7], s49 offen sc1\n" \
"s_xor_b32 s43, s49, s29\n" \
"buffer_store_dwordx4 v[100:103], v214, s[4:7], s43 offen sc1\n" \
"s_waitcnt vmcnt(4)\n" \
"v_mov_b32 v28, v44\n" \
"v_mov_b32 v29, v45\n" \
"v_mov_b32 v30, v46\n" \
"v_mov_b32 v31, v47\n" \
"v_mov_b32 v32, v48\n" \
"v_mov_b32 v33, v49\n" \
"v_mov_b32 v34, v50\n" \
"v_mov_b32 v35, v51\n" \
"v_mov_b32 v36, v52\n" \
"v_mov_b32 v37, v53\n" \
"v_mov_b32 v38, v54\n" \
"v_mov_b32 v39, v55\n" \
"v_mov_b32 v40, v56\n" \
"v_mov_b32 v41, v57\n" \
"v_mov_b32 v42, v58\n" \
"v_mov_b32 v43, v59\n" \
"s_add_u32 s40, s40, 1\n" \
"s_waitcnt lgkmcnt(0)\n" \
"s_barrier\n" \
"s_cmp_lt_u32 s40, 63\n" \
"s_cbranch_scc1 .LX1_loop\n" \
"s_add_u32 s43, s30, s40\n" \
"s_add_u32 s43, s43, -1\n" \
"v_mov_b32 v241, s43\n" \
"buffer_store_dword v241, v220, s[4:7], 0 offen sc1\n" \
"ds_read_b128 v[0:3], v216 offset:0\n" \
"ds_read_b128 v[4:7], v216 offset:1024\n" \
"ds_read_b128 v[8:11], v216 offset:2048\n" \
"ds_read_b128 v[12:15], v216 offset:3072\n" \
"ds_read_b128 v[16:19], v216 offset:4096\n" \
"ds_read_b128 v[20:23], v216 offset:5120\n" \
"ds_read_b128 v[24:27], v216 offset:6144\n" \
"v_mov_b32 v60, 0\n" \
"v_mov_b32 v61, 0\n" \
"v_mov_b32 v62, 0\n" \
"v_mov_b32 v63, 0\n" \
"v_mov_b32 v64, 0\n" \
"v_mov_b32 v65, 0\n" \
"v_mov_b32 v66, 0\n" \
"v_mov_b32 v67, 0\n" \
"v_mov_b32 v68, 0\n" \
"v_mov_b32 v69, 0\n" \
"v_mov_b32 v70, 0\n" \
"v_mov_b32 v71, 0\n" \
"v_mov_b32 v72, 0\n" \
"v_mov_b32 v73, 0\n" \
"v_mov_b32 v74, 0\n" \
"v_mov_b32 v75, 0\n" \
"s_nop 1\n" \
"s_waitcnt lgkmcnt(6)\n" \
"v_smfmac_f32_16x16x64_bf16 v[60:63], v[0:3], a[0:7], v200\n" \
"v_smfmac_f32_16x16x64_bf16 v[64:67], v[0:3], a[56:63], v200\n" \
"v_smfmac_f32_16x16x64_bf16 v[68:71], v[0:3], a[112:119], v200\n" \
"v_smfmac_f32_16x16x64_bf16 v[72:75], v[0:3], a[168:175], v200\n" \
"s_waitcnt lgkmcnt(5)\n" \
"v_smfmac_f32_16x16x64_bf16 v[60:63], v[4:7], a[8:15], v200\n" \
"v_smfmac_f32_16x16x64_bf16 v[64:67], v[4:7], a[64:71], v200\n" \
"v_smfmac_f32_16x16x64_bf16 v[68:71], v[4:7], a[120:127], v200\n" \
"v_smfmac_f32_16x16x64_bf16 v[72:75], v[4:7], a[176:183], v200\n" \
"s_waitcnt lgkmcnt(4)\n" \
"v_smfmac_f32_16x16x64_bf16 v[60:63], v[8:11], a[16:23], v200\n" \
"v_smfmac_f32_16x16x64_bf16 v[64:67], v[8:11], a[72:79], v200\n" \
"v_smfmac_f32_16x16x64_bf16 v[68:71], v[8:11], a[128:135], v200\n" \
"v_smfmac_f32_16x16x64_bf16 v[72:75], v[8:11], a[184:191], v200\n" \
"s_waitcnt lgkmcnt(3)\n" \
"v_smfmac_f32_16x16x64_bf16 v[60:63], v[12:15], a[24:31], v200\n" \
"v_smfmac_f32_16x16x64_bf16 v[64:67], v[12:15], a[80:87], v200\n" \
"v_smfmac_f32_16x16x64_bf16 v[68:71], v[12:15], a[136:143], v200\n" \
"v_smfmac_f32_16x16x64_bf16 v[72:75], v[12:15], a[192:199], v200\n" \
"s_waitcnt lgkmcnt(2)\n" \
"v_smfmac_f32_16x16x64_bf16 v[60:63], v[16:19], a[32:39], v200\n" \
"v_smfmac_f32_16x16x64_bf16 v[64:67], v[16:19], a[88:95], v200\n" \
"v_smfmac_f32_16x16x64_bf16 v[68:71], v[16:19], a[144:151], v200\n" \
"v_smfmac_f32_16x16x64_bf16 v[72:75], v[16:19], a[200:207], v200\n" \
"s_waitcnt lgkmcnt(1)\n" \
"v_smfmac_f32_16x16x64_bf16 v[60:63], v[20:23], a[40:47], v200\n" \
"v_smfmac_f32_16x16x64_bf16 v[64:67], v[20:23], a[96:103], v200\n" \
"v_smfmac_f32_16x16x64_bf16 v[68:71], v[20:23], a[152:159], v200\n" \
"v_smfmac_f32_16x16x64_bf16 v[72:75], v[20:23], a[208:215], v200\n" \
"s_waitcnt lgkmcnt(0)\n" \
"v_smfmac_f32_16x16x64_bf16 v[60:63], v[24:27], a[48:55], v200\n" \
"v_smfmac_f32_16x16x64_bf16 v[64:67], v[24:27], a[104:111], v200\n" \
"v_smfmac_f32_16x16x64_bf16 v[68:71], v[24:27], a[160:167], v200\n" \
"v_smfmac_f32_16x16x64_bf16 v[72:75], v[24:27], a[216:223], v200\n" \
"s_nop 7\n" \
"v_permlane32_swap_b32 v60, v64\n" \
"v_permlane32_swap_b32 v61, v65\n" \
"v_permlane32_swap_b32 v62, v66\n" \
"v_permlane32_swap_b32 v63, v67\n" \
"v_add_f32 v60, v60, v64\n" \
"v_add_f32 v61, v61, v65\n" \
"v_add_f32 v62, v62, v66\n" \
"v_add_f32 v63, v63, v67\n" \
"v_cvt_pk_bf16_f32 v184, v60, v61\n" \
"v_cvt_pk_bf16_f32 v185, v62, v63\n" \
"ds_write_b64 v217, v[184:185] offset:0\n" \
"v_permlane32_swap_b32 v68, v72\n" \
"v_permlane32_swap_b32 v69, v73\n" \
"v_permlane32_swap_b32 v70, v74\n" \
"v_permlane32_swap_b32 v71, v75\n" \
"v_add_f32 v68, v68, v72\n" \
"v_add_f32 v69, v69, v73\n" \
"v_add_f32 v70, v70, v74\n" \
"v_add_f32 v71, v71, v75\n" \
"v_cvt_pk_bf16_f32 v186, v68, v69\n" \
"v_cvt_pk_bf16_f32 v187, v70, v71\n" \
"ds_write_b64 v217, v[186:187] offset:512\n" \
"ds_read_b64_tr_b16 v[0:1], v218 offset:0\n" \
"ds_read_b64_tr_b16 v[2:3], v218 offset:64\n" \
"s_add_u32 s43, s40, 0\n" \
"s_mul_i32 s43, s43, 114688\n" \
"s_waitcnt lgkmcnt(0)\n" \
"buffer_store_dwordx4 v[0:3], v219, s[16:19], s43 offen sc1\n" \
"s_waitcnt vmcnt(0)\n" \
"s_barrier\n" \
"s_add_u32 s43, s30, s40\n" \
"v_mov_b32 v241, s43\n" \
"buffer_store_dword v241, v220, s[4:7], 0 offen sc1\n" \
"ds_read_b128 v[0:3], v216 offset:7168\n" \
"ds_read_b128 v[4:7], v216 offset:8192\n" \
"ds_read_b128 v[8:11], v216 offset:9216\n" \
"ds_read_b128 v[12:15], v216 offset:10240\n" \
"ds_read_b128 v[16:19], v216 offset:11264\n" \
"ds_read_b128 v[20:23], v216 offset:12288\n" \
"ds_read_b128 v[24:27], v216 offset:13312\n" \
"v_mov_b32 v60, 0\n" \
"v_mov_b32 v61, 0\n" \
"v_mov_b32 v62, 0\n" \
"v_mov_b32 v63, 0\n" \
"v_mov_b32 v64, 0\n" \
"v_mov_b32 v65, 0\n" \
"v_mov_b32 v66, 0\n" \
"v_mov_b32 v67, 0\n" \
"v_mov_b32 v68, 0\n" \
"v_mov_b32 v69, 0\n" \
"v_mov_b32 v70, 0\n" \
"v_mov_b32 v71, 0\n" \
"v_mov_b32 v72, 0\n" \
"v_mov_b32 v73, 0\n" \
"v_mov_b32 v74, 0\n" \
"v_mov_b32 v75, 0\n" \
"s_nop 1\n" \
"s_waitcnt lgkmcnt(6)\n" \
"v_smfmac_f32_16x16x64_bf16 v[60:63], v[0:3], a[0:7], v200\n" \
"v_smfmac_f32_16x16x64_bf16 v[64:67], v[0:3], a[56:63], v200\n" \
"v_smfmac_f32_16x16x64_bf16 v[68:71], v[0:3], a[112:119], v200\n" \
"v_smfmac_f32_16x16x64_bf16 v[72:75], v[0:3], a[168:175], v200\n" \
"s_waitcnt lgkmcnt(5)\n" \
"v_smfmac_f32_16x16x64_bf16 v[60:63], v[4:7], a[8:15], v200\n" \
"v_smfmac_f32_16x16x64_bf16 v[64:67], v[4:7], a[64:71], v200\n" \
"v_smfmac_f32_16x16x64_bf16 v[68:71], v[4:7], a[120:127], v200\n" \
"v_smfmac_f32_16x16x64_bf16 v[72:75], v[4:7], a[176:183], v200\n" \
"s_waitcnt lgkmcnt(4)\n" \
"v_smfmac_f32_16x16x64_bf16 v[60:63], v[8:11], a[16:23], v200\n" \
"v_smfmac_f32_16x16x64_bf16 v[64:67], v[8:11], a[72:79], v200\n" \
"v_smfmac_f32_16x16x64_bf16 v[68:71], v[8:11], a[128:135], v200\n" \
"v_smfmac_f32_16x16x64_bf16 v[72:75], v[8:11], a[184:191], v200\n" \
"s_waitcnt lgkmcnt(3)\n" \
"v_smfmac_f32_16x16x64_bf16 v[60:63], v[12:15], a[24:31], v200\n" \
"v_smfmac_f32_16x16x64_bf16 v[64:67], v[12:15], a[80:87], v200\n" \
"v_smfmac_f32_16x16x64_bf16 v[68:71], v[12:15], a[136:143], v200\n" \
"v_smfmac_f32_16x16x64_bf16 v[72:75], v[12:15], a[192:199], v200\n" \
"s_waitcnt lgkmcnt(2)\n" \
"v_smfmac_f32_16x16x64_bf16 v[60:63], v[16:19], a[32:39], v200\n" \
"v_smfmac_f32_16x16x64_bf16 v[64:67], v[16:19], a[88:95], v200\n" \
"v_smfmac_f32_16x16x64_bf16 v[68:71], v[16:19], a[144:151], v200\n" \
"v_smfmac_f32_16x16x64_bf16 v[72:75], v[16:19], a[200:207], v200\n" \
"s_waitcnt lgkmcnt(1)\n" \
"v_smfmac_f32_16x16x64_bf16 v[60:63], v[20:23], a[40:47], v200\n" \
"v_smfmac_f32_16x16x64_bf16 v[64:67], v[20:23], a[96:103], v200\n" \
"v_smfmac_f32_16x16x64_bf16 v[68:71], v[20:23], a[152:159], v200\n" \
"v_smfmac_f32_16x16x64_bf16 v[72:75], v[20:23], a[208:215], v200\n" \
"s_waitcnt lgkmcnt(0)\n" \
"v_smfmac_f32_16x16x64_bf16 v[60:63], v[24:27], a[48:55], v200\n" \
"v_smfmac_f32_16x16x64_bf16 v[64:67], v[24:27], a[104:111], v200\n" \
"v_smfmac_f32_16x16x64_bf16 v[68:71], v[24:27], a[160:167], v200\n" \
"v_smfmac_f32_16x16x64_bf16 v[72:75], v[24:27], a[216:223], v200\n" \
"s_nop 7\n" \
"v_permlane32_swap_b32 v60, v64\n" \
"v_permlane32_swap_b32 v61, v65\n" \
"v_permlane32_swap_b32 v62, v66\n" \
"v_permlane32_swap_b32 v63, v67\n" \
"v_add_f32 v60, v60, v64\n" \
"v_add_f32 v61, v61, v65\n" \
"v_add_f32 v62, v62, v66\n" \
"v_add_f32 v63, v63, v67\n" \
"v_cvt_pk_bf16_f32 v184, v60, v61\n" \
"v_cvt_pk_bf16_f32 v185, v62, v63\n" \
"ds_write_b64 v217, v[184:185] offset:0\n" \
"v_permlane32_swap_b32 v68, v72\n" \
"v_permlane32_swap_b32 v69, v73\n" \
"v_permlane32_swap_b32 v70, v74\n" \
"v_permlane32_swap_b32 v71, v75\n" \
"v_add_f32 v68, v68, v72\n" \
"v_add_f32 v69, v69, v73\n" \
"v_add_f32 v70, v70, v74\n" \
"v_add_f32 v71, v71, v75\n" \
"v_cvt_pk_bf16_f32 v186, v68, v69\n" \
"v_cvt_pk_bf16_f32 v187, v70, v71\n" \
"ds_write_b64 v217, v[186:187] offset:512\n" \
"ds_read_b64_tr_b16 v[0:1], v218 offset:0\n" \
"ds_read_b64_tr_b16 v[2:3], v218 offset:64\n" \
"s_add_u32 s43, s40, 1\n" \
"s_mul_i32 s43, s43, 114688\n" \
"s_waitcnt lgkmcnt(0)\n" \
"buffer_store_dwordx4 v[0:3], v219, s[16:19], s43 offen sc1\n" \
"s_waitcnt vmcnt(0)\n" \
"s_barrier\n" \
"s_add_u32 s43, s30, s40\n" \
"s_add_u32 s43, s43, 1\n" \
"v_mov_b32 v241, s43\n" \
"buffer_store_dword v241, v220, s[4:7], 0 offen sc1\n" \
".LX1_end:\n" \
"s_endpgm\n" \
".p2align 6\n" \
"fk_role_X2:\n" \
"ds_read_b32 v200, v255 offset:0\n" \
"ds_read_b32 v201, v255 offset:256\n" \
"ds_read_b32 v202, v255 offset:512\n" \
"ds_read_b32 v203, v255 offset:768\n" \
"ds_read_b32 v204, v255 offset:1024\n" \
"ds_read_b32 v205, v255 offset:1280\n" \
"ds_read_b32 v206, v255 offset:1536\n" \
"ds_read_b32 v207, v255 offset:1792\n" \
"ds_read_b32 v208, v255 offset:2048\n" \
"ds_read_b32 v209, v255 offset:2304\n" \
"ds_read_b32 v210, v255 offset:2560\n" \
"ds_read_b32 v211, v255 offset:2816\n" \
"ds_read_b32 v212, v255 offset:3072\n" \
"ds_read_b32 v213, v255 offset:3328\n" \
"ds_read_b32 v214, v255 offset:3584\n" \
"ds_read_b32 v215, v255 offset:3840\n" \
"ds_read_b32 v216, v255 offset:4096\n" \
"ds_read_b32 v217, v255 offset:4352\n" \
"ds_read_b32 v218, v255 offset:4608\n" \
"ds_read_b32 v219, v255 offset:4864\n" \
"ds_read_b32 v220, v255 offset:5120\n" \
"ds_read_b32 v221, v255 offset:5376\n" \
"ds_read_b32 v254, v255 offset:7936\n" \
"s_waitcnt lgkmcnt(0)\n" \
"v_readlane_b32 s4, v254, 0\n" \
"v_readlane_b32 s5, v254, 1\n" \
"v_readlane_b32 s6, v254, 2\n" \
"v_readlane_b32 s7, v254, 3\n" \
"v_readlane_b32 s8, v254, 4\n" \
"v_readlane_b32 s9, v254, 5\n" \
"v_readlane_b32 s10, v254, 6\n" \
"v_readlane_b32 s11, v254, 7\n" \
"v_readlane_b32 s12, v254, 8\n" \
"v_readlane_b32 s13, v254, 9\n" \
"v_readlane_b32 s14, v254, 10\n" \
"v_readlane_b32 s15, v254, 11\n" \
"v_readlane_b32 s16, v254, 12\n" \
"v_readlane_b32 s17, v254, 13\n" \
"v_readlane_b32 s18, v254, 14\n" \
"v_readlane_b32 s19, v254, 15\n" \
"v_readlane_b32 s20, v254, 16\n" \
"v_readlane_b32 s21, v254, 17\n" \
"v_readlane_b32 s22, v254, 18\n" \
"v_readlane_b32 s23, v254, 19\n" \
"v_readlane_b32 s24, v254, 20\n" \
"v_readlane_b32 s25, v254, 21\n" \
"v_readlane_b32 s26, v254, 22\n" \
"v_readlane_b32 s27, v254, 23\n" \
"v_readlane_b32 s28, v254, 24\n" \
"v_readlane_b32 s29, v254, 25\n" \
"v_readlane_b32 s30, v254, 26\n" \
"v_readlane_b32 s31, v254, 27\n" \
"v_readlane_b32 s32, v254, 28\n" \
"v_readlane_b32 s33, v254, 29\n" \
"v_readlane_b32 s34, v254, 30\n" \
"v_readlane_b32 s35, v254, 31\n" \
"v_readlane_b32 s36, v254, 32\n" \
"v_readlane_b32 s37, v254, 33\n" \
"v_readlane_b32 s38, v254, 34\n" \
"v_readlane_b32 s39, v254, 35\n" \
"s_mov_b32 s40, 0\n" \
"s_mov_b32 s51, 0\n" \
"s_nop 0\n" \
"v_mov_b32 v252, s38\n" \
"s_nop 4\n" \
"v_mov_b32 v100, -1\n" \
"v_mov_b32 v101, -1\n" \
"v_mov_b32 v102, -1\n" \
"v_mov_b32 v103, -1\n" \
"s_mov_b32 s35, -1\n" \
"s_mov_b32 s2, 0\n" \
"s_mov_b32 s3, -1\n" \
"s_add_u32 s43, s30, 1\n" \
"s_mov_b32 s50, 0\n" \
".LX2_w81l:\n" \
"buffer_load_dword v240, v204, s[4:7], 0 offen sc1\n" \
"s_waitcnt vmcnt(0)\n" \
".LX2_w81c:\n" \
"v_subrev_u32 v241, s43, v240\n" \
"v_cmp_gt_u32 vcc, 0x100, v241\n" \
"s_cmp_eq_u64 vcc, -1\n" \
"s_cbranch_scc1 .LX2_w81d\n" \
"s_sleep 2\n" \
"s_add_u32 s50, s50, 1\n" \
"s_cmp_lt_u32 s50, 0x20000\n" \
"s_cbranch_scc1 .LX2_w81l\n" \
"s_branch .LX2_end\n" \
".LX2_w81d:\n" \
"v_readlane_b32 s43, v202, 0\n" \
"s_nop 4\n" \
"buffer_load_dwordx4 a[0:3], v201, s[8:11], s43 offen sc1\n" \
"buffer_load_dwordx4 a[4:7], v201, s[8:11], s43 offen offset:1024 sc1\n" \
"v_readlane_b32 s43, v202, 7\n" \
"s_nop 4\n" \
"buffer_load_dwordx4 a[56:59], v201, s[8:11], s43 offen sc1\n" \
"buffer_load_dwordx4 a[60:63], v201, s[8:11], s43 offen offset:1024 sc1\n" \
"v_readlane_b32 s43, v202, 14\n" \
"s_nop 4\n" \
"buffer_load_dwordx4 a[112:115], v201, s[8:11], s43 offen sc1\n" \
"buffer_load_dwordx4 a[116:119], v201, s[8:11], s43 offen offset:1024 sc1\n" \
"v_readlane_b32 s43, v202, 21\n" \
"s_nop 4\n" \
"buffer_load_dwordx4 a[168:171], v201, s[8:11], s43 offen sc1\n" \
"buffer_load_dwordx4 a[172:175], v201, s[8:11], s43 offen offset:1024 sc1\n" \
"v_readlane_b32 s43, v202, 28\n" \
"s_nop 4\n" \
"buffer_load_dwordx4 a[224:227], v201, s[8:11], s43 offen sc1\n" \
"buffer_load_dwordx4 a[228:231], v201, s[8:11], s43 offen offset:1024 sc1\n" \
"v_readlane_b32 s43, v202, 35\n" \
"s_nop 4\n" \
"buffer_load_dwordx4 v[128:131], v201, s[8:11], s43 offen sc1\n" \
"buffer_load_dwordx4 v[132:135], v201, s[8:11], s43 offen offset:1024 sc1\n" \
"s_add_u32 s43, s30, 2\n" \
"s_mov_b32 s50, 0\n" \
".LX2_w82l:\n" \
"buffer_load_dword v240, v204, s[4:7], 0 offen sc1\n" \
"s_waitcnt vmcnt(0)\n" \
".LX2_w82c:\n" \
"v_subrev_u32 v241, s43, v240\n" \
"v_cmp_gt_u32 vcc, 0x100, v241\n" \
"s_cmp_eq_u64 vcc, -1\n" \
"s_cbranch_scc1 .LX2_w82d\n" \
"s_sleep 2\n" \
"s_add_u32 s50, s50, 1\n" \
"s_cmp_lt_u32 s50, 0x20000\n" \
"s_cbranch_scc1 .LX2_w82l\n" \
"s_branch .LX2_end\n" \
".LX2_w82d:\n" \
"v_readlane_b32 s43, v202, 1\n" \
"s_nop 4\n" \
"buffer_load_dwordx4 a[8:11], v201, s[8:11], s43 offen sc1\n" \
"buffer_load_dwordx4 a[12:15], v201, s[8:11], s43 offen offset:1024 sc1\n" \
"v_readlane_b32 s43, v202, 8\n" \
"s_nop 4\n" \
"buffer_load_dwordx4 a[64:67], v201, s[8:11], s43 offen sc1\n" \
"buffer_load_dwordx4 a[68:71], v201, s[8:11], s43 offen offset:1024 sc1\n" \
"v_readlane_b32 s43, v202, 15\n" \
"s_nop 4\n" \
"buffer_load_dwordx4 a[120:123], v201, s[8:11], s43 offen sc1\n" \
"buffer_load_dwordx4 a[124:127], v201, s[8:11], s43 offen offset:1024 sc1\n" \
"v_readlane_b32 s43, v202, 22\n" \
"s_nop 4\n" \
"buffer_load_dwordx4 a[176:179], v201, s[8:11], s43 offen sc1\n" \
"buffer_load_dwordx4 a[180:183], v201, s[8:11], s43 offen offset:1024 sc1\n" \
"v_readlane_b32 s43, v202, 29\n" \
"s_nop 4\n" \
"buffer_load_dwordx4 a[232:235], v201, s[8:11], s43 offen sc1\n" \
"buffer_load_dwordx4 a[236:239], v201, s[8:11], s43 offen offset:1024 sc1\n" \
"v_readlane_b32 s43, v202, 36\n" \
"s_nop 4\n" \
"buffer_load_dwordx4 v[136:139], v201, s[8:11], s43 offen sc1\n" \
"buffer_load_dwordx4 v[140:143], v201, s[8:11], s43 offen offset:1024 sc1\n" \
"s_add_u32 s43, s30, 3\n" \
"s_mov_b32 s50, 0\n" \
".LX2_w83l:\n" \
"buffer_load_dword v240, v204, s[4:7], 0 offen sc1\n" \
"s_waitcnt vmcnt(0)\n" \
".LX2_w83c:\n" \
"v_subrev_u32 v241, s43, v240\n" \
"v_cmp_gt_u32 vcc, 0x100, v241\n" \
"s_cmp_eq_u64 vcc, -1\n" \
"s_cbranch_scc1 .LX2_w83d\n" \
"s_sleep 2\n" \
"s_add_u32 s50, s50, 1\n" \
"s_cmp_lt_u32 s50, 0x20000\n" \
"s_cbranch_scc1 .LX2_w83l\n" \
"s_branch .LX2_end\n" \
".LX2_w83d:\n" \
"v_readlane_b32 s43, v202, 2\n" \
"s_nop 4\n" \
"buffer_load_dwordx4 a[16:19], v201, s[8:11], s43 offen sc1\n" \
"buffer_load_dwordx4 a[20:23], v201, s[8:11], s43 offen offset:1024 sc1\n" \
"v_readlane_b32 s43, v202, 9\n" \
"s_nop 4\n" \
"buffer_load_dwordx4 a[72:75], v201, s[8:11], s43 offen sc1\n" \
"buffer_load_dwordx4 a[76:79], v201, s[8:11], s43 offen offset:1024 sc1\n" \
"v_readlane_b32 s43, v202, 16\n" \
"s_nop 4\n" \
"buffer_load_dwordx4 a[128:131], v201, s[8:11], s43 offen sc1\n" \
"buffer_load_dwordx4 a[132:135], v201, s[8:11], s43 offen offset:1024 sc1\n" \
"v_readlane_b32 s43, v202, 23\n" \
"s_nop 4\n" \
"buffer_load_dwordx4 a[184:187], v201, s[8:11], s43 offen sc1\n" \
"buffer_load_dwordx4 a[188:191], v201, s[8:11], s43 offen offset:1024 sc1\n" \
"v_readlane_b32 s43, v202, 30\n" \
"s_nop 4\n" \
"buffer_load_dwordx4 a[240:243], v201, s[8:11], s43 offen sc1\n" \
"buffer_load_dwordx4 a[244:247], v201, s[8:11], s43 offen offset:1024 sc1\n" \
"v_readlane_b32 s43, v202, 37\n" \
"s_nop 4\n" \
"buffer_load_dwordx4 v[144:147], v201, s[8:11], s43 offen sc1\n" \
"buffer_load_dwordx4 v[148:151], v201, s[8:11], s43 offen offset:1024 sc1\n" \
"s_add_u32 s43, s30, 4\n" \
"s_mov_b32 s50, 0\n" \
".LX2_w84l:\n" \
"buffer_load_dword v240, v204, s[4:7], 0 offen sc1\n" \
"s_waitcnt vmcnt(0)\n" \
".LX2_w84c:\n" \
"v_subrev_u32 v241, s43, v240\n" \
"v_cmp_gt_u32 vcc, 0x100, v241\n" \
"s_cmp_eq_u64 vcc, -1\n" \
"s_cbranch_scc1 .LX2_w84d\n" \
"s_sleep 2\n" \
"s_add_u32 s50, s50, 1\n" \
"s_cmp_lt_u32 s50, 0x20000\n" \
"s_cbranch_scc1 .LX2_w84l\n" \
"s_branch .LX2_end\n" \
".LX2_w84d:\n" \
"v_readlane_b32 s43, v202, 3\n" \
"s_nop 4\n" \
"buffer_load_dwordx4 a[24:27], v201, s[8:11], s43 offen sc1\n" \
"buffer_load_dwordx4 a[28:31], v201, s[8:11], s43 offen offset:1024 sc1\n" \
"v_readlane_b32 s43, v202, 10\n" \
"s_nop 4\n" \
"buffer_load_dwordx4 a[80:83], v201, s[8:11], s43 offen sc1\n" \
"buffer_load_dwordx4 a[84:87], v201, s[8:11], s43 offen offset:1024 sc1\n" \
"v_readlane_b32 s43, v202, 17\n" \
"s_nop 4\n" \
"buffer_load_dwordx4 a[136:139], v201, s[8:11], s43 offen sc1\n" \
"buffer_load_dwordx4 a[140:143], v201, s[8:11], s43 offen offset:1024 sc1\n" \
"v_readlane_b32 s43, v202, 24\n" \
"s_nop 4\n" \
"buffer_load_dwordx4 a[192:195], v201, s[8:11], s43 offen sc1\n" \
"buffer_load_dwordx4 a[196:199], v201, s[8:11], s43 offen offset:1024 sc1\n" \
"v_readlane_b32 s43, v202, 31\n" \
"s_nop 4\n" \
"buffer_load_dwordx4 a[248:251], v201, s[8:11], s43 offen sc1\n" \
"buffer_load_dwordx4 a[252:255], v201, s[8:11], s43 offen offset:1024 sc1\n" \
"v_readlane_b32 s43, v202, 38\n" \
"s_nop 4\n" \
"buffer_load_dwordx4 v[152:155], v201, s[8:11], s43 offen sc1\n" \
"buffer_load_dwordx4 v[156:159], v201, s[8:11], s43 offen offset:1024 sc1\n" \
"s_add_u32 s43, s30, 5\n" \
"s_mov_b32 s50, 0\n" \
".LX2_w85l:\n" \
"buffer_load_dword v240, v204, s[4:7], 0 offen sc1\n" \
"s_waitcnt vmcnt(0)\n" \
".LX2_w85c:\n" \
"v_subrev_u32 v241, s43, v240\n" \
"v_cmp_gt_u32 vcc, 0x100, v241\n" \
"s_cmp_eq_u64 vcc, -1\n" \
"s_cbranch_scc1 .LX2_w85d\n" \
"s_sleep 2\n" \
"s_add_u32 s50, s50, 1\n" \
"s_cmp_lt_u32 s50, 0x20000\n" \
"s_cbranch_scc1 .LX2_w85l\n" \
"s_branch .LX2_end\n" \
".LX2_w85d:\n" \
"v_readlane_b32 s43, v202, 4\n" \
"s_nop 4\n" \
"buffer_load_dwordx4 a[32:35], v201, s[8:11], s43 offen sc1\n" \
"buffer_load_dwordx4 a[36:39], v201, s[8:11], s43 offen offset:1024 sc1\n" \
"v_readlane_b32 s43, v202, 11\n" \
"s_nop 4\n" \
"buffer_load_dwordx4 a[88:91], v201, s[8:11], s43 offen sc1\n" \
"buffer_load_dwordx4 a[92:95], v201, s[8:11], s43 offen offset:1024 sc1\n" \
"v_readlane_b32 s43, v202, 18\n" \
"s_nop 4\n" \
"buffer_load_dwordx4 a[144:147], v201, s[8:11], s43 offen sc1\n" \
"buffer_load_dwordx4 a[148:151], v201, s[8:11], s43 offen offset:1024 sc1\n" \
"v_readlane_b32 s43, v202, 25\n" \
"s_nop 4\n" \
"buffer_load_dwordx4 a[200:203], v201, s[8:11], s43 offen sc1\n" \
"buffer_load_dwordx4 a[204:207], v201, s[8:11], s43 offen offset:1024 sc1\n" \
"v_readlane_b32 s43, v202, 32\n" \
"s_nop 4\n" \
"buffer_load_dwordx4 v[104:107], v201, s[8:11], s43 offen sc1\n" \
"buffer_load_dwordx4 v[108:111], v201, s[8:11], s43 offen offset:1024 sc1\n" \
"v_readlane_b32 s43, v202, 39\n" \
"s_nop 4\n" \
"buffer_load_dwordx4 v[160:163], v201, s[8:11], s43 offen sc1\n" \
"buffer_load_dwordx4 v[164:167], v201, s[8:11], s43 offen offset:1024 sc1\n" \
"s_add_u32 s43, s30, 6\n" \
"s_mov_b32 s50, 0\n" \
".LX2_w86l:\n" \
"buffer_load_dword v240, v204, s[4:7], 0 offen sc1\n" \
"s_waitcnt vmcnt(0)\n" \
".LX2_w86c:\n" \
"v_subrev_u32 v241, s43, v240\n" \
"v_cmp_gt_u32 vcc, 0x100, v241\n" \
"s_cmp_eq_u64 vcc, -1\n" \
"s_cbranch_scc1 .LX2_w86d\n" \
"s_sleep 2\n" \
"s_add_u32 s50, s50, 1\n" \
"s_cmp_lt_u32 s50, 0x20000\n" \
"s_cbranch_scc1 .LX2_w86l\n" \
"s_branch .LX2_end\n" \
".LX2_w86d:\n" \
"v_readlane_b32 s43, v202, 5\n" \
"s_nop 4\n" \
"buffer_load_dwordx4 a[40:43], v201, s[8:11], s43 offen sc1\n" \
"buffer_load_dwordx4 a[44:47], v201, s[8:11], s43 offen offset:1024 sc1\n" \
"v_readlane_b32 s43, v202, 12\n" \
"s_nop 4\n" \
"buffer_load_dwordx4 a[96:99], v201, s[8:11], s43 offen sc1\n" \
"buffer_load_dwordx4 a[100:103], v201, s[8:11], s43 offen offset:1024 sc1\n" \
"v_readlane_b32 s43, v202, 19\n" \
"s_nop 4\n" \
"buffer_load_dwordx4 a[152:155], v201, s[8:11], s43 offen sc1\n" \
"buffer_load_dwordx4 a[156:159], v201, s[8:11], s43 offen offset:1024 sc1\n" \
"v_readlane_b32 s43, v202, 26\n" \
"s_nop 4\n" \
"buffer_load_dwordx4 a[208:211], v201, s[8:11], s43 offen sc1\n" \
"buffer_load_dwordx4 a[212:215], v201, s[8:11], s43 offen offset:1024 sc1\n" \
"v_readlane_b32 s43, v202, 33\n" \
"s_nop 4\n" \
"buffer_load_dwordx4 v[112:115], v201, s[8:11], s43 offen sc1\n" \
"buffer_load_dwordx4 v[116:119], v201, s[8:11], s43 offen offset:1024 sc1\n" \
"v_readlane_b32 s43, v202, 40\n" \
"s_nop 4\n" \
"buffer_load_dwordx4 v[168:171], v201, s[8:11], s43 offen sc1\n" \
"buffer_load_dwordx4 v[172:175], v201, s[8:11], s43 offen offset:1024 sc1\n" \
"s_add_u32 s43, s30, 7\n" \
"s_mov_b32 s50, 0\n" \
".LX2_w87l:\n" \
"buffer_load_dword v240, v204, s[4:7], 0 offen sc1\n" \
"s_waitcnt vmcnt(0)\n" \
".LX2_w87c:\n" \
"v_subrev_u32 v241, s43, v240\n" \
"v_cmp_gt_u32 vcc, 0x100, v241\n" \
"s_cmp_eq_u64 vcc, -1\n" \
"s_cbranch_scc1 .LX2_w87d\n" \
"s_sleep 2\n" \
"s_add_u32 s50, s50, 1\n" \
"s_cmp_lt_u32 s50, 0x20000\n" \
"s_cbranch_scc1 .LX2_w87l\n" \
"s_branch .LX2_end\n" \
".LX2_w87d:\n" \
"v_readlane_b32 s43, v202, 6\n" \
"s_nop 4\n" \
"buffer_load_dwordx4 a[48:51], v201, s[8:11], s43 offen sc1\n" \
"buffer_load_dwordx4 a[52:55], v201, s[8:11], s43 offen offset:1024 sc1\n" \
"v_readlane_b32 s43, v202, 13\n" \
"s_nop 4\n" \
"buffer_load_dwordx4 a[104:107], v201, s[8:11], s43 offen sc1\n" \
"buffer_load_dwordx4 a[108:111], v201, s[8:11], s43 offen offset:1024 sc1\n" \
"v_readlane_b32 s43, v202, 20\n" \
"s_nop 4\n" \
"buffer_load_dwordx4 a[160:163], v201, s[8:11], s43 offen sc1\n" \
"buffer_load_dwordx4 a[164:167], v201, s[8:11], s43 offen offset:1024 sc1\n" \
"v_readlane_b32 s43, v202, 27\n" \
"s_nop 4\n" \
"buffer_load_dwordx4 a[216:219], v201, s[8:11], s43 offen sc1\n" \
"buffer_load_dwordx4 a[220:223], v201, s[8:11], s43 offen offset:1024 sc1\n" \
"v_readlane_b32 s43, v202, 34\n" \
"s_nop 4\n" \
"buffer_load_dwordx4 v[120:123], v201, s[8:11], s43 offen sc1\n" \
"buffer_load_dwordx4 v[124:127], v201, s[8:11], s43 offen offset:1024 sc1\n" \
"v_readlane_b32 s43, v202, 41\n" \
"s_nop 4\n" \
"buffer_load_dwordx4 v[176:179], v201, s[8:11], s43 offen sc1\n" \
"buffer_load_dwordx4 v[180:183], v201, s[8:11], s43 offen offset:1024 sc1\n" \
"s_add_u32 s43, s30, 9\n" \
"s_mov_b32 s50, 0\n" \
".LX2_w88l:\n" \
"buffer_load_dword v240, v215, s[4:7], 0 offen sc1\n" \
"s_waitcnt vmcnt(0)\n" \
".LX2_w88c:\n" \
"v_subrev_u32 v241, s43, v240\n" \
"v_cmp_gt_u32 vcc, 0x100, v241\n" \
"s_cmp_eq_u64 vcc, -1\n" \
"s_cbranch_scc1 .LX2_w88d\n" \
"s_sleep 2\n" \
"s_add_u32 s50, s50, 1\n" \
"s_cmp_lt_u32 s50, 0x20000\n" \
"s_cbranch_scc1 .LX2_w88l\n" \
"s_branch .LX2_end\n" \
".LX2_w88d:\n" \
"s_add_u32 s50, s40, 0\n" \
"s_mul_i32 s50, s50, 458752\n" \
"s_add_u32 s50, s50, s34\n" \
"buffer_load_dwordx4 v[28:31], v201, s[12:15], s50 offen sc1\n" \
"buffer_load_dwordx4 v[32:35], v201, s[12:15], s50 offen offset:1024 sc1\n" \
"buffer_load_dwordx4 v[36:39], v201, s[12:15], s50 offen offset:2048 sc1\n" \
"buffer_load_dwordx4 v[40:43], v201, s[12:15], s50 offen offset:3072 sc1\n" \
"s_add_u32 s43, s30, 1\n" \
"s_mov_b32 s50, 0\n" \
".LX2_w89l:\n" \
"buffer_load_dword v240, v205, s[4:7], 0 offen sc1\n" \
"s_waitcnt vmcnt(0)\n" \
".LX2_w89c:\n" \
"v_subrev_u32 v241, s43, v240\n" \
"v_cmp_gt_u32 vcc, 0x100, v241\n" \
"s_cmp_eq_u64 vcc, -1\n" \
"s_cbranch_scc1 .LX2_w89d\n" \
"s_sleep 2\n" \
"s_add_u32 s50, s50, 1\n" \
"s_cmp_lt_u32 s50, 0x20000\n" \
"s_cbranch_scc1 .LX2_w89l\n" \
"s_branch .LX2_end\n" \
".LX2_w89d:\n" \
"s_add_u32 s43, s30, 1\n" \
"s_mov_b32 s50, 0\n" \
".LX2_w90l:\n" \
"buffer_load_dword v240, v206, s[4:7], 0 offen sc1\n" \
"s_waitcnt vmcnt(0)\n" \
".LX2_w90c:\n" \
"v_subrev_u32 v241, s43, v240\n" \
"v_cmp_gt_u32 vcc, 0x100, v241\n" \
"s_cmp_eq_u64 vcc, -1\n" \
"s_cbranch_scc1 .LX2_w90d\n" \
"s_sleep 2\n" \
"s_add_u32 s50, s50, 1\n" \
"s_cmp_lt_u32 s50, 0x20000\n" \
"s_cbranch_scc1 .LX2_w90l\n" \
"s_branch .LX2_end\n" \
".LX2_w90d:\n" \
"s_barrier\n" \
".LX2_loop:\n" \
"s_add_u32 s43, s40, 2\n" \
"s_and_b32 s43, s43, 3\n" \
"s_bitcmp1_b32 s43, 0\n" \
"s_cselect_b32 s49, s32, s31\n" \
"s_lshl_b32 s43, s43, 19\n" \
"s_add_u32 s49, s49, s43\n" \
"ds_read_b128 v[0:3], v216 offset:0\n" \
"ds_read_b128 v[4:7], v216 offset:1024\n" \
"ds_read_b128 v[8:11], v216 offset:2048\n" \
"ds_read_b128 v[12:15], v216 offset:3072\n" \
"ds_read_b128 v[16:19], v216 offset:4096\n" \
"ds_read_b128 v[20:23], v216 offset:5120\n" \
"ds_read_b128 v[24:27], v216 offset:6144\n" \
"v_mov_b32 v60, 0\n" \
"v_mov_b32 v61, 0\n" \
"v_mov_b32 v62, 0\n" \
"v_mov_b32 v63, 0\n" \
"v_mov_b32 v64, 0\n" \
"v_mov_b32 v65, 0\n" \
"v_mov_b32 v66, 0\n" \
"v_mov_b32 v67, 0\n" \
"v_mov_b32 v68, 0\n" \
"v_mov_b32 v69, 0\n" \
"v_mov_b32 v70, 0\n" \
"v_mov_b32 v71, 0\n" \
"v_mov_b32 v72, 0\n" \
"v_mov_b32 v73, 0\n" \
"v_mov_b32 v74, 0\n" \
"v_mov_b32 v75, 0\n" \
"v_mov_b32 v76, 0\n" \
"v_mov_b32 v77, 0\n" \
"v_mov_b32 v78, 0\n" \
"v_mov_b32 v79, 0\n" \
"v_mov_b32 v80, 0\n" \
"v_mov_b32 v81, 0\n" \
"v_mov_b32 v82, 0\n" \
"v_mov_b32 v83, 0\n" \
"s_nop 1\n" \
"s_waitcnt lgkmcnt(6)\n" \
"v_smfmac_f32_16x16x64_bf16 v[60:63], v[0:3], a[0:7], v200\n" \
"v_smfmac_f32_16x16x64_bf16 v[64:67], v[0:3], a[56:63], v200\n" \
"v_smfmac_f32_16x16x64_bf16 v[68:71], v[0:3], a[112:119], v200\n" \
"v_smfmac_f32_16x16x64_bf16 v[72:75], v[0:3], a[168:175], v200\n" \
"v_smfmac_f32_16x16x64_bf16 v[76:79], v[0:3], a[224:231], v200\n" \
"v_smfmac_f32_16x16x64_bf16 v[80:83], v[0:3], v[128:135], v200\n" \
"s_waitcnt lgkmcnt(5)\n" \
"v_smfmac_f32_16x16x64_bf16 v[60:63], v[4:7], a[8:15], v200\n" \
"v_smfmac_f32_16x16x64_bf16 v[64:67], v[4:7], a[64:71], v200\n" \
"v_smfmac_f32_16x16x64_bf16 v[68:71], v[4:7], a[120:127], v200\n" \
"v_smfmac_f32_16x16x64_bf16 v[72:75], v[4:7], a[176:183], v200\n" \
"v_smfmac_f32_16x16x64_bf16 v[76:79], v[4:7], a[232:239], v200\n" \
"v_smfmac_f32_16x16x64_bf16 v[80:83], v[4:7], v[136:143], v200\n" \
"s_waitcnt lgkmcnt(4)\n" \
"v_smfmac_f32_16x16x64_bf16 v[60:63], v[8:11], a[16:23], v200\n" \
"v_smfmac_f32_16x16x64_bf16 v[64:67], v[8:11], a[72:79], v200\n" \
"v_smfmac_f32_16x16x64_bf16 v[68:71], v[8:11], a[128:135], v200\n" \
"v_smfmac_f32_16x16x64_bf16 v[72:75], v[8:11], a[184:191], v200\n" \
"v_smfmac_f32_16x16x64_bf16 v[76:79], v[8:11], a[240:247], v200\n" \
"v_smfmac_f32_16x16x64_bf16 v[80:83], v[8:11], v[144:151], v200\n" \
"s_waitcnt lgkmcnt(3)\n" \
"v_smfmac_f32_16x16x64_bf16 v[60:63], v[12:15], a[24:31], v200\n" \
"v_smfmac_f32_16x16x64_bf16 v[64:67], v[12:15], a[80:87], v200\n" \
"v_smfmac_f32_16x16x64_bf16 v[68:71], v[12:15], a[136:143], v200\n" \
"v_smfmac_f32_16x16x64_bf16 v[72:75], v[12:15], a[192:199], v200\n" \
"v_smfmac_f32_16x16x64_bf16 v[76:79], v[12:15], a[248:255], v200\n" \
"v_smfmac_f32_16x16x64_bf16 v[80:83], v[12:15], v[152:159], v200\n" \
"s_waitcnt lgkmcnt(2)\n" \
"v_smfmac_f32_16x16x64_bf16 v[60:63], v[16:19], a[32:39], v200\n" \
"v_smfmac_f32_16x16x64_bf16 v[64:67], v[16:19], a[88:95], v200\n" \
"v_smfmac_f32_16x16x64_bf16 v[68:71], v[16:19], a[144:151], v200\n" \
"v_smfmac_f32_16x16x64_bf16 v[72:75], v[16:19], a[200:207], v200\n" \
"s_barrier\n" \
"s_add_u32 s50, s40, 1\n" \
"s_mul_i32 s50, s50, 458752\n" \
"s_add_u32 s50, s50, s34\n" \
"buffer_load_dwordx4 v[44:47], v201, s[12:15], s50 offen sc1\n" \
"buffer_load_dwordx4 v[48:51], v201, s[12:15], s50 offen offset:1024 sc1\n" \
"buffer_load_dwordx4 v[52:55], v201, s[12:15], s50 offen offset:2048 sc1\n" \
"buffer_load_dwordx4 v[56:59], v201, s[12:15], s50 offen offset:3072 sc1\n" \
"buffer_load_dword v240, v215, s[4:7], 0 offen sc1\n" \
"v_smfmac_f32_16x16x64_bf16 v[76:79], v[16:19], v[104:111], v200\n" \
"v_smfmac_f32_16x16x64_bf16 v[80:83], v[16:19], v[160:167], v200\n" \
"s_waitcnt lgkmcnt(1)\n" \
"v_smfmac_f32_16x16x64_bf16 v[60:63], v[20:23], a[40:47], v200\n" \
"v_smfmac_f32_16x16x64_bf16 v[64:67], v[20:23], a[96:103], v200\n" \
"v_smfmac_f32_16x16x64_bf16 v[68:71], v[20:23], a[152:159], v200\n" \
"v_smfmac_f32_16x16x64_bf16 v[72:75], v[20:23], a[208:215], v200\n" \
"v_smfmac_f32_16x16x64_bf16 v[76:79], v[20:23], v[112:119], v200\n" \
"v_smfmac_f32_16x16x64_bf16 v[80:83], v[20:23], v[168:175], v200\n" \
"s_waitcnt lgkmcnt(0)\n" \
"v_smfmac_f32_16x16x64_bf16 v[60:63], v[24:27], a[48:55], v200\n" \
"v_smfmac_f32_16x16x64_bf16 v[64:67], v[24:27], a[104:111], v200\n" \
"v_smfmac_f32_16x16x64_bf16 v[68:71], v[24:27], a[160:167], v200\n" \
"v_smfmac_f32_16x16x64_bf16 v[72:75], v[24:27], a[216:223], v200\n" \
"v_smfmac_f32_16x16x64_bf16 v[76:79], v[24:27], v[120:127], v200\n" \
"v_smfmac_f32_16x16x64_bf16 v[80:83], v[24:27], v[176:183], v200\n" \
"s_nop 7\n" \
"v_permlane32_swap_b32 v60, v64\n" \
"v_permlane32_swap_b32 v61, v65\n" \
"v_permlane32_swap_b32 v62, v66\n" \
"v_permlane32_swap_b32 v63, v67\n" \
"v_add_f32 v60, v60, v64\n" \
"v_add_f32 v61, v61, v65\n" \
"v_add_f32 v62, v62, v66\n" \
"v_add_f32 v63, v63, v67\n" \
"v_cvt_pk_bf16_f32 v184, v60, v61\n" \
"v_cvt_pk_bf16_f32 v185, v62, v63\n" \
"ds_write_b64 v217, v[184:185] offset:0\n" \
"v_permlane32_swap_b32 v68, v72\n" \
"v_permlane32_swap_b32 v69, v73\n" \
"v_permlane32_swap_b32 v70, v74\n" \
"v_permlane32_swap_b32 v71, v75\n" \
"v_add_f32 v68, v68, v72\n" \
"v_add_f32 v69, v69, v73\n" \
"v_add_f32 v70, v70, v74\n" \
"v_add_f32 v71, v71, v75\n" \
"v_cvt_pk_bf16_f32 v186, v68, v69\n" \
"v_cvt_pk_bf16_f32 v187, v70, v71\n" \
"ds_write_b64 v217, v[186:187] offset:512\n" \
"v_permlane32_swap_b32 v76, v80\n" \
"v_permlane32_swap_b32 v77, v81\n" \
"v_permlane32_swap_b32 v78, v82\n" \
"v_permlane32_swap_b32 v79, v83\n" \
"v_add_f32 v76, v76, v80\n" \
"v_add_f32 v77, v77, v81\n" \
"v_add_f32 v78, v78, v82\n" \
"v_add_f32 v79, v79, v83\n" \
"v_cvt_pk_bf16_f32 v188, v76, v77\n" \
"v_cvt_pk_bf16_f32 v189, v78, v79\n" \
"ds_write_b64 v217, v[188:189] offset:1024\n" \
"ds_read_b64_tr_b16 v[0:1], v218 offset:0\n" \
"ds_read_b64_tr_b16 v[2:3], v218 offset:64\n" \
"ds_read_b64_tr_b16 v[4:5], v218 offset:1024\n" \
"ds_read_b64_tr_b16 v[6:7], v218 offset:1088\n" \
"s_add_u32 s43, s40, 0\n" \
"s_mul_i32 s43, s43, 114688\n" \
"s_waitcnt lgkmcnt(0)\n" \
"buffer_store_dwordx4 v[0:3], v219, s[16:19], s43 offen sc1\n" \
"buffer_store_dwordx4 v[4:7], v221, s[16:19], s43 offen offset:1024 sc1\n" \
"s_waitcnt vmcnt(7)\n" \
"s_barrier\n" \
"ds_read_b64_tr_b16 v[92:93], v213\n" \
"ds_read_b64_tr_b16 v[94:95], v213 offset:64\n" \
"ds_read_b32 v84, v252 offset:16\n" \
"v_mov_b32 v245, v245\n" \
"s_nop 2\n" \
"v_mov_b32 v245, v245\n" \
"s_nop 2\n" \
"v_mov_b32 v245, v245\n" \
"s_waitcnt lgkmcnt(1)\n" \
"v_mfma_f32_16x16x32_bf16 v[68:71], v[92:95], v[28:31], 0\n" \
"s_waitcnt lgkmcnt(0)\n" \
"v_xor_b32 v85, s29, v84\n" \
"v_mfma_f32_16x16x32_bf16 v[76:79], v[92:95], v[36:39], 0\n" \
"v_add_u32 v84, v84, v210\n" \
"v_add_u32 v85, v85, v210\n" \
"v_mfma_f32_16x16x32_bf16 v[72:75], v[92:95], v[32:35], 0\n" \
"s_nop 1\n" \
"v_mfma_f32_16x16x32_bf16 v[80:83], v[92:95], v[40:43], 0\n" \
"v_cvt_pk_bf16_f32 v96, v68, v69\n" \
"v_cvt_pk_bf16_f32 v97, v70, v71\n" \
"v_cvt_pk_bf16_f32 v98, v76, v77\n" \
"v_cvt_pk_bf16_f32 v99, v78, v79\n" \
"s_mov_b64 exec, s[2:3]\n" \
"v_cvt_pk_bf16_f32 v96, v72, v73\n" \
"v_cvt_pk_bf16_f32 v97, v74, v75\n" \
"s_nop 0\n" \
"v_cvt_pk_bf16_f32 v98, v80, v81\n" \
"v_cvt_pk_bf16_f32 v99, v82, v83\n" \
"s_mov_b64 exec, -1\n" \
"s_nop 1\n" \
"v_permlane16_swap_b32 v96, v98\n" \
"v_permlane16_swap_b32 v97, v99\n" \
"buffer_store_dwordx4 v[96:99], v84, s[4:7], 0 offen sc1\n" \
"s_barrier\n" \
"buffer_store_dwordx4 v[96:99], v85, s[4:7], 0 offen sc1\n" \
"s_nop 15\n" \
"s_nop 15\n" \
"s_nop 15\n" \
"s_nop 15\n" \
"s_nop 15\n" \
"buffer_store_dwordx4 v[100:103], v214, s[4:7], s49 offen sc1\n" \
"s_xor_b32 s43, s49, s29\n" \
"buffer_store_dwordx4 v[100:103], v214, s[4:7], s43 offen sc1\n" \
"s_waitcnt vmcnt(4)\n" \
"v_mov_b32 v28, v44\n" \
"v_mov_b32 v29, v45\n" \
"v_mov_b32 v30, v46\n" \
"v_mov_b32 v31, v47\n" \
"v_mov_b32 v32, v48\n" \
"v_mov_b32 v33, v49\n" \
"v_mov_b32 v34, v50\n" \
"v_mov_b32 v35, v51\n" \
"v_mov_b32 v36, v52\n" \
"v_mov_b32 v37, v53\n" \
"v_mov_b32 v38, v54\n" \
"v_mov_b32 v39, v55\n" \
"v_mov_b32 v40, v56\n" \
"v_mov_b32 v41, v57\n" \
"v_mov_b32 v42, v58\n" \
"v_mov_b32 v43, v59\n" \
"s_add_u32 s36, s40, 10\n" \
"s_min_u32 s36, s36, 71\n" \
"s_add_u32 s43, s30, s36\n" \
"s_mov_b32 s50, 0\n" \
"s_branch .LX2_w91c\n" \
".LX2_w91l:\n" \
"buffer_load_dword v240, v215, s[4:7], 0 offen sc1\n" \
"s_waitcnt vmcnt(0)\n" \
".LX2_w91c:\n" \
"v_subrev_u32 v241, s43, v240\n" \
"v_cmp_gt_u32 vcc, 0x100, v241\n" \
"s_cmp_eq_u64 vcc, -1\n" \
"s_cbranch_scc1 .LX2_w91d\n" \
"s_sleep 2\n" \
"s_add_u32 s50, s50, 1\n" \
"s_cmp_lt_u32 s50, 0x20000\n" \
"s_cbranch_scc1 .LX2_w91l\n" \
"s_branch .LX2_end\n" \
".LX2_w91d:\n" \
"s_add_u32 s40, s40, 1\n" \
"s_waitcnt lgkmcnt(0)\n" \
"s_barrier\n" \
"s_cmp_lt_u32 s40, 63\n" \
"s_cbranch_scc1 .LX2_loop\n" \
"ds_read_b128 v[0:3], v216 offset:0\n" \
"ds_read_b128 v[4:7], v216 offset:1024\n" \
"ds_read_b128 v[8:11], v216 offset:2048\n" \
"ds_read_b128 v[12:15], v216 offset:3072\n" \
"ds_read_b128 v[16:19], v216 offset:4096\n" \
"ds_read_b128 v[20:23], v216 offset:5120\n" \
"ds_read_b128 v[24:27], v216 offset:6144\n" \
"v_mov_b32 v60, 0\n" \
"v_mov_b32 v61, 0\n" \
"v_mov_b32 v62, 0\n" \
"v_mov_b32 v63, 0\n" \
"v_mov_b32 v64, 0\n" \
"v_mov_b32 v65, 0\n" \
"v_mov_b32 v66, 0\n" \
"v_mov_b32 v67, 0\n" \
"v_mov_b32 v68, 0\n" \
"v_mov_b32 v69, 0\n" \
"v_mov_b32 v70, 0\n" \
"v_mov_b32 v71, 0\n" \
"v_mov_b32 v72, 0\n" \
"v_mov_b32 v73, 0\n" \
"v_mov_b32 v74, 0\n" \
"v_mov_b32 v75, 0\n" \
"v_mov_b32 v76, 0\n" \
"v_mov_b32 v77, 0\n" \
"v_mov_b32 v78, 0\n" \
"v_mov_b32 v79, 0\n" \
"v_mov_b32 v80, 0\n" \
"v_mov_b32 v81, 0\n" \
"v_mov_b32 v82, 0\n" \
"v_mov_b32 v83, 0\n" \
"s_nop 1\n" \
"s_waitcnt lgkmcnt(6)\n" \
"v_smfmac_f32_16x16x64_bf16 v[60:63], v[0:3], a[0:7], v200\n" \
"v_smfmac_f32_16x16x64_bf16 v[64:67], v[0:3], a[56:63], v200\n" \
"v_smfmac_f32_16x16x64_bf16 v[68:71], v[0:3], a[112:119], v200\n" \
"v_smfmac_f32_16x16x64_bf16 v[72:75], v[0:3], a[168:175], v200\n" \
"v_smfmac_f32_16x16x64_bf16 v[76:79], v[0:3], a[224:231], v200\n" \
"v_smfmac_f32_16x16x64_bf16 v[80:83], v[0:3], v[128:135], v200\n" \
"s_waitcnt lgkmcnt(5)\n" \
"v_smfmac_f32_16x16x64_bf16 v[60:63], v[4:7], a[8:15], v200\n" \
"v_smfmac_f32_16x16x64_bf16 v[64:67], v[4:7], a[64:71], v200\n" \
"v_smfmac_f32_16x16x64_bf16 v[68:71], v[4:7], a[120:127], v200\n" \
"v_smfmac_f32_16x16x64_bf16 v[72:75], v[4:7], a[176:183], v200\n" \
"v_smfmac_f32_16x16x64_bf16 v[76:79], v[4:7], a[232:239], v200\n" \
"v_smfmac_f32_16x16x64_bf16 v[80:83], v[4:7], v[136:143], v200\n" \
"s_waitcnt lgkmcnt(4)\n" \
"v_smfmac_f32_16x16x64_bf16 v[60:63], v[8:11], a[16:23], v200\n" \
"v_smfmac_f32_16x16x64_bf16 v[64:67], v[8:11], a[72:79], v200\n" \
"v_smfmac_f32_16x16x64_bf16 v[68:71], v[8:11], a[128:135], v200\n" \
"v_smfmac_f32_16x16x64_bf16 v[72:75], v[8:11], a[184:191], v200\n" \
"v_smfmac_f32_16x16x64_bf16 v[76:79], v[8:11], a[240:247], v200\n" \
"v_smfmac_f32_16x16x64_bf16 v[80:83], v[8:11], v[144:151], v200\n" \
"s_waitcnt lgkmcnt(3)\n" \
"v_smfmac_f32_16x16x64_bf16 v[60:63], v[12:15], a[24:31], v200\n" \
"v_smfmac_f32_16x16x64_bf16 v[64:67], v[12:15], a[80:87], v200\n" \
"v_smfmac_f32_16x16x64_bf16 v[68:71], v[12:15], a[136:143], v200\n" \
"v_smfmac_f32_16x16x64_bf16 v[72:75], v[12:15], a[192:199], v200\n" \
"v_smfmac_f32_16x16x64_bf16 v[76:79], v[12:15], a[248:255], v200\n" \
"v_smfmac_f32_16x16x64_bf16 v[80:83], v[12:15], v[152:159], v200\n" \
"s_waitcnt lgkmcnt(2)\n" \
"v_smfmac_f32_16x16x64_bf16 v[60:63], v[16:19], a[32:39], v200\n" \
"v_smfmac_f32_16x16x64_bf16 v[64:67], v[16:19], a[88:95], v200\n" \
"v_smfmac_f32_16x16x64_bf16 v[68:71], v[16:19], a[144:151], v200\n" \
"v_smfmac_f32_16x16x64_bf16 v[72:75], v[16:19], a[200:207], v200\n" \
"v_smfmac_f32_16x16x64_bf16 v[76:79], v[16:19], v[104:111], v200\n" \
"v_smfmac_f32_16x16x64_bf16 v[80:83], v[16:19], v[160:167], v200\n" \
"s_waitcnt lgkmcnt(1)\n" \
"v_smfmac_f32_16x16x64_bf16 v[60:63], v[20:23], a[40:47], v200\n" \
"v_smfmac_f32_16x16x64_bf16 v[64:67], v[20:23], a[96:103], v200\n" \
"v_smfmac_f32_16x16x64_bf16 v[68:71], v[20:23], a[152:159], v200\n" \
"v_smfmac_f32_16x16x64_bf16 v[72:75], v[20:23], a[208:215], v200\n" \
"v_smfmac_f32_16x16x64_bf16 v[76:79], v[20:23], v[112:119], v200\n" \
"v_smfmac_f32_16x16x64_bf16 v[80:83], v[20:23], v[168:175], v200\n" \
"s_waitcnt lgkmcnt(0)\n" \
"v_smfmac_f32_16x16x64_bf16 v[60:63], v[24:27], a[48:55], v200\n" \
"v_smfmac_f32_16x16x64_bf16 v[64:67], v[24:27], a[104:111], v200\n" \
"v_smfmac_f32_16x16x64_bf16 v[68:71], v[24:27], a[160:167], v200\n" \
"v_smfmac_f32_16x16x64_bf16 v[72:75], v[24:27], a[216:223], v200\n" \
"v_smfmac_f32_16x16x64_bf16 v[76:79], v[24:27], v[120:127], v200\n" \
"v_smfmac_f32_16x16x64_bf16 v[80:83], v[24:27], v[176:183], v200\n" \
"s_nop 7\n" \
"v_permlane32_swap_b32 v60, v64\n" \
"v_permlane32_swap_b32 v61, v65\n" \
"v_permlane32_swap_b32 v62, v66\n" \
"v_permlane32_swap_b32 v63, v67\n" \
"v_add_f32 v60, v60, v64\n" \
"v_add_f32 v61, v61, v65\n" \
"v_add_f32 v62, v62, v66\n" \
"v_add_f32 v63, v63, v67\n" \
"v_cvt_pk_bf16_f32 v184, v60, v61\n" \
"v_cvt_pk_bf16_f32 v185, v62, v63\n" \
"ds_write_b64 v217, v[184:185] offset:0\n" \
"v_permlane32_swap_b32 v68, v72\n" \
"v_permlane32_swap_b32 v69, v73\n" \
"v_permlane32_swap_b32 v70, v74\n" \
"v_permlane32_swap_b32 v71, v75\n" \
"v_add_f32 v68, v68, v72\n" \
"v_add_f32 v69, v69, v73\n" \
"v_add_f32 v70, v70, v74\n" \
"v_add_f32 v71, v71, v75\n" \
"v_cvt_pk_bf16_f32 v186, v68, v69\n" \
"v_cvt_pk_bf16_f32 v187, v70, v71\n" \
"ds_write_b64 v217, v[186:187] offset:512\n" \
"v_permlane32_swap_b32 v76, v80\n" \
"v_permlane32_swap_b32 v77, v81\n" \
"v_permlane32_swap_b32 v78, v82\n" \
"v_permlane32_swap_b32 v79, v83\n" \
"v_add_f32 v76, v76, v80\n" \
"v_add_f32 v77, v77, v81\n" \
"v_add_f32 v78, v78, v82\n" \
"v_add_f32 v79, v79, v83\n" \
"v_cvt_pk_bf16_f32 v188, v76, v77\n" \
"v_cvt_pk_bf16_f32 v189, v78, v79\n" \
"ds_write_b64 v217, v[188:189] offset:1024\n" \
"ds_read_b64_tr_b16 v[0:1], v218 offset:0\n" \
"ds_read_b64_tr_b16 v[2:3], v218 offset:64\n" \
"ds_read_b64_tr_b16 v[4:5], v218 offset:1024\n" \
"ds_read_b64_tr_b16 v[6:7], v218 offset:1088\n" \
"s_add_u32 s43, s40, 0\n" \
"s_mul_i32 s43, s43, 114688\n" \
"s_waitcnt lgkmcnt(0)\n" \
"buffer_store_dwordx4 v[0:3], v219, s[16:19], s43 offen sc1\n" \
"buffer_store_dwordx4 v[4:7], v221, s[16:19], s43 offen offset:1024 sc1\n" \
"s_waitcnt vmcnt(0)\n" \
"s_barrier\n" \
"ds_read_b128 v[0:3], v216 offset:7168\n" \
"ds_read_b128 v[4:7], v216 offset:8192\n" \
"ds_read_b128 v[8:11], v216 offset:9216\n" \
"ds_read_b128 v[12:15], v216 offset:10240\n" \
"ds_read_b128 v[16:19], v216 offset:11264\n" \
"ds_read_b128 v[20:23], v216 offset:12288\n" \
"ds_read_b128 v[24:27], v216 offset:13312\n" \
"v_mov_b32 v60, 0\n" \
"v_mov_b32 v61, 0\n" \
"v_mov_b32 v62, 0\n" \
"v_mov_b32 v63, 0\n" \
"v_mov_b32 v64, 0\n" \
"v_mov_b32 v65, 0\n" \
"v_mov_b32 v66, 0\n" \
"v_mov_b32 v67, 0\n" \
"v_mov_b32 v68, 0\n" \
"v_mov_b32 v69, 0\n" \
"v_mov_b32 v70, 0\n" \
"v_mov_b32 v71, 0\n" \
"v_mov_b32 v72, 0\n" \
"v_mov_b32 v73, 0\n" \
"v_mov_b32 v74, 0\n" \
"v_mov_b32 v75, 0\n" \
"v_mov_b32 v76, 0\n" \
"v_mov_b32 v77, 0\n" \
"v_mov_b32 v78, 0\n" \
"v_mov_b32 v79, 0\n" \
"v_mov_b32 v80, 0\n" \
"v_mov_b32 v81, 0\n" \
"v_mov_b32 v82, 0\n" \
"v_mov_b32 v83, 0\n" \
"s_nop 1\n" \
"s_waitcnt lgkmcnt(6)\n" \
"v_smfmac_f32_16x16x64_bf16 v[60:63], v[0:3], a[0:7], v200\n" \
"v_smfmac_f32_16x16x64_bf16 v[64:67], v[0:3], a[56:63], v200\n" \
"v_smfmac_f32_16x16x64_bf16 v[68:71], v[0:3], a[112:119], v200\n" \
"v_smfmac_f32_16x16x64_bf16 v[72:75], v[0:3], a[168:175], v200\n" \
"v_smfmac_f32_16x16x64_bf16 v[76:79], v[0:3], a[224:231], v200\n" \
"v_smfmac_f32_16x16x64_bf16 v[80:83], v[0:3], v[128:135], v200\n" \
"s_waitcnt lgkmcnt(5)\n" \
"v_smfmac_f32_16x16x64_bf16 v[60:63], v[4:7], a[8:15], v200\n" \
"v_smfmac_f32_16x16x64_bf16 v[64:67], v[4:7], a[64:71], v200\n" \
"v_smfmac_f32_16x16x64_bf16 v[68:71], v[4:7], a[120:127], v200\n" \
"v_smfmac_f32_16x16x64_bf16 v[72:75], v[4:7], a[176:183], v200\n" \
"v_smfmac_f32_16x16x64_bf16 v[76:79], v[4:7], a[232:239], v200\n" \
"v_smfmac_f32_16x16x64_bf16 v[80:83], v[4:7], v[136:143], v200\n" \
"s_waitcnt lgkmcnt(4)\n" \
"v_smfmac_f32_16x16x64_bf16 v[60:63], v[8:11], a[16:23], v200\n" \
"v_smfmac_f32_16x16x64_bf16 v[64:67], v[8:11], a[72:79], v200\n" \
"v_smfmac_f32_16x16x64_bf16 v[68:71], v[8:11], a[128:135], v200\n" \
"v_smfmac_f32_16x16x64_bf16 v[72:75], v[8:11], a[184:191], v200\n" \
"v_smfmac_f32_16x16x64_bf16 v[76:79], v[8:11], a[240:247], v200\n" \
"v_smfmac_f32_16x16x64_bf16 v[80:83], v[8:11], v[144:151], v200\n" \
"s_waitcnt lgkmcnt(3)\n" \
"v_smfmac_f32_16x16x64_bf16 v[60:63], v[12:15], a[24:31], v200\n" \
"v_smfmac_f32_16x16x64_bf16 v[64:67], v[12:15], a[80:87], v200\n" \
"v_smfmac_f32_16x16x64_bf16 v[68:71], v[12:15], a[136:143], v200\n" \
"v_smfmac_f32_16x16x64_bf16 v[72:75], v[12:15], a[192:199], v200\n" \
"v_smfmac_f32_16x16x64_bf16 v[76:79], v[12:15], a[248:255], v200\n" \
"v_smfmac_f32_16x16x64_bf16 v[80:83], v[12:15], v[152:159], v200\n" \
"s_waitcnt lgkmcnt(2)\n" \
"v_smfmac_f32_16x16x64_bf16 v[60:63], v[16:19], a[32:39], v200\n" \
"v_smfmac_f32_16x16x64_bf16 v[64:67], v[16:19], a[88:95], v200\n" \
"v_smfmac_f32_16x16x64_bf16 v[68:71], v[16:19], a[144:151], v200\n" \
"v_smfmac_f32_16x16x64_bf16 v[72:75], v[16:19], a[200:207], v200\n" \
"v_smfmac_f32_16x16x64_bf16 v[76:79], v[16:19], v[104:111], v200\n" \
"v_smfmac_f32_16x16x64_bf16 v[80:83], v[16:19], v[160:167], v200\n" \
"s_waitcnt lgkmcnt(1)\n" \
"v_smfmac_f32_16x16x64_bf16 v[60:63], v[20:23], a[40:47], v200\n" \
"v_smfmac_f32_16x16x64_bf16 v[64:67], v[20:23], a[96:103], v200\n" \
"v_smfmac_f32_16x16x64_bf16 v[68:71], v[20:23], a[152:159], v200\n" \
"v_smfmac_f32_16x16x64_bf16 v[72:75], v[20:23], a[208:215], v200\n" \
"v_smfmac_f32_16x16x64_bf16 v[76:79], v[20:23], v[112:119], v200\n" \
"v_smfmac_f32_16x16x64_bf16 v[80:83], v[20:23], v[168:175], v200\n" \
"s_waitcnt lgkmcnt(0)\n" \
"v_smfmac_f32_16x16x64_bf16 v[60:63], v[24:27], a[48:55], v200\n" \
"v_smfmac_f32_16x16x64_bf16 v[64:67], v[24:27], a[104:111], v200\n" \
"v_smfmac_f32_16x16x64_bf16 v[68:71], v[24:27], a[160:167], v200\n" \
"v_smfmac_f32_16x16x64_bf16 v[72:75], v[24:27], a[216:223], v200\n" \
"v_smfmac_f32_16x16x64_bf16 v[76:79], v[24:27], v[120:127], v200\n" \
"v_smfmac_f32_16x16x64_bf16 v[80:83], v[24:27], v[176:183], v200\n" \
"s_nop 7\n" \
"v_permlane32_swap_b32 v60, v64\n" \
"v_permlane32_swap_b32 v61, v65\n" \
"v_permlane32_swap_b32 v62, v66\n" \
"v_permlane32_swap_b32 v63, v67\n" \
"v_add_f32 v60, v60, v64\n" \
"v_add_f32 v61, v61, v65\n" \
"v_add_f32 v62, v62, v66\n" \
"v_add_f32 v63, v63, v67\n" \
"v_cvt_pk_bf16_f32 v184, v60, v61\n" \
"v_cvt_pk_bf16_f32 v185, v62, v63\n" \
"ds_write_b64 v217, v[184:185] offset:0\n" \
"v_permlane32_swap_b32 v68, v72\n" \
"v_permlane32_swap_b32 v69, v73\n" \
"v_permlane32_swap_b32 v70, v74\n" \
"v_permlane32_swap_b32 v71, v75\n" \
"v_add_f32 v68, v68, v72\n" \
"v_add_f32 v69, v69, v73\n" \
"v_add_f32 v70, v70, v74\n" \
"v_add_f32 v71, v71, v75\n" \
"v_cvt_pk_bf16_f32 v186, v68, v69\n" \
"v_cvt_pk_bf16_f32 v187, v70, v71\n" \
"ds_write_b64 v217, v[186:187] offset:512\n" \
"v_permlane32_swap_b32 v76, v80\n" \
"v_permlane32_swap_b32 v77, v81\n" \
"v_permlane32_swap_b32 v78, v82\n" \
"v_permlane32_swap_b32 v79, v83\n" \
"v_add_f32 v76, v76, v80\n" \
"v_add_f32 v77, v77, v81\n" \
"v_add_f32 v78, v78, v82\n" \
"v_add_f32 v79, v79, v83\n" \
"v_cvt_pk_bf16_f32 v188, v76, v77\n" \
"v_cvt_pk_bf16_f32 v189, v78, v79\n" \
"ds_write_b64 v217, v[188:189] offset:1024\n" \
"ds_read_b64_tr_b16 v[0:1], v218 offset:0\n" \
"ds_read_b64_tr_b16 v[2:3], v218 offset:64\n" \
"ds_read_b64_tr_b16 v[4:5], v218 offset:1024\n" \
"ds_read_b64_tr_b16 v[6:7], v218 offset:1088\n" \
"s_add_u32 s43, s40, 1\n" \
"s_mul_i32 s43, s43, 114688\n" \
"s_waitcnt lgkmcnt(0)\n" \
"buffer_store_dwordx4 v[0:3], v219, s[16:19], s43 offen sc1\n" \
"buffer_store_dwordx4 v[4:7], v221, s[16:19], s43 offen offset:1024 sc1\n" \
"s_waitcnt vmcnt(0)\n" \
"s_barrier\n" \
".LX2_end:\n" \
"s_endpgm\n" \
".p2align 6\n" \
"fk_role_X3:\n" \
"ds_read_b32 v200, v255 offset:0\n" \
"ds_read_b32 v201, v255 offset:256\n" \
"ds_read_b32 v202, v255 offset:512\n" \
"ds_read_b32 v203, v255 offset:768\n" \
"ds_read_b32 v204, v255 offset:1024\n" \
"ds_read_b32 v205, v255 offset:1280\n" \
"ds_read_b32 v206, v255 offset:1536\n" \
"ds_read_b32 v207, v255 offset:1792\n" \
"ds_read_b32 v208, v255 offset:2048\n" \
"ds_read_b32 v209, v255 offset:2304\n" \
"ds_read_b32 v210, v255 offset:2560\n" \
"ds_read_b32 v211, v255 offset:2816\n" \
"ds_read_b32 v212, v255 offset:3072\n" \
"ds_read_b32 v213, v255 offset:3328\n" \
"ds_read_b32 v214, v255 offset:3584\n" \
"ds_read_b32 v215, v255 offset:3840\n" \
"ds_read_b32 v216, v255 offset:4096\n" \
"ds_read_b32 v217, v255 offset:4352\n" \
"ds_read_b32 v218, v255 offset:4608\n" \
"ds_read_b32 v219, v255 offset:4864\n" \
"ds_read_b32 v220, v255 offset:5120\n" \
"ds_read_b32 v221, v255 offset:5376\n" \
"ds_read_b32 v254, v255 offset:7936\n" \
"s_waitcnt lgkmcnt(0)\n" \
"v_readlane_b32 s4, v254, 0\n" \
"v_readlane_b32 s5, v254, 1\n" \
"v_readlane_b32 s6, v254, 2\n" \
"v_readlane_b32 s7, v254, 3\n" \
"v_readlane_b32 s8, v254, 4\n" \
"v_readlane_b32 s9, v254, 5\n" \
"v_readlane_b32 s10, v254, 6\n" \
"v_readlane_b32 s11, v254, 7\n" \
"v_readlane_b32 s12, v254, 8\n" \
"v_readlane_b32 s13, v254, 9\n" \
"v_readlane_b32 s14, v254, 10\n" \
"v_readlane_b32 s15, v254, 11\n" \
"v_readlane_b32 s16, v254, 12\n" \
"v_readlane_b32 s17, v254, 13\n" \
"v_readlane_b32 s18, v254, 14\n" \
"v_readlane_b32 s19, v254, 15\n" \
"v_readlane_b32 s20, v254, 16\n" \
"v_readlane_b32 s21, v254, 17\n" \
"v_readlane_b32 s22, v254, 18\n" \
"v_readlane_b32 s23, v254, 19\n" \
"v_readlane_b32 s24, v254, 20\n" \
"v_readlane_b32 s25, v254, 21\n" \
"v_readlane_b32 s26, v254, 22\n" \
"v_readlane_b32 s27, v254, 23\n" \
"v_readlane_b32 s28, v254, 24\n" \
"v_readlane_b32 s29, v254, 25\n" \
"v_readlane_b32 s30, v254, 26\n" \
"v_readlane_b32 s31, v254, 27\n" \
"v_readlane_b32 s32, v254, 28\n" \
"v_readlane_b32 s33, v254, 29\n" \
"v_readlane_b32 s34, v254, 30\n" \
"v_readlane_b32 s35, v254, 31\n" \
"v_readlane_b32 s36, v254, 32\n" \
"v_readlane_b32 s37, v254, 33\n" \
"v_readlane_b32 s38, v254, 34\n" \
"v_readlane_b32 s39, v254, 35\n" \
"s_mov_b32 s40, 0\n" \
"s_mov_b32 s51, 0\n" \
"s_nop 0\n" \
"v_mov_b32 v252, s38\n" \
"s_nop 4\n" \
"v_mov_b32 v100, -1\n" \
"v_mov_b32 v101, -1\n" \
"v_mov_b32 v102, -1\n" \
"v_mov_b32 v103, -1\n" \
"s_mov_b32 s35, -1\n" \
"s_mov_b32 s2, 0\n" \
"s_mov_b32 s3, -1\n" \
"s_add_u32 s43, s30, 9\n" \
"s_mov_b32 s50, 0\n" \
".LX3_w92l:\n" \
"buffer_load_dword v240, v215, s[4:7], 0 offen sc1\n" \
"s_waitcnt vmcnt(0)\n" \
".LX3_w92c:\n" \
"v_subrev_u32 v241, s43, v240\n" \
"v_cmp_gt_u32 vcc, 0x100, v241\n" \
"s_cmp_eq_u64 vcc, -1\n" \
"s_cbranch_scc1 .LX3_w92d\n" \
"s_sleep 2\n" \
"s_add_u32 s50, s50, 1\n" \
"s_cmp_lt_u32 s50, 0x20000\n" \
"s_cbranch_scc1 .LX3_w92l\n" \
"s_branch .LX3_end\n" \
".LX3_w92d:\n" \
"s_add_u32 s43, s40, 0\n" \
"s_mul_i32 s43, s43, 458752\n" \
"s_add_u32 s43, s43, s33\n" \
"buffer_load_dwordx4 a[0:3], v201, s[8:11], s43 offen sc1\n" \
"buffer_load_dwordx4 a[4:7], v201, s[8:11], s43 offen offset:1024 sc1\n" \
"buffer_load_dwordx4 a[8:11], v201, s[8:11], s43 offen offset:2048 sc1\n" \
"buffer_load_dwordx4 a[12:15], v201, s[8:11], s43 offen offset:3072 sc1\n" \
"s_add_u32 s43, s43, 4096\n" \
"buffer_load_dwordx4 a[16:19], v201, s[8:11], s43 offen sc1\n" \
"buffer_load_dwordx4 a[20:23], v201, s[8:11], s43 offen offset:1024 sc1\n" \
"buffer_load_dwordx4 a[24:27], v201, s[8:11], s43 offen offset:2048 sc1\n" \
"buffer_load_dwordx4 a[28:31], v201, s[8:11], s43 offen offset:3072 sc1\n" \
"s_add_u32 s43, s43, 4096\n" \
"buffer_load_dwordx4 a[32:35], v201, s[8:11], s43 offen sc1\n" \
"buffer_load_dwordx4 a[36:39], v201, s[8:11], s43 offen offset:1024 sc1\n" \
"buffer_load_dwordx4 a[40:43], v201, s[8:11], s43 offen offset:2048 sc1\n" \
"buffer_load_dwordx4 a[44:47], v201, s[8:11], s43 offen offset:3072 sc1\n" \
"s_add_u32 s43, s43, 4096\n" \
"buffer_load_dwordx4 a[48:51], v201, s[8:11], s43 offen sc1\n" \
"buffer_load_dwordx4 a[52:55], v201, s[8:11], s43 offen offset:1024 sc1\n" \
"buffer_load_dwordx4 a[56:59], v201, s[8:11], s43 offen offset:2048 sc1\n" \
"buffer_load_dwordx4 a[60:63], v201, s[8:11], s43 offen offset:3072 sc1\n" \
"s_add_u32 s43, s43, 4096\n" \
"buffer_load_dwordx4 a[64:67], v201, s[8:11], s43 offen sc1\n" \
"buffer_load_dwordx4 a[68:71], v201, s[8:11], s43 offen offset:1024 sc1\n" \
"buffer_load_dwordx4 a[72:75], v201, s[8:11], s43 offen offset:2048 sc1\n" \
"buffer_load_dwordx4 a[76:79], v201, s[8:11], s43 offen offset:3072 sc1\n" \
"s_add_u32 s43, s43, 4096\n" \
"buffer_load_dwordx4 a[80:83], v201, s[8:11], s43 offen sc1\n" \
"buffer_load_dwordx4 a[84:87], v201, s[8:11], s43 offen offset:1024 sc1\n" \
"buffer_load_dwordx4 a[88:91], v201, s[8:11], s43 offen offset:2048 sc1\n" \
"buffer_load_dwordx4 a[92:95], v201, s[8:11], s43 offen offset:3072 sc1\n" \
"s_add_u32 s43, s43, 4096\n" \
"buffer_load_dwordx4 a[96:99], v201, s[8:11], s43 offen sc1\n" \
"buffer_load_dwordx4 a[100:103], v201, s[8:11], s43 offen offset:1024 sc1\n" \
"buffer_load_dwordx4 a[104:107], v201, s[8:11], s43 offen offset:2048 sc1\n" \
"buffer_load_dwordx4 a[108:111], v201, s[8:11], s43 offen offset:3072 sc1\n" \
"s_add_u32 s50, s40, 0\n" \
"s_mul_i32 s50, s50, 458752\n" \
"s_add_u32 s50, s50, s34\n" \
"buffer_load_dwordx4 v[28:31], v201, s[12:15], s50 offen sc1\n" \
"buffer_load_dwordx4 v[32:35], v201, s[12:15], s50 offen offset:1024 sc1\n" \
"s_add_u32 s43, s30, 1\n" \
"s_mov_b32 s50, 0\n" \
".LX3_w93l:\n" \
"buffer_load_dword v240, v205, s[4:7], 0 offen sc1\n" \
"s_waitcnt vmcnt(0)\n" \
".LX3_w93c:\n" \
"v_subrev_u32 v241, s43, v240\n" \
"v_cmp_gt_u32 vcc, 0x100, v241\n" \
"s_cmp_eq_u64 vcc, -1\n" \
"s_cbranch_scc1 .LX3_w93d\n" \
"s_sleep 2\n" \
"s_add_u32 s50, s50, 1\n" \
"s_cmp_lt_u32 s50, 0x20000\n" \
"s_cbranch_scc1 .LX3_w93l\n" \
"s_branch .LX3_end\n" \
".LX3_w93d:\n" \
"s_add_u32 s43, s30, 1\n" \
"s_mov_b32 s50, 0\n" \
".LX3_w94l:\n" \
"buffer_load_dword v240, v206, s[4:7], 0 offen sc1\n" \
"s_waitcnt vmcnt(0)\n" \
".LX3_w94c:\n" \
"v_subrev_u32 v241, s43, v240\n" \
"v_cmp_gt_u32 vcc, 0x100, v241\n" \
"s_cmp_eq_u64 vcc, -1\n" \
"s_cbranch_scc1 .LX3_w94d\n" \
"s_sleep 2\n" \
"s_add_u32 s50, s50, 1\n" \
"s_cmp_lt_u32 s50, 0x20000\n" \
"s_cbranch_scc1 .LX3_w94l\n" \
"s_branch .LX3_end\n" \
".LX3_w94d:\n" \
"s_barrier\n" \
".LX3_loop:\n" \
"s_add_u32 s43, s40, 2\n" \
"s_and_b32 s43, s43, 3\n" \
"s_bitcmp1_b32 s43, 0\n" \
"s_cselect_b32 s49, s32, s31\n" \
"s_lshl_b32 s43, s43, 19\n" \
"s_add_u32 s49, s49, s43\n" \
"ds_read_b96 v[248:250], v252\n" \
"s_waitcnt lgkmcnt(0)\n" \
"v_readfirstlane_b32 s44, v248\n" \
"v_readfirstlane_b32 s45, v249\n" \
"v_readfirstlane_b32 s46, v250\n" \
"s_mul_i32 s42, s25, s46\n" \
"s_lshr_b32 s42, s42, 12\n" \
"s_add_u32 s43, s40, 0\n" \
"s_and_b32 s43, s43, 3\n" \
"s_bitcmp1_b32 s43, 0\n" \
"s_cselect_b32 s47, s32, s31\n" \
"s_lshl_b32 s43, s43, 19\n" \
"s_add_u32 s47, s47, s43\n" \
"s_sub_u32 s43, s24, s44\n" \
"s_add_u32 s43, s43, s28\n" \
"s_max_i32 s43, s43, 0xfffd0000\n" \
"s_min_i32 s43, s43, 0x4000\n" \
"s_mul_i32 s43, s43, s46\n" \
"s_ashr_i32 s43, s43, 12\n" \
"s_add_u32 s41, s45, s43\n" \
"s_branch .LX3_a1\n" \
".LX3_retrya:\n" \
"s_add_u32 s51, s51, 1\n" \
"s_cmp_gt_u32 s51, 0x20000\n" \
"s_cbranch_scc1 .LX3_end\n" \
".LX3_a0:\n" \
"s_add_u32 s41, s41, s42\n" \
"s_add_u32 s24, s24, s25\n" \
"s_xor_b32 s31, s31, s29\n" \
"s_xor_b32 s32, s32, s29\n" \
"s_xor_b32 s47, s47, s29\n" \
"s_xor_b32 s48, s48, s29\n" \
"s_add_u32 s39, s39, 1\n" \
"buffer_store_dwordx4 v[100:103], v214, s[4:7], s47 offen sc1\n" \
"buffer_store_dwordx4 v[100:103], v217, s[4:7], s47 offen sc1\n" \
"buffer_store_dwordx4 v[100:103], v218, s[4:7], s47 offen sc1\n" \
"buffer_store_dwordx4 v[100:103], v219, s[4:7], s47 offen sc1\n" \
".p2align 6\n" \
".LX3_a1:\n" \
"s_memtime vcc\n" \
"s_waitcnt lgkmcnt(0)\n" \
"s_sub_u32 s43, s41, vcc_lo\n" \
"s_cmp_gt_i32 s43, 128\n" \
"s_cbranch_scc1 .LX3_a1\n" \
"s_cmp_lt_i32 s43, s37\n" \
"s_cbranch_scc1 .LX3_a0\n" \
"s_max_i32 s43, s43, 0\n" \
"s_and_b32 s43, s43, -4\n" \
"s_getpc_b64 vcc\n" \
".LX3_a3:\n" \
"s_sub_u32 s43, .LX3_a4-.LX3_a3, s43\n" \
"s_add_u32 vcc_lo, vcc_lo, s43\n" \
"s_addc_u32 vcc_hi, vcc_hi, 0\n" \
"s_setpc_b64 vcc\n" \
".rept 32\n" \
"s_nop 0\n" \
".endr\n" \
".LX3_a4:\n" \
"buffer_load_dwordx4 v[0:3], v208, s[4:7], s47 offen sc1\n" \
"buffer_load_dwordx4 v[4:7], v208, s[4:7], s47 offen offset:1024 sc1\n" \
"buffer_load_dwordx4 v[8:11], v208, s[4:7], s47 offen offset:2048 sc1\n" \
"buffer_load_dwordx4 v[12:15], v208, s[4:7], s47 offen offset:3072 sc1\n" \
"buffer_load_dwordx4 v[16:19], v209, s[4:7], s47 offen sc1\n" \
"buffer_load_dwordx4 v[20:23], v209, s[4:7], s47 offen offset:1024 sc1\n" \
"buffer_load_dwordx4 v[24:27], v209, s[4:7], s47 offen offset:2048 sc1\n" \
"s_cmp_eq_u32 s35, s40\n" \
"s_cbranch_scc1 .LX3_nb\n" \
"s_barrier\n" \
".LX3_nb:\n" \
"s_mov_b32 s35, s40\n" \
"v_mov_b32 v96, s47\n" \
"ds_write_b32 v252, v96 offset:16\n" \
"v_mov_b32 v60, 0\n" \
"v_mov_b32 v61, 0\n" \
"v_mov_b32 v62, 0\n" \
"v_mov_b32 v63, 0\n" \
"v_mov_b32 v64, 0\n" \
"v_mov_b32 v65, 0\n" \
"v_mov_b32 v66, 0\n" \
"v_mov_b32 v67, 0\n" \
"s_lshl_b32 s43, s40, 7\n" \
"buffer_load_dwordx4 v[236:239], v207, s[4:7], s43 offen sc1\n" \
"s_add_u32 s50, s40, 1\n" \
"s_mul_i32 s50, s50, 458752\n" \
"s_add_u32 s50, s50, s34\n" \
"buffer_load_dwordx4 v[44:47], v201, s[12:15], s50 offen sc1\n" \
"buffer_load_dwordx4 v[48:51], v201, s[12:15], s50 offen offset:1024 sc1\n" \
"s_waitcnt vmcnt(9)\n" \
"v_smfmac_f32_16x16x64_bf16 v[60:63], v[0:3], a[0:7], v200\n" \
"v_smfmac_f32_16x16x64_bf16 v[64:67], v[0:3], a[56:63], v200\n" \
"s_waitcnt vmcnt(8)\n" \
"v_smfmac_f32_16x16x64_bf16 v[60:63], v[4:7], a[8:15], v200\n" \
"v_smfmac_f32_16x16x64_bf16 v[64:67], v[4:7], a[64:71], v200\n" \
"s_waitcnt vmcnt(7)\n" \
"v_smfmac_f32_16x16x64_bf16 v[60:63], v[8:11], a[16:23], v200\n" \
"v_smfmac_f32_16x16x64_bf16 v[64:67], v[8:11], a[72:79], v200\n" \
"s_waitcnt vmcnt(6)\n" \
"v_smfmac_f32_16x16x64_bf16 v[60:63], v[12:15], a[24:31], v200\n" \
"v_smfmac_f32_16x16x64_bf16 v[64:67], v[12:15], a[80:87], v200\n" \
"s_waitcnt vmcnt(5)\n" \
"v_smfmac_f32_16x16x64_bf16 v[60:63], v[16:19], a[32:39], v200\n" \
"v_smfmac_f32_16x16x64_bf16 v[64:67], v[16:19], a[88:95], v200\n" \
"s_waitcnt vmcnt(4)\n" \
"v_smfmac_f32_16x16x64_bf16 v[60:63], v[20:23], a[40:47], v200\n" \
"v_smfmac_f32_16x16x64_bf16 v[64:67], v[20:23], a[96:103], v200\n" \
"s_waitcnt vmcnt(3)\n" \
"v_smfmac_f32_16x16x64_bf16 v[60:63], v[24:27], a[48:55], v200\n" \
"v_smfmac_f32_16x16x64_bf16 v[64:67], v[24:27], a[104:111], v200\n" \
"s_nop 2\n" \
"v_max3_u32 v84, v0, v0, v1\n" \
"v_max3_u32 v84, v84, v2, v3\n" \
"v_max3_u32 v84, v84, v4, v5\n" \
"v_max3_u32 v84, v84, v6, v7\n" \
"v_max3_u32 v84, v84, v8, v9\n" \
"v_permlane32_swap_b32 v60, v64\n" \
"v_permlane32_swap_b32 v61, v65\n" \
"v_permlane32_swap_b32 v62, v66\n" \
"v_permlane32_swap_b32 v63, v67\n" \
"v_pk_add_f32 v[60:61], v[60:61], v[64:65]\n" \
"v_pk_add_f32 v[62:63], v[62:63], v[66:67]\n" \
"v_max3_u32 v84, v84, v10, v11\n" \
"v_max_f32_dpp v88, v60, v60 row_ror:8 row_mask:0xf bank_mask:0x3\n" \
"v_max_f32_dpp v89, v61, v61 row_ror:8 row_mask:0xf bank_mask:0x3\n" \
"v_max_f32_dpp v88, v62, v62 row_ror:8 row_mask:0xf bank_mask:0xc\n" \
"v_max_f32_dpp v89, v63, v63 row_ror:8 row_mask:0xf bank_mask:0xc\n" \
"v_max3_u32 v84, v84, v12, v13\n" \
"v_max_f32_dpp v90, v88, v88 row_shl:4 row_mask:0xf bank_mask:0x5\n" \
"v_max_f32_dpp v90, v89, v89 row_shr:4 row_mask:0xf bank_mask:0xa\n" \
"v_max3_u32 v84, v84, v14, v15\n" \
"v_max3_u32 v84, v84, v16, v17\n" \
"v_max_f32_dpp v88, v90, v90 quad_perm:[1,0,3,2] row_mask:0xf bank_mask:0xf\n" \
"v_max3_u32 v84, v84, v18, v19\n" \
"v_max3_u32 v84, v84, v20, v21\n" \
"v_max_f32_dpp v86, v88, v88 quad_perm:[2,3,0,1] row_mask:0xf bank_mask:0xf\n" \
"v_max_f32_dpp v89, v88, v88 quad_perm:[2,3,0,1] row_mask:0xf bank_mask:0xf\n" \
"v_max3_u32 v84, v84, v22, v23\n" \
"v_max3_u32 v84, v84, v24, v25\n" \
"v_permlane32_swap_b32 v86, v89\n" \
"v_max_f32 v86, v86, v89\n" \
"v_max3_u32 v84, v84, v26, v27\n" \
"v_cmp_eq_u32 vcc, -1, v84\n" \
"v_subrev_f32_dpp v60, v86, v60 row_newbcast:0 row_mask:0xf bank_mask:0xf\n" \
"v_subrev_f32_dpp v61, v86, v61 row_newbcast:4 row_mask:0xf bank_mask:0xf\n" \
"v_subrev_f32_dpp v62, v86, v62 row_newbcast:8 row_mask:0xf bank_mask:0xf\n" \
"v_subrev_f32_dpp v63, v86, v63 row_newbcast:12 row_mask:0xf bank_mask:0xf\n" \
"v_exp_f32 v60, v60\n" \
"v_exp_f32 v61, v61\n" \
"v_exp_f32 v62, v62\n" \
"v_exp_f32 v63, v63\n" \
"v_cvt_pk_bf16_f32 v92, v60, v61\n" \
"v_cvt_pk_bf16_f32 v93, v62, v63\n" \
"ds_write_b64 v212, v[92:93]\n" \
"s_cbranch_vccnz .LX3_retrya\n" \
"v_add_f32_dpp v88, v60, v60 row_ror:8 row_mask:0xf bank_mask:0x3\n" \
"v_add_f32_dpp v89, v61, v61 row_ror:8 row_mask:0xf bank_mask:0x3\n" \
"v_add_f32_dpp v88, v62, v62 row_ror:8 row_mask:0xf bank_mask:0xc\n" \
"v_add_f32_dpp v89, v63, v63 row_ror:8 row_mask:0xf bank_mask:0xc\n" \
"v_mov_b32 v245, v245\n" \
"v_add_f32_dpp v90, v88, v88 row_shl:4 row_mask:0xf bank_mask:0x5\n" \
"v_add_f32_dpp v90, v89, v89 row_shr:4 row_mask:0xf bank_mask:0xa\n" \
"v_mov_b32 v245, v245\n" \
"v_mov_b32 v245, v245\n" \
"v_add_f32_dpp v88, v90, v90 quad_perm:[1,0,3,2] row_mask:0xf bank_mask:0xf\n" \
"v_mov_b32 v245, v245\n" \
"v_mov_b32 v245, v245\n" \
"v_add_f32_dpp v87, v88, v88 quad_perm:[2,3,0,1] row_mask:0xf bank_mask:0xf\n" \
"buffer_store_dwordx2 v[86:87], v211, s[4:7], s47 offen sc1\n" \
"s_nop 0\n" \
"v_mov_b32 v245, v245\n" \
"s_waitcnt lgkmcnt(0)\n" \
"s_barrier\n" \
"ds_read_b64_tr_b16 v[92:93], v213\n" \
"ds_read_b64_tr_b16 v[94:95], v213 offset:64\n" \
"ds_read_b32 v84, v252 offset:16\n" \
"v_mov_b32 v245, v245\n" \
"s_nop 2\n" \
"v_mov_b32 v245, v245\n" \
"s_nop 2\n" \
"v_mov_b32 v245, v245\n" \
"s_waitcnt lgkmcnt(1)\n" \
"v_mfma_f32_16x16x32_bf16 v[68:71], v[92:95], v[28:31], 0\n" \
"s_waitcnt lgkmcnt(0)\n" \
"v_xor_b32 v85, s29, v84\n" \
"v_mfma_f32_16x16x32_bf16 v[72:75], v[92:95], v[32:35], 0\n" \
"v_add_u32 v84, v84, v210\n" \
"v_add_u32 v85, v85, v210\n" \
"v_mov_b32 v245, v245\n" \
"v_mov_b32 v245, v245\n" \
"v_mov_b32 v245, v245\n" \
"v_cvt_pk_bf16_f32 v96, v68, v69\n" \
"v_cvt_pk_bf16_f32 v97, v70, v71\n" \
"s_mov_b64 exec, s[2:3]\n" \
"v_cvt_pk_bf16_f32 v96, v72, v73\n" \
"v_cvt_pk_bf16_f32 v97, v74, v75\n" \
"s_mov_b64 exec, -1\n" \
"v_mov_b32 v98, v96\n" \
"v_mov_b32 v99, v97\n" \
"s_nop 1\n" \
"v_permlane16_swap_b32 v96, v98\n" \
"v_permlane16_swap_b32 v97, v99\n" \
"buffer_store_dwordx4 v[96:99], v84, s[4:7], 0 offen sc1\n" \
"s_barrier\n" \
"buffer_store_dwordx4 v[96:99], v85, s[4:7], 0 offen sc1\n" \
"s_xor_b32 s43, s47, s29\n" \
"buffer_store_dwordx2 v[86:87], v211, s[4:7], s43 offen sc1\n" \
"ds_write_b128 v216, v[0:3] offset:0\n" \
"ds_write_b128 v216, v[4:7] offset:1024\n" \
"ds_write_b128 v216, v[8:11] offset:2048\n" \
"ds_write_b128 v216, v[12:15] offset:3072\n" \
"ds_write_b128 v216, v[16:19] offset:4096\n" \
"ds_write_b128 v216, v[20:23] offset:5120\n" \
"ds_write_b128 v216, v[24:27] offset:6144\n" \
"s_add_u32 s43, s40, 1\n" \
"s_mul_i32 s43, s43, 458752\n" \
"s_add_u32 s43, s43, s33\n" \
"buffer_load_dwordx4 a[0:3], v201, s[8:11], s43 offen sc1\n" \
"buffer_load_dwordx4 a[4:7], v201, s[8:11], s43 offen offset:1024 sc1\n" \
"buffer_load_dwordx4 a[8:11], v201, s[8:11], s43 offen offset:2048 sc1\n" \
"buffer_load_dwordx4 a[12:15], v201, s[8:11], s43 offen offset:3072 sc1\n" \
"s_add_u32 s43, s43, 4096\n" \
"buffer_load_dwordx4 a[16:19], v201, s[8:11], s43 offen sc1\n" \
"buffer_load_dwordx4 a[20:23], v201, s[8:11], s43 offen offset:1024 sc1\n" \
"buffer_load_dwordx4 a[24:27], v201, s[8:11], s43 offen offset:2048 sc1\n" \
"buffer_load_dwordx4 a[28:31], v201, s[8:11], s43 offen offset:3072 sc1\n" \
"s_add_u32 s43, s43, 4096\n" \
"buffer_load_dwordx4 a[32:35], v201, s[8:11], s43 offen sc1\n" \
"buffer_load_dwordx4 a[36:39], v201, s[8:11], s43 offen offset:1024 sc1\n" \
"buffer_load_dwordx4 a[40:43], v201, s[8:11], s43 offen offset:2048 sc1\n" \
"buffer_load_dwordx4 a[44:47], v201, s[8:11], s43 offen offset:3072 sc1\n" \
"s_add_u32 s43, s43, 4096\n" \
"buffer_load_dwordx4 a[48:51], v201, s[8:11], s43 offen sc1\n" \
"buffer_load_dwordx4 a[52:55], v201, s[8:11], s43 offen offset:1024 sc1\n" \
"buffer_load_dwordx4 a[56:59], v201, s[8:11], s43 offen offset:2048 sc1\n" \
"buffer_load_dwordx4 a[60:63], v201, s[8:11], s43 offen offset:3072 sc1\n" \
"s_add_u32 s43, s43, 4096\n" \
"buffer_load_dwordx4 a[64:67], v201, s[8:11], s43 offen sc1\n" \
"buffer_load_dwordx4 a[68:71], v201, s[8:11], s43 offen offset:1024 sc1\n" \
"buffer_load_dwordx4 a[72:75], v201, s[8:11], s43 offen offset:2048 sc1\n" \
"buffer_load_dwordx4 a[76:79], v201, s[8:11], s43 offen offset:3072 sc1\n" \
"s_add_u32 s43, s43, 4096\n" \
"buffer_load_dwordx4 a[80:83], v201, s[8:11], s43 offen sc1\n" \
"buffer_load_dwordx4 a[84:87], v201, s[8:11], s43 offen offset:1024 sc1\n" \
"buffer_load_dwordx4 a[88:91], v201, s[8:11], s43 offen offset:2048 sc1\n" \
"buffer_load_dwordx4 a[92:95], v201, s[8:11], s43 offen offset:3072 sc1\n" \
"s_add_u32 s43, s43, 4096\n" \
"buffer_load_dwordx4 a[96:99], v201, s[8:11], s43 offen sc1\n" \
"buffer_load_dwordx4 a[100:103], v201, s[8:11], s43 offen offset:1024 sc1\n" \
"buffer_load_dwordx4 a[104:107], v201, s[8:11], s43 offen offset:2048 sc1\n" \
"buffer_load_dwordx4 a[108:111], v201, s[8:11], s43 offen offset:3072 sc1\n" \
"buffer_store_dwordx4 v[100:103], v214, s[4:7], s49 offen sc1\n" \
"s_xor_b32 s43, s49, s29\n" \
"buffer_store_dwordx4 v[100:103], v214, s[4:7], s43 offen sc1\n" \
"s_waitcnt vmcnt(34)\n" \
"v_mov_b32 v28, v44\n" \
"v_mov_b32 v29, v45\n" \
"v_mov_b32 v30, v46\n" \
"v_mov_b32 v31, v47\n" \
"v_mov_b32 v32, v48\n" \
"v_mov_b32 v33, v49\n" \
"v_mov_b32 v34, v50\n" \
"v_mov_b32 v35, v51\n" \
"v_max_u32 v236, v236, v238\n" \
"v_max_u32 v237, v237, v239\n" \
"v_and_b32 v236, 0xfff, v236\n" \
"v_and_b32 v237, 0xfff, v237\n" \
"s_nop 0\n" \
"v_readfirstlane_b32 s26, v236\n" \
"v_readfirstlane_b32 s27, v237\n" \
"s_add_u32 s24, s24, s26\n" \
"s_add_u32 s24, s24, s27\n" \
"s_add_u32 s40, s40, 1\n" \
"s_waitcnt lgkmcnt(0)\n" \
"s_barrier\n" \
"s_cmp_lt_u32 s40, 63\n" \
"s_cbranch_scc1 .LX3_loop\n" \
"ds_read_b96 v[248:250], v252\n" \
"s_waitcnt lgkmcnt(0)\n" \
"v_readfirstlane_b32 s44, v248\n" \
"v_readfirstlane_b32 s45, v249\n" \
"v_readfirstlane_b32 s46, v250\n" \
"s_mul_i32 s42, s25, s46\n" \
"s_lshr_b32 s42, s42, 12\n" \
"s_add_u32 s43, s40, 0\n" \
"s_and_b32 s43, s43, 3\n" \
"s_bitcmp1_b32 s43, 0\n" \
"s_cselect_b32 s47, s32, s31\n" \
"s_lshl_b32 s43, s43, 19\n" \
"s_add_u32 s47, s47, s43\n" \
"s_sub_u32 s43, s24, s44\n" \
"s_add_u32 s43, s43, s28\n" \
"s_max_i32 s43, s43, 0xfffd0000\n" \
"s_min_i32 s43, s43, 0x4000\n" \
"s_mul_i32 s43, s43, s46\n" \
"s_ashr_i32 s43, s43, 12\n" \
"s_add_u32 s41, s45, s43\n" \
"s_branch .LX3_z1\n" \
".LX3_retryz:\n" \
"s_add_u32 s51, s51, 1\n" \
"s_cmp_gt_u32 s51, 0x20000\n" \
"s_cbranch_scc1 .LX3_end\n" \
".LX3_z0:\n" \
"s_add_u32 s41, s41, s42\n" \
"s_add_u32 s24, s24, s25\n" \
"s_xor_b32 s31, s31, s29\n" \
"s_xor_b32 s32, s32, s29\n" \
"s_xor_b32 s47, s47, s29\n" \
"s_xor_b32 s48, s48, s29\n" \
"s_add_u32 s39, s39, 1\n" \
".p2align 6\n" \
".LX3_z1:\n" \
"s_memtime vcc\n" \
"s_waitcnt lgkmcnt(0)\n" \
"s_sub_u32 s43, s41, vcc_lo\n" \
"s_cmp_gt_i32 s43, 128\n" \
"s_cbranch_scc1 .LX3_z1\n" \
"s_cmp_lt_i32 s43, s37\n" \
"s_cbranch_scc1 .LX3_z0\n" \
"s_max_i32 s43, s43, 0\n" \
"s_and_b32 s43, s43, -4\n" \
"s_getpc_b64 vcc\n" \
".LX3_z3:\n" \
"s_sub_u32 s43, .LX3_z4-.LX3_z3, s43\n" \
"s_add_u32 vcc_lo, vcc_lo, s43\n" \
"s_addc_u32 vcc_hi, vcc_hi, 0\n" \
"s_setpc_b64 vcc\n" \
".rept 32\n" \
"s_nop 0\n" \
".endr\n" \
".LX3_z4:\n" \
"buffer_load_dwordx4 v[0:3], v208, s[4:7], s47 offen sc1\n" \
"buffer_load_dwordx4 v[4:7], v208, s[4:7], s47 offen offset:1024 sc1\n" \
"buffer_load_dwordx4 v[8:11], v208, s[4:7], s47 offen offset:2048 sc1\n" \
"buffer_load_dwordx4 v[12:15], v208, s[4:7], s47 offen offset:3072 sc1\n" \
"buffer_load_dwordx4 v[16:19], v209, s[4:7], s47 offen sc1\n" \
"buffer_load_dwordx4 v[20:23], v209, s[4:7], s47 offen offset:1024 sc1\n" \
"buffer_load_dwordx4 v[24:27], v209, s[4:7], s47 offen offset:2048 sc1\n" \
"s_waitcnt vmcnt(0)\n" \
"ds_write_b128 v216, v[0:3] offset:7168\n" \
"ds_write_b128 v216, v[4:7] offset:8192\n" \
"ds_write_b128 v216, v[8:11] offset:9216\n" \
"ds_write_b128 v216, v[12:15] offset:10240\n" \
"ds_write_b128 v216, v[16:19] offset:11264\n" \
"ds_write_b128 v216, v[20:23] offset:12288\n" \
"ds_write_b128 v216, v[24:27] offset:13312\n" \
"v_max3_u32 v84, v0, v0, v1\n" \
"v_max3_u32 v84, v84, v2, v3\n" \
"v_max3_u32 v84, v84, v4, v5\n" \
"v_max3_u32 v84, v84, v6, v7\n" \
"v_max3_u32 v84, v84, v8, v9\n" \
"v_max3_u32 v84, v84, v10, v11\n" \
"v_max3_u32 v84, v84, v12, v13\n" \
"v_max3_u32 v84, v84, v14, v15\n" \
"v_max3_u32 v84, v84, v16, v17\n" \
"v_max3_u32 v84, v84, v18, v19\n" \
"v_max3_u32 v84, v84, v20, v21\n" \
"v_max3_u32 v84, v84, v22, v23\n" \
"v_max3_u32 v84, v84, v24, v25\n" \
"v_max3_u32 v84, v84, v26, v27\n" \
"v_cmp_eq_u32 vcc, -1, v84\n" \
"s_cbranch_vccnz .LX3_retryz\n" \
"s_waitcnt lgkmcnt(0)\n" \
"s_barrier\n" \
"s_barrier\n" \
".LX3_end:\n" \
"s_endpgm\n"
typedef __attribute__((ext_vector_type(16))) float f16v;
DI u4 pack8(const f16v& x, int s, float sc) { return (u4){pk2(x[8 * s] * sc, x[8 * s + 1] * sc), pk2(x[8 * s + 2] * sc, x[8 * s + 3] * sc), pk2(x[8 * s + 4] * sc, x[8 * s + 5] * sc), pk2(x[8 * s + 6] * sc, x[8 * s + 7] * sc)}; }
DI u4 pack8(const f16v& x, int s) { return (u4){pk2(x[8 * s], x[8 * s + 1]), pk2(x[8 * s + 2], x[8 * s + 3]), pk2(x[8 * s + 4], x[8 * s + 5]), pk2(x[8 * s + 6], x[8 * s + 7])}; }
DI void dma_pair(u4 srd, int v0, int v1, int soff, unsigned m) {
    asm volatile("s_mov_b32 m0, %4\n\ts_nop 0\n\tbuffer_load_dwordx4 %0, %2, %3 offen lds\n\tbuffer_load_dwordx4 %1, %2, %3 offen offset:1024 lds" :: "v"(v0), "v"(v1), "s"(srd), "s"(soff), "s"(m) : "memory");
}
DI void dma_one(u4 srd, int v0, int soff, unsigned m) {
    asm volatile("s_mov_b32 m0, %3\n\ts_nop 0\n\tbuffer_load_dwordx4 %0, %1, %2 offen lds" :: "v"(v0), "s"(srd), "s"(soff), "s"(m) : "memory");
}
DI void mV_va(f16v& c, u4 x, u4 y) { asm volatile("v_mfma_f32_32x32x16_bf16 %0, %1, %2, %0" : "+v"(c) : "v"(x), "a"(y)); }
DI void mV_va0(f16v& c, u4 x, u4 y) { asm volatile("v_mfma_f32_32x32x16_bf16 %0, %1, %2, 0" : "=&v"(c) : "v"(x), "a"(y)); }
DI void mV_av(f16v& c, u4 x, u4 y) { asm volatile("v_mfma_f32_32x32x16_bf16 %0, %1, %2, %0" : "+v"(c) : "a"(x), "v"(y)); }
DI void mV_av0(f16v& c, u4 x, u4 y) { asm volatile("v_mfma_f32_32x32x16_bf16 %0, %1, %2, 0" : "=&v"(c) : "a"(x), "v"(y)); }
DI void mV_vv(f16v& c, u4 x, u4 y) { asm volatile("v_mfma_f32_32x32x16_bf16 %0, %1, %2, %0" : "+v"(c) : "v"(x), "v"(y)); }
DI void mV_vv0(f16v& c, u4 x, u4 y) { asm volatile("v_mfma_f32_32x32x16_bf16 %0, %1, %2, 0" : "=&v"(c) : "v"(x), "v"(y)); }
template <int N, int PD, class RD, class MM, class EX> DI void pipe(RD rd, MM mm, EX ex) {
    u4 f[PD];
#pragma unroll
    for (int j = 0; j < PD; ++j) f[j] = rd(j);
    SB();
#pragma unroll
    for (int j = 0; j < N; ++j) { mm(j, f[j % PD]); if (j + PD < N) f[j % PD] = rd(j + PD); ex(j); SB(); }
}
DI u4 sld128(u4 srd, int voff, int soff) { u4 v; asm volatile("s_nop 4\n\tbuffer_load_dwordx4 %0, %1, %2, %3 offen sc1" : "=v"(v) : "v"(voff), "s"(srd), "s"(soff) : "memory"); return v; }
DI u2 sld64(u4 srd, int voff, int soff) { u2 v; asm volatile("s_nop 4\n\tbuffer_load_dwordx2 %0, %1, %2, %3 offen sc1" : "=v"(v) : "v"(voff), "s"(srd), "s"(soff) : "memory"); return v; }
DI void sst128(u4 v, u4 srd, int voff, int soff) { asm volatile("s_nop 4\n\tbuffer_store_dwordx4 %0, %1, %2, %3 offen sc1\n\ts_nop 1" :: "v"(v), "v"(voff), "s"(srd), "s"(soff) : "memory"); }
DI void sst64(u2 v, u4 srd, int voff, int soff) { asm volatile("s_nop 4\n\tbuffer_store_dwordx2 %0, %1, %2, %3 offen sc1" :: "v"(v), "v"(voff), "s"(srd), "s"(soff) : "memory"); }
DI void sst32(unsigned v, u4 srd, int voff, int soff) { asm volatile("s_nop 4\n\tbuffer_store_dword %0, %1, %2, %3 offen sc1" :: "v"(v), "v"(voff), "s"(srd), "s"(soff) : "memory"); }
DI unsigned sld32(u4 srd, int voff, int soff) { unsigned v; asm volatile("s_nop 4\n\tbuffer_load_dword %0, %1, %2, %3 offen sc1" : "=v"(v) : "v"(voff), "s"(srd), "s"(soff) : "memory"); return v; }
template <int N> DI void waitvm() { asm volatile("s_waitcnt vmcnt(%0)" :: "i"(N) : "memory"); }
DI int swzr(int row) { return ((row & 1) << 2) | (row & 8) | ((row >> 1) & 3); }
#define SLICE 32768
#define CSCALE (LOG2E * 0.0625f)

struct SideCtx {
    char LAS* sm; unsigned smb; int lane, wave;
    int vr0, vr1, vc0, vc1;
};
DI int fresh_lane() { int l; asm volatile("v_mbcnt_lo_u32_b32 %0, -1, 0\n\tv_mbcnt_hi_u32_b32 %0, -1, %0" : "=v"(l)); __builtin_assume(l >= 0 && l < 64); return l; }
DI SideCtx make_ctx(int wave) {
    SideCtx c; c.sm = (char LAS*)smem_; c.smb = (unsigned)(size_t)c.sm; c.lane = fresh_lane(); c.wave = wave;
    asm volatile("" : "+s"(c.wave), "+s"(c.smb));
    const int l = c.lane;
    { int row = 4 * c.wave + (l >> 5); c.vr0 = row * 8192 + (((l & 31) ^ swzr(row)) * 16); row += 2; c.vr1 = row * 8192 + (((l & 31) ^ swzr(row)) * 16) - 1024; }
    { int row = l >> 3; c.vc0 = row * 896 + (((l & 7) ^ (((row >> 1) & 1) << 2)) * 16); c.vc1 = c.vc0 + 8 * 896 - 1024; }
    return c;
}
DI void dma_rows_q(const SideCtx& c, u4 srd, int h, int r0, int slot, int g) { dma_pair(srd, c.vr0, c.vr1, (r0 + 16 * g) * 8192 + h * 512, c.smb + slot * SLICE + (8 * g + 2 * c.wave) * 1024); }
DI void dma_cols_q(const SideCtx& c, u4 srd, int h, int c0, int slot, int g) { const int k = 8 * c.wave + 2 * g; dma_pair(srd, c.vc0, c.vc1, (h * 256 + 8 * k) * 896 + c0 * 2, c.smb + slot * SLICE + k * 1024); }
DI void dma_rows(const SideCtx& c, u4 srd, int h, int r0, int slot) {
#pragma unroll
    for (int g = 0; g < 4; ++g) dma_rows_q(c, srd, h, r0, slot, g);
}
DI void dma_cols(const SideCtx& c, u4 srd, int h, int c0, int slot) {
#pragma unroll
    for (int g = 0; g < 4; ++g) dma_cols_q(c, srd, h, c0, slot, g);
}
DI u4 rd_rows(const SideCtx& c, int slot, int ks, int tau) {
    const int l = c.lane, hi = l >> 5, ro = l & 31;
    const int rowc = 32 * (ro >> 4) + 4 * ((ro >> 2) & 1) + 8 * ((ro >> 1) & 1) + 2 * ((ro >> 3) & 1) + (ro & 1);
    const int ch = (2 * (ks & 7) + hi) ^ swzr(rowc);
    return *(const u4 LAS*)(c.sm + (rowc * 512 + ch * 16) + (slot * SLICE + tau * 8192 + (ks >> 3) * 256));
}
DI u4 tr_cols(const SideCtx& c, int slot, int ks, int nt) {
    const int l = c.lane, hi = l >> 5, blk = (l >> 4) & 1, r = (l & 15) >> 2, p = l & 3;
    u2 o[2];
#pragma unroll
    for (int t = 0; t < 2; ++t) {
        const int ch = (4 * nt + 2 * blk + (p >> 1)) ^ (((r >> 1) & 1) << 2);
        o[t] = trread(c.sm + ((8 * hi + r) * 128 + ch * 16 + 8 * (p & 1)) + (slot * SLICE + ks * 2048 + t * 512));
    }
    return (u4){o[0][0], o[0][1], o[1][0], o[1][1]};
}
#define PF_AHEAD 12
#define NSEG1 8
DI void set_flag(const SideCtx& c, u4 rhb, int off, unsigned val) {
    waitvm<0>(); bar();
    if (c.wave == 0) sst32(val, rhb, c.lane == 0 ? off : OOB, 0);
}
DI void wait_prog(u4 rhb, int voff_or_oob, unsigned want) {
    unsigned sp = 0;
#pragma nounroll
    for (;;) {
        unsigned v = sld32(rhb, voff_or_oob, 0); WAITV(0, "+v"(v));
        if (__all((v - want < 256u) | (voff_or_oob >= OOB))) break;
        asm volatile("s_sleep 4" ::: "memory");
        if (++sp > SPIN_MAX) __builtin_amdgcn_endpgm();
    }
}
#define MFW32(x, y) asm volatile("s_nop 11" : "+v"(x), "+v"(y))
DI int perm16(int r) { return 4 * ((r >> 2) & 1) + 8 * ((r >> 1) & 1) + 2 * ((r >> 3) & 1) + (r & 1); }
DI void w_first(int wave, int type, int h, int sub) {
    CArgs& a = get_args();
    const SideCtx c = make_ctx(wave);
    h = __builtin_amdgcn_readfirstlane(h); sub = __builtin_amdgcn_readfirstlane(sub); asm volatile("" : "+s"(h), "+s"(sub));
    const u4 rw1 = mksrd(type ? a.Wv : a.Wk, 448 * 8192), rw2 = mksrd(type ? a.Wo : a.Wq, 448 * 8192);
    const int c1 = sub == 3 ? 384 : sub * 128 + 64;
    if (type) { dma_cols(c, rw2, h, sub * 128, 2); dma_cols(c, rw2, h, c1, 3); }
    else { dma_rows(c, rw2, h, sub * 128, 2); dma_rows(c, rw2, h, c1, 3); }
    dma_rows(c, rw1, h, 0, 0); dma_rows(c, rw1, h, 64, 1);
}
template <int N> struct IntC { static constexpr int value = N; };
template <int TYPE>
DI void wgu_task(unsigned tag, int vb, int wave, int h, int sub) {
    CArgs& a = get_args();
    const SideCtx c = make_ctx(wave);
    const int lane = c.lane, hi = lane >> 5, l31 = lane & 31, w = c.wave;
    h = __builtin_amdgcn_readfirstlane(h); sub = __builtin_amdgcn_readfirstlane(sub); asm volatile("" : "+s"(h), "+s"(sub));
    const u4 rw1 = mksrd(TYPE ? a.Wv : a.Wk, 448 * 8192), rx = mksrd(a.X, 2048 * 896), rhb = mksrd(a.HB, HB_SZ);
    const int o_fl = (w == 0 && lane == 0) ? O_FW + ((h * 2 + TYPE) * 4 + sub) * 128 : OOB;
    int xo[7];
#pragma unroll
    for (int k = 0; k < 7; ++k) { const int n = 64 * (7 * (w & 1) + k) + lane, row = (n * 1171) >> 16, pos = n - 56 * row; xo[k] = (row + 16 * (w >> 1)) * 896 + ((pos ^ ((row >> 1) & 7)) * 16); }
    auto xdma = [&](int s, int reg, int k) { dma_one(rx, xo[k], s * 28672, c.smb + reg + (7 * w + k) * 1024); };
    const int m = sub * 4 + w;
    u4 wk[28];
    f16v d[2][2];
    {
        u4 kf[16];
        waitvm<16>(); bar();
        const int rowp = 16 * (l31 >> 4) + perm16(l31 & 15);
#pragma unroll
        for (int ks = 0; ks < 16; ++ks)
            kf[ks] = TYPE ? tr_cols(c, 2 + (w >> 1), ks, w & 1) : *(const u4 LAS*)(c.sm + 2 * SLICE + w * 16384 + (rowp * 512 + ((2 * (ks & 7) + hi) ^ swzr(rowp)) * 16) + (ks >> 3) * 256);
        bar();
#pragma unroll
        for (int i = 0; i < PF_AHEAD / 4; ++i) dma_one(rx, lane < 56 ? (vb >> 3) * 896 + lane * 16 : OOB, (4 * i + w) * 28672, c.smb + 4 * SLICE + 28672 + w * 1024);
        dma_rows(c, rw1, h, 128, 2);
        const u4 rout = mksrd(TYPE ? a.WvoF : a.WqkF, 28 * 16 * 14336);
        const int t16 = 2 * m + (l31 >> 4);
        const int o_st = m > 13 ? OOB : (TYPE ? t16 * 16 + h : h * 28 + t16) * 14336 + (hi * 16 + (TYPE ? l31 & 15 : perm16(l31 & 15))) * 16;
        auto epi = [&](int v, int e) {
            const u4 p = TYPE ? pack8(d[v & 1][e & 1], e >> 1) : pack8(d[v & 1][e & 1], e >> 1, CSCALE);
            st128(p, rout, o_st, (v * 2 + (e >> 1)) * 1024 + (e & 1) * 512);
            const auto s0 = __builtin_amdgcn_permlane32_swap(p[0], p[1], false, false), s1 = __builtin_amdgcn_permlane32_swap(p[2], p[3], false, false);
            wk[4 * v + e] = (u4){s0[0], s1[0], s0[1], s1[1]};
            asm volatile("" : "+a"(wk[4 * v + e]));
        };
#pragma unroll
        for (int u = 0; u < 7; ++u) {
            if (u == 0) waitvm<16 + PF_AHEAD / 4>(); else if (u == 1) waitvm<16 + PF_AHEAD / 4>(); else if (u == 4) waitvm<21>(); else if (u == 6) waitvm<19>(); else waitvm<20>();
            bar();
            if (u >= 3) st32(tag + u - 2, rhb, o_fl, 0);
            pipe<32, 4>([&](int j) { return rd_rows(c, u & 3, j >> 1, j & 1); },
                        [&](int j, u4 f) { if (j < 2) mV_vv0(d[u & 1][j & 1], f, kf[j >> 1]); else mV_vv(d[u & 1][j & 1], f, kf[j >> 1]); },
                        [&](int j) { if (u > 0 && j >= 4 && j < 16 && (j - 4) % 3 == 0) { if (j == 4) asm volatile("s_nop 3" : "+v"(d[(u - 1) & 1][0]), "+v"(d[(u - 1) & 1][1])); epi(u - 1, (j - 4) / 3); }
                                     if (u < 4) { if (j >= 15 && (j & 3) == 3 && j < 31) dma_rows_q(c, rw1, h, 64 * (u + 3), (u + 3) & 3, (j - 15) >> 2); }
                                     else if (j >= 15 && (j & 1) && j < 29) xdma(u - 4, (u == 4 ? 4 : u == 5 ? 3 : 0) * SLICE, (j - 15) >> 1); });
        }
        MFW32(d[0][0], d[0][1]);
#pragma unroll
        for (int e = 0; e < 4; ++e) epi(6, e);
    }
    const u4 rout = mksrd(TYPE ? a.UF : a.GF, 64 * 16 * 28 * 1024);
    auto o_out = [&](int mm) { return mm > 13 ? OOB : h * 28672 + (TYPE ? (2 * mm + (l31 >> 4)) * 1024 : mm * 1024 + (l31 >> 4) * 14336) + (hi * 16 + (l31 & 15)) * 16; };
    const int o_st = o_out(m);
    const char LAS* xb[4];
#pragma unroll
    for (int k = 0; k < 4; ++k) xb[k] = c.sm + l31 * 896 + ((2 * k + hi) ^ ((l31 >> 1) & 7)) * 16;
    const int o_pf = (w == 1 && lane < 56) ? (vb >> 3) * 896 + lane * 16 : OOB;
    unsigned ring = 0x21034;
    auto mm1 = [&](f16v& acc, const u4& wt, u4 f, bool first) __attribute__((always_inline)) {
        if (TYPE) { if (first) mV_va0(acc, f, wt); else mV_va(acc, f, wt); } else { if (first) mV_av0(acc, wt, f); else mV_av(acc, wt, f); } };
    auto seg = [&](auto first_, auto par_, int i) __attribute__((always_inline)) {
        constexpr int FI = decltype(first_)::value, PAR = decltype(par_)::value;
        if (FI == 0) waitvm<23>(); else if (FI == 1) waitvm<9>(); else waitvm<18>();
        bar();
        st32(FI == 0 ? tag + 5 : FI == 1 ? tag + 7 : tag + 5 + i, rhb, o_fl, 0);
        dma_one(rx, o_pf, (i + PF_AHEAD) * 28672, c.smb + 4 * SLICE + 28672 + w * 1024);
        const int r0 = (ring & 7) * SLICE, r3 = ((ring >> 12) & 7) * SLICE;
        ring = (ring >> 4) | ((ring & 7) << 16);
        pipe<28, 4>([&](int k) { return *(const u4 LAS*)(xb[k & 3] + r0 + (k >> 2) * 128); },
                    [&](int k, u4 f) { mm1(d[PAR][0], wk[k], f, k == 0); },
                    [&](int k) { if (FI != 0 && (k == 2 || k == 4)) { if (k == 2) asm volatile("s_nop 5" : "+v"(d[PAR ^ 1][0])); st128(pack8(d[PAR ^ 1][0], k == 4), rout, o_st, (i - 1) * 458752 + (k == 4) * 512); }
                                 if (k >= 6 && k % 3 == 0 && k < 27) xdma(i + 3, r3, k / 3 - 2); });
    };
    seg(IntC<0>{}, IntC<0>{}, 0); seg(IntC<1>{}, IntC<1>{}, 1);
#pragma nounroll
    for (int i = 2; i < NSEG1; i += 2) { seg(IntC<2>{}, IntC<0>{}, i); seg(IntC<2>{}, IntC<1>{}, i + 1); }
    asm volatile("s_nop 11" : "+v"(d[1][0]));
    sst128(pack8(d[1][0], 0), rout, o_st, (NSEG1 - 1) * 458752); sst128(pack8(d[1][0], 1), rout, o_st, (NSEG1 - 1) * 458752 + 512);
    if (sub & 1) { set_flag(c, rhb, O_FW + ((h * 2 + TYPE) * 4 + sub) * 128, tag + 7 + NSEG); return; }
    wait_prog(rhb, lane < 2 ? O_FW + ((h * 2 + TYPE) * 4 + 1 + 2 * lane) * 128 : OOB, tag + 7);
    bar();
    if (w == 0) sst32(tag + 7 + NSEG1, rhb, o_fl, 0);
    const int pfo = lane < 56 ? ((vb >> 3 & 7) + (vb >> 7) * 8) * 1792 + lane * 16 : OOB;
    if (w == 3) {
        int xp[7];
#pragma unroll
        for (int k = 0; k < 7; ++k) { const int n = 64 * k + lane, row = (n * 1171) >> 16, pos = n - 56 * row; xp[k] = row * 896 + ((pos ^ ((row >> 1) & 7)) * 16); xo[k] -= 16 * 896; }
        auto segl = [&](auto first_, auto par_, int i) __attribute__((always_inline)) {
            constexpr int FI = decltype(first_)::value, PAR = decltype(par_)::value;
            waitvm<62>();
            bar();
            const int r0 = (ring & 7) * SLICE, r3 = ((ring >> 12) & 7) * SLICE;
            ring = (ring >> 4) | ((ring & 7) << 16);
            pipe<28, 4>([&](int k) { return *(const u4 LAS*)(xb[k & 3] + r0 + (k >> 2) * 128); },
                        [&](int k, u4 f) { mm1(d[PAR][0], wk[k], f, k == 0); },
                        [&](int k) { if (k == 0) { if (FI) { asm volatile("s_nop 9" : "+v"(d[PAR ^ 1][0])); st128(pack8(d[PAR ^ 1][0], 0), rout, o_st, (i - 1) * 458752); st128(pack8(d[PAR ^ 1][0], 1), rout, o_st, (i - 1) * 458752 + 512); }
                                                   dma_pair(rx, pfo, pfo - 128, (i + PF_AHEAD) * 28672, c.smb + 4 * SLICE + 28672); }
                                     dma_one(rx, (k / 7) & 1 ? xo[k % 7] : xp[k % 7], (i + 3) * 28672 + (k / 14) * 14336, c.smb + r3 + k * 1024); });
        };
        segl(IntC<0>{}, IntC<0>{}, NSEG1); segl(IntC<1>{}, IntC<1>{}, NSEG1 + 1);
#pragma nounroll
        for (int i = NSEG1 + 2; i < NSEG; i += 2) { segl(IntC<1>{}, IntC<0>{}, i); segl(IntC<1>{}, IntC<1>{}, i + 1); }
        asm volatile("s_nop 11" : "+v"(d[1][0]));
    } else {
        const int m2 = sub == 0 ? 4 + w : w == 2 ? 7 : 12 + w, t16 = 2 * m2 + (l31 >> 4);
        const int o_s2 = o_out(m2);
        u4 w2[28];
        {
            const u4 rin = mksrd(TYPE ? a.WvoF : a.WqkF, 28 * 16 * 14336);
            const int o_in = (TYPE ? t16 * 16 + h : h * 28 + t16) * 14336 + (hi * 16 + (TYPE ? l31 & 15 : perm16(l31 & 15))) * 16;
#pragma unroll
            for (int k = 0; k < 28; ++k) w2[k] = sld128(rin, o_in, k * 512);
            waitvm<0>();
#pragma unroll
            for (int k = 0; k < 28; ++k) {
                PIN(w2[k]);
                const auto s0 = __builtin_amdgcn_permlane32_swap(w2[k][0], w2[k][1], false, false), s1 = __builtin_amdgcn_permlane32_swap(w2[k][2], w2[k][3], false, false);
                w2[k] = (u4){s0[0], s1[0], s0[1], s1[1]};
                asm volatile("" : "+a"(w2[k]));
            }
        }
        auto seg2 = [&](auto first_, auto par_, int i) __attribute__((always_inline)) {
            constexpr int FI = decltype(first_)::value, PAR = decltype(par_)::value;
            waitvm<5>();
            bar();
            st32(tag + (i < NSEG1 + 2 ? 7 + NSEG1 : 5 + i), rhb, o_fl, 0);
            const int r0 = (ring & 7) * SLICE;
            ring = (ring >> 4) | ((ring & 7) << 16);
            pipe<28, 3>([&](int k) { return *(const u4 LAS*)(xb[k & 3] + r0 + (k >> 2) * 128); },
                        [&](int k, u4 f) { mm1(d[PAR][0], wk[k], f, k == 0); mm1(d[PAR][1], w2[k], f, k == 0); },
                        [&](int k) { if (FI && k >= 2 && k < 10 && (k & 1) == 0) { if (k == 2) asm volatile("s_nop 3" : "+v"(d[PAR ^ 1][0]), "+v"(d[PAR ^ 1][1]));
                                         st128(pack8(d[PAR ^ 1][k >= 6], (k >> 1) & 1), rout, k >= 6 ? o_s2 : o_st, (i - 1) * 458752 + ((k >> 1) & 1) * 512); } });
        };
        seg2(IntC<0>{}, IntC<0>{}, NSEG1); seg2(IntC<1>{}, IntC<1>{}, NSEG1 + 1);
#pragma nounroll
        for (int i = NSEG1 + 2; i < NSEG; i += 2) { seg2(IntC<1>{}, IntC<0>{}, i); seg2(IntC<1>{}, IntC<1>{}, i + 1); }
        MFW32(d[1][0], d[1][1]);
        sst128(pack8(d[1][1], 0), rout, o_s2, 63 * 458752); sst128(pack8(d[1][1], 1), rout, o_s2, 63 * 458752 + 512);
    }
    sst128(pack8(d[1][0], 0), rout, o_st, 63 * 458752); sst128(pack8(d[1][0], 1), rout, o_st, 63 * 458752 + 512);
    set_flag(c, rhb, O_FW + ((h * 2 + TYPE) * 4 + sub) * 128, tag + 7 + NSEG);
}

DI void x_image(const SideCtx& c, u4 rx, int s) {
#pragma unroll
    for (int k = 0; k < 7; ++k) {
        const int n = 64 * (7 * (c.wave & 1) + k) + c.lane, row = (n * 1171) >> 16, pos = n - 56 * row;
        dma_one(rx, row * 896 + ((pos ^ ((row >> 1) & 7)) * 16), (s * 32 + 16 * (c.wave >> 1)) * 896, c.smb + 40960 + (7 * c.wave + k) * 1024);
    }
}
DI void e_task(unsigned tag, int wave, int s, int hg) {
    CArgs& a = get_args();
    const SideCtx c = make_ctx(wave);
    const int lane = c.lane, hi = lane >> 5, l31 = lane & 31;
    s = __builtin_amdgcn_readfirstlane(s); hg = __builtin_amdgcn_readfirstlane(hg); asm volatile("" : "+s"(s), "+s"(hg));
    const int sh = s * 16 + 4 * hg + c.wave;
    const u4 rhb = mksrd(a.HB, HB_SZ), rg = mksrd(a.GF, 64 * 16 * 28 * 1024);
    bar();
    x_image(c, mksrd(a.X, 2048 * 896), s);
    wait_prog(rhb, lane < 4 ? O_FW + ((4 * hg + c.wave) * 8 + lane) * 128 : OOB, tag + 8 + s);
    u4 g[28];
    const int o_g = (l31 >> 4) * 14336 + (hi * 16 + (l31 & 15)) * 16;
#pragma unroll
    for (int k = 0; k < 28; ++k) asm volatile("buffer_load_dwordx4 %0, %1, %2, %3 offen sc1" : "=a"(g[k]) : "v"(o_g), "s"(rg), "s"(sh * 28672 + k * 512) : "memory");
    bar();
    f16v sx;
    pipe<28, 4>([&](int k) { const char LAS* b = c.sm + 40960 + l31 * 896 + hi * 8 + (k >> 2) * 128;
                             const u2 c0 = *(const u2 LAS*)(b + ((2 * (k & 3)) ^ ((l31 >> 1) & 7)) * 16), c1 = *(const u2 LAS*)(b + ((2 * (k & 3) + 1) ^ ((l31 >> 1) & 7)) * 16);
                             return (u4){c0[0], c0[1], c1[0], c1[1]}; },
                [&](int k, u4 f) { if (k == 0) WAITV(21, "+a"(g[0]), "+a"(g[1]), "+a"(g[2]), "+a"(g[3]), "+a"(g[4]), "+a"(g[5]), "+a"(g[6]));
                                   if (k == 7) WAITV(14, "+a"(g[7]), "+a"(g[8]), "+a"(g[9]), "+a"(g[10]), "+a"(g[11]), "+a"(g[12]), "+a"(g[13]));
                                   if (k == 14) WAITV(7, "+a"(g[14]), "+a"(g[15]), "+a"(g[16]), "+a"(g[17]), "+a"(g[18]), "+a"(g[19]), "+a"(g[20]));
                                   if (k == 21) WAITV(0, "+a"(g[21]), "+a"(g[22]), "+a"(g[23]), "+a"(g[24]), "+a"(g[25]), "+a"(g[26]), "+a"(g[27]));
                                   u4 y = {f[0], f[2], f[1], f[3]}; asm volatile("s_nop 1" : "+v"(y));
                                   if (k == 0) mV_av0(sx, g[k], y); else mV_av(sx, g[k], y); },
                [&](int) {});
    asm volatile("s_nop 11" : "+v"(sx));
    float m = -3.0e38f;
#pragma unroll
    for (int j = 0; j < 16; ++j) { const int key = (j & 3) + 8 * (j >> 2) + 4 * hi; if (key > l31) sx[j] = -INFINITY; m = fmaxf(m, sx[j]); }
    m = x32max(m);
    float z = 0.f;
#pragma unroll
    for (int j = 0; j < 16; ++j) { sx[j] = __builtin_amdgcn_exp2f(sx[j] - m); z += sx[j]; }
    z = x32add(z);
    const u4 rex = mksrd(a.EX, 64 * 16 * 2048), rmz = mksrd(a.MZX, 64 * 16 * 256);
    sst128(pack8(sx, 0), rex, lane * 16, sh * 2048); sst128(pack8(sx, 1), rex, lane * 16, sh * 2048 + 1024);
    sst64((u2){__float_as_uint(m), __float_as_uint(z)}, rmz, lane < 32 ? lane * 8 : OOB, sh * 256);
    set_flag(c, rhb, O_FE + (s * 4 + hg) * 4, tag + 1);
}
#define P_XSLP 0
DI void slp(int n) {
#pragma nounroll
    for (int i = 0; i < n; i += 8) asm volatile("s_sleep 8");
}
DI void x_task(unsigned tag, int wave, int s, int cb) {
    CArgs& a = get_args();
    const SideCtx c = make_ctx(wave);
    const int lane = c.lane, hi = lane >> 5, l31 = lane & 31, w = c.wave;
    s = __builtin_amdgcn_readfirstlane(s); cb = __builtin_amdgcn_readfirstlane(cb); asm volatile("" : "+s"(s), "+s"(cb));
    const u4 rhb = mksrd(a.HB, HB_SZ);
    const u4 ru = mksrd(a.UF, 64 * 16 * 28 * 1024), rex = mksrd(a.EX, 64 * 16 * 2048), rmz = mksrd(a.MZX, 64 * 16 * 256);
    const u4 rum = mksrd(a.UM, 65 * 28 * 4096), rtf = mksrd(a.TF, 65 * 32 * 3584), rx = mksrd(a.X, 2048 * 896);
    bar();
    x_image(c, rx, s);
    wait_prog(rhb, lane < 16 ? O_FW + (((4 * w + (lane >> 2)) * 2 + 1) * 4 + (lane & 3)) * 128 : lane == 16 ? O_FE + (s * 4 + w) * 4 : OOB, lane < 16 ? tag + 8 + s : tag + 1);
    u4 uf[4][2][2], ex[4][2]; u2 mz[4];
    const int o_u = (l31 >> 4) * 1024 + hi * 256 + (l31 & 15) * 16;
#pragma unroll
    for (int k = 0; k < 4; ++k) {
        const int sh = s * 16 + 4 * w + k;
#pragma unroll
        for (int sg = 0; sg < 2; ++sg) {
            ex[k][sg] = sld128(rex, lane * 16, sh * 2048 + sg * 1024);
#pragma unroll
            for (int nt = 0; nt < 2; ++nt) uf[k][sg][nt] = sld128(ru, o_u, (sh * 28 + 4 * cb + 2 * nt) * 1024 + sg * 512);
        }
        mz[k] = sld64(rmz, l31 * 8, sh * 256);
        asm volatile("s_sleep %0" :: "i"(P_XSLP));
    }
    waitvm<0>();
    f16v rr[4][2];
#pragma unroll
    for (int k = 0; k < 4; ++k) {
        asm volatile("" : "+v"(mz[k]), "+v"(ex[k][0]), "+v"(ex[k][1]), "+v"(uf[k][0][0]), "+v"(uf[k][0][1]), "+v"(uf[k][1][0]), "+v"(uf[k][1][1]));
        mV_vv0(rr[k][0], uf[k][0][0], ex[k][0]); mV_vv0(rr[k][1], uf[k][0][1], ex[k][0]);
        mV_vv(rr[k][0], uf[k][1][0], ex[k][1]); mV_vv(rr[k][1], uf[k][1][1], ex[k][1]);
        MFW32(rr[k][0], rr[k][1]);
    }
    bar();
    wait_prog(rhb, lane < 8 ? O_FT + (8 * w + lane) * 128 : lane < 12 ? O_FT + (24 + 4 * cb + lane) * 128 : OOB, tag + s + 1);
    u4 tf[28]; u4 um[2][2];
    const int o_tf = (l31 >> 3) * 7168 + hi * 128 + (l31 & 7) * 16, o_um = (l31 >> 4) * 4096 + hi * 256 + (l31 & 15) * 16;
#pragma unroll
    for (int k = 0; k < 28; ++k) {
        asm volatile("buffer_load_dwordx4 %0, %1, %2, %3 offen sc1" : "=a"(tf[k]) : "v"(o_tf), "s"(rtf), "s"(((s + 1) * 32 + 8 * w) * 3584 + k * 256) : "memory");
        if (k % 7 == 6) slp(s < 58 ? P_XSLP : 0);
    }
#pragma unroll
    for (int k = 0; k < 4; ++k) um[k >> 1][k & 1] = ld128(rum, o_um, (s * 28 + 28 + 4 * cb + 2 * (k & 1)) * 4096 + w * 1024 + (k >> 1) * 512);
    f16v sm_;
    pipe<28, 4>([&](int k) { return *(const u4 LAS*)(c.sm + 40960 + l31 * 896 + (((2 * (k & 3) + hi) ^ ((l31 >> 1) & 7)) * 16) + (k >> 2) * 128); },
                [&](int k, u4 f) { if (k == 0) WAITV(25, "+a"(tf[0]), "+a"(tf[1]), "+a"(tf[2]), "+a"(tf[3]), "+a"(tf[4]), "+a"(tf[5]), "+a"(tf[6]));
                                   if (k == 7) WAITV(18, "+a"(tf[7]), "+a"(tf[8]), "+a"(tf[9]), "+a"(tf[10]), "+a"(tf[11]), "+a"(tf[12]), "+a"(tf[13]));
                                   if (k == 14) WAITV(11, "+a"(tf[14]), "+a"(tf[15]), "+a"(tf[16]), "+a"(tf[17]), "+a"(tf[18]), "+a"(tf[19]), "+a"(tf[20]));
                                   if (k == 21) WAITV(4, "+a"(tf[21]), "+a"(tf[22]), "+a"(tf[23]), "+a"(tf[24]), "+a"(tf[25]), "+a"(tf[26]), "+a"(tf[27]));
                                   if (k == 0) mV_av0(sm_, tf[k], f); else mV_av(sm_, tf[k], f); },
                [&](int) {});
    waitvm<0>();
    asm volatile("s_nop 11" : "+v"(sm_));
#pragma unroll
    for (int k = 0; k < 4; ++k) PIN(um[k >> 1][k & 1]);
    u2 pm[4]; float scx[4];
#pragma unroll
    for (int k = 0; k < 4; ++k) {
        const f4 sc = {sm_[4 * k], sm_[4 * k + 1], sm_[4 * k + 2], sm_[4 * k + 3]};
        const float mx = __uint_as_float(mz[k][0]);
        float m = fmaxf(x32max(fmaxf(fmaxf(sc[0], sc[1]), fmaxf(sc[2], sc[3]))), mx);
        float e[4];
#pragma unroll
        for (int j = 0; j < 4; ++j) e[j] = __builtin_amdgcn_exp2f(sc[j] - m);
        const float cx = __builtin_amdgcn_exp2f(mx - m);
        const float z = x32add((e[0] + e[1]) + (e[2] + e[3])) + cx * __uint_as_float(mz[k][1]);
        const float inv = __builtin_amdgcn_rcpf(z);
        scx[k] = cx * inv;
        pm[k] = (u2){pk2(e[0] * inv, e[1] * inv), pk2(e[2] * inv, e[3] * inv)};
    }
    f16v d0, d1;
    {
        u4 p0 = {pm[0][0], pm[0][1], pm[2][0], pm[2][1]}, p1 = {pm[1][0], pm[1][1], pm[3][0], pm[3][1]};
        asm volatile("s_nop 1\n\tv_mfma_f32_32x32x16_bf16 %0, %2, %6, 0\n\tv_mfma_f32_32x32x16_bf16 %1, %3, %6, 0\n\tv_mfma_f32_32x32x16_bf16 %0, %4, %7, %0\n\tv_mfma_f32_32x32x16_bf16 %1, %5, %7, %1"
                     : "=&v"(d0), "=&v"(d1) : "v"(um[0][0]), "v"(um[0][1]), "v"(um[1][0]), "v"(um[1][1]), "v"(p0), "v"(p1));
    }
    f16v a0 = (rr[0][0] * scx[0] + rr[1][0] * scx[1]) + (rr[2][0] * scx[2] + rr[3][0] * scx[3]);
    f16v a1 = (rr[0][1] * scx[0] + rr[1][1] * scx[1]) + (rr[2][1] * scx[2] + rr[3][1] * scx[3]);
    asm volatile("s_nop 11" : "+v"(d0), "+v"(d1), "+v"(a0), "+v"(a1));
    a0 += d0; a1 += d1;
#pragma unroll
    for (int g = 0; g < 4; ++g) {
        *(f4 LAS*)(c.sm + w * 8704 + l31 * 272 + (8 * g + 4 * hi) * 4) = (f4){a0[4 * g], a0[4 * g + 1], a0[4 * g + 2], a0[4 * g + 3]};
        *(f4 LAS*)(c.sm + w * 8704 + l31 * 272 + 128 + (8 * g + 4 * hi) * 4) = (f4){a1[4 * g], a1[4 * g + 1], a1[4 * g + 2], a1[4 * g + 3]};
    }
    bar();
    {
        const int t = w * 8 + (lane >> 3), c8 = lane & 7;
        f4 s0 = {0.f, 0.f, 0.f, 0.f}, s1 = s0;
#pragma unroll
        for (int p = 0; p < 4; ++p) { s0 += *(const f4 LAS*)(c.sm + p * 8704 + t * 272 + c8 * 32); s1 += *(const f4 LAS*)(c.sm + p * 8704 + t * 272 + c8 * 32 + 16); }
        sst128((u4){pk2(s0[0], s0[1]), pk2(s0[2], s0[3]), pk2(s1[0], s1[1]), pk2(s1[2], s1[3])}, mksrd(a.out, 2048 * 1792), t * 1792 + c8 * 16, s * 32 * 1792 + 896 + cb * 128);
    }
}

#define P_ASLP 8
#define PA_UT 0
#define PA_TU 33792
#define PA_KH (PA_TU + 35456)
#define PA_KL (PA_KH + 7680)
#define PA_KT (PA_KL + 7680)
#define PA_RED (PA_KT + 12288)
DI float bf2f(u16 x) { return __uint_as_float((unsigned)x << 16); }
DI void parta_task(unsigned tag, int wave, int task) {
    CArgs& a = get_args();
    const SideCtx c = make_ctx(wave);
    char LAS* sm = c.sm;
    const int lane = c.lane, w = c.wave, tid = w * 64 + lane, hi = lane >> 5, l31 = lane & 31, l15 = lane & 15, kk = lane >> 4;
    task = __builtin_amdgcn_readfirstlane(task); asm volatile("" : "+s"(task));
    const int d0 = task * 4;
    const u4 rpa = mksrd(a.PA, 56 * 32768);
    bar();
#pragma unroll
    for (int i = 0; i < 8; ++i) {
        const int l = tid + 256 * i;
        const u2 v = *(const u2*)(a.Au + (size_t)l * 448 + d0);
#pragma unroll
        for (int ch = 0; ch < 4; ++ch) *(u16 LAS*)(sm + PA_UT + ch * 4224 + (2048 - l) * 2) = (u16)(v[ch >> 1] >> (16 * (ch & 1)));
    }
    { const int i = tid, ch = i >> 6, m = i & 63; *(u16 LAS*)(sm + PA_UT + ch * 4224 + (m == 0 ? 0 : (2048 + m) * 2)) = 0; }
    for (int i = tid; i < 2 * 31 * 5; i += 256) { const int x = i / 155, r = i % 155; *(u4 LAS*)(sm + (x ? PA_KL : PA_KH) + r * 16) = (u4){0u, 0u, 0u, 0u}; }
    int toff[8];
#pragma unroll
    for (int j = 0; j < 8; ++j) {
        const int p = (int)bfr((float)(tid * 8 + j));
        int col, ar = (p & 255) >> 4;
        if (p < 256) col = p & 15; else if (p < 512) col = 16 + ((p & 15) >> 1); else if (p < 1024) col = 24 + ((p - 512) >> 8) * 4 + ((p & 15) >> 2);
        else if (p < 2048) col = 32 + ((p - 1024) >> 8) * 2 + ((p & 15) >> 3); else col = 40;
        toff[j] = (col * 16 + ar) * 4;
    }
    float qpos[3];
    qpos[0] = (float)l15;
    qpos[1] = l15 < 8 ? (float)(256 + 2 * l15) : (float)(512 + ((l15 - 8) >> 2) * 256 + (l15 & 3) * 4);
    qpos[2] = l15 < 8 ? (float)(1024 + (l15 >> 1) * 256 + (l15 & 1) * 8) : 2048.f;
    const float ppos = (float)(16 * l15);
    {
        {
            const int d = d0 + w;
            const float dt = bfr(expf(bf2f(a.Alogdt[d])));
            char LAS* pt = sm + PA_RED + w * 9216;
#pragma unroll
            for (int i = 0; i < 4; ++i) {
                const int n = lane + 64 * i;
                const float ar = -bfr(expf(bf2f(a.AlogA[d * 256 + n])));
                *(f4 LAS*)(pt + n * 16) = (f4){ar * dt * LOG2E, bf2f(a.Aim[d * 256 + n]) * dt * 0.15915494309189535f, 2.f * bf2f(a.ACr[d * 256 + n]) * dt, 2.f * bf2f(a.ACi[d * 256 + n]) * dt};
            }
            f4 acc[3] = {{0.f, 0.f, 0.f, 0.f}, {0.f, 0.f, 0.f, 0.f}, {0.f, 0.f, 0.f, 0.f}};
#pragma unroll 2
            for (int st = 0; st < 64; ++st) {
                const f4 z = *(const f4 LAS*)(pt + (4 * st + kk) * 16);
                float er, ei;
                { const float ex = __builtin_amdgcn_exp2f(z[0] * ppos); const float rv = __builtin_amdgcn_fractf(z[1] * ppos);
                  er = ex * __builtin_amdgcn_cosf(rv); ei = ex * __builtin_amdgcn_sinf(rv); }
                const float are = z[2] * er - z[3] * ei, aim = -(z[2] * ei + z[3] * er);
#pragma unroll
                for (int t = 0; t < 3; ++t) {
                    const float ex = __builtin_amdgcn_exp2f(z[0] * qpos[t]); const float rv = __builtin_amdgcn_fractf(z[1] * qpos[t]);
                    const float br = ex * __builtin_amdgcn_cosf(rv), bi = ex * __builtin_amdgcn_sinf(rv);
                    acc[t] = __builtin_amdgcn_mfma_f32_16x16x4f32(are, br, acc[t], 0, 0, 0);
                    acc[t] = __builtin_amdgcn_mfma_f32_16x16x4f32(aim, bi, acc[t], 0, 0, 0);
                }
            }
#pragma unroll
            for (int t = 0; t < 3; ++t) *(f4 LAS*)(sm + PA_KT + w * 3072 + ((t * 16 + l15) * 16 + 4 * kk) * 4) = acc[t];
        }
        bar();
#pragma nounroll
        for (int cc = 0; cc < 4; ++cc) {
            const int ch = cc;
            {
                float kv[8];
#pragma unroll
                for (int j = 0; j < 8; ++j) kv[j] = *(const float LAS*)(sm + PA_KT + cc * 3072 + toff[j]);
                u4 h4, l4;
#pragma unroll
                for (int j = 0; j < 4; ++j) { h4[j] = pk2(kv[2 * j], kv[2 * j + 1]);
                    l4[j] = pk2(kv[2 * j] - __uint_as_float(h4[j] << 16), kv[2 * j + 1] - __uint_as_float(h4[j] & 0xffff0000u)); }
                const int o = (31 + (tid >> 2)) * 80 + (tid & 3) * 16;
                *(u4 LAS*)(sm + PA_KH + o) = h4; *(u4 LAS*)(sm + PA_KL + o) = l4;
            }
            for (int i = tid; i < 262; i += 256) {
                const u4 x0 = *(const u4 LAS*)(sm + PA_UT + ch * 4224 + i * 16), x1 = *(const u4 LAS*)(sm + PA_UT + ch * 4224 + i * 16 + 16);
                const unsigned r[8] = {x0[0], x0[1], x0[2], x0[3], x1[0], x1[1], x1[2], x1[3]};
#pragma unroll
                for (int cp = 0; cp < 8; ++cp) {
                    u4 o;
#pragma unroll
                    for (int j = 0; j < 4; ++j) o[j] = (cp & 1) ? __builtin_amdgcn_alignbit(r[(cp >> 1) + j + 1], r[(cp >> 1) + j], 16) : r[(cp >> 1) + j];
                    *(u4 LAS*)(sm + PA_TU + (cp * 276 + (cp ? 1 : 0) + i) * 16) = o;
                }
            }
            bar();
            {
                f16v y0, y1;
                const int m00 = 2048 - l31 + 8 * hi, cp = m00 & 7;
                const char LAS* ab = sm + PA_TU + (cp * 276 + (cp ? 1 : 0) + (m00 >> 3)) * 16 - 63 * 64;
                const char LAS* bb = sm + PA_KH + l31 * 80 + hi * 16;
                const char LAS* a1w = ab - w * 64; const char LAS* b1w = bb - w * 80;
                const char LAS* a2w = ab + w * 64; const char LAS* b2w = bb + w * 80;
                u4 fr[2][4];
                auto rdg = [&](int g, int sl) __attribute__((always_inline)) {
                    const int i = (g & 15) >> 1, ks = g & 1;
                    if (g < 16) { fr[sl][0] = *(const u4 LAS*)(b1w + (31 - 4 * i) * 80 + ks * 32); fr[sl][2] = *(const u4 LAS*)(a1w + (63 - 4 * i) * 64 + ks * 32);
                                  fr[sl][3] = *(const u4 LAS*)(a1w + (31 - 4 * i) * 64 + ks * 32); fr[sl][1] = *(const u4 LAS*)(b1w + 7680 + (31 - 4 * i) * 80 + ks * 32); }
                    else { fr[sl][0] = *(const u4 LAS*)(b2w + (32 + 4 * i) * 80 + ks * 32); fr[sl][3] = *(const u4 LAS*)(a2w + (32 + 4 * i) * 64 + ks * 32);
                           fr[sl][1] = *(const u4 LAS*)(b2w + 7680 + (32 + 4 * i) * 80 + ks * 32); }
                };
                rdg(0, 0); SB();
#pragma unroll
                for (int g = 0; g < 32; ++g) {
                    const int sl = g & 1;
                    if (g + 1 < 32) rdg(g + 1, sl ^ 1);
                    SB();
                    if (g == 0) { mV_vv0(y0, fr[sl][2], fr[sl][0]); mV_vv0(y1, fr[sl][3], fr[sl][0]); }
                    else { if (g < 16) mV_vv(y0, fr[sl][2], fr[sl][0]); mV_vv(y1, fr[sl][3], fr[sl][0]); }
                    if (g < 16) mV_vv(y0, fr[sl][2], fr[sl][1]);
                    mV_vv(y1, fr[sl][3], fr[sl][1]);
                    SB();
                }
                MFW32(y0, y1);
#pragma unroll
                for (int g = 0; g < 4; ++g) {
                    *(f4 LAS*)(sm + PA_RED + w * 9216 + l31 * 144 + (8 * g + 4 * hi) * 4) = (f4){y0[4 * g], y0[4 * g + 1], y0[4 * g + 2], y0[4 * g + 3]};
                    *(f4 LAS*)(sm + PA_RED + w * 9216 + (32 + l31) * 144 + (8 * g + 4 * hi) * 4) = (f4){y1[4 * g], y1[4 * g + 1], y1[4 * g + 2], y1[4 * g + 3]};
                }
            }
            bar();
            {
                f4 s0 = {0.f, 0.f, 0.f, 0.f}, s1 = s0;
#pragma unroll
                for (int p = 0; p < 4; ++p) { const char LAS* q_ = sm + PA_RED + p * 9216 + (tid >> 2) * 144 + (tid & 3) * 32; s0 += *(const f4 LAS*)q_; s1 += *(const f4 LAS*)(q_ + 16); }
                const float dsk = bf2f(a.ADsk[d0 + ch]);
                float yv[8];
#pragma unroll
                for (int j = 0; j < 8; ++j) yv[j] = bfr(j < 4 ? s0[j & 3] : s1[j & 3]) + bfr(bf2f(*(const u16 LAS*)(sm + PA_UT + ch * 4224 + (2048 - (tid * 8 + j)) * 2)) * dsk);
                sst128((u4){pk2(yv[0], yv[1]), pk2(yv[2], yv[3]), pk2(yv[4], yv[5]), pk2(yv[6], yv[7])}, rpa, tid * 16, (d0 + ch) * 4096);
            }
            bar();
        }
    }
    set_flag(c, mksrd(a.HB, HB_SZ), O_FPA + task * 4, tag + 1);
}
DI void pa_out(unsigned tag, int wave, int t) {
    CArgs& a = get_args();
    const SideCtx c = make_ctx(wave);
    const int lane = c.lane, tid = c.wave * 64 + lane;
    t = __builtin_amdgcn_readfirstlane(t); asm volatile("" : "+s"(t));
    const u4 rpa = mksrd(a.PA, 56 * 32768), ro = mksrd(a.out, 2048 * 1792);
    wait_prog(mksrd(a.HB, HB_SZ), lane < 16 ? O_FPA + (t & ~7) * 8 + lane * 4 : OOB, tag + 1);
    u4 v[8];
#pragma unroll
    for (int i = 0; i < 8; ++i) v[i] = sld128(rpa, (tid >> 5) * 4096 + (tid & 31) * 16, ((t >> 3) * 64 + i * 8) * 4096 + (t & 7) * 512);
    WAITV(0, "+v"(v[0]), "+v"(v[1]), "+v"(v[2]), "+v"(v[3]), "+v"(v[4]), "+v"(v[5]), "+v"(v[6]), "+v"(v[7]));
    bar();
#pragma unroll
    for (int i = 0; i < 8; ++i) *(u4 LAS*)(c.sm + (i * 8 + (tid >> 5)) * 528 + (tid & 31) * 16) = v[i];
    bar();
#pragma unroll
    for (int k = 0; k < 8; ++k) {
        const char LAS* p = c.sm + (tid & 7) * 8 * 528 + ((tid >> 3) + 32 * k) * 2;
        u4 o;
#pragma unroll
        for (int j = 0; j < 4; ++j) o[j] = (unsigned)*(const u16 LAS*)(p + (2 * j) * 528) | ((unsigned)*(const u16 LAS*)(p + (2 * j + 1) * 528) << 16);
        sst128(o, ro, (tid >> 3) * 1792 + (tid & 7) * 16, ((t & 7) * 256 + 32 * k) * 1792 + (t >> 3) * 128);
        asm volatile("s_sleep %0" :: "i"(P_ASLP));
    }
}
#define L_TAB 0
#define L_CLK 32768
#define L_MEM 33792
#define L_WRK 49152
DI void edge(unsigned& ce, unsigned& ne) {
    const unsigned n0 = rt_now(); u64 t, c; int n = 0;
#pragma nounroll
    do { asm volatile("s_memrealtime %0\n\ts_memtime %1\n\ts_waitcnt lgkmcnt(0)" : "=&s"(t), "=&s"(c) :: "memory"); } while ((unsigned)t == n0 && ++n < 64);
    ce = (unsigned)c - 30u; ne = (unsigned)t;
}
DI unsigned srdw(const void* p, unsigned bytes, int i) { const u64 x = (u64)p; return i == 0 ? (unsigned)x : i == 1 ? (unsigned)(x >> 32) & 0xffffu : i == 2 ? bytes : 0x00020000u; }
#define RUN(r) asm volatile("v_mov_b32 v255, %0\n\ts_getpc_b64 vcc\n.Lfk_from_" #r ":\n\ts_add_u32 vcc_lo, vcc_lo, fk_role_" #r "-.Lfk_from_" #r "\n\ts_addc_u32 vcc_hi, vcc_hi, 0\n\ts_setpc_b64 vcc" :: "v"(tab) : "memory", "vcc", "scc", "s99", "v255", "a255")
DI void chain_role(unsigned tag, int vb, int W, int role, int id, int xh) {
    CArgs& a = get_args();
    char LAS* sm = (char LAS*)smem_;
    const unsigned smb = (unsigned)(size_t)sm;
    const int lane = fresh_lane(), q = lane >> 4, l15 = lane & 15, i8 = lane & 7, hb = (lane >> 3) & 1;
    unsigned LAS* tb = (unsigned LAS*)(sm + L_TAB + W * 8192) + lane;
    const u4 r = mksrd(a.HB, HB_SZ);
    unsigned c0, n0, c1, n1; edge(c0, n0);
    if (role == 0 && W == 0) {
        const int o = xh * (2 << 20) + O_PROBE + (id & 15) * 128; unsigned best = 1016;
#pragma nounroll
        for (int rep = 0; rep < 2; ++rep) {
            sst32(rep, r, lane < 4 ? o + lane * 8192 : OOB, 0); waitvm<0>();
#pragma nounroll
            for (int k = 0; k < 4; ++k) {
                const int kk = (k + id) & 3, ol = lane == 0 ? o + kk * 8192 : OOB;
                const unsigned t0 = now32(); unsigned v = ld32(r, ol, 0); WAITV(0, "+v"(v));
                const unsigned d = now32() - t0;
                if (lane == kk) best = min(best, d);
            }
        }
        sst32(tag + 1 + (best >> 2), r, lane < 4 ? O_PAR + id * 16 + lane * 4 : OOB, 0);
    } else if (role) {
        unsigned v = sld32(mksrd(role == 1 ? a.UM : W == 0 ? a.GF : W == 1 ? a.UF : a.TF, role == 1 ? 65 * 28 * 4096 : W < 2 ? 64 * 16 * 28 * 1024 : 65 * 32 * 3584), lane << 19, 0); WAITV(0, "+v"(v));
    }
    const int cp = xh * 32768;
    const void *p1, *p2 = a.UF; unsigned x1 = 0, x2 = 0, po = 0; int poi, fw, wk;
    if (role == 0) {
        const int part = id & 1, h = id >> 1, rr = l15 >> 2, c = lane & 3;
        const int bq = q == 0 ? 0 : q == 1 ? 528 : q == 2 ? 1152 : 1680;
        tb[8 * 64] = O_MEM + cp + lane * 16 + part * 4096; tb[9 * 64] = O_MEM + cp + lane * 16 + (1 - part) * 4096; tb[10 * 64] = O_MEM + cp + lane * 16 + 3072;
        tb[11 * 64] = smb + L_WRK + bq + l15 * 8 + (W == 3 ? 3 * 2304 + part * 256 : 0); tb[14 * 64] = smb + L_WRK + bq + l15 * 8 + 3 * 2304 + (1 - part) * 256;
        tb[12 * 64] = smb + L_WRK + (c == 0 ? 0 : c == 2 ? 528 : c == 1 ? 1152 : 1680) + (16 * (q >> 1) + 4 * (q & 1) + 8 * (rr >> 1) + (rr & 1)) * 8;
        const int op = O_SMM + id * 256 + hb * 128 + (4 * (q & 1) + 2 * (q >> 1)) * 16 + i8 * 4;
        tb[13 * 64] = op + cp; tb[15 * 64] = op + (cp ^ 32768);
        int tile, chunk;
        if (W < 3) {
            const int i = lane < 28 ? lane : 0;
            const int p = i < 8 ? i : i < 19 ? (int)(0x43424130210ull >> (4 * (i - 8))) & 7 : 5 + (i - 19) / 3, k = i < 8 ? 0 : i < 19 ? (int)(0x32221212111ull >> (4 * (i - 8))) & 7 : 1 + (i - 19) % 3;
            const int n = 3 * (p & 3) + W;
            tile = 4 * (4 * (p < 4 ? part : 1 - part) + (n >> 2)) + (n & 3); chunk = k < 3 ? 4 * part + (W + k) % 3 : 3;
        } else {
            const int p = lane < 2 ? 2 + lane : (lane - 2) & 3, k = lane < 2 || lane >= 14 ? 3 : (lane - 2) >> 2;
            tile = 12 + (p < 2 ? 2 * part + p : 2 * (1 - part) + p - 2); chunk = k < 3 ? 4 * part + k : 3;
        }
        wk = ((h * 28 + tile) * 7 + chunk) * 2048; fw = h * 8 + (lane & 3);
        poi = (W == 0 && q < 2) ? O_SMM + q * 32768 + id * 256 + l15 * 16 : OOB;
        p1 = a.WqkF; x1 = 4 * part + 1;
    } else if (role == 1) {
        const int hc = 4 * W + ((q & 1) << 1) + (q >> 1), k = i8 - 4 * hb;
        const int ocp = W == 0 ? O_MEM : W == 3 ? O_MEM + 32768 : O_MEMX + (W - 1) * 32768;
        tb[8 * 64] = O_MEM + cp + lane * 16; tb[9 * 64] = O_MEM + cp + lane * 16 + 4096;
        tb[10 * 64] = O_SMM + cp + hc * 512 + l15 * 16; tb[11 * 64] = O_MZ + hc * 128 + l15 * 8;
        tb[12 * 64] = RXW(id >> 1) + (id & 1) * 4096 + hc * 256 + l15 * 16;
        tb[13 * 64] = ocp + id * 256 + lane * 4;
        tb[14 * 64] = k == 0 ? 0xffffu : k == 1 ? 0xffff0000u : 0u; tb[15 * 64] = k == 2 ? 0xffffu : k == 3 ? 0xffff0000u : 0u;
        tb[16 * 64] = smb + L_WRK + W * 512 + lane * 8;
        tb[17 * 64] = smb + L_WRK + ((lane & 32) + (lane & 2) * 8 + (lane & 1) * 8 + ((lane >> 2) & 7)) * 8;
        tb[18 * 64] = id * 4096 + W * 1024 + lane * 16;
        tb[19 * 64] = san(*(const unsigned*)(a.mem0 + ((lane >> 2) & 7) * 448 + id * 16 + (lane >> 5) * 8 + (lane & 3) * 2));
        tb[20 * 64] = smb + L_MEM + lane * 16;
        tb[21 * 64] = lane == 0 ? O_FT + (32 + id) * 128 : OOB;
        tb[23 * 64] = (q == 0 ? O_MEM : q == 3 ? O_MEM + 32768 : O_MEMX + (q - 1) * 32768) + id * 256 + l15 * 16;
        const bool lead = id == 14 * xh;
        tb[22 * 64] = lane != 0 ? OOB : W == 2 ? (lead ? O_REC + xh * 8 : OOB) : id == 0 ? O_EPOCH : OOB;
        if (W == 3) sst64((u2){REC_A, REC_B}, r, lead && lane < 3 ? O_REC + lane * 128 + xh * 8 : OOB, 0);
        const int ord = ((W == 0 ? 0x6543210 : W == 1 ? 0x3106542 : W == 2 ? 0x4321065 : 0x6542103) >> (4 * (lane % 7))) & 7;
        wk = ((id * 16 + 4 * W + (lane / 7 & 3)) * 7 + ord) * 2048; fw = (4 * W + (lane & 3)) * 8 + 4 + (id >> 3);
        poi = q == 0 ? ocp + id * 256 + l15 * 16 : OOB;
        p1 = a.WvoF; p2 = a.UM; po = OFF_P;
    } else {
        const int h = id >> 1, half = id & 1, pt = 4 * W + q;
        tb[8 * 64] = O_MEMX + cp + lane * 16; tb[9 * 64] = O_MEMX + cp + lane * 16 + 4096;
        tb[10 * 64] = (W < 3 || (q & 1) == 0) ? RXW(half * 7 + 2 * W + (q & 1)) + (q >> 1) * 4096 + h * 256 + l15 * 16 : OOB;
        tb[11 * 64] = ((lane & 3) == 0 && half == 0) ? O_MZ + h * 128 + (lane >> 2) * 8 : OOB;
        tb[12 * 64] = smb + L_WRK + lane * 8;
        tb[13 * 64] = smb + L_WRK + ((q >> 1) * 32 + (lane & 1) * 16 + 4 * (q & 1) + (l15 >> 2)) * 8;
        poi = pt < 14 ? RXW(half * 7 + (pt >> 1)) + (pt & 1) * 4096 + h * 256 + l15 * 16 : (pt == 14 && half == 0 && l15 < 8) ? O_MZ + h * 128 + l15 * 16 : OOB;
        tb[14 * 64] = poi;
        tb[15 * 64] = O_FW + (h * 8 + i8) * 128;
        tb[16 * 64] = smb + L_MEM + lane * 16;
        tb[17 * 64] = smb + L_WRK + 1024 + W * 2048 + ((q >> 1) * 16 + l15) * 16 + (q & 1) * 8;
        tb[18 * 64] = smb + L_WRK + 1024 + W * 2048 + (8 * q + (l15 >> 2) + 32 * ((lane >> 1) & 1)) * 16 + (lane & 1) * 8;
        const int otf = id * 3584 + W * 1024 + (4 * hb + q) * 128 + i8 * 16;
        tb[19 * 64] = otf; tb[21 * 64] = hb ? OOB : otf;
        tb[20 * 64] = lane == 0 ? O_FT + id * 128 : OOB;
        if (W == 3)
            for (int k = 0; k < 3; ++k) tb[(17 + k) * 64] = RXW(half * 7 + 2 * k + (q >> 1)) + (q & 1) * 4096 + h * 256 + l15 * 16;
        wk = ((h * 28 + half * 14 + 4 * W) * 7 + (lane < 42 ? lane : 0)) * 2048; fw = h * 8 + (lane & 3);
        p1 = W == 3 ? a.GF : a.WqkF; x1 = h * 28 * 1024; x2 = (h * 28 + half * 14 + 4 * W) * 1024; po = OFF_X;
    }
    tb[0 * 64] = (lane & 8) ? 0xEEEE : 0x4444; tb[1 * 64] = lane * 16; tb[2 * 64] = wk; tb[3 * 64] = ((vb >> 3) * 16 + (vb & 3) * 4 + W) * 1280 + lane * 4;
    tb[4 * 64] = O_FW + fw * 128; tb[5 * 64] = O_RDY + lane * 4; tb[6 * 64] = O_RDY + 256 + lane * 4; tb[7 * 64] = O_REC;
    wait_prog(r, O_PAR + lane * 4, tag + 1); wait_prog(r, O_PAR + 256 + lane * 4, tag + 1);
    unsigned la = sld32(r, O_PAR + lane * 4, 0), lb = sld32(r, O_PAR + 256 + lane * 4, 0); WAITV(0, "+v"(la), "+v"(lb));
    unsigned lat = (la - tag) + ((lb - tag) << 16);
#pragma unroll
    for (int sft = 4; sft < 64; sft <<= 1) lat += __shfl_xor(lat, sft);
    la = ((lat & 0xffffu) << 2) | (lane & 3); lb = ((lat >> 16) << 2) | (lane & 3);
    la = min(la, (unsigned)__shfl_xor(la, 1)); la = min(la, (unsigned)__shfl_xor(la, 2)); lb = min(lb, (unsigned)__shfl_xor(lb, 1)); lb = min(lb, (unsigned)__shfl_xor(lb, 2));
    const unsigned pa = __builtin_amdgcn_readfirstlane(la) & 3, pb = __builtin_amdgcn_readfirstlane(lb) & 3;
    const unsigned dx = (1u << 21) | ((pa ^ pb) << 13);
#pragma unroll
    for (int i = 0; i < 8; ++i) sst128((u4){FF, FF, FF, FF}, r, poi, (((pa << 13) ^ ((i & 1) << 14)) + (i & 3) * 524288) ^ (i & 4 ? dx : 0u));
    edge(c1, n1);
    const unsigned f = min(max((unsigned)((float)(c1 - c0) * 252.f / (float)(n1 - n0)), 0x800u), 0x2000u);
    const unsigned k0 = n1 * 16u / LAT_R, sl0 = (pa << 13) ^ ((k0 & 1) ? dx : 0u);
    const unsigned sz1 = (role == 2 && W == 3) ? 64 * 16 * 28 * 1024 : 28 * 16 * 14336, sz2 = role == 1 ? 65 * 28 * 4096 : 64 * 16 * 28 * 1024;
    unsigned sv;
    if (lane < 4) sv = srdw(a.HB, HB_SZ, lane); else if (lane < 8) sv = srdw(p1, sz1, lane - 4); else if (lane < 12) sv = srdw(p2, sz2, lane - 8); else if (lane < 16) sv = srdw(a.TF, 65 * 32 * 3584, lane - 12);
    else if (lane < 20) sv = srdw(a.TS, 512 * 1280, lane - 16);
    else sv = lane == 20 ? k0 * LAT_R : lane == 21 ? LAT_R : lane == 24 ? po : lane == 25 ? dx : lane == 26 ? tag : lane == 27 ? sl0 : lane == 28 ? sl0 ^ 16384u : lane == 29 ? x1 : lane == 30 ? x2
            : lane == 33 ? (unsigned)TOL_C : lane == 34 ? smb + L_CLK : 0u;
    tb[31 * 64] = sv;
    if (W == 0 && lane < 2) *(u4 LAS*)(sm + L_CLK + lane * 16) = lane == 0 ? (u4){n1 * 16u, c1, f, 0u} : (u4){0u, 0u, 0u, 0u};
    waitvm<0>(); bar();
    if (W == 0) sst32(tag + 1, r, lane == 0 ? O_RDY + ((vb & 3) * 32 + (vb >> 3)) * 4 : OOB, 0);
    const unsigned tab = smb + L_TAB + W * 8192 + lane * 4;
    if (role == 0) { if (W == 0) RUN(S0); else if (W == 1) RUN(S1); else if (W == 2) RUN(S2); else RUN(S3); }
    else if (role == 1) { if (W == 0) RUN(P0); else if (W == 1) RUN(P1); else if (W == 2) RUN(P2); else RUN(P3); }
    else { if (W == 0) RUN(X0); else if (W == 1) RUN(X1); else if (W == 2) RUN(X2); else RUN(X3); }
}

#define NPA_FAR 10
#define PAO_AT 118
__global__ __launch_bounds__(256, 1) void fused_kernel(Args a_) {
    CArgs& a = get_args();
    const int wave = __builtin_amdgcn_readfirstlane(threadIdx.x >> 6);
    const u4 rhb = mksrd(a.HB, HB_SZ);
    unsigned ep = 0;
    if (wave == 0) ep = ld32(rhb, O_EPOCH, 0);
    else {
        u64 pc; unsigned len;
        asm volatile("s_getpc_b64 %0\nfk_code_lo:\n\ts_mov_b32 %1, fk_code_hi-fk_code_lo" : "=s"(pc), "=s"(len));
        const u4 rc = mksrd((const void*)pc, len & ~15u);
        const int vo = (((blockIdx.x >> 3) * 3 + wave - 1) * 2) * 1024 + (threadIdx.x & 63) * 16;
#pragma unroll
        for (int i = 0; i < 2; ++i) dma_one(rc, vo + i * 1024, 0, (unsigned)(size_t)(char LAS*)smem_ + 147456 + (wave * 3 + i) * 1024);
    }
    const int xcc = __builtin_amdgcn_s_getreg((31 << 11) | 20) & 7;
    int vb = xcc + (blockIdx.x & ~7);
    unsigned tag;
#pragma nounroll
    for (int pass = 0;; ++pass) {
        if (vb & 4) w_first(wave, (vb >> 5) & 1, 4 * (vb & 3) + ((vb >> 3) & 3), vb >> 6); else waitvm<0>();
        if (pass) break;
        if (wave == 0) {
            WAITV(28, "+v"(ep));
            if (threadIdx.x == 0) {
                const unsigned tg = ep + 0x9E3779B4u;
                unsigned slot = (unsigned)xcc * 32u + (blockIdx.x >> 3);
                for (int k = 0; k < 256; ++k, slot = (slot + 1u) & 255u)
                    if (__hip_atomic_exchange((unsigned*)(a.HB + O_CNT) + slot * 32u, tg, __ATOMIC_RELAXED, __HIP_MEMORY_SCOPE_AGENT) != tg) break;
                *(u2 LAS*)(smem_ + 147456) = (u2){(slot >> 5) + 8u * (slot & 31u), tg};
            }
        }
        bar();
        const int got = __builtin_amdgcn_readfirstlane(*(const unsigned LAS*)(smem_ + 147456));
        tag = __builtin_amdgcn_readfirstlane(*(const unsigned LAS*)(smem_ + 147460));
        if (got == vb) break;
        vb = got; waitvm<0>(); bar();
    }
    const int res = vb & 7, idx = vb >> 3;
    int j;
    const int xq = res & 3;
    unsigned* const tick = (unsigned*)(a.HB + O_TICK) + xq * 32;
    if (res < 4) {
        const int xh = res >> 1;
        if (idx < 16 || ((res & 1) && idx < 30)) { chain_role(tag, vb, wave, (res & 1) ? idx >> 4 : 2, idx < 16 ? xh * 16 + idx : xh * 14 + idx - 16, xh); return; }
        if (wave == 0) sst32(tag + 1, rhb, fresh_lane() == 0 ? O_RDY + (res * 32 + idx) * 4 : OOB, 0);
        j = -2;
    } else {
        if (idx == 0 && wave == 0) sst32(0u, rhb, fresh_lane() == 0 ? O_TICK + xq * 128 : OOB, 0);
        if (idx & 4) wgu_task<1>(tag, vb, wave, 4 * xq + (idx & 3), idx >> 3); else wgu_task<0>(tag, vb, wave, 4 * xq + (idx & 3), idx >> 3);
        wait_prog(rhb, fresh_lane() == 0 ? O_FW + xq * 32 * 128 : OOB, tag + 1);
        j = 0;
    }
    int round = 0;
#pragma nounroll
    for (; round < 256; ++round) {
        if (j >= 0) {
            if (wave == 0 && fresh_lane() == 0) *(unsigned LAS*)(smem_ + 160 * 1024 - 16) = __hip_atomic_fetch_add(tick, 1u, __ATOMIC_RELAXED, __HIP_MEMORY_SCOPE_AGENT);
            bar();
            j = __builtin_amdgcn_readfirstlane(*(const unsigned LAS*)(smem_ + 160 * 1024 - 16));
            if ((unsigned)j >= NPA_FAR + 190u) break;
        }
        int k = j - NPA_FAR;
        if (k < 0) {
            parta_task(tag, wave, j < 0 ? 2 * ((res & 1) ? (res >> 1) * 2 + idx + 2 : (res >> 1) * 16 + idx - 16) + j + 2 : 72 + xq * NPA_FAR + j);
            if (j < 0 && ++j == 0) break;
            continue;
        }
        if (k >= PAO_AT && k < PAO_AT + 14) { pa_out(tag, wave, (k - PAO_AT) * 4 + xq); bar(); continue; }
        if (k >= PAO_AT) k -= 14;
        const int g = k < 162 ? (k + 14) / 11 : 16 + (k - 162) / 7, o = k < 162 ? (k + 14) % 11 : (k - 162) % 7;
        if (k < 8 || o >= 7) e_task(tag, wave, 4 * (k < 8 ? k >> 2 : g) + xq, k < 8 ? k & 3 : o - 7);
        else { x_task(tag, wave, 4 * (g - 2) + xq, o); bar(); }
    }
}
__device__ __attribute__((used, noinline)) void fused_kernel_roles() { asm volatile("s_endpgm\n" ASM_ROLES "fk_code_hi:"); }

extern "C" void kernel_launch(void* const* d_in, const int* in_sizes, int n_in, void* d_out, int out_size, void* d_ws, size_t ws_size, hipStream_t stream) {
    Args a;
    a.Au = (const u16*)d_in[0]; a.AlogA = (const u16*)d_in[1]; a.Aim = (const u16*)d_in[2]; a.ACr = (const u16*)d_in[3]; a.ACi = (const u16*)d_in[4];
    a.Alogdt = (const u16*)d_in[5]; a.ADsk = (const u16*)d_in[6]; a.X = (const u16*)d_in[7]; a.mem0 = (const u16*)d_in[8];
    a.Wq = (const u16*)d_in[9]; a.Wk = (const u16*)d_in[10]; a.Wv = (const u16*)d_in[11]; a.Wo = (const u16*)d_in[12];
    a.out = (u16*)d_out;
    char* base = (char*)(((size_t)d_ws + (2u << 20) - 1) & ~(size_t)((2u << 20) - 1));
    a.HB = base;
    size_t off = HB_SZ;
    auto alloc = [&](size_t n) { char* p = base + off; off += (n + 4095) & ~(size_t)4095; return p; };
    a.WvoF = (u16*)alloc(28 * 16 * 14336); a.WqkF = (u16*)alloc(28 * 16 * 14336); a.GF = (u16*)alloc((size_t)64 * 16 * 28 * 1024); a.UF = (u16*)alloc((size_t)64 * 16 * 28 * 1024);
    a.EX = (u16*)alloc(64 * 16 * 2048); a.UM = (u16*)alloc(65 * 28 * 4096); a.MZX = (float*)alloc(64 * 16 * 256); a.TF = (u16*)alloc(65 * 32 * 3584); a.PA = (u16*)alloc(56 * 32768);
    a.TS = (unsigned*)alloc(768 * 1280);
    (void)hipFuncSetAttribute((const void*)fused_kernel, hipFuncAttributeMaxDynamicSharedMemorySize, 160 * 1024);
    fused_kernel<<<256, 256, 160 * 1024, stream>>>(a);
}
```

```cpp
#include <hip/hip_runtime.h>
#include <stdint.h>
typedef unsigned short u16;
typedef unsigned long long u64;
typedef __attribute__((ext_vector_type(8))) __bf16 bf8;
typedef __attribute__((ext_vector_type(16))) __bf16 bf16v;
typedef __attribute__((ext_vector_type(2))) __bf16 bf2;
typedef __attribute__((ext_vector_type(2))) float f2;
typedef __attribute__((ext_vector_type(4))) float f4;
typedef __attribute__((ext_vector_type(4))) unsigned u4;
typedef __attribute__((ext_vector_type(2))) unsigned u2;
typedef __attribute__((ext_vector_type(8))) unsigned u8v;
typedef __attribute__((ext_vector_type(4))) short s4;
#define LAS __attribute__((address_space(3)))
#define DI __device__ __forceinline__
#define SB() __builtin_amdgcn_sched_barrier(0)

DI unsigned pk2(float a, float b) { f2 v = {a, b}; return __builtin_bit_cast(unsigned, __builtin_convertvector(v, bf2)); }
DI float bfr(float a) { return __uint_as_float(pk2(a, a) & 0xffff0000u); }
DI unsigned san(unsigned x) { return x < 0xFFFFFFFEu ? x : 0xFFFFFFFEu; }
DI void bar() { asm volatile("s_waitcnt lgkmcnt(0)\n\ts_barrier" ::: "memory"); SB(); }
DI float x32max(float a) { auto r = __builtin_amdgcn_permlane32_swap(__float_as_uint(a), __float_as_uint(a), false, false); return fmaxf(__uint_as_float(r[0]), __uint_as_float(r[1])); }
DI float x32add(float a) { auto r = __builtin_amdgcn_permlane32_swap(__float_as_uint(a), __float_as_uint(a), false, false); return __uint_as_float(r[0]) + __uint_as_float(r[1]); }
DI u2 trread(const char LAS* p) { return __builtin_bit_cast(u2, __builtin_amdgcn_ds_read_tr16_b64_v4i16((LAS s4*)p)); }
DI u4 mksrd(const void* p, unsigned bytes) { u64 x = (u64)p; return (u4){(unsigned)x, (unsigned)(x >> 32) & 0xffffu, bytes, 0x00020000u}; }
DI u4 ld128(u4 srd, int voff, int soff) { u4 v; asm volatile("buffer_load_dwordx4 %0, %1, %2, %3 offen sc1" : "=v"(v) : "v"(voff), "s"(srd), "s"(soff) : "memory"); return v; }
DI unsigned ld32(u4 srd, int voff, int soff) { unsigned v; asm volatile("buffer_load_dword %0, %1, %2, %3 offen sc1" : "=v"(v) : "v"(voff), "s"(srd), "s"(soff) : "memory"); return v; }
DI void st128(u4 v, u4 srd, int voff, int soff) { asm volatile("buffer_store_dwordx4 %0, %1, %2, %3 offen sc1\n\ts_nop 1" :: "v"(v), "v"(voff), "s"(srd), "s"(soff) : "memory"); }
DI void st32(unsigned v, u4 srd, int voff, int soff) { asm volatile("buffer_store_dword %0, %1, %2, %3 offen sc1" :: "v"(v), "v"(voff), "s"(srd), "s"(soff) : "memory"); }
#define PIN(x) asm volatile("" : "+v"(x))
#define WAITV(n, ...) asm volatile("s_waitcnt vmcnt(" #n ")" : __VA_ARGS__ :: "memory")

#define NSEG 64
#define O_MEM 0
#define O_SMM (2 * 32768)
#define O_MZ (4 * 32768)
#define O_MEMX 16384
#define RXW(n) ((n) < 7 ? (5 + (n)) * 32768 : 16384 + ((n) - 5) * 32768)
#define O_PROBE (15 * 32768)
#define O_FL (4 << 20)
#define O_PAR O_FL
#define O_RDY (O_FL + 512)
#define O_FPA (O_FL + 1024)
#define O_FE (O_FL + 2048)
#define O_FW (O_FL + 4096)
#define O_FT (O_FW + 16384)
#define O_REC (O_FT + 8192)
#define O_CNT (O_REC + 16384)
#define O_EPOCH (O_CNT + 32768)
#define O_TICK (O_EPOCH + 128)
#define HB_SZ (5 << 20)
#define SPIN_MAX (1u << 17)
#define LOG2E 1.4426950408889634f
#define FF 0xFFFFFFFFu
#define OOB 0x7f000000

struct Args {
    const u16 *Au, *AlogA, *Aim, *ACr, *ACi, *Alogdt, *ADsk, *X, *mem0, *Wq, *Wk, *Wv, *Wo;
    u16* out;
    char* HB;
    u16 *WvoF, *WqkF, *GF, *UF, *EX, *UM, *TF, *PA; float *MZX;
    unsigned* TS;
};
typedef const Args __attribute__((address_space(4))) CArgs;
DI CArgs& get_args() { CArgs* p = (CArgs*)__builtin_amdgcn_kernarg_segment_ptr(); asm volatile("" : "+s"(p)); return *p; }
extern __shared__ __attribute__((aligned(16))) char smem_[];
DI unsigned now32() { u64 t; asm volatile("s_memtime %0\n\ts_waitcnt lgkmcnt(0)" : "=s"(t) :: "memory"); return (unsigned)t; }
DI unsigned rt_now() { u64 t; asm volatile("s_memrealtime %0\n\ts_waitcnt lgkmcnt(0)" : "=s"(t) :: "memory"); return (unsigned)t; }
#define REC_A 1260
#define REC_B 1045
#define LAT_R 1160u
#define OFF_P 60
#define OFF_X 20
#define TOL_C -100
#define ASM_ROLES \
".p2align 6\n" \
"fk_role_S0:\n" \
"ds_read_b32 v200, v255 offset:0\n" \
"ds_read_b32 v201, v255 offset:256\n" \
"ds_read_b32 v202, v255 offset:512\n" \
"ds_read_b32 v203, v255 offset:768\n" \
"ds_read_b32 v204, v255 offset:1024\n" \
"ds_read_b32 v205, v255 offset:1280\n" \
"ds_read_b32 v206, v255 offset:1536\n" \
"ds_read_b32 v207, v255 offset:1792\n" \
"ds_read_b32 v208, v255 offset:2048\n" \
"ds_read_b32 v209, v255 offset:2304\n" \
"ds_read_b32 v210, v255 offset:2560\n" \
"ds_read_b32 v211, v255 offset:2816\n" \
"ds_read_b32 v212, v255 offset:3072\n" \
"ds_read_b32 v213, v255 offset:3328\n" \
"ds_read_b32 v214, v255 offset:3584\n" \
"ds_read_b32 v215, v255 offset:3840\n" \
"ds_read_b32 v254, v255 offset:7936\n" \
"s_waitcnt lgkmcnt(0)\n" \
"v_readlane_b32 s4, v254, 0\n" \
"v_readlane_b32 s5, v254, 1\n" \
"v_readlane_b32 s6, v254, 2\n" \
"v_readlane_b32 s7, v254, 3\n" \
"v_readlane_b32 s8, v254, 4\n" \
"v_readlane_b32 s9, v254, 5\n" \
"v_readlane_b32 s10, v254, 6\n" \
"v_readlane_b32 s11, v254, 7\n" \
"v_readlane_b32 s12, v254, 8\n" \
"v_readlane_b32 s13, v254, 9\n" \
"v_readlane_b32 s14, v254, 10\n" \
"v_readlane_b32 s15, v254, 11\n" \
"v_readlane_b32 s16, v254, 12\n" \
"v_readlane_b32 s17, v254, 13\n" \
"v_readlane_b32 s18, v254, 14\n" \
"v_readlane_b32 s19, v254, 15\n" \
"v_readlane_b32 s20, v254, 16\n" \
"v_readlane_b32 s21, v254, 17\n" \
"v_readlane_b32 s22, v254, 18\n" \
"v_readlane_b32 s23, v254, 19\n" \
"v_readlane_b32 s24, v254, 20\n" \
"v_readlane_b32 s25, v254, 21\n" \
"v_readlane_b32 s26, v254, 22\n" \
"v_readlane_b32 s27, v254, 23\n" \
"v_readlane_b32 s28, v254, 24\n" \
"v_readlane_b32 s29, v254, 25\n" \
"v_readlane_b32 s30, v254, 26\n" \
"v_readlane_b32 s31, v254, 27\n" \
"v_readlane_b32 s32, v254, 28\n" \
"v_readlane_b32 s33, v254, 29\n" \
"v_readlane_b32 s34, v254, 30\n" \
"v_readlane_b32 s35, v254, 31\n" \
"v_readlane_b32 s36, v254, 32\n" \
"v_readlane_b32 s37, v254, 33\n" \
"v_readlane_b32 s38, v254, 34\n" \
"v_readlane_b32 s39, v254, 35\n" \
"s_mov_b32 s40, 0\n" \
"s_mov_b32 s51, 0\n" \
"s_nop 0\n" \
"v_mov_b32 v252, s38\n" \
"s_nop 4\n" \
"v_mov_b32 v188, -1\n" \
"s_mov_b32 s28, s33\n" \
"s_mov_b32 s33, -1\n" \
"v_writelane_b32 v251, s6, 0\n" \
"v_writelane_b32 v251, s10, 1\n" \
"v_writelane_b32 v251, s14, 2\n" \
"v_writelane_b32 v251, s18, 3\n" \
"s_mov_b32 s6, 0\n" \
"s_mov_b32 s10, 0\n" \
"s_mov_b32 s14, 0\n" \
"s_mov_b32 s18, 0\n" \
"s_mov_b32 s40, -1\n" \
"s_branch .LS0_pre\n" \
".LS0_wet:\n" \
"s_waitcnt vmcnt(0)\n" \
"v_readlane_b32 s6, v251, 0\n" \
"v_readlane_b32 s10, v251, 1\n" \
"v_readlane_b32 s14, v251, 2\n" \
"v_readlane_b32 s18, v251, 3\n" \
"s_nop 4\n" \
"s_mov_b32 s33, s28\n" \
"s_add_u32 s34, s33, 1\n" \
"s_add_u32 s36, s33, 2\n" \
"s_add_u32 s43, s30, s33\n" \
"s_mov_b32 s50, 0\n" \
".LS0_w1l:\n" \
"buffer_load_dword v240, v204, s[4:7], 0 offen sc1\n" \
"s_waitcnt vmcnt(0)\n" \
".LS0_w1c:\n" \
"v_subrev_u32 v241, s43, v240\n" \
"v_cmp_gt_u32 vcc, 0x100, v241\n" \
"s_cmp_eq_u64 vcc, -1\n" \
"s_cbranch_scc1 .LS0_w1d\n" \
"s_sleep 2\n" \
"s_add_u32 s50, s50, 1\n" \
"s_cmp_lt_u32 s50, 0x20000\n" \
"s_cbranch_scc1 .LS0_w1l\n" \
"s_branch .LS0_end\n" \
".LS0_w1d:\n" \
"v_readlane_b32 s43, v202, 0\n" \
"s_nop 4\n" \
"buffer_load_dwordx4 a[0:3], v201, s[8:11], s43 offen sc1\n" \
"buffer_load_dwordx4 a[4:7], v201, s[8:11], s43 offen offset:1024 sc1\n" \
"v_readlane_b32 s43, v202, 1\n" \
"s_nop 4\n" \
"buffer_load_dwordx4 a[8:11], v201, s[8:11], s43 offen sc1\n" \
"buffer_load_dwordx4 a[12:15], v201, s[8:11], s43 offen offset:1024 sc1\n" \
"v_readlane_b32 s43, v202, 2\n" \
"s_nop 4\n" \
"buffer_load_dwordx4 a[16:19], v201, s[8:11], s43 offen sc1\n" \
"buffer_load_dwordx4 a[20:23], v201, s[8:11], s43 offen offset:1024 sc1\n" \
"v_readlane_b32 s43, v202, 3\n" \
"s_nop 4\n" \
"buffer_load_dwordx4 a[24:27], v201, s[8:11], s43 offen sc1\n" \
"buffer_load_dwordx4 a[28:31], v201, s[8:11], s43 offen offset:1024 sc1\n" \
"v_readlane_b32 s43, v202, 4\n" \
"s_nop 4\n" \
"buffer_load_dwordx4 a[32:35], v201, s[8:11], s43 offen sc1\n" \
"buffer_load_dwordx4 a[36:39], v201, s[8:11], s43 offen offset:1024 sc1\n" \
"v_readlane_b32 s43, v202, 5\n" \
"s_nop 4\n" \
"buffer_load_dwordx4 a[40:43], v201, s[8:11], s43 offen sc1\n" \
"buffer_load_dwordx4 a[44:47], v201, s[8:11], s43 offen offset:1024 sc1\n" \
"v_readlane_b32 s43, v202, 6\n" \
"s_nop 4\n" \
"buffer_load_dwordx4 a[48:51], v201, s[8:11], s43 offen sc1\n" \
"buffer_load_dwordx4 a[52:55], v201, s[8:11], s43 offen offset:1024 sc1\n" \
"v_readlane_b32 s43, v202, 7\n" \
"s_nop 4\n" \
"buffer_load_dwordx4 a[56:59], v201, s[8:11], s43 offen sc1\n" \
"buffer_load_dwordx4 a[60:63], v201, s[8:11], s43 offen offset:1024 sc1\n" \
"s_add_u32 s43, s30, s34\n" \
"s_mov_b32 s50, 0\n" \
".LS0_w2l:\n" \
"buffer_load_dword v240, v204, s[4:7], 0 offen sc1\n" \
"s_waitcnt vmcnt(0)\n" \
".LS0_w2c:\n" \
"v_subrev_u32 v241, s43, v240\n" \
"v_cmp_gt_u32 vcc, 0x100, v241\n" \
"s_cmp_eq_u64 vcc, -1\n" \
"s_cbranch_scc1 .LS0_w2d\n" \
"s_sleep 2\n" \
"s_add_u32 s50, s50, 1\n" \
"s_cmp_lt_u32 s50, 0x20000\n" \
"s_cbranch_scc1 .LS0_w2l\n" \
"s_branch .LS0_end\n" \
".LS0_w2d:\n" \
"v_readlane_b32 s43, v202, 8\n" \
"s_nop 4\n" \
"buffer_load_dwordx4 a[64:67], v201, s[8:11], s43 offen sc1\n" \
"buffer_load_dwordx4 a[68:71], v201, s[8:11], s43 offen offset:1024 sc1\n" \
"v_readlane_b32 s43, v202, 9\n" \
"s_nop 4\n" \
"buffer_load_dwordx4 a[72:75], v201, s[8:11], s43 offen sc1\n" \
"buffer_load_dwordx4 a[76:79], v201, s[8:11], s43 offen offset:1024 sc1\n" \
"v_readlane_b32 s43, v202, 10\n" \
"s_nop 4\n" \
"buffer_load_dwordx4 a[80:83], v201, s[8:11], s43 offen sc1\n" \
"buffer_load_dwordx4 a[84:87], v201, s[8:11], s43 offen offset:1024 sc1\n" \
"v_readlane_b32 s43, v202, 12\n" \
"s_nop 4\n" \
"buffer_load_dwordx4 a[96:99], v201, s[8:11], s43 offen sc1\n" \
"buffer_load_dwordx4 a[100:103], v201, s[8:11], s43 offen offset:1024 sc1\n" \
"v_readlane_b32 s43, v202, 14\n" \
"s_nop 4\n" \
"buffer_load_dwordx4 a[112:115], v201, s[8:11], s43 offen sc1\n" \
"buffer_load_dwordx4 a[116:119], v201, s[8:11], s43 offen offset:1024 sc1\n" \
"v_readlane_b32 s43, v202, 19\n" \
"s_nop 4\n" \
"buffer_load_dwordx4 a[152:155], v201, s[8:11], s43 offen sc1\n" \
"buffer_load_dwordx4 a[156:159], v201, s[8:11], s43 offen offset:1024 sc1\n" \
"v_readlane_b32 s43, v202, 22\n" \
"s_nop 4\n" \
"buffer_load_dwordx4 a[176:179], v201, s[8:11], s43 offen sc1\n" \
"buffer_load_dwordx4 a[180:183], v201, s[8:11], s43 offen offset:1024 sc1\n" \
"v_readlane_b32 s43, v202, 25\n" \
"s_nop 4\n" \
"buffer_load_dwordx4 a[200:203], v201, s[8:11], s43 offen sc1\n" \
"buffer_load_dwordx4 a[204:207], v201, s[8:11], s43 offen offset:1024 sc1\n" \
"s_add_u32 s43, s30, s36\n" \
"s_mov_b32 s50, 0\n" \
".LS0_w3l:\n" \
"buffer_load_dword v240, v204, s[4:7], 0 offen sc1\n" \
"s_waitcnt vmcnt(0)\n" \
".LS0_w3c:\n" \
"v_subrev_u32 v241, s43, v240\n" \
"v_cmp_gt_u32 vcc, 0x100, v241\n" \
"s_cmp_eq_u64 vcc, -1\n" \
"s_cbranch_scc1 .LS0_w3d\n" \
"s_sleep 2\n" \
"s_add_u32 s50, s50, 1\n" \
"s_cmp_lt_u32 s50, 0x20000\n" \
"s_cbranch_scc1 .LS0_w3l\n" \
"s_branch .LS0_end\n" \
".LS0_w3d:\n" \
"v_readlane_b32 s43, v202, 11\n" \
"s_nop 4\n" \
"buffer_load_dwordx4 a[88:91], v201, s[8:11], s43 offen sc1\n" \
"buffer_load_dwordx4 a[92:95], v201, s[8:11], s43 offen offset:1024 sc1\n" \
"v_readlane_b32 s43, v202, 13\n" \
"s_nop 4\n" \
"buffer_load_dwordx4 a[104:107], v201, s[8:11], s43 offen sc1\n" \
"buffer_load_dwordx4 a[108:111], v201, s[8:11], s43 offen offset:1024 sc1\n" \
"v_readlane_b32 s43, v202, 15\n" \
"s_nop 4\n" \
"buffer_load_dwordx4 a[120:123], v201, s[8:11], s43 offen sc1\n" \
"buffer_load_dwordx4 a[124:127], v201, s[8:11], s43 offen offset:1024 sc1\n" \
"v_readlane_b32 s43, v202, 16\n" \
"s_nop 4\n" \
"buffer_load_dwordx4 a[128:131], v201, s[8:11], s43 offen sc1\n" \
"buffer_load_dwordx4 a[132:135], v201, s[8:11], s43 offen offset:1024 sc1\n" \
"v_readlane_b32 s43, v202, 17\n" \
"s_nop 4\n" \
"buffer_load_dwordx4 a[136:139], v201, s[8:11], s43 offen sc1\n" \
"buffer_load_dwordx4 a[140:143], v201, s[8:11], s43 offen offset:1024 sc1\n" \
"v_readlane_b32 s43, v202, 20\n" \
"s_nop 4\n" \
"buffer_load_dwordx4 a[160:163], v201, s[8:11], s43 offen sc1\n" \
"buffer_load_dwordx4 a[164:167], v201, s[8:11], s43 offen offset:1024 sc1\n" \
"v_readlane_b32 s43, v202, 23\n" \
"s_nop 4\n" \
"buffer_load_dwordx4 a[184:187], v201, s[8:11], s43 offen sc1\n" \
"buffer_load_dwordx4 a[188:191], v201, s[8:11], s43 offen offset:1024 sc1\n" \
"v_readlane_b32 s43, v202, 26\n" \
"s_nop 4\n" \
"buffer_load_dwordx4 a[208:211], v201, s[8:11], s43 offen sc1\n" \
"buffer_load_dwordx4 a[212:215], v201, s[8:11], s43 offen offset:1024 sc1\n" \
"s_add_u32 s43, s30, 4\n" \
"s_mov_b32 s50, 0\n" \
".LS0_w4l:\n" \
"buffer_load_dword v240, v204, s[4:7], 0 offen sc1\n" \
"s_waitcnt vmcnt(0)\n" \
".LS0_w4c:\n" \
"v_subrev_u32 v241, s43, v240\n" \
"v_cmp_gt_u32 vcc, 0x100, v241\n" \
"s_cmp_eq_u64 vcc, -1\n" \
"s_cbranch_scc1 .LS0_w4d\n" \
"s_sleep 2\n" \
"s_add_u32 s50, s50, 1\n" \
"s_cmp_lt_u32 s50, 0x20000\n" \
"s_cbranch_scc1 .LS0_w4l\n" \
"s_branch .LS0_end\n" \
".LS0_w4d:\n" \
"v_readlane_b32 s43, v202, 18\n" \
"s_nop 4\n" \
"buffer_load_dwordx4 a[144:147], v201, s[8:11], s43 offen sc1\n" \
"buffer_load_dwordx4 a[148:151], v201, s[8:11], s43 offen offset:1024 sc1\n" \
"v_readlane_b32 s43, v202, 21\n" \
"s_nop 4\n" \
"buffer_load_dwordx4 a[168:171], v201, s[8:11], s43 offen sc1\n" \
"buffer_load_dwordx4 a[172:175], v201, s[8:11], s43 offen offset:1024 sc1\n" \
"v_readlane_b32 s43, v202, 24\n" \
"s_nop 4\n" \
"buffer_load_dwordx4 a[192:195], v201, s[8:11], s43 offen sc1\n" \
"buffer_load_dwordx4 a[196:199], v201, s[8:11], s43 offen offset:1024 sc1\n" \
"v_readlane_b32 s43, v202, 27\n" \
"s_nop 4\n" \
"buffer_load_dwordx4 a[216:219], v201, s[8:11], s43 offen sc1\n" \
"buffer_load_dwordx4 a[220:223], v201, s[8:11], s43 offen offset:1024 sc1\n" \
"s_mov_b32 s33, -1\n" \
"s_add_u32 s43, s30, 1\n" \
"s_mov_b32 s50, 0\n" \
".LS0_w5l:\n" \
"buffer_load_dword v240, v205, s[4:7], 0 offen sc1\n" \
"s_waitcnt vmcnt(0)\n" \
".LS0_w5c:\n" \
"v_subrev_u32 v241, s43, v240\n" \
"v_cmp_gt_u32 vcc, 0x100, v241\n" \
"s_cmp_eq_u64 vcc, -1\n" \
"s_cbranch_scc1 .LS0_w5d\n" \
"s_sleep 2\n" \
"s_add_u32 s50, s50, 1\n" \
"s_cmp_lt_u32 s50, 0x20000\n" \
"s_cbranch_scc1 .LS0_w5l\n" \
"s_branch .LS0_end\n" \
".LS0_w5d:\n" \
"s_add_u32 s43, s30, 1\n" \
"s_mov_b32 s50, 0\n" \
".LS0_w6l:\n" \
"buffer_load_dword v240, v206, s[4:7], 0 offen sc1\n" \
"s_waitcnt vmcnt(0)\n" \
".LS0_w6c:\n" \
"v_subrev_u32 v241, s43, v240\n" \
"v_cmp_gt_u32 vcc, 0x100, v241\n" \
"s_cmp_eq_u64 vcc, -1\n" \
"s_cbranch_scc1 .LS0_w6d\n" \
"s_sleep 2\n" \
"s_add_u32 s50, s50, 1\n" \
"s_cmp_lt_u32 s50, 0x20000\n" \
"s_cbranch_scc1 .LS0_w6l\n" \
"s_branch .LS0_end\n" \
".LS0_w6d:\n" \
".LS0_pre:\n" \
"s_barrier\n" \
"ds_read_b96 v[248:250], v252\n" \
"s_waitcnt lgkmcnt(0)\n" \
"v_readfirstlane_b32 s44, v248\n" \
"v_readfirstlane_b32 s45, v249\n" \
"v_readfirstlane_b32 s46, v250\n" \
"s_mul_i32 s42, s25, s46\n" \
"s_lshr_b32 s42, s42, 12\n" \
".LS0_loop:\n" \
"s_add_u32 s43, s40, 0\n" \
"s_and_b32 s43, s43, 3\n" \
"s_bitcmp1_b32 s43, 0\n" \
"s_cselect_b32 s47, s32, s31\n" \
"s_lshl_b32 s43, s43, 19\n" \
"s_add_u32 s47, s47, s43\n" \
"s_sub_u32 s43, s24, s44\n" \
"s_max_i32 s43, s43, 0xfffd0000\n" \
"s_min_i32 s43, s43, 0x4000\n" \
"s_mul_i32 s43, s43, s46\n" \
"s_ashr_i32 s43, s43, 12\n" \
"s_add_u32 s41, s45, s43\n" \
"s_branch .LS0_a1\n" \
".LS0_retry:\n" \
"s_add_u32 s51, s51, 1\n" \
"s_cmp_gt_u32 s51, 0x20000\n" \
"s_cbranch_scc1 .LS0_end\n" \
".LS0_a0:\n" \
"s_add_u32 s41, s41, s42\n" \
"s_add_u32 s24, s24, s25\n" \
"s_xor_b32 s31, s31, s29\n" \
"s_xor_b32 s32, s32, s29\n" \
"s_xor_b32 s47, s47, s29\n" \
"s_xor_b32 s48, s48, s29\n" \
"s_add_u32 s39, s39, 1\n" \
".p2align 6\n" \
".LS0_a1:\n" \
"s_memtime vcc\n" \
"s_waitcnt lgkmcnt(0)\n" \
"s_sub_u32 s43, s41, vcc_lo\n" \
"s_cmp_gt_i32 s43, 128\n" \
"s_cbranch_scc1 .LS0_a1\n" \
"s_cmp_lt_i32 s43, s37\n" \
"s_cbranch_scc1 .LS0_a0\n" \
"s_max_i32 s43, s43, 0\n" \
"s_and_b32 s43, s43, -4\n" \
"s_getpc_b64 vcc\n" \
".LS0_a3:\n" \
"s_sub_u32 s43, .LS0_a4-.LS0_a3, s43\n" \
"s_add_u32 vcc_lo, vcc_lo, s43\n" \
"s_addc_u32 vcc_hi, vcc_hi, 0\n" \
"s_setpc_b64 vcc\n" \
".rept 32\n" \
"s_nop 0\n" \
".endr\n" \
".LS0_a4:\n" \
"buffer_load_dwordx4 v[0:3], v208, s[4:7], s47 offen sc1\n" \
"s_nop 9\n" \
"buffer_load_dwordx4 v[4:7], v208, s[4:7], s47 offen offset:1024 sc1\n" \
"s_nop 9\n" \
"buffer_load_dwordx4 v[8:11], v208, s[4:7], s47 offen offset:2048 sc1\n" \
"s_nop 9\n" \
"buffer_load_dwordx4 v[12:15], v210, s[4:7], s47 offen sc1\n" \
"s_add_u32 s35, s35, 1\n" \
"v_mov_b32 v28, 0\n" \
"v_mov_b32 v29, 0\n" \
"v_mov_b32 v30, 0\n" \
"v_mov_b32 v31, 0\n" \
"v_mov_b32 v32, 0\n" \
"v_mov_b32 v33, 0\n" \
"v_mov_b32 v34, 0\n" \
"v_mov_b32 v35, 0\n" \
"v_mov_b32 v36, 0\n" \
"v_mov_b32 v37, 0\n" \
"v_mov_b32 v38, 0\n" \
"v_mov_b32 v39, 0\n" \
"v_mov_b32 v40, 0\n" \
"v_mov_b32 v41, 0\n" \
"v_mov_b32 v42, 0\n" \
"v_mov_b32 v43, 0\n" \
"v_mov_b32 v44, 0\n" \
"v_mov_b32 v45, 0\n" \
"v_mov_b32 v46, 0\n" \
"v_mov_b32 v47, 0\n" \
"v_mov_b32 v48, 0\n" \
"v_mov_b32 v49, 0\n" \
"v_mov_b32 v50, 0\n" \
"v_mov_b32 v51, 0\n" \
"v_mov_b32 v52, 0\n" \
"v_mov_b32 v53, 0\n" \
"v_mov_b32 v54, 0\n" \
"v_mov_b32 v55, 0\n" \
"v_mov_b32 v56, 0\n" \
"v_mov_b32 v57, 0\n" \
"v_mov_b32 v58, 0\n" \
"v_mov_b32 v59, 0\n" \
"s_waitcnt vmcnt(3)\n" \
"v_smfmac_f32_16x16x64_bf16 v[28:31], v[0:3], a[0:7], v200\n" \
"v_smfmac_f32_16x16x64_bf16 v[32:35], v[0:3], a[8:15], v200\n" \
"v_smfmac_f32_16x16x64_bf16 v[36:39], v[0:3], a[16:23], v200\n" \
"v_smfmac_f32_16x16x64_bf16 v[40:43], v[0:3], a[24:31], v200\n" \
"v_smfmac_f32_16x16x64_bf16 v[44:47], v[0:3], a[32:39], v200\n" \
"v_smfmac_f32_16x16x64_bf16 v[48:51], v[0:3], a[40:47], v200\n" \
"v_smfmac_f32_16x16x64_bf16 v[52:55], v[0:3], a[48:55], v200\n" \
"v_smfmac_f32_16x16x64_bf16 v[56:59], v[0:3], a[56:63], v200\n" \
"s_waitcnt vmcnt(2)\n" \
"v_smfmac_f32_16x16x64_bf16 v[28:31], v[4:7], a[64:71], v200\n" \
"v_smfmac_f32_16x16x64_bf16 v[32:35], v[4:7], a[72:79], v200\n" \
"v_smfmac_f32_16x16x64_bf16 v[36:39], v[4:7], a[80:87], v200\n" \
"s_waitcnt vmcnt(1)\n" \
"v_smfmac_f32_16x16x64_bf16 v[28:31], v[8:11], a[88:95], v200\n" \
"s_nop 2\n" \
"v_smfmac_f32_16x16x64_bf16 v[40:43], v[4:7], a[96:103], v200\n" \
"s_nop 2\n" \
"v_smfmac_f32_16x16x64_bf16 v[32:35], v[8:11], a[104:111], v200\n" \
"s_nop 0\n" \
"v_cvt_pk_bf16_f32 v128, v28, v29\n" \
"v_cvt_pk_bf16_f32 v129, v30, v31\n" \
"v_smfmac_f32_16x16x64_bf16 v[44:47], v[4:7], a[112:119], v200\n" \
"ds_write_b64 v211, v[128:129] offset:0\n" \
"s_nop 1\n" \
"v_smfmac_f32_16x16x64_bf16 v[36:39], v[8:11], a[120:127], v200\n" \
"s_nop 0\n" \
"v_cvt_pk_bf16_f32 v130, v32, v33\n" \
"v_cvt_pk_bf16_f32 v131, v34, v35\n" \
"v_smfmac_f32_16x16x64_bf16 v[44:47], v[8:11], a[128:135], v200\n" \
"ds_write_b64 v211, v[130:131] offset:384\n" \
"s_nop 1\n" \
"v_smfmac_f32_16x16x64_bf16 v[40:43], v[8:11], a[136:143], v200\n" \
"s_waitcnt vmcnt(0)\n" \
"v_cvt_pk_bf16_f32 v132, v36, v37\n" \
"v_cvt_pk_bf16_f32 v133, v38, v39\n" \
"v_smfmac_f32_16x16x64_bf16 v[44:47], v[12:15], a[144:151], v200\n" \
"ds_write_b64 v211, v[132:133] offset:2560\n" \
"s_nop 1\n" \
"v_smfmac_f32_16x16x64_bf16 v[48:51], v[4:7], a[152:159], v200\n" \
"s_nop 0\n" \
"v_cvt_pk_bf16_f32 v134, v40, v41\n" \
"v_cvt_pk_bf16_f32 v135, v42, v43\n" \
"v_smfmac_f32_16x16x64_bf16 v[48:51], v[8:11], a[160:167], v200\n" \
"ds_write_b64 v211, v[134:135] offset:4736\n" \
"v_cvt_pk_bf16_f32 v136, v44, v45\n" \
"v_cvt_pk_bf16_f32 v137, v46, v47\n" \
"v_smfmac_f32_16x16x64_bf16 v[48:51], v[12:15], a[168:175], v200\n" \
"ds_write_b64 v211, v[136:137] offset:9216\n" \
"s_nop 1\n" \
"v_smfmac_f32_16x16x64_bf16 v[52:55], v[4:7], a[176:183], v200\n" \
"s_nop 2\n" \
"v_smfmac_f32_16x16x64_bf16 v[52:55], v[8:11], a[184:191], v200\n" \
"s_nop 0\n" \
"v_cvt_pk_bf16_f32 v138, v48, v49\n" \
"v_cvt_pk_bf16_f32 v139, v50, v51\n" \
"v_smfmac_f32_16x16x64_bf16 v[52:55], v[12:15], a[192:199], v200\n" \
"ds_write_b64 v211, v[138:139] offset:9600\n" \
"s_waitcnt lgkmcnt(2)\n" \
"s_barrier\n" \
"v_smfmac_f32_16x16x64_bf16 v[56:59], v[4:7], a[200:207], v200\n" \
"s_nop 2\n" \
"v_smfmac_f32_16x16x64_bf16 v[56:59], v[8:11], a[208:215], v200\n" \
"s_nop 2\n" \
"v_smfmac_f32_16x16x64_bf16 v[56:59], v[12:15], a[216:223], v200\n" \
"v_cvt_pk_bf16_f32 v140, v52, v53\n" \
"v_cvt_pk_bf16_f32 v141, v54, v55\n" \
"s_nop 1\n" \
"ds_write_b64 v211, v[140:141] offset:11776\n" \
"s_nop 2\n" \
"v_cvt_pk_bf16_f32 v142, v56, v57\n" \
"v_cvt_pk_bf16_f32 v143, v58, v59\n" \
"ds_write_b64 v211, v[142:143] offset:13952\n" \
"s_waitcnt lgkmcnt(2)\n" \
"s_barrier\n" \
"s_waitcnt lgkmcnt(1)\n" \
"s_barrier\n" \
"s_waitcnt lgkmcnt(0)\n" \
"s_barrier\n" \
"s_nop 3\n" \
"ds_read_b32 v64, v252 offset:20\n" \
"s_waitcnt lgkmcnt(0)\n" \
"v_readfirstlane_b32 s43, v64\n" \
"s_cmp_eq_u32 s43, s35\n" \
"s_cbranch_scc1 .LS0_retry\n" \
"s_nop 15\n" \
"v_max3_u32 v64, v0, v0, v1\n" \
"v_max3_u32 v64, v64, v2, v3\n" \
"v_max3_u32 v64, v64, v4, v5\n" \
"v_max3_u32 v64, v64, v6, v7\n" \
"v_max3_u32 v64, v64, v8, v9\n" \
"v_max3_u32 v64, v64, v10, v11\n" \
"v_max3_u32 v64, v64, v12, v13\n" \
"v_max3_u32 v64, v64, v14, v15\n" \
"v_cmp_eq_u32 vcc, -1, v64\n" \
"s_cbranch_vccnz .LS0_veto\n" \
".LS0_back:\n" \
"s_waitcnt lgkmcnt(0)\n" \
"s_barrier\n" \
"ds_read_b32 v64, v252 offset:16\n" \
"s_waitcnt lgkmcnt(0)\n" \
"v_readfirstlane_b32 s43, v64\n" \
"s_cmp_eq_u32 s43, s35\n" \
"s_cbranch_scc1 .LS0_retry\n" \
"s_waitcnt lgkmcnt(0)\n" \
"s_barrier\n" \
"ds_read_b64 v[246:247], v252 offset:24\n" \
"ds_read_b96 v[248:250], v252\n" \
"s_waitcnt lgkmcnt(0)\n" \
"v_readfirstlane_b32 s44, v248\n" \
"v_readfirstlane_b32 s45, v249\n" \
"v_readfirstlane_b32 s46, v250\n" \
"v_readfirstlane_b32 s26, v246\n" \
"v_readfirstlane_b32 s27, v247\n" \
"s_mul_i32 s42, s25, s46\n" \
"s_lshr_b32 s42, s42, 12\n" \
"s_add_u32 s40, s40, 1\n" \
"s_add_u32 s24, s24, s26\n" \
"s_add_u32 s24, s24, s27\n" \
"s_cmp_eq_u32 s40, 0\n" \
"s_cbranch_scc1 .LS0_wet\n" \
"s_cmp_lt_u32 s40, 63\n" \
"s_cbranch_scc1 .LS0_loop\n" \
".LS0_end:\n" \
"s_endpgm\n" \
".LS0_veto:\n" \
"v_mov_b32 v64, s35\n" \
"ds_write_b32 v252, v64 offset:16\n" \
"s_branch .LS0_back\n" \
".p2align 6\n" \
"fk_role_S1:\n" \
"ds_read_b32 v200, v255 offset:0\n" \
"ds_read_b32 v201, v255 offset:256\n" \
"ds_read_b32 v202, v255 offset:512\n" \
"ds_read_b32 v203, v255 offset:768\n" \
"ds_read_b32 v204, v255 offset:1024\n" \
"ds_read_b32 v205, v255 offset:1280\n" \
"ds_read_b32 v206, v255 offset:1536\n" \
"ds_read_b32 v207, v255 offset:1792\n" \
"ds_read_b32 v208, v255 offset:2048\n" \
"ds_read_b32 v209, v255 offset:2304\n" \
"ds_read_b32 v210, v255 offset:2560\n" \
"ds_read_b32 v211, v255 offset:2816\n" \
"ds_read_b32 v212, v255 offset:3072\n" \
"ds_read_b32 v213, v255 offset:3328\n" \
"ds_read_b32 v214, v255 offset:3584\n" \
"ds_read_b32 v215, v255 offset:3840\n" \
"ds_read_b32 v254, v255 offset:7936\n" \
"s_waitcnt lgkmcnt(0)\n" \
"v_readlane_b32 s4, v254, 0\n" \
"v_readlane_b32 s5, v254, 1\n" \
"v_readlane_b32 s6, v254, 2\n" \
"v_readlane_b32 s7, v254, 3\n" \
"v_readlane_b32 s8, v254, 4\n" \
"v_readlane_b32 s9, v254, 5\n" \
"v_readlane_b32 s10, v254, 6\n" \
"v_readlane_b32 s11, v254, 7\n" \
"v_readlane_b32 s12, v254, 8\n" \
"v_readlane_b32 s13, v254, 9\n" \
"v_readlane_b32 s14, v254, 10\n" \
"v_readlane_b32 s15, v254, 11\n" \
"v_readlane_b32 s16, v254, 12\n" \
"v_readlane_b32 s17, v254, 13\n" \
"v_readlane_b32 s18, v254, 14\n" \
"v_readlane_b32 s19, v254, 15\n" \
"v_readlane_b32 s20, v254, 16\n" \
"v_readlane_b32 s21, v254, 17\n" \
"v_readlane_b32 s22, v254, 18\n" \
"v_readlane_b32 s23, v254, 19\n" \
"v_readlane_b32 s24, v254, 20\n" \
"v_readlane_b32 s25, v254, 21\n" \
"v_readlane_b32 s26, v254, 22\n" \
"v_readlane_b32 s27, v254, 23\n" \
"v_readlane_b32 s28, v254, 24\n" \
"v_readlane_b32 s29, v254, 25\n" \
"v_readlane_b32 s30, v254, 26\n" \
"v_readlane_b32 s31, v254, 27\n" \
"v_readlane_b32 s32, v254, 28\n" \
"v_readlane_b32 s33, v254, 29\n" \
"v_readlane_b32 s34, v254, 30\n" \
"v_readlane_b32 s35, v254, 31\n" \
"v_readlane_b32 s36, v254, 32\n" \
"v_readlane_b32 s37, v254, 33\n" \
"v_readlane_b32 s38, v254, 34\n" \
"v_readlane_b32 s39, v254, 35\n" \
"s_mov_b32 s40, 0\n" \
"s_mov_b32 s51, 0\n" \
"s_nop 0\n" \
"v_mov_b32 v252, s38\n" \
"s_nop 4\n" \
"v_mov_b32 v188, -1\n" \
"s_mov_b32 s28, s33\n" \
"s_mov_b32 s33, -1\n" \
"v_writelane_b32 v251, s6, 0\n" \
"v_writelane_b32 v251, s10, 1\n" \
"v_writelane_b32 v251, s14, 2\n" \
"v_writelane_b32 v251, s18, 3\n" \
"s_mov_b32 s6, 0\n" \
"s_mov_b32 s10, 0\n" \
"s_mov_b32 s14, 0\n" \
"s_mov_b32 s18, 0\n" \
"s_mov_b32 s40, -1\n" \
"s_branch .LS1_pre\n" \
".LS1_wet:\n" \
"s_waitcnt vmcnt(0)\n" \
"v_readlane_b32 s6, v251, 0\n" \
"v_readlane_b32 s10, v251, 1\n" \
"v_readlane_b32 s14, v251, 2\n" \
"v_readlane_b32 s18, v251, 3\n" \
"s_nop 4\n" \
"s_mov_b32 s33, s28\n" \
"s_add_u32 s34, s33, 1\n" \
"s_add_u32 s36, s33, 2\n" \
"s_add_u32 s43, s30, s33\n" \
"s_mov_b32 s50, 0\n" \
".LS1_w7l:\n" \
"buffer_load_dword v240, v204, s[4:7], 0 offen sc1\n" \
"s_waitcnt vmcnt(0)\n" \
".LS1_w7c:\n" \
"v_subrev_u32 v241, s43, v240\n" \
"v_cmp_gt_u32 vcc, 0x100, v241\n" \
"s_cmp_eq_u64 vcc, -1\n" \
"s_cbranch_scc1 .LS1_w7d\n" \
"s_sleep 2\n" \
"s_add_u32 s50, s50, 1\n" \
"s_cmp_lt_u32 s50, 0x20000\n" \
"s_cbranch_scc1 .LS1_w7l\n" \
"s_branch .LS1_end\n" \
".LS1_w7d:\n" \
"v_readlane_b32 s43, v202, 11\n" \
"s_nop 4\n" \
"buffer_load_dwordx4 a[88:91], v201, s[8:11], s43 offen sc1\n" \
"buffer_load_dwordx4 a[92:95], v201, s[8:11], s43 offen offset:1024 sc1\n" \
"v_readlane_b32 s43, v202, 13\n" \
"s_nop 4\n" \
"buffer_load_dwordx4 a[104:107], v201, s[8:11], s43 offen sc1\n" \
"buffer_load_dwordx4 a[108:111], v201, s[8:11], s43 offen offset:1024 sc1\n" \
"v_readlane_b32 s43, v202, 15\n" \
"s_nop 4\n" \
"buffer_load_dwordx4 a[120:123], v201, s[8:11], s43 offen sc1\n" \
"buffer_load_dwordx4 a[124:127], v201, s[8:11], s43 offen offset:1024 sc1\n" \
"v_readlane_b32 s43, v202, 16\n" \
"s_nop 4\n" \
"buffer_load_dwordx4 a[128:131], v201, s[8:11], s43 offen sc1\n" \
"buffer_load_dwordx4 a[132:135], v201, s[8:11], s43 offen offset:1024 sc1\n" \
"v_readlane_b32 s43, v202, 17\n" \
"s_nop 4\n" \
"buffer_load_dwordx4 a[136:139], v201, s[8:11], s43 offen sc1\n" \
"buffer_load_dwordx4 a[140:143], v201, s[8:11], s43 offen offset:1024 sc1\n" \
"v_readlane_b32 s43, v202, 20\n" \
"s_nop 4\n" \
"buffer_load_dwordx4 a[160:163], v201, s[8:11], s43 offen sc1\n" \
"buffer_load_dwordx4 a[164:167], v201, s[8:11], s43 offen offset:1024 sc1\n" \
"v_readlane_b32 s43, v202, 23\n" \
"s_nop 4\n" \
"buffer_load_dwordx4 a[184:187], v201, s[8:11], s43 offen sc1\n" \
"buffer_load_dwordx4 a[188:191], v201, s[8:11], s43 offen offset:1024 sc1\n" \
"v_readlane_b32 s43, v202, 26\n" \
"s_nop 4\n" \
"buffer_load_dwordx4 a[208:211], v201, s[8:11], s43 offen sc1\n" \
"buffer_load_dwordx4 a[212:215], v201, s[8:11], s43 offen offset:1024 sc1\n" \
"s_add_u32 s43, s30, s34\n" \
"s_mov_b32 s50, 0\n" \
".LS1_w8l:\n" \
"buffer_load_dword v240, v204, s[4:7], 0 offen sc1\n" \
"s_waitcnt vmcnt(0)\n" \
".LS1_w8c:\n" \
"v_subrev_u32 v241, s43, v240\n" \
"v_cmp_gt_u32 vcc, 0x100, v241\n" \
"s_cmp_eq_u64 vcc, -1\n" \
"s_cbranch_scc1 .LS1_w8d\n" \
"s_sleep 2\n" \
"s_add_u32 s50, s50, 1\n" \
"s_cmp_lt_u32 s50, 0x20000\n" \
"s_cbranch_scc1 .LS1_w8l\n" \
"s_branch .LS1_end\n" \
".LS1_w8d:\n" \
"v_readlane_b32 s43, v202, 0\n" \
"s_nop 4\n" \
"buffer_load_dwordx4 a[0:3], v201, s[8:11], s43 offen sc1\n" \
"buffer_load_dwordx4 a[4:7], v201, s[8:11], s43 offen offset:1024 sc1\n" \
"v_readlane_b32 s43, v202, 1\n" \
"s_nop 4\n" \
"buffer_load_dwordx4 a[8:11], v201, s[8:11], s43 offen sc1\n" \
"buffer_load_dwordx4 a[12:15], v201, s[8:11], s43 offen offset:1024 sc1\n" \
"v_readlane_b32 s43, v202, 2\n" \
"s_nop 4\n" \
"buffer_load_dwordx4 a[16:19], v201, s[8:11], s43 offen sc1\n" \
"buffer_load_dwordx4 a[20:23], v201, s[8:11], s43 offen offset:1024 sc1\n" \
"v_readlane_b32 s43, v202, 3\n" \
"s_nop 4\n" \
"buffer_load_dwordx4 a[24:27], v201, s[8:11], s43 offen sc1\n" \
"buffer_load_dwordx4 a[28:31], v201, s[8:11], s43 offen offset:1024 sc1\n" \
"v_readlane_b32 s43, v202, 4\n" \
"s_nop 4\n" \
"buffer_load_dwordx4 a[32:35], v201, s[8:11], s43 offen sc1\n" \
"buffer_load_dwordx4 a[36:39], v201, s[8:11], s43 offen offset:1024 sc1\n" \
"v_readlane_b32 s43, v202, 5\n" \
"s_nop 4\n" \
"buffer_load_dwordx4 a[40:43], v201, s[8:11], s43 offen sc1\n" \
"buffer_load_dwordx4 a[44:47], v201, s[8:11], s43 offen offset:1024 sc1\n" \
"v_readlane_b32 s43, v202, 6\n" \
"s_nop 4\n" \
"buffer_load_dwordx4 a[48:51], v201, s[8:11], s43 offen sc1\n" \
"buffer_load_dwordx4 a[52:55], v201, s[8:11], s43 offen offset:1024 sc1\n" \
"v_readlane_b32 s43, v202, 7\n" \
"s_nop 4\n" \
"buffer_load_dwordx4 a[56:59], v201, s[8:11], s43 offen sc1\n" \
"buffer_load_dwordx4 a[60:63], v201, s[8:11], s43 offen offset:1024 sc1\n" \
"s_add_u32 s43, s30, s36\n" \
"s_mov_b32 s50, 0\n" \
".LS1_w9l:\n" \
"buffer_load_dword v240, v204, s[4:7], 0 offen sc1\n" \
"s_waitcnt vmcnt(0)\n" \
".LS1_w9c:\n" \
"v_subrev_u32 v241, s43, v240\n" \
"v_cmp_gt_u32 vcc, 0x100, v241\n" \
"s_cmp_eq_u64 vcc, -1\n" \
"s_cbranch_scc1 .LS1_w9d\n" \
"s_sleep 2\n" \
"s_add_u32 s50, s50, 1\n" \
"s_cmp_lt_u32 s50, 0x20000\n" \
"s_cbranch_scc1 .LS1_w9l\n" \
"s_branch .LS1_end\n" \
".LS1_w9d:\n" \
"v_readlane_b32 s43, v202, 8\n" \
"s_nop 4\n" \
"buffer_load_dwordx4 a[64:67], v201, s[8:11], s43 offen sc1\n" \
"buffer_load_dwordx4 a[68:71], v201, s[8:11], s43 offen offset:1024 sc1\n" \
"v_readlane_b32 s43, v202, 9\n" \
"s_nop 4\n" \
"buffer_load_dwordx4 a[72:75], v201, s[8:11], s43 offen sc1\n" \
"buffer_load_dwordx4 a[76:79], v201, s[8:11], s43 offen offset:1024 sc1\n" \
"v_readlane_b32 s43, v202, 10\n" \
"s_nop 4\n" \
"buffer_load_dwordx4 a[80:83], v201, s[8:11], s43 offen sc1\n" \
"buffer_load_dwordx4 a[84:87], v201, s[8:11], s43 offen offset:1024 sc1\n" \
"v_readlane_b32 s43, v202, 12\n" \
"s_nop 4\n" \
"buffer_load_dwordx4 a[96:99], v201, s[8:11], s43 offen sc1\n" \
"buffer_load_dwordx4 a[100:103], v201, s[8:11], s43 offen offset:1024 sc1\n" \
"v_readlane_b32 s43, v202, 14\n" \
"s_nop 4\n" \
"buffer_load_dwordx4 a[112:115], v201, s[8:11], s43 offen sc1\n" \
"buffer_load_dwordx4 a[116:119], v201, s[8:11], s43 offen offset:1024 sc1\n" \
"v_readlane_b32 s43, v202, 19\n" \
"s_nop 4\n" \
"buffer_load_dwordx4 a[152:155], v201, s[8:11], s43 offen sc1\n" \
"buffer_load_dwordx4 a[156:159], v201, s[8:11], s43 offen offset:1024 sc1\n" \
"v_readlane_b32 s43, v202, 22\n" \
"s_nop 4\n" \
"buffer_load_dwordx4 a[176:179], v201, s[8:11], s43 offen sc1\n" \
"buffer_load_dwordx4 a[180:183], v201, s[8:11], s43 offen offset:1024 sc1\n" \
"v_readlane_b32 s43, v202, 25\n" \
"s_nop 4\n" \
"buffer_load_dwordx4 a[200:203], v201, s[8:11], s43 offen sc1\n" \
"buffer_load_dwordx4 a[204:207], v201, s[8:11], s43 offen offset:1024 sc1\n" \
"s_add_u32 s43, s30, 4\n" \
"s_mov_b32 s50, 0\n" \
".LS1_w10l:\n" \
"buffer_load_dword v240, v204, s[4:7], 0 offen sc1\n" \
"s_waitcnt vmcnt(0)\n" \
".LS1_w10c:\n" \
"v_subrev_u32 v241, s43, v240\n" \
"v_cmp_gt_u32 vcc, 0x100, v241\n" \
"s_cmp_eq_u64 vcc, -1\n" \
"s_cbranch_scc1 .LS1_w10d\n" \
"s_sleep 2\n" \
"s_add_u32 s50, s50, 1\n" \
"s_cmp_lt_u32 s50, 0x20000\n" \
"s_cbranch_scc1 .LS1_w10l\n" \
"s_branch .LS1_end\n" \
".LS1_w10d:\n" \
"v_readlane_b32 s43, v202, 18\n" \
"s_nop 4\n" \
"buffer_load_dwordx4 a[144:147], v201, s[8:11], s43 offen sc1\n" \
"buffer_load_dwordx4 a[148:151], v201, s[8:11], s43 offen offset:1024 sc1\n" \
"v_readlane_b32 s43, v202, 21\n" \
"s_nop 4\n" \
"buffer_load_dwordx4 a[168:171], v201, s[8:11], s43 offen sc1\n" \
"buffer_load_dwordx4 a[172:175], v201, s[8:11], s43 offen offset:1024 sc1\n" \
"v_readlane_b32 s43, v202, 24\n" \
"s_nop 4\n" \
"buffer_load_dwordx4 a[192:195], v201, s[8:11], s43 offen sc1\n" \
"buffer_load_dwordx4 a[196:199], v201, s[8:11], s43 offen offset:1024 sc1\n" \
"v_readlane_b32 s43, v202, 27\n" \
"s_nop 4\n" \
"buffer_load_dwordx4 a[216:219], v201, s[8:11], s43 offen sc1\n" \
"buffer_load_dwordx4 a[220:223], v201, s[8:11], s43 offen offset:1024 sc1\n" \
"s_mov_b32 s33, -1\n" \
"s_add_u32 s43, s30, 1\n" \
"s_mov_b32 s50, 0\n" \
".LS1_w11l:\n" \
"buffer_load_dword v240, v205, s[4:7], 0 offen sc1\n" \
"s_waitcnt vmcnt(0)\n" \
".LS1_w11c:\n" \
"v_subrev_u32 v241, s43, v240\n" \
"v_cmp_gt_u32 vcc, 0x100, v241\n" \
"s_cmp_eq_u64 vcc, -1\n" \
"s_cbranch_scc1 .LS1_w11d\n" \
"s_sleep 2\n" \
"s_add_u32 s50, s50, 1\n" \
"s_cmp_lt_u32 s50, 0x20000\n" \
"s_cbranch_scc1 .LS1_w11l\n" \
"s_branch .LS1_end\n" \
".LS1_w11d:\n" \
"s_add_u32 s43, s30, 1\n" \
"s_mov_b32 s50, 0\n" \
".LS1_w12l:\n" \
"buffer_load_dword v240, v206, s[4:7], 0 offen sc1\n" \
"s_waitcnt vmcnt(0)\n" \
".LS1_w12c:\n" \
"v_subrev_u32 v241, s43, v240\n" \
"v_cmp_gt_u32 vcc, 0x100, v241\n" \
"s_cmp_eq_u64 vcc, -1\n" \
"s_cbranch_scc1 .LS1_w12d\n" \
"s_sleep 2\n" \
"s_add_u32 s50, s50, 1\n" \
"s_cmp_lt_u32 s50, 0x20000\n" \
"s_cbranch_scc1 .LS1_w12l\n" \
"s_branch .LS1_end\n" \
".LS1_w12d:\n" \
".LS1_pre:\n" \
"s_barrier\n" \
"ds_read_b96 v[248:250], v252\n" \
"s_waitcnt lgkmcnt(0)\n" \
"v_readfirstlane_b32 s44, v248\n" \
"v_readfirstlane_b32 s45, v249\n" \
"v_readfirstlane_b32 s46, v250\n" \
"s_mul_i32 s42, s25, s46\n" \
"s_lshr_b32 s42, s42, 12\n" \
".LS1_loop:\n" \
"s_add_u32 s43, s40, 0\n" \
"s_and_b32 s43, s43, 3\n" \
"s_bitcmp1_b32 s43, 0\n" \
"s_cselect_b32 s47, s32, s31\n" \
"s_lshl_b32 s43, s43, 19\n" \
"s_add_u32 s47, s47, s43\n" \
"s_sub_u32 s43, s24, s44\n" \
"s_max_i32 s43, s43, 0xfffd0000\n" \
"s_min_i32 s43, s43, 0x4000\n" \
"s_mul_i32 s43, s43, s46\n" \
"s_ashr_i32 s43, s43, 12\n" \
"s_add_u32 s41, s45, s43\n" \
"s_add_u32 s41, s41, 4\n" \
"s_branch .LS1_a1\n" \
".LS1_retry:\n" \
"s_add_u32 s51, s51, 1\n" \
"s_cmp_gt_u32 s51, 0x20000\n" \
"s_cbranch_scc1 .LS1_end\n" \
".LS1_a0:\n" \
"s_add_u32 s41, s41, s42\n" \
"s_add_u32 s24, s24, s25\n" \
"s_xor_b32 s31, s31, s29\n" \
"s_xor_b32 s32, s32, s29\n" \
"s_xor_b32 s47, s47, s29\n" \
"s_xor_b32 s48, s48, s29\n" \
"s_add_u32 s39, s39, 1\n" \
".p2align 6\n" \
".LS1_a1:\n" \
"s_memtime vcc\n" \
"s_waitcnt lgkmcnt(0)\n" \
"s_sub_u32 s43, s41, vcc_lo\n" \
"s_cmp_gt_i32 s43, 128\n" \
"s_cbranch_scc1 .LS1_a1\n" \
"s_cmp_lt_i32 s43, s37\n" \
"s_cbranch_scc1 .LS1_a0\n" \
"s_max_i32 s43, s43, 0\n" \
"s_and_b32 s43, s43, -4\n" \
"s_getpc_b64 vcc\n" \
".LS1_a3:\n" \
"s_sub_u32 s43, .LS1_a4-.LS1_a3, s43\n" \
"s_add_u32 vcc_lo, vcc_lo, s43\n" \
"s_addc_u32 vcc_hi, vcc_hi, 0\n" \
"s_setpc_b64 vcc\n" \
".rept 32\n" \
"s_nop 0\n" \
".endr\n" \
".LS1_a4:\n" \
"buffer_load_dwordx4 v[0:3], v208, s[4:7], s47 offen offset:1024 sc1\n" \
"s_nop 9\n" \
"buffer_load_dwordx4 v[4:7], v208, s[4:7], s47 offen offset:2048 sc1\n" \
"s_nop 9\n" \
"buffer_load_dwordx4 v[8:11], v208, s[4:7], s47 offen sc1\n" \
"s_nop 9\n" \
"buffer_load_dwordx4 v[12:15], v210, s[4:7], s47 offen sc1\n" \
"s_add_u32 s35, s35, 1\n" \
"v_mov_b32 v28, 0\n" \
"v_mov_b32 v29, 0\n" \
"v_mov_b32 v30, 0\n" \
"v_mov_b32 v31, 0\n" \
"v_mov_b32 v32, 0\n" \
"v_mov_b32 v33, 0\n" \
"v_mov_b32 v34, 0\n" \
"v_mov_b32 v35, 0\n" \
"v_mov_b32 v36, 0\n" \
"v_mov_b32 v37, 0\n" \
"v_mov_b32 v38, 0\n" \
"v_mov_b32 v39, 0\n" \
"v_mov_b32 v40, 0\n" \
"v_mov_b32 v41, 0\n" \
"v_mov_b32 v42, 0\n" \
"v_mov_b32 v43, 0\n" \
"v_mov_b32 v44, 0\n" \
"v_mov_b32 v45, 0\n" \
"v_mov_b32 v46, 0\n" \
"v_mov_b32 v47, 0\n" \
"v_mov_b32 v48, 0\n" \
"v_mov_b32 v49, 0\n" \
"v_mov_b32 v50, 0\n" \
"v_mov_b32 v51, 0\n" \
"v_mov_b32 v52, 0\n" \
"v_mov_b32 v53, 0\n" \
"v_mov_b32 v54, 0\n" \
"v_mov_b32 v55, 0\n" \
"v_mov_b32 v56, 0\n" \
"v_mov_b32 v57, 0\n" \
"v_mov_b32 v58, 0\n" \
"v_mov_b32 v59, 0\n" \
"s_waitcnt vmcnt(3)\n" \
"v_smfmac_f32_16x16x64_bf16 v[28:31], v[0:3], a[0:7], v200\n" \
"v_smfmac_f32_16x16x64_bf16 v[32:35], v[0:3], a[8:15], v200\n" \
"v_smfmac_f32_16x16x64_bf16 v[36:39], v[0:3], a[16:23], v200\n" \
"v_smfmac_f32_16x16x64_bf16 v[40:43], v[0:3], a[24:31], v200\n" \
"v_smfmac_f32_16x16x64_bf16 v[44:47], v[0:3], a[32:39], v200\n" \
"v_smfmac_f32_16x16x64_bf16 v[48:51], v[0:3], a[40:47], v200\n" \
"v_smfmac_f32_16x16x64_bf16 v[52:55], v[0:3], a[48:55], v200\n" \
"v_smfmac_f32_16x16x64_bf16 v[56:59], v[0:3], a[56:63], v200\n" \
"s_waitcnt vmcnt(2)\n" \
"v_smfmac_f32_16x16x64_bf16 v[28:31], v[4:7], a[64:71], v200\n" \
"v_smfmac_f32_16x16x64_bf16 v[32:35], v[4:7], a[72:79], v200\n" \
"v_smfmac_f32_16x16x64_bf16 v[36:39], v[4:7], a[80:87], v200\n" \
"s_waitcnt vmcnt(1)\n" \
"v_smfmac_f32_16x16x64_bf16 v[28:31], v[8:11], a[88:95], v200\n" \
"s_nop 2\n" \
"v_smfmac_f32_16x16x64_bf16 v[40:43], v[4:7], a[96:103], v200\n" \
"s_nop 2\n" \
"v_smfmac_f32_16x16x64_bf16 v[32:35], v[8:11], a[104:111], v200\n" \
"s_nop 0\n" \
"v_cvt_pk_bf16_f32 v128, v28, v29\n" \
"v_cvt_pk_bf16_f32 v129, v30, v31\n" \
"v_smfmac_f32_16x16x64_bf16 v[44:47], v[4:7], a[112:119], v200\n" \
"ds_write_b64 v211, v[128:129] offset:128\n" \
"s_nop 1\n" \
"v_smfmac_f32_16x16x64_bf16 v[36:39], v[8:11], a[120:127], v200\n" \
"s_nop 0\n" \
"v_cvt_pk_bf16_f32 v130, v32, v33\n" \
"v_cvt_pk_bf16_f32 v131, v34, v35\n" \
"v_smfmac_f32_16x16x64_bf16 v[44:47], v[8:11], a[128:135], v200\n" \
"ds_write_b64 v211, v[130:131] offset:2304\n" \
"s_nop 1\n" \
"v_smfmac_f32_16x16x64_bf16 v[40:43], v[8:11], a[136:143], v200\n" \
"s_waitcnt vmcnt(0)\n" \
"v_cvt_pk_bf16_f32 v132, v36, v37\n" \
"v_cvt_pk_bf16_f32 v133, v38, v39\n" \
"v_smfmac_f32_16x16x64_bf16 v[44:47], v[12:15], a[144:151], v200\n" \
"ds_write_b64 v211, v[132:133] offset:2688\n" \
"s_nop 1\n" \
"v_smfmac_f32_16x16x64_bf16 v[48:51], v[4:7], a[152:159], v200\n" \
"s_nop 0\n" \
"v_cvt_pk_bf16_f32 v134, v40, v41\n" \
"v_cvt_pk_bf16_f32 v135, v42, v43\n" \
"v_smfmac_f32_16x16x64_bf16 v[48:51], v[8:11], a[160:167], v200\n" \
"ds_write_b64 v211, v[134:135] offset:4864\n" \
"v_cvt_pk_bf16_f32 v136, v44, v45\n" \
"v_cvt_pk_bf16_f32 v137, v46, v47\n" \
"v_smfmac_f32_16x16x64_bf16 v[48:51], v[12:15], a[168:175], v200\n" \
"ds_write_b64 v211, v[136:137] offset:9344\n" \
"s_nop 1\n" \
"v_smfmac_f32_16x16x64_bf16 v[52:55], v[4:7], a[176:183], v200\n" \
"s_nop 2\n" \
"v_smfmac_f32_16x16x64_bf16 v[52:55], v[8:11], a[184:191], v200\n" \
"s_nop 0\n" \
"v_cvt_pk_bf16_f32 v138, v48, v49\n" \
"v_cvt_pk_bf16_f32 v139, v50, v51\n" \
"v_smfmac_f32_16x16x64_bf16 v[52:55], v[12:15], a[192:199], v200\n" \
"ds_write_b64 v211, v[138:139] offset:11520\n" \
"s_waitcnt lgkmcnt(2)\n" \
"s_barrier\n" \
"v_smfmac_f32_16x16x64_bf16 v[56:59], v[4:7], a[200:207], v200\n" \
"s_nop 2\n" \
"v_smfmac_f32_16x16x64_bf16 v[56:59], v[8:11], a[208:215], v200\n" \
"s_nop 2\n" \
"v_smfmac_f32_16x16x64_bf16 v[56:59], v[12:15], a[216:223], v200\n" \
"v_cvt_pk_bf16_f32 v140, v52, v53\n" \
"v_cvt_pk_bf16_f32 v141, v54, v55\n" \
"s_nop 1\n" \
"ds_write_b64 v211, v[140:141] offset:11904\n" \
"s_nop 2\n" \
"v_cvt_pk_bf16_f32 v142, v56, v57\n" \
"v_cvt_pk_bf16_f32 v143, v58, v59\n" \
"ds_write_b64 v211, v[142:143] offset:14080\n" \
"s_waitcnt lgkmcnt(2)\n" \
"s_barrier\n" \
"s_waitcnt lgkmcnt(1)\n" \
"s_barrier\n" \
"s_waitcnt lgkmcnt(0)\n" \
"s_barrier\n" \
"s_nop 3\n" \
"ds_read_b32 v64, v252 offset:20\n" \
"s_waitcnt lgkmcnt(0)\n" \
"v_readfirstlane_b32 s43, v64\n" \
"s_cmp_eq_u32 s43, s35\n" \
"s_cbranch_scc1 .LS1_retry\n" \
"s_nop 15\n" \
"v_max3_u32 v64, v0, v0, v1\n" \
"v_max3_u32 v64, v64, v2, v3\n" \
"v_max3_u32 v64, v64, v4, v5\n" \
"v_max3_u32 v64, v64, v6, v7\n" \
"v_max3_u32 v64, v64, v8, v9\n" \
"v_max3_u32 v64, v64, v10, v11\n" \
"v_max3_u32 v64, v64, v12, v13\n" \
"v_max3_u32 v64, v64, v14, v15\n" \
"v_cmp_eq_u32 vcc, -1, v64\n" \
"s_cbranch_vccnz .LS1_veto\n" \
".LS1_back:\n" \
"s_waitcnt lgkmcnt(0)\n" \
"s_barrier\n" \
"ds_read_b32 v64, v252 offset:16\n" \
"s_waitcnt lgkmcnt(0)\n" \
"v_readfirstlane_b32 s43, v64\n" \
"s_cmp_eq_u32 s43, s35\n" \
"s_cbranch_scc1 .LS1_retry\n" \
"s_waitcnt lgkmcnt(0)\n" \
"s_barrier\n" \
"ds_read_b64 v[246:247], v252 offset:24\n" \
"ds_read_b96 v[248:250], v252\n" \
"s_waitcnt lgkmcnt(0)\n" \
"v_readfirstlane_b32 s44, v248\n" \
"v_readfirstlane_b32 s45, v249\n" \
"v_readfirstlane_b32 s46, v250\n" \
"v_readfirstlane_b32 s26, v246\n" \
"v_readfirstlane_b32 s27, v247\n" \
"s_mul_i32 s42, s25, s46\n" \
"s_lshr_b32 s42, s42, 12\n" \
"s_add_u32 s40, s40, 1\n" \
"s_add_u32 s24, s24, s26\n" \
"s_add_u32 s24, s24, s27\n" \
"s_cmp_eq_u32 s40, 0\n" \
"s_cbranch_scc1 .LS1_wet\n" \
"s_cmp_lt_u32 s40, 63\n" \
"s_cbranch_scc1 .LS1_loop\n" \
".LS1_end:\n" \
"s_endpgm\n" \
".LS1_veto:\n" \
"v_mov_b32 v64, s35\n" \
"ds_write_b32 v252, v64 offset:16\n" \
"s_branch .LS1_back\n" \
".p2align 6\n" \
"fk_role_S2:\n" \
"ds_read_b32 v200, v255 offset:0\n" \
"ds_read_b32 v201, v255 offset:256\n" \
"ds_read_b32 v202, v255 offset:512\n" \
"ds_read_b32 v203, v255 offset:768\n" \
"ds_read_b32 v204, v255 offset:1024\n" \
"ds_read_b32 v205, v255 offset:1280\n" \
"ds_read_b32 v206, v255 offset:1536\n" \
"ds_read_b32 v207, v255 offset:1792\n" \
"ds_read_b32 v208, v255 offset:2048\n" \
"ds_read_b32 v209, v255 offset:2304\n" \
"ds_read_b32 v210, v255 offset:2560\n" \
"ds_read_b32 v211, v255 offset:2816\n" \
"ds_read_b32 v212, v255 offset:3072\n" \
"ds_read_b32 v213, v255 offset:3328\n" \
"ds_read_b32 v214, v255 offset:3584\n" \
"ds_read_b32 v215, v255 offset:3840\n" \
"ds_read_b32 v254, v255 offset:7936\n" \
"s_waitcnt lgkmcnt(0)\n" \
"v_readlane_b32 s4, v254, 0\n" \
"v_readlane_b32 s5, v254, 1\n" \
"v_readlane_b32 s6, v254, 2\n" \
"v_readlane_b32 s7, v254, 3\n" \
"v_readlane_b32 s8, v254, 4\n" \
"v_readlane_b32 s9, v254, 5\n" \
"v_readlane_b32 s10, v254, 6\n" \
"v_readlane_b32 s11, v254, 7\n" \
"v_readlane_b32 s12, v254, 8\n" \
"v_readlane_b32 s13, v254, 9\n" \
"v_readlane_b32 s14, v254, 10\n" \
"v_readlane_b32 s15, v254, 11\n" \
"v_readlane_b32 s16, v254, 12\n" \
"v_readlane_b32 s17, v254, 13\n" \
"v_readlane_b32 s18, v254, 14\n" \
"v_readlane_b32 s19, v254, 15\n" \
"v_readlane_b32 s20, v254, 16\n" \
"v_readlane_b32 s21, v254, 17\n" \
"v_readlane_b32 s22, v254, 18\n" \
"v_readlane_b32 s23, v254, 19\n" \
"v_readlane_b32 s24, v254, 20\n" \
"v_readlane_b32 s25, v254, 21\n" \
"v_readlane_b32 s26, v254, 22\n" \
"v_readlane_b32 s27, v254, 23\n" \
"v_readlane_b32 s28, v254, 24\n" \
"v_readlane_b32 s29, v254, 25\n" \
"v_readlane_b32 s30, v254, 26\n" \
"v_readlane_b32 s31, v254, 27\n" \
"v_readlane_b32 s32, v254, 28\n" \
"v_readlane_b32 s33, v254, 29\n" \
"v_readlane_b32 s34, v254, 30\n" \
"v_readlane_b32 s35, v254, 31\n" \
"v_readlane_b32 s36, v254, 32\n" \
"v_readlane_b32 s37, v254, 33\n" \
"v_readlane_b32 s38, v254, 34\n" \
"v_readlane_b32 s39, v254, 35\n" \
"s_mov_b32 s40, 0\n" \
"s_mov_b32 s51, 0\n" \
"s_nop 0\n" \
"v_mov_b32 v252, s38\n" \
"s_nop 4\n" \
"v_mov_b32 v188, -1\n" \
"s_mov_b32 s28, s33\n" \
"s_mov_b32 s33, -1\n" \
"v_writelane_b32 v251, s6, 0\n" \
"v_writelane_b32 v251, s10, 1\n" \
"v_writelane_b32 v251, s14, 2\n" \
"v_writelane_b32 v251, s18, 3\n" \
"s_mov_b32 s6, 0\n" \
"s_mov_b32 s10, 0\n" \
"s_mov_b32 s14, 0\n" \
"s_mov_b32 s18, 0\n" \
"s_mov_b32 s40, -1\n" \
"s_branch .LS2_pre\n" \
".LS2_wet:\n" \
"s_waitcnt vmcnt(0)\n" \
"v_readlane_b32 s6, v251, 0\n" \
"v_readlane_b32 s10, v251, 1\n" \
"v_readlane_b32 s14, v251, 2\n" \
"v_readlane_b32 s18, v251, 3\n" \
"s_nop 4\n" \
"s_mov_b32 s33, s28\n" \
"s_add_u32 s34, s33, 1\n" \
"s_add_u32 s36, s33, 2\n" \
"s_add_u32 s43, s30, s33\n" \
"s_mov_b32 s50, 0\n" \
".LS2_w13l:\n" \
"buffer_load_dword v240, v204, s[4:7], 0 offen sc1\n" \
"s_waitcnt vmcnt(0)\n" \
".LS2_w13c:\n" \
"v_subrev_u32 v241, s43, v240\n" \
"v_cmp_gt_u32 vcc, 0x100, v241\n" \
"s_cmp_eq_u64 vcc, -1\n" \
"s_cbranch_scc1 .LS2_w13d\n" \
"s_sleep 2\n" \
"s_add_u32 s50, s50, 1\n" \
"s_cmp_lt_u32 s50, 0x20000\n" \
"s_cbranch_scc1 .LS2_w13l\n" \
"s_branch .LS2_end\n" \
".LS2_w13d:\n" \
"v_readlane_b32 s43, v202, 8\n" \
"s_nop 4\n" \
"buffer_load_dwordx4 a[64:67], v201, s[8:11], s43 offen sc1\n" \
"buffer_load_dwordx4 a[68:71], v201, s[8:11], s43 offen offset:1024 sc1\n" \
"v_readlane_b32 s43, v202, 9\n" \
"s_nop 4\n" \
"buffer_load_dwordx4 a[72:75], v201, s[8:11], s43 offen sc1\n" \
"buffer_load_dwordx4 a[76:79], v201, s[8:11], s43 offen offset:1024 sc1\n" \
"v_readlane_b32 s43, v202, 10\n" \
"s_nop 4\n" \
"buffer_load_dwordx4 a[80:83], v201, s[8:11], s43 offen sc1\n" \
"buffer_load_dwordx4 a[84:87], v201, s[8:11], s43 offen offset:1024 sc1\n" \
"v_readlane_b32 s43, v202, 12\n" \
"s_nop 4\n" \
"buffer_load_dwordx4 a[96:99], v201, s[8:11], s43 offen sc1\n" \
"buffer_load_dwordx4 a[100:103], v201, s[8:11], s43 offen offset:1024 sc1\n" \
"v_readlane_b32 s43, v202, 14\n" \
"s_nop 4\n" \
"buffer_load_dwordx4 a[112:115], v201, s[8:11], s43 offen sc1\n" \
"buffer_load_dwordx4 a[116:119], v201, s[8:11], s43 offen offset:1024 sc1\n" \
"v_readlane_b32 s43, v202, 19\n" \
"s_nop 4\n" \
"buffer_load_dwordx4 a[152:155], v201, s[8:11], s43 offen sc1\n" \
"buffer_load_dwordx4 a[156:159], v201, s[8:11], s43 offen offset:1024 sc1\n" \
"v_readlane_b32 s43, v202, 22\n" \
"s_nop 4\n" \
"buffer_load_dwordx4 a[176:179], v201, s[8:11], s43 offen sc1\n" \
"buffer_load_dwordx4 a[180:183], v201, s[8:11], s43 offen offset:1024 sc1\n" \
"v_readlane_b32 s43, v202, 25\n" \
"s_nop 4\n" \
"buffer_load_dwordx4 a[200:203], v201, s[8:11], s43 offen sc1\n" \
"buffer_load_dwordx4 a[204:207], v201, s[8:11], s43 offen offset:1024 sc1\n" \
"s_add_u32 s43, s30, s34\n" \
"s_mov_b32 s50, 0\n" \
".LS2_w14l:\n" \
"buffer_load_dword v240, v204, s[4:7], 0 offen sc1\n" \
"s_waitcnt vmcnt(0)\n" \
".LS2_w14c:\n" \
"v_subrev_u32 v241, s43, v240\n" \
"v_cmp_gt_u32 vcc, 0x100, v241\n" \
"s_cmp_eq_u64 vcc, -1\n" \
"s_cbranch_scc1 .LS2_w14d\n" \
"s_sleep 2\n" \
"s_add_u32 s50, s50, 1\n" \
"s_cmp_lt_u32 s50, 0x20000\n" \
"s_cbranch_scc1 .LS2_w14l\n" \
"s_branch .LS2_end\n" \
".LS2_w14d:\n" \
"v_readlane_b32 s43, v202, 11\n" \
"s_nop 4\n" \
"buffer_load_dwordx4 a[88:91], v201, s[8:11], s43 offen sc1\n" \
"buffer_load_dwordx4 a[92:95], v201, s[8:11], s43 offen offset:1024 sc1\n" \
"v_readlane_b32 s43, v202, 13\n" \
"s_nop 4\n" \
"buffer_load_dwordx4 a[104:107], v201, s[8:11], s43 offen sc1\n" \
"buffer_load_dwordx4 a[108:111], v201, s[8:11], s43 offen offset:1024 sc1\n" \
"v_readlane_b32 s43, v202, 15\n" \
"s_nop 4\n" \
"buffer_load_dwordx4 a[120:123], v201, s[8:11], s43 offen sc1\n" \
"buffer_load_dwordx4 a[124:127], v201, s[8:11], s43 offen offset:1024 sc1\n" \
"v_readlane_b32 s43, v202, 16\n" \
"s_nop 4\n" \
"buffer_load_dwordx4 a[128:131], v201, s[8:11], s43 offen sc1\n" \
"buffer_load_dwordx4 a[132:135], v201, s[8:11], s43 offen offset:1024 sc1\n" \
"v_readlane_b32 s43, v202, 17\n" \
"s_nop 4\n" \
"buffer_load_dwordx4 a[136:139], v201, s[8:11], s43 offen sc1\n" \
"buffer_load_dwordx4 a[140:143], v201, s[8:11], s43 offen offset:1024 sc1\n" \
"v_readlane_b32 s43, v202, 20\n" \
"s_nop 4\n" \
"buffer_load_dwordx4 a[160:163], v201, s[8:11], s43 offen sc1\n" \
"buffer_load_dwordx4 a[164:167], v201, s[8:11], s43 offen offset:1024 sc1\n" \
"v_readlane_b32 s43, v202, 23\n" \
"s_nop 4\n" \
"buffer_load_dwordx4 a[184:187], v201, s[8:11], s43 offen sc1\n" \
"buffer_load_dwordx4 a[188:191], v201, s[8:11], s43 offen offset:1024 sc1\n" \
"v_readlane_b32 s43, v202, 26\n" \
"s_nop 4\n" \
"buffer_load_dwordx4 a[208:211], v201, s[8:11], s43 offen sc1\n" \
"buffer_load_dwordx4 a[212:215], v201, s[8:11], s43 offen offset:1024 sc1\n" \
"s_add_u32 s43, s30, s36\n" \
"s_mov_b32 s50, 0\n" \
".LS2_w15l:\n" \
"buffer_load_dword v240, v204, s[4:7], 0 offen sc1\n" \
"s_waitcnt vmcnt(0)\n" \
".LS2_w15c:\n" \
"v_subrev_u32 v241, s43, v240\n" \
"v_cmp_gt_u32 vcc, 0x100, v241\n" \
"s_cmp_eq_u64 vcc, -1\n" \
"s_cbranch_scc1 .LS2_w15d\n" \
"s_sleep 2\n" \
"s_add_u32 s50, s50, 1\n" \
"s_cmp_lt_u32 s50, 0x20000\n" \
"s_cbranch_scc1 .LS2_w15l\n" \
"s_branch .LS2_end\n" \
".LS2_w15d:\n" \
"v_readlane_b32 s43, v202, 0\n" \
"s_nop 4\n" \
"buffer_load_dwordx4 a[0:3], v201, s[8:11], s43 offen sc1\n" \
"buffer_load_dwordx4 a[4:7], v201, s[8:11], s43 offen offset:1024 sc1\n" \
"v_readlane_b32 s43, v202, 1\n" \
"s_nop 4\n" \
"buffer_load_dwordx4 a[8:11], v201, s[8:11], s43 offen sc1\n" \
"buffer_load_dwordx4 a[12:15], v201, s[8:11], s43 offen offset:1024 sc1\n" \
"v_readlane_b32 s43, v202, 2\n" \
"s_nop 4\n" \
"buffer_load_dwordx4 a[16:19], v201, s[8:11], s43 offen sc1\n" \
"buffer_load_dwordx4 a[20:23], v201, s[8:11], s43 offen offset:1024 sc1\n" \
"v_readlane_b32 s43, v202, 3\n" \
"s_nop 4\n" \
"buffer_load_dwordx4 a[24:27], v201, s[8:11], s43 offen sc1\n" \
"buffer_load_dwordx4 a[28:31], v201, s[8:11], s43 offen offset:1024 sc1\n" \
"v_readlane_b32 s43, v202, 4\n" \
"s_nop 4\n" \
"buffer_load_dwordx4 a[32:35], v201, s[8:11], s43 offen sc1\n" \
"buffer_load_dwordx4 a[36:39], v201, s[8:11], s43 offen offset:1024 sc1\n" \
"v_readlane_b32 s43, v202, 5\n" \
"s_nop 4\n" \
"buffer_load_dwordx4 a[40:43], v201, s[8:11], s43 offen sc1\n" \
"buffer_load_dwordx4 a[44:47], v201, s[8:11], s43 offen offset:1024 sc1\n" \
"v_readlane_b32 s43, v202, 6\n" \
"s_nop 4\n" \
"buffer_load_dwordx4 a[48:51], v201, s[8:11], s43 offen sc1\n" \
"buffer_load_dwordx4 a[52:55], v201, s[8:11], s43 offen offset:1024 sc1\n" \
"v_readlane_b32 s43, v202, 7\n" \
"s_nop 4\n" \
"buffer_load_dwordx4 a[56:59], v201, s[8:11], s43 offen sc1\n" \
"buffer_load_dwordx4 a[60:63], v201, s[8:11], s43 offen offset:1024 sc1\n" \
"s_add_u32 s43, s30, 4\n" \
"s_mov_b32 s50, 0\n" \
".LS2_w16l:\n" \
"buffer_load_dword v240, v204, s[4:7], 0 offen sc1\n" \
"s_waitcnt vmcnt(0)\n" \
".LS2_w16c:\n" \
"v_subrev_u32 v241, s43, v240\n" \
"v_cmp_gt_u32 vcc, 0x100, v241\n" \
"s_cmp_eq_u64 vcc, -1\n" \
"s_cbranch_scc1 .LS2_w16d\n" \
"s_sleep 2\n" \
"s_add_u32 s50, s50, 1\n" \
"s_cmp_lt_u32 s50, 0x20000\n" \
"s_cbranch_scc1 .LS2_w16l\n" \
"s_branch .LS2_end\n" \
".LS2_w16d:\n" \
"v_readlane_b32 s43, v202, 18\n" \
"s_nop 4\n" \
"buffer_load_dwordx4 a[144:147], v201, s[8:11], s43 offen sc1\n" \
"buffer_load_dwordx4 a[148:151], v201, s[8:11], s43 offen offset:1024 sc1\n" \
"v_readlane_b32 s43, v202, 21\n" \
"s_nop 4\n" \
"buffer_load_dwordx4 a[168:171], v201, s[8:11], s43 offen sc1\n" \
"buffer_load_dwordx4 a[172:175], v201, s[8:11], s43 offen offset:1024 sc1\n" \
"v_readlane_b32 s43, v202, 24\n" \
"s_nop 4\n" \
"buffer_load_dwordx4 a[192:195], v201, s[8:11], s43 offen sc1\n" \
"buffer_load_dwordx4 a[196:199], v201, s[8:11], s43 offen offset:1024 sc1\n" \
"v_readlane_b32 s43, v202, 27\n" \
"s_nop 4\n" \
"buffer_load_dwordx4 a[216:219], v201, s[8:11], s43 offen sc1\n" \
"buffer_load_dwordx4 a[220:223], v201, s[8:11], s43 offen offset:1024 sc1\n" \
"s_mov_b32 s33, -1\n" \
"s_add_u32 s43, s30, 1\n" \
"s_mov_b32 s50, 0\n" \
".LS2_w17l:\n" \
"buffer_load_dword v240, v205, s[4:7], 0 offen sc1\n" \
"s_waitcnt vmcnt(0)\n" \
".LS2_w17c:\n" \
"v_subrev_u32 v241, s43, v240\n" \
"v_cmp_gt_u32 vcc, 0x100, v241\n" \
"s_cmp_eq_u64 vcc, -1\n" \
"s_cbranch_scc1 .LS2_w17d\n" \
"s_sleep 2\n" \
"s_add_u32 s50, s50, 1\n" \
"s_cmp_lt_u32 s50, 0x20000\n" \
"s_cbranch_scc1 .LS2_w17l\n" \
"s_branch .LS2_end\n" \
".LS2_w17d:\n" \
"s_add_u32 s43, s30, 1\n" \
"s_mov_b32 s50, 0\n" \
".LS2_w18l:\n" \
"buffer_load_dword v240, v206, s[4:7], 0 offen sc1\n" \
"s_waitcnt vmcnt(0)\n" \
".LS2_w18c:\n" \
"v_subrev_u32 v241, s43, v240\n" \
"v_cmp_gt_u32 vcc, 0x100, v241\n" \
"s_cmp_eq_u64 vcc, -1\n" \
"s_cbranch_scc1 .LS2_w18d\n" \
"s_sleep 2\n" \
"s_add_u32 s50, s50, 1\n" \
"s_cmp_lt_u32 s50, 0x20000\n" \
"s_cbranch_scc1 .LS2_w18l\n" \
"s_branch .LS2_end\n" \
".LS2_w18d:\n" \
".LS2_pre:\n" \
"ds_read_b96 v[248:250], v252\n" \
"s_waitcnt lgkmcnt(0)\n" \
"v_readfirstlane_b32 s44, v248\n" \
"v_readfirstlane_b32 s45, v249\n" \
"v_readfirstlane_b32 s46, v250\n" \
"s_mul_i32 s42, s25, s46\n" \
"s_lshr_b32 s42, s42, 12\n" \
".p2align 3\n" \
"s_memtime s[54:55]\n" \
"s_memrealtime s[56:57]\n" \
"s_memtime s[58:59]\n" \
"s_memrealtime s[60:61]\n" \
"s_nop 0\n" \
"s_nop 0\n" \
"s_memtime s[62:63]\n" \
"s_memrealtime s[64:65]\n" \
"s_memtime s[66:67]\n" \
"s_memrealtime s[68:69]\n" \
"s_memtime s[70:71]\n" \
"s_memrealtime s[72:73]\n" \
"s_memtime s[74:75]\n" \
"s_memrealtime s[76:77]\n" \
"s_nop 0\n" \
"s_nop 0\n" \
"s_memtime s[78:79]\n" \
"s_memrealtime s[80:81]\n" \
"s_memtime s[82:83]\n" \
"s_memrealtime s[84:85]\n" \
"s_memtime s[86:87]\n" \
"s_memrealtime s[88:89]\n" \
"s_memtime s[90:91]\n" \
"s_memrealtime s[92:93]\n" \
"s_nop 0\n" \
"s_nop 0\n" \
"s_memtime s[94:95]\n" \
"s_memrealtime s[96:97]\n" \
"s_memtime s[98:99]\n" \
"s_mov_b32 s52, 0\n" \
"s_mov_b32 s53, 4096\n" \
"s_waitcnt lgkmcnt(0)\n" \
"s_cmp_lg_u32 s96, s56\n" \
"s_cselect_b32 s52, s90, s52\n" \
"s_cselect_b32 s53, s98, s53\n" \
"s_cmp_lg_u32 s92, s56\n" \
"s_cselect_b32 s52, s86, s52\n" \
"s_cselect_b32 s53, s94, s53\n" \
"s_cmp_lg_u32 s88, s56\n" \
"s_cselect_b32 s52, s82, s52\n" \
"s_cselect_b32 s53, s90, s53\n" \
"s_cmp_lg_u32 s84, s56\n" \
"s_cselect_b32 s52, s78, s52\n" \
"s_cselect_b32 s53, s86, s53\n" \
"s_cmp_lg_u32 s80, s56\n" \
"s_cselect_b32 s52, s74, s52\n" \
"s_cselect_b32 s53, s82, s53\n" \
"s_cmp_lg_u32 s76, s56\n" \
"s_cselect_b32 s52, s70, s52\n" \
"s_cselect_b32 s53, s78, s53\n" \
"s_cmp_lg_u32 s72, s56\n" \
"s_cselect_b32 s52, s66, s52\n" \
"s_cselect_b32 s53, s74, s53\n" \
"s_cmp_lg_u32 s68, s56\n" \
"s_cselect_b32 s52, s62, s52\n" \
"s_cselect_b32 s53, s70, s53\n" \
"s_cmp_lg_u32 s64, s56\n" \
"s_cselect_b32 s52, s58, s52\n" \
"s_cselect_b32 s53, s66, s53\n" \
"s_cmp_lg_u32 s60, s56\n" \
"s_cselect_b32 s52, s54, s52\n" \
"s_cselect_b32 s53, s62, s53\n" \
"s_sub_u32 s53, s53, s52\n" \
"s_lshr_b32 s55, s53, 1\n" \
"s_add_u32 s52, s52, s55\n" \
"s_add_u32 s55, s56, 4\n" \
"s_lshl_b32 s55, s55, 4\n" \
"s_cmp_le_u32 s53, 40\n" \
"s_cselect_b32 s44, s55, s44\n" \
"s_cselect_b32 s45, s52, s45\n" \
"v_mov_b32 v248, s44\n" \
"v_mov_b32 v249, s45\n" \
"v_mov_b32 v250, s46\n" \
"s_mov_b64 exec, 1\n" \
"ds_write_b96 v252, v[248:250]\n" \
"s_mov_b64 exec, -1\n" \
".p2align 3\n" \
"s_memtime s[54:55]\n" \
"s_memrealtime s[56:57]\n" \
"s_memtime s[58:59]\n" \
"s_memrealtime s[60:61]\n" \
"s_nop 0\n" \
"s_nop 0\n" \
"s_memtime s[62:63]\n" \
"s_memrealtime s[64:65]\n" \
"s_memtime s[66:67]\n" \
"s_memrealtime s[68:69]\n" \
"s_memtime s[70:71]\n" \
"s_memrealtime s[72:73]\n" \
"s_memtime s[74:75]\n" \
"s_memrealtime s[76:77]\n" \
"s_nop 0\n" \
"s_nop 0\n" \
"s_memtime s[78:79]\n" \
"s_memrealtime s[80:81]\n" \
"s_memtime s[82:83]\n" \
"s_memrealtime s[84:85]\n" \
"s_memtime s[86:87]\n" \
"s_memrealtime s[88:89]\n" \
"s_memtime s[90:91]\n" \
"s_memrealtime s[92:93]\n" \
"s_nop 0\n" \
"s_nop 0\n" \
"s_memtime s[94:95]\n" \
"s_memrealtime s[96:97]\n" \
"s_memtime s[98:99]\n" \
"s_mov_b32 s52, 0\n" \
"s_mov_b32 s53, 4096\n" \
"s_waitcnt lgkmcnt(0)\n" \
"s_cmp_lg_u32 s96, s56\n" \
"s_cselect_b32 s52, s90, s52\n" \
"s_cselect_b32 s53, s98, s53\n" \
"s_cmp_lg_u32 s92, s56\n" \
"s_cselect_b32 s52, s86, s52\n" \
"s_cselect_b32 s53, s94, s53\n" \
"s_cmp_lg_u32 s88, s56\n" \
"s_cselect_b32 s52, s82, s52\n" \
"s_cselect_b32 s53, s90, s53\n" \
"s_cmp_lg_u32 s84, s56\n" \
"s_cselect_b32 s52, s78, s52\n" \
"s_cselect_b32 s53, s86, s53\n" \
"s_cmp_lg_u32 s80, s56\n" \
"s_cselect_b32 s52, s74, s52\n" \
"s_cselect_b32 s53, s82, s53\n" \
"s_cmp_lg_u32 s76, s56\n" \
"s_cselect_b32 s52, s70, s52\n" \
"s_cselect_b32 s53, s78, s53\n" \
"s_cmp_lg_u32 s72, s56\n" \
"s_cselect_b32 s52, s66, s52\n" \
"s_cselect_b32 s53, s74, s53\n" \
"s_cmp_lg_u32 s68, s56\n" \
"s_cselect_b32 s52, s62, s52\n" \
"s_cselect_b32 s53, s70, s53\n" \
"s_cmp_lg_u32 s64, s56\n" \
"s_cselect_b32 s52, s58, s52\n" \
"s_cselect_b32 s53, s66, s53\n" \
"s_cmp_lg_u32 s60, s56\n" \
"s_cselect_b32 s52, s54, s52\n" \
"s_cselect_b32 s53, s62, s53\n" \
"s_sub_u32 s53, s53, s52\n" \
"s_lshr_b32 s55, s53, 1\n" \
"s_add_u32 s52, s52, s55\n" \
"s_add_u32 s55, s56, 4\n" \
"s_lshl_b32 s55, s55, 4\n" \
"s_cmp_le_u32 s53, 40\n" \
"s_cselect_b32 s44, s55, s44\n" \
"s_cselect_b32 s45, s52, s45\n" \
"v_mov_b32 v248, s44\n" \
"v_mov_b32 v249, s45\n" \
"v_mov_b32 v250, s46\n" \
"s_mov_b64 exec, 1\n" \
"ds_write_b96 v252, v[248:250]\n" \
"s_mov_b64 exec, -1\n" \
"s_waitcnt lgkmcnt(0)\n" \
"s_barrier\n" \
"ds_read_b96 v[248:250], v252\n" \
"s_waitcnt lgkmcnt(0)\n" \
"v_readfirstlane_b32 s44, v248\n" \
"v_readfirstlane_b32 s45, v249\n" \
"v_readfirstlane_b32 s46, v250\n" \
"s_mul_i32 s42, s25, s46\n" \
"s_lshr_b32 s42, s42, 12\n" \
".LS2_loop:\n" \
"s_add_u32 s43, s40, 0\n" \
"s_and_b32 s43, s43, 3\n" \
"s_bitcmp1_b32 s43, 0\n" \
"s_cselect_b32 s47, s32, s31\n" \
"s_lshl_b32 s43, s43, 19\n" \
"s_add_u32 s47, s47, s43\n" \
"s_sub_u32 s43, s24, s44\n" \
"s_max_i32 s43, s43, 0xfffd0000\n" \
"s_min_i32 s43, s43, 0x4000\n" \
"s_mul_i32 s43, s43, s46\n" \
"s_ashr_i32 s43, s43, 12\n" \
"s_add_u32 s41, s45, s43\n" \
"s_add_u32 s41, s41, 8\n" \
"s_branch .LS2_a1\n" \
".LS2_retry:\n" \
"s_add_u32 s51, s51, 1\n" \
"s_cmp_gt_u32 s51, 0x20000\n" \
"s_cbranch_scc1 .LS2_end\n" \
".LS2_a0:\n" \
"s_add_u32 s41, s41, s42\n" \
"s_add_u32 s24, s24, s25\n" \
"s_xor_b32 s31, s31, s29\n" \
"s_xor_b32 s32, s32, s29\n" \
"s_xor_b32 s47, s47, s29\n" \
"s_xor_b32 s48, s48, s29\n" \
"s_add_u32 s39, s39, 1\n" \
".p2align 6\n" \
".LS2_a1:\n" \
"s_memtime vcc\n" \
"s_waitcnt lgkmcnt(0)\n" \
"s_sub_u32 s43, s41, vcc_lo\n" \
"s_cmp_gt_i32 s43, 128\n" \
"s_cbranch_scc1 .LS2_a1\n" \
"s_cmp_lt_i32 s43, s37\n" \
"s_cbranch_scc1 .LS2_a0\n" \
"s_max_i32 s43, s43, 0\n" \
"s_and_b32 s43, s43, -4\n" \
"s_getpc_b64 vcc\n" \
".LS2_a3:\n" \
"s_sub_u32 s43, .LS2_a4-.LS2_a3, s43\n" \
"s_add_u32 vcc_lo, vcc_lo, s43\n" \
"s_addc_u32 vcc_hi, vcc_hi, 0\n" \
"s_setpc_b64 vcc\n" \
".rept 32\n" \
"s_nop 0\n" \
".endr\n" \
".LS2_a4:\n" \
"buffer_load_dwordx4 v[0:3], v208, s[4:7], s47 offen offset:2048 sc1\n" \
"s_nop 9\n" \
"buffer_load_dwordx4 v[4:7], v208, s[4:7], s47 offen sc1\n" \
"s_nop 9\n" \
"buffer_load_dwordx4 v[8:11], v208, s[4:7], s47 offen offset:1024 sc1\n" \
"s_nop 9\n" \
"buffer_load_dwordx4 v[12:15], v210, s[4:7], s47 offen sc1\n" \
"s_add_u32 s35, s35, 1\n" \
"v_mov_b32 v28, 0\n" \
"v_mov_b32 v29, 0\n" \
"v_mov_b32 v30, 0\n" \
"v_mov_b32 v31, 0\n" \
"v_mov_b32 v32, 0\n" \
"v_mov_b32 v33, 0\n" \
"v_mov_b32 v34, 0\n" \
"v_mov_b32 v35, 0\n" \
"v_mov_b32 v36, 0\n" \
"v_mov_b32 v37, 0\n" \
"v_mov_b32 v38, 0\n" \
"v_mov_b32 v39, 0\n" \
"v_mov_b32 v40, 0\n" \
"v_mov_b32 v41, 0\n" \
"v_mov_b32 v42, 0\n" \
"v_mov_b32 v43, 0\n" \
"v_mov_b32 v44, 0\n" \
"v_mov_b32 v45, 0\n" \
"v_mov_b32 v46, 0\n" \
"v_mov_b32 v47, 0\n" \
"v_mov_b32 v48, 0\n" \
"v_mov_b32 v49, 0\n" \
"v_mov_b32 v50, 0\n" \
"v_mov_b32 v51, 0\n" \
"v_mov_b32 v52, 0\n" \
"v_mov_b32 v53, 0\n" \
"v_mov_b32 v54, 0\n" \
"v_mov_b32 v55, 0\n" \
"v_mov_b32 v56, 0\n" \
"v_mov_b32 v57, 0\n" \
"v_mov_b32 v58, 0\n" \
"v_mov_b32 v59, 0\n" \
"s_waitcnt vmcnt(3)\n" \
"v_smfmac_f32_16x16x64_bf16 v[28:31], v[0:3], a[0:7], v200\n" \
"v_smfmac_f32_16x16x64_bf16 v[32:35], v[0:3], a[8:15], v200\n" \
"v_smfmac_f32_16x16x64_bf16 v[36:39], v[0:3], a[16:23], v200\n" \
"v_smfmac_f32_16x16x64_bf16 v[40:43], v[0:3], a[24:31], v200\n" \
"v_smfmac_f32_16x16x64_bf16 v[44:47], v[0:3], a[32:39], v200\n" \
"v_smfmac_f32_16x16x64_bf16 v[48:51], v[0:3], a[40:47], v200\n" \
"v_smfmac_f32_16x16x64_bf16 v[52:55], v[0:3], a[48:55], v200\n" \
"v_smfmac_f32_16x16x64_bf16 v[56:59], v[0:3], a[56:63], v200\n" \
"s_waitcnt vmcnt(2)\n" \
"v_smfmac_f32_16x16x64_bf16 v[28:31], v[4:7], a[64:71], v200\n" \
"v_smfmac_f32_16x16x64_bf16 v[32:35], v[4:7], a[72:79], v200\n" \
"v_smfmac_f32_16x16x64_bf16 v[36:39], v[4:7], a[80:87], v200\n" \
"s_waitcnt vmcnt(1)\n" \
"v_smfmac_f32_16x16x64_bf16 v[28:31], v[8:11], a[88:95], v200\n" \
"s_nop 2\n" \
"v_smfmac_f32_16x16x64_bf16 v[40:43], v[4:7], a[96:103], v200\n" \
"s_nop 2\n" \
"v_smfmac_f32_16x16x64_bf16 v[32:35], v[8:11], a[104:111], v200\n" \
"s_nop 0\n" \
"v_cvt_pk_bf16_f32 v128, v28, v29\n" \
"v_cvt_pk_bf16_f32 v129, v30, v31\n" \
"v_smfmac_f32_16x16x64_bf16 v[44:47], v[4:7], a[112:119], v200\n" \
"ds_write_b64 v211, v[128:129] offset:256\n" \
"s_nop 1\n" \
"v_smfmac_f32_16x16x64_bf16 v[36:39], v[8:11], a[120:127], v200\n" \
"s_nop 0\n" \
"v_cvt_pk_bf16_f32 v130, v32, v33\n" \
"v_cvt_pk_bf16_f32 v131, v34, v35\n" \
"v_smfmac_f32_16x16x64_bf16 v[44:47], v[8:11], a[128:135], v200\n" \
"ds_write_b64 v211, v[130:131] offset:2432\n" \
"s_nop 1\n" \
"v_smfmac_f32_16x16x64_bf16 v[40:43], v[8:11], a[136:143], v200\n" \
"s_waitcnt vmcnt(0)\n" \
"v_cvt_pk_bf16_f32 v132, v36, v37\n" \
"v_cvt_pk_bf16_f32 v133, v38, v39\n" \
"v_smfmac_f32_16x16x64_bf16 v[44:47], v[12:15], a[144:151], v200\n" \
"ds_write_b64 v211, v[132:133] offset:4608\n" \
"s_nop 1\n" \
"v_smfmac_f32_16x16x64_bf16 v[48:51], v[4:7], a[152:159], v200\n" \
"s_nop 0\n" \
"v_cvt_pk_bf16_f32 v134, v40, v41\n" \
"v_cvt_pk_bf16_f32 v135, v42, v43\n" \
"v_smfmac_f32_16x16x64_bf16 v[48:51], v[8:11], a[160:167], v200\n" \
"ds_write_b64 v211, v[134:135] offset:4992\n" \
"v_cvt_pk_bf16_f32 v136, v44, v45\n" \
"v_cvt_pk_bf16_f32 v137, v46, v47\n" \
"v_smfmac_f32_16x16x64_bf16 v[48:51], v[12:15], a[168:175], v200\n" \
"ds_write_b64 v211, v[136:137] offset:9472\n" \
"s_nop 1\n" \
"v_smfmac_f32_16x16x64_bf16 v[52:55], v[4:7], a[176:183], v200\n" \
"s_nop 2\n" \
"v_smfmac_f32_16x16x64_bf16 v[52:55], v[8:11], a[184:191], v200\n" \
"s_nop 0\n" \
"v_cvt_pk_bf16_f32 v138, v48, v49\n" \
"v_cvt_pk_bf16_f32 v139, v50, v51\n" \
"v_smfmac_f32_16x16x64_bf16 v[52:55], v[12:15], a[192:199], v200\n" \
"ds_write_b64 v211, v[138:139] offset:11648\n" \
"s_waitcnt lgkmcnt(2)\n" \
"s_barrier\n" \
"v_smfmac_f32_16x16x64_bf16 v[56:59], v[4:7], a[200:207], v200\n" \
"s_nop 2\n" \
"v_smfmac_f32_16x16x64_bf16 v[56:59], v[8:11], a[208:215], v200\n" \
"s_nop 2\n" \
"v_smfmac_f32_16x16x64_bf16 v[56:59], v[12:15], a[216:223], v200\n" \
"v_cvt_pk_bf16_f32 v140, v52, v53\n" \
"v_cvt_pk_bf16_f32 v141, v54, v55\n" \
"s_nop 1\n" \
"ds_write_b64 v211, v[140:141] offset:13824\n" \
"s_nop 2\n" \
"v_cvt_pk_bf16_f32 v142, v56, v57\n" \
"v_cvt_pk_bf16_f32 v143, v58, v59\n" \
"ds_write_b64 v211, v[142:143] offset:14208\n" \
"s_waitcnt lgkmcnt(2)\n" \
"s_barrier\n" \
"s_waitcnt lgkmcnt(1)\n" \
"s_barrier\n" \
"s_waitcnt lgkmcnt(0)\n" \
"s_barrier\n" \
"s_nop 3\n" \
"ds_read_b32 v64, v252 offset:20\n" \
"s_waitcnt lgkmcnt(0)\n" \
"v_readfirstlane_b32 s43, v64\n" \
"s_cmp_eq_u32 s43, s35\n" \
"s_cbranch_scc1 .LS2_retry\n" \
"s_nop 15\n" \
"v_max3_u32 v64, v0, v0, v1\n" \
"v_max3_u32 v64, v64, v2, v3\n" \
"v_max3_u32 v64, v64, v4, v5\n" \
"v_max3_u32 v64, v64, v6, v7\n" \
"v_max3_u32 v64, v64, v8, v9\n" \
"v_max3_u32 v64, v64, v10, v11\n" \
"v_max3_u32 v64, v64, v12, v13\n" \
"v_max3_u32 v64, v64, v14, v15\n" \
"v_cmp_eq_u32 vcc, -1, v64\n" \
"s_cbranch_vccnz .LS2_veto\n" \
".LS2_back:\n" \
"s_waitcnt lgkmcnt(0)\n" \
"s_barrier\n" \
"ds_read_b32 v64, v252 offset:16\n" \
"s_waitcnt lgkmcnt(0)\n" \
"v_readfirstlane_b32 s43, v64\n" \
"s_cmp_eq_u32 s43, s35\n" \
"s_cbranch_scc1 .LS2_retry\n" \
".p2align 3\n" \
"s_memtime s[54:55]\n" \
"s_memrealtime s[56:57]\n" \
"s_memtime s[58:59]\n" \
"s_memrealtime s[60:61]\n" \
"s_nop 0\n" \
"s_nop 0\n" \
"s_memtime s[62:63]\n" \
"s_memrealtime s[64:65]\n" \
"s_memtime s[66:67]\n" \
"s_memrealtime s[68:69]\n" \
"s_memtime s[70:71]\n" \
"s_memrealtime s[72:73]\n" \
"s_memtime s[74:75]\n" \
"s_memrealtime s[76:77]\n" \
"s_nop 0\n" \
"s_nop 0\n" \
"s_memtime s[78:79]\n" \
"s_memrealtime s[80:81]\n" \
"s_memtime s[82:83]\n" \
"s_memrealtime s[84:85]\n" \
"s_memtime s[86:87]\n" \
"s_memrealtime s[88:89]\n" \
"s_memtime s[90:91]\n" \
"s_memrealtime s[92:93]\n" \
"s_nop 0\n" \
"s_nop 0\n" \
"s_memtime s[94:95]\n" \
"s_memrealtime s[96:97]\n" \
"s_memtime s[98:99]\n" \
"s_mov_b32 s52, 0\n" \
"s_mov_b32 s53, 4096\n" \
"s_waitcnt lgkmcnt(0)\n" \
"s_cmp_lg_u32 s96, s56\n" \
"s_cselect_b32 s52, s90, s52\n" \
"s_cselect_b32 s53, s98, s53\n" \
"s_cmp_lg_u32 s92, s56\n" \
"s_cselect_b32 s52, s86, s52\n" \
"s_cselect_b32 s53, s94, s53\n" \
"s_cmp_lg_u32 s88, s56\n" \
"s_cselect_b32 s52, s82, s52\n" \
"s_cselect_b32 s53, s90, s53\n" \
"s_cmp_lg_u32 s84, s56\n" \
"s_cselect_b32 s52, s78, s52\n" \
"s_cselect_b32 s53, s86, s53\n" \
"s_cmp_lg_u32 s80, s56\n" \
"s_cselect_b32 s52, s74, s52\n" \
"s_cselect_b32 s53, s82, s53\n" \
"s_cmp_lg_u32 s76, s56\n" \
"s_cselect_b32 s52, s70, s52\n" \
"s_cselect_b32 s53, s78, s53\n" \
"s_cmp_lg_u32 s72, s56\n" \
"s_cselect_b32 s52, s66, s52\n" \
"s_cselect_b32 s53, s74, s53\n" \
"s_cmp_lg_u32 s68, s56\n" \
"s_cselect_b32 s52, s62, s52\n" \
"s_cselect_b32 s53, s70, s53\n" \
"s_cmp_lg_u32 s64, s56\n" \
"s_cselect_b32 s52, s58, s52\n" \
"s_cselect_b32 s53, s66, s53\n" \
"s_cmp_lg_u32 s60, s56\n" \
"s_cselect_b32 s52, s54, s52\n" \
"s_cselect_b32 s53, s62, s53\n" \
"s_sub_u32 s53, s53, s52\n" \
"s_lshr_b32 s55, s53, 1\n" \
"s_add_u32 s52, s52, s55\n" \
"s_add_u32 s55, s56, 4\n" \
"s_lshl_b32 s55, s55, 4\n" \
"s_sub_u32 s59, s55, s44\n" \
"s_cmp_lt_u32 s59, 2600\n" \
"s_cselect_b32 s63, 1, 3\n" \
"s_mul_i32 s59, s59, s46\n" \
"s_ashr_i32 s59, s59, 12\n" \
"s_add_u32 s59, s59, s45\n" \
"s_sub_u32 s59, s52, s59\n" \
"s_max_i32 s59, s59, -96\n" \
"s_min_i32 s59, s59, 96\n" \
"s_ashr_i32 s63, s59, s63\n" \
"s_add_u32 s63, s63, s46\n" \
"s_max_i32 s63, s63, 0x800\n" \
"s_min_i32 s63, s63, 0x2000\n" \
"s_cmp_le_u32 s53, 40\n" \
"s_cselect_b32 s44, s55, s44\n" \
"s_cselect_b32 s45, s52, s45\n" \
"s_cselect_b32 s46, s63, s46\n" \
"v_mov_b32 v248, s44\n" \
"v_mov_b32 v249, s45\n" \
"v_mov_b32 v250, s46\n" \
"s_mov_b64 exec, 1\n" \
"ds_write_b96 v252, v[248:250]\n" \
"s_mov_b64 exec, -1\n" \
"s_waitcnt lgkmcnt(0)\n" \
"s_barrier\n" \
"ds_read_b64 v[246:247], v252 offset:24\n" \
"ds_read_b96 v[248:250], v252\n" \
"s_waitcnt lgkmcnt(0)\n" \
"v_readfirstlane_b32 s44, v248\n" \
"v_readfirstlane_b32 s45, v249\n" \
"v_readfirstlane_b32 s46, v250\n" \
"v_readfirstlane_b32 s26, v246\n" \
"v_readfirstlane_b32 s27, v247\n" \
"s_mul_i32 s42, s25, s46\n" \
"s_lshr_b32 s42, s42, 12\n" \
"s_add_u32 s40, s40, 1\n" \
"s_add_u32 s24, s24, s26\n" \
"s_add_u32 s24, s24, s27\n" \
"s_cmp_eq_u32 s40, 0\n" \
"s_cbranch_scc1 .LS2_wet\n" \
"s_cmp_lt_u32 s40, 63\n" \
"s_cbranch_scc1 .LS2_loop\n" \
".LS2_end:\n" \
"s_endpgm\n" \
".LS2_veto:\n" \
"v_mov_b32 v64, s35\n" \
"ds_write_b32 v252, v64 offset:16\n" \
"s_branch .LS2_back\n" \
".p2align 6\n" \
"fk_role_S3:\n" \
"ds_read_b32 v200, v255 offset:0\n" \
"ds_read_b32 v201, v255 offset:256\n" \
"ds_read_b32 v202, v255 offset:512\n" \
"ds_read_b32 v203, v255 offset:768\n" \
"ds_read_b32 v204, v255 offset:1024\n" \
"ds_read_b32 v205, v255 offset:1280\n" \
"ds_read_b32 v206, v255 offset:1536\n" \
"ds_read_b32 v207, v255 offset:1792\n" \
"ds_read_b32 v208, v255 offset:2048\n" \
"ds_read_b32 v209, v255 offset:2304\n" \
"ds_read_b32 v210, v255 offset:2560\n" \
"ds_read_b32 v211, v255 offset:2816\n" \
"ds_read_b32 v212, v255 offset:3072\n" \
"ds_read_b32 v213, v255 offset:3328\n" \
"ds_read_b32 v214, v255 offset:3584\n" \
"ds_read_b32 v215, v255 offset:3840\n" \
"ds_read_b32 v254, v255 offset:7936\n" \
"s_waitcnt lgkmcnt(0)\n" \
"v_readlane_b32 s4, v254, 0\n" \
"v_readlane_b32 s5, v254, 1\n" \
"v_readlane_b32 s6, v254, 2\n" \
"v_readlane_b32 s7, v254, 3\n" \
"v_readlane_b32 s8, v254, 4\n" \
"v_readlane_b32 s9, v254, 5\n" \
"v_readlane_b32 s10, v254, 6\n" \
"v_readlane_b32 s11, v254, 7\n" \
"v_readlane_b32 s12, v254, 8\n" \
"v_readlane_b32 s13, v254, 9\n" \
"v_readlane_b32 s14, v254, 10\n" \
"v_readlane_b32 s15, v254, 11\n" \
"v_readlane_b32 s16, v254, 12\n" \
"v_readlane_b32 s17, v254, 13\n" \
"v_readlane_b32 s18, v254, 14\n" \
"v_readlane_b32 s19, v254, 15\n" \
"v_readlane_b32 s20, v254, 16\n" \
"v_readlane_b32 s21, v254, 17\n" \
"v_readlane_b32 s22, v254, 18\n" \
"v_readlane_b32 s23, v254, 19\n" \
"v_readlane_b32 s24, v254, 20\n" \
"v_readlane_b32 s25, v254, 21\n" \
"v_readlane_b32 s26, v254, 22\n" \
"v_readlane_b32 s27, v254, 23\n" \
"v_readlane_b32 s28, v254, 24\n" \
"v_readlane_b32 s29, v254, 25\n" \
"v_readlane_b32 s30, v254, 26\n" \
"v_readlane_b32 s31, v254, 27\n" \
"v_readlane_b32 s32, v254, 28\n" \
"v_readlane_b32 s33, v254, 29\n" \
"v_readlane_b32 s34, v254, 30\n" \
"v_readlane_b32 s35, v254, 31\n" \
"v_readlane_b32 s36, v254, 32\n" \
"v_readlane_b32 s37, v254, 33\n" \
"v_readlane_b32 s38, v254, 34\n" \
"v_readlane_b32 s39, v254, 35\n" \
"s_mov_b32 s40, 0\n" \
"s_mov_b32 s51, 0\n" \
"s_nop 0\n" \
"v_mov_b32 v252, s38\n" \
"s_nop 4\n" \
"v_mov_b32 v188, -1\n" \
"s_mov_b32 s28, s33\n" \
"s_mov_b32 s33, -1\n" \
"v_writelane_b32 v251, s6, 0\n" \
"v_writelane_b32 v251, s10, 1\n" \
"v_writelane_b32 v251, s14, 2\n" \
"v_writelane_b32 v251, s18, 3\n" \
"s_mov_b32 s6, 0\n" \
"s_mov_b32 s10, 0\n" \
"s_mov_b32 s14, 0\n" \
"s_mov_b32 s18, 0\n" \
"s_mov_b32 s40, -1\n" \
"s_branch .LS3_pre\n" \
".LS3_wet:\n" \
"s_waitcnt vmcnt(0)\n" \
"v_readlane_b32 s6, v251, 0\n" \
"v_readlane_b32 s10, v251, 1\n" \
"v_readlane_b32 s14, v251, 2\n" \
"v_readlane_b32 s18, v251, 3\n" \
"s_nop 4\n" \
"s_mov_b32 s33, s28\n" \
"s_add_u32 s34, s33, 1\n" \
"s_add_u32 s36, s33, 2\n" \
"s_add_u32 s43, s30, s33\n" \
"s_mov_b32 s50, 0\n" \
".LS3_w19l:\n" \
"buffer_load_dword v240, v204, s[4:7], 0 offen sc1\n" \
"s_waitcnt vmcnt(0)\n" \
".LS3_w19c:\n" \
"v_subrev_u32 v241, s43, v240\n" \
"v_cmp_gt_u32 vcc, 0x100, v241\n" \
"s_cmp_eq_u64 vcc, -1\n" \
"s_cbranch_scc1 .LS3_w19d\n" \
"s_sleep 2\n" \
"s_add_u32 s50, s50, 1\n" \
"s_cmp_lt_u32 s50, 0x20000\n" \
"s_cbranch_scc1 .LS3_w19l\n" \
"s_branch .LS3_end\n" \
".LS3_w19d:\n" \
"v_readlane_b32 s43, v202, 2\n" \
"s_nop 4\n" \
"buffer_load_dwordx4 a[16:19], v201, s[8:11], s43 offen sc1\n" \
"buffer_load_dwordx4 a[20:23], v201, s[8:11], s43 offen offset:1024 sc1\n" \
"v_readlane_b32 s43, v202, 3\n" \
"s_nop 4\n" \
"buffer_load_dwordx4 a[24:27], v201, s[8:11], s43 offen sc1\n" \
"buffer_load_dwordx4 a[28:31], v201, s[8:11], s43 offen offset:1024 sc1\n" \
"v_readlane_b32 s43, v202, 4\n" \
"s_nop 4\n" \
"buffer_load_dwordx4 a[32:35], v201, s[8:11], s43 offen sc1\n" \
"buffer_load_dwordx4 a[36:39], v201, s[8:11], s43 offen offset:1024 sc1\n" \
"v_readlane_b32 s43, v202, 5\n" \
"s_nop 4\n" \
"buffer_load_dwordx4 a[40:43], v201, s[8:11], s43 offen sc1\n" \
"buffer_load_dwordx4 a[44:47], v201, s[8:11], s43 offen offset:1024 sc1\n" \
"s_add_u32 s43, s30, s34\n" \
"s_mov_b32 s50, 0\n" \
".LS3_w20l:\n" \
"buffer_load_dword v240, v204, s[4:7], 0 offen sc1\n" \
"s_waitcnt vmcnt(0)\n" \
".LS3_w20c:\n" \
"v_subrev_u32 v241, s43, v240\n" \
"v_cmp_gt_u32 vcc, 0x100, v241\n" \
"s_cmp_eq_u64 vcc, -1\n" \
"s_cbranch_scc1 .LS3_w20d\n" \
"s_sleep 2\n" \
"s_add_u32 s50, s50, 1\n" \
"s_cmp_lt_u32 s50, 0x20000\n" \
"s_cbranch_scc1 .LS3_w20l\n" \
"s_branch .LS3_end\n" \
".LS3_w20d:\n" \
"v_readlane_b32 s43, v202, 6\n" \
"s_nop 4\n" \
"buffer_load_dwordx4 a[48:51], v201, s[8:11], s43 offen sc1\n" \
"buffer_load_dwordx4 a[52:55], v201, s[8:11], s43 offen offset:1024 sc1\n" \
"v_readlane_b32 s43, v202, 7\n" \
"s_nop 4\n" \
"buffer_load_dwordx4 a[56:59], v201, s[8:11], s43 offen sc1\n" \
"buffer_load_dwordx4 a[60:63], v201, s[8:11], s43 offen offset:1024 sc1\n" \
"v_readlane_b32 s43, v202, 8\n" \
"s_nop 4\n" \
"buffer_load_dwordx4 a[64:67], v201, s[8:11], s43 offen sc1\n" \
"buffer_load_dwordx4 a[68:71], v201, s[8:11], s43 offen offset:1024 sc1\n" \
"v_readlane_b32 s43, v202, 9\n" \
"s_nop 4\n" \
"buffer_load_dwordx4 a[72:75], v201, s[8:11], s43 offen sc1\n" \
"buffer_load_dwordx4 a[76:79], v201, s[8:11], s43 offen offset:1024 sc1\n" \
"s_add_u32 s43, s30, s36\n" \
"s_mov_b32 s50, 0\n" \
".LS3_w21l:\n" \
"buffer_load_dword v240, v204, s[4:7], 0 offen sc1\n" \
"s_waitcnt vmcnt(0)\n" \
".LS3_w21c:\n" \
"v_subrev_u32 v241, s43, v240\n" \
"v_cmp_gt_u32 vcc, 0x100, v241\n" \
"s_cmp_eq_u64 vcc, -1\n" \
"s_cbranch_scc1 .LS3_w21d\n" \
"s_sleep 2\n" \
"s_add_u32 s50, s50, 1\n" \
"s_cmp_lt_u32 s50, 0x20000\n" \
"s_cbranch_scc1 .LS3_w21l\n" \
"s_branch .LS3_end\n" \
".LS3_w21d:\n" \
"v_readlane_b32 s43, v202, 10\n" \
"s_nop 4\n" \
"buffer_load_dwordx4 a[80:83], v201, s[8:11], s43 offen sc1\n" \
"buffer_load_dwordx4 a[84:87], v201, s[8:11], s43 offen offset:1024 sc1\n" \
"v_readlane_b32 s43, v202, 11\n" \
"s_nop 4\n" \
"buffer_load_dwordx4 a[88:91], v201, s[8:11], s43 offen sc1\n" \
"buffer_load_dwordx4 a[92:95], v201, s[8:11], s43 offen offset:1024 sc1\n" \
"v_readlane_b32 s43, v202, 12\n" \
"s_nop 4\n" \
"buffer_load_dwordx4 a[96:99], v201, s[8:11], s43 offen sc1\n" \
"buffer_load_dwordx4 a[100:103], v201, s[8:11], s43 offen offset:1024 sc1\n" \
"v_readlane_b32 s43, v202, 13\n" \
"s_nop 4\n" \
"buffer_load_dwordx4 a[104:107], v201, s[8:11], s43 offen sc1\n" \
"buffer_load_dwordx4 a[108:111], v201, s[8:11], s43 offen offset:1024 sc1\n" \
"s_add_u32 s43, s30, 4\n" \
"s_mov_b32 s50, 0\n" \
".LS3_w22l:\n" \
"buffer_load_dword v240, v204, s[4:7], 0 offen sc1\n" \
"s_waitcnt vmcnt(0)\n" \
".LS3_w22c:\n" \
"v_subrev_u32 v241, s43, v240\n" \
"v_cmp_gt_u32 vcc, 0x100, v241\n" \
"s_cmp_eq_u64 vcc, -1\n" \
"s_cbranch_scc1 .LS3_w22d\n" \
"s_sleep 2\n" \
"s_add_u32 s50, s50, 1\n" \
"s_cmp_lt_u32 s50, 0x20000\n" \
"s_cbranch_scc1 .LS3_w22l\n" \
"s_branch .LS3_end\n" \
".LS3_w22d:\n" \
"v_readlane_b32 s43, v202, 0\n" \
"s_nop 4\n" \
"buffer_load_dwordx4 a[0:3], v201, s[8:11], s43 offen sc1\n" \
"buffer_load_dwordx4 a[4:7], v201, s[8:11], s43 offen offset:1024 sc1\n" \
"v_readlane_b32 s43, v202, 1\n" \
"s_nop 4\n" \
"buffer_load_dwordx4 a[8:11], v201, s[8:11], s43 offen sc1\n" \
"buffer_load_dwordx4 a[12:15], v201, s[8:11], s43 offen offset:1024 sc1\n" \
"s_mov_b32 s33, -1\n" \
"s_add_u32 s43, s30, 1\n" \
"s_mov_b32 s50, 0\n" \
".LS3_w23l:\n" \
"buffer_load_dword v240, v205, s[4:7], 0 offen sc1\n" \
"s_waitcnt vmcnt(0)\n" \
".LS3_w23c:\n" \
"v_subrev_u32 v241, s43, v240\n" \
"v_cmp_gt_u32 vcc, 0x100, v241\n" \
"s_cmp_eq_u64 vcc, -1\n" \
"s_cbranch_scc1 .LS3_w23d\n" \
"s_sleep 2\n" \
"s_add_u32 s50, s50, 1\n" \
"s_cmp_lt_u32 s50, 0x20000\n" \
"s_cbranch_scc1 .LS3_w23l\n" \
"s_branch .LS3_end\n" \
".LS3_w23d:\n" \
"s_add_u32 s43, s30, 1\n" \
"s_mov_b32 s50, 0\n" \
".LS3_w24l:\n" \
"buffer_load_dword v240, v206, s[4:7], 0 offen sc1\n" \
"s_waitcnt vmcnt(0)\n" \
".LS3_w24c:\n" \
"v_subrev_u32 v241, s43, v240\n" \
"v_cmp_gt_u32 vcc, 0x100, v241\n" \
"s_cmp_eq_u64 vcc, -1\n" \
"s_cbranch_scc1 .LS3_w24d\n" \
"s_sleep 2\n" \
"s_add_u32 s50, s50, 1\n" \
"s_cmp_lt_u32 s50, 0x20000\n" \
"s_cbranch_scc1 .LS3_w24l\n" \
"s_branch .LS3_end\n" \
".LS3_w24d:\n" \
".LS3_pre:\n" \
"s_barrier\n" \
"ds_read_b96 v[248:250], v252\n" \
"s_waitcnt lgkmcnt(0)\n" \
"v_readfirstlane_b32 s44, v248\n" \
"v_readfirstlane_b32 s45, v249\n" \
"v_readfirstlane_b32 s46, v250\n" \
"s_mul_i32 s42, s25, s46\n" \
"s_lshr_b32 s42, s42, 12\n" \
".LS3_loop:\n" \
"s_add_u32 s43, s40, 0\n" \
"s_and_b32 s43, s43, 3\n" \
"s_bitcmp1_b32 s43, 0\n" \
"s_cselect_b32 s47, s32, s31\n" \
"s_lshl_b32 s43, s43, 19\n" \
"s_add_u32 s47, s47, s43\n" \
"s_add_u32 s43, s40, 2\n" \
"s_and_b32 s43, s43, 3\n" \
"s_bitcmp1_b32 s43, 0\n" \
"s_cselect_b32 s49, s32, s31\n" \
"s_lshl_b32 s43, s43, 19\n" \
"s_add_u32 s49, s49, s43\n" \
"s_sub_u32 s43, s24, s44\n" \
"s_max_i32 s43, s43, 0xfffd0000\n" \
"s_min_i32 s43, s43, 0x4000\n" \
"s_mul_i32 s43, s43, s46\n" \
"s_ashr_i32 s43, s43, 12\n" \
"s_add_u32 s41, s45, s43\n" \
"s_add_u32 s41, s41, 12\n" \
"s_branch .LS3_a1\n" \
".LS3_retry:\n" \
"s_add_u32 s51, s51, 1\n" \
"s_cmp_gt_u32 s51, 0x20000\n" \
"s_cbranch_scc1 .LS3_end\n" \
".LS3_a0:\n" \
"s_add_u32 s41, s41, s42\n" \
"s_add_u32 s24, s24, s25\n" \
"s_xor_b32 s31, s31, s29\n" \
"s_xor_b32 s32, s32, s29\n" \
"s_xor_b32 s47, s47, s29\n" \
"s_xor_b32 s48, s48, s29\n" \
"s_add_u32 s39, s39, 1\n" \
"buffer_store_dword v188, v213, s[4:7], s47 offen sc1\n" \
"buffer_store_dword v188, v215, s[4:7], s47 offen sc1\n" \
".p2align 6\n" \
".LS3_a1:\n" \
"s_memtime vcc\n" \
"s_waitcnt lgkmcnt(0)\n" \
"s_sub_u32 s43, s41, vcc_lo\n" \
"s_cmp_gt_i32 s43, 128\n" \
"s_cbranch_scc1 .LS3_a1\n" \
"s_cmp_lt_i32 s43, s37\n" \
"s_cbranch_scc1 .LS3_a0\n" \
"s_max_i32 s43, s43, 0\n" \
"s_and_b32 s43, s43, -4\n" \
"s_getpc_b64 vcc\n" \
".LS3_a3:\n" \
"s_sub_u32 s43, .LS3_a4-.LS3_a3, s43\n" \
"s_add_u32 vcc_lo, vcc_lo, s43\n" \
"s_addc_u32 vcc_hi, vcc_hi, 0\n" \
"s_setpc_b64 vcc\n" \
".rept 32\n" \
"s_nop 0\n" \
".endr\n" \
".LS3_a4:\n" \
"buffer_load_dwordx4 v[12:15], v210, s[4:7], s47 offen sc1\n" \
"s_nop 9\n" \
"buffer_load_dwordx4 v[0:3], v208, s[4:7], s47 offen sc1\n" \
"s_nop 9\n" \
"buffer_load_dwordx4 v[4:7], v208, s[4:7], s47 offen offset:1024 sc1\n" \
"s_nop 9\n" \
"buffer_load_dwordx4 v[8:11], v208, s[4:7], s47 offen offset:2048 sc1\n" \
"s_nop 9\n" \
"buffer_load_dwordx4 v[16:19], v209, s[4:7], s47 offen sc1\n" \
"s_nop 3\n" \
"buffer_load_dwordx4 v[20:23], v209, s[4:7], s47 offen offset:1024 sc1\n" \
"s_nop 3\n" \
"buffer_load_dwordx4 v[24:27], v209, s[4:7], s47 offen offset:2048 sc1\n" \
"s_lshl_b32 s43, s40, 7\n" \
"buffer_load_dwordx4 v[236:239], v207, s[4:7], s43 offen sc1\n" \
"s_add_u32 s35, s35, 1\n" \
"v_mov_b32 v28, 0\n" \
"v_mov_b32 v29, 0\n" \
"v_mov_b32 v30, 0\n" \
"v_mov_b32 v31, 0\n" \
"v_mov_b32 v32, 0\n" \
"v_mov_b32 v33, 0\n" \
"v_mov_b32 v34, 0\n" \
"v_mov_b32 v35, 0\n" \
"v_mov_b32 v36, 0\n" \
"v_mov_b32 v37, 0\n" \
"v_mov_b32 v38, 0\n" \
"v_mov_b32 v39, 0\n" \
"v_mov_b32 v40, 0\n" \
"v_mov_b32 v41, 0\n" \
"v_mov_b32 v42, 0\n" \
"v_mov_b32 v43, 0\n" \
"v_mov_b32 v68, 0\n" \
"v_mov_b32 v69, 0\n" \
"v_mov_b32 v70, 0\n" \
"v_mov_b32 v71, 0\n" \
"s_waitcnt vmcnt(7)\n" \
"v_smfmac_f32_16x16x64_bf16 v[36:39], v[12:15], a[0:7], v200\n" \
"v_smfmac_f32_16x16x64_bf16 v[40:43], v[12:15], a[8:15], v200\n" \
"v_max3_u32 v64, v12, v12, v13\n" \
"v_max3_u32 v64, v64, v14, v15\n" \
"s_waitcnt vmcnt(6)\n" \
"v_smfmac_f32_16x16x64_bf16 v[28:31], v[0:3], a[16:23], v200\n" \
"v_smfmac_f32_16x16x64_bf16 v[32:35], v[0:3], a[24:31], v200\n" \
"v_smfmac_f32_16x16x64_bf16 v[36:39], v[0:3], a[32:39], v200\n" \
"v_smfmac_f32_16x16x64_bf16 v[40:43], v[0:3], a[40:47], v200\n" \
"v_max3_u32 v64, v64, v0, v1\n" \
"v_max3_u32 v64, v64, v2, v3\n" \
"s_waitcnt vmcnt(5)\n" \
"v_smfmac_f32_16x16x64_bf16 v[28:31], v[4:7], a[48:55], v200\n" \
"v_smfmac_f32_16x16x64_bf16 v[32:35], v[4:7], a[56:63], v200\n" \
"v_smfmac_f32_16x16x64_bf16 v[36:39], v[4:7], a[64:71], v200\n" \
"v_smfmac_f32_16x16x64_bf16 v[40:43], v[4:7], a[72:79], v200\n" \
"v_max3_u32 v64, v64, v4, v5\n" \
"v_max3_u32 v64, v64, v6, v7\n" \
"s_waitcnt vmcnt(4)\n" \
"v_smfmac_f32_16x16x64_bf16 v[28:31], v[8:11], a[80:87], v200\n" \
"v_smfmac_f32_16x16x64_bf16 v[32:35], v[8:11], a[88:95], v200\n" \
"v_smfmac_f32_16x16x64_bf16 v[36:39], v[8:11], a[96:103], v200\n" \
"v_smfmac_f32_16x16x64_bf16 v[40:43], v[8:11], a[104:111], v200\n" \
"v_max3_u32 v64, v64, v8, v9\n" \
"v_max3_u32 v64, v64, v10, v11\n" \
"s_nop 2\n" \
"v_cvt_pk_bf16_f32 v128, v28, v29\n" \
"v_cvt_pk_bf16_f32 v129, v30, v31\n" \
"v_cvt_pk_bf16_f32 v130, v32, v33\n" \
"v_cvt_pk_bf16_f32 v131, v34, v35\n" \
"ds_write_b64 v211, v[128:129] offset:0\n" \
"v_cvt_pk_bf16_f32 v132, v36, v37\n" \
"v_cvt_pk_bf16_f32 v133, v38, v39\n" \
"ds_write_b64 v211, v[130:131] offset:128\n" \
"v_cvt_pk_bf16_f32 v134, v40, v41\n" \
"v_cvt_pk_bf16_f32 v135, v42, v43\n" \
"ds_write_b64 v214, v[132:133] offset:0\n" \
"ds_write_b64 v214, v[134:135] offset:128\n" \
"ds_read_b64_tr_b16 v[96:97], v212 offset:6912\n" \
"ds_read_b64_tr_b16 v[98:99], v212 offset:6928\n" \
"ds_read_b64_tr_b16 v[100:101], v212 offset:7168\n" \
"ds_read_b64_tr_b16 v[102:103], v212 offset:7184\n" \
"s_waitcnt vmcnt(1)\n" \
"v_max3_u32 v64, v64, v16, v17\n" \
"v_max3_u32 v64, v64, v18, v19\n" \
"v_max3_u32 v64, v64, v20, v21\n" \
"v_max3_u32 v64, v64, v22, v23\n" \
"v_max3_u32 v64, v64, v24, v25\n" \
"v_max3_u32 v64, v64, v26, v27\n" \
"v_cmp_eq_u32 vcc, -1, v64\n" \
"s_barrier\n" \
"ds_read_b64_tr_b16 v[72:73], v212 offset:0\n" \
"ds_read_b64_tr_b16 v[74:75], v212 offset:16\n" \
"ds_read_b64_tr_b16 v[76:77], v212 offset:256\n" \
"ds_read_b64_tr_b16 v[78:79], v212 offset:272\n" \
"ds_read_b64_tr_b16 v[80:81], v212 offset:2304\n" \
"ds_read_b64_tr_b16 v[82:83], v212 offset:2320\n" \
"ds_read_b64_tr_b16 v[84:85], v212 offset:2560\n" \
"ds_read_b64_tr_b16 v[86:87], v212 offset:2576\n" \
"s_cbranch_vccnz .LS3_veto\n" \
"s_waitcnt lgkmcnt(8)\n" \
"v_smfmac_f32_16x16x64_bf16 v[68:71], v[12:15], v[96:103], v200\n" \
"ds_read_b64_tr_b16 v[88:89], v212 offset:4608\n" \
"ds_read_b64_tr_b16 v[90:91], v212 offset:4624\n" \
"ds_read_b64_tr_b16 v[92:93], v212 offset:4864\n" \
"ds_read_b64_tr_b16 v[94:95], v212 offset:4880\n" \
"s_waitcnt lgkmcnt(8)\n" \
"v_smfmac_f32_16x16x64_bf16 v[68:71], v[0:3], v[72:79], v200\n" \
"s_waitcnt lgkmcnt(4)\n" \
"v_smfmac_f32_16x16x64_bf16 v[68:71], v[4:7], v[80:87], v200\n" \
"s_waitcnt lgkmcnt(0)\n" \
"v_smfmac_f32_16x16x64_bf16 v[68:71], v[8:11], v[88:95], v200\n" \
"s_barrier\n" \
"ds_read_b64_tr_b16 v[104:105], v212 offset:9216\n" \
"ds_read_b64_tr_b16 v[106:107], v212 offset:9232\n" \
"ds_read_b64_tr_b16 v[108:109], v212 offset:9472\n" \
"ds_read_b64_tr_b16 v[110:111], v212 offset:9488\n" \
"s_nop 2\n" \
"v_cmp_u_f32 vcc, v68, v68\n" \
"s_barrier\n" \
"ds_read_b64_tr_b16 v[112:113], v212 offset:11520\n" \
"ds_read_b64_tr_b16 v[114:115], v212 offset:11536\n" \
"ds_read_b64_tr_b16 v[116:117], v212 offset:11776\n" \
"ds_read_b64_tr_b16 v[118:119], v212 offset:11792\n" \
"s_cbranch_vccnz .LS3_veto2\n" \
".LS3_veto2r:\n" \
"s_barrier\n" \
"ds_read_b64_tr_b16 v[120:121], v212 offset:13824\n" \
"ds_read_b64_tr_b16 v[122:123], v212 offset:13840\n" \
"ds_read_b64_tr_b16 v[124:125], v212 offset:14080\n" \
"ds_read_b64_tr_b16 v[126:127], v212 offset:14096\n" \
"s_waitcnt lgkmcnt(8)\n" \
"v_smfmac_f32_16x16x64_bf16 v[68:71], v[16:19], v[104:111], v200\n" \
"s_waitcnt lgkmcnt(4)\n" \
"v_smfmac_f32_16x16x64_bf16 v[68:71], v[20:23], v[112:119], v200\n" \
"s_waitcnt lgkmcnt(0)\n" \
"v_smfmac_f32_16x16x64_bf16 v[68:71], v[24:27], v[120:127], v200\n" \
"s_nop 7\n" \
"v_add_f32_dpp v128, v68, v68 row_shl:4 row_mask:0xf bank_mask:0x5\n" \
"v_add_f32_dpp v129, v70, v70 row_shl:4 row_mask:0xf bank_mask:0x5\n" \
"v_add_f32_dpp v128, v69, v69 row_shr:4 row_mask:0xf bank_mask:0xa\n" \
"v_add_f32_dpp v129, v71, v71 row_shr:4 row_mask:0xf bank_mask:0xa\n" \
"s_nop 1\n" \
"v_permlane32_swap_b32 v128, v129\n" \
"v_add_f32 v128, v128, v129\n" \
"buffer_store_dword v128, v213, s[4:7], s47 offen sc1\n" \
"buffer_store_dword v128, v215, s[4:7], s47 offen sc1\n" \
"s_xor_b32 s43, s47, s29\n" \
"buffer_store_dword v128, v213, s[4:7], s43 offen sc1\n" \
"buffer_store_dword v128, v215, s[4:7], s43 offen sc1\n" \
"s_waitcnt lgkmcnt(0)\n" \
"s_barrier\n" \
"ds_read_b32 v64, v252 offset:16\n" \
"s_waitcnt lgkmcnt(0)\n" \
"v_readfirstlane_b32 s43, v64\n" \
"s_cmp_eq_u32 s43, s35\n" \
"s_cbranch_scc1 .LS3_retry\n" \
"v_cmp_u_f32 vcc, v128, v128\n" \
"s_cbranch_vccnz .LS3_nan\n" \
".LS3_nanr:\n" \
"s_waitcnt vmcnt(4)\n" \
"v_max_u32 v236, v236, v238\n" \
"v_max_u32 v237, v237, v239\n" \
"v_and_b32 v236, 0xfff, v236\n" \
"v_and_b32 v237, 0xfff, v237\n" \
"s_mov_b64 exec, 1\n" \
"ds_write_b64 v252, v[236:237] offset:24\n" \
"s_mov_b64 exec, -1\n" \
"buffer_store_dword v188, v213, s[4:7], s49 offen sc1\n" \
"buffer_store_dword v188, v215, s[4:7], s49 offen sc1\n" \
"s_xor_b32 s43, s49, s29\n" \
"buffer_store_dword v188, v213, s[4:7], s43 offen sc1\n" \
"buffer_store_dword v188, v215, s[4:7], s43 offen sc1\n" \
"s_waitcnt lgkmcnt(0)\n" \
"s_barrier\n" \
"ds_read_b64 v[246:247], v252 offset:24\n" \
"ds_read_b96 v[248:250], v252\n" \
"s_waitcnt lgkmcnt(0)\n" \
"v_readfirstlane_b32 s44, v248\n" \
"v_readfirstlane_b32 s45, v249\n" \
"v_readfirstlane_b32 s46, v250\n" \
"v_readfirstlane_b32 s26, v246\n" \
"v_readfirstlane_b32 s27, v247\n" \
"s_mul_i32 s42, s25, s46\n" \
"s_lshr_b32 s42, s42, 12\n" \
"s_add_u32 s40, s40, 1\n" \
"s_add_u32 s24, s24, s26\n" \
"s_add_u32 s24, s24, s27\n" \
"s_cmp_eq_u32 s40, 0\n" \
"s_cbranch_scc1 .LS3_wet\n" \
"s_cmp_lt_u32 s40, 63\n" \
"s_cbranch_scc1 .LS3_loop\n" \
".LS3_end:\n" \
"s_endpgm\n" \
".LS3_nan:\n" \
"v_cmp_u_f32 vcc, v128, v128\n" \
"v_mov_b32 v129, 0x7fc00001\n" \
"s_nop 1\n" \
"v_cndmask_b32 v128, v128, v129, vcc\n" \
"buffer_store_dword v128, v213, s[4:7], s47 offen sc1\n" \
"buffer_store_dword v128, v215, s[4:7], s47 offen sc1\n" \
"s_xor_b32 s43, s47, s29\n" \
"buffer_store_dword v128, v213, s[4:7], s43 offen sc1\n" \
"buffer_store_dword v128, v215, s[4:7], s43 offen sc1\n" \
"s_branch .LS3_nanr\n" \
".LS3_veto:\n" \
"v_mov_b32 v64, s35\n" \
"ds_write_b32 v252, v64 offset:20\n" \
"s_barrier\n" \
"s_barrier\n" \
"s_waitcnt lgkmcnt(0)\n" \
"s_barrier\n" \
"s_branch .LS3_retry\n" \
".LS3_veto2:\n" \
"s_cmp_eq_u32 s33, s40\n" \
"s_cbranch_scc1 .LS3_veto2r\n" \
"s_mov_b32 s33, s40\n" \
"v_mov_b32 v64, s35\n" \
"ds_write_b32 v252, v64 offset:20\n" \
"s_waitcnt lgkmcnt(0)\n" \
"s_barrier\n" \
"s_branch .LS3_retry\n" \
".p2align 6\n" \
"fk_role_P0:\n" \
"ds_read_b32 v200, v255 offset:0\n" \
"ds_read_b32 v201, v255 offset:256\n" \
"ds_read_b32 v202, v255 offset:512\n" \
"ds_read_b32 v203, v255 offset:768\n" \
"ds_read_b32 v204, v255 offset:1024\n" \
"ds_read_b32 v205, v255 offset:1280\n" \
"ds_read_b32 v206, v255 offset:1536\n" \
"ds_read_b32 v207, v255 offset:1792\n" \
"ds_read_b32 v208, v255 offset:2048\n" \
"ds_read_b32 v209, v255 offset:2304\n" \
"ds_read_b32 v210, v255 offset:2560\n" \
"ds_read_b32 v211, v255 offset:2816\n" \
"ds_read_b32 v212, v255 offset:3072\n" \
"ds_read_b32 v213, v255 offset:3328\n" \
"ds_read_b32 v214, v255 offset:3584\n" \
"ds_read_b32 v215, v255 offset:3840\n" \
"ds_read_b32 v216, v255 offset:4096\n" \
"ds_read_b32 v217, v255 offset:4352\n" \
"ds_read_b32 v218, v255 offset:4608\n" \
"ds_read_b32 v219, v255 offset:4864\n" \
"ds_read_b32 v220, v255 offset:5120\n" \
"ds_read_b32 v221, v255 offset:5376\n" \
"ds_read_b32 v222, v255 offset:5632\n" \
"ds_read_b32 v223, v255 offset:5888\n" \
"ds_read_b32 v254, v255 offset:7936\n" \
"s_waitcnt lgkmcnt(0)\n" \
"v_readlane_b32 s4, v254, 0\n" \
"v_readlane_b32 s5, v254, 1\n" \
"v_readlane_b32 s6, v254, 2\n" \
"v_readlane_b32 s7, v254, 3\n" \
"v_readlane_b32 s8, v254, 4\n" \
"v_readlane_b32 s9, v254, 5\n" \
"v_readlane_b32 s10, v254, 6\n" \
"v_readlane_b32 s11, v254, 7\n" \
"v_readlane_b32 s12, v254, 8\n" \
"v_readlane_b32 s13, v254, 9\n" \
"v_readlane_b32 s14, v254, 10\n" \
"v_readlane_b32 s15, v254, 11\n" \
"v_readlane_b32 s16, v254, 12\n" \
"v_readlane_b32 s17, v254, 13\n" \
"v_readlane_b32 s18, v254, 14\n" \
"v_readlane_b32 s19, v254, 15\n" \
"v_readlane_b32 s20, v254, 16\n" \
"v_readlane_b32 s21, v254, 17\n" \
"v_readlane_b32 s22, v254, 18\n" \
"v_readlane_b32 s23, v254, 19\n" \
"v_readlane_b32 s24, v254, 20\n" \
"v_readlane_b32 s25, v254, 21\n" \
"v_readlane_b32 s26, v254, 22\n" \
"v_readlane_b32 s27, v254, 23\n" \
"v_readlane_b32 s28, v254, 24\n" \
"v_readlane_b32 s29, v254, 25\n" \
"v_readlane_b32 s30, v254, 26\n" \
"v_readlane_b32 s31, v254, 27\n" \
"v_readlane_b32 s32, v254, 28\n" \
"v_readlane_b32 s33, v254, 29\n" \
"v_readlane_b32 s34, v254, 30\n" \
"v_readlane_b32 s35, v254, 31\n" \
"v_readlane_b32 s36, v254, 32\n" \
"v_readlane_b32 s37, v254, 33\n" \
"v_readlane_b32 s38, v254, 34\n" \
"v_readlane_b32 s39, v254, 35\n" \
"s_mov_b32 s40, 0\n" \
"s_mov_b32 s51, 0\n" \
"s_nop 0\n" \
"v_mov_b32 v252, s38\n" \
"s_nop 4\n" \
"v_mov_b32 v112, -1\n" \
"v_mov_b32 v113, -1\n" \
"v_mov_b32 v114, -1\n" \
"v_mov_b32 v115, -1\n" \
"v_mov_b32 v109, 0x7fc00001\n" \
"v_writelane_b32 v251, s6, 0\n" \
"v_writelane_b32 v251, s10, 1\n" \
"v_writelane_b32 v251, s14, 2\n" \
"v_writelane_b32 v251, s18, 3\n" \
"s_mov_b32 s6, 0\n" \
"s_mov_b32 s10, 0\n" \
"s_mov_b32 s14, 0\n" \
"s_mov_b32 s18, 0\n" \
"s_mov_b32 s40, -1\n" \
"s_branch .LP0_pre\n" \
".LP0_wet:\n" \
"s_waitcnt vmcnt(0)\n" \
"v_readlane_b32 s6, v251, 0\n" \
"v_readlane_b32 s10, v251, 1\n" \
"v_readlane_b32 s14, v251, 2\n" \
"v_readlane_b32 s18, v251, 3\n" \
"s_nop 4\n" \
"s_mov_b32 s33, 0\n" \
"s_mov_b32 s34, 0\n" \
"s_add_u32 s43, s30, 1\n" \
"s_mov_b32 s50, 0\n" \
".LP0_w25l:\n" \
"buffer_load_dword v240, v204, s[4:7], 0 offen sc1\n" \
"s_waitcnt vmcnt(0)\n" \
".LP0_w25c:\n" \
"v_subrev_u32 v241, s43, v240\n" \
"v_cmp_gt_u32 vcc, 0x100, v241\n" \
"s_cmp_eq_u64 vcc, -1\n" \
"s_cbranch_scc1 .LP0_w25d\n" \
"s_sleep 2\n" \
"s_add_u32 s50, s50, 1\n" \
"s_cmp_lt_u32 s50, 0x20000\n" \
"s_cbranch_scc1 .LP0_w25l\n" \
"s_branch .LP0_end\n" \
".LP0_w25d:\n" \
"v_readlane_b32 s43, v202, 0\n" \
"s_nop 4\n" \
"buffer_load_dwordx4 a[0:3], v201, s[8:11], s43 offen sc1\n" \
"buffer_load_dwordx4 a[4:7], v201, s[8:11], s43 offen offset:1024 sc1\n" \
"v_readlane_b32 s43, v202, 7\n" \
"s_nop 4\n" \
"buffer_load_dwordx4 a[56:59], v201, s[8:11], s43 offen sc1\n" \
"buffer_load_dwordx4 a[60:63], v201, s[8:11], s43 offen offset:1024 sc1\n" \
"v_readlane_b32 s43, v202, 14\n" \
"s_nop 4\n" \
"buffer_load_dwordx4 a[112:115], v201, s[8:11], s43 offen sc1\n" \
"buffer_load_dwordx4 a[116:119], v201, s[8:11], s43 offen offset:1024 sc1\n" \
"v_readlane_b32 s43, v202, 21\n" \
"s_nop 4\n" \
"buffer_load_dwordx4 a[168:171], v201, s[8:11], s43 offen sc1\n" \
"buffer_load_dwordx4 a[172:175], v201, s[8:11], s43 offen offset:1024 sc1\n" \
"s_add_u32 s43, s30, 2\n" \
"s_mov_b32 s50, 0\n" \
".LP0_w26l:\n" \
"buffer_load_dword v240, v204, s[4:7], 0 offen sc1\n" \
"s_waitcnt vmcnt(0)\n" \
".LP0_w26c:\n" \
"v_subrev_u32 v241, s43, v240\n" \
"v_cmp_gt_u32 vcc, 0x100, v241\n" \
"s_cmp_eq_u64 vcc, -1\n" \
"s_cbranch_scc1 .LP0_w26d\n" \
"s_sleep 2\n" \
"s_add_u32 s50, s50, 1\n" \
"s_cmp_lt_u32 s50, 0x20000\n" \
"s_cbranch_scc1 .LP0_w26l\n" \
"s_branch .LP0_end\n" \
".LP0_w26d:\n" \
"v_readlane_b32 s43, v202, 1\n" \
"s_nop 4\n" \
"buffer_load_dwordx4 a[8:11], v201, s[8:11], s43 offen sc1\n" \
"buffer_load_dwordx4 a[12:15], v201, s[8:11], s43 offen offset:1024 sc1\n" \
"v_readlane_b32 s43, v202, 8\n" \
"s_nop 4\n" \
"buffer_load_dwordx4 a[64:67], v201, s[8:11], s43 offen sc1\n" \
"buffer_load_dwordx4 a[68:71], v201, s[8:11], s43 offen offset:1024 sc1\n" \
"v_readlane_b32 s43, v202, 15\n" \
"s_nop 4\n" \
"buffer_load_dwordx4 a[120:123], v201, s[8:11], s43 offen sc1\n" \
"buffer_load_dwordx4 a[124:127], v201, s[8:11], s43 offen offset:1024 sc1\n" \
"v_readlane_b32 s43, v202, 22\n" \
"s_nop 4\n" \
"buffer_load_dwordx4 a[176:179], v201, s[8:11], s43 offen sc1\n" \
"buffer_load_dwordx4 a[180:183], v201, s[8:11], s43 offen offset:1024 sc1\n" \
"s_add_u32 s43, s30, 3\n" \
"s_mov_b32 s50, 0\n" \
".LP0_w27l:\n" \
"buffer_load_dword v240, v204, s[4:7], 0 offen sc1\n" \
"s_waitcnt vmcnt(0)\n" \
".LP0_w27c:\n" \
"v_subrev_u32 v241, s43, v240\n" \
"v_cmp_gt_u32 vcc, 0x100, v241\n" \
"s_cmp_eq_u64 vcc, -1\n" \
"s_cbranch_scc1 .LP0_w27d\n" \
"s_sleep 2\n" \
"s_add_u32 s50, s50, 1\n" \
"s_cmp_lt_u32 s50, 0x20000\n" \
"s_cbranch_scc1 .LP0_w27l\n" \
"s_branch .LP0_end\n" \
".LP0_w27d:\n" \
"v_readlane_b32 s43, v202, 2\n" \
"s_nop 4\n" \
"buffer_load_dwordx4 a[16:19], v201, s[8:11], s43 offen sc1\n" \
"buffer_load_dwordx4 a[20:23], v201, s[8:11], s43 offen offset:1024 sc1\n" \
"v_readlane_b32 s43, v202, 9\n" \
"s_nop 4\n" \
"buffer_load_dwordx4 a[72:75], v201, s[8:11], s43 offen sc1\n" \
"buffer_load_dwordx4 a[76:79], v201, s[8:11], s43 offen offset:1024 sc1\n" \
"v_readlane_b32 s43, v202, 16\n" \
"s_nop 4\n" \
"buffer_load_dwordx4 a[128:131], v201, s[8:11], s43 offen sc1\n" \
"buffer_load_dwordx4 a[132:135], v201, s[8:11], s43 offen offset:1024 sc1\n" \
"v_readlane_b32 s43, v202, 23\n" \
"s_nop 4\n" \
"buffer_load_dwordx4 a[184:187], v201, s[8:11], s43 offen sc1\n" \
"buffer_load_dwordx4 a[188:191], v201, s[8:11], s43 offen offset:1024 sc1\n" \
"s_add_u32 s43, s30, 4\n" \
"s_mov_b32 s50, 0\n" \
".LP0_w28l:\n" \
"buffer_load_dword v240, v204, s[4:7], 0 offen sc1\n" \
"s_waitcnt vmcnt(0)\n" \
".LP0_w28c:\n" \
"v_subrev_u32 v241, s43, v240\n" \
"v_cmp_gt_u32 vcc, 0x100, v241\n" \
"s_cmp_eq_u64 vcc, -1\n" \
"s_cbranch_scc1 .LP0_w28d\n" \
"s_sleep 2\n" \
"s_add_u32 s50, s50, 1\n" \
"s_cmp_lt_u32 s50, 0x20000\n" \
"s_cbranch_scc1 .LP0_w28l\n" \
"s_branch .LP0_end\n" \
".LP0_w28d:\n" \
"v_readlane_b32 s43, v202, 3\n" \
"s_nop 4\n" \
"buffer_load_dwordx4 a[24:27], v201, s[8:11], s43 offen sc1\n" \
"buffer_load_dwordx4 a[28:31], v201, s[8:11], s43 offen offset:1024 sc1\n" \
"v_readlane_b32 s43, v202, 10\n" \
"s_nop 4\n" \
"buffer_load_dwordx4 a[80:83], v201, s[8:11], s43 offen sc1\n" \
"buffer_load_dwordx4 a[84:87], v201, s[8:11], s43 offen offset:1024 sc1\n" \
"v_readlane_b32 s43, v202, 17\n" \
"s_nop 4\n" \
"buffer_load_dwordx4 a[136:139], v201, s[8:11], s43 offen sc1\n" \
"buffer_load_dwordx4 a[140:143], v201, s[8:11], s43 offen offset:1024 sc1\n" \
"v_readlane_b32 s43, v202, 24\n" \
"s_nop 4\n" \
"buffer_load_dwordx4 a[192:195], v201, s[8:11], s43 offen sc1\n" \
"buffer_load_dwordx4 a[196:199], v201, s[8:11], s43 offen offset:1024 sc1\n" \
"s_add_u32 s43, s30, 5\n" \
"s_mov_b32 s50, 0\n" \
".LP0_w29l:\n" \
"buffer_load_dword v240, v204, s[4:7], 0 offen sc1\n" \
"s_waitcnt vmcnt(0)\n" \
".LP0_w29c:\n" \
"v_subrev_u32 v241, s43, v240\n" \
"v_cmp_gt_u32 vcc, 0x100, v241\n" \
"s_cmp_eq_u64 vcc, -1\n" \
"s_cbranch_scc1 .LP0_w29d\n" \
"s_sleep 2\n" \
"s_add_u32 s50, s50, 1\n" \
"s_cmp_lt_u32 s50, 0x20000\n" \
"s_cbranch_scc1 .LP0_w29l\n" \
"s_branch .LP0_end\n" \
".LP0_w29d:\n" \
"v_readlane_b32 s43, v202, 4\n" \
"s_nop 4\n" \
"buffer_load_dwordx4 a[32:35], v201, s[8:11], s43 offen sc1\n" \
"buffer_load_dwordx4 a[36:39], v201, s[8:11], s43 offen offset:1024 sc1\n" \
"v_readlane_b32 s43, v202, 11\n" \
"s_nop 4\n" \
"buffer_load_dwordx4 a[88:91], v201, s[8:11], s43 offen sc1\n" \
"buffer_load_dwordx4 a[92:95], v201, s[8:11], s43 offen offset:1024 sc1\n" \
"v_readlane_b32 s43, v202, 18\n" \
"s_nop 4\n" \
"buffer_load_dwordx4 a[144:147], v201, s[8:11], s43 offen sc1\n" \
"buffer_load_dwordx4 a[148:151], v201, s[8:11], s43 offen offset:1024 sc1\n" \
"v_readlane_b32 s43, v202, 25\n" \
"s_nop 4\n" \
"buffer_load_dwordx4 a[200:203], v201, s[8:11], s43 offen sc1\n" \
"buffer_load_dwordx4 a[204:207], v201, s[8:11], s43 offen offset:1024 sc1\n" \
"s_add_u32 s43, s30, 6\n" \
"s_mov_b32 s50, 0\n" \
".LP0_w30l:\n" \
"buffer_load_dword v240, v204, s[4:7], 0 offen sc1\n" \
"s_waitcnt vmcnt(0)\n" \
".LP0_w30c:\n" \
"v_subrev_u32 v241, s43, v240\n" \
"v_cmp_gt_u32 vcc, 0x100, v241\n" \
"s_cmp_eq_u64 vcc, -1\n" \
"s_cbranch_scc1 .LP0_w30d\n" \
"s_sleep 2\n" \
"s_add_u32 s50, s50, 1\n" \
"s_cmp_lt_u32 s50, 0x20000\n" \
"s_cbranch_scc1 .LP0_w30l\n" \
"s_branch .LP0_end\n" \
".LP0_w30d:\n" \
"v_readlane_b32 s43, v202, 5\n" \
"s_nop 4\n" \
"buffer_load_dwordx4 a[40:43], v201, s[8:11], s43 offen sc1\n" \
"buffer_load_dwordx4 a[44:47], v201, s[8:11], s43 offen offset:1024 sc1\n" \
"v_readlane_b32 s43, v202, 12\n" \
"s_nop 4\n" \
"buffer_load_dwordx4 a[96:99], v201, s[8:11], s43 offen sc1\n" \
"buffer_load_dwordx4 a[100:103], v201, s[8:11], s43 offen offset:1024 sc1\n" \
"v_readlane_b32 s43, v202, 19\n" \
"s_nop 4\n" \
"buffer_load_dwordx4 a[152:155], v201, s[8:11], s43 offen sc1\n" \
"buffer_load_dwordx4 a[156:159], v201, s[8:11], s43 offen offset:1024 sc1\n" \
"v_readlane_b32 s43, v202, 26\n" \
"s_nop 4\n" \
"buffer_load_dwordx4 a[208:211], v201, s[8:11], s43 offen sc1\n" \
"buffer_load_dwordx4 a[212:215], v201, s[8:11], s43 offen offset:1024 sc1\n" \
"s_add_u32 s43, s30, 7\n" \
"s_mov_b32 s50, 0\n" \
".LP0_w31l:\n" \
"buffer_load_dword v240, v204, s[4:7], 0 offen sc1\n" \
"s_waitcnt vmcnt(0)\n" \
".LP0_w31c:\n" \
"v_subrev_u32 v241, s43, v240\n" \
"v_cmp_gt_u32 vcc, 0x100, v241\n" \
"s_cmp_eq_u64 vcc, -1\n" \
"s_cbranch_scc1 .LP0_w31d\n" \
"s_sleep 2\n" \
"s_add_u32 s50, s50, 1\n" \
"s_cmp_lt_u32 s50, 0x20000\n" \
"s_cbranch_scc1 .LP0_w31l\n" \
"s_branch .LP0_end\n" \
".LP0_w31d:\n" \
"v_readlane_b32 s43, v202, 6\n" \
"s_nop 4\n" \
"buffer_load_dwordx4 a[48:51], v201, s[8:11], s43 offen sc1\n" \
"buffer_load_dwordx4 a[52:55], v201, s[8:11], s43 offen offset:1024 sc1\n" \
"v_readlane_b32 s43, v202, 13\n" \
"s_nop 4\n" \
"buffer_load_dwordx4 a[104:107], v201, s[8:11], s43 offen sc1\n" \
"buffer_load_dwordx4 a[108:111], v201, s[8:11], s43 offen offset:1024 sc1\n" \
"v_readlane_b32 s43, v202, 20\n" \
"s_nop 4\n" \
"buffer_load_dwordx4 a[160:163], v201, s[8:11], s43 offen sc1\n" \
"buffer_load_dwordx4 a[164:167], v201, s[8:11], s43 offen offset:1024 sc1\n" \
"v_readlane_b32 s43, v202, 27\n" \
"s_nop 4\n" \
"buffer_load_dwordx4 a[216:219], v201, s[8:11], s43 offen sc1\n" \
"buffer_load_dwordx4 a[220:223], v201, s[8:11], s43 offen offset:1024 sc1\n" \
"s_add_u32 s43, s30, 1\n" \
"s_mov_b32 s50, 0\n" \
".LP0_w32l:\n" \
"buffer_load_dword v240, v205, s[4:7], 0 offen sc1\n" \
"s_waitcnt vmcnt(0)\n" \
".LP0_w32c:\n" \
"v_subrev_u32 v241, s43, v240\n" \
"v_cmp_gt_u32 vcc, 0x100, v241\n" \
"s_cmp_eq_u64 vcc, -1\n" \
"s_cbranch_scc1 .LP0_w32d\n" \
"s_sleep 2\n" \
"s_add_u32 s50, s50, 1\n" \
"s_cmp_lt_u32 s50, 0x20000\n" \
"s_cbranch_scc1 .LP0_w32l\n" \
"s_branch .LP0_end\n" \
".LP0_w32d:\n" \
"s_add_u32 s43, s30, 1\n" \
"s_mov_b32 s50, 0\n" \
".LP0_w33l:\n" \
"buffer_load_dword v240, v206, s[4:7], 0 offen sc1\n" \
"s_waitcnt vmcnt(0)\n" \
".LP0_w33c:\n" \
"v_subrev_u32 v241, s43, v240\n" \
"v_cmp_gt_u32 vcc, 0x100, v241\n" \
"s_cmp_eq_u64 vcc, -1\n" \
"s_cbranch_scc1 .LP0_w33d\n" \
"s_sleep 2\n" \
"s_add_u32 s50, s50, 1\n" \
"s_cmp_lt_u32 s50, 0x20000\n" \
"s_cbranch_scc1 .LP0_w33l\n" \
"s_branch .LP0_end\n" \
".LP0_w33d:\n" \
".LP0_pre:\n" \
"s_add_u32 s43, s40, 0\n" \
"s_and_b32 s43, s43, 3\n" \
"s_bitcmp1_b32 s43, 0\n" \
"s_cselect_b32 s47, s32, s31\n" \
"s_lshl_b32 s43, s43, 19\n" \
"s_add_u32 s47, s47, s43\n" \
"buffer_store_dword v219, v213, s[4:7], s47 offen sc1\n" \
"s_xor_b32 s43, s47, s29\n" \
"buffer_store_dword v219, v213, s[4:7], s43 offen sc1\n" \
"s_barrier\n" \
"ds_read_b96 v[248:250], v252\n" \
"s_waitcnt lgkmcnt(0)\n" \
"v_readfirstlane_b32 s44, v248\n" \
"v_readfirstlane_b32 s45, v249\n" \
"v_readfirstlane_b32 s46, v250\n" \
"s_mul_i32 s42, s25, s46\n" \
"s_lshr_b32 s42, s42, 12\n" \
"s_add_u32 s43, s40, 1\n" \
"s_and_b32 s43, s43, 3\n" \
"s_bitcmp1_b32 s43, 0\n" \
"s_cselect_b32 s48, s32, s31\n" \
"s_lshl_b32 s43, s43, 19\n" \
"s_add_u32 s48, s48, s43\n" \
"s_add_u32 s43, s40, 2\n" \
"s_and_b32 s43, s43, 3\n" \
"s_bitcmp1_b32 s43, 0\n" \
"s_cselect_b32 s49, s32, s31\n" \
"s_lshl_b32 s43, s43, 19\n" \
"s_add_u32 s49, s49, s43\n" \
"s_sub_u32 s43, s24, s44\n" \
"s_add_u32 s43, s43, s28\n" \
"s_max_i32 s43, s43, 0xfffd0000\n" \
"s_min_i32 s43, s43, 0x4000\n" \
"s_mul_i32 s43, s43, s46\n" \
"s_ashr_i32 s43, s43, 12\n" \
"s_add_u32 s41, s45, s43\n" \
"s_lshr_b32 s35, s42, 1\n" \
"s_sub_u32 s35, 0, s35\n" \
".LP0_loop:\n" \
"s_branch .LP0_m1\n" \
".LP0_retry1:\n" \
"s_add_u32 s51, s51, 1\n" \
"s_cmp_gt_u32 s51, 0x20000\n" \
"s_cbranch_scc1 .LP0_end\n" \
".LP0_m0:\n" \
"s_add_u32 s41, s41, s42\n" \
"s_add_u32 s24, s24, s25\n" \
"s_xor_b32 s31, s31, s29\n" \
"s_xor_b32 s32, s32, s29\n" \
"s_xor_b32 s47, s47, s29\n" \
"s_xor_b32 s48, s48, s29\n" \
"s_add_u32 s39, s39, 1\n" \
"s_bitcmp1_b32 s31, 21\n" \
"s_cselect_b32 s43, s29, 0\n" \
"v_mov_b32 v116, s43\n" \
"ds_write_b32 v252, v116 offset:16\n" \
".LP0_m1:\n" \
"s_memtime vcc\n" \
"s_waitcnt lgkmcnt(0)\n" \
"s_sub_u32 s43, s41, vcc_lo\n" \
"s_cmp_gt_i32 s43, 0\n" \
"s_cbranch_scc1 .LP0_m1\n" \
"s_cmp_lt_i32 s43, s35\n" \
"s_cbranch_scc1 .LP0_m0\n" \
"buffer_load_dwordx4 v[0:3], v208, s[4:7], s47 offen sc1\n" \
"buffer_load_dwordx4 v[4:7], v208, s[4:7], s47 offen offset:1024 sc1\n" \
"s_nop 15\n" \
"s_nop 13\n" \
"s_mul_i32 s50, s40, 114688\n" \
"buffer_store_dwordx4 v[68:71], v218, s[12:15], s50 offen sc1\n" \
"buffer_store_dword v112, v213, s[4:7], s49 offen sc1\n" \
"s_xor_b32 s43, s49, s29\n" \
"buffer_store_dword v112, v213, s[4:7], s43 offen sc1\n" \
"s_add_u32 s43, s30, s40\n" \
"s_add_u32 s43, s43, -1\n" \
"v_mov_b32 v241, s43\n" \
"buffer_store_dword v241, v221, s[4:7], 0 offen sc1\n" \
"s_bitcmp1_b32 s31, 21\n" \
"s_cselect_b32 s43, s29, 0\n" \
"v_mov_b32 v116, s43\n" \
"ds_write_b32 v252, v116 offset:16\n" \
"v_mov_b32 v28, 0\n" \
"v_mov_b32 v29, 0\n" \
"v_mov_b32 v30, 0\n" \
"v_mov_b32 v31, 0\n" \
"v_mov_b32 v32, 0\n" \
"v_mov_b32 v33, 0\n" \
"v_mov_b32 v34, 0\n" \
"v_mov_b32 v35, 0\n" \
"v_mov_b32 v36, 0\n" \
"v_mov_b32 v37, 0\n" \
"v_mov_b32 v38, 0\n" \
"v_mov_b32 v39, 0\n" \
"v_mov_b32 v40, 0\n" \
"v_mov_b32 v41, 0\n" \
"v_mov_b32 v42, 0\n" \
"v_mov_b32 v43, 0\n" \
"s_waitcnt vmcnt(5)\n" \
"ds_write_b128 v220, v[0:3] offset:0\n" \
"s_waitcnt vmcnt(4)\n" \
"ds_write_b128 v220, v[4:7] offset:1024\n" \
"v_max3_u32 v44, v0, v0, v1\n" \
"v_max3_u32 v44, v44, v2, v3\n" \
"v_max3_u32 v44, v44, v4, v5\n" \
"v_max3_u32 v44, v44, v6, v7\n" \
"v_cmp_eq_u32 vcc, -1, v44\n" \
"v_smfmac_f32_16x16x64_bf16 v[28:31], v[0:3], a[0:7], v200\n" \
"v_smfmac_f32_16x16x64_bf16 v[32:35], v[0:3], a[56:63], v200\n" \
"s_cbranch_vccnz .LP0_retry1\n" \
"s_waitcnt lgkmcnt(0)\n" \
"s_barrier\n" \
"ds_read_b128 v[8:11], v220 offset:2048\n" \
"ds_read_b128 v[12:15], v220 offset:3072\n" \
"ds_read_b128 v[16:19], v220 offset:4096\n" \
"ds_read_b128 v[20:23], v220 offset:5120\n" \
"ds_read_b128 v[24:27], v220 offset:6144\n" \
"ds_read_b64 v[246:247], v252 offset:24\n" \
"ds_read_b96 v[248:250], v252\n" \
"v_smfmac_f32_16x16x64_bf16 v[36:39], v[0:3], a[112:119], v200\n" \
"v_smfmac_f32_16x16x64_bf16 v[40:43], v[0:3], a[168:175], v200\n" \
"v_smfmac_f32_16x16x64_bf16 v[28:31], v[4:7], a[8:15], v200\n" \
"v_smfmac_f32_16x16x64_bf16 v[32:35], v[4:7], a[64:71], v200\n" \
"v_smfmac_f32_16x16x64_bf16 v[36:39], v[4:7], a[120:127], v200\n" \
"v_smfmac_f32_16x16x64_bf16 v[40:43], v[4:7], a[176:183], v200\n" \
"s_waitcnt lgkmcnt(6)\n" \
"v_smfmac_f32_16x16x64_bf16 v[28:31], v[8:11], a[16:23], v200\n" \
"v_smfmac_f32_16x16x64_bf16 v[32:35], v[8:11], a[72:79], v200\n" \
"v_smfmac_f32_16x16x64_bf16 v[36:39], v[8:11], a[128:135], v200\n" \
"v_smfmac_f32_16x16x64_bf16 v[40:43], v[8:11], a[184:191], v200\n" \
"s_waitcnt lgkmcnt(0)\n" \
"v_readfirstlane_b32 s44, v248\n" \
"v_readfirstlane_b32 s45, v249\n" \
"v_smfmac_f32_16x16x64_bf16 v[28:31], v[12:15], a[24:31], v200\n" \
"v_readfirstlane_b32 s46, v250\n" \
"v_readfirstlane_b32 s26, v246\n" \
"v_readfirstlane_b32 s27, v247\n" \
"v_smfmac_f32_16x16x64_bf16 v[32:35], v[12:15], a[80:87], v200\n" \
"s_mul_i32 s42, s25, s46\n" \
"s_lshr_b32 s42, s42, 12\n" \
"s_add_u32 s24, s24, s26\n" \
"v_smfmac_f32_16x16x64_bf16 v[36:39], v[12:15], a[136:143], v200\n" \
"s_sub_u32 s43, s24, s44\n" \
"s_max_i32 s43, s43, 0xfffd0000\n" \
"s_min_i32 s43, s43, 0x4000\n" \
"v_smfmac_f32_16x16x64_bf16 v[40:43], v[12:15], a[192:199], v200\n" \
"s_mul_i32 s43, s43, s46\n" \
"s_ashr_i32 s43, s43, 12\n" \
"s_add_u32 s41, s45, s43\n" \
"s_cmp_eq_u32 s40, 63\n" \
"s_cbranch_scc1 .LP0_last\n" \
"s_branch .LP0_z1\n" \
".LP0_z0:\n" \
"s_add_u32 s41, s41, s42\n" \
"s_add_u32 s24, s24, s25\n" \
"s_xor_b32 s31, s31, s29\n" \
"s_xor_b32 s32, s32, s29\n" \
"s_xor_b32 s47, s47, s29\n" \
"s_xor_b32 s48, s48, s29\n" \
"s_add_u32 s39, s39, 1\n" \
"buffer_store_dwordx4 v[112:115], v223, s[4:7], s48 offen sc1\n" \
"s_bitcmp1_b32 s31, 21\n" \
"s_cselect_b32 s43, s29, 0\n" \
"v_mov_b32 v116, s43\n" \
"ds_write_b32 v252, v116 offset:16\n" \
".LP0_z1:\n" \
"s_memtime vcc\n" \
"s_waitcnt lgkmcnt(0)\n" \
"s_sub_u32 s43, s41, vcc_lo\n" \
"s_cmp_gt_i32 s43, 200\n" \
"s_cbranch_scc1 .LP0_z1\n" \
"s_cmp_lt_i32 s43, s37\n" \
"s_cbranch_scc1 .LP0_z0\n" \
"buffer_load_dwordx2 v[64:65], v211, s[4:7], s47 offen sc1\n" \
".p2align 6\n" \
".LP0_b1:\n" \
"s_memtime vcc\n" \
"s_waitcnt lgkmcnt(0)\n" \
"s_sub_u32 s43, s41, vcc_lo\n" \
"s_cmp_gt_i32 s43, 128\n" \
"s_cbranch_scc1 .LP0_b1\n" \
"s_cmp_lt_i32 s43, s37\n" \
"s_cbranch_scc1 .LP0_z0\n" \
"s_max_i32 s43, s43, 0\n" \
"s_and_b32 s43, s43, -4\n" \
"s_getpc_b64 vcc\n" \
".LP0_b3:\n" \
"s_sub_u32 s43, .LP0_b4-.LP0_b3, s43\n" \
"s_add_u32 vcc_lo, vcc_lo, s43\n" \
"s_addc_u32 vcc_hi, vcc_hi, 0\n" \
"s_setpc_b64 vcc\n" \
".rept 32\n" \
"s_nop 0\n" \
".endr\n" \
".LP0_b4:\n" \
"buffer_load_dwordx4 v[56:59], v210, s[4:7], s47 offen sc1\n" \
"buffer_load_dwordx4 v[60:63], v210, s[4:7], s47 offen offset:256 sc1\n" \
"v_smfmac_f32_16x16x64_bf16 v[28:31], v[16:19], a[32:39], v200\n" \
"v_smfmac_f32_16x16x64_bf16 v[32:35], v[16:19], a[88:95], v200\n" \
"v_smfmac_f32_16x16x64_bf16 v[36:39], v[16:19], a[144:151], v200\n" \
"v_smfmac_f32_16x16x64_bf16 v[40:43], v[16:19], a[200:207], v200\n" \
"v_smfmac_f32_16x16x64_bf16 v[28:31], v[20:23], a[40:47], v200\n" \
"v_smfmac_f32_16x16x64_bf16 v[32:35], v[20:23], a[96:103], v200\n" \
"buffer_load_dwordx4 v[50:53], v212, s[4:7], s47 offen sc1\n" \
"v_smfmac_f32_16x16x64_bf16 v[36:39], v[20:23], a[152:159], v200\n" \
"v_smfmac_f32_16x16x64_bf16 v[40:43], v[20:23], a[208:215], v200\n" \
"v_smfmac_f32_16x16x64_bf16 v[28:31], v[24:27], a[48:55], v200\n" \
"v_smfmac_f32_16x16x64_bf16 v[32:35], v[24:27], a[104:111], v200\n" \
"v_smfmac_f32_16x16x64_bf16 v[36:39], v[24:27], a[160:167], v200\n" \
"v_smfmac_f32_16x16x64_bf16 v[40:43], v[24:27], a[216:223], v200\n" \
"s_nop 2\n" \
"v_mov_b32 v245, v245\n" \
"v_mov_b32 v245, v245\n" \
"v_mov_b32 v245, v245\n" \
"v_permlane32_swap_b32 v28, v32\n" \
"v_permlane32_swap_b32 v29, v33\n" \
"v_permlane32_swap_b32 v30, v34\n" \
"v_permlane32_swap_b32 v31, v35\n" \
"v_add_f32 v28, v28, v32\n" \
"v_add_f32 v29, v29, v33\n" \
"v_add_f32 v30, v30, v34\n" \
"v_add_f32 v31, v31, v35\n" \
"v_cvt_pk_bf16_f32 v68, v28, v29\n" \
"v_cvt_pk_bf16_f32 v69, v30, v31\n" \
"v_permlane32_swap_b32 v36, v40\n" \
"v_permlane32_swap_b32 v37, v41\n" \
"v_permlane32_swap_b32 v38, v42\n" \
"v_permlane32_swap_b32 v39, v43\n" \
"v_add_f32 v36, v36, v40\n" \
"v_add_f32 v37, v37, v41\n" \
"v_add_f32 v38, v38, v42\n" \
"v_add_f32 v39, v39, v43\n" \
"v_cvt_pk_bf16_f32 v70, v36, v37\n" \
"v_cvt_pk_bf16_f32 v71, v38, v39\n" \
"v_mov_b32 v48, v68\n" \
"v_mov_b32 v49, v69\n" \
"v_mov_b32 v54, v70\n" \
"v_mov_b32 v55, v71\n" \
"s_nop 1\n" \
"v_permlane16_swap_b32 v48, v54\n" \
"v_permlane16_swap_b32 v49, v55\n" \
".LP0_wait:\n" \
"s_waitcnt vmcnt(1)\n" \
"v_pk_add_f32 v[72:73], v[56:57], v[60:61]\n" \
"v_pk_add_f32 v[74:75], v[58:59], v[62:63]\n" \
"v_max3_f32 v77, v72, v73, v64\n" \
"v_max3_f32 v77, v77, v74, v75\n" \
"s_nop 1\n" \
"v_max_f32_dpp v76, v77, v77 row_ror:8 row_mask:0xf bank_mask:0xf\n" \
"v_pk_add_f32 v[72:73], v[72:73], v[76:77] op_sel_hi:[1,0] neg_lo:[0,1] neg_hi:[0,1]\n" \
"v_pk_add_f32 v[74:75], v[74:75], v[76:77] op_sel_hi:[1,0] neg_lo:[0,1] neg_hi:[0,1]\n" \
"v_sub_f32 v78, v64, v76\n" \
"v_exp_f32 v72, v72\n" \
"v_exp_f32 v73, v73\n" \
"v_exp_f32 v74, v74\n" \
"v_exp_f32 v75, v75\n" \
"v_exp_f32 v78, v78\n" \
"v_pk_add_f32 v[80:81], v[72:73], v[74:75]\n" \
"v_fma_f32 v80, v78, v65, v80\n" \
"v_add_f32 v81, v80, v81\n" \
"s_nop 1\n" \
"v_add_f32_dpp v80, v81, v81 row_ror:8 row_mask:0xf bank_mask:0xf\n" \
"v_rcp_f32 v80, v80\n" \
"s_nop 0\n" \
"v_mul_f32 v78, v78, v80\n" \
"v_pk_mul_f32 v[72:73], v[72:73], v[80:81] op_sel_hi:[1,0]\n" \
"v_pk_mul_f32 v[74:75], v[74:75], v[80:81] op_sel_hi:[1,0]\n" \
"v_cvt_pk_bf16_f32 v78, v78, v78\n" \
"v_cvt_pk_bf16_f32 v82, v72, v73\n" \
"v_cvt_pk_bf16_f32 v83, v74, v75\n" \
"v_and_b32 v84, v78, v214\n" \
"v_and_b32 v85, v78, v215\n" \
"v_cvt_pk_bf16_f32 v86, v72, v73\n" \
"v_cvt_pk_bf16_f32 v87, v74, v75\n" \
"s_waitcnt vmcnt(0)\n" \
"v_mfma_f32_16x16x32_bf16 v[88:91], v[48:51], v[82:85], 0\n" \
"v_mfma_f32_16x16x32_bf16 v[92:95], v[52:55], v[84:87], 0\n" \
"s_nop 2\n" \
"v_mov_b32 v245, v245\n" \
"s_nop 2\n" \
"v_mov_b32 v245, v245\n" \
"v_add_f32_dpp v96, v92, v88 row_ror:8 row_mask:0xf bank_mask:0x3\n" \
"v_add_f32_dpp v97, v93, v89 row_ror:8 row_mask:0xf bank_mask:0x3\n" \
"v_add_f32_dpp v96, v90, v94 row_ror:8 row_mask:0xf bank_mask:0xc\n" \
"v_add_f32_dpp v97, v91, v95 row_ror:8 row_mask:0xf bank_mask:0xc\n" \
"ds_write_b64 v216, v[96:97]\n" \
"v_cmp_u_f32 vcc, v96, v97\n" \
"s_bitcmp1_b32 s31, 21\n" \
"s_cselect_b32 s18, s29, 0\n" \
"s_xor_b32 s18, s18, s48\n" \
"v_mov_b32 v245, v245\n" \
"s_xor_b32 s19, s18, s29\n" \
"s_nop 0\n" \
"s_nop 0\n" \
"v_mov_b32 v245, v245\n" \
"s_nop 0\n" \
"s_nop 0\n" \
"s_nop 0\n" \
"v_mov_b32 v245, v245\n" \
"s_cbranch_vccnz .LP0_cls\n" \
".LP0_clsr:\n" \
"s_waitcnt lgkmcnt(0)\n" \
"s_barrier\n" \
"ds_read_b64 v[100:101], v217 offset:0\n" \
"ds_read_b64 v[102:103], v217 offset:512\n" \
"ds_read_b32 v116, v252 offset:16\n" \
"ds_read_b64 v[104:105], v217 offset:1024\n" \
"ds_read_b64 v[106:107], v217 offset:1536\n" \
"v_mov_b32 v245, v245\n" \
"v_mov_b32 v245, v245\n" \
"s_add_u32 s40, s40, 1\n" \
"s_add_u32 s24, s24, s27\n" \
"s_mov_b32 s47, s48\n" \
"v_mov_b32 v245, v245\n" \
"s_waitcnt lgkmcnt(2)\n" \
"v_xad_u32 v117, v116, s18, v213\n" \
"v_pk_add_f32 v[100:101], v[100:101], v[102:103]\n" \
"s_waitcnt lgkmcnt(0)\n" \
"v_pk_add_f32 v[104:105], v[104:105], v[106:107]\n" \
"v_pk_add_f32 v[100:101], v[100:101], v[104:105]\n" \
"v_cvt_pk_bf16_f32 v96, v100, v101\n" \
"buffer_store_dword v96, v117, s[4:7], 0 offen sc1\n" \
"s_add_u32 s43, s40, 1\n" \
"s_and_b32 s43, s43, 3\n" \
"s_bitcmp1_b32 s43, 0\n" \
"s_cselect_b32 s48, s32, s31\n" \
"s_lshl_b32 s43, s43, 19\n" \
"s_add_u32 s48, s48, s43\n" \
"s_add_u32 s43, s40, 2\n" \
"s_and_b32 s43, s43, 3\n" \
"s_bitcmp1_b32 s43, 0\n" \
"s_cselect_b32 s49, s32, s31\n" \
"s_lshl_b32 s43, s43, 19\n" \
"s_add_u32 s49, s49, s43\n" \
"s_sub_u32 s43, s24, s44\n" \
"s_add_u32 s43, s43, s28\n" \
"s_max_i32 s43, s43, 0xfffd0000\n" \
"s_min_i32 s43, s43, 0x4000\n" \
"s_mul_i32 s43, s43, s46\n" \
"s_ashr_i32 s43, s43, 12\n" \
"s_add_u32 s41, s45, s43\n" \
"s_lshr_b32 s35, s42, 1\n" \
"s_sub_u32 s35, 0, s35\n" \
"v_xad_u32 v116, v116, s19, v213\n" \
"buffer_store_dword v96, v116, s[4:7], 0 offen sc1\n" \
"s_cmp_eq_u32 s40, 0\n" \
"s_cbranch_scc1 .LP0_wet\n" \
"s_branch .LP0_loop\n" \
"s_branch .LP0_y1\n" \
".LP0_retry2:\n" \
"s_add_u32 s51, s51, 1\n" \
"s_cmp_gt_u32 s51, 0x20000\n" \
"s_cbranch_scc1 .LP0_end\n" \
".LP0_y0:\n" \
"s_add_u32 s41, s41, s42\n" \
"s_add_u32 s24, s24, s25\n" \
"s_xor_b32 s31, s31, s29\n" \
"s_xor_b32 s32, s32, s29\n" \
"s_xor_b32 s47, s47, s29\n" \
"s_xor_b32 s48, s48, s29\n" \
"s_add_u32 s39, s39, 1\n" \
"buffer_store_dwordx4 v[112:115], v223, s[4:7], s48 offen sc1\n" \
"s_bitcmp1_b32 s31, 21\n" \
"s_cselect_b32 s43, s29, 0\n" \
"v_mov_b32 v116, s43\n" \
"ds_write_b32 v252, v116 offset:16\n" \
".LP0_y1:\n" \
"s_memtime vcc\n" \
"s_waitcnt lgkmcnt(0)\n" \
"s_sub_u32 s43, s41, vcc_lo\n" \
"s_cmp_gt_i32 s43, 200\n" \
"s_cbranch_scc1 .LP0_y1\n" \
"s_cmp_lt_i32 s43, s37\n" \
"s_cbranch_scc1 .LP0_y0\n" \
"buffer_load_dwordx2 v[64:65], v211, s[4:7], s47 offen sc1\n" \
".p2align 6\n" \
".LP0_c1:\n" \
"s_memtime vcc\n" \
"s_waitcnt lgkmcnt(0)\n" \
"s_sub_u32 s43, s41, vcc_lo\n" \
"s_cmp_gt_i32 s43, 128\n" \
"s_cbranch_scc1 .LP0_c1\n" \
"s_cmp_lt_i32 s43, s37\n" \
"s_cbranch_scc1 .LP0_y0\n" \
"s_max_i32 s43, s43, 0\n" \
"s_and_b32 s43, s43, -4\n" \
"s_getpc_b64 vcc\n" \
".LP0_c3:\n" \
"s_sub_u32 s43, .LP0_c4-.LP0_c3, s43\n" \
"s_add_u32 vcc_lo, vcc_lo, s43\n" \
"s_addc_u32 vcc_hi, vcc_hi, 0\n" \
"s_setpc_b64 vcc\n" \
".rept 32\n" \
"s_nop 0\n" \
".endr\n" \
".LP0_c4:\n" \
"buffer_load_dwordx4 v[56:59], v210, s[4:7], s47 offen sc1\n" \
"buffer_load_dwordx4 v[60:63], v210, s[4:7], s47 offen offset:256 sc1\n" \
"s_nop 2\n" \
"v_mov_b32 v245, v245\n" \
"s_nop 2\n" \
"v_mov_b32 v245, v245\n" \
"s_nop 2\n" \
"v_mov_b32 v245, v245\n" \
"s_nop 2\n" \
"v_mov_b32 v245, v245\n" \
"s_nop 2\n" \
"v_mov_b32 v245, v245\n" \
"s_nop 2\n" \
"v_mov_b32 v245, v245\n" \
"buffer_load_dwordx4 v[50:53], v212, s[4:7], s47 offen sc1\n" \
"s_nop 2\n" \
"v_mov_b32 v245, v245\n" \
"s_nop 2\n" \
"v_mov_b32 v245, v245\n" \
"s_nop 2\n" \
"v_mov_b32 v245, v245\n" \
"s_nop 2\n" \
"v_mov_b32 v245, v245\n" \
"s_nop 2\n" \
"v_mov_b32 v245, v245\n" \
"s_nop 2\n" \
"v_mov_b32 v245, v245\n" \
"s_nop 2\n" \
"v_mov_b32 v245, v245\n" \
"s_nop 2\n" \
"v_mov_b32 v245, v245\n" \
"s_nop 2\n" \
"v_mov_b32 v245, v245\n" \
"s_nop 2\n" \
"v_mov_b32 v245, v245\n" \
"s_nop 2\n" \
"v_mov_b32 v245, v245\n" \
"s_nop 2\n" \
"v_mov_b32 v245, v245\n" \
"s_nop 2\n" \
"v_mov_b32 v245, v245\n" \
"s_nop 2\n" \
"v_mov_b32 v245, v245\n" \
"s_nop 2\n" \
"v_mov_b32 v245, v245\n" \
"s_nop 2\n" \
"v_mov_b32 v245, v245\n" \
"s_nop 2\n" \
"v_mov_b32 v245, v245\n" \
"s_branch .LP0_wait\n" \
".LP0_cls:\n" \
"s_waitcnt vmcnt(0)\n" \
"v_max3_u32 v44, v64, v64, v65\n" \
"v_max3_u32 v44, v44, v50, v51\n" \
"v_max3_u32 v44, v44, v52, v53\n" \
"v_cmp_eq_u32 vcc, -1, v44\n" \
"s_cbranch_vccnz .LP0_retry2\n" \
"v_cmp_u_f32 vcc, v56, v56\n" \
"v_cmp_ne_u32 s[2:3], v56, v109\n" \
"s_and_b64 vcc, vcc, s[2:3]\n" \
"s_cbranch_vccnz .LP0_retry2\n" \
"v_cmp_u_f32 vcc, v57, v57\n" \
"v_cmp_ne_u32 s[2:3], v57, v109\n" \
"s_and_b64 vcc, vcc, s[2:3]\n" \
"s_cbranch_vccnz .LP0_retry2\n" \
"v_cmp_u_f32 vcc, v58, v58\n" \
"v_cmp_ne_u32 s[2:3], v58, v109\n" \
"s_and_b64 vcc, vcc, s[2:3]\n" \
"s_cbranch_vccnz .LP0_retry2\n" \
"v_cmp_u_f32 vcc, v59, v59\n" \
"v_cmp_ne_u32 s[2:3], v59, v109\n" \
"s_and_b64 vcc, vcc, s[2:3]\n" \
"s_cbranch_vccnz .LP0_retry2\n" \
"v_cmp_u_f32 vcc, v60, v60\n" \
"v_cmp_ne_u32 s[2:3], v60, v109\n" \
"s_and_b64 vcc, vcc, s[2:3]\n" \
"s_cbranch_vccnz .LP0_retry2\n" \
"v_cmp_u_f32 vcc, v61, v61\n" \
"v_cmp_ne_u32 s[2:3], v61, v109\n" \
"s_and_b64 vcc, vcc, s[2:3]\n" \
"s_cbranch_vccnz .LP0_retry2\n" \
"v_cmp_u_f32 vcc, v62, v62\n" \
"v_cmp_ne_u32 s[2:3], v62, v109\n" \
"s_and_b64 vcc, vcc, s[2:3]\n" \
"s_cbranch_vccnz .LP0_retry2\n" \
"v_cmp_u_f32 vcc, v63, v63\n" \
"v_cmp_ne_u32 s[2:3], v63, v109\n" \
"s_and_b64 vcc, vcc, s[2:3]\n" \
"s_cbranch_vccnz .LP0_retry2\n" \
"s_branch .LP0_clsr\n" \
".LP0_last:\n" \
"v_smfmac_f32_16x16x64_bf16 v[28:31], v[16:19], a[32:39], v200\n" \
"v_smfmac_f32_16x16x64_bf16 v[32:35], v[16:19], a[88:95], v200\n" \
"v_smfmac_f32_16x16x64_bf16 v[36:39], v[16:19], a[144:151], v200\n" \
"v_smfmac_f32_16x16x64_bf16 v[40:43], v[16:19], a[200:207], v200\n" \
"v_smfmac_f32_16x16x64_bf16 v[28:31], v[20:23], a[40:47], v200\n" \
"v_smfmac_f32_16x16x64_bf16 v[32:35], v[20:23], a[96:103], v200\n" \
"v_smfmac_f32_16x16x64_bf16 v[36:39], v[20:23], a[152:159], v200\n" \
"v_smfmac_f32_16x16x64_bf16 v[40:43], v[20:23], a[208:215], v200\n" \
"v_smfmac_f32_16x16x64_bf16 v[28:31], v[24:27], a[48:55], v200\n" \
"v_smfmac_f32_16x16x64_bf16 v[32:35], v[24:27], a[104:111], v200\n" \
"v_smfmac_f32_16x16x64_bf16 v[36:39], v[24:27], a[160:167], v200\n" \
"v_smfmac_f32_16x16x64_bf16 v[40:43], v[24:27], a[216:223], v200\n" \
"s_waitcnt vmcnt(0)\n" \
"s_nop 7\n" \
"v_permlane32_swap_b32 v28, v32\n" \
"v_permlane32_swap_b32 v29, v33\n" \
"v_permlane32_swap_b32 v30, v34\n" \
"v_permlane32_swap_b32 v31, v35\n" \
"v_add_f32 v28, v28, v32\n" \
"v_add_f32 v29, v29, v33\n" \
"v_add_f32 v30, v30, v34\n" \
"v_add_f32 v31, v31, v35\n" \
"v_cvt_pk_bf16_f32 v68, v28, v29\n" \
"v_cvt_pk_bf16_f32 v69, v30, v31\n" \
"v_permlane32_swap_b32 v36, v40\n" \
"v_permlane32_swap_b32 v37, v41\n" \
"v_permlane32_swap_b32 v38, v42\n" \
"v_permlane32_swap_b32 v39, v43\n" \
"v_add_f32 v36, v36, v40\n" \
"v_add_f32 v37, v37, v41\n" \
"v_add_f32 v38, v38, v42\n" \
"v_add_f32 v39, v39, v43\n" \
"v_cvt_pk_bf16_f32 v70, v36, v37\n" \
"v_cvt_pk_bf16_f32 v71, v38, v39\n" \
"s_mov_b32 s50, 7340032\n" \
"buffer_store_dwordx4 v[68:71], v218, s[12:15], s50 offen sc1\n" \
"s_waitcnt vmcnt(0)\n" \
"s_barrier\n" \
"s_add_u32 s43, s30, s40\n" \
"s_add_u32 s43, s43, 1\n" \
"v_mov_b32 v241, s43\n" \
"buffer_store_dword v241, v221, s[4:7], 0 offen sc1\n" \
".LP0_end:\n" \
"s_endpgm\n" \
".p2align 6\n" \
"fk_role_P1:\n" \
"ds_read_b32 v200, v255 offset:0\n" \
"ds_read_b32 v201, v255 offset:256\n" \
"ds_read_b32 v202, v255 offset:512\n" \
"ds_read_b32 v203, v255 offset:768\n" \
"ds_read_b32 v204, v255 offset:1024\n" \
"ds_read_b32 v205, v255 offset:1280\n" \
"ds_read_b32 v206, v255 offset:1536\n" \
"ds_read_b32 v207, v255 offset:1792\n" \
"ds_read_b32 v208, v255 offset:2048\n" \
"ds_read_b32 v209, v255 offset:2304\n" \
"ds_read_b32 v210, v255 offset:2560\n" \
"ds_read_b32 v211, v255 offset:2816\n" \
"ds_read_b32 v212, v255 offset:3072\n" \
"ds_read_b32 v213, v255 offset:3328\n" \
"ds_read_b32 v214, v255 offset:3584\n" \
"ds_read_b32 v215, v255 offset:3840\n" \
"ds_read_b32 v216, v255 offset:4096\n" \
"ds_read_b32 v217, v255 offset:4352\n" \
"ds_read_b32 v218, v255 offset:4608\n" \
"ds_read_b32 v219, v255 offset:4864\n" \
"ds_read_b32 v220, v255 offset:5120\n" \
"ds_read_b32 v221, v255 offset:5376\n" \
"ds_read_b32 v222, v255 offset:5632\n" \
"ds_read_b32 v223, v255 offset:5888\n" \
"ds_read_b32 v254, v255 offset:7936\n" \
"s_waitcnt lgkmcnt(0)\n" \
"v_readlane_b32 s4, v254, 0\n" \
"v_readlane_b32 s5, v254, 1\n" \
"v_readlane_b32 s6, v254, 2\n" \
"v_readlane_b32 s7, v254, 3\n" \
"v_readlane_b32 s8, v254, 4\n" \
"v_readlane_b32 s9, v254, 5\n" \
"v_readlane_b32 s10, v254, 6\n" \
"v_readlane_b32 s11, v254, 7\n" \
"v_readlane_b32 s12, v254, 8\n" \
"v_readlane_b32 s13, v254, 9\n" \
"v_readlane_b32 s14, v254, 10\n" \
"v_readlane_b32 s15, v254, 11\n" \
"v_readlane_b32 s16, v254, 12\n" \
"v_readlane_b32 s17, v254, 13\n" \
"v_readlane_b32 s18, v254, 14\n" \
"v_readlane_b32 s19, v254, 15\n" \
"v_readlane_b32 s20, v254, 16\n" \
"v_readlane_b32 s21, v254, 17\n" \
"v_readlane_b32 s22, v254, 18\n" \
"v_readlane_b32 s23, v254, 19\n" \
"v_readlane_b32 s24, v254, 20\n" \
"v_readlane_b32 s25, v254, 21\n" \
"v_readlane_b32 s26, v254, 22\n" \
"v_readlane_b32 s27, v254, 23\n" \
"v_readlane_b32 s28, v254, 24\n" \
"v_readlane_b32 s29, v254, 25\n" \
"v_readlane_b32 s30, v254, 26\n" \
"v_readlane_b32 s31, v254, 27\n" \
"v_readlane_b32 s32, v254, 28\n" \
"v_readlane_b32 s33, v254, 29\n" \
"v_readlane_b32 s34, v254, 30\n" \
"v_readlane_b32 s35, v254, 31\n" \
"v_readlane_b32 s36, v254, 32\n" \
"v_readlane_b32 s37, v254, 33\n" \
"v_readlane_b32 s38, v254, 34\n" \
"v_readlane_b32 s39, v254, 35\n" \
"s_mov_b32 s40, 0\n" \
"s_mov_b32 s51, 0\n" \
"s_nop 0\n" \
"v_mov_b32 v252, s38\n" \
"s_nop 4\n" \
"v_mov_b32 v112, -1\n" \
"v_mov_b32 v113, -1\n" \
"v_mov_b32 v114, -1\n" \
"v_mov_b32 v115, -1\n" \
"v_mov_b32 v109, 0x7fc00001\n" \
"v_writelane_b32 v251, s6, 0\n" \
"v_writelane_b32 v251, s10, 1\n" \
"v_writelane_b32 v251, s14, 2\n" \
"v_writelane_b32 v251, s18, 3\n" \
"s_mov_b32 s6, 0\n" \
"s_mov_b32 s10, 0\n" \
"s_mov_b32 s14, 0\n" \
"s_mov_b32 s18, 0\n" \
"s_mov_b32 s40, -1\n" \
"s_branch .LP1_pre\n" \
".LP1_wet:\n" \
"s_waitcnt vmcnt(0)\n" \
"v_readlane_b32 s6, v251, 0\n" \
"v_readlane_b32 s10, v251, 1\n" \
"v_readlane_b32 s14, v251, 2\n" \
"v_readlane_b32 s18, v251, 3\n" \
"s_nop 4\n" \
"s_mov_b32 s33, 0\n" \
"s_mov_b32 s34, 0\n" \
"s_add_u32 s43, s30, 1\n" \
"s_mov_b32 s50, 0\n" \
".LP1_w34l:\n" \
"buffer_load_dword v240, v204, s[4:7], 0 offen sc1\n" \
"s_waitcnt vmcnt(0)\n" \
".LP1_w34c:\n" \
"v_subrev_u32 v241, s43, v240\n" \
"v_cmp_gt_u32 vcc, 0x100, v241\n" \
"s_cmp_eq_u64 vcc, -1\n" \
"s_cbranch_scc1 .LP1_w34d\n" \
"s_sleep 2\n" \
"s_add_u32 s50, s50, 1\n" \
"s_cmp_lt_u32 s50, 0x20000\n" \
"s_cbranch_scc1 .LP1_w34l\n" \
"s_branch .LP1_end\n" \
".LP1_w34d:\n" \
"v_readlane_b32 s43, v202, 4\n" \
"s_nop 4\n" \
"buffer_load_dwordx4 a[32:35], v201, s[8:11], s43 offen sc1\n" \
"buffer_load_dwordx4 a[36:39], v201, s[8:11], s43 offen offset:1024 sc1\n" \
"v_readlane_b32 s43, v202, 11\n" \
"s_nop 4\n" \
"buffer_load_dwordx4 a[88:91], v201, s[8:11], s43 offen sc1\n" \
"buffer_load_dwordx4 a[92:95], v201, s[8:11], s43 offen offset:1024 sc1\n" \
"v_readlane_b32 s43, v202, 18\n" \
"s_nop 4\n" \
"buffer_load_dwordx4 a[144:147], v201, s[8:11], s43 offen sc1\n" \
"buffer_load_dwordx4 a[148:151], v201, s[8:11], s43 offen offset:1024 sc1\n" \
"v_readlane_b32 s43, v202, 25\n" \
"s_nop 4\n" \
"buffer_load_dwordx4 a[200:203], v201, s[8:11], s43 offen sc1\n" \
"buffer_load_dwordx4 a[204:207], v201, s[8:11], s43 offen offset:1024 sc1\n" \
"s_add_u32 s43, s30, 2\n" \
"s_mov_b32 s50, 0\n" \
".LP1_w35l:\n" \
"buffer_load_dword v240, v204, s[4:7], 0 offen sc1\n" \
"s_waitcnt vmcnt(0)\n" \
".LP1_w35c:\n" \
"v_subrev_u32 v241, s43, v240\n" \
"v_cmp_gt_u32 vcc, 0x100, v241\n" \
"s_cmp_eq_u64 vcc, -1\n" \
"s_cbranch_scc1 .LP1_w35d\n" \
"s_sleep 2\n" \
"s_add_u32 s50, s50, 1\n" \
"s_cmp_lt_u32 s50, 0x20000\n" \
"s_cbranch_scc1 .LP1_w35l\n" \
"s_branch .LP1_end\n" \
".LP1_w35d:\n" \
"v_readlane_b32 s43, v202, 5\n" \
"s_nop 4\n" \
"buffer_load_dwordx4 a[40:43], v201, s[8:11], s43 offen sc1\n" \
"buffer_load_dwordx4 a[44:47], v201, s[8:11], s43 offen offset:1024 sc1\n" \
"v_readlane_b32 s43, v202, 12\n" \
"s_nop 4\n" \
"buffer_load_dwordx4 a[96:99], v201, s[8:11], s43 offen sc1\n" \
"buffer_load_dwordx4 a[100:103], v201, s[8:11], s43 offen offset:1024 sc1\n" \
"v_readlane_b32 s43, v202, 19\n" \
"s_nop 4\n" \
"buffer_load_dwordx4 a[152:155], v201, s[8:11], s43 offen sc1\n" \
"buffer_load_dwordx4 a[156:159], v201, s[8:11], s43 offen offset:1024 sc1\n" \
"v_readlane_b32 s43, v202, 26\n" \
"s_nop 4\n" \
"buffer_load_dwordx4 a[208:211], v201, s[8:11], s43 offen sc1\n" \
"buffer_load_dwordx4 a[212:215], v201, s[8:11], s43 offen offset:1024 sc1\n" \
"s_add_u32 s43, s30, 3\n" \
"s_mov_b32 s50, 0\n" \
".LP1_w36l:\n" \
"buffer_load_dword v240, v204, s[4:7], 0 offen sc1\n" \
"s_waitcnt vmcnt(0)\n" \
".LP1_w36c:\n" \
"v_subrev_u32 v241, s43, v240\n" \
"v_cmp_gt_u32 vcc, 0x100, v241\n" \
"s_cmp_eq_u64 vcc, -1\n" \
"s_cbranch_scc1 .LP1_w36d\n" \
"s_sleep 2\n" \
"s_add_u32 s50, s50, 1\n" \
"s_cmp_lt_u32 s50, 0x20000\n" \
"s_cbranch_scc1 .LP1_w36l\n" \
"s_branch .LP1_end\n" \
".LP1_w36d:\n" \
"v_readlane_b32 s43, v202, 0\n" \
"s_nop 4\n" \
"buffer_load_dwordx4 a[0:3], v201, s[8:11], s43 offen sc1\n" \
"buffer_load_dwordx4 a[4:7], v201, s[8:11], s43 offen offset:1024 sc1\n" \
"v_readlane_b32 s43, v202, 7\n" \
"s_nop 4\n" \
"buffer_load_dwordx4 a[56:59], v201, s[8:11], s43 offen sc1\n" \
"buffer_load_dwordx4 a[60:63], v201, s[8:11], s43 offen offset:1024 sc1\n" \
"v_readlane_b32 s43, v202, 14\n" \
"s_nop 4\n" \
"buffer_load_dwordx4 a[112:115], v201, s[8:11], s43 offen sc1\n" \
"buffer_load_dwordx4 a[116:119], v201, s[8:11], s43 offen offset:1024 sc1\n" \
"v_readlane_b32 s43, v202, 21\n" \
"s_nop 4\n" \
"buffer_load_dwordx4 a[168:171], v201, s[8:11], s43 offen sc1\n" \
"buffer_load_dwordx4 a[172:175], v201, s[8:11], s43 offen offset:1024 sc1\n" \
"s_add_u32 s43, s30, 4\n" \
"s_mov_b32 s50, 0\n" \
".LP1_w37l:\n" \
"buffer_load_dword v240, v204, s[4:7], 0 offen sc1\n" \
"s_waitcnt vmcnt(0)\n" \
".LP1_w37c:\n" \
"v_subrev_u32 v241, s43, v240\n" \
"v_cmp_gt_u32 vcc, 0x100, v241\n" \
"s_cmp_eq_u64 vcc, -1\n" \
"s_cbranch_scc1 .LP1_w37d\n" \
"s_sleep 2\n" \
"s_add_u32 s50, s50, 1\n" \
"s_cmp_lt_u32 s50, 0x20000\n" \
"s_cbranch_scc1 .LP1_w37l\n" \
"s_branch .LP1_end\n" \
".LP1_w37d:\n" \
"v_readlane_b32 s43, v202, 6\n" \
"s_nop 4\n" \
"buffer_load_dwordx4 a[48:51], v201, s[8:11], s43 offen sc1\n" \
"buffer_load_dwordx4 a[52:55], v201, s[8:11], s43 offen offset:1024 sc1\n" \
"v_readlane_b32 s43, v202, 13\n" \
"s_nop 4\n" \
"buffer_load_dwordx4 a[104:107], v201, s[8:11], s43 offen sc1\n" \
"buffer_load_dwordx4 a[108:111], v201, s[8:11], s43 offen offset:1024 sc1\n" \
"v_readlane_b32 s43, v202, 20\n" \
"s_nop 4\n" \
"buffer_load_dwordx4 a[160:163], v201, s[8:11], s43 offen sc1\n" \
"buffer_load_dwordx4 a[164:167], v201, s[8:11], s43 offen offset:1024 sc1\n" \
"v_readlane_b32 s43, v202, 27\n" \
"s_nop 4\n" \
"buffer_load_dwordx4 a[216:219], v201, s[8:11], s43 offen sc1\n" \
"buffer_load_dwordx4 a[220:223], v201, s[8:11], s43 offen offset:1024 sc1\n" \
"s_add_u32 s43, s30, 5\n" \
"s_mov_b32 s50, 0\n" \
".LP1_w38l:\n" \
"buffer_load_dword v240, v204, s[4:7], 0 offen sc1\n" \
"s_waitcnt vmcnt(0)\n" \
".LP1_w38c:\n" \
"v_subrev_u32 v241, s43, v240\n" \
"v_cmp_gt_u32 vcc, 0x100, v241\n" \
"s_cmp_eq_u64 vcc, -1\n" \
"s_cbranch_scc1 .LP1_w38d\n" \
"s_sleep 2\n" \
"s_add_u32 s50, s50, 1\n" \
"s_cmp_lt_u32 s50, 0x20000\n" \
"s_cbranch_scc1 .LP1_w38l\n" \
"s_branch .LP1_end\n" \
".LP1_w38d:\n" \
"v_readlane_b32 s43, v202, 1\n" \
"s_nop 4\n" \
"buffer_load_dwordx4 a[8:11], v201, s[8:11], s43 offen sc1\n" \
"buffer_load_dwordx4 a[12:15], v201, s[8:11], s43 offen offset:1024 sc1\n" \
"v_readlane_b32 s43, v202, 8\n" \
"s_nop 4\n" \
"buffer_load_dwordx4 a[64:67], v201, s[8:11], s43 offen sc1\n" \
"buffer_load_dwordx4 a[68:71], v201, s[8:11], s43 offen offset:1024 sc1\n" \
"v_readlane_b32 s43, v202, 15\n" \
"s_nop 4\n" \
"buffer_load_dwordx4 a[120:123], v201, s[8:11], s43 offen sc1\n" \
"buffer_load_dwordx4 a[124:127], v201, s[8:11], s43 offen offset:1024 sc1\n" \
"v_readlane_b32 s43, v202, 22\n" \
"s_nop 4\n" \
"buffer_load_dwordx4 a[176:179], v201, s[8:11], s43 offen sc1\n" \
"buffer_load_dwordx4 a[180:183], v201, s[8:11], s43 offen offset:1024 sc1\n" \
"s_add_u32 s43, s30, 6\n" \
"s_mov_b32 s50, 0\n" \
".LP1_w39l:\n" \
"buffer_load_dword v240, v204, s[4:7], 0 offen sc1\n" \
"s_waitcnt vmcnt(0)\n" \
".LP1_w39c:\n" \
"v_subrev_u32 v241, s43, v240\n" \
"v_cmp_gt_u32 vcc, 0x100, v241\n" \
"s_cmp_eq_u64 vcc, -1\n" \
"s_cbranch_scc1 .LP1_w39d\n" \
"s_sleep 2\n" \
"s_add_u32 s50, s50, 1\n" \
"s_cmp_lt_u32 s50, 0x20000\n" \
"s_cbranch_scc1 .LP1_w39l\n" \
"s_branch .LP1_end\n" \
".LP1_w39d:\n" \
"v_readlane_b32 s43, v202, 2\n" \
"s_nop 4\n" \
"buffer_load_dwordx4 a[16:19], v201, s[8:11], s43 offen sc1\n" \
"buffer_load_dwordx4 a[20:23], v201, s[8:11], s43 offen offset:1024 sc1\n" \
"v_readlane_b32 s43, v202, 9\n" \
"s_nop 4\n" \
"buffer_load_dwordx4 a[72:75], v201, s[8:11], s43 offen sc1\n" \
"buffer_load_dwordx4 a[76:79], v201, s[8:11], s43 offen offset:1024 sc1\n" \
"v_readlane_b32 s43, v202, 16\n" \
"s_nop 4\n" \
"buffer_load_dwordx4 a[128:131], v201, s[8:11], s43 offen sc1\n" \
"buffer_load_dwordx4 a[132:135], v201, s[8:11], s43 offen offset:1024 sc1\n" \
"v_readlane_b32 s43, v202, 23\n" \
"s_nop 4\n" \
"buffer_load_dwordx4 a[184:187], v201, s[8:11], s43 offen sc1\n" \
"buffer_load_dwordx4 a[188:191], v201, s[8:11], s43 offen offset:1024 sc1\n" \
"s_add_u32 s43, s30, 7\n" \
"s_mov_b32 s50, 0\n" \
".LP1_w40l:\n" \
"buffer_load_dword v240, v204, s[4:7], 0 offen sc1\n" \
"s_waitcnt vmcnt(0)\n" \
".LP1_w40c:\n" \
"v_subrev_u32 v241, s43, v240\n" \
"v_cmp_gt_u32 vcc, 0x100, v241\n" \
"s_cmp_eq_u64 vcc, -1\n" \
"s_cbranch_scc1 .LP1_w40d\n" \
"s_sleep 2\n" \
"s_add_u32 s50, s50, 1\n" \
"s_cmp_lt_u32 s50, 0x20000\n" \
"s_cbranch_scc1 .LP1_w40l\n" \
"s_branch .LP1_end\n" \
".LP1_w40d:\n" \
"v_readlane_b32 s43, v202, 3\n" \
"s_nop 4\n" \
"buffer_load_dwordx4 a[24:27], v201, s[8:11], s43 offen sc1\n" \
"buffer_load_dwordx4 a[28:31], v201, s[8:11], s43 offen offset:1024 sc1\n" \
"v_readlane_b32 s43, v202, 10\n" \
"s_nop 4\n" \
"buffer_load_dwordx4 a[80:83], v201, s[8:11], s43 offen sc1\n" \
"buffer_load_dwordx4 a[84:87], v201, s[8:11], s43 offen offset:1024 sc1\n" \
"v_readlane_b32 s43, v202, 17\n" \
"s_nop 4\n" \
"buffer_load_dwordx4 a[136:139], v201, s[8:11], s43 offen sc1\n" \
"buffer_load_dwordx4 a[140:143], v201, s[8:11], s43 offen offset:1024 sc1\n" \
"v_readlane_b32 s43, v202, 24\n" \
"s_nop 4\n" \
"buffer_load_dwordx4 a[192:195], v201, s[8:11], s43 offen sc1\n" \
"buffer_load_dwordx4 a[196:199], v201, s[8:11], s43 offen offset:1024 sc1\n" \
"s_add_u32 s43, s30, 1\n" \
"s_mov_b32 s50, 0\n" \
".LP1_w41l:\n" \
"buffer_load_dword v240, v205, s[4:7], 0 offen sc1\n" \
"s_waitcnt vmcnt(0)\n" \
".LP1_w41c:\n" \
"v_subrev_u32 v241, s43, v240\n" \
"v_cmp_gt_u32 vcc, 0x100, v241\n" \
"s_cmp_eq_u64 vcc, -1\n" \
"s_cbranch_scc1 .LP1_w41d\n" \
"s_sleep 2\n" \
"s_add_u32 s50, s50, 1\n" \
"s_cmp_lt_u32 s50, 0x20000\n" \
"s_cbranch_scc1 .LP1_w41l\n" \
"s_branch .LP1_end\n" \
".LP1_w41d:\n" \
"s_add_u32 s43, s30, 1\n" \
"s_mov_b32 s50, 0\n" \
".LP1_w42l:\n" \
"buffer_load_dword v240, v206, s[4:7], 0 offen sc1\n" \
"s_waitcnt vmcnt(0)\n" \
".LP1_w42c:\n" \
"v_subrev_u32 v241, s43, v240\n" \
"v_cmp_gt_u32 vcc, 0x100, v241\n" \
"s_cmp_eq_u64 vcc, -1\n" \
"s_cbranch_scc1 .LP1_w42d\n" \
"s_sleep 2\n" \
"s_add_u32 s50, s50, 1\n" \
"s_cmp_lt_u32 s50, 0x20000\n" \
"s_cbranch_scc1 .LP1_w42l\n" \
"s_branch .LP1_end\n" \
".LP1_w42d:\n" \
".LP1_pre:\n" \
"s_add_u32 s43, s40, 0\n" \
"s_and_b32 s43, s43, 3\n" \
"s_bitcmp1_b32 s43, 0\n" \
"s_cselect_b32 s47, s32, s31\n" \
"s_lshl_b32 s43, s43, 19\n" \
"s_add_u32 s47, s47, s43\n" \
"buffer_store_dword v219, v213, s[4:7], s47 offen sc1\n" \
"s_xor_b32 s43, s47, s29\n" \
"buffer_store_dword v219, v213, s[4:7], s43 offen sc1\n" \
"s_lshl_b32 s43, s40, 7\n" \
"buffer_load_dwordx4 v[236:239], v207, s[4:7], s43 offen sc1\n" \
"s_waitcnt vmcnt(0)\n" \
"s_barrier\n" \
"ds_read_b96 v[248:250], v252\n" \
"s_waitcnt lgkmcnt(0)\n" \
"v_readfirstlane_b32 s44, v248\n" \
"v_readfirstlane_b32 s45, v249\n" \
"v_readfirstlane_b32 s46, v250\n" \
"s_mul_i32 s42, s25, s46\n" \
"s_lshr_b32 s42, s42, 12\n" \
"s_add_u32 s43, s40, 1\n" \
"s_and_b32 s43, s43, 3\n" \
"s_bitcmp1_b32 s43, 0\n" \
"s_cselect_b32 s48, s32, s31\n" \
"s_lshl_b32 s43, s43, 19\n" \
"s_add_u32 s48, s48, s43\n" \
"s_add_u32 s43, s40, 2\n" \
"s_and_b32 s43, s43, 3\n" \
"s_bitcmp1_b32 s43, 0\n" \
"s_cselect_b32 s49, s32, s31\n" \
"s_lshl_b32 s43, s43, 19\n" \
"s_add_u32 s49, s49, s43\n" \
"s_sub_u32 s43, s24, s44\n" \
"s_add_u32 s43, s43, s28\n" \
"s_max_i32 s43, s43, 0xfffd0000\n" \
"s_min_i32 s43, s43, 0x4000\n" \
"s_mul_i32 s43, s43, s46\n" \
"s_ashr_i32 s43, s43, 12\n" \
"s_add_u32 s41, s45, s43\n" \
"s_lshr_b32 s35, s42, 1\n" \
"s_sub_u32 s35, 0, s35\n" \
"v_max_u32 v236, v236, v238\n" \
"v_max_u32 v237, v237, v239\n" \
"v_and_b32 v236, 0xfff, v236\n" \
"v_and_b32 v237, 0xfff, v237\n" \
"s_mov_b64 exec, 1\n" \
"ds_write_b64 v252, v[236:237] offset:24\n" \
"s_mov_b64 exec, -1\n" \
".LP1_loop:\n" \
"s_branch .LP1_m1\n" \
".LP1_retry1:\n" \
"s_add_u32 s51, s51, 1\n" \
"s_cmp_gt_u32 s51, 0x20000\n" \
"s_cbranch_scc1 .LP1_end\n" \
".LP1_m0:\n" \
"s_add_u32 s41, s41, s42\n" \
"s_add_u32 s24, s24, s25\n" \
"s_xor_b32 s31, s31, s29\n" \
"s_xor_b32 s32, s32, s29\n" \
"s_xor_b32 s47, s47, s29\n" \
"s_xor_b32 s48, s48, s29\n" \
"s_add_u32 s39, s39, 1\n" \
"s_bitcmp1_b32 s31, 21\n" \
"s_cselect_b32 s43, s29, 0\n" \
"v_mov_b32 v116, s43\n" \
"ds_write_b32 v252, v116 offset:16\n" \
".LP1_m1:\n" \
"s_memtime vcc\n" \
"s_waitcnt lgkmcnt(0)\n" \
"s_sub_u32 s43, s41, vcc_lo\n" \
"s_cmp_gt_i32 s43, 0\n" \
"s_cbranch_scc1 .LP1_m1\n" \
"s_cmp_lt_i32 s43, s35\n" \
"s_cbranch_scc1 .LP1_m0\n" \
"buffer_load_dwordx4 v[0:3], v208, s[4:7], s47 offen offset:2048 sc1\n" \
"buffer_load_dwordx4 v[4:7], v209, s[4:7], s47 offen sc1\n" \
"s_nop 15\n" \
"s_nop 13\n" \
"s_mul_i32 s50, s40, 114688\n" \
"buffer_store_dwordx4 v[68:71], v218, s[12:15], s50 offen sc1\n" \
"buffer_store_dword v112, v213, s[4:7], s49 offen sc1\n" \
"s_xor_b32 s43, s49, s29\n" \
"buffer_store_dword v112, v213, s[4:7], s43 offen sc1\n" \
"s_lshl_b32 s43, s40, 7\n" \
"buffer_load_dwordx4 v[236:239], v207, s[4:7], s43 offen offset:128 sc1\n" \
"v_mov_b32 v28, 0\n" \
"v_mov_b32 v29, 0\n" \
"v_mov_b32 v30, 0\n" \
"v_mov_b32 v31, 0\n" \
"v_mov_b32 v32, 0\n" \
"v_mov_b32 v33, 0\n" \
"v_mov_b32 v34, 0\n" \
"v_mov_b32 v35, 0\n" \
"v_mov_b32 v36, 0\n" \
"v_mov_b32 v37, 0\n" \
"v_mov_b32 v38, 0\n" \
"v_mov_b32 v39, 0\n" \
"v_mov_b32 v40, 0\n" \
"v_mov_b32 v41, 0\n" \
"v_mov_b32 v42, 0\n" \
"v_mov_b32 v43, 0\n" \
"s_waitcnt vmcnt(5)\n" \
"ds_write_b128 v220, v[0:3] offset:2048\n" \
"s_waitcnt vmcnt(4)\n" \
"ds_write_b128 v220, v[4:7] offset:4096\n" \
"v_max3_u32 v44, v0, v0, v1\n" \
"v_max3_u32 v44, v44, v2, v3\n" \
"v_max3_u32 v44, v44, v4, v5\n" \
"v_max3_u32 v44, v44, v6, v7\n" \
"v_cmp_eq_u32 vcc, -1, v44\n" \
"v_smfmac_f32_16x16x64_bf16 v[28:31], v[0:3], a[0:7], v200\n" \
"v_smfmac_f32_16x16x64_bf16 v[32:35], v[0:3], a[56:63], v200\n" \
"s_cbranch_vccnz .LP1_retry1\n" \
"s_waitcnt lgkmcnt(0)\n" \
"s_barrier\n" \
"ds_read_b128 v[8:11], v220 offset:5120\n" \
"ds_read_b128 v[12:15], v220 offset:6144\n" \
"ds_read_b128 v[16:19], v220 offset:0\n" \
"ds_read_b128 v[20:23], v220 offset:1024\n" \
"ds_read_b128 v[24:27], v220 offset:3072\n" \
"ds_read_b64 v[246:247], v252 offset:24\n" \
"ds_read_b96 v[248:250], v252\n" \
"v_smfmac_f32_16x16x64_bf16 v[36:39], v[0:3], a[112:119], v200\n" \
"v_smfmac_f32_16x16x64_bf16 v[40:43], v[0:3], a[168:175], v200\n" \
"v_smfmac_f32_16x16x64_bf16 v[28:31], v[4:7], a[8:15], v200\n" \
"v_smfmac_f32_16x16x64_bf16 v[32:35], v[4:7], a[64:71], v200\n" \
"v_smfmac_f32_16x16x64_bf16 v[36:39], v[4:7], a[120:127], v200\n" \
"v_smfmac_f32_16x16x64_bf16 v[40:43], v[4:7], a[176:183], v200\n" \
"s_waitcnt lgkmcnt(6)\n" \
"v_smfmac_f32_16x16x64_bf16 v[28:31], v[8:11], a[16:23], v200\n" \
"v_smfmac_f32_16x16x64_bf16 v[32:35], v[8:11], a[72:79], v200\n" \
"v_smfmac_f32_16x16x64_bf16 v[36:39], v[8:11], a[128:135], v200\n" \
"v_smfmac_f32_16x16x64_bf16 v[40:43], v[8:11], a[184:191], v200\n" \
"s_waitcnt lgkmcnt(0)\n" \
"v_readfirstlane_b32 s44, v248\n" \
"v_readfirstlane_b32 s45, v249\n" \
"v_smfmac_f32_16x16x64_bf16 v[28:31], v[12:15], a[24:31], v200\n" \
"v_readfirstlane_b32 s46, v250\n" \
"v_readfirstlane_b32 s26, v246\n" \
"v_readfirstlane_b32 s27, v247\n" \
"v_smfmac_f32_16x16x64_bf16 v[32:35], v[12:15], a[80:87], v200\n" \
"s_mul_i32 s42, s25, s46\n" \
"s_lshr_b32 s42, s42, 12\n" \
"s_add_u32 s24, s24, s26\n" \
"v_smfmac_f32_16x16x64_bf16 v[36:39], v[12:15], a[136:143], v200\n" \
"s_sub_u32 s43, s24, s44\n" \
"s_max_i32 s43, s43, 0xfffd0000\n" \
"s_min_i32 s43, s43, 0x4000\n" \
"v_smfmac_f32_16x16x64_bf16 v[40:43], v[12:15], a[192:199], v200\n" \
"s_mul_i32 s43, s43, s46\n" \
"s_ashr_i32 s43, s43, 12\n" \
"s_add_u32 s41, s45, s43\n" \
"s_cmp_eq_u32 s40, 63\n" \
"s_cbranch_scc1 .LP1_last\n" \
"s_branch .LP1_z1\n" \
".LP1_z0:\n" \
"s_add_u32 s41, s41, s42\n" \
"s_add_u32 s24, s24, s25\n" \
"s_xor_b32 s31, s31, s29\n" \
"s_xor_b32 s32, s32, s29\n" \
"s_xor_b32 s47, s47, s29\n" \
"s_xor_b32 s48, s48, s29\n" \
"s_add_u32 s39, s39, 1\n" \
"buffer_store_dwordx4 v[112:115], v223, s[4:7], s48 offen sc1\n" \
"s_bitcmp1_b32 s31, 21\n" \
"s_cselect_b32 s43, s29, 0\n" \
"v_mov_b32 v116, s43\n" \
"ds_write_b32 v252, v116 offset:16\n" \
".LP1_z1:\n" \
"s_memtime vcc\n" \
"s_waitcnt lgkmcnt(0)\n" \
"s_sub_u32 s43, s41, vcc_lo\n" \
"s_cmp_gt_i32 s43, 200\n" \
"s_cbranch_scc1 .LP1_z1\n" \
"s_cmp_lt_i32 s43, s37\n" \
"s_cbranch_scc1 .LP1_z0\n" \
"buffer_load_dwordx2 v[64:65], v211, s[4:7], s47 offen sc1\n" \
".p2align 6\n" \
".LP1_b1:\n" \
"s_memtime vcc\n" \
"s_waitcnt lgkmcnt(0)\n" \
"s_sub_u32 s43, s41, vcc_lo\n" \
"s_cmp_gt_i32 s43, 128\n" \
"s_cbranch_scc1 .LP1_b1\n" \
"s_cmp_lt_i32 s43, s37\n" \
"s_cbranch_scc1 .LP1_z0\n" \
"s_max_i32 s43, s43, 0\n" \
"s_and_b32 s43, s43, -4\n" \
"s_getpc_b64 vcc\n" \
".LP1_b3:\n" \
"s_sub_u32 s43, .LP1_b4-.LP1_b3, s43\n" \
"s_add_u32 vcc_lo, vcc_lo, s43\n" \
"s_addc_u32 vcc_hi, vcc_hi, 0\n" \
"s_setpc_b64 vcc\n" \
".rept 32\n" \
"s_nop 0\n" \
".endr\n" \
".LP1_b4:\n" \
"buffer_load_dwordx4 v[56:59], v210, s[4:7], s47 offen sc1\n" \
"buffer_load_dwordx4 v[60:63], v210, s[4:7], s47 offen offset:256 sc1\n" \
"v_smfmac_f32_16x16x64_bf16 v[28:31], v[16:19], a[32:39], v200\n" \
"v_smfmac_f32_16x16x64_bf16 v[32:35], v[16:19], a[88:95], v200\n" \
"v_smfmac_f32_16x16x64_bf16 v[36:39], v[16:19], a[144:151], v200\n" \
"v_smfmac_f32_16x16x64_bf16 v[40:43], v[16:19], a[200:207], v200\n" \
"v_smfmac_f32_16x16x64_bf16 v[28:31], v[20:23], a[40:47], v200\n" \
"v_smfmac_f32_16x16x64_bf16 v[32:35], v[20:23], a[96:103], v200\n" \
"buffer_load_dwordx4 v[50:53], v212, s[4:7], s47 offen sc1\n" \
"v_smfmac_f32_16x16x64_bf16 v[36:39], v[20:23], a[152:159], v200\n" \
"v_smfmac_f32_16x16x64_bf16 v[40:43], v[20:23], a[208:215], v200\n" \
"v_smfmac_f32_16x16x64_bf16 v[28:31], v[24:27], a[48:55], v200\n" \
"v_smfmac_f32_16x16x64_bf16 v[32:35], v[24:27], a[104:111], v200\n" \
"v_smfmac_f32_16x16x64_bf16 v[36:39], v[24:27], a[160:167], v200\n" \
"v_smfmac_f32_16x16x64_bf16 v[40:43], v[24:27], a[216:223], v200\n" \
"s_nop 2\n" \
"v_mov_b32 v245, v245\n" \
"v_mov_b32 v245, v245\n" \
"v_mov_b32 v245, v245\n" \
"v_permlane32_swap_b32 v28, v32\n" \
"v_permlane32_swap_b32 v29, v33\n" \
"v_permlane32_swap_b32 v30, v34\n" \
"v_permlane32_swap_b32 v31, v35\n" \
"v_add_f32 v28, v28, v32\n" \
"v_add_f32 v29, v29, v33\n" \
"v_add_f32 v30, v30, v34\n" \
"v_add_f32 v31, v31, v35\n" \
"v_cvt_pk_bf16_f32 v68, v28, v29\n" \
"v_cvt_pk_bf16_f32 v69, v30, v31\n" \
"v_permlane32_swap_b32 v36, v40\n" \
"v_permlane32_swap_b32 v37, v41\n" \
"v_permlane32_swap_b32 v38, v42\n" \
"v_permlane32_swap_b32 v39, v43\n" \
"v_add_f32 v36, v36, v40\n" \
"v_add_f32 v37, v37, v41\n" \
"v_add_f32 v38, v38, v42\n" \
"v_add_f32 v39, v39, v43\n" \
"v_cvt_pk_bf16_f32 v70, v36, v37\n" \
"v_cvt_pk_bf16_f32 v71, v38, v39\n" \
"v_mov_b32 v48, v68\n" \
"v_mov_b32 v49, v69\n" \
"v_mov_b32 v54, v70\n" \
"v_mov_b32 v55, v71\n" \
"s_nop 1\n" \
"v_permlane16_swap_b32 v48, v54\n" \
"v_permlane16_swap_b32 v49, v55\n" \
".LP1_wait:\n" \
"s_waitcnt vmcnt(1)\n" \
"v_pk_add_f32 v[72:73], v[56:57], v[60:61]\n" \
"v_pk_add_f32 v[74:75], v[58:59], v[62:63]\n" \
"v_max3_f32 v77, v72, v73, v64\n" \
"v_max3_f32 v77, v77, v74, v75\n" \
"s_nop 1\n" \
"v_max_f32_dpp v76, v77, v77 row_ror:8 row_mask:0xf bank_mask:0xf\n" \
"v_pk_add_f32 v[72:73], v[72:73], v[76:77] op_sel_hi:[1,0] neg_lo:[0,1] neg_hi:[0,1]\n" \
"v_pk_add_f32 v[74:75], v[74:75], v[76:77] op_sel_hi:[1,0] neg_lo:[0,1] neg_hi:[0,1]\n" \
"v_sub_f32 v78, v64, v76\n" \
"v_exp_f32 v72, v72\n" \
"v_exp_f32 v73, v73\n" \
"v_exp_f32 v74, v74\n" \
"v_exp_f32 v75, v75\n" \
"v_exp_f32 v78, v78\n" \
"v_pk_add_f32 v[80:81], v[72:73], v[74:75]\n" \
"v_fma_f32 v80, v78, v65, v80\n" \
"v_add_f32 v81, v80, v81\n" \
"s_nop 1\n" \
"v_add_f32_dpp v80, v81, v81 row_ror:8 row_mask:0xf bank_mask:0xf\n" \
"v_rcp_f32 v80, v80\n" \
"s_nop 0\n" \
"v_mul_f32 v78, v78, v80\n" \
"v_pk_mul_f32 v[72:73], v[72:73], v[80:81] op_sel_hi:[1,0]\n" \
"v_pk_mul_f32 v[74:75], v[74:75], v[80:81] op_sel_hi:[1,0]\n" \
"v_cvt_pk_bf16_f32 v78, v78, v78\n" \
"v_cvt_pk_bf16_f32 v82, v72, v73\n" \
"v_cvt_pk_bf16_f32 v83, v74, v75\n" \
"v_and_b32 v84, v78, v214\n" \
"v_and_b32 v85, v78, v215\n" \
"v_cvt_pk_bf16_f32 v86, v72, v73\n" \
"v_cvt_pk_bf16_f32 v87, v74, v75\n" \
"s_waitcnt vmcnt(0)\n" \
"v_mfma_f32_16x16x32_bf16 v[88:91], v[48:51], v[82:85], 0\n" \
"v_mfma_f32_16x16x32_bf16 v[92:95], v[52:55], v[84:87], 0\n" \
"s_nop 2\n" \
"v_mov_b32 v245, v245\n" \
"s_nop 2\n" \
"v_mov_b32 v245, v245\n" \
"v_add_f32_dpp v96, v92, v88 row_ror:8 row_mask:0xf bank_mask:0x3\n" \
"v_add_f32_dpp v97, v93, v89 row_ror:8 row_mask:0xf bank_mask:0x3\n" \
"v_add_f32_dpp v96, v90, v94 row_ror:8 row_mask:0xf bank_mask:0xc\n" \
"v_add_f32_dpp v97, v91, v95 row_ror:8 row_mask:0xf bank_mask:0xc\n" \
"ds_write_b64 v216, v[96:97]\n" \
"v_cmp_u_f32 vcc, v96, v97\n" \
"s_bitcmp1_b32 s31, 21\n" \
"s_cselect_b32 s18, s29, 0\n" \
"s_xor_b32 s18, s18, s48\n" \
"v_mov_b32 v245, v245\n" \
"s_xor_b32 s19, s18, s29\n" \
"s_nop 0\n" \
"s_nop 0\n" \
"v_mov_b32 v245, v245\n" \
"s_nop 0\n" \
"s_nop 0\n" \
"s_nop 0\n" \
"v_mov_b32 v245, v245\n" \
"s_cbranch_vccnz .LP1_cls\n" \
".LP1_clsr:\n" \
"s_waitcnt lgkmcnt(0)\n" \
"s_barrier\n" \
"ds_read_b64 v[100:101], v217 offset:0\n" \
"ds_read_b64 v[102:103], v217 offset:512\n" \
"ds_read_b32 v116, v252 offset:16\n" \
"ds_read_b64 v[104:105], v217 offset:1024\n" \
"ds_read_b64 v[106:107], v217 offset:1536\n" \
"v_mov_b32 v245, v245\n" \
"v_mov_b32 v245, v245\n" \
"s_add_u32 s40, s40, 1\n" \
"s_add_u32 s24, s24, s27\n" \
"s_mov_b32 s47, s48\n" \
"v_mov_b32 v245, v245\n" \
"s_waitcnt lgkmcnt(2)\n" \
"v_xad_u32 v117, v116, s18, v213\n" \
"v_pk_add_f32 v[100:101], v[100:101], v[102:103]\n" \
"s_waitcnt lgkmcnt(0)\n" \
"v_pk_add_f32 v[104:105], v[104:105], v[106:107]\n" \
"v_pk_add_f32 v[100:101], v[100:101], v[104:105]\n" \
"v_cvt_pk_bf16_f32 v96, v100, v101\n" \
"s_add_u32 s43, s40, 1\n" \
"s_and_b32 s43, s43, 3\n" \
"s_bitcmp1_b32 s43, 0\n" \
"s_cselect_b32 s48, s32, s31\n" \
"s_lshl_b32 s43, s43, 19\n" \
"s_add_u32 s48, s48, s43\n" \
"s_add_u32 s43, s40, 2\n" \
"s_and_b32 s43, s43, 3\n" \
"buffer_store_dword v96, v117, s[4:7], 0 offen sc1\n" \
"s_bitcmp1_b32 s43, 0\n" \
"s_cselect_b32 s49, s32, s31\n" \
"s_lshl_b32 s43, s43, 19\n" \
"s_add_u32 s49, s49, s43\n" \
"s_sub_u32 s43, s24, s44\n" \
"s_add_u32 s43, s43, s28\n" \
"s_max_i32 s43, s43, 0xfffd0000\n" \
"s_min_i32 s43, s43, 0x4000\n" \
"s_mul_i32 s43, s43, s46\n" \
"s_ashr_i32 s43, s43, 12\n" \
"s_add_u32 s41, s45, s43\n" \
"s_lshr_b32 s35, s42, 1\n" \
"s_sub_u32 s35, 0, s35\n" \
"v_max_u32 v236, v236, v238\n" \
"v_max_u32 v237, v237, v239\n" \
"v_and_b32 v236, 0xfff, v236\n" \
"v_and_b32 v237, 0xfff, v237\n" \
"s_mov_b64 exec, 1\n" \
"ds_write_b64 v252, v[236:237] offset:24\n" \
"s_mov_b64 exec, -1\n" \
"v_xad_u32 v116, v116, s19, v213\n" \
"buffer_store_dword v96, v116, s[4:7], 0 offen sc1\n" \
"s_cmp_eq_u32 s40, 0\n" \
"s_cbranch_scc1 .LP1_wet\n" \
"s_branch .LP1_loop\n" \
"s_branch .LP1_y1\n" \
".LP1_retry2:\n" \
"s_add_u32 s51, s51, 1\n" \
"s_cmp_gt_u32 s51, 0x20000\n" \
"s_cbranch_scc1 .LP1_end\n" \
".LP1_y0:\n" \
"s_add_u32 s41, s41, s42\n" \
"s_add_u32 s24, s24, s25\n" \
"s_xor_b32 s31, s31, s29\n" \
"s_xor_b32 s32, s32, s29\n" \
"s_xor_b32 s47, s47, s29\n" \
"s_xor_b32 s48, s48, s29\n" \
"s_add_u32 s39, s39, 1\n" \
"buffer_store_dwordx4 v[112:115], v223, s[4:7], s48 offen sc1\n" \
"s_bitcmp1_b32 s31, 21\n" \
"s_cselect_b32 s43, s29, 0\n" \
"v_mov_b32 v116, s43\n" \
"ds_write_b32 v252, v116 offset:16\n" \
".LP1_y1:\n" \
"s_memtime vcc\n" \
"s_waitcnt lgkmcnt(0)\n" \
"s_sub_u32 s43, s41, vcc_lo\n" \
"s_cmp_gt_i32 s43, 200\n" \
"s_cbranch_scc1 .LP1_y1\n" \
"s_cmp_lt_i32 s43, s37\n" \
"s_cbranch_scc1 .LP1_y0\n" \
"buffer_load_dwordx2 v[64:65], v211, s[4:7], s47 offen sc1\n" \
".p2align 6\n" \
".LP1_c1:\n" \
"s_memtime vcc\n" \
"s_waitcnt lgkmcnt(0)\n" \
"s_sub_u32 s43, s41, vcc_lo\n" \
"s_cmp_gt_i32 s43, 128\n" \
"s_cbranch_scc1 .LP1_c1\n" \
"s_cmp_lt_i32 s43, s37\n" \
"s_cbranch_scc1 .LP1_y0\n" \
"s_max_i32 s43, s43, 0\n" \
"s_and_b32 s43, s43, -4\n" \
"s_getpc_b64 vcc\n" \
".LP1_c3:\n" \
"s_sub_u32 s43, .LP1_c4-.LP1_c3, s43\n" \
"s_add_u32 vcc_lo, vcc_lo, s43\n" \
"s_addc_u32 vcc_hi, vcc_hi, 0\n" \
"s_setpc_b64 vcc\n" \
".rept 32\n" \
"s_nop 0\n" \
".endr\n" \
".LP1_c4:\n" \
"buffer_load_dwordx4 v[56:59], v210, s[4:7], s47 offen sc1\n" \
"buffer_load_dwordx4 v[60:63], v210, s[4:7], s47 offen offset:256 sc1\n" \
"s_nop 2\n" \
"v_mov_b32 v245, v245\n" \
"s_nop 2\n" \
"v_mov_b32 v245, v245\n" \
"s_nop 2\n" \
"v_mov_b32 v245, v245\n" \
"s_nop 2\n" \
"v_mov_b32 v245, v245\n" \
"s_nop 2\n" \
"v_mov_b32 v245, v245\n" \
"s_nop 2\n" \
"v_mov_b32 v245, v245\n" \
"buffer_load_dwordx4 v[50:53], v212, s[4:7], s47 offen sc1\n" \
"s_nop 2\n" \
"v_mov_b32 v245, v245\n" \
"s_nop 2\n" \
"v_mov_b32 v245, v245\n" \
"s_nop 2\n" \
"v_mov_b32 v245, v245\n" \
"s_nop 2\n" \
"v_mov_b32 v245, v245\n" \
"s_nop 2\n" \
"v_mov_b32 v245, v245\n" \
"s_nop 2\n" \
"v_mov_b32 v245, v245\n" \
"s_nop 2\n" \
"v_mov_b32 v245, v245\n" \
"s_nop 2\n" \
"v_mov_b32 v245, v245\n" \
"s_nop 2\n" \
"v_mov_b32 v245, v245\n" \
"s_nop 2\n" \
"v_mov_b32 v245, v245\n" \
"s_nop 2\n" \
"v_mov_b32 v245, v245\n" \
"s_nop 2\n" \
"v_mov_b32 v245, v245\n" \
"s_nop 2\n" \
"v_mov_b32 v245, v245\n" \
"s_nop 2\n" \
"v_mov_b32 v245, v245\n" \
"s_nop 2\n" \
"v_mov_b32 v245, v245\n" \
"s_nop 2\n" \
"v_mov_b32 v245, v245\n" \
"s_nop 2\n" \
"v_mov_b32 v245, v245\n" \
"s_branch .LP1_wait\n" \
".LP1_cls:\n" \
"s_waitcnt vmcnt(0)\n" \
"v_max3_u32 v44, v64, v64, v65\n" \
"v_max3_u32 v44, v44, v50, v51\n" \
"v_max3_u32 v44, v44, v52, v53\n" \
"v_cmp_eq_u32 vcc, -1, v44\n" \
"s_cbranch_vccnz .LP1_retry2\n" \
"v_cmp_u_f32 vcc, v56, v56\n" \
"v_cmp_ne_u32 s[2:3], v56, v109\n" \
"s_and_b64 vcc, vcc, s[2:3]\n" \
"s_cbranch_vccnz .LP1_retry2\n" \
"v_cmp_u_f32 vcc, v57, v57\n" \
"v_cmp_ne_u32 s[2:3], v57, v109\n" \
"s_and_b64 vcc, vcc, s[2:3]\n" \
"s_cbranch_vccnz .LP1_retry2\n" \
"v_cmp_u_f32 vcc, v58, v58\n" \
"v_cmp_ne_u32 s[2:3], v58, v109\n" \
"s_and_b64 vcc, vcc, s[2:3]\n" \
"s_cbranch_vccnz .LP1_retry2\n" \
"v_cmp_u_f32 vcc, v59, v59\n" \
"v_cmp_ne_u32 s[2:3], v59, v109\n" \
"s_and_b64 vcc, vcc, s[2:3]\n" \
"s_cbranch_vccnz .LP1_retry2\n" \
"v_cmp_u_f32 vcc, v60, v60\n" \
"v_cmp_ne_u32 s[2:3], v60, v109\n" \
"s_and_b64 vcc, vcc, s[2:3]\n" \
"s_cbranch_vccnz .LP1_retry2\n" \
"v_cmp_u_f32 vcc, v61, v61\n" \
"v_cmp_ne_u32 s[2:3], v61, v109\n" \
"s_and_b64 vcc, vcc, s[2:3]\n" \
"s_cbranch_vccnz .LP1_retry2\n" \
"v_cmp_u_f32 vcc, v62, v62\n" \
"v_cmp_ne_u32 s[2:3], v62, v109\n" \
"s_and_b64 vcc, vcc, s[2:3]\n" \
"s_cbranch_vccnz .LP1_retry2\n" \
"v_cmp_u_f32 vcc, v63, v63\n" \
"v_cmp_ne_u32 s[2:3], v63, v109\n" \
"s_and_b64 vcc, vcc, s[2:3]\n" \
"s_cbranch_vccnz .LP1_retry2\n" \
"s_branch .LP1_clsr\n" \
".LP1_last:\n" \
"v_smfmac_f32_16x16x64_bf16 v[28:31], v[16:19], a[32:39], v200\n" \
"v_smfmac_f32_16x16x64_bf16 v[32:35], v[16:19], a[88:95], v200\n" \
"v_smfmac_f32_16x16x64_bf16 v[36:39], v[16:19], a[144:151], v200\n" \
"v_smfmac_f32_16x16x64_bf16 v[40:43], v[16:19], a[200:207], v200\n" \
"v_smfmac_f32_16x16x64_bf16 v[28:31], v[20:23], a[40:47], v200\n" \
"v_smfmac_f32_16x16x64_bf16 v[32:35], v[20:23], a[96:103], v200\n" \
"v_smfmac_f32_16x16x64_bf16 v[36:39], v[20:23], a[152:159], v200\n" \
"v_smfmac_f32_16x16x64_bf16 v[40:43], v[20:23], a[208:215], v200\n" \
"v_smfmac_f32_16x16x64_bf16 v[28:31], v[24:27], a[48:55], v200\n" \
"v_smfmac_f32_16x16x64_bf16 v[32:35], v[24:27], a[104:111], v200\n" \
"v_smfmac_f32_16x16x64_bf16 v[36:39], v[24:27], a[160:167], v200\n" \
"v_smfmac_f32_16x16x64_bf16 v[40:43], v[24:27], a[216:223], v200\n" \
"s_waitcnt vmcnt(0)\n" \
"s_nop 7\n" \
"v_permlane32_swap_b32 v28, v32\n" \
"v_permlane32_swap_b32 v29, v33\n" \
"v_permlane32_swap_b32 v30, v34\n" \
"v_permlane32_swap_b32 v31, v35\n" \
"v_add_f32 v28, v28, v32\n" \
"v_add_f32 v29, v29, v33\n" \
"v_add_f32 v30, v30, v34\n" \
"v_add_f32 v31, v31, v35\n" \
"v_cvt_pk_bf16_f32 v68, v28, v29\n" \
"v_cvt_pk_bf16_f32 v69, v30, v31\n" \
"v_permlane32_swap_b32 v36, v40\n" \
"v_permlane32_swap_b32 v37, v41\n" \
"v_permlane32_swap_b32 v38, v42\n" \
"v_permlane32_swap_b32 v39, v43\n" \
"v_add_f32 v36, v36, v40\n" \
"v_add_f32 v37, v37, v41\n" \
"v_add_f32 v38, v38, v42\n" \
"v_add_f32 v39, v39, v43\n" \
"v_cvt_pk_bf16_f32 v70, v36, v37\n" \
"v_cvt_pk_bf16_f32 v71, v38, v39\n" \
"s_mov_b32 s50, 7340032\n" \
"buffer_store_dwordx4 v[68:71], v218, s[12:15], s50 offen sc1\n" \
"s_waitcnt vmcnt(0)\n" \
"s_barrier\n" \
"v_mov_b32 v241, s30\n" \
"buffer_store_dword v241, v222, s[4:7], 0 offen sc1\n" \
".LP1_end:\n" \
"s_endpgm\n" \
".p2align 6\n" \
"fk_role_P2:\n" \
"ds_read_b32 v200, v255 offset:0\n" \
"ds_read_b32 v201, v255 offset:256\n" \
"ds_read_b32 v202, v255 offset:512\n" \
"ds_read_b32 v203, v255 offset:768\n" \
"ds_read_b32 v204, v255 offset:1024\n" \
"ds_read_b32 v205, v255 offset:1280\n" \
"ds_read_b32 v206, v255 offset:1536\n" \
"ds_read_b32 v207, v255 offset:1792\n" \
"ds_read_b32 v208, v255 offset:2048\n" \
"ds_read_b32 v209, v255 offset:2304\n" \
"ds_read_b32 v210, v255 offset:2560\n" \
"ds_read_b32 v211, v255 offset:2816\n" \
"ds_read_b32 v212, v255 offset:3072\n" \
"ds_read_b32 v213, v255 offset:3328\n" \
"ds_read_b32 v214, v255 offset:3584\n" \
"ds_read_b32 v215, v255 offset:3840\n" \
"ds_read_b32 v216, v255 offset:4096\n" \
"ds_read_b32 v217, v255 offset:4352\n" \
"ds_read_b32 v218, v255 offset:4608\n" \
"ds_read_b32 v219, v255 offset:4864\n" \
"ds_read_b32 v220, v255 offset:5120\n" \
"ds_read_b32 v221, v255 offset:5376\n" \
"ds_read_b32 v222, v255 offset:5632\n" \
"ds_read_b32 v223, v255 offset:5888\n" \
"ds_read_b32 v254, v255 offset:7936\n" \
"s_waitcnt lgkmcnt(0)\n" \
"v_readlane_b32 s4, v254, 0\n" \
"v_readlane_b32 s5, v254, 1\n" \
"v_readlane_b32 s6, v254, 2\n" \
"v_readlane_b32 s7, v254, 3\n" \
"v_readlane_b32 s8, v254, 4\n" \
"v_readlane_b32 s9, v254, 5\n" \
"v_readlane_b32 s10, v254, 6\n" \
"v_readlane_b32 s11, v254, 7\n" \
"v_readlane_b32 s12, v254, 8\n" \
"v_readlane_b32 s13, v254, 9\n" \
"v_readlane_b32 s14, v254, 10\n" \
"v_readlane_b32 s15, v254, 11\n" \
"v_readlane_b32 s16, v254, 12\n" \
"v_readlane_b32 s17, v254, 13\n" \
"v_readlane_b32 s18, v254, 14\n" \
"v_readlane_b32 s19, v254, 15\n" \
"v_readlane_b32 s20, v254, 16\n" \
"v_readlane_b32 s21, v254, 17\n" \
"v_readlane_b32 s22, v254, 18\n" \
"v_readlane_b32 s23, v254, 19\n" \
"v_readlane_b32 s24, v254, 20\n" \
"v_readlane_b32 s25, v254, 21\n" \
"v_readlane_b32 s26, v254, 22\n" \
"v_readlane_b32 s27, v254, 23\n" \
"v_readlane_b32 s28, v254, 24\n" \
"v_readlane_b32 s29, v254, 25\n" \
"v_readlane_b32 s30, v254, 26\n" \
"v_readlane_b32 s31, v254, 27\n" \
"v_readlane_b32 s32, v254, 28\n" \
"v_readlane_b32 s33, v254, 29\n" \
"v_readlane_b32 s34, v254, 30\n" \
"v_readlane_b32 s35, v254, 31\n" \
"v_readlane_b32 s36, v254, 32\n" \
"v_readlane_b32 s37, v254, 33\n" \
"v_readlane_b32 s38, v254, 34\n" \
"v_readlane_b32 s39, v254, 35\n" \
"s_mov_b32 s40, 0\n" \
"s_mov_b32 s51, 0\n" \
"s_nop 0\n" \
"v_mov_b32 v252, s38\n" \
"s_nop 4\n" \
"v_mov_b32 v112, -1\n" \
"v_mov_b32 v113, -1\n" \
"v_mov_b32 v114, -1\n" \
"v_mov_b32 v115, -1\n" \
"v_mov_b32 v109, 0x7fc00001\n" \
"v_writelane_b32 v251, s6, 0\n" \
"v_writelane_b32 v251, s10, 1\n" \
"v_writelane_b32 v251, s14, 2\n" \
"v_writelane_b32 v251, s18, 3\n" \
"s_mov_b32 s6, 0\n" \
"s_mov_b32 s10, 0\n" \
"s_mov_b32 s14, 0\n" \
"s_mov_b32 s18, 0\n" \
"s_mov_b32 s40, -1\n" \
"s_branch .LP2_pre\n" \
".LP2_wet:\n" \
"s_waitcnt vmcnt(0)\n" \
"v_readlane_b32 s6, v251, 0\n" \
"v_readlane_b32 s10, v251, 1\n" \
"v_readlane_b32 s14, v251, 2\n" \
"v_readlane_b32 s18, v251, 3\n" \
"s_nop 4\n" \
"s_mov_b32 s33, 0\n" \
"s_mov_b32 s34, 0\n" \
"s_add_u32 s43, s30, 1\n" \
"s_mov_b32 s50, 0\n" \
".LP2_w43l:\n" \
"buffer_load_dword v240, v204, s[4:7], 0 offen sc1\n" \
"s_waitcnt vmcnt(0)\n" \
".LP2_w43c:\n" \
"v_subrev_u32 v241, s43, v240\n" \
"v_cmp_gt_u32 vcc, 0x100, v241\n" \
"s_cmp_eq_u64 vcc, -1\n" \
"s_cbranch_scc1 .LP2_w43d\n" \
"s_sleep 2\n" \
"s_add_u32 s50, s50, 1\n" \
"s_cmp_lt_u32 s50, 0x20000\n" \
"s_cbranch_scc1 .LP2_w43l\n" \
"s_branch .LP2_end\n" \
".LP2_w43d:\n" \
"v_readlane_b32 s43, v202, 2\n" \
"s_nop 4\n" \
"buffer_load_dwordx4 a[16:19], v201, s[8:11], s43 offen sc1\n" \
"buffer_load_dwordx4 a[20:23], v201, s[8:11], s43 offen offset:1024 sc1\n" \
"v_readlane_b32 s43, v202, 9\n" \
"s_nop 4\n" \
"buffer_load_dwordx4 a[72:75], v201, s[8:11], s43 offen sc1\n" \
"buffer_load_dwordx4 a[76:79], v201, s[8:11], s43 offen offset:1024 sc1\n" \
"v_readlane_b32 s43, v202, 16\n" \
"s_nop 4\n" \
"buffer_load_dwordx4 a[128:131], v201, s[8:11], s43 offen sc1\n" \
"buffer_load_dwordx4 a[132:135], v201, s[8:11], s43 offen offset:1024 sc1\n" \
"v_readlane_b32 s43, v202, 23\n" \
"s_nop 4\n" \
"buffer_load_dwordx4 a[184:187], v201, s[8:11], s43 offen sc1\n" \
"buffer_load_dwordx4 a[188:191], v201, s[8:11], s43 offen offset:1024 sc1\n" \
"s_add_u32 s43, s30, 2\n" \
"s_mov_b32 s50, 0\n" \
".LP2_w44l:\n" \
"buffer_load_dword v240, v204, s[4:7], 0 offen sc1\n" \
"s_waitcnt vmcnt(0)\n" \
".LP2_w44c:\n" \
"v_subrev_u32 v241, s43, v240\n" \
"v_cmp_gt_u32 vcc, 0x100, v241\n" \
"s_cmp_eq_u64 vcc, -1\n" \
"s_cbranch_scc1 .LP2_w44d\n" \
"s_sleep 2\n" \
"s_add_u32 s50, s50, 1\n" \
"s_cmp_lt_u32 s50, 0x20000\n" \
"s_cbranch_scc1 .LP2_w44l\n" \
"s_branch .LP2_end\n" \
".LP2_w44d:\n" \
"v_readlane_b32 s43, v202, 3\n" \
"s_nop 4\n" \
"buffer_load_dwordx4 a[24:27], v201, s[8:11], s43 offen sc1\n" \
"buffer_load_dwordx4 a[28:31], v201, s[8:11], s43 offen offset:1024 sc1\n" \
"v_readlane_b32 s43, v202, 10\n" \
"s_nop 4\n" \
"buffer_load_dwordx4 a[80:83], v201, s[8:11], s43 offen sc1\n" \
"buffer_load_dwordx4 a[84:87], v201, s[8:11], s43 offen offset:1024 sc1\n" \
"v_readlane_b32 s43, v202, 17\n" \
"s_nop 4\n" \
"buffer_load_dwordx4 a[136:139], v201, s[8:11], s43 offen sc1\n" \
"buffer_load_dwordx4 a[140:143], v201, s[8:11], s43 offen offset:1024 sc1\n" \
"v_readlane_b32 s43, v202, 24\n" \
"s_nop 4\n" \
"buffer_load_dwordx4 a[192:195], v201, s[8:11], s43 offen sc1\n" \
"buffer_load_dwordx4 a[196:199], v201, s[8:11], s43 offen offset:1024 sc1\n" \
"s_add_u32 s43, s30, 3\n" \
"s_mov_b32 s50, 0\n" \
".LP2_w45l:\n" \
"buffer_load_dword v240, v204, s[4:7], 0 offen sc1\n" \
"s_waitcnt vmcnt(0)\n" \
".LP2_w45c:\n" \
"v_subrev_u32 v241, s43, v240\n" \
"v_cmp_gt_u32 vcc, 0x100, v241\n" \
"s_cmp_eq_u64 vcc, -1\n" \
"s_cbranch_scc1 .LP2_w45d\n" \
"s_sleep 2\n" \
"s_add_u32 s50, s50, 1\n" \
"s_cmp_lt_u32 s50, 0x20000\n" \
"s_cbranch_scc1 .LP2_w45l\n" \
"s_branch .LP2_end\n" \
".LP2_w45d:\n" \
"v_readlane_b32 s43, v202, 4\n" \
"s_nop 4\n" \
"buffer_load_dwordx4 a[32:35], v201, s[8:11], s43 offen sc1\n" \
"buffer_load_dwordx4 a[36:39], v201, s[8:11], s43 offen offset:1024 sc1\n" \
"v_readlane_b32 s43, v202, 11\n" \
"s_nop 4\n" \
"buffer_load_dwordx4 a[88:91], v201, s[8:11], s43 offen sc1\n" \
"buffer_load_dwordx4 a[92:95], v201, s[8:11], s43 offen offset:1024 sc1\n" \
"v_readlane_b32 s43, v202, 18\n" \
"s_nop 4\n" \
"buffer_load_dwordx4 a[144:147], v201, s[8:11], s43 offen sc1\n" \
"buffer_load_dwordx4 a[148:151], v201, s[8:11], s43 offen offset:1024 sc1\n" \
"v_readlane_b32 s43, v202, 25\n" \
"s_nop 4\n" \
"buffer_load_dwordx4 a[200:203], v201, s[8:11], s43 offen sc1\n" \
"buffer_load_dwordx4 a[204:207], v201, s[8:11], s43 offen offset:1024 sc1\n" \
"s_add_u32 s43, s30, 4\n" \
"s_mov_b32 s50, 0\n" \
".LP2_w46l:\n" \
"buffer_load_dword v240, v204, s[4:7], 0 offen sc1\n" \
"s_waitcnt vmcnt(0)\n" \
".LP2_w46c:\n" \
"v_subrev_u32 v241, s43, v240\n" \
"v_cmp_gt_u32 vcc, 0x100, v241\n" \
"s_cmp_eq_u64 vcc, -1\n" \
"s_cbranch_scc1 .LP2_w46d\n" \
"s_sleep 2\n" \
"s_add_u32 s50, s50, 1\n" \
"s_cmp_lt_u32 s50, 0x20000\n" \
"s_cbranch_scc1 .LP2_w46l\n" \
"s_branch .LP2_end\n" \
".LP2_w46d:\n" \
"v_readlane_b32 s43, v202, 5\n" \
"s_nop 4\n" \
"buffer_load_dwordx4 a[40:43], v201, s[8:11], s43 offen sc1\n" \
"buffer_load_dwordx4 a[44:47], v201, s[8:11], s43 offen offset:1024 sc1\n" \
"v_readlane_b32 s43, v202, 12\n" \
"s_nop 4\n" \
"buffer_load_dwordx4 a[96:99], v201, s[8:11], s43 offen sc1\n" \
"buffer_load_dwordx4 a[100:103], v201, s[8:11], s43 offen offset:1024 sc1\n" \
"v_readlane_b32 s43, v202, 19\n" \
"s_nop 4\n" \
"buffer_load_dwordx4 a[152:155], v201, s[8:11], s43 offen sc1\n" \
"buffer_load_dwordx4 a[156:159], v201, s[8:11], s43 offen offset:1024 sc1\n" \
"v_readlane_b32 s43, v202, 26\n" \
"s_nop 4\n" \
"buffer_load_dwordx4 a[208:211], v201, s[8:11], s43 offen sc1\n" \
"buffer_load_dwordx4 a[212:215], v201, s[8:11], s43 offen offset:1024 sc1\n" \
"s_add_u32 s43, s30, 5\n" \
"s_mov_b32 s50, 0\n" \
".LP2_w47l:\n" \
"buffer_load_dword v240, v204, s[4:7], 0 offen sc1\n" \
"s_waitcnt vmcnt(0)\n" \
".LP2_w47c:\n" \
"v_subrev_u32 v241, s43, v240\n" \
"v_cmp_gt_u32 vcc, 0x100, v241\n" \
"s_cmp_eq_u64 vcc, -1\n" \
"s_cbranch_scc1 .LP2_w47d\n" \
"s_sleep 2\n" \
"s_add_u32 s50, s50, 1\n" \
"s_cmp_lt_u32 s50, 0x20000\n" \
"s_cbranch_scc1 .LP2_w47l\n" \
"s_branch .LP2_end\n" \
".LP2_w47d:\n" \
"v_readlane_b32 s43, v202, 6\n" \
"s_nop 4\n" \
"buffer_load_dwordx4 a[48:51], v201, s[8:11], s43 offen sc1\n" \
"buffer_load_dwordx4 a[52:55], v201, s[8:11], s43 offen offset:1024 sc1\n" \
"v_readlane_b32 s43, v202, 13\n" \
"s_nop 4\n" \
"buffer_load_dwordx4 a[104:107], v201, s[8:11], s43 offen sc1\n" \
"buffer_load_dwordx4 a[108:111], v201, s[8:11], s43 offen offset:1024 sc1\n" \
"v_readlane_b32 s43, v202, 20\n" \
"s_nop 4\n" \
"buffer_load_dwordx4 a[160:163], v201, s[8:11], s43 offen sc1\n" \
"buffer_load_dwordx4 a[164:167], v201, s[8:11], s43 offen offset:1024 sc1\n" \
"v_readlane_b32 s43, v202, 27\n" \
"s_nop 4\n" \
"buffer_load_dwordx4 a[216:219], v201, s[8:11], s43 offen sc1\n" \
"buffer_load_dwordx4 a[220:223], v201, s[8:11], s43 offen offset:1024 sc1\n" \
"s_add_u32 s43, s30, 6\n" \
"s_mov_b32 s50, 0\n" \
".LP2_w48l:\n" \
"buffer_load_dword v240, v204, s[4:7], 0 offen sc1\n" \
"s_waitcnt vmcnt(0)\n" \
".LP2_w48c:\n" \
"v_subrev_u32 v241, s43, v240\n" \
"v_cmp_gt_u32 vcc, 0x100, v241\n" \
"s_cmp_eq_u64 vcc, -1\n" \
"s_cbranch_scc1 .LP2_w48d\n" \
"s_sleep 2\n" \
"s_add_u32 s50, s50, 1\n" \
"s_cmp_lt_u32 s50, 0x20000\n" \
"s_cbranch_scc1 .LP2_w48l\n" \
"s_branch .LP2_end\n" \
".LP2_w48d:\n" \
"v_readlane_b32 s43, v202, 0\n" \
"s_nop 4\n" \
"buffer_load_dwordx4 a[0:3], v201, s[8:11], s43 offen sc1\n" \
"buffer_load_dwordx4 a[4:7], v201, s[8:11], s43 offen offset:1024 sc1\n" \
"v_readlane_b32 s43, v202, 7\n" \
"s_nop 4\n" \
"buffer_load_dwordx4 a[56:59], v201, s[8:11], s43 offen sc1\n" \
"buffer_load_dwordx4 a[60:63], v201, s[8:11], s43 offen offset:1024 sc1\n" \
"v_readlane_b32 s43, v202, 14\n" \
"s_nop 4\n" \
"buffer_load_dwordx4 a[112:115], v201, s[8:11], s43 offen sc1\n" \
"buffer_load_dwordx4 a[116:119], v201, s[8:11], s43 offen offset:1024 sc1\n" \
"v_readlane_b32 s43, v202, 21\n" \
"s_nop 4\n" \
"buffer_load_dwordx4 a[168:171], v201, s[8:11], s43 offen sc1\n" \
"buffer_load_dwordx4 a[172:175], v201, s[8:11], s43 offen offset:1024 sc1\n" \
"s_add_u32 s43, s30, 7\n" \
"s_mov_b32 s50, 0\n" \
".LP2_w49l:\n" \
"buffer_load_dword v240, v204, s[4:7], 0 offen sc1\n" \
"s_waitcnt vmcnt(0)\n" \
".LP2_w49c:\n" \
"v_subrev_u32 v241, s43, v240\n" \
"v_cmp_gt_u32 vcc, 0x100, v241\n" \
"s_cmp_eq_u64 vcc, -1\n" \
"s_cbranch_scc1 .LP2_w49d\n" \
"s_sleep 2\n" \
"s_add_u32 s50, s50, 1\n" \
"s_cmp_lt_u32 s50, 0x20000\n" \
"s_cbranch_scc1 .LP2_w49l\n" \
"s_branch .LP2_end\n" \
".LP2_w49d:\n" \
"v_readlane_b32 s43, v202, 1\n" \
"s_nop 4\n" \
"buffer_load_dwordx4 a[8:11], v201, s[8:11], s43 offen sc1\n" \
"buffer_load_dwordx4 a[12:15], v201, s[8:11], s43 offen offset:1024 sc1\n" \
"v_readlane_b32 s43, v202, 8\n" \
"s_nop 4\n" \
"buffer_load_dwordx4 a[64:67], v201, s[8:11], s43 offen sc1\n" \
"buffer_load_dwordx4 a[68:71], v201, s[8:11], s43 offen offset:1024 sc1\n" \
"v_readlane_b32 s43, v202, 15\n" \
"s_nop 4\n" \
"buffer_load_dwordx4 a[120:123], v201, s[8:11], s43 offen sc1\n" \
"buffer_load_dwordx4 a[124:127], v201, s[8:11], s43 offen offset:1024 sc1\n" \
"v_readlane_b32 s43, v202, 22\n" \
"s_nop 4\n" \
"buffer_load_dwordx4 a[176:179], v201, s[8:11], s43 offen sc1\n" \
"buffer_load_dwordx4 a[180:183], v201, s[8:11], s43 offen offset:1024 sc1\n" \
"s_add_u32 s43, s30, 1\n" \
"s_mov_b32 s50, 0\n" \
".LP2_w50l:\n" \
"buffer_load_dword v240, v205, s[4:7], 0 offen sc1\n" \
"s_waitcnt vmcnt(0)\n" \
".LP2_w50c:\n" \
"v_subrev_u32 v241, s43, v240\n" \
"v_cmp_gt_u32 vcc, 0x100, v241\n" \
"s_cmp_eq_u64 vcc, -1\n" \
"s_cbranch_scc1 .LP2_w50d\n" \
"s_sleep 2\n" \
"s_add_u32 s50, s50, 1\n" \
"s_cmp_lt_u32 s50, 0x20000\n" \
"s_cbranch_scc1 .LP2_w50l\n" \
"s_branch .LP2_end\n" \
".LP2_w50d:\n" \
"s_add_u32 s43, s30, 1\n" \
"s_mov_b32 s50, 0\n" \
".LP2_w51l:\n" \
"buffer_load_dword v240, v206, s[4:7], 0 offen sc1\n" \
"s_waitcnt vmcnt(0)\n" \
".LP2_w51c:\n" \
"v_subrev_u32 v241, s43, v240\n" \
"v_cmp_gt_u32 vcc, 0x100, v241\n" \
"s_cmp_eq_u64 vcc, -1\n" \
"s_cbranch_scc1 .LP2_w51d\n" \
"s_sleep 2\n" \
"s_add_u32 s50, s50, 1\n" \
"s_cmp_lt_u32 s50, 0x20000\n" \
"s_cbranch_scc1 .LP2_w51l\n" \
"s_branch .LP2_end\n" \
".LP2_w51d:\n" \
".LP2_pre:\n" \
"s_add_u32 s43, s40, 0\n" \
"s_and_b32 s43, s43, 3\n" \
"s_bitcmp1_b32 s43, 0\n" \
"s_cselect_b32 s47, s32, s31\n" \
"s_lshl_b32 s43, s43, 19\n" \
"s_add_u32 s47, s47, s43\n" \
"buffer_store_dword v219, v213, s[4:7], s47 offen sc1\n" \
"s_xor_b32 s43, s47, s29\n" \
"buffer_store_dword v219, v213, s[4:7], s43 offen sc1\n" \
"s_barrier\n" \
"ds_read_b96 v[248:250], v252\n" \
"s_waitcnt lgkmcnt(0)\n" \
"v_readfirstlane_b32 s44, v248\n" \
"v_readfirstlane_b32 s45, v249\n" \
"v_readfirstlane_b32 s46, v250\n" \
"s_mul_i32 s42, s25, s46\n" \
"s_lshr_b32 s42, s42, 12\n" \
"s_add_u32 s43, s40, 1\n" \
"s_and_b32 s43, s43, 3\n" \
"s_bitcmp1_b32 s43, 0\n" \
"s_cselect_b32 s48, s32, s31\n" \
"s_lshl_b32 s43, s43, 19\n" \
"s_add_u32 s48, s48, s43\n" \
"s_add_u32 s43, s40, 2\n" \
"s_and_b32 s43, s43, 3\n" \
"s_bitcmp1_b32 s43, 0\n" \
"s_cselect_b32 s49, s32, s31\n" \
"s_lshl_b32 s43, s43, 19\n" \
"s_add_u32 s49, s49, s43\n" \
"s_sub_u32 s43, s24, s44\n" \
"s_add_u32 s43, s43, s28\n" \
"s_max_i32 s43, s43, 0xfffd0000\n" \
"s_min_i32 s43, s43, 0x4000\n" \
"s_mul_i32 s43, s43, s46\n" \
"s_ashr_i32 s43, s43, 12\n" \
"s_add_u32 s41, s45, s43\n" \
"s_lshr_b32 s35, s42, 1\n" \
"s_sub_u32 s35, 0, s35\n" \
".LP2_loop:\n" \
"s_branch .LP2_m1\n" \
".LP2_retry1:\n" \
"s_add_u32 s51, s51, 1\n" \
"s_cmp_gt_u32 s51, 0x20000\n" \
"s_cbranch_scc1 .LP2_end\n" \
".LP2_m0:\n" \
"s_add_u32 s41, s41, s42\n" \
"s_add_u32 s24, s24, s25\n" \
"s_xor_b32 s31, s31, s29\n" \
"s_xor_b32 s32, s32, s29\n" \
"s_xor_b32 s47, s47, s29\n" \
"s_xor_b32 s48, s48, s29\n" \
"s_add_u32 s39, s39, 1\n" \
"s_bitcmp1_b32 s31, 21\n" \
"s_cselect_b32 s43, s29, 0\n" \
"v_mov_b32 v116, s43\n" \
"ds_write_b32 v252, v116 offset:16\n" \
".LP2_m1:\n" \
"s_memtime vcc\n" \
"s_waitcnt lgkmcnt(0)\n" \
"s_sub_u32 s43, s41, vcc_lo\n" \
"s_cmp_gt_i32 s43, 0\n" \
"s_cbranch_scc1 .LP2_m1\n" \
"s_cmp_lt_i32 s43, s35\n" \
"s_cbranch_scc1 .LP2_m0\n" \
"buffer_load_dwordx4 v[0:3], v209, s[4:7], s47 offen offset:1024 sc1\n" \
"buffer_load_dwordx4 v[4:7], v209, s[4:7], s47 offen offset:2048 sc1\n" \
"s_cmp_lg_u32 s39, s34\n" \
"s_cselect_b32 s43, 4, 0\n" \
"s_cmp_lt_u32 s40, 6\n" \
"s_cselect_b32 s43, 0, s43\n" \
"s_add_u32 s33, s33, s43\n" \
"s_min_u32 s33, s33, 40\n" \
"s_mov_b32 s34, s39\n" \
"s_cmp_lt_u32 s40, 4\n" \
"s_cselect_b32 s50, 60, 0\n" \
"s_add_u32 s50, s50, s33\n" \
"s_sub_u32 s43, 6042, s46\n" \
"s_mul_i32 s36, s43, 150\n" \
"s_ashr_i32 s36, s36, 10\n" \
"s_add_u32 s36, s36, s50\n" \
"s_add_u32 s36, s36, 1060\n" \
"v_mov_b32 v110, s36\n" \
"s_mul_i32 s36, s43, 110\n" \
"s_ashr_i32 s36, s36, 10\n" \
"s_add_u32 s36, s36, s50\n" \
"s_add_u32 s36, s36, 845\n" \
"v_mov_b32 v111, s36\n" \
"s_nop 15\n" \
"s_nop 13\n" \
"s_mul_i32 s50, s40, 114688\n" \
"buffer_store_dwordx4 v[68:71], v218, s[12:15], s50 offen sc1\n" \
"buffer_store_dword v112, v213, s[4:7], s49 offen sc1\n" \
"s_xor_b32 s43, s49, s29\n" \
"buffer_store_dword v112, v213, s[4:7], s43 offen sc1\n" \
"s_lshl_b32 s43, s40, 7\n" \
"buffer_store_dwordx2 v[110:111], v222, s[4:7], s43 offen offset:384 sc1\n" \
"v_mov_b32 v28, 0\n" \
"v_mov_b32 v29, 0\n" \
"v_mov_b32 v30, 0\n" \
"v_mov_b32 v31, 0\n" \
"v_mov_b32 v32, 0\n" \
"v_mov_b32 v33, 0\n" \
"v_mov_b32 v34, 0\n" \
"v_mov_b32 v35, 0\n" \
"v_mov_b32 v36, 0\n" \
"v_mov_b32 v37, 0\n" \
"v_mov_b32 v38, 0\n" \
"v_mov_b32 v39, 0\n" \
"v_mov_b32 v40, 0\n" \
"v_mov_b32 v41, 0\n" \
"v_mov_b32 v42, 0\n" \
"v_mov_b32 v43, 0\n" \
"s_waitcnt vmcnt(5)\n" \
"ds_write_b128 v220, v[0:3] offset:5120\n" \
"s_waitcnt vmcnt(4)\n" \
"ds_write_b128 v220, v[4:7] offset:6144\n" \
"v_max3_u32 v44, v0, v0, v1\n" \
"v_max3_u32 v44, v44, v2, v3\n" \
"v_max3_u32 v44, v44, v4, v5\n" \
"v_max3_u32 v44, v44, v6, v7\n" \
"v_cmp_eq_u32 vcc, -1, v44\n" \
"v_smfmac_f32_16x16x64_bf16 v[28:31], v[0:3], a[0:7], v200\n" \
"v_smfmac_f32_16x16x64_bf16 v[32:35], v[0:3], a[56:63], v200\n" \
"s_cbranch_vccnz .LP2_retry1\n" \
"s_waitcnt lgkmcnt(0)\n" \
"s_barrier\n" \
"ds_read_b128 v[8:11], v220 offset:0\n" \
"ds_read_b128 v[12:15], v220 offset:1024\n" \
"ds_read_b128 v[16:19], v220 offset:2048\n" \
"ds_read_b128 v[20:23], v220 offset:3072\n" \
"ds_read_b128 v[24:27], v220 offset:4096\n" \
"ds_read_b64 v[246:247], v252 offset:24\n" \
"ds_read_b96 v[248:250], v252\n" \
"v_smfmac_f32_16x16x64_bf16 v[36:39], v[0:3], a[112:119], v200\n" \
"v_smfmac_f32_16x16x64_bf16 v[40:43], v[0:3], a[168:175], v200\n" \
"v_smfmac_f32_16x16x64_bf16 v[28:31], v[4:7], a[8:15], v200\n" \
"v_smfmac_f32_16x16x64_bf16 v[32:35], v[4:7], a[64:71], v200\n" \
"v_smfmac_f32_16x16x64_bf16 v[36:39], v[4:7], a[120:127], v200\n" \
"v_smfmac_f32_16x16x64_bf16 v[40:43], v[4:7], a[176:183], v200\n" \
"s_waitcnt lgkmcnt(6)\n" \
"v_smfmac_f32_16x16x64_bf16 v[28:31], v[8:11], a[16:23], v200\n" \
"v_smfmac_f32_16x16x64_bf16 v[32:35], v[8:11], a[72:79], v200\n" \
"v_smfmac_f32_16x16x64_bf16 v[36:39], v[8:11], a[128:135], v200\n" \
"v_smfmac_f32_16x16x64_bf16 v[40:43], v[8:11], a[184:191], v200\n" \
"s_waitcnt lgkmcnt(0)\n" \
"v_readfirstlane_b32 s44, v248\n" \
"v_readfirstlane_b32 s45, v249\n" \
"v_smfmac_f32_16x16x64_bf16 v[28:31], v[12:15], a[24:31], v200\n" \
"v_readfirstlane_b32 s46, v250\n" \
"v_readfirstlane_b32 s26, v246\n" \
"v_readfirstlane_b32 s27, v247\n" \
"v_smfmac_f32_16x16x64_bf16 v[32:35], v[12:15], a[80:87], v200\n" \
"s_mul_i32 s42, s25, s46\n" \
"s_lshr_b32 s42, s42, 12\n" \
"s_add_u32 s24, s24, s26\n" \
"v_smfmac_f32_16x16x64_bf16 v[36:39], v[12:15], a[136:143], v200\n" \
"s_sub_u32 s43, s24, s44\n" \
"s_max_i32 s43, s43, 0xfffd0000\n" \
"s_min_i32 s43, s43, 0x4000\n" \
"v_smfmac_f32_16x16x64_bf16 v[40:43], v[12:15], a[192:199], v200\n" \
"s_mul_i32 s43, s43, s46\n" \
"s_ashr_i32 s43, s43, 12\n" \
"s_add_u32 s41, s45, s43\n" \
"s_cmp_eq_u32 s40, 63\n" \
"s_cbranch_scc1 .LP2_last\n" \
"s_branch .LP2_z1\n" \
".LP2_z0:\n" \
"s_add_u32 s41, s41, s42\n" \
"s_add_u32 s24, s24, s25\n" \
"s_xor_b32 s31, s31, s29\n" \
"s_xor_b32 s32, s32, s29\n" \
"s_xor_b32 s47, s47, s29\n" \
"s_xor_b32 s48, s48, s29\n" \
"s_add_u32 s39, s39, 1\n" \
"buffer_store_dwordx4 v[112:115], v223, s[4:7], s48 offen sc1\n" \
"s_bitcmp1_b32 s31, 21\n" \
"s_cselect_b32 s43, s29, 0\n" \
"v_mov_b32 v116, s43\n" \
"ds_write_b32 v252, v116 offset:16\n" \
".LP2_z1:\n" \
"s_memtime vcc\n" \
"s_waitcnt lgkmcnt(0)\n" \
"s_sub_u32 s43, s41, vcc_lo\n" \
"s_cmp_gt_i32 s43, 200\n" \
"s_cbranch_scc1 .LP2_z1\n" \
"s_cmp_lt_i32 s43, s37\n" \
"s_cbranch_scc1 .LP2_z0\n" \
"buffer_load_dwordx2 v[64:65], v211, s[4:7], s47 offen sc1\n" \
".p2align 6\n" \
".LP2_b1:\n" \
"s_memtime vcc\n" \
"s_waitcnt lgkmcnt(0)\n" \
"s_sub_u32 s43, s41, vcc_lo\n" \
"s_cmp_gt_i32 s43, 128\n" \
"s_cbranch_scc1 .LP2_b1\n" \
"s_cmp_lt_i32 s43, s37\n" \
"s_cbranch_scc1 .LP2_z0\n" \
"s_max_i32 s43, s43, 0\n" \
"s_and_b32 s43, s43, -4\n" \
"s_getpc_b64 vcc\n" \
".LP2_b3:\n" \
"s_sub_u32 s43, .LP2_b4-.LP2_b3, s43\n" \
"s_add_u32 vcc_lo, vcc_lo, s43\n" \
"s_addc_u32 vcc_hi, vcc_hi, 0\n" \
"s_setpc_b64 vcc\n" \
".rept 32\n" \
"s_nop 0\n" \
".endr\n" \
".LP2_b4:\n" \
"buffer_load_dwordx4 v[56:59], v210, s[4:7], s47 offen sc1\n" \
"buffer_load_dwordx4 v[60:63], v210, s[4:7], s47 offen offset:256 sc1\n" \
"v_smfmac_f32_16x16x64_bf16 v[28:31], v[16:19], a[32:39], v200\n" \
"v_smfmac_f32_16x16x64_bf16 v[32:35], v[16:19], a[88:95], v200\n" \
"v_smfmac_f32_16x16x64_bf16 v[36:39], v[16:19], a[144:151], v200\n" \
"v_smfmac_f32_16x16x64_bf16 v[40:43], v[16:19], a[200:207], v200\n" \
"v_smfmac_f32_16x16x64_bf16 v[28:31], v[20:23], a[40:47], v200\n" \
"v_smfmac_f32_16x16x64_bf16 v[32:35], v[20:23], a[96:103], v200\n" \
"buffer_load_dwordx4 v[50:53], v212, s[4:7], s47 offen sc1\n" \
"v_smfmac_f32_16x16x64_bf16 v[36:39], v[20:23], a[152:159], v200\n" \
"v_smfmac_f32_16x16x64_bf16 v[40:43], v[20:23], a[208:215], v200\n" \
"v_smfmac_f32_16x16x64_bf16 v[28:31], v[24:27], a[48:55], v200\n" \
"v_smfmac_f32_16x16x64_bf16 v[32:35], v[24:27], a[104:111], v200\n" \
"v_smfmac_f32_16x16x64_bf16 v[36:39], v[24:27], a[160:167], v200\n" \
"v_smfmac_f32_16x16x64_bf16 v[40:43], v[24:27], a[216:223], v200\n" \
"s_nop 2\n" \
"v_mov_b32 v245, v245\n" \
"v_mov_b32 v245, v245\n" \
"v_mov_b32 v245, v245\n" \
"v_permlane32_swap_b32 v28, v32\n" \
"v_permlane32_swap_b32 v29, v33\n" \
"v_permlane32_swap_b32 v30, v34\n" \
"v_permlane32_swap_b32 v31, v35\n" \
"v_add_f32 v28, v28, v32\n" \
"v_add_f32 v29, v29, v33\n" \
"v_add_f32 v30, v30, v34\n" \
"v_add_f32 v31, v31, v35\n" \
"v_cvt_pk_bf16_f32 v68, v28, v29\n" \
"v_cvt_pk_bf16_f32 v69, v30, v31\n" \
"v_permlane32_swap_b32 v36, v40\n" \
"v_permlane32_swap_b32 v37, v41\n" \
"v_permlane32_swap_b32 v38, v42\n" \
"v_permlane32_swap_b32 v39, v43\n" \
"v_add_f32 v36, v36, v40\n" \
"v_add_f32 v37, v37, v41\n" \
"v_add_f32 v38, v38, v42\n" \
"v_add_f32 v39, v39, v43\n" \
"v_cvt_pk_bf16_f32 v70, v36, v37\n" \
"v_cvt_pk_bf16_f32 v71, v38, v39\n" \
"v_mov_b32 v48, v68\n" \
"v_mov_b32 v49, v69\n" \
"v_mov_b32 v54, v70\n" \
"v_mov_b32 v55, v71\n" \
"s_nop 1\n" \
"v_permlane16_swap_b32 v48, v54\n" \
"v_permlane16_swap_b32 v49, v55\n" \
".LP2_wait:\n" \
"s_waitcnt vmcnt(1)\n" \
"v_pk_add_f32 v[72:73], v[56:57], v[60:61]\n" \
"v_pk_add_f32 v[74:75], v[58:59], v[62:63]\n" \
"v_max3_f32 v77, v72, v73, v64\n" \
"v_max3_f32 v77, v77, v74, v75\n" \
"s_nop 1\n" \
"v_max_f32_dpp v76, v77, v77 row_ror:8 row_mask:0xf bank_mask:0xf\n" \
"v_pk_add_f32 v[72:73], v[72:73], v[76:77] op_sel_hi:[1,0] neg_lo:[0,1] neg_hi:[0,1]\n" \
"v_pk_add_f32 v[74:75], v[74:75], v[76:77] op_sel_hi:[1,0] neg_lo:[0,1] neg_hi:[0,1]\n" \
"v_sub_f32 v78, v64, v76\n" \
"v_exp_f32 v72, v72\n" \
"v_exp_f32 v73, v73\n" \
"v_exp_f32 v74, v74\n" \
"v_exp_f32 v75, v75\n" \
"v_exp_f32 v78, v78\n" \
"v_pk_add_f32 v[80:81], v[72:73], v[74:75]\n" \
"v_fma_f32 v80, v78, v65, v80\n" \
"v_add_f32 v81, v80, v81\n" \
"s_nop 1\n" \
"v_add_f32_dpp v80, v81, v81 row_ror:8 row_mask:0xf bank_mask:0xf\n" \
"v_rcp_f32 v80, v80\n" \
"s_nop 0\n" \
"v_mul_f32 v78, v78, v80\n" \
"v_pk_mul_f32 v[72:73], v[72:73], v[80:81] op_sel_hi:[1,0]\n" \
"v_pk_mul_f32 v[74:75], v[74:75], v[80:81] op_sel_hi:[1,0]\n" \
"v_cvt_pk_bf16_f32 v78, v78, v78\n" \
"v_cvt_pk_bf16_f32 v82, v72, v73\n" \
"v_cvt_pk_bf16_f32 v83, v74, v75\n" \
"v_and_b32 v84, v78, v214\n" \
"v_and_b32 v85, v78, v215\n" \
"v_cvt_pk_bf16_f32 v86, v72, v73\n" \
"v_cvt_pk_bf16_f32 v87, v74, v75\n" \
"s_waitcnt vmcnt(0)\n" \
"v_mfma_f32_16x16x32_bf16 v[88:91], v[48:51], v[82:85], 0\n" \
"v_mfma_f32_16x16x32_bf16 v[92:95], v[52:55], v[84:87], 0\n" \
"s_nop 2\n" \
"v_mov_b32 v245, v245\n" \
"s_nop 2\n" \
"v_mov_b32 v245, v245\n" \
"v_add_f32_dpp v96, v92, v88 row_ror:8 row_mask:0xf bank_mask:0x3\n" \
"v_add_f32_dpp v97, v93, v89 row_ror:8 row_mask:0xf bank_mask:0x3\n" \
"v_add_f32_dpp v96, v90, v94 row_ror:8 row_mask:0xf bank_mask:0xc\n" \
"v_add_f32_dpp v97, v91, v95 row_ror:8 row_mask:0xf bank_mask:0xc\n" \
"ds_write_b64 v216, v[96:97]\n" \
"v_cmp_u_f32 vcc, v96, v97\n" \
"s_bitcmp1_b32 s31, 21\n" \
"s_cselect_b32 s18, s29, 0\n" \
"s_xor_b32 s18, s18, s48\n" \
"v_mov_b32 v245, v245\n" \
"s_xor_b32 s19, s18, s29\n" \
"s_nop 0\n" \
"s_nop 0\n" \
"v_mov_b32 v245, v245\n" \
"s_nop 0\n" \
"s_nop 0\n" \
"s_nop 0\n" \
"v_mov_b32 v245, v245\n" \
"s_cbranch_vccnz .LP2_cls\n" \
".LP2_clsr:\n" \
"s_waitcnt lgkmcnt(0)\n" \
"s_barrier\n" \
"ds_read_b64 v[100:101], v217 offset:0\n" \
"ds_read_b64 v[102:103], v217 offset:512\n" \
"ds_read_b32 v116, v252 offset:16\n" \
"ds_read_b64 v[104:105], v217 offset:1024\n" \
"ds_read_b64 v[106:107], v217 offset:1536\n" \
"v_mov_b32 v245, v245\n" \
"v_mov_b32 v245, v245\n" \
"s_add_u32 s40, s40, 1\n" \
"s_add_u32 s24, s24, s27\n" \
"s_mov_b32 s47, s48\n" \
"v_mov_b32 v245, v245\n" \
"s_waitcnt lgkmcnt(2)\n" \
"v_xad_u32 v117, v116, s18, v213\n" \
"v_pk_add_f32 v[100:101], v[100:101], v[102:103]\n" \
"s_waitcnt lgkmcnt(0)\n" \
"v_pk_add_f32 v[104:105], v[104:105], v[106:107]\n" \
"v_pk_add_f32 v[100:101], v[100:101], v[104:105]\n" \
"v_cvt_pk_bf16_f32 v96, v100, v101\n" \
"s_add_u32 s43, s40, 1\n" \
"s_and_b32 s43, s43, 3\n" \
"s_bitcmp1_b32 s43, 0\n" \
"s_cselect_b32 s48, s32, s31\n" \
"s_lshl_b32 s43, s43, 19\n" \
"s_add_u32 s48, s48, s43\n" \
"s_add_u32 s43, s40, 2\n" \
"s_and_b32 s43, s43, 3\n" \
"buffer_store_dword v96, v117, s[4:7], 0 offen sc1\n" \
"s_bitcmp1_b32 s43, 0\n" \
"s_cselect_b32 s49, s32, s31\n" \
"s_lshl_b32 s43, s43, 19\n" \
"s_add_u32 s49, s49, s43\n" \
"s_sub_u32 s43, s24, s44\n" \
"s_add_u32 s43, s43, s28\n" \
"s_max_i32 s43, s43, 0xfffd0000\n" \
"s_min_i32 s43, s43, 0x4000\n" \
"s_mul_i32 s43, s43, s46\n" \
"s_ashr_i32 s43, s43, 12\n" \
"s_add_u32 s41, s45, s43\n" \
"s_lshr_b32 s35, s42, 1\n" \
"s_sub_u32 s35, 0, s35\n" \
"s_nop 6\n" \
"v_xad_u32 v116, v116, s19, v213\n" \
"buffer_store_dword v96, v116, s[4:7], 0 offen sc1\n" \
"s_cmp_eq_u32 s40, 0\n" \
"s_cbranch_scc1 .LP2_wet\n" \
"s_branch .LP2_loop\n" \
"s_branch .LP2_y1\n" \
".LP2_retry2:\n" \
"s_add_u32 s51, s51, 1\n" \
"s_cmp_gt_u32 s51, 0x20000\n" \
"s_cbranch_scc1 .LP2_end\n" \
".LP2_y0:\n" \
"s_add_u32 s41, s41, s42\n" \
"s_add_u32 s24, s24, s25\n" \
"s_xor_b32 s31, s31, s29\n" \
"s_xor_b32 s32, s32, s29\n" \
"s_xor_b32 s47, s47, s29\n" \
"s_xor_b32 s48, s48, s29\n" \
"s_add_u32 s39, s39, 1\n" \
"buffer_store_dwordx4 v[112:115], v223, s[4:7], s48 offen sc1\n" \
"s_bitcmp1_b32 s31, 21\n" \
"s_cselect_b32 s43, s29, 0\n" \
"v_mov_b32 v116, s43\n" \
"ds_write_b32 v252, v116 offset:16\n" \
".LP2_y1:\n" \
"s_memtime vcc\n" \
"s_waitcnt lgkmcnt(0)\n" \
"s_sub_u32 s43, s41, vcc_lo\n" \
"s_cmp_gt_i32 s43, 200\n" \
"s_cbranch_scc1 .LP2_y1\n" \
"s_cmp_lt_i32 s43, s37\n" \
"s_cbranch_scc1 .LP2_y0\n" \
"buffer_load_dwordx2 v[64:65], v211, s[4:7], s47 offen sc1\n" \
".p2align 6\n" \
".LP2_c1:\n" \
"s_memtime vcc\n" \
"s_waitcnt lgkmcnt(0)\n" \
"s_sub_u32 s43, s41, vcc_lo\n" \
"s_cmp_gt_i32 s43, 128\n" \
"s_cbranch_scc1 .LP2_c1\n" \
"s_cmp_lt_i32 s43, s37\n" \
"s_cbranch_scc1 .LP2_y0\n" \
"s_max_i32 s43, s43, 0\n" \
"s_and_b32 s43, s43, -4\n" \
"s_getpc_b64 vcc\n" \
".LP2_c3:\n" \
"s_sub_u32 s43, .LP2_c4-.LP2_c3, s43\n" \
"s_add_u32 vcc_lo, vcc_lo, s43\n" \
"s_addc_u32 vcc_hi, vcc_hi, 0\n" \
"s_setpc_b64 vcc\n" \
".rept 32\n" \
"s_nop 0\n" \
".endr\n" \
".LP2_c4:\n" \
"buffer_load_dwordx4 v[56:59], v210, s[4:7], s47 offen sc1\n" \
"buffer_load_dwordx4 v[60:63], v210, s[4:7], s47 offen offset:256 sc1\n" \
"s_nop 2\n" \
"v_mov_b32 v245, v245\n" \
"s_nop 2\n" \
"v_mov_b32 v245, v245\n" \
"s_nop 2\n" \
"v_mov_b32 v245, v245\n" \
"s_nop 2\n" \
"v_mov_b32 v245, v245\n" \
"s_nop 2\n" \
"v_mov_b32 v245, v245\n" \
"s_nop 2\n" \
"v_mov_b32 v245, v245\n" \
"buffer_load_dwordx4 v[50:53], v212, s[4:7], s47 offen sc1\n" \
"s_nop 2\n" \
"v_mov_b32 v245, v245\n" \
"s_nop 2\n" \
"v_mov_b32 v245, v245\n" \
"s_nop 2\n" \
"v_mov_b32 v245, v245\n" \
"s_nop 2\n" \
"v_mov_b32 v245, v245\n" \
"s_nop 2\n" \
"v_mov_b32 v245, v245\n" \
"s_nop 2\n" \
"v_mov_b32 v245, v245\n" \
"s_nop 2\n" \
"v_mov_b32 v245, v245\n" \
"s_nop 2\n" \
"v_mov_b32 v245, v245\n" \
"s_nop 2\n" \
"v_mov_b32 v245, v245\n" \
"s_nop 2\n" \
"v_mov_b32 v245, v245\n" \
"s_nop 2\n" \
"v_mov_b32 v245, v245\n" \
"s_nop 2\n" \
"v_mov_b32 v245, v245\n" \
"s_nop 2\n" \
"v_mov_b32 v245, v245\n" \
"s_nop 2\n" \
"v_mov_b32 v245, v245\n" \
"s_nop 2\n" \
"v_mov_b32 v245, v245\n" \
"s_nop 2\n" \
"v_mov_b32 v245, v245\n" \
"s_nop 2\n" \
"v_mov_b32 v245, v245\n" \
"s_branch .LP2_wait\n" \
".LP2_cls:\n" \
"s_waitcnt vmcnt(0)\n" \
"v_max3_u32 v44, v64, v64, v65\n" \
"v_max3_u32 v44, v44, v50, v51\n" \
"v_max3_u32 v44, v44, v52, v53\n" \
"v_cmp_eq_u32 vcc, -1, v44\n" \
"s_cbranch_vccnz .LP2_retry2\n" \
"v_cmp_u_f32 vcc, v56, v56\n" \
"v_cmp_ne_u32 s[2:3], v56, v109\n" \
"s_and_b64 vcc, vcc, s[2:3]\n" \
"s_cbranch_vccnz .LP2_retry2\n" \
"v_cmp_u_f32 vcc, v57, v57\n" \
"v_cmp_ne_u32 s[2:3], v57, v109\n" \
"s_and_b64 vcc, vcc, s[2:3]\n" \
"s_cbranch_vccnz .LP2_retry2\n" \
"v_cmp_u_f32 vcc, v58, v58\n" \
"v_cmp_ne_u32 s[2:3], v58, v109\n" \
"s_and_b64 vcc, vcc, s[2:3]\n" \
"s_cbranch_vccnz .LP2_retry2\n" \
"v_cmp_u_f32 vcc, v59, v59\n" \
"v_cmp_ne_u32 s[2:3], v59, v109\n" \
"s_and_b64 vcc, vcc, s[2:3]\n" \
"s_cbranch_vccnz .LP2_retry2\n" \
"v_cmp_u_f32 vcc, v60, v60\n" \
"v_cmp_ne_u32 s[2:3], v60, v109\n" \
"s_and_b64 vcc, vcc, s[2:3]\n" \
"s_cbranch_vccnz .LP2_retry2\n" \
"v_cmp_u_f32 vcc, v61, v61\n" \
"v_cmp_ne_u32 s[2:3], v61, v109\n" \
"s_and_b64 vcc, vcc, s[2:3]\n" \
"s_cbranch_vccnz .LP2_retry2\n" \
"v_cmp_u_f32 vcc, v62, v62\n" \
"v_cmp_ne_u32 s[2:3], v62, v109\n" \
"s_and_b64 vcc, vcc, s[2:3]\n" \
"s_cbranch_vccnz .LP2_retry2\n" \
"v_cmp_u_f32 vcc, v63, v63\n" \
"v_cmp_ne_u32 s[2:3], v63, v109\n" \
"s_and_b64 vcc, vcc, s[2:3]\n" \
"s_cbranch_vccnz .LP2_retry2\n" \
"s_branch .LP2_clsr\n" \
".LP2_last:\n" \
"v_smfmac_f32_16x16x64_bf16 v[28:31], v[16:19], a[32:39], v200\n" \
"v_smfmac_f32_16x16x64_bf16 v[32:35], v[16:19], a[88:95], v200\n" \
"v_smfmac_f32_16x16x64_bf16 v[36:39], v[16:19], a[144:151], v200\n" \
"v_smfmac_f32_16x16x64_bf16 v[40:43], v[16:19], a[200:207], v200\n" \
"v_smfmac_f32_16x16x64_bf16 v[28:31], v[20:23], a[40:47], v200\n" \
"v_smfmac_f32_16x16x64_bf16 v[32:35], v[20:23], a[96:103], v200\n" \
"v_smfmac_f32_16x16x64_bf16 v[36:39], v[20:23], a[152:159], v200\n" \
"v_smfmac_f32_16x16x64_bf16 v[40:43], v[20:23], a[208:215], v200\n" \
"v_smfmac_f32_16x16x64_bf16 v[28:31], v[24:27], a[48:55], v200\n" \
"v_smfmac_f32_16x16x64_bf16 v[32:35], v[24:27], a[104:111], v200\n" \
"v_smfmac_f32_16x16x64_bf16 v[36:39], v[24:27], a[160:167], v200\n" \
"v_smfmac_f32_16x16x64_bf16 v[40:43], v[24:27], a[216:223], v200\n" \
"s_waitcnt vmcnt(0)\n" \
"s_nop 7\n" \
"v_permlane32_swap_b32 v28, v32\n" \
"v_permlane32_swap_b32 v29, v33\n" \
"v_permlane32_swap_b32 v30, v34\n" \
"v_permlane32_swap_b32 v31, v35\n" \
"v_add_f32 v28, v28, v32\n" \
"v_add_f32 v29, v29, v33\n" \
"v_add_f32 v30, v30, v34\n" \
"v_add_f32 v31, v31, v35\n" \
"v_cvt_pk_bf16_f32 v68, v28, v29\n" \
"v_cvt_pk_bf16_f32 v69, v30, v31\n" \
"v_permlane32_swap_b32 v36, v40\n" \
"v_permlane32_swap_b32 v37, v41\n" \
"v_permlane32_swap_b32 v38, v42\n" \
"v_permlane32_swap_b32 v39, v43\n" \
"v_add_f32 v36, v36, v40\n" \
"v_add_f32 v37, v37, v41\n" \
"v_add_f32 v38, v38, v42\n" \
"v_add_f32 v39, v39, v43\n" \
"v_cvt_pk_bf16_f32 v70, v36, v37\n" \
"v_cvt_pk_bf16_f32 v71, v38, v39\n" \
"s_mov_b32 s50, 7340032\n" \
"buffer_store_dwordx4 v[68:71], v218, s[12:15], s50 offen sc1\n" \
"s_waitcnt vmcnt(0)\n" \
"s_barrier\n" \
".LP2_end:\n" \
"s_endpgm\n" \
".p2align 6\n" \
"fk_role_P3:\n" \
"ds_read_b32 v200, v255 offset:0\n" \
"ds_read_b32 v201, v255 offset:256\n" \
"ds_read_b32 v202, v255 offset:512\n" \
"ds_read_b32 v203, v255 offset:768\n" \
"ds_read_b32 v204, v255 offset:1024\n" \
"ds_read_b32 v205, v255 offset:1280\n" \
"ds_read_b32 v206, v255 offset:1536\n" \
"ds_read_b32 v207, v255 offset:1792\n" \
"ds_read_b32 v208, v255 offset:2048\n" \
"ds_read_b32 v209, v255 offset:2304\n" \
"ds_read_b32 v210, v255 offset:2560\n" \
"ds_read_b32 v211, v255 offset:2816\n" \
"ds_read_b32 v212, v255 offset:3072\n" \
"ds_read_b32 v213, v255 offset:3328\n" \
"ds_read_b32 v214, v255 offset:3584\n" \
"ds_read_b32 v215, v255 offset:3840\n" \
"ds_read_b32 v216, v255 offset:4096\n" \
"ds_read_b32 v217, v255 offset:4352\n" \
"ds_read_b32 v218, v255 offset:4608\n" \
"ds_read_b32 v219, v255 offset:4864\n" \
"ds_read_b32 v220, v255 offset:5120\n" \
"ds_read_b32 v221, v255 offset:5376\n" \
"ds_read_b32 v222, v255 offset:5632\n" \
"ds_read_b32 v223, v255 offset:5888\n" \
"ds_read_b32 v254, v255 offset:7936\n" \
"s_waitcnt lgkmcnt(0)\n" \
"v_readlane_b32 s4, v254, 0\n" \
"v_readlane_b32 s5, v254, 1\n" \
"v_readlane_b32 s6, v254, 2\n" \
"v_readlane_b32 s7, v254, 3\n" \
"v_readlane_b32 s8, v254, 4\n" \
"v_readlane_b32 s9, v254, 5\n" \
"v_readlane_b32 s10, v254, 6\n" \
"v_readlane_b32 s11, v254, 7\n" \
"v_readlane_b32 s12, v254, 8\n" \
"v_readlane_b32 s13, v254, 9\n" \
"v_readlane_b32 s14, v254, 10\n" \
"v_readlane_b32 s15, v254, 11\n" \
"v_readlane_b32 s16, v254, 12\n" \
"v_readlane_b32 s17, v254, 13\n" \
"v_readlane_b32 s18, v254, 14\n" \
"v_readlane_b32 s19, v254, 15\n" \
"v_readlane_b32 s20, v254, 16\n" \
"v_readlane_b32 s21, v254, 17\n" \
"v_readlane_b32 s22, v254, 18\n" \
"v_readlane_b32 s23, v254, 19\n" \
"v_readlane_b32 s24, v254, 20\n" \
"v_readlane_b32 s25, v254, 21\n" \
"v_readlane_b32 s26, v254, 22\n" \
"v_readlane_b32 s27, v254, 23\n" \
"v_readlane_b32 s28, v254, 24\n" \
"v_readlane_b32 s29, v254, 25\n" \
"v_readlane_b32 s30, v254, 26\n" \
"v_readlane_b32 s31, v254, 27\n" \
"v_readlane_b32 s32, v254, 28\n" \
"v_readlane_b32 s33, v254, 29\n" \
"v_readlane_b32 s34, v254, 30\n" \
"v_readlane_b32 s35, v254, 31\n" \
"v_readlane_b32 s36, v254, 32\n" \
"v_readlane_b32 s37, v254, 33\n" \
"v_readlane_b32 s38, v254, 34\n" \
"v_readlane_b32 s39, v254, 35\n" \
"s_mov_b32 s40, 0\n" \
"s_mov_b32 s51, 0\n" \
"s_nop 0\n" \
"v_mov_b32 v252, s38\n" \
"s_nop 4\n" \
"v_mov_b32 v112, -1\n" \
"v_mov_b32 v113, -1\n" \
"v_mov_b32 v114, -1\n" \
"v_mov_b32 v115, -1\n" \
"v_mov_b32 v109, 0x7fc00001\n" \
"v_writelane_b32 v251, s6, 0\n" \
"v_writelane_b32 v251, s10, 1\n" \
"v_writelane_b32 v251, s14, 2\n" \
"v_writelane_b32 v251, s18, 3\n" \
"s_mov_b32 s6, 0\n" \
"s_mov_b32 s10, 0\n" \
"s_mov_b32 s14, 0\n" \
"s_mov_b32 s18, 0\n" \
"s_mov_b32 s40, -1\n" \
"s_branch .LP3_pre\n" \
".LP3_wet:\n" \
"s_waitcnt vmcnt(0)\n" \
"v_readlane_b32 s6, v251, 0\n" \
"v_readlane_b32 s10, v251, 1\n" \
"v_readlane_b32 s14, v251, 2\n" \
"v_readlane_b32 s18, v251, 3\n" \
"s_nop 4\n" \
"s_mov_b32 s33, 0\n" \
"s_mov_b32 s34, 0\n" \
"s_add_u32 s43, s30, 1\n" \
"s_mov_b32 s50, 0\n" \
".LP3_w52l:\n" \
"buffer_load_dword v240, v204, s[4:7], 0 offen sc1\n" \
"s_waitcnt vmcnt(0)\n" \
".LP3_w52c:\n" \
"v_subrev_u32 v241, s43, v240\n" \
"v_cmp_gt_u32 vcc, 0x100, v241\n" \
"s_cmp_eq_u64 vcc, -1\n" \
"s_cbranch_scc1 .LP3_w52d\n" \
"s_sleep 2\n" \
"s_add_u32 s50, s50, 1\n" \
"s_cmp_lt_u32 s50, 0x20000\n" \
"s_cbranch_scc1 .LP3_w52l\n" \
"s_branch .LP3_end\n" \
".LP3_w52d:\n" \
"v_readlane_b32 s43, v202, 1\n" \
"s_nop 4\n" \
"buffer_load_dwordx4 a[8:11], v201, s[8:11], s43 offen sc1\n" \
"buffer_load_dwordx4 a[12:15], v201, s[8:11], s43 offen offset:1024 sc1\n" \
"v_readlane_b32 s43, v202, 8\n" \
"s_nop 4\n" \
"buffer_load_dwordx4 a[64:67], v201, s[8:11], s43 offen sc1\n" \
"buffer_load_dwordx4 a[68:71], v201, s[8:11], s43 offen offset:1024 sc1\n" \
"v_readlane_b32 s43, v202, 15\n" \
"s_nop 4\n" \
"buffer_load_dwordx4 a[120:123], v201, s[8:11], s43 offen sc1\n" \
"buffer_load_dwordx4 a[124:127], v201, s[8:11], s43 offen offset:1024 sc1\n" \
"v_readlane_b32 s43, v202, 22\n" \
"s_nop 4\n" \
"buffer_load_dwordx4 a[176:179], v201, s[8:11], s43 offen sc1\n" \
"buffer_load_dwordx4 a[180:183], v201, s[8:11], s43 offen offset:1024 sc1\n" \
"s_add_u32 s43, s30, 2\n" \
"s_mov_b32 s50, 0\n" \
".LP3_w53l:\n" \
"buffer_load_dword v240, v204, s[4:7], 0 offen sc1\n" \
"s_waitcnt vmcnt(0)\n" \
".LP3_w53c:\n" \
"v_subrev_u32 v241, s43, v240\n" \
"v_cmp_gt_u32 vcc, 0x100, v241\n" \
"s_cmp_eq_u64 vcc, -1\n" \
"s_cbranch_scc1 .LP3_w53d\n" \
"s_sleep 2\n" \
"s_add_u32 s50, s50, 1\n" \
"s_cmp_lt_u32 s50, 0x20000\n" \
"s_cbranch_scc1 .LP3_w53l\n" \
"s_branch .LP3_end\n" \
".LP3_w53d:\n" \
"v_readlane_b32 s43, v202, 2\n" \
"s_nop 4\n" \
"buffer_load_dwordx4 a[16:19], v201, s[8:11], s43 offen sc1\n" \
"buffer_load_dwordx4 a[20:23], v201, s[8:11], s43 offen offset:1024 sc1\n" \
"v_readlane_b32 s43, v202, 9\n" \
"s_nop 4\n" \
"buffer_load_dwordx4 a[72:75], v201, s[8:11], s43 offen sc1\n" \
"buffer_load_dwordx4 a[76:79], v201, s[8:11], s43 offen offset:1024 sc1\n" \
"v_readlane_b32 s43, v202, 16\n" \
"s_nop 4\n" \
"buffer_load_dwordx4 a[128:131], v201, s[8:11], s43 offen sc1\n" \
"buffer_load_dwordx4 a[132:135], v201, s[8:11], s43 offen offset:1024 sc1\n" \
"v_readlane_b32 s43, v202, 23\n" \
"s_nop 4\n" \
"buffer_load_dwordx4 a[184:187], v201, s[8:11], s43 offen sc1\n" \
"buffer_load_dwordx4 a[188:191], v201, s[8:11], s43 offen offset:1024 sc1\n" \
"s_add_u32 s43, s30, 3\n" \
"s_mov_b32 s50, 0\n" \
".LP3_w54l:\n" \
"buffer_load_dword v240, v204, s[4:7], 0 offen sc1\n" \
"s_waitcnt vmcnt(0)\n" \
".LP3_w54c:\n" \
"v_subrev_u32 v241, s43, v240\n" \
"v_cmp_gt_u32 vcc, 0x100, v241\n" \
"s_cmp_eq_u64 vcc, -1\n" \
"s_cbranch_scc1 .LP3_w54d\n" \
"s_sleep 2\n" \
"s_add_u32 s50, s50, 1\n" \
"s_cmp_lt_u32 s50, 0x20000\n" \
"s_cbranch_scc1 .LP3_w54l\n" \
"s_branch .LP3_end\n" \
".LP3_w54d:\n" \
"v_readlane_b32 s43, v202, 3\n" \
"s_nop 4\n" \
"buffer_load_dwordx4 a[24:27], v201, s[8:11], s43 offen sc1\n" \
"buffer_load_dwordx4 a[28:31], v201, s[8:11], s43 offen offset:1024 sc1\n" \
"v_readlane_b32 s43, v202, 10\n" \
"s_nop 4\n" \
"buffer_load_dwordx4 a[80:83], v201, s[8:11], s43 offen sc1\n" \
"buffer_load_dwordx4 a[84:87], v201, s[8:11], s43 offen offset:1024 sc1\n" \
"v_readlane_b32 s43, v202, 17\n" \
"s_nop 4\n" \
"buffer_load_dwordx4 a[136:139], v201, s[8:11], s43 offen sc1\n" \
"buffer_load_dwordx4 a[140:143], v201, s[8:11], s43 offen offset:1024 sc1\n" \
"v_readlane_b32 s43, v202, 24\n" \
"s_nop 4\n" \
"buffer_load_dwordx4 a[192:195], v201, s[8:11], s43 offen sc1\n" \
"buffer_load_dwordx4 a[196:199], v201, s[8:11], s43 offen offset:1024 sc1\n" \
"s_add_u32 s43, s30, 4\n" \
"s_mov_b32 s50, 0\n" \
".LP3_w55l:\n" \
"buffer_load_dword v240, v204, s[4:7], 0 offen sc1\n" \
"s_waitcnt vmcnt(0)\n" \
".LP3_w55c:\n" \
"v_subrev_u32 v241, s43, v240\n" \
"v_cmp_gt_u32 vcc, 0x100, v241\n" \
"s_cmp_eq_u64 vcc, -1\n" \
"s_cbranch_scc1 .LP3_w55d\n" \
"s_sleep 2\n" \
"s_add_u32 s50, s50, 1\n" \
"s_cmp_lt_u32 s50, 0x20000\n" \
"s_cbranch_scc1 .LP3_w55l\n" \
"s_branch .LP3_end\n" \
".LP3_w55d:\n" \
"v_readlane_b32 s43, v202, 0\n" \
"s_nop 4\n" \
"buffer_load_dwordx4 a[0:3], v201, s[8:11], s43 offen sc1\n" \
"buffer_load_dwordx4 a[4:7], v201, s[8:11], s43 offen offset:1024 sc1\n" \
"v_readlane_b32 s43, v202, 7\n" \
"s_nop 4\n" \
"buffer_load_dwordx4 a[56:59], v201, s[8:11], s43 offen sc1\n" \
"buffer_load_dwordx4 a[60:63], v201, s[8:11], s43 offen offset:1024 sc1\n" \
"v_readlane_b32 s43, v202, 14\n" \
"s_nop 4\n" \
"buffer_load_dwordx4 a[112:115], v201, s[8:11], s43 offen sc1\n" \
"buffer_load_dwordx4 a[116:119], v201, s[8:11], s43 offen offset:1024 sc1\n" \
"v_readlane_b32 s43, v202, 21\n" \
"s_nop 4\n" \
"buffer_load_dwordx4 a[168:171], v201, s[8:11], s43 offen sc1\n" \
"buffer_load_dwordx4 a[172:175], v201, s[8:11], s43 offen offset:1024 sc1\n" \
"s_add_u32 s43, s30, 5\n" \
"s_mov_b32 s50, 0\n" \
".LP3_w56l:\n" \
"buffer_load_dword v240, v204, s[4:7], 0 offen sc1\n" \
"s_waitcnt vmcnt(0)\n" \
".LP3_w56c:\n" \
"v_subrev_u32 v241, s43, v240\n" \
"v_cmp_gt_u32 vcc, 0x100, v241\n" \
"s_cmp_eq_u64 vcc, -1\n" \
"s_cbranch_scc1 .LP3_w56d\n" \
"s_sleep 2\n" \
"s_add_u32 s50, s50, 1\n" \
"s_cmp_lt_u32 s50, 0x20000\n" \
"s_cbranch_scc1 .LP3_w56l\n" \
"s_branch .LP3_end\n" \
".LP3_w56d:\n" \
"v_readlane_b32 s43, v202, 4\n" \
"s_nop 4\n" \
"buffer_load_dwordx4 a[32:35], v201, s[8:11], s43 offen sc1\n" \
"buffer_load_dwordx4 a[36:39], v201, s[8:11], s43 offen offset:1024 sc1\n" \
"v_readlane_b32 s43, v202, 11\n" \
"s_nop 4\n" \
"buffer_load_dwordx4 a[88:91], v201, s[8:11], s43 offen sc1\n" \
"buffer_load_dwordx4 a[92:95], v201, s[8:11], s43 offen offset:1024 sc1\n" \
"v_readlane_b32 s43, v202, 18\n" \
"s_nop 4\n" \
"buffer_load_dwordx4 a[144:147], v201, s[8:11], s43 offen sc1\n" \
"buffer_load_dwordx4 a[148:151], v201, s[8:11], s43 offen offset:1024 sc1\n" \
"v_readlane_b32 s43, v202, 25\n" \
"s_nop 4\n" \
"buffer_load_dwordx4 a[200:203], v201, s[8:11], s43 offen sc1\n" \
"buffer_load_dwordx4 a[204:207], v201, s[8:11], s43 offen offset:1024 sc1\n" \
"s_add_u32 s43, s30, 6\n" \
"s_mov_b32 s50, 0\n" \
".LP3_w57l:\n" \
"buffer_load_dword v240, v204, s[4:7], 0 offen sc1\n" \
"s_waitcnt vmcnt(0)\n" \
".LP3_w57c:\n" \
"v_subrev_u32 v241, s43, v240\n" \
"v_cmp_gt_u32 vcc, 0x100, v241\n" \
"s_cmp_eq_u64 vcc, -1\n" \
"s_cbranch_scc1 .LP3_w57d\n" \
"s_sleep 2\n" \
"s_add_u32 s50, s50, 1\n" \
"s_cmp_lt_u32 s50, 0x20000\n" \
"s_cbranch_scc1 .LP3_w57l\n" \
"s_branch .LP3_end\n" \
".LP3_w57d:\n" \
"v_readlane_b32 s43, v202, 5\n" \
"s_nop 4\n" \
"buffer_load_dwordx4 a[40:43], v201, s[8:11], s43 offen sc1\n" \
"buffer_load_dwordx4 a[44:47], v201, s[8:11], s43 offen offset:1024 sc1\n" \
"v_readlane_b32 s43, v202, 12\n" \
"s_nop 4\n" \
"buffer_load_dwordx4 a[96:99], v201, s[8:11], s43 offen sc1\n" \
"buffer_load_dwordx4 a[100:103], v201, s[8:11], s43 offen offset:1024 sc1\n" \
"v_readlane_b32 s43, v202, 19\n" \
"s_nop 4\n" \
"buffer_load_dwordx4 a[152:155], v201, s[8:11], s43 offen sc1\n" \
"buffer_load_dwordx4 a[156:159], v201, s[8:11], s43 offen offset:1024 sc1\n" \
"v_readlane_b32 s43, v202, 26\n" \
"s_nop 4\n" \
"buffer_load_dwordx4 a[208:211], v201, s[8:11], s43 offen sc1\n" \
"buffer_load_dwordx4 a[212:215], v201, s[8:11], s43 offen offset:1024 sc1\n" \
"s_add_u32 s43, s30, 7\n" \
"s_mov_b32 s50, 0\n" \
".LP3_w58l:\n" \
"buffer_load_dword v240, v204, s[4:7], 0 offen sc1\n" \
"s_waitcnt vmcnt(0)\n" \
".LP3_w58c:\n" \
"v_subrev_u32 v241, s43, v240\n" \
"v_cmp_gt_u32 vcc, 0x100, v241\n" \
"s_cmp_eq_u64 vcc, -1\n" \
"s_cbranch_scc1 .LP3_w58d\n" \
"s_sleep 2\n" \
"s_add_u32 s50, s50, 1\n" \
"s_cmp_lt_u32 s50, 0x20000\n" \
"s_cbranch_scc1 .LP3_w58l\n" \
"s_branch .LP3_end\n" \
".LP3_w58d:\n" \
"v_readlane_b32 s43, v202, 6\n" \
"s_nop 4\n" \
"buffer_load_dwordx4 a[48:51], v201, s[8:11], s43 offen sc1\n" \
"buffer_load_dwordx4 a[52:55], v201, s[8:11], s43 offen offset:1024 sc1\n" \
"v_readlane_b32 s43, v202, 13\n" \
"s_nop 4\n" \
"buffer_load_dwordx4 a[104:107], v201, s[8:11], s43 offen sc1\n" \
"buffer_load_dwordx4 a[108:111], v201, s[8:11], s43 offen offset:1024 sc1\n" \
"v_readlane_b32 s43, v202, 20\n" \
"s_nop 4\n" \
"buffer_load_dwordx4 a[160:163], v201, s[8:11], s43 offen sc1\n" \
"buffer_load_dwordx4 a[164:167], v201, s[8:11], s43 offen offset:1024 sc1\n" \
"v_readlane_b32 s43, v202, 27\n" \
"s_nop 4\n" \
"buffer_load_dwordx4 a[216:219], v201, s[8:11], s43 offen sc1\n" \
"buffer_load_dwordx4 a[220:223], v201, s[8:11], s43 offen offset:1024 sc1\n" \
"s_add_u32 s43, s30, 1\n" \
"s_mov_b32 s50, 0\n" \
".LP3_w59l:\n" \
"buffer_load_dword v240, v205, s[4:7], 0 offen sc1\n" \
"s_waitcnt vmcnt(0)\n" \
".LP3_w59c:\n" \
"v_subrev_u32 v241, s43, v240\n" \
"v_cmp_gt_u32 vcc, 0x100, v241\n" \
"s_cmp_eq_u64 vcc, -1\n" \
"s_cbranch_scc1 .LP3_w59d\n" \
"s_sleep 2\n" \
"s_add_u32 s50, s50, 1\n" \
"s_cmp_lt_u32 s50, 0x20000\n" \
"s_cbranch_scc1 .LP3_w59l\n" \
"s_branch .LP3_end\n" \
".LP3_w59d:\n" \
"s_add_u32 s43, s30, 1\n" \
"s_mov_b32 s50, 0\n" \
".LP3_w60l:\n" \
"buffer_load_dword v240, v206, s[4:7], 0 offen sc1\n" \
"s_waitcnt vmcnt(0)\n" \
".LP3_w60c:\n" \
"v_subrev_u32 v241, s43, v240\n" \
"v_cmp_gt_u32 vcc, 0x100, v241\n" \
"s_cmp_eq_u64 vcc, -1\n" \
"s_cbranch_scc1 .LP3_w60d\n" \
"s_sleep 2\n" \
"s_add_u32 s50, s50, 1\n" \
"s_cmp_lt_u32 s50, 0x20000\n" \
"s_cbranch_scc1 .LP3_w60l\n" \
"s_branch .LP3_end\n" \
".LP3_w60d:\n" \
".LP3_pre:\n" \
"s_add_u32 s43, s40, 0\n" \
"s_and_b32 s43, s43, 3\n" \
"s_bitcmp1_b32 s43, 0\n" \
"s_cselect_b32 s47, s32, s31\n" \
"s_lshl_b32 s43, s43, 19\n" \
"s_add_u32 s47, s47, s43\n" \
"buffer_store_dword v219, v213, s[4:7], s47 offen sc1\n" \
"s_xor_b32 s43, s47, s29\n" \
"buffer_store_dword v219, v213, s[4:7], s43 offen sc1\n" \
"ds_read_b96 v[248:250], v252\n" \
"s_waitcnt lgkmcnt(0)\n" \
"v_readfirstlane_b32 s44, v248\n" \
"v_readfirstlane_b32 s45, v249\n" \
"v_readfirstlane_b32 s46, v250\n" \
"s_mul_i32 s42, s25, s46\n" \
"s_lshr_b32 s42, s42, 12\n" \
".p2align 3\n" \
"s_memtime s[54:55]\n" \
"s_memrealtime s[56:57]\n" \
"s_memtime s[58:59]\n" \
"s_memrealtime s[60:61]\n" \
"s_nop 0\n" \
"s_nop 0\n" \
"s_memtime s[62:63]\n" \
"s_memrealtime s[64:65]\n" \
"s_memtime s[66:67]\n" \
"s_memrealtime s[68:69]\n" \
"s_memtime s[70:71]\n" \
"s_memrealtime s[72:73]\n" \
"s_memtime s[74:75]\n" \
"s_memrealtime s[76:77]\n" \
"s_nop 0\n" \
"s_nop 0\n" \
"s_memtime s[78:79]\n" \
"s_memrealtime s[80:81]\n" \
"s_memtime s[82:83]\n" \
"s_memrealtime s[84:85]\n" \
"s_memtime s[86:87]\n" \
"s_memrealtime s[88:89]\n" \
"s_memtime s[90:91]\n" \
"s_memrealtime s[92:93]\n" \
"s_nop 0\n" \
"s_nop 0\n" \
"s_memtime s[94:95]\n" \
"s_memrealtime s[96:97]\n" \
"s_memtime s[98:99]\n" \
"s_mov_b32 s52, 0\n" \
"s_mov_b32 s53, 4096\n" \
"s_waitcnt lgkmcnt(0)\n" \
"s_cmp_lg_u32 s96, s56\n" \
"s_cselect_b32 s52, s90, s52\n" \
"s_cselect_b32 s53, s98, s53\n" \
"s_cmp_lg_u32 s92, s56\n" \
"s_cselect_b32 s52, s86, s52\n" \
"s_cselect_b32 s53, s94, s53\n" \
"s_cmp_lg_u32 s88, s56\n" \
"s_cselect_b32 s52, s82, s52\n" \
"s_cselect_b32 s53, s90, s53\n" \
"s_cmp_lg_u32 s84, s56\n" \
"s_cselect_b32 s52, s78, s52\n" \
"s_cselect_b32 s53, s86, s53\n" \
"s_cmp_lg_u32 s80, s56\n" \
"s_cselect_b32 s52, s74, s52\n" \
"s_cselect_b32 s53, s82, s53\n" \
"s_cmp_lg_u32 s76, s56\n" \
"s_cselect_b32 s52, s70, s52\n" \
"s_cselect_b32 s53, s78, s53\n" \
"s_cmp_lg_u32 s72, s56\n" \
"s_cselect_b32 s52, s66, s52\n" \
"s_cselect_b32 s53, s74, s53\n" \
"s_cmp_lg_u32 s68, s56\n" \
"s_cselect_b32 s52, s62, s52\n" \
"s_cselect_b32 s53, s70, s53\n" \
"s_cmp_lg_u32 s64, s56\n" \
"s_cselect_b32 s52, s58, s52\n" \
"s_cselect_b32 s53, s66, s53\n" \
"s_cmp_lg_u32 s60, s56\n" \
"s_cselect_b32 s52, s54, s52\n" \
"s_cselect_b32 s53, s62, s53\n" \
"s_sub_u32 s53, s53, s52\n" \
"s_lshr_b32 s55, s53, 1\n" \
"s_add_u32 s52, s52, s55\n" \
"s_add_u32 s55, s56, 4\n" \
"s_lshl_b32 s55, s55, 4\n" \
"s_cmp_le_u32 s53, 40\n" \
"s_cselect_b32 s44, s55, s44\n" \
"s_cselect_b32 s45, s52, s45\n" \
"v_mov_b32 v248, s44\n" \
"v_mov_b32 v249, s45\n" \
"v_mov_b32 v250, s46\n" \
"s_mov_b64 exec, 1\n" \
"ds_write_b96 v252, v[248:250]\n" \
"s_mov_b64 exec, -1\n" \
".p2align 3\n" \
"s_memtime s[54:55]\n" \
"s_memrealtime s[56:57]\n" \
"s_memtime s[58:59]\n" \
"s_memrealtime s[60:61]\n" \
"s_nop 0\n" \
"s_nop 0\n" \
"s_memtime s[62:63]\n" \
"s_memrealtime s[64:65]\n" \
"s_memtime s[66:67]\n" \
"s_memrealtime s[68:69]\n" \
"s_memtime s[70:71]\n" \
"s_memrealtime s[72:73]\n" \
"s_memtime s[74:75]\n" \
"s_memrealtime s[76:77]\n" \
"s_nop 0\n" \
"s_nop 0\n" \
"s_memtime s[78:79]\n" \
"s_memrealtime s[80:81]\n" \
"s_memtime s[82:83]\n" \
"s_memrealtime s[84:85]\n" \
"s_memtime s[86:87]\n" \
"s_memrealtime s[88:89]\n" \
"s_memtime s[90:91]\n" \
"s_memrealtime s[92:93]\n" \
"s_nop 0\n" \
"s_nop 0\n" \
"s_memtime s[94:95]\n" \
"s_memrealtime s[96:97]\n" \
"s_memtime s[98:99]\n" \
"s_mov_b32 s52, 0\n" \
"s_mov_b32 s53, 4096\n" \
"s_waitcnt lgkmcnt(0)\n" \
"s_cmp_lg_u32 s96, s56\n" \
"s_cselect_b32 s52, s90, s52\n" \
"s_cselect_b32 s53, s98, s53\n" \
"s_cmp_lg_u32 s92, s56\n" \
"s_cselect_b32 s52, s86, s52\n" \
"s_cselect_b32 s53, s94, s53\n" \
"s_cmp_lg_u32 s88, s56\n" \
"s_cselect_b32 s52, s82, s52\n" \
"s_cselect_b32 s53, s90, s53\n" \
"s_cmp_lg_u32 s84, s56\n" \
"s_cselect_b32 s52, s78, s52\n" \
"s_cselect_b32 s53, s86, s53\n" \
"s_cmp_lg_u32 s80, s56\n" \
"s_cselect_b32 s52, s74, s52\n" \
"s_cselect_b32 s53, s82, s53\n" \
"s_cmp_lg_u32 s76, s56\n" \
"s_cselect_b32 s52, s70, s52\n" \
"s_cselect_b32 s53, s78, s53\n" \
"s_cmp_lg_u32 s72, s56\n" \
"s_cselect_b32 s52, s66, s52\n" \
"s_cselect_b32 s53, s74, s53\n" \
"s_cmp_lg_u32 s68, s56\n" \
"s_cselect_b32 s52, s62, s52\n" \
"s_cselect_b32 s53, s70, s53\n" \
"s_cmp_lg_u32 s64, s56\n" \
"s_cselect_b32 s52, s58, s52\n" \
"s_cselect_b32 s53, s66, s53\n" \
"s_cmp_lg_u32 s60, s56\n" \
"s_cselect_b32 s52, s54, s52\n" \
"s_cselect_b32 s53, s62, s53\n" \
"s_sub_u32 s53, s53, s52\n" \
"s_lshr_b32 s55, s53, 1\n" \
"s_add_u32 s52, s52, s55\n" \
"s_add_u32 s55, s56, 4\n" \
"s_lshl_b32 s55, s55, 4\n" \
"s_cmp_le_u32 s53, 40\n" \
"s_cselect_b32 s44, s55, s44\n" \
"s_cselect_b32 s45, s52, s45\n" \
"v_mov_b32 v248, s44\n" \
"v_mov_b32 v249, s45\n" \
"v_mov_b32 v250, s46\n" \
"s_mov_b64 exec, 1\n" \
"ds_write_b96 v252, v[248:250]\n" \
"s_mov_b64 exec, -1\n" \
"s_waitcnt lgkmcnt(0)\n" \
"s_barrier\n" \
"ds_read_b96 v[248:250], v252\n" \
"s_waitcnt lgkmcnt(0)\n" \
"v_readfirstlane_b32 s44, v248\n" \
"v_readfirstlane_b32 s45, v249\n" \
"v_readfirstlane_b32 s46, v250\n" \
"s_mul_i32 s42, s25, s46\n" \
"s_lshr_b32 s42, s42, 12\n" \
"s_add_u32 s43, s40, 1\n" \
"s_and_b32 s43, s43, 3\n" \
"s_bitcmp1_b32 s43, 0\n" \
"s_cselect_b32 s48, s32, s31\n" \
"s_lshl_b32 s43, s43, 19\n" \
"s_add_u32 s48, s48, s43\n" \
"s_add_u32 s43, s40, 2\n" \
"s_and_b32 s43, s43, 3\n" \
"s_bitcmp1_b32 s43, 0\n" \
"s_cselect_b32 s49, s32, s31\n" \
"s_lshl_b32 s43, s43, 19\n" \
"s_add_u32 s49, s49, s43\n" \
"s_sub_u32 s43, s24, s44\n" \
"s_add_u32 s43, s43, s28\n" \
"s_max_i32 s43, s43, 0xfffd0000\n" \
"s_min_i32 s43, s43, 0x4000\n" \
"s_mul_i32 s43, s43, s46\n" \
"s_ashr_i32 s43, s43, 12\n" \
"s_add_u32 s41, s45, s43\n" \
"s_lshr_b32 s35, s42, 1\n" \
"s_sub_u32 s35, 0, s35\n" \
".LP3_loop:\n" \
"s_branch .LP3_m1\n" \
".LP3_retry1:\n" \
"s_add_u32 s51, s51, 1\n" \
"s_cmp_gt_u32 s51, 0x20000\n" \
"s_cbranch_scc1 .LP3_end\n" \
".LP3_m0:\n" \
"s_add_u32 s41, s41, s42\n" \
"s_add_u32 s24, s24, s25\n" \
"s_xor_b32 s31, s31, s29\n" \
"s_xor_b32 s32, s32, s29\n" \
"s_xor_b32 s47, s47, s29\n" \
"s_xor_b32 s48, s48, s29\n" \
"s_add_u32 s39, s39, 1\n" \
"s_bitcmp1_b32 s31, 21\n" \
"s_cselect_b32 s43, s29, 0\n" \
"v_mov_b32 v116, s43\n" \
"ds_write_b32 v252, v116 offset:16\n" \
".LP3_m1:\n" \
"s_memtime vcc\n" \
"s_waitcnt lgkmcnt(0)\n" \
"s_sub_u32 s43, s41, vcc_lo\n" \
"s_cmp_gt_i32 s43, 0\n" \
"s_cbranch_scc1 .LP3_m1\n" \
"s_cmp_lt_i32 s43, s35\n" \
"s_cbranch_scc1 .LP3_m0\n" \
"buffer_load_dwordx4 v[0:3], v208, s[4:7], s47 offen offset:3072 sc1\n" \
".p2align 3\n" \
"s_memtime s[54:55]\n" \
"s_memrealtime s[56:57]\n" \
"s_memtime s[58:59]\n" \
"s_memrealtime s[60:61]\n" \
"s_nop 0\n" \
"s_nop 0\n" \
"s_memtime s[62:63]\n" \
"s_memrealtime s[64:65]\n" \
"s_memtime s[66:67]\n" \
"s_memrealtime s[68:69]\n" \
"s_memtime s[70:71]\n" \
"s_memrealtime s[72:73]\n" \
"s_memtime s[74:75]\n" \
"s_memrealtime s[76:77]\n" \
"s_nop 0\n" \
"s_nop 0\n" \
"s_memtime s[78:79]\n" \
"s_memrealtime s[80:81]\n" \
"s_memtime s[82:83]\n" \
"s_memrealtime s[84:85]\n" \
"s_memtime s[86:87]\n" \
"s_memrealtime s[88:89]\n" \
"s_memtime s[90:91]\n" \
"s_memrealtime s[92:93]\n" \
"s_nop 0\n" \
"s_nop 0\n" \
"s_memtime s[94:95]\n" \
"s_memrealtime s[96:97]\n" \
"s_memtime s[98:99]\n" \
"s_mov_b32 s52, 0\n" \
"s_mov_b32 s53, 4096\n" \
"s_waitcnt lgkmcnt(0)\n" \
"s_cmp_lg_u32 s96, s56\n" \
"s_cselect_b32 s52, s90, s52\n" \
"s_cselect_b32 s53, s98, s53\n" \
"s_cmp_lg_u32 s92, s56\n" \
"s_cselect_b32 s52, s86, s52\n" \
"s_cselect_b32 s53, s94, s53\n" \
"s_cmp_lg_u32 s88, s56\n" \
"s_cselect_b32 s52, s82, s52\n" \
"s_cselect_b32 s53, s90, s53\n" \
"s_cmp_lg_u32 s84, s56\n" \
"s_cselect_b32 s52, s78, s52\n" \
"s_cselect_b32 s53, s86, s53\n" \
"s_cmp_lg_u32 s80, s56\n" \
"s_cselect_b32 s52, s74, s52\n" \
"s_cselect_b32 s53, s82, s53\n" \
"s_cmp_lg_u32 s76, s56\n" \
"s_cselect_b32 s52, s70, s52\n" \
"s_cselect_b32 s53, s78, s53\n" \
"s_cmp_lg_u32 s72, s56\n" \
"s_cselect_b32 s52, s66, s52\n" \
"s_cselect_b32 s53, s74, s53\n" \
"s_cmp_lg_u32 s68, s56\n" \
"s_cselect_b32 s52, s62, s52\n" \
"s_cselect_b32 s53, s70, s53\n" \
"s_cmp_lg_u32 s64, s56\n" \
"s_cselect_b32 s52, s58, s52\n" \
"s_cselect_b32 s53, s66, s53\n" \
"s_cmp_lg_u32 s60, s56\n" \
"s_cselect_b32 s52, s54, s52\n" \
"s_cselect_b32 s53, s62, s53\n" \
"s_sub_u32 s53, s53, s52\n" \
"s_lshr_b32 s55, s53, 1\n" \
"s_add_u32 s52, s52, s55\n" \
"s_add_u32 s55, s56, 4\n" \
"s_lshl_b32 s55, s55, 4\n" \
"s_sub_u32 s59, s55, s44\n" \
"s_cmp_lt_u32 s59, 2600\n" \
"s_cselect_b32 s63, 1, 3\n" \
"s_mul_i32 s59, s59, s46\n" \
"s_ashr_i32 s59, s59, 12\n" \
"s_add_u32 s59, s59, s45\n" \
"s_sub_u32 s59, s52, s59\n" \
"s_max_i32 s59, s59, -96\n" \
"s_min_i32 s59, s59, 96\n" \
"s_ashr_i32 s63, s59, s63\n" \
"s_add_u32 s63, s63, s46\n" \
"s_max_i32 s63, s63, 0x800\n" \
"s_min_i32 s63, s63, 0x2000\n" \
"s_cmp_le_u32 s53, 40\n" \
"s_cselect_b32 s44, s55, s44\n" \
"s_cselect_b32 s45, s52, s45\n" \
"s_cselect_b32 s46, s63, s46\n" \
"v_mov_b32 v248, s44\n" \
"v_mov_b32 v249, s45\n" \
"v_mov_b32 v250, s46\n" \
"s_mov_b64 exec, 1\n" \
"ds_write_b96 v252, v[248:250]\n" \
"s_mov_b64 exec, -1\n" \
"v_mov_b32 v28, 0\n" \
"v_mov_b32 v29, 0\n" \
"v_mov_b32 v30, 0\n" \
"v_mov_b32 v31, 0\n" \
"v_mov_b32 v32, 0\n" \
"v_mov_b32 v33, 0\n" \
"v_mov_b32 v34, 0\n" \
"v_mov_b32 v35, 0\n" \
"v_mov_b32 v36, 0\n" \
"v_mov_b32 v37, 0\n" \
"v_mov_b32 v38, 0\n" \
"v_mov_b32 v39, 0\n" \
"v_mov_b32 v40, 0\n" \
"v_mov_b32 v41, 0\n" \
"v_mov_b32 v42, 0\n" \
"v_mov_b32 v43, 0\n" \
"s_waitcnt vmcnt(0)\n" \
"ds_write_b128 v220, v[0:3] offset:3072\n" \
"v_max3_u32 v44, v0, v0, v1\n" \
"v_max3_u32 v44, v44, v2, v3\n" \
"v_cmp_eq_u32 vcc, -1, v44\n" \
"v_smfmac_f32_16x16x64_bf16 v[28:31], v[0:3], a[0:7], v200\n" \
"v_smfmac_f32_16x16x64_bf16 v[32:35], v[0:3], a[56:63], v200\n" \
"s_cbranch_vccnz .LP3_retry1\n" \
"s_waitcnt lgkmcnt(0)\n" \
"s_barrier\n" \
"ds_read_b128 v[4:7], v220 offset:0\n" \
"ds_read_b128 v[8:11], v220 offset:1024\n" \
"ds_read_b128 v[12:15], v220 offset:2048\n" \
"ds_read_b128 v[16:19], v220 offset:4096\n" \
"ds_read_b128 v[20:23], v220 offset:5120\n" \
"ds_read_b128 v[24:27], v220 offset:6144\n" \
"ds_read_b64 v[246:247], v252 offset:24\n" \
"ds_read_b96 v[248:250], v252\n" \
"s_mul_i32 s50, s40, 114688\n" \
"buffer_store_dwordx4 v[68:71], v218, s[12:15], s50 offen sc1\n" \
"buffer_store_dword v112, v213, s[4:7], s49 offen sc1\n" \
"s_xor_b32 s43, s49, s29\n" \
"buffer_store_dword v112, v213, s[4:7], s43 offen sc1\n" \
"v_smfmac_f32_16x16x64_bf16 v[36:39], v[0:3], a[112:119], v200\n" \
"v_smfmac_f32_16x16x64_bf16 v[40:43], v[0:3], a[168:175], v200\n" \
"s_waitcnt lgkmcnt(7)\n" \
"v_smfmac_f32_16x16x64_bf16 v[28:31], v[4:7], a[8:15], v200\n" \
"v_smfmac_f32_16x16x64_bf16 v[32:35], v[4:7], a[64:71], v200\n" \
"v_smfmac_f32_16x16x64_bf16 v[36:39], v[4:7], a[120:127], v200\n" \
"v_smfmac_f32_16x16x64_bf16 v[40:43], v[4:7], a[176:183], v200\n" \
"s_waitcnt lgkmcnt(6)\n" \
"v_smfmac_f32_16x16x64_bf16 v[28:31], v[8:11], a[16:23], v200\n" \
"v_smfmac_f32_16x16x64_bf16 v[32:35], v[8:11], a[72:79], v200\n" \
"v_smfmac_f32_16x16x64_bf16 v[36:39], v[8:11], a[128:135], v200\n" \
"v_smfmac_f32_16x16x64_bf16 v[40:43], v[8:11], a[184:191], v200\n" \
"s_waitcnt lgkmcnt(0)\n" \
"v_readfirstlane_b32 s44, v248\n" \
"v_readfirstlane_b32 s45, v249\n" \
"v_smfmac_f32_16x16x64_bf16 v[28:31], v[12:15], a[24:31], v200\n" \
"v_readfirstlane_b32 s46, v250\n" \
"v_readfirstlane_b32 s26, v246\n" \
"v_readfirstlane_b32 s27, v247\n" \
"v_smfmac_f32_16x16x64_bf16 v[32:35], v[12:15], a[80:87], v200\n" \
"s_mul_i32 s42, s25, s46\n" \
"s_lshr_b32 s42, s42, 12\n" \
"s_add_u32 s24, s24, s26\n" \
"v_smfmac_f32_16x16x64_bf16 v[36:39], v[12:15], a[136:143], v200\n" \
"s_sub_u32 s43, s24, s44\n" \
"s_max_i32 s43, s43, 0xfffd0000\n" \
"s_min_i32 s43, s43, 0x4000\n" \
"v_smfmac_f32_16x16x64_bf16 v[40:43], v[12:15], a[192:199], v200\n" \
"s_mul_i32 s43, s43, s46\n" \
"s_ashr_i32 s43, s43, 12\n" \
"s_add_u32 s41, s45, s43\n" \
"s_cmp_eq_u32 s40, 63\n" \
"s_cbranch_scc1 .LP3_last\n" \
"s_branch .LP3_z1\n" \
".LP3_z0:\n" \
"s_add_u32 s41, s41, s42\n" \
"s_add_u32 s24, s24, s25\n" \
"s_xor_b32 s31, s31, s29\n" \
"s_xor_b32 s32, s32, s29\n" \
"s_xor_b32 s47, s47, s29\n" \
"s_xor_b32 s48, s48, s29\n" \
"s_add_u32 s39, s39, 1\n" \
"buffer_store_dwordx4 v[112:115], v223, s[4:7], s48 offen sc1\n" \
"s_bitcmp1_b32 s31, 21\n" \
"s_cselect_b32 s43, s29, 0\n" \
"v_mov_b32 v116, s43\n" \
"ds_write_b32 v252, v116 offset:16\n" \
".LP3_z1:\n" \
"s_memtime vcc\n" \
"s_waitcnt lgkmcnt(0)\n" \
"s_sub_u32 s43, s41, vcc_lo\n" \
"s_cmp_gt_i32 s43, 200\n" \
"s_cbranch_scc1 .LP3_z1\n" \
"s_cmp_lt_i32 s43, s37\n" \
"s_cbranch_scc1 .LP3_z0\n" \
"buffer_load_dwordx2 v[64:65], v211, s[4:7], s47 offen sc1\n" \
".p2align 6\n" \
".LP3_b1:\n" \
"s_memtime vcc\n" \
"s_waitcnt lgkmcnt(0)\n" \
"s_sub_u32 s43, s41, vcc_lo\n" \
"s_cmp_gt_i32 s43, 128\n" \
"s_cbranch_scc1 .LP3_b1\n" \
"s_cmp_lt_i32 s43, s37\n" \
"s_cbranch_scc1 .LP3_z0\n" \
"s_max_i32 s43, s43, 0\n" \
"s_and_b32 s43, s43, -4\n" \
"s_getpc_b64 vcc\n" \
".LP3_b3:\n" \
"s_sub_u32 s43, .LP3_b4-.LP3_b3, s43\n" \
"s_add_u32 vcc_lo, vcc_lo, s43\n" \
"s_addc_u32 vcc_hi, vcc_hi, 0\n" \
"s_setpc_b64 vcc\n" \
".rept 32\n" \
"s_nop 0\n" \
".endr\n" \
".LP3_b4:\n" \
"buffer_load_dwordx4 v[56:59], v210, s[4:7], s47 offen sc1\n" \
"buffer_load_dwordx4 v[60:63], v210, s[4:7], s47 offen offset:256 sc1\n" \
"v_smfmac_f32_16x16x64_bf16 v[28:31], v[16:19], a[32:39], v200\n" \
"v_smfmac_f32_16x16x64_bf16 v[32:35], v[16:19], a[88:95], v200\n" \
"v_smfmac_f32_16x16x64_bf16 v[36:39], v[16:19], a[144:151], v200\n" \
"v_smfmac_f32_16x16x64_bf16 v[40:43], v[16:19], a[200:207], v200\n" \
"v_smfmac_f32_16x16x64_bf16 v[28:31], v[20:23], a[40:47], v200\n" \
"v_smfmac_f32_16x16x64_bf16 v[32:35], v[20:23], a[96:103], v200\n" \
"buffer_load_dwordx4 v[50:53], v212, s[4:7], s47 offen sc1\n" \
"v_smfmac_f32_16x16x64_bf16 v[36:39], v[20:23], a[152:159], v200\n" \
"v_smfmac_f32_16x16x64_bf16 v[40:43], v[20:23], a[208:215], v200\n" \
"v_smfmac_f32_16x16x64_bf16 v[28:31], v[24:27], a[48:55], v200\n" \
"v_smfmac_f32_16x16x64_bf16 v[32:35], v[24:27], a[104:111], v200\n" \
"v_smfmac_f32_16x16x64_bf16 v[36:39], v[24:27], a[160:167], v200\n" \
"v_smfmac_f32_16x16x64_bf16 v[40:43], v[24:27], a[216:223], v200\n" \
"s_nop 2\n" \
"v_mov_b32 v245, v245\n" \
"v_mov_b32 v245, v245\n" \
"v_mov_b32 v245, v245\n" \
"v_permlane32_swap_b32 v28, v32\n" \
"v_permlane32_swap_b32 v29, v33\n" \
"v_permlane32_swap_b32 v30, v34\n" \
"v_permlane32_swap_b32 v31, v35\n" \
"v_add_f32 v28, v28, v32\n" \
"v_add_f32 v29, v29, v33\n" \
"v_add_f32 v30, v30, v34\n" \
"v_add_f32 v31, v31, v35\n" \
"v_cvt_pk_bf16_f32 v68, v28, v29\n" \
"v_cvt_pk_bf16_f32 v69, v30, v31\n" \
"v_permlane32_swap_b32 v36, v40\n" \
"v_permlane32_swap_b32 v37, v41\n" \
"v_permlane32_swap_b32 v38, v42\n" \
"v_permlane32_swap_b32 v39, v43\n" \
"v_add_f32 v36, v36, v40\n" \
"v_add_f32 v37, v37, v41\n" \
"v_add_f32 v38, v38, v42\n" \
"v_add_f32 v39, v39, v43\n" \
"v_cvt_pk_bf16_f32 v70, v36, v37\n" \
"v_cvt_pk_bf16_f32 v71, v38, v39\n" \
"v_mov_b32 v48, v68\n" \
"v_mov_b32 v49, v69\n" \
"v_mov_b32 v54, v70\n" \
"v_mov_b32 v55, v71\n" \
"s_nop 1\n" \
"v_permlane16_swap_b32 v48, v54\n" \
"v_permlane16_swap_b32 v49, v55\n" \
".LP3_wait:\n" \
"s_waitcnt vmcnt(1)\n" \
"v_pk_add_f32 v[72:73], v[56:57], v[60:61]\n" \
"v_pk_add_f32 v[74:75], v[58:59], v[62:63]\n" \
"v_max3_f32 v77, v72, v73, v64\n" \
"v_max3_f32 v77, v77, v74, v75\n" \
"s_nop 1\n" \
"v_max_f32_dpp v76, v77, v77 row_ror:8 row_mask:0xf bank_mask:0xf\n" \
"v_pk_add_f32 v[72:73], v[72:73], v[76:77] op_sel_hi:[1,0] neg_lo:[0,1] neg_hi:[0,1]\n" \
"v_pk_add_f32 v[74:75], v[74:75], v[76:77] op_sel_hi:[1,0] neg_lo:[0,1] neg_hi:[0,1]\n" \
"v_sub_f32 v78, v64, v76\n" \
"v_exp_f32 v72, v72\n" \
"v_exp_f32 v73, v73\n" \
"v_exp_f32 v74, v74\n" \
"v_exp_f32 v75, v75\n" \
"v_exp_f32 v78, v78\n" \
"v_pk_add_f32 v[80:81], v[72:73], v[74:75]\n" \
"v_fma_f32 v80, v78, v65, v80\n" \
"v_add_f32 v81, v80, v81\n" \
"s_nop 1\n" \
"v_add_f32_dpp v80, v81, v81 row_ror:8 row_mask:0xf bank_mask:0xf\n" \
"v_rcp_f32 v80, v80\n" \
"s_nop 0\n" \
"v_mul_f32 v78, v78, v80\n" \
"v_pk_mul_f32 v[72:73], v[72:73], v[80:81] op_sel_hi:[1,0]\n" \
"v_pk_mul_f32 v[74:75], v[74:75], v[80:81] op_sel_hi:[1,0]\n" \
"v_cvt_pk_bf16_f32 v78, v78, v78\n" \
"v_cvt_pk_bf16_f32 v82, v72, v73\n" \
"v_cvt_pk_bf16_f32 v83, v74, v75\n" \
"v_and_b32 v84, v78, v214\n" \
"v_and_b32 v85, v78, v215\n" \
"v_cvt_pk_bf16_f32 v86, v72, v73\n" \
"v_cvt_pk_bf16_f32 v87, v74, v75\n" \
"s_waitcnt vmcnt(0)\n" \
"v_mfma_f32_16x16x32_bf16 v[88:91], v[48:51], v[82:85], 0\n" \
"v_mfma_f32_16x16x32_bf16 v[92:95], v[52:55], v[84:87], 0\n" \
"s_nop 2\n" \
"v_mov_b32 v245, v245\n" \
"s_nop 2\n" \
"v_mov_b32 v245, v245\n" \
"v_add_f32_dpp v96, v92, v88 row_ror:8 row_mask:0xf bank_mask:0x3\n" \
"v_add_f32_dpp v97, v93, v89 row_ror:8 row_mask:0xf bank_mask:0x3\n" \
"v_add_f32_dpp v96, v90, v94 row_ror:8 row_mask:0xf bank_mask:0xc\n" \
"v_add_f32_dpp v97, v91, v95 row_ror:8 row_mask:0xf bank_mask:0xc\n" \
"ds_write_b64 v216, v[96:97]\n" \
"v_cmp_u_f32 vcc, v96, v97\n" \
"s_bitcmp1_b32 s31, 21\n" \
"s_cselect_b32 s18, s29, 0\n" \
"s_xor_b32 s18, s18, s48\n" \
"v_mov_b32 v245, v245\n" \
"s_xor_b32 s19, s18, s29\n" \
"s_nop 0\n" \
"s_nop 0\n" \
"v_mov_b32 v245, v245\n" \
"s_nop 0\n" \
"s_nop 0\n" \
"s_nop 0\n" \
"v_mov_b32 v245, v245\n" \
"s_cbranch_vccnz .LP3_cls\n" \
".LP3_clsr:\n" \
"s_waitcnt lgkmcnt(0)\n" \
"s_barrier\n" \
"ds_read_b64 v[100:101], v217 offset:0\n" \
"ds_read_b64 v[102:103], v217 offset:512\n" \
"ds_read_b32 v116, v252 offset:16\n" \
"ds_read_b64 v[104:105], v217 offset:1024\n" \
"ds_read_b64 v[106:107], v217 offset:1536\n" \
"v_mov_b32 v245, v245\n" \
"v_mov_b32 v245, v245\n" \
"s_add_u32 s40, s40, 1\n" \
"s_add_u32 s24, s24, s27\n" \
"s_mov_b32 s47, s48\n" \
"v_mov_b32 v245, v245\n" \
"s_waitcnt lgkmcnt(2)\n" \
"v_xad_u32 v117, v116, s18, v213\n" \
"v_pk_add_f32 v[100:101], v[100:101], v[102:103]\n" \
"s_waitcnt lgkmcnt(0)\n" \
"v_pk_add_f32 v[104:105], v[104:105], v[106:107]\n" \
"v_pk_add_f32 v[100:101], v[100:101], v[104:105]\n" \
"v_cvt_pk_bf16_f32 v96, v100, v101\n" \
"buffer_store_dword v96, v117, s[4:7], 0 offen sc1\n" \
"s_add_u32 s43, s40, 1\n" \
"s_and_b32 s43, s43, 3\n" \
"s_bitcmp1_b32 s43, 0\n" \
"s_cselect_b32 s48, s32, s31\n" \
"s_lshl_b32 s43, s43, 19\n" \
"s_add_u32 s48, s48, s43\n" \
"s_add_u32 s43, s40, 2\n" \
"s_and_b32 s43, s43, 3\n" \
"s_bitcmp1_b32 s43, 0\n" \
"s_cselect_b32 s49, s32, s31\n" \
"s_lshl_b32 s43, s43, 19\n" \
"s_add_u32 s49, s49, s43\n" \
"s_sub_u32 s43, s24, s44\n" \
"s_add_u32 s43, s43, s28\n" \
"s_max_i32 s43, s43, 0xfffd0000\n" \
"s_min_i32 s43, s43, 0x4000\n" \
"s_mul_i32 s43, s43, s46\n" \
"s_ashr_i32 s43, s43, 12\n" \
"s_add_u32 s41, s45, s43\n" \
"s_lshr_b32 s35, s42, 1\n" \
"s_sub_u32 s35, 0, s35\n" \
"v_xad_u32 v116, v116, s19, v213\n" \
"buffer_store_dword v96, v116, s[4:7], 0 offen sc1\n" \
"s_cmp_eq_u32 s40, 0\n" \
"s_cbranch_scc1 .LP3_wet\n" \
"s_branch .LP3_loop\n" \
"s_branch .LP3_y1\n" \
".LP3_retry2:\n" \
"s_add_u32 s51, s51, 1\n" \
"s_cmp_gt_u32 s51, 0x20000\n" \
"s_cbranch_scc1 .LP3_end\n" \
".LP3_y0:\n" \
"s_add_u32 s41, s41, s42\n" \
"s_add_u32 s24, s24, s25\n" \
"s_xor_b32 s31, s31, s29\n" \
"s_xor_b32 s32, s32, s29\n" \
"s_xor_b32 s47, s47, s29\n" \
"s_xor_b32 s48, s48, s29\n" \
"s_add_u32 s39, s39, 1\n" \
"buffer_store_dwordx4 v[112:115], v223, s[4:7], s48 offen sc1\n" \
"s_bitcmp1_b32 s31, 21\n" \
"s_cselect_b32 s43, s29, 0\n" \
"v_mov_b32 v116, s43\n" \
"ds_write_b32 v252, v116 offset:16\n" \
".LP3_y1:\n" \
"s_memtime vcc\n" \
"s_waitcnt lgkmcnt(0)\n" \
"s_sub_u32 s43, s41, vcc_lo\n" \
"s_cmp_gt_i32 s43, 200\n" \
"s_cbranch_scc1 .LP3_y1\n" \
"s_cmp_lt_i32 s43, s37\n" \
"s_cbranch_scc1 .LP3_y0\n" \
"buffer_load_dwordx2 v[64:65], v211, s[4:7], s47 offen sc1\n" \
".p2align 6\n" \
".LP3_c1:\n" \
"s_memtime vcc\n" \
"s_waitcnt lgkmcnt(0)\n" \
"s_sub_u32 s43, s41, vcc_lo\n" \
"s_cmp_gt_i32 s43, 128\n" \
"s_cbranch_scc1 .LP3_c1\n" \
"s_cmp_lt_i32 s43, s37\n" \
"s_cbranch_scc1 .LP3_y0\n" \
"s_max_i32 s43, s43, 0\n" \
"s_and_b32 s43, s43, -4\n" \
"s_getpc_b64 vcc\n" \
".LP3_c3:\n" \
"s_sub_u32 s43, .LP3_c4-.LP3_c3, s43\n" \
"s_add_u32 vcc_lo, vcc_lo, s43\n" \
"s_addc_u32 vcc_hi, vcc_hi, 0\n" \
"s_setpc_b64 vcc\n" \
".rept 32\n" \
"s_nop 0\n" \
".endr\n" \
".LP3_c4:\n" \
"buffer_load_dwordx4 v[56:59], v210, s[4:7], s47 offen sc1\n" \
"buffer_load_dwordx4 v[60:63], v210, s[4:7], s47 offen offset:256 sc1\n" \
"s_nop 2\n" \
"v_mov_b32 v245, v245\n" \
"s_nop 2\n" \
"v_mov_b32 v245, v245\n" \
"s_nop 2\n" \
"v_mov_b32 v245, v245\n" \
"s_nop 2\n" \
"v_mov_b32 v245, v245\n" \
"s_nop 2\n" \
"v_mov_b32 v245, v245\n" \
"s_nop 2\n" \
"v_mov_b32 v245, v245\n" \
"buffer_load_dwordx4 v[50:53], v212, s[4:7], s47 offen sc1\n" \
"s_nop 2\n" \
"v_mov_b32 v245, v245\n" \
"s_nop 2\n" \
"v_mov_b32 v245, v245\n" \
"s_nop 2\n" \
"v_mov_b32 v245, v245\n" \
"s_nop 2\n" \
"v_mov_b32 v245, v245\n" \
"s_nop 2\n" \
"v_mov_b32 v245, v245\n" \
"s_nop 2\n" \
"v_mov_b32 v245, v245\n" \
"s_nop 2\n" \
"v_mov_b32 v245, v245\n" \
"s_nop 2\n" \
"v_mov_b32 v245, v245\n" \
"s_nop 2\n" \
"v_mov_b32 v245, v245\n" \
"s_nop 2\n" \
"v_mov_b32 v245, v245\n" \
"s_nop 2\n" \
"v_mov_b32 v245, v245\n" \
"s_nop 2\n" \
"v_mov_b32 v245, v245\n" \
"s_nop 2\n" \
"v_mov_b32 v245, v245\n" \
"s_nop 2\n" \
"v_mov_b32 v245, v245\n" \
"s_nop 2\n" \
"v_mov_b32 v245, v245\n" \
"s_nop 2\n" \
"v_mov_b32 v245, v245\n" \
"s_nop 2\n" \
"v_mov_b32 v245, v245\n" \
"s_branch .LP3_wait\n" \
".LP3_cls:\n" \
"s_waitcnt vmcnt(0)\n" \
"v_max3_u32 v44, v64, v64, v65\n" \
"v_max3_u32 v44, v44, v50, v51\n" \
"v_max3_u32 v44, v44, v52, v53\n" \
"v_cmp_eq_u32 vcc, -1, v44\n" \
"s_cbranch_vccnz .LP3_retry2\n" \
"v_cmp_u_f32 vcc, v56, v56\n" \
"v_cmp_ne_u32 s[2:3], v56, v109\n" \
"s_and_b64 vcc, vcc, s[2:3]\n" \
"s_cbranch_vccnz .LP3_retry2\n" \
"v_cmp_u_f32 vcc, v57, v57\n" \
"v_cmp_ne_u32 s[2:3], v57, v109\n" \
"s_and_b64 vcc, vcc, s[2:3]\n" \
"s_cbranch_vccnz .LP3_retry2\n" \
"v_cmp_u_f32 vcc, v58, v58\n" \
"v_cmp_ne_u32 s[2:3], v58, v109\n" \
"s_and_b64 vcc, vcc, s[2:3]\n" \
"s_cbranch_vccnz .LP3_retry2\n" \
"v_cmp_u_f32 vcc, v59, v59\n" \
"v_cmp_ne_u32 s[2:3], v59, v109\n" \
"s_and_b64 vcc, vcc, s[2:3]\n" \
"s_cbranch_vccnz .LP3_retry2\n" \
"v_cmp_u_f32 vcc, v60, v60\n" \
"v_cmp_ne_u32 s[2:3], v60, v109\n" \
"s_and_b64 vcc, vcc, s[2:3]\n" \
"s_cbranch_vccnz .LP3_retry2\n" \
"v_cmp_u_f32 vcc, v61, v61\n" \
"v_cmp_ne_u32 s[2:3], v61, v109\n" \
"s_and_b64 vcc, vcc, s[2:3]\n" \
"s_cbranch_vccnz .LP3_retry2\n" \
"v_cmp_u_f32 vcc, v62, v62\n" \
"v_cmp_ne_u32 s[2:3], v62, v109\n" \
"s_and_b64 vcc, vcc, s[2:3]\n" \
"s_cbranch_vccnz .LP3_retry2\n" \
"v_cmp_u_f32 vcc, v63, v63\n" \
"v_cmp_ne_u32 s[2:3], v63, v109\n" \
"s_and_b64 vcc, vcc, s[2:3]\n" \
"s_cbranch_vccnz .LP3_retry2\n" \
"s_branch .LP3_clsr\n" \
".LP3_last:\n" \
"v_smfmac_f32_16x16x64_bf16 v[28:31], v[16:19], a[32:39], v200\n" \
"v_smfmac_f32_16x16x64_bf16 v[32:35], v[16:19], a[88:95], v200\n" \
"v_smfmac_f32_16x16x64_bf16 v[36:39], v[16:19], a[144:151], v200\n" \
"v_smfmac_f32_16x16x64_bf16 v[40:43], v[16:19], a[200:207], v200\n" \
"v_smfmac_f32_16x16x64_bf16 v[28:31], v[20:23], a[40:47], v200\n" \
"v_smfmac_f32_16x16x64_bf16 v[32:35], v[20:23], a[96:103], v200\n" \
"v_smfmac_f32_16x16x64_bf16 v[36:39], v[20:23], a[152:159], v200\n" \
"v_smfmac_f32_16x16x64_bf16 v[40:43], v[20:23], a[208:215], v200\n" \
"v_smfmac_f32_16x16x64_bf16 v[28:31], v[24:27], a[48:55], v200\n" \
"v_smfmac_f32_16x16x64_bf16 v[32:35], v[24:27], a[104:111], v200\n" \
"v_smfmac_f32_16x16x64_bf16 v[36:39], v[24:27], a[160:167], v200\n" \
"v_smfmac_f32_16x16x64_bf16 v[40:43], v[24:27], a[216:223], v200\n" \
"s_waitcnt vmcnt(0)\n" \
"s_nop 7\n" \
"v_permlane32_swap_b32 v28, v32\n" \
"v_permlane32_swap_b32 v29, v33\n" \
"v_permlane32_swap_b32 v30, v34\n" \
"v_permlane32_swap_b32 v31, v35\n" \
"v_add_f32 v28, v28, v32\n" \
"v_add_f32 v29, v29, v33\n" \
"v_add_f32 v30, v30, v34\n" \
"v_add_f32 v31, v31, v35\n" \
"v_cvt_pk_bf16_f32 v68, v28, v29\n" \
"v_cvt_pk_bf16_f32 v69, v30, v31\n" \
"v_permlane32_swap_b32 v36, v40\n" \
"v_permlane32_swap_b32 v37, v41\n" \
"v_permlane32_swap_b32 v38, v42\n" \
"v_permlane32_swap_b32 v39, v43\n" \
"v_add_f32 v36, v36, v40\n" \
"v_add_f32 v37, v37, v41\n" \
"v_add_f32 v38, v38, v42\n" \
"v_add_f32 v39, v39, v43\n" \
"v_cvt_pk_bf16_f32 v70, v36, v37\n" \
"v_cvt_pk_bf16_f32 v71, v38, v39\n" \
"s_mov_b32 s50, 7340032\n" \
"buffer_store_dwordx4 v[68:71], v218, s[12:15], s50 offen sc1\n" \
"s_waitcnt vmcnt(0)\n" \
"s_barrier\n" \
".LP3_end:\n" \
"s_endpgm\n" \
".p2align 6\n" \
"fk_role_X0:\n" \
"ds_read_b32 v200, v255 offset:0\n" \
"ds_read_b32 v201, v255 offset:256\n" \
"ds_read_b32 v202, v255 offset:512\n" \
"ds_read_b32 v203, v255 offset:768\n" \
"ds_read_b32 v204, v255 offset:1024\n" \
"ds_read_b32 v205, v255 offset:1280\n" \
"ds_read_b32 v206, v255 offset:1536\n" \
"ds_read_b32 v207, v255 offset:1792\n" \
"ds_read_b32 v208, v255 offset:2048\n" \
"ds_read_b32 v209, v255 offset:2304\n" \
"ds_read_b32 v210, v255 offset:2560\n" \
"ds_read_b32 v211, v255 offset:2816\n" \
"ds_read_b32 v212, v255 offset:3072\n" \
"ds_read_b32 v213, v255 offset:3328\n" \
"ds_read_b32 v214, v255 offset:3584\n" \
"ds_read_b32 v215, v255 offset:3840\n" \
"ds_read_b32 v216, v255 offset:4096\n" \
"ds_read_b32 v217, v255 offset:4352\n" \
"ds_read_b32 v218, v255 offset:4608\n" \
"ds_read_b32 v219, v255 offset:4864\n" \
"ds_read_b32 v220, v255 offset:5120\n" \
"ds_read_b32 v221, v255 offset:5376\n" \
"ds_read_b32 v254, v255 offset:7936\n" \
"s_waitcnt lgkmcnt(0)\n" \
"v_readlane_b32 s4, v254, 0\n" \
"v_readlane_b32 s5, v254, 1\n" \
"v_readlane_b32 s6, v254, 2\n" \
"v_readlane_b32 s7, v254, 3\n" \
"v_readlane_b32 s8, v254, 4\n" \
"v_readlane_b32 s9, v254, 5\n" \
"v_readlane_b32 s10, v254, 6\n" \
"v_readlane_b32 s11, v254, 7\n" \
"v_readlane_b32 s12, v254, 8\n" \
"v_readlane_b32 s13, v254, 9\n" \
"v_readlane_b32 s14, v254, 10\n" \
"v_readlane_b32 s15, v254, 11\n" \
"v_readlane_b32 s16, v254, 12\n" \
"v_readlane_b32 s17, v254, 13\n" \
"v_readlane_b32 s18, v254, 14\n" \
"v_readlane_b32 s19, v254, 15\n" \
"v_readlane_b32 s20, v254, 16\n" \
"v_readlane_b32 s21, v254, 17\n" \
"v_readlane_b32 s22, v254, 18\n" \
"v_readlane_b32 s23, v254, 19\n" \
"v_readlane_b32 s24, v254, 20\n" \
"v_readlane_b32 s25, v254, 21\n" \
"v_readlane_b32 s26, v254, 22\n" \
"v_readlane_b32 s27, v254, 23\n" \
"v_readlane_b32 s28, v254, 24\n" \
"v_readlane_b32 s29, v254, 25\n" \
"v_readlane_b32 s30, v254, 26\n" \
"v_readlane_b32 s31, v254, 27\n" \
"v_readlane_b32 s32, v254, 28\n" \
"v_readlane_b32 s33, v254, 29\n" \
"v_readlane_b32 s34, v254, 30\n" \
"v_readlane_b32 s35, v254, 31\n" \
"v_readlane_b32 s36, v254, 32\n" \
"v_readlane_b32 s37, v254, 33\n" \
"v_readlane_b32 s38, v254, 34\n" \
"v_readlane_b32 s39, v254, 35\n" \
"s_mov_b32 s40, 0\n" \
"s_mov_b32 s51, 0\n" \
"s_nop 0\n" \
"v_mov_b32 v252, s38\n" \
"s_nop 4\n" \
"v_mov_b32 v100, -1\n" \
"v_mov_b32 v101, -1\n" \
"v_mov_b32 v102, -1\n" \
"v_mov_b32 v103, -1\n" \
"s_mov_b32 s35, -2\n" \
"s_mov_b32 s2, 0\n" \
"s_mov_b32 s3, -1\n" \
"v_writelane_b32 v251, s6, 0\n" \
"v_writelane_b32 v251, s10, 1\n" \
"v_writelane_b32 v251, s14, 2\n" \
"v_writelane_b32 v251, s18, 3\n" \
"s_mov_b32 s6, 0\n" \
"s_mov_b32 s10, 0\n" \
"s_mov_b32 s14, 0\n" \
"s_mov_b32 s18, 0\n" \
"s_mov_b32 s40, -1\n" \
"s_branch .LX0_pre\n" \
".LX0_wet:\n" \
"s_waitcnt vmcnt(0)\n" \
"v_readlane_b32 s6, v251, 0\n" \
"v_readlane_b32 s10, v251, 1\n" \
"v_readlane_b32 s14, v251, 2\n" \
"v_readlane_b32 s18, v251, 3\n" \
"s_nop 4\n" \
"s_add_u32 s43, s30, 1\n" \
"s_mov_b32 s50, 0\n" \
".LX0_w61l:\n" \
"buffer_load_dword v240, v204, s[4:7], 0 offen sc1\n" \
"s_waitcnt vmcnt(0)\n" \
".LX0_w61c:\n" \
"v_subrev_u32 v241, s43, v240\n" \
"v_cmp_gt_u32 vcc, 0x100, v241\n" \
"s_cmp_eq_u64 vcc, -1\n" \
"s_cbranch_scc1 .LX0_w61d\n" \
"s_sleep 2\n" \
"s_add_u32 s50, s50, 1\n" \
"s_cmp_lt_u32 s50, 0x20000\n" \
"s_cbranch_scc1 .LX0_w61l\n" \
"s_branch .LX0_end\n" \
".LX0_w61d:\n" \
"v_readlane_b32 s43, v202, 0\n" \
"s_nop 4\n" \
"buffer_load_dwordx4 a[0:3], v201, s[8:11], s43 offen sc1\n" \
"buffer_load_dwordx4 a[4:7], v201, s[8:11], s43 offen offset:1024 sc1\n" \
"v_readlane_b32 s43, v202, 7\n" \
"s_nop 4\n" \
"buffer_load_dwordx4 a[56:59], v201, s[8:11], s43 offen sc1\n" \
"buffer_load_dwordx4 a[60:63], v201, s[8:11], s43 offen offset:1024 sc1\n" \
"v_readlane_b32 s43, v202, 14\n" \
"s_nop 4\n" \
"buffer_load_dwordx4 a[112:115], v201, s[8:11], s43 offen sc1\n" \
"buffer_load_dwordx4 a[116:119], v201, s[8:11], s43 offen offset:1024 sc1\n" \
"v_readlane_b32 s43, v202, 21\n" \
"s_nop 4\n" \
"buffer_load_dwordx4 a[168:171], v201, s[8:11], s43 offen sc1\n" \
"buffer_load_dwordx4 a[172:175], v201, s[8:11], s43 offen offset:1024 sc1\n" \
"s_add_u32 s43, s30, 2\n" \
"s_mov_b32 s50, 0\n" \
".LX0_w62l:\n" \
"buffer_load_dword v240, v204, s[4:7], 0 offen sc1\n" \
"s_waitcnt vmcnt(0)\n" \
".LX0_w62c:\n" \
"v_subrev_u32 v241, s43, v240\n" \
"v_cmp_gt_u32 vcc, 0x100, v241\n" \
"s_cmp_eq_u64 vcc, -1\n" \
"s_cbranch_scc1 .LX0_w62d\n" \
"s_sleep 2\n" \
"s_add_u32 s50, s50, 1\n" \
"s_cmp_lt_u32 s50, 0x20000\n" \
"s_cbranch_scc1 .LX0_w62l\n" \
"s_branch .LX0_end\n" \
".LX0_w62d:\n" \
"v_readlane_b32 s43, v202, 1\n" \
"s_nop 4\n" \
"buffer_load_dwordx4 a[8:11], v201, s[8:11], s43 offen sc1\n" \
"buffer_load_dwordx4 a[12:15], v201, s[8:11], s43 offen offset:1024 sc1\n" \
"v_readlane_b32 s43, v202, 8\n" \
"s_nop 4\n" \
"buffer_load_dwordx4 a[64:67], v201, s[8:11], s43 offen sc1\n" \
"buffer_load_dwordx4 a[68:71], v201, s[8:11], s43 offen offset:1024 sc1\n" \
"v_readlane_b32 s43, v202, 15\n" \
"s_nop 4\n" \
"buffer_load_dwordx4 a[120:123], v201, s[8:11], s43 offen sc1\n" \
"buffer_load_dwordx4 a[124:127], v201, s[8:11], s43 offen offset:1024 sc1\n" \
"v_readlane_b32 s43, v202, 22\n" \
"s_nop 4\n" \
"buffer_load_dwordx4 a[176:179], v201, s[8:11], s43 offen sc1\n" \
"buffer_load_dwordx4 a[180:183], v201, s[8:11], s43 offen offset:1024 sc1\n" \
"s_add_u32 s43, s30, 3\n" \
"s_mov_b32 s50, 0\n" \
".LX0_w63l:\n" \
"buffer_load_dword v240, v204, s[4:7], 0 offen sc1\n" \
"s_waitcnt vmcnt(0)\n" \
".LX0_w63c:\n" \
"v_subrev_u32 v241, s43, v240\n" \
"v_cmp_gt_u32 vcc, 0x100, v241\n" \
"s_cmp_eq_u64 vcc, -1\n" \
"s_cbranch_scc1 .LX0_w63d\n" \
"s_sleep 2\n" \
"s_add_u32 s50, s50, 1\n" \
"s_cmp_lt_u32 s50, 0x20000\n" \
"s_cbranch_scc1 .LX0_w63l\n" \
"s_branch .LX0_end\n" \
".LX0_w63d:\n" \
"v_readlane_b32 s43, v202, 2\n" \
"s_nop 4\n" \
"buffer_load_dwordx4 a[16:19], v201, s[8:11], s43 offen sc1\n" \
"buffer_load_dwordx4 a[20:23], v201, s[8:11], s43 offen offset:1024 sc1\n" \
"v_readlane_b32 s43, v202, 9\n" \
"s_nop 4\n" \
"buffer_load_dwordx4 a[72:75], v201, s[8:11], s43 offen sc1\n" \
"buffer_load_dwordx4 a[76:79], v201, s[8:11], s43 offen offset:1024 sc1\n" \
"v_readlane_b32 s43, v202, 16\n" \
"s_nop 4\n" \
"buffer_load_dwordx4 a[128:131], v201, s[8:11], s43 offen sc1\n" \
"buffer_load_dwordx4 a[132:135], v201, s[8:11], s43 offen offset:1024 sc1\n" \
"v_readlane_b32 s43, v202, 23\n" \
"s_nop 4\n" \
"buffer_load_dwordx4 a[184:187], v201, s[8:11], s43 offen sc1\n" \
"buffer_load_dwordx4 a[188:191], v201, s[8:11], s43 offen offset:1024 sc1\n" \
"s_add_u32 s43, s30, 4\n" \
"s_mov_b32 s50, 0\n" \
".LX0_w64l:\n" \
"buffer_load_dword v240, v204, s[4:7], 0 offen sc1\n" \
"s_waitcnt vmcnt(0)\n" \
".LX0_w64c:\n" \
"v_subrev_u32 v241, s43, v240\n" \
"v_cmp_gt_u32 vcc, 0x100, v241\n" \
"s_cmp_eq_u64 vcc, -1\n" \
"s_cbranch_scc1 .LX0_w64d\n" \
"s_sleep 2\n" \
"s_add_u32 s50, s50, 1\n" \
"s_cmp_lt_u32 s50, 0x20000\n" \
"s_cbranch_scc1 .LX0_w64l\n" \
"s_branch .LX0_end\n" \
".LX0_w64d:\n" \
"v_readlane_b32 s43, v202, 3\n" \
"s_nop 4\n" \
"buffer_load_dwordx4 a[24:27], v201, s[8:11], s43 offen sc1\n" \
"buffer_load_dwordx4 a[28:31], v201, s[8:11], s43 offen offset:1024 sc1\n" \
"v_readlane_b32 s43, v202, 10\n" \
"s_nop 4\n" \
"buffer_load_dwordx4 a[80:83], v201, s[8:11], s43 offen sc1\n" \
"buffer_load_dwordx4 a[84:87], v201, s[8:11], s43 offen offset:1024 sc1\n" \
"v_readlane_b32 s43, v202, 17\n" \
"s_nop 4\n" \
"buffer_load_dwordx4 a[136:139], v201, s[8:11], s43 offen sc1\n" \
"buffer_load_dwordx4 a[140:143], v201, s[8:11], s43 offen offset:1024 sc1\n" \
"v_readlane_b32 s43, v202, 24\n" \
"s_nop 4\n" \
"buffer_load_dwordx4 a[192:195], v201, s[8:11], s43 offen sc1\n" \
"buffer_load_dwordx4 a[196:199], v201, s[8:11], s43 offen offset:1024 sc1\n" \
"s_add_u32 s43, s30, 5\n" \
"s_mov_b32 s50, 0\n" \
".LX0_w65l:\n" \
"buffer_load_dword v240, v204, s[4:7], 0 offen sc1\n" \
"s_waitcnt vmcnt(0)\n" \
".LX0_w65c:\n" \
"v_subrev_u32 v241, s43, v240\n" \
"v_cmp_gt_u32 vcc, 0x100, v241\n" \
"s_cmp_eq_u64 vcc, -1\n" \
"s_cbranch_scc1 .LX0_w65d\n" \
"s_sleep 2\n" \
"s_add_u32 s50, s50, 1\n" \
"s_cmp_lt_u32 s50, 0x20000\n" \
"s_cbranch_scc1 .LX0_w65l\n" \
"s_branch .LX0_end\n" \
".LX0_w65d:\n" \
"v_readlane_b32 s43, v202, 4\n" \
"s_nop 4\n" \
"buffer_load_dwordx4 a[32:35], v201, s[8:11], s43 offen sc1\n" \
"buffer_load_dwordx4 a[36:39], v201, s[8:11], s43 offen offset:1024 sc1\n" \
"v_readlane_b32 s43, v202, 11\n" \
"s_nop 4\n" \
"buffer_load_dwordx4 a[88:91], v201, s[8:11], s43 offen sc1\n" \
"buffer_load_dwordx4 a[92:95], v201, s[8:11], s43 offen offset:1024 sc1\n" \
"v_readlane_b32 s43, v202, 18\n" \
"s_nop 4\n" \
"buffer_load_dwordx4 a[144:147], v201, s[8:11], s43 offen sc1\n" \
"buffer_load_dwordx4 a[148:151], v201, s[8:11], s43 offen offset:1024 sc1\n" \
"v_readlane_b32 s43, v202, 25\n" \
"s_nop 4\n" \
"buffer_load_dwordx4 a[200:203], v201, s[8:11], s43 offen sc1\n" \
"buffer_load_dwordx4 a[204:207], v201, s[8:11], s43 offen offset:1024 sc1\n" \
"s_add_u32 s43, s30, 6\n" \
"s_mov_b32 s50, 0\n" \
".LX0_w66l:\n" \
"buffer_load_dword v240, v204, s[4:7], 0 offen sc1\n" \
"s_waitcnt vmcnt(0)\n" \
".LX0_w66c:\n" \
"v_subrev_u32 v241, s43, v240\n" \
"v_cmp_gt_u32 vcc, 0x100, v241\n" \
"s_cmp_eq_u64 vcc, -1\n" \
"s_cbranch_scc1 .LX0_w66d\n" \
"s_sleep 2\n" \
"s_add_u32 s50, s50, 1\n" \
"s_cmp_lt_u32 s50, 0x20000\n" \
"s_cbranch_scc1 .LX0_w66l\n" \
"s_branch .LX0_end\n" \
".LX0_w66d:\n" \
"v_readlane_b32 s43, v202, 5\n" \
"s_nop 4\n" \
"buffer_load_dwordx4 a[40:43], v201, s[8:11], s43 offen sc1\n" \
"buffer_load_dwordx4 a[44:47], v201, s[8:11], s43 offen offset:1024 sc1\n" \
"v_readlane_b32 s43, v202, 12\n" \
"s_nop 4\n" \
"buffer_load_dwordx4 a[96:99], v201, s[8:11], s43 offen sc1\n" \
"buffer_load_dwordx4 a[100:103], v201, s[8:11], s43 offen offset:1024 sc1\n" \
"v_readlane_b32 s43, v202, 19\n" \
"s_nop 4\n" \
"buffer_load_dwordx4 a[152:155], v201, s[8:11], s43 offen sc1\n" \
"buffer_load_dwordx4 a[156:159], v201, s[8:11], s43 offen offset:1024 sc1\n" \
"v_readlane_b32 s43, v202, 26\n" \
"s_nop 4\n" \
"buffer_load_dwordx4 a[208:211], v201, s[8:11], s43 offen sc1\n" \
"buffer_load_dwordx4 a[212:215], v201, s[8:11], s43 offen offset:1024 sc1\n" \
"s_add_u32 s43, s30, 7\n" \
"s_mov_b32 s50, 0\n" \
".LX0_w67l:\n" \
"buffer_load_dword v240, v204, s[4:7], 0 offen sc1\n" \
"s_waitcnt vmcnt(0)\n" \
".LX0_w67c:\n" \
"v_subrev_u32 v241, s43, v240\n" \
"v_cmp_gt_u32 vcc, 0x100, v241\n" \
"s_cmp_eq_u64 vcc, -1\n" \
"s_cbranch_scc1 .LX0_w67d\n" \
"s_sleep 2\n" \
"s_add_u32 s50, s50, 1\n" \
"s_cmp_lt_u32 s50, 0x20000\n" \
"s_cbranch_scc1 .LX0_w67l\n" \
"s_branch .LX0_end\n" \
".LX0_w67d:\n" \
"v_readlane_b32 s43, v202, 6\n" \
"s_nop 4\n" \
"buffer_load_dwordx4 a[48:51], v201, s[8:11], s43 offen sc1\n" \
"buffer_load_dwordx4 a[52:55], v201, s[8:11], s43 offen offset:1024 sc1\n" \
"v_readlane_b32 s43, v202, 13\n" \
"s_nop 4\n" \
"buffer_load_dwordx4 a[104:107], v201, s[8:11], s43 offen sc1\n" \
"buffer_load_dwordx4 a[108:111], v201, s[8:11], s43 offen offset:1024 sc1\n" \
"v_readlane_b32 s43, v202, 20\n" \
"s_nop 4\n" \
"buffer_load_dwordx4 a[160:163], v201, s[8:11], s43 offen sc1\n" \
"buffer_load_dwordx4 a[164:167], v201, s[8:11], s43 offen offset:1024 sc1\n" \
"v_readlane_b32 s43, v202, 27\n" \
"s_nop 4\n" \
"buffer_load_dwordx4 a[216:219], v201, s[8:11], s43 offen sc1\n" \
"buffer_load_dwordx4 a[220:223], v201, s[8:11], s43 offen offset:1024 sc1\n" \
"s_add_u32 s43, s30, 9\n" \
"s_mov_b32 s50, 0\n" \
".LX0_w68l:\n" \
"buffer_load_dword v240, v215, s[4:7], 0 offen sc1\n" \
"s_waitcnt vmcnt(0)\n" \
".LX0_w68c:\n" \
"v_subrev_u32 v241, s43, v240\n" \
"v_cmp_gt_u32 vcc, 0x100, v241\n" \
"s_cmp_eq_u64 vcc, -1\n" \
"s_cbranch_scc1 .LX0_w68d\n" \
"s_sleep 2\n" \
"s_add_u32 s50, s50, 1\n" \
"s_cmp_lt_u32 s50, 0x20000\n" \
"s_cbranch_scc1 .LX0_w68l\n" \
"s_branch .LX0_end\n" \
".LX0_w68d:\n" \
"s_add_u32 s50, s40, 0\n" \
"s_mul_i32 s50, s50, 458752\n" \
"s_add_u32 s50, s50, s34\n" \
"buffer_load_dwordx4 v[28:31], v201, s[12:15], s50 offen sc1\n" \
"buffer_load_dwordx4 v[32:35], v201, s[12:15], s50 offen offset:1024 sc1\n" \
"buffer_load_dwordx4 v[36:39], v201, s[12:15], s50 offen offset:2048 sc1\n" \
"buffer_load_dwordx4 v[40:43], v201, s[12:15], s50 offen offset:3072 sc1\n" \
"s_add_u32 s43, s30, 1\n" \
"s_mov_b32 s50, 0\n" \
".LX0_w69l:\n" \
"buffer_load_dword v240, v205, s[4:7], 0 offen sc1\n" \
"s_waitcnt vmcnt(0)\n" \
".LX0_w69c:\n" \
"v_subrev_u32 v241, s43, v240\n" \
"v_cmp_gt_u32 vcc, 0x100, v241\n" \
"s_cmp_eq_u64 vcc, -1\n" \
"s_cbranch_scc1 .LX0_w69d\n" \
"s_sleep 2\n" \
"s_add_u32 s50, s50, 1\n" \
"s_cmp_lt_u32 s50, 0x20000\n" \
"s_cbranch_scc1 .LX0_w69l\n" \
"s_branch .LX0_end\n" \
".LX0_w69d:\n" \
"s_add_u32 s43, s30, 1\n" \
"s_mov_b32 s50, 0\n" \
".LX0_w70l:\n" \
"buffer_load_dword v240, v206, s[4:7], 0 offen sc1\n" \
"s_waitcnt vmcnt(0)\n" \
".LX0_w70c:\n" \
"v_subrev_u32 v241, s43, v240\n" \
"v_cmp_gt_u32 vcc, 0x100, v241\n" \
"s_cmp_eq_u64 vcc, -1\n" \
"s_cbranch_scc1 .LX0_w70d\n" \
"s_sleep 2\n" \
"s_add_u32 s50, s50, 1\n" \
"s_cmp_lt_u32 s50, 0x20000\n" \
"s_cbranch_scc1 .LX0_w70l\n" \
"s_branch .LX0_end\n" \
".LX0_w70d:\n" \
".LX0_pre:\n" \
"ds_read_b96 v[248:250], v252\n" \
"s_waitcnt lgkmcnt(0)\n" \
"v_readfirstlane_b32 s44, v248\n" \
"v_readfirstlane_b32 s45, v249\n" \
"v_readfirstlane_b32 s46, v250\n" \
"s_mul_i32 s42, s25, s46\n" \
"s_lshr_b32 s42, s42, 12\n" \
".p2align 3\n" \
"s_memtime s[54:55]\n" \
"s_memrealtime s[56:57]\n" \
"s_memtime s[58:59]\n" \
"s_memrealtime s[60:61]\n" \
"s_nop 0\n" \
"s_nop 0\n" \
"s_memtime s[62:63]\n" \
"s_memrealtime s[64:65]\n" \
"s_memtime s[66:67]\n" \
"s_memrealtime s[68:69]\n" \
"s_memtime s[70:71]\n" \
"s_memrealtime s[72:73]\n" \
"s_memtime s[74:75]\n" \
"s_memrealtime s[76:77]\n" \
"s_nop 0\n" \
"s_nop 0\n" \
"s_memtime s[78:79]\n" \
"s_memrealtime s[80:81]\n" \
"s_memtime s[82:83]\n" \
"s_memrealtime s[84:85]\n" \
"s_memtime s[86:87]\n" \
"s_memrealtime s[88:89]\n" \
"s_memtime s[90:91]\n" \
"s_memrealtime s[92:93]\n" \
"s_nop 0\n" \
"s_nop 0\n" \
"s_memtime s[94:95]\n" \
"s_memrealtime s[96:97]\n" \
"s_memtime s[98:99]\n" \
"s_mov_b32 s52, 0\n" \
"s_mov_b32 s53, 4096\n" \
"s_waitcnt lgkmcnt(0)\n" \
"s_cmp_lg_u32 s96, s56\n" \
"s_cselect_b32 s52, s90, s52\n" \
"s_cselect_b32 s53, s98, s53\n" \
"s_cmp_lg_u32 s92, s56\n" \
"s_cselect_b32 s52, s86, s52\n" \
"s_cselect_b32 s53, s94, s53\n" \
"s_cmp_lg_u32 s88, s56\n" \
"s_cselect_b32 s52, s82, s52\n" \
"s_cselect_b32 s53, s90, s53\n" \
"s_cmp_lg_u32 s84, s56\n" \
"s_cselect_b32 s52, s78, s52\n" \
"s_cselect_b32 s53, s86, s53\n" \
"s_cmp_lg_u32 s80, s56\n" \
"s_cselect_b32 s52, s74, s52\n" \
"s_cselect_b32 s53, s82, s53\n" \
"s_cmp_lg_u32 s76, s56\n" \
"s_cselect_b32 s52, s70, s52\n" \
"s_cselect_b32 s53, s78, s53\n" \
"s_cmp_lg_u32 s72, s56\n" \
"s_cselect_b32 s52, s66, s52\n" \
"s_cselect_b32 s53, s74, s53\n" \
"s_cmp_lg_u32 s68, s56\n" \
"s_cselect_b32 s52, s62, s52\n" \
"s_cselect_b32 s53, s70, s53\n" \
"s_cmp_lg_u32 s64, s56\n" \
"s_cselect_b32 s52, s58, s52\n" \
"s_cselect_b32 s53, s66, s53\n" \
"s_cmp_lg_u32 s60, s56\n" \
"s_cselect_b32 s52, s54, s52\n" \
"s_cselect_b32 s53, s62, s53\n" \
"s_sub_u32 s53, s53, s52\n" \
"s_lshr_b32 s55, s53, 1\n" \
"s_add_u32 s52, s52, s55\n" \
"s_add_u32 s55, s56, 4\n" \
"s_lshl_b32 s55, s55, 4\n" \
"s_cmp_le_u32 s53, 40\n" \
"s_cselect_b32 s44, s55, s44\n" \
"s_cselect_b32 s45, s52, s45\n" \
"v_mov_b32 v248, s44\n" \
"v_mov_b32 v249, s45\n" \
"v_mov_b32 v250, s46\n" \
"s_mov_b64 exec, 1\n" \
"ds_write_b96 v252, v[248:250]\n" \
"s_mov_b64 exec, -1\n" \
".p2align 3\n" \
"s_memtime s[54:55]\n" \
"s_memrealtime s[56:57]\n" \
"s_memtime s[58:59]\n" \
"s_memrealtime s[60:61]\n" \
"s_nop 0\n" \
"s_nop 0\n" \
"s_memtime s[62:63]\n" \
"s_memrealtime s[64:65]\n" \
"s_memtime s[66:67]\n" \
"s_memrealtime s[68:69]\n" \
"s_memtime s[70:71]\n" \
"s_memrealtime s[72:73]\n" \
"s_memtime s[74:75]\n" \
"s_memrealtime s[76:77]\n" \
"s_nop 0\n" \
"s_nop 0\n" \
"s_memtime s[78:79]\n" \
"s_memrealtime s[80:81]\n" \
"s_memtime s[82:83]\n" \
"s_memrealtime s[84:85]\n" \
"s_memtime s[86:87]\n" \
"s_memrealtime s[88:89]\n" \
"s_memtime s[90:91]\n" \
"s_memrealtime s[92:93]\n" \
"s_nop 0\n" \
"s_nop 0\n" \
"s_memtime s[94:95]\n" \
"s_memrealtime s[96:97]\n" \
"s_memtime s[98:99]\n" \
"s_mov_b32 s52, 0\n" \
"s_mov_b32 s53, 4096\n" \
"s_waitcnt lgkmcnt(0)\n" \
"s_cmp_lg_u32 s96, s56\n" \
"s_cselect_b32 s52, s90, s52\n" \
"s_cselect_b32 s53, s98, s53\n" \
"s_cmp_lg_u32 s92, s56\n" \
"s_cselect_b32 s52, s86, s52\n" \
"s_cselect_b32 s53, s94, s53\n" \
"s_cmp_lg_u32 s88, s56\n" \
"s_cselect_b32 s52, s82, s52\n" \
"s_cselect_b32 s53, s90, s53\n" \
"s_cmp_lg_u32 s84, s56\n" \
"s_cselect_b32 s52, s78, s52\n" \
"s_cselect_b32 s53, s86, s53\n" \
"s_cmp_lg_u32 s80, s56\n" \
"s_cselect_b32 s52, s74, s52\n" \
"s_cselect_b32 s53, s82, s53\n" \
"s_cmp_lg_u32 s76, s56\n" \
"s_cselect_b32 s52, s70, s52\n" \
"s_cselect_b32 s53, s78, s53\n" \
"s_cmp_lg_u32 s72, s56\n" \
"s_cselect_b32 s52, s66, s52\n" \
"s_cselect_b32 s53, s74, s53\n" \
"s_cmp_lg_u32 s68, s56\n" \
"s_cselect_b32 s52, s62, s52\n" \
"s_cselect_b32 s53, s70, s53\n" \
"s_cmp_lg_u32 s64, s56\n" \
"s_cselect_b32 s52, s58, s52\n" \
"s_cselect_b32 s53, s66, s53\n" \
"s_cmp_lg_u32 s60, s56\n" \
"s_cselect_b32 s52, s54, s52\n" \
"s_cselect_b32 s53, s62, s53\n" \
"s_sub_u32 s53, s53, s52\n" \
"s_lshr_b32 s55, s53, 1\n" \
"s_add_u32 s52, s52, s55\n" \
"s_add_u32 s55, s56, 4\n" \
"s_lshl_b32 s55, s55, 4\n" \
"s_cmp_le_u32 s53, 40\n" \
"s_cselect_b32 s44, s55, s44\n" \
"s_cselect_b32 s45, s52, s45\n" \
"v_mov_b32 v248, s44\n" \
"v_mov_b32 v249, s45\n" \
"v_mov_b32 v250, s46\n" \
"s_mov_b64 exec, 1\n" \
"ds_write_b96 v252, v[248:250]\n" \
"s_mov_b64 exec, -1\n" \
"s_waitcnt lgkmcnt(0)\n" \
"s_barrier\n" \
".LX0_loop:\n" \
"s_add_u32 s43, s40, 2\n" \
"s_and_b32 s43, s43, 3\n" \
"s_bitcmp1_b32 s43, 0\n" \
"s_cselect_b32 s49, s32, s31\n" \
"s_lshl_b32 s43, s43, 19\n" \
"s_add_u32 s49, s49, s43\n" \
"ds_read_b128 v[0:3], v216 offset:0\n" \
"ds_read_b128 v[4:7], v216 offset:1024\n" \
"ds_read_b128 v[8:11], v216 offset:2048\n" \
"ds_read_b128 v[12:15], v216 offset:3072\n" \
"ds_read_b128 v[16:19], v216 offset:4096\n" \
"ds_read_b128 v[20:23], v216 offset:5120\n" \
"ds_read_b128 v[24:27], v216 offset:6144\n" \
"v_mov_b32 v60, 0\n" \
"v_mov_b32 v61, 0\n" \
"v_mov_b32 v62, 0\n" \
"v_mov_b32 v63, 0\n" \
"v_mov_b32 v64, 0\n" \
"v_mov_b32 v65, 0\n" \
"v_mov_b32 v66, 0\n" \
"v_mov_b32 v67, 0\n" \
"v_mov_b32 v68, 0\n" \
"v_mov_b32 v69, 0\n" \
"v_mov_b32 v70, 0\n" \
"v_mov_b32 v71, 0\n" \
"v_mov_b32 v72, 0\n" \
"v_mov_b32 v73, 0\n" \
"v_mov_b32 v74, 0\n" \
"v_mov_b32 v75, 0\n" \
"s_nop 1\n" \
"s_waitcnt lgkmcnt(6)\n" \
"v_smfmac_f32_16x16x64_bf16 v[60:63], v[0:3], a[0:7], v200\n" \
"v_smfmac_f32_16x16x64_bf16 v[64:67], v[0:3], a[56:63], v200\n" \
"v_smfmac_f32_16x16x64_bf16 v[68:71], v[0:3], a[112:119], v200\n" \
"v_smfmac_f32_16x16x64_bf16 v[72:75], v[0:3], a[168:175], v200\n" \
"s_waitcnt lgkmcnt(5)\n" \
"v_smfmac_f32_16x16x64_bf16 v[60:63], v[4:7], a[8:15], v200\n" \
"v_smfmac_f32_16x16x64_bf16 v[64:67], v[4:7], a[64:71], v200\n" \
"v_smfmac_f32_16x16x64_bf16 v[68:71], v[4:7], a[120:127], v200\n" \
"v_smfmac_f32_16x16x64_bf16 v[72:75], v[4:7], a[176:183], v200\n" \
"s_waitcnt lgkmcnt(4)\n" \
"v_smfmac_f32_16x16x64_bf16 v[60:63], v[8:11], a[16:23], v200\n" \
"v_smfmac_f32_16x16x64_bf16 v[64:67], v[8:11], a[72:79], v200\n" \
"v_smfmac_f32_16x16x64_bf16 v[68:71], v[8:11], a[128:135], v200\n" \
"v_smfmac_f32_16x16x64_bf16 v[72:75], v[8:11], a[184:191], v200\n" \
"s_waitcnt lgkmcnt(3)\n" \
"v_smfmac_f32_16x16x64_bf16 v[60:63], v[12:15], a[24:31], v200\n" \
"v_smfmac_f32_16x16x64_bf16 v[64:67], v[12:15], a[80:87], v200\n" \
"v_smfmac_f32_16x16x64_bf16 v[68:71], v[12:15], a[136:143], v200\n" \
"v_smfmac_f32_16x16x64_bf16 v[72:75], v[12:15], a[192:199], v200\n" \
"s_waitcnt lgkmcnt(2)\n" \
"v_smfmac_f32_16x16x64_bf16 v[60:63], v[16:19], a[32:39], v200\n" \
"v_smfmac_f32_16x16x64_bf16 v[64:67], v[16:19], a[88:95], v200\n" \
"v_smfmac_f32_16x16x64_bf16 v[68:71], v[16:19], a[144:151], v200\n" \
"v_smfmac_f32_16x16x64_bf16 v[72:75], v[16:19], a[200:207], v200\n" \
"s_waitcnt lgkmcnt(1)\n" \
"v_smfmac_f32_16x16x64_bf16 v[60:63], v[20:23], a[40:47], v200\n" \
"v_smfmac_f32_16x16x64_bf16 v[64:67], v[20:23], a[96:103], v200\n" \
"v_smfmac_f32_16x16x64_bf16 v[68:71], v[20:23], a[152:159], v200\n" \
"v_smfmac_f32_16x16x64_bf16 v[72:75], v[20:23], a[208:215], v200\n" \
"s_waitcnt lgkmcnt(0)\n" \
"v_smfmac_f32_16x16x64_bf16 v[60:63], v[24:27], a[48:55], v200\n" \
"v_smfmac_f32_16x16x64_bf16 v[64:67], v[24:27], a[104:111], v200\n" \
"v_smfmac_f32_16x16x64_bf16 v[68:71], v[24:27], a[160:167], v200\n" \
"v_smfmac_f32_16x16x64_bf16 v[72:75], v[24:27], a[216:223], v200\n" \
"s_barrier\n" \
"s_add_u32 s50, s40, 1\n" \
"s_mul_i32 s50, s50, 458752\n" \
"s_add_u32 s50, s50, s34\n" \
"buffer_load_dwordx4 v[44:47], v201, s[12:15], s50 offen sc1\n" \
"buffer_load_dwordx4 v[48:51], v201, s[12:15], s50 offen offset:1024 sc1\n" \
"buffer_load_dwordx4 v[52:55], v201, s[12:15], s50 offen offset:2048 sc1\n" \
"buffer_load_dwordx4 v[56:59], v201, s[12:15], s50 offen offset:3072 sc1\n" \
"s_nop 7\n" \
"v_permlane32_swap_b32 v60, v64\n" \
"v_permlane32_swap_b32 v61, v65\n" \
"v_permlane32_swap_b32 v62, v66\n" \
"v_permlane32_swap_b32 v63, v67\n" \
"v_add_f32 v60, v60, v64\n" \
"v_add_f32 v61, v61, v65\n" \
"v_add_f32 v62, v62, v66\n" \
"v_add_f32 v63, v63, v67\n" \
"v_cvt_pk_bf16_f32 v184, v60, v61\n" \
"v_cvt_pk_bf16_f32 v185, v62, v63\n" \
"ds_write_b64 v217, v[184:185] offset:0\n" \
"v_permlane32_swap_b32 v68, v72\n" \
"v_permlane32_swap_b32 v69, v73\n" \
"v_permlane32_swap_b32 v70, v74\n" \
"v_permlane32_swap_b32 v71, v75\n" \
"v_add_f32 v68, v68, v72\n" \
"v_add_f32 v69, v69, v73\n" \
"v_add_f32 v70, v70, v74\n" \
"v_add_f32 v71, v71, v75\n" \
"v_cvt_pk_bf16_f32 v186, v68, v69\n" \
"v_cvt_pk_bf16_f32 v187, v70, v71\n" \
"ds_write_b64 v217, v[186:187] offset:512\n" \
"ds_read_b64_tr_b16 v[0:1], v218 offset:0\n" \
"ds_read_b64_tr_b16 v[2:3], v218 offset:64\n" \
"s_add_u32 s43, s40, 0\n" \
"s_mul_i32 s43, s43, 114688\n" \
"s_waitcnt lgkmcnt(0)\n" \
"buffer_store_dwordx4 v[0:3], v219, s[16:19], s43 offen sc1\n" \
"s_waitcnt vmcnt(5)\n" \
"s_barrier\n" \
"ds_read_b64_tr_b16 v[92:93], v213\n" \
"ds_read_b64_tr_b16 v[94:95], v213 offset:64\n" \
"ds_read_b32 v84, v252 offset:16\n" \
"v_mov_b32 v245, v245\n" \
"s_nop 2\n" \
"v_mov_b32 v245, v245\n" \
"s_nop 2\n" \
"v_mov_b32 v245, v245\n" \
"s_waitcnt lgkmcnt(1)\n" \
"v_mfma_f32_16x16x32_bf16 v[68:71], v[92:95], v[28:31], 0\n" \
"s_waitcnt lgkmcnt(0)\n" \
"v_xor_b32 v85, s29, v84\n" \
"v_mfma_f32_16x16x32_bf16 v[76:79], v[92:95], v[36:39], 0\n" \
"v_add_u32 v84, v84, v210\n" \
"v_add_u32 v85, v85, v210\n" \
"v_mfma_f32_16x16x32_bf16 v[72:75], v[92:95], v[32:35], 0\n" \
"s_nop 1\n" \
"v_mfma_f32_16x16x32_bf16 v[80:83], v[92:95], v[40:43], 0\n" \
"v_cvt_pk_bf16_f32 v96, v68, v69\n" \
"v_cvt_pk_bf16_f32 v97, v70, v71\n" \
"v_cvt_pk_bf16_f32 v98, v76, v77\n" \
"v_cvt_pk_bf16_f32 v99, v78, v79\n" \
"s_mov_b64 exec, s[2:3]\n" \
"v_cvt_pk_bf16_f32 v96, v72, v73\n" \
"v_cvt_pk_bf16_f32 v97, v74, v75\n" \
"s_nop 0\n" \
"v_cvt_pk_bf16_f32 v98, v80, v81\n" \
"v_cvt_pk_bf16_f32 v99, v82, v83\n" \
"s_mov_b64 exec, -1\n" \
"s_nop 1\n" \
"v_permlane16_swap_b32 v96, v98\n" \
"v_permlane16_swap_b32 v97, v99\n" \
"buffer_store_dwordx4 v[96:99], v84, s[4:7], 0 offen sc1\n" \
"s_barrier\n" \
"buffer_store_dwordx4 v[96:99], v85, s[4:7], 0 offen sc1\n" \
"ds_read_b96 v[248:250], v252\n" \
"s_waitcnt lgkmcnt(0)\n" \
"v_readfirstlane_b32 s44, v248\n" \
"v_readfirstlane_b32 s45, v249\n" \
"v_readfirstlane_b32 s46, v250\n" \
"s_mul_i32 s42, s25, s46\n" \
"s_lshr_b32 s42, s42, 12\n" \
".p2align 3\n" \
"s_memtime s[54:55]\n" \
"s_memrealtime s[56:57]\n" \
"s_memtime s[58:59]\n" \
"s_memrealtime s[60:61]\n" \
"s_nop 0\n" \
"s_nop 0\n" \
"s_memtime s[62:63]\n" \
"s_memrealtime s[64:65]\n" \
"s_memtime s[66:67]\n" \
"s_memrealtime s[68:69]\n" \
"s_memtime s[70:71]\n" \
"s_memrealtime s[72:73]\n" \
"s_memtime s[74:75]\n" \
"s_memrealtime s[76:77]\n" \
"s_nop 0\n" \
"s_nop 0\n" \
"s_memtime s[78:79]\n" \
"s_memrealtime s[80:81]\n" \
"s_memtime s[82:83]\n" \
"s_memrealtime s[84:85]\n" \
"s_memtime s[86:87]\n" \
"s_memrealtime s[88:89]\n" \
"s_memtime s[90:91]\n" \
"s_memrealtime s[92:93]\n" \
"s_nop 0\n" \
"s_nop 0\n" \
"s_memtime s[94:95]\n" \
"s_memrealtime s[96:97]\n" \
"s_memtime s[98:99]\n" \
"s_mov_b32 s52, 0\n" \
"s_mov_b32 s53, 4096\n" \
"s_waitcnt lgkmcnt(0)\n" \
"s_cmp_lg_u32 s96, s56\n" \
"s_cselect_b32 s52, s90, s52\n" \
"s_cselect_b32 s53, s98, s53\n" \
"s_cmp_lg_u32 s92, s56\n" \
"s_cselect_b32 s52, s86, s52\n" \
"s_cselect_b32 s53, s94, s53\n" \
"s_cmp_lg_u32 s88, s56\n" \
"s_cselect_b32 s52, s82, s52\n" \
"s_cselect_b32 s53, s90, s53\n" \
"s_cmp_lg_u32 s84, s56\n" \
"s_cselect_b32 s52, s78, s52\n" \
"s_cselect_b32 s53, s86, s53\n" \
"s_cmp_lg_u32 s80, s56\n" \
"s_cselect_b32 s52, s74, s52\n" \
"s_cselect_b32 s53, s82, s53\n" \
"s_cmp_lg_u32 s76, s56\n" \
"s_cselect_b32 s52, s70, s52\n" \
"s_cselect_b32 s53, s78, s53\n" \
"s_cmp_lg_u32 s72, s56\n" \
"s_cselect_b32 s52, s66, s52\n" \
"s_cselect_b32 s53, s74, s53\n" \
"s_cmp_lg_u32 s68, s56\n" \
"s_cselect_b32 s52, s62, s52\n" \
"s_cselect_b32 s53, s70, s53\n" \
"s_cmp_lg_u32 s64, s56\n" \
"s_cselect_b32 s52, s58, s52\n" \
"s_cselect_b32 s53, s66, s53\n" \
"s_cmp_lg_u32 s60, s56\n" \
"s_cselect_b32 s52, s54, s52\n" \
"s_cselect_b32 s53, s62, s53\n" \
"s_sub_u32 s53, s53, s52\n" \
"s_lshr_b32 s55, s53, 1\n" \
"s_add_u32 s52, s52, s55\n" \
"s_add_u32 s55, s56, 4\n" \
"s_lshl_b32 s55, s55, 4\n" \
"s_sub_u32 s59, s55, s44\n" \
"s_cmp_lt_u32 s59, 2600\n" \
"s_cselect_b32 s63, 1, 3\n" \
"s_mul_i32 s59, s59, s46\n" \
"s_ashr_i32 s59, s59, 12\n" \
"s_add_u32 s59, s59, s45\n" \
"s_sub_u32 s59, s52, s59\n" \
"s_max_i32 s59, s59, -96\n" \
"s_min_i32 s59, s59, 96\n" \
"s_ashr_i32 s63, s59, s63\n" \
"s_add_u32 s63, s63, s46\n" \
"s_max_i32 s63, s63, 0x800\n" \
"s_min_i32 s63, s63, 0x2000\n" \
"s_cmp_le_u32 s53, 40\n" \
"s_cselect_b32 s44, s55, s44\n" \
"s_cselect_b32 s45, s52, s45\n" \
"s_cselect_b32 s46, s63, s46\n" \
"v_mov_b32 v248, s44\n" \
"v_mov_b32 v249, s45\n" \
"v_mov_b32 v250, s46\n" \
"s_mov_b64 exec, 1\n" \
"ds_write_b96 v252, v[248:250]\n" \
"s_mov_b64 exec, -1\n" \
"buffer_store_dwordx4 v[100:103], v214, s[4:7], s49 offen sc1\n" \
"s_xor_b32 s43, s49, s29\n" \
"buffer_store_dwordx4 v[100:103], v214, s[4:7], s43 offen sc1\n" \
"s_waitcnt vmcnt(4)\n" \
"v_mov_b32 v28, v44\n" \
"v_mov_b32 v29, v45\n" \
"v_mov_b32 v30, v46\n" \
"v_mov_b32 v31, v47\n" \
"v_mov_b32 v32, v48\n" \
"v_mov_b32 v33, v49\n" \
"v_mov_b32 v34, v50\n" \
"v_mov_b32 v35, v51\n" \
"v_mov_b32 v36, v52\n" \
"v_mov_b32 v37, v53\n" \
"v_mov_b32 v38, v54\n" \
"v_mov_b32 v39, v55\n" \
"v_mov_b32 v40, v56\n" \
"v_mov_b32 v41, v57\n" \
"v_mov_b32 v42, v58\n" \
"v_mov_b32 v43, v59\n" \
"s_add_u32 s40, s40, 1\n" \
"s_waitcnt lgkmcnt(0)\n" \
"s_barrier\n" \
"s_cmp_eq_u32 s40, 0\n" \
"s_cbranch_scc1 .LX0_wet\n" \
"s_cmp_lt_u32 s40, 63\n" \
"s_cbranch_scc1 .LX0_loop\n" \
"ds_read_b128 v[0:3], v216 offset:0\n" \
"ds_read_b128 v[4:7], v216 offset:1024\n" \
"ds_read_b128 v[8:11], v216 offset:2048\n" \
"ds_read_b128 v[12:15], v216 offset:3072\n" \
"ds_read_b128 v[16:19], v216 offset:4096\n" \
"ds_read_b128 v[20:23], v216 offset:5120\n" \
"ds_read_b128 v[24:27], v216 offset:6144\n" \
"v_mov_b32 v60, 0\n" \
"v_mov_b32 v61, 0\n" \
"v_mov_b32 v62, 0\n" \
"v_mov_b32 v63, 0\n" \
"v_mov_b32 v64, 0\n" \
"v_mov_b32 v65, 0\n" \
"v_mov_b32 v66, 0\n" \
"v_mov_b32 v67, 0\n" \
"v_mov_b32 v68, 0\n" \
"v_mov_b32 v69, 0\n" \
"v_mov_b32 v70, 0\n" \
"v_mov_b32 v71, 0\n" \
"v_mov_b32 v72, 0\n" \
"v_mov_b32 v73, 0\n" \
"v_mov_b32 v74, 0\n" \
"v_mov_b32 v75, 0\n" \
"s_nop 1\n" \
"s_waitcnt lgkmcnt(6)\n" \
"v_smfmac_f32_16x16x64_bf16 v[60:63], v[0:3], a[0:7], v200\n" \
"v_smfmac_f32_16x16x64_bf16 v[64:67], v[0:3], a[56:63], v200\n" \
"v_smfmac_f32_16x16x64_bf16 v[68:71], v[0:3], a[112:119], v200\n" \
"v_smfmac_f32_16x16x64_bf16 v[72:75], v[0:3], a[168:175], v200\n" \
"s_waitcnt lgkmcnt(5)\n" \
"v_smfmac_f32_16x16x64_bf16 v[60:63], v[4:7], a[8:15], v200\n" \
"v_smfmac_f32_16x16x64_bf16 v[64:67], v[4:7], a[64:71], v200\n" \
"v_smfmac_f32_16x16x64_bf16 v[68:71], v[4:7], a[120:127], v200\n" \
"v_smfmac_f32_16x16x64_bf16 v[72:75], v[4:7], a[176:183], v200\n" \
"s_waitcnt lgkmcnt(4)\n" \
"v_smfmac_f32_16x16x64_bf16 v[60:63], v[8:11], a[16:23], v200\n" \
"v_smfmac_f32_16x16x64_bf16 v[64:67], v[8:11], a[72:79], v200\n" \
"v_smfmac_f32_16x16x64_bf16 v[68:71], v[8:11], a[128:135], v200\n" \
"v_smfmac_f32_16x16x64_bf16 v[72:75], v[8:11], a[184:191], v200\n" \
"s_waitcnt lgkmcnt(3)\n" \
"v_smfmac_f32_16x16x64_bf16 v[60:63], v[12:15], a[24:31], v200\n" \
"v_smfmac_f32_16x16x64_bf16 v[64:67], v[12:15], a[80:87], v200\n" \
"v_smfmac_f32_16x16x64_bf16 v[68:71], v[12:15], a[136:143], v200\n" \
"v_smfmac_f32_16x16x64_bf16 v[72:75], v[12:15], a[192:199], v200\n" \
"s_waitcnt lgkmcnt(2)\n" \
"v_smfmac_f32_16x16x64_bf16 v[60:63], v[16:19], a[32:39], v200\n" \
"v_smfmac_f32_16x16x64_bf16 v[64:67], v[16:19], a[88:95], v200\n" \
"v_smfmac_f32_16x16x64_bf16 v[68:71], v[16:19], a[144:151], v200\n" \
"v_smfmac_f32_16x16x64_bf16 v[72:75], v[16:19], a[200:207], v200\n" \
"s_waitcnt lgkmcnt(1)\n" \
"v_smfmac_f32_16x16x64_bf16 v[60:63], v[20:23], a[40:47], v200\n" \
"v_smfmac_f32_16x16x64_bf16 v[64:67], v[20:23], a[96:103], v200\n" \
"v_smfmac_f32_16x16x64_bf16 v[68:71], v[20:23], a[152:159], v200\n" \
"v_smfmac_f32_16x16x64_bf16 v[72:75], v[20:23], a[208:215], v200\n" \
"s_waitcnt lgkmcnt(0)\n" \
"v_smfmac_f32_16x16x64_bf16 v[60:63], v[24:27], a[48:55], v200\n" \
"v_smfmac_f32_16x16x64_bf16 v[64:67], v[24:27], a[104:111], v200\n" \
"v_smfmac_f32_16x16x64_bf16 v[68:71], v[24:27], a[160:167], v200\n" \
"v_smfmac_f32_16x16x64_bf16 v[72:75], v[24:27], a[216:223], v200\n" \
"s_nop 7\n" \
"v_permlane32_swap_b32 v60, v64\n" \
"v_permlane32_swap_b32 v61, v65\n" \
"v_permlane32_swap_b32 v62, v66\n" \
"v_permlane32_swap_b32 v63, v67\n" \
"v_add_f32 v60, v60, v64\n" \
"v_add_f32 v61, v61, v65\n" \
"v_add_f32 v62, v62, v66\n" \
"v_add_f32 v63, v63, v67\n" \
"v_cvt_pk_bf16_f32 v184, v60, v61\n" \
"v_cvt_pk_bf16_f32 v185, v62, v63\n" \
"ds_write_b64 v217, v[184:185] offset:0\n" \
"v_permlane32_swap_b32 v68, v72\n" \
"v_permlane32_swap_b32 v69, v73\n" \
"v_permlane32_swap_b32 v70, v74\n" \
"v_permlane32_swap_b32 v71, v75\n" \
"v_add_f32 v68, v68, v72\n" \
"v_add_f32 v69, v69, v73\n" \
"v_add_f32 v70, v70, v74\n" \
"v_add_f32 v71, v71, v75\n" \
"v_cvt_pk_bf16_f32 v186, v68, v69\n" \
"v_cvt_pk_bf16_f32 v187, v70, v71\n" \
"ds_write_b64 v217, v[186:187] offset:512\n" \
"ds_read_b64_tr_b16 v[0:1], v218 offset:0\n" \
"ds_read_b64_tr_b16 v[2:3], v218 offset:64\n" \
"s_add_u32 s43, s40, 0\n" \
"s_mul_i32 s43, s43, 114688\n" \
"s_waitcnt lgkmcnt(0)\n" \
"buffer_store_dwordx4 v[0:3], v219, s[16:19], s43 offen sc1\n" \
"s_waitcnt vmcnt(0)\n" \
"s_barrier\n" \
"ds_read_b128 v[0:3], v216 offset:7168\n" \
"ds_read_b128 v[4:7], v216 offset:8192\n" \
"ds_read_b128 v[8:11], v216 offset:9216\n" \
"ds_read_b128 v[12:15], v216 offset:10240\n" \
"ds_read_b128 v[16:19], v216 offset:11264\n" \
"ds_read_b128 v[20:23], v216 offset:12288\n" \
"ds_read_b128 v[24:27], v216 offset:13312\n" \
"v_mov_b32 v60, 0\n" \
"v_mov_b32 v61, 0\n" \
"v_mov_b32 v62, 0\n" \
"v_mov_b32 v63, 0\n" \
"v_mov_b32 v64, 0\n" \
"v_mov_b32 v65, 0\n" \
"v_mov_b32 v66, 0\n" \
"v_mov_b32 v67, 0\n" \
"v_mov_b32 v68, 0\n" \
"v_mov_b32 v69, 0\n" \
"v_mov_b32 v70, 0\n" \
"v_mov_b32 v71, 0\n" \
"v_mov_b32 v72, 0\n" \
"v_mov_b32 v73, 0\n" \
"v_mov_b32 v74, 0\n" \
"v_mov_b32 v75, 0\n" \
"s_nop 1\n" \
"s_waitcnt lgkmcnt(6)\n" \
"v_smfmac_f32_16x16x64_bf16 v[60:63], v[0:3], a[0:7], v200\n" \
"v_smfmac_f32_16x16x64_bf16 v[64:67], v[0:3], a[56:63], v200\n" \
"v_smfmac_f32_16x16x64_bf16 v[68:71], v[0:3], a[112:119], v200\n" \
"v_smfmac_f32_16x16x64_bf16 v[72:75], v[0:3], a[168:175], v200\n" \
"s_waitcnt lgkmcnt(5)\n" \
"v_smfmac_f32_16x16x64_bf16 v[60:63], v[4:7], a[8:15], v200\n" \
"v_smfmac_f32_16x16x64_bf16 v[64:67], v[4:7], a[64:71], v200\n" \
"v_smfmac_f32_16x16x64_bf16 v[68:71], v[4:7], a[120:127], v200\n" \
"v_smfmac_f32_16x16x64_bf16 v[72:75], v[4:7], a[176:183], v200\n" \
"s_waitcnt lgkmcnt(4)\n" \
"v_smfmac_f32_16x16x64_bf16 v[60:63], v[8:11], a[16:23], v200\n" \
"v_smfmac_f32_16x16x64_bf16 v[64:67], v[8:11], a[72:79], v200\n" \
"v_smfmac_f32_16x16x64_bf16 v[68:71], v[8:11], a[128:135], v200\n" \
"v_smfmac_f32_16x16x64_bf16 v[72:75], v[8:11], a[184:191], v200\n" \
"s_waitcnt lgkmcnt(3)\n" \
"v_smfmac_f32_16x16x64_bf16 v[60:63], v[12:15], a[24:31], v200\n" \
"v_smfmac_f32_16x16x64_bf16 v[64:67], v[12:15], a[80:87], v200\n" \
"v_smfmac_f32_16x16x64_bf16 v[68:71], v[12:15], a[136:143], v200\n" \
"v_smfmac_f32_16x16x64_bf16 v[72:75], v[12:15], a[192:199], v200\n" \
"s_waitcnt lgkmcnt(2)\n" \
"v_smfmac_f32_16x16x64_bf16 v[60:63], v[16:19], a[32:39], v200\n" \
"v_smfmac_f32_16x16x64_bf16 v[64:67], v[16:19], a[88:95], v200\n" \
"v_smfmac_f32_16x16x64_bf16 v[68:71], v[16:19], a[144:151], v200\n" \
"v_smfmac_f32_16x16x64_bf16 v[72:75], v[16:19], a[200:207], v200\n" \
"s_waitcnt lgkmcnt(1)\n" \
"v_smfmac_f32_16x16x64_bf16 v[60:63], v[20:23], a[40:47], v200\n" \
"v_smfmac_f32_16x16x64_bf16 v[64:67], v[20:23], a[96:103], v200\n" \
"v_smfmac_f32_16x16x64_bf16 v[68:71], v[20:23], a[152:159], v200\n" \
"v_smfmac_f32_16x16x64_bf16 v[72:75], v[20:23], a[208:215], v200\n" \
"s_waitcnt lgkmcnt(0)\n" \
"v_smfmac_f32_16x16x64_bf16 v[60:63], v[24:27], a[48:55], v200\n" \
"v_smfmac_f32_16x16x64_bf16 v[64:67], v[24:27], a[104:111], v200\n" \
"v_smfmac_f32_16x16x64_bf16 v[68:71], v[24:27], a[160:167], v200\n" \
"v_smfmac_f32_16x16x64_bf16 v[72:75], v[24:27], a[216:223], v200\n" \
"s_nop 7\n" \
"v_permlane32_swap_b32 v60, v64\n" \
"v_permlane32_swap_b32 v61, v65\n" \
"v_permlane32_swap_b32 v62, v66\n" \
"v_permlane32_swap_b32 v63, v67\n" \
"v_add_f32 v60, v60, v64\n" \
"v_add_f32 v61, v61, v65\n" \
"v_add_f32 v62, v62, v66\n" \
"v_add_f32 v63, v63, v67\n" \
"v_cvt_pk_bf16_f32 v184, v60, v61\n" \
"v_cvt_pk_bf16_f32 v185, v62, v63\n" \
"ds_write_b64 v217, v[184:185] offset:0\n" \
"v_permlane32_swap_b32 v68, v72\n" \
"v_permlane32_swap_b32 v69, v73\n" \
"v_permlane32_swap_b32 v70, v74\n" \
"v_permlane32_swap_b32 v71, v75\n" \
"v_add_f32 v68, v68, v72\n" \
"v_add_f32 v69, v69, v73\n" \
"v_add_f32 v70, v70, v74\n" \
"v_add_f32 v71, v71, v75\n" \
"v_cvt_pk_bf16_f32 v186, v68, v69\n" \
"v_cvt_pk_bf16_f32 v187, v70, v71\n" \
"ds_write_b64 v217, v[186:187] offset:512\n" \
"ds_read_b64_tr_b16 v[0:1], v218 offset:0\n" \
"ds_read_b64_tr_b16 v[2:3], v218 offset:64\n" \
"s_add_u32 s43, s40, 1\n" \
"s_mul_i32 s43, s43, 114688\n" \
"s_waitcnt lgkmcnt(0)\n" \
"buffer_store_dwordx4 v[0:3], v219, s[16:19], s43 offen sc1\n" \
"s_waitcnt vmcnt(0)\n" \
"s_barrier\n" \
".LX0_end:\n" \
"s_endpgm\n" \
".p2align 6\n" \
"fk_role_X1:\n" \
"ds_read_b32 v200, v255 offset:0\n" \
"ds_read_b32 v201, v255 offset:256\n" \
"ds_read_b32 v202, v255 offset:512\n" \
"ds_read_b32 v203, v255 offset:768\n" \
"ds_read_b32 v204, v255 offset:1024\n" \
"ds_read_b32 v205, v255 offset:1280\n" \
"ds_read_b32 v206, v255 offset:1536\n" \
"ds_read_b32 v207, v255 offset:1792\n" \
"ds_read_b32 v208, v255 offset:2048\n" \
"ds_read_b32 v209, v255 offset:2304\n" \
"ds_read_b32 v210, v255 offset:2560\n" \
"ds_read_b32 v211, v255 offset:2816\n" \
"ds_read_b32 v212, v255 offset:3072\n" \
"ds_read_b32 v213, v255 offset:3328\n" \
"ds_read_b32 v214, v255 offset:3584\n" \
"ds_read_b32 v215, v255 offset:3840\n" \
"ds_read_b32 v216, v255 offset:4096\n" \
"ds_read_b32 v217, v255 offset:4352\n" \
"ds_read_b32 v218, v255 offset:4608\n" \
"ds_read_b32 v219, v255 offset:4864\n" \
"ds_read_b32 v220, v255 offset:5120\n" \
"ds_read_b32 v221, v255 offset:5376\n" \
"ds_read_b32 v254, v255 offset:7936\n" \
"s_waitcnt lgkmcnt(0)\n" \
"v_readlane_b32 s4, v254, 0\n" \
"v_readlane_b32 s5, v254, 1\n" \
"v_readlane_b32 s6, v254, 2\n" \
"v_readlane_b32 s7, v254, 3\n" \
"v_readlane_b32 s8, v254, 4\n" \
"v_readlane_b32 s9, v254, 5\n" \
"v_readlane_b32 s10, v254, 6\n" \
"v_readlane_b32 s11, v254, 7\n" \
"v_readlane_b32 s12, v254, 8\n" \
"v_readlane_b32 s13, v254, 9\n" \
"v_readlane_b32 s14, v254, 10\n" \
"v_readlane_b32 s15, v254, 11\n" \
"v_readlane_b32 s16, v254, 12\n" \
"v_readlane_b32 s17, v254, 13\n" \
"v_readlane_b32 s18, v254, 14\n" \
"v_readlane_b32 s19, v254, 15\n" \
"v_readlane_b32 s20, v254, 16\n" \
"v_readlane_b32 s21, v254, 17\n" \
"v_readlane_b32 s22, v254, 18\n" \
"v_readlane_b32 s23, v254, 19\n" \
"v_readlane_b32 s24, v254, 20\n" \
"v_readlane_b32 s25, v254, 21\n" \
"v_readlane_b32 s26, v254, 22\n" \
"v_readlane_b32 s27, v254, 23\n" \
"v_readlane_b32 s28, v254, 24\n" \
"v_readlane_b32 s29, v254, 25\n" \
"v_readlane_b32 s30, v254, 26\n" \
"v_readlane_b32 s31, v254, 27\n" \
"v_readlane_b32 s32, v254, 28\n" \
"v_readlane_b32 s33, v254, 29\n" \
"v_readlane_b32 s34, v254, 30\n" \
"v_readlane_b32 s35, v254, 31\n" \
"v_readlane_b32 s36, v254, 32\n" \
"v_readlane_b32 s37, v254, 33\n" \
"v_readlane_b32 s38, v254, 34\n" \
"v_readlane_b32 s39, v254, 35\n" \
"s_mov_b32 s40, 0\n" \
"s_mov_b32 s51, 0\n" \
"s_nop 0\n" \
"v_mov_b32 v252, s38\n" \
"s_nop 4\n" \
"v_mov_b32 v100, -1\n" \
"v_mov_b32 v101, -1\n" \
"v_mov_b32 v102, -1\n" \
"v_mov_b32 v103, -1\n" \
"s_mov_b32 s35, -2\n" \
"s_mov_b32 s2, 0\n" \
"s_mov_b32 s3, -1\n" \
"v_writelane_b32 v251, s6, 0\n" \
"v_writelane_b32 v251, s10, 1\n" \
"v_writelane_b32 v251, s14, 2\n" \
"v_writelane_b32 v251, s18, 3\n" \
"s_mov_b32 s6, 0\n" \
"s_mov_b32 s10, 0\n" \
"s_mov_b32 s14, 0\n" \
"s_mov_b32 s18, 0\n" \
"s_mov_b32 s40, -1\n" \
"s_branch .LX1_pre\n" \
".LX1_wet:\n" \
"s_waitcnt vmcnt(0)\n" \
"v_readlane_b32 s6, v251, 0\n" \
"v_readlane_b32 s10, v251, 1\n" \
"v_readlane_b32 s14, v251, 2\n" \
"v_readlane_b32 s18, v251, 3\n" \
"s_nop 4\n" \
"s_add_u32 s43, s30, 1\n" \
"s_mov_b32 s50, 0\n" \
".LX1_w71l:\n" \
"buffer_load_dword v240, v204, s[4:7], 0 offen sc1\n" \
"s_waitcnt vmcnt(0)\n" \
".LX1_w71c:\n" \
"v_subrev_u32 v241, s43, v240\n" \
"v_cmp_gt_u32 vcc, 0x100, v241\n" \
"s_cmp_eq_u64 vcc, -1\n" \
"s_cbranch_scc1 .LX1_w71d\n" \
"s_sleep 2\n" \
"s_add_u32 s50, s50, 1\n" \
"s_cmp_lt_u32 s50, 0x20000\n" \
"s_cbranch_scc1 .LX1_w71l\n" \
"s_branch .LX1_end\n" \
".LX1_w71d:\n" \
"v_readlane_b32 s43, v202, 0\n" \
"s_nop 4\n" \
"buffer_load_dwordx4 a[0:3], v201, s[8:11], s43 offen sc1\n" \
"buffer_load_dwordx4 a[4:7], v201, s[8:11], s43 offen offset:1024 sc1\n" \
"v_readlane_b32 s43, v202, 7\n" \
"s_nop 4\n" \
"buffer_load_dwordx4 a[56:59], v201, s[8:11], s43 offen sc1\n" \
"buffer_load_dwordx4 a[60:63], v201, s[8:11], s43 offen offset:1024 sc1\n" \
"v_readlane_b32 s43, v202, 14\n" \
"s_nop 4\n" \
"buffer_load_dwordx4 a[112:115], v201, s[8:11], s43 offen sc1\n" \
"buffer_load_dwordx4 a[116:119], v201, s[8:11], s43 offen offset:1024 sc1\n" \
"v_readlane_b32 s43, v202, 21\n" \
"s_nop 4\n" \
"buffer_load_dwordx4 a[168:171], v201, s[8:11], s43 offen sc1\n" \
"buffer_load_dwordx4 a[172:175], v201, s[8:11], s43 offen offset:1024 sc1\n" \
"s_add_u32 s43, s30, 2\n" \
"s_mov_b32 s50, 0\n" \
".LX1_w72l:\n" \
"buffer_load_dword v240, v204, s[4:7], 0 offen sc1\n" \
"s_waitcnt vmcnt(0)\n" \
".LX1_w72c:\n" \
"v_subrev_u32 v241, s43, v240\n" \
"v_cmp_gt_u32 vcc, 0x100, v241\n" \
"s_cmp_eq_u64 vcc, -1\n" \
"s_cbranch_scc1 .LX1_w72d\n" \
"s_sleep 2\n" \
"s_add_u32 s50, s50, 1\n" \
"s_cmp_lt_u32 s50, 0x20000\n" \
"s_cbranch_scc1 .LX1_w72l\n" \
"s_branch .LX1_end\n" \
".LX1_w72d:\n" \
"v_readlane_b32 s43, v202, 1\n" \
"s_nop 4\n" \
"buffer_load_dwordx4 a[8:11], v201, s[8:11], s43 offen sc1\n" \
"buffer_load_dwordx4 a[12:15], v201, s[8:11], s43 offen offset:1024 sc1\n" \
"v_readlane_b32 s43, v202, 8\n" \
"s_nop 4\n" \
"buffer_load_dwordx4 a[64:67], v201, s[8:11], s43 offen sc1\n" \
"buffer_load_dwordx4 a[68:71], v201, s[8:11], s43 offen offset:1024 sc1\n" \
"v_readlane_b32 s43, v202, 15\n" \
"s_nop 4\n" \
"buffer_load_dwordx4 a[120:123], v201, s[8:11], s43 offen sc1\n" \
"buffer_load_dwordx4 a[124:127], v201, s[8:11], s43 offen offset:1024 sc1\n" \
"v_readlane_b32 s43, v202, 22\n" \
"s_nop 4\n" \
"buffer_load_dwordx4 a[176:179], v201, s[8:11], s43 offen sc1\n" \
"buffer_load_dwordx4 a[180:183], v201, s[8:11], s43 offen offset:1024 sc1\n" \
"s_add_u32 s43, s30, 3\n" \
"s_mov_b32 s50, 0\n" \
".LX1_w73l:\n" \
"buffer_load_dword v240, v204, s[4:7], 0 offen sc1\n" \
"s_waitcnt vmcnt(0)\n" \
".LX1_w73c:\n" \
"v_subrev_u32 v241, s43, v240\n" \
"v_cmp_gt_u32 vcc, 0x100, v241\n" \
"s_cmp_eq_u64 vcc, -1\n" \
"s_cbranch_scc1 .LX1_w73d\n" \
"s_sleep 2\n" \
"s_add_u32 s50, s50, 1\n" \
"s_cmp_lt_u32 s50, 0x20000\n" \
"s_cbranch_scc1 .LX1_w73l\n" \
"s_branch .LX1_end\n" \
".LX1_w73d:\n" \
"v_readlane_b32 s43, v202, 2\n" \
"s_nop 4\n" \
"buffer_load_dwordx4 a[16:19], v201, s[8:11], s43 offen sc1\n" \
"buffer_load_dwordx4 a[20:23], v201, s[8:11], s43 offen offset:1024 sc1\n" \
"v_readlane_b32 s43, v202, 9\n" \
"s_nop 4\n" \
"buffer_load_dwordx4 a[72:75], v201, s[8:11], s43 offen sc1\n" \
"buffer_load_dwordx4 a[76:79], v201, s[8:11], s43 offen offset:1024 sc1\n" \
"v_readlane_b32 s43, v202, 16\n" \
"s_nop 4\n" \
"buffer_load_dwordx4 a[128:131], v201, s[8:11], s43 offen sc1\n" \
"buffer_load_dwordx4 a[132:135], v201, s[8:11], s43 offen offset:1024 sc1\n" \
"v_readlane_b32 s43, v202, 23\n" \
"s_nop 4\n" \
"buffer_load_dwordx4 a[184:187], v201, s[8:11], s43 offen sc1\n" \
"buffer_load_dwordx4 a[188:191], v201, s[8:11], s43 offen offset:1024 sc1\n" \
"s_add_u32 s43, s30, 4\n" \
"s_mov_b32 s50, 0\n" \
".LX1_w74l:\n" \
"buffer_load_dword v240, v204, s[4:7], 0 offen sc1\n" \
"s_waitcnt vmcnt(0)\n" \
".LX1_w74c:\n" \
"v_subrev_u32 v241, s43, v240\n" \
"v_cmp_gt_u32 vcc, 0x100, v241\n" \
"s_cmp_eq_u64 vcc, -1\n" \
"s_cbranch_scc1 .LX1_w74d\n" \
"s_sleep 2\n" \
"s_add_u32 s50, s50, 1\n" \
"s_cmp_lt_u32 s50, 0x20000\n" \
"s_cbranch_scc1 .LX1_w74l\n" \
"s_branch .LX1_end\n" \
".LX1_w74d:\n" \
"v_readlane_b32 s43, v202, 3\n" \
"s_nop 4\n" \
"buffer_load_dwordx4 a[24:27], v201, s[8:11], s43 offen sc1\n" \
"buffer_load_dwordx4 a[28:31], v201, s[8:11], s43 offen offset:1024 sc1\n" \
"v_readlane_b32 s43, v202, 10\n" \
"s_nop 4\n" \
"buffer_load_dwordx4 a[80:83], v201, s[8:11], s43 offen sc1\n" \
"buffer_load_dwordx4 a[84:87], v201, s[8:11], s43 offen offset:1024 sc1\n" \
"v_readlane_b32 s43, v202, 17\n" \
"s_nop 4\n" \
"buffer_load_dwordx4 a[136:139], v201, s[8:11], s43 offen sc1\n" \
"buffer_load_dwordx4 a[140:143], v201, s[8:11], s43 offen offset:1024 sc1\n" \
"v_readlane_b32 s43, v202, 24\n" \
"s_nop 4\n" \
"buffer_load_dwordx4 a[192:195], v201, s[8:11], s43 offen sc1\n" \
"buffer_load_dwordx4 a[196:199], v201, s[8:11], s43 offen offset:1024 sc1\n" \
"s_add_u32 s43, s30, 5\n" \
"s_mov_b32 s50, 0\n" \
".LX1_w75l:\n" \
"buffer_load_dword v240, v204, s[4:7], 0 offen sc1\n" \
"s_waitcnt vmcnt(0)\n" \
".LX1_w75c:\n" \
"v_subrev_u32 v241, s43, v240\n" \
"v_cmp_gt_u32 vcc, 0x100, v241\n" \
"s_cmp_eq_u64 vcc, -1\n" \
"s_cbranch_scc1 .LX1_w75d\n" \
"s_sleep 2\n" \
"s_add_u32 s50, s50, 1\n" \
"s_cmp_lt_u32 s50, 0x20000\n" \
"s_cbranch_scc1 .LX1_w75l\n" \
"s_branch .LX1_end\n" \
".LX1_w75d:\n" \
"v_readlane_b32 s43, v202, 4\n" \
"s_nop 4\n" \
"buffer_load_dwordx4 a[32:35], v201, s[8:11], s43 offen sc1\n" \
"buffer_load_dwordx4 a[36:39], v201, s[8:11], s43 offen offset:1024 sc1\n" \
"v_readlane_b32 s43, v202, 11\n" \
"s_nop 4\n" \
"buffer_load_dwordx4 a[88:91], v201, s[8:11], s43 offen sc1\n" \
"buffer_load_dwordx4 a[92:95], v201, s[8:11], s43 offen offset:1024 sc1\n" \
"v_readlane_b32 s43, v202, 18\n" \
"s_nop 4\n" \
"buffer_load_dwordx4 a[144:147], v201, s[8:11], s43 offen sc1\n" \
"buffer_load_dwordx4 a[148:151], v201, s[8:11], s43 offen offset:1024 sc1\n" \
"v_readlane_b32 s43, v202, 25\n" \
"s_nop 4\n" \
"buffer_load_dwordx4 a[200:203], v201, s[8:11], s43 offen sc1\n" \
"buffer_load_dwordx4 a[204:207], v201, s[8:11], s43 offen offset:1024 sc1\n" \
"s_add_u32 s43, s30, 6\n" \
"s_mov_b32 s50, 0\n" \
".LX1_w76l:\n" \
"buffer_load_dword v240, v204, s[4:7], 0 offen sc1\n" \
"s_waitcnt vmcnt(0)\n" \
".LX1_w76c:\n" \
"v_subrev_u32 v241, s43, v240\n" \
"v_cmp_gt_u32 vcc, 0x100, v241\n" \
"s_cmp_eq_u64 vcc, -1\n" \
"s_cbranch_scc1 .LX1_w76d\n" \
"s_sleep 2\n" \
"s_add_u32 s50, s50, 1\n" \
"s_cmp_lt_u32 s50, 0x20000\n" \
"s_cbranch_scc1 .LX1_w76l\n" \
"s_branch .LX1_end\n" \
".LX1_w76d:\n" \
"v_readlane_b32 s43, v202, 5\n" \
"s_nop 4\n" \
"buffer_load_dwordx4 a[40:43], v201, s[8:11], s43 offen sc1\n" \
"buffer_load_dwordx4 a[44:47], v201, s[8:11], s43 offen offset:1024 sc1\n" \
"v_readlane_b32 s43, v202, 12\n" \
"s_nop 4\n" \
"buffer_load_dwordx4 a[96:99], v201, s[8:11], s43 offen sc1\n" \
"buffer_load_dwordx4 a[100:103], v201, s[8:11], s43 offen offset:1024 sc1\n" \
"v_readlane_b32 s43, v202, 19\n" \
"s_nop 4\n" \
"buffer_load_dwordx4 a[152:155], v201, s[8:11], s43 offen sc1\n" \
"buffer_load_dwordx4 a[156:159], v201, s[8:11], s43 offen offset:1024 sc1\n" \
"v_readlane_b32 s43, v202, 26\n" \
"s_nop 4\n" \
"buffer_load_dwordx4 a[208:211], v201, s[8:11], s43 offen sc1\n" \
"buffer_load_dwordx4 a[212:215], v201, s[8:11], s43 offen offset:1024 sc1\n" \
"s_add_u32 s43, s30, 7\n" \
"s_mov_b32 s50, 0\n" \
".LX1_w77l:\n" \
"buffer_load_dword v240, v204, s[4:7], 0 offen sc1\n" \
"s_waitcnt vmcnt(0)\n" \
".LX1_w77c:\n" \
"v_subrev_u32 v241, s43, v240\n" \
"v_cmp_gt_u32 vcc, 0x100, v241\n" \
"s_cmp_eq_u64 vcc, -1\n" \
"s_cbranch_scc1 .LX1_w77d\n" \
"s_sleep 2\n" \
"s_add_u32 s50, s50, 1\n" \
"s_cmp_lt_u32 s50, 0x20000\n" \
"s_cbranch_scc1 .LX1_w77l\n" \
"s_branch .LX1_end\n" \
".LX1_w77d:\n" \
"v_readlane_b32 s43, v202, 6\n" \
"s_nop 4\n" \
"buffer_load_dwordx4 a[48:51], v201, s[8:11], s43 offen sc1\n" \
"buffer_load_dwordx4 a[52:55], v201, s[8:11], s43 offen offset:1024 sc1\n" \
"v_readlane_b32 s43, v202, 13\n" \
"s_nop 4\n" \
"buffer_load_dwordx4 a[104:107], v201, s[8:11], s43 offen sc1\n" \
"buffer_load_dwordx4 a[108:111], v201, s[8:11], s43 offen offset:1024 sc1\n" \
"v_readlane_b32 s43, v202, 20\n" \
"s_nop 4\n" \
"buffer_load_dwordx4 a[160:163], v201, s[8:11], s43 offen sc1\n" \
"buffer_load_dwordx4 a[164:167], v201, s[8:11], s43 offen offset:1024 sc1\n" \
"v_readlane_b32 s43, v202, 27\n" \
"s_nop 4\n" \
"buffer_load_dwordx4 a[216:219], v201, s[8:11], s43 offen sc1\n" \
"buffer_load_dwordx4 a[220:223], v201, s[8:11], s43 offen offset:1024 sc1\n" \
"s_add_u32 s43, s30, 9\n" \
"s_mov_b32 s50, 0\n" \
".LX1_w78l:\n" \
"buffer_load_dword v240, v215, s[4:7], 0 offen sc1\n" \
"s_waitcnt vmcnt(0)\n" \
".LX1_w78c:\n" \
"v_subrev_u32 v241, s43, v240\n" \
"v_cmp_gt_u32 vcc, 0x100, v241\n" \
"s_cmp_eq_u64 vcc, -1\n" \
"s_cbranch_scc1 .LX1_w78d\n" \
"s_sleep 2\n" \
"s_add_u32 s50, s50, 1\n" \
"s_cmp_lt_u32 s50, 0x20000\n" \
"s_cbranch_scc1 .LX1_w78l\n" \
"s_branch .LX1_end\n" \
".LX1_w78d:\n" \
"s_add_u32 s50, s40, 0\n" \
"s_mul_i32 s50, s50, 458752\n" \
"s_add_u32 s50, s50, s34\n" \
"buffer_load_dwordx4 v[28:31], v201, s[12:15], s50 offen sc1\n" \
"buffer_load_dwordx4 v[32:35], v201, s[12:15], s50 offen offset:1024 sc1\n" \
"buffer_load_dwordx4 v[36:39], v201, s[12:15], s50 offen offset:2048 sc1\n" \
"buffer_load_dwordx4 v[40:43], v201, s[12:15], s50 offen offset:3072 sc1\n" \
"s_add_u32 s43, s30, 1\n" \
"s_mov_b32 s50, 0\n" \
".LX1_w79l:\n" \
"buffer_load_dword v240, v205, s[4:7], 0 offen sc1\n" \
"s_waitcnt vmcnt(0)\n" \
".LX1_w79c:\n" \
"v_subrev_u32 v241, s43, v240\n" \
"v_cmp_gt_u32 vcc, 0x100, v241\n" \
"s_cmp_eq_u64 vcc, -1\n" \
"s_cbranch_scc1 .LX1_w79d\n" \
"s_sleep 2\n" \
"s_add_u32 s50, s50, 1\n" \
"s_cmp_lt_u32 s50, 0x20000\n" \
"s_cbranch_scc1 .LX1_w79l\n" \
"s_branch .LX1_end\n" \
".LX1_w79d:\n" \
"s_add_u32 s43, s30, 1\n" \
"s_mov_b32 s50, 0\n" \
".LX1_w80l:\n" \
"buffer_load_dword v240, v206, s[4:7], 0 offen sc1\n" \
"s_waitcnt vmcnt(0)\n" \
".LX1_w80c:\n" \
"v_subrev_u32 v241, s43, v240\n" \
"v_cmp_gt_u32 vcc, 0x100, v241\n" \
"s_cmp_eq_u64 vcc, -1\n" \
"s_cbranch_scc1 .LX1_w80d\n" \
"s_sleep 2\n" \
"s_add_u32 s50, s50, 1\n" \
"s_cmp_lt_u32 s50, 0x20000\n" \
"s_cbranch_scc1 .LX1_w80l\n" \
"s_branch .LX1_end\n" \
".LX1_w80d:\n" \
".LX1_pre:\n" \
"s_barrier\n" \
".LX1_loop:\n" \
"s_add_u32 s43, s40, 2\n" \
"s_and_b32 s43, s43, 3\n" \
"s_bitcmp1_b32 s43, 0\n" \
"s_cselect_b32 s49, s32, s31\n" \
"s_lshl_b32 s43, s43, 19\n" \
"s_add_u32 s49, s49, s43\n" \
"s_add_u32 s43, s30, s40\n" \
"s_add_u32 s43, s43, -1\n" \
"v_mov_b32 v241, s43\n" \
"buffer_store_dword v241, v220, s[4:7], 0 offen sc1\n" \
"ds_read_b128 v[0:3], v216 offset:0\n" \
"ds_read_b128 v[4:7], v216 offset:1024\n" \
"ds_read_b128 v[8:11], v216 offset:2048\n" \
"ds_read_b128 v[12:15], v216 offset:3072\n" \
"ds_read_b128 v[16:19], v216 offset:4096\n" \
"ds_read_b128 v[20:23], v216 offset:5120\n" \
"ds_read_b128 v[24:27], v216 offset:6144\n" \
"v_mov_b32 v60, 0\n" \
"v_mov_b32 v61, 0\n" \
"v_mov_b32 v62, 0\n" \
"v_mov_b32 v63, 0\n" \
"v_mov_b32 v64, 0\n" \
"v_mov_b32 v65, 0\n" \
"v_mov_b32 v66, 0\n" \
"v_mov_b32 v67, 0\n" \
"v_mov_b32 v68, 0\n" \
"v_mov_b32 v69, 0\n" \
"v_mov_b32 v70, 0\n" \
"v_mov_b32 v71, 0\n" \
"v_mov_b32 v72, 0\n" \
"v_mov_b32 v73, 0\n" \
"v_mov_b32 v74, 0\n" \
"v_mov_b32 v75, 0\n" \
"s_nop 1\n" \
"s_waitcnt lgkmcnt(6)\n" \
"v_smfmac_f32_16x16x64_bf16 v[60:63], v[0:3], a[0:7], v200\n" \
"v_smfmac_f32_16x16x64_bf16 v[64:67], v[0:3], a[56:63], v200\n" \
"v_smfmac_f32_16x16x64_bf16 v[68:71], v[0:3], a[112:119], v200\n" \
"v_smfmac_f32_16x16x64_bf16 v[72:75], v[0:3], a[168:175], v200\n" \
"s_waitcnt lgkmcnt(5)\n" \
"v_smfmac_f32_16x16x64_bf16 v[60:63], v[4:7], a[8:15], v200\n" \
"v_smfmac_f32_16x16x64_bf16 v[64:67], v[4:7], a[64:71], v200\n" \
"v_smfmac_f32_16x16x64_bf16 v[68:71], v[4:7], a[120:127], v200\n" \
"v_smfmac_f32_16x16x64_bf16 v[72:75], v[4:7], a[176:183], v200\n" \
"s_waitcnt lgkmcnt(4)\n" \
"v_smfmac_f32_16x16x64_bf16 v[60:63], v[8:11], a[16:23], v200\n" \
"v_smfmac_f32_16x16x64_bf16 v[64:67], v[8:11], a[72:79], v200\n" \
"v_smfmac_f32_16x16x64_bf16 v[68:71], v[8:11], a[128:135], v200\n" \
"v_smfmac_f32_16x16x64_bf16 v[72:75], v[8:11], a[184:191], v200\n" \
"s_waitcnt lgkmcnt(3)\n" \
"v_smfmac_f32_16x16x64_bf16 v[60:63], v[12:15], a[24:31], v200\n" \
"v_smfmac_f32_16x16x64_bf16 v[64:67], v[12:15], a[80:87], v200\n" \
"v_smfmac_f32_16x16x64_bf16 v[68:71], v[12:15], a[136:143], v200\n" \
"v_smfmac_f32_16x16x64_bf16 v[72:75], v[12:15], a[192:199], v200\n" \
"s_waitcnt lgkmcnt(2)\n" \
"v_smfmac_f32_16x16x64_bf16 v[60:63], v[16:19], a[32:39], v200\n" \
"v_smfmac_f32_16x16x64_bf16 v[64:67], v[16:19], a[88:95], v200\n" \
"v_smfmac_f32_16x16x64_bf16 v[68:71], v[16:19], a[144:151], v200\n" \
"v_smfmac_f32_16x16x64_bf16 v[72:75], v[16:19], a[200:207], v200\n" \
"s_waitcnt lgkmcnt(1)\n" \
"v_smfmac_f32_16x16x64_bf16 v[60:63], v[20:23], a[40:47], v200\n" \
"v_smfmac_f32_16x16x64_bf16 v[64:67], v[20:23], a[96:103], v200\n" \
"v_smfmac_f32_16x16x64_bf16 v[68:71], v[20:23], a[152:159], v200\n" \
"v_smfmac_f32_16x16x64_bf16 v[72:75], v[20:23], a[208:215], v200\n" \
"s_waitcnt lgkmcnt(0)\n" \
"v_smfmac_f32_16x16x64_bf16 v[60:63], v[24:27], a[48:55], v200\n" \
"v_smfmac_f32_16x16x64_bf16 v[64:67], v[24:27], a[104:111], v200\n" \
"v_smfmac_f32_16x16x64_bf16 v[68:71], v[24:27], a[160:167], v200\n" \
"v_smfmac_f32_16x16x64_bf16 v[72:75], v[24:27], a[216:223], v200\n" \
"s_barrier\n" \
"s_add_u32 s50, s40, 1\n" \
"s_mul_i32 s50, s50, 458752\n" \
"s_add_u32 s50, s50, s34\n" \
"buffer_load_dwordx4 v[44:47], v201, s[12:15], s50 offen sc1\n" \
"buffer_load_dwordx4 v[48:51], v201, s[12:15], s50 offen offset:1024 sc1\n" \
"buffer_load_dwordx4 v[52:55], v201, s[12:15], s50 offen offset:2048 sc1\n" \
"buffer_load_dwordx4 v[56:59], v201, s[12:15], s50 offen offset:3072 sc1\n" \
"s_nop 7\n" \
"v_permlane32_swap_b32 v60, v64\n" \
"v_permlane32_swap_b32 v61, v65\n" \
"v_permlane32_swap_b32 v62, v66\n" \
"v_permlane32_swap_b32 v63, v67\n" \
"v_add_f32 v60, v60, v64\n" \
"v_add_f32 v61, v61, v65\n" \
"v_add_f32 v62, v62, v66\n" \
"v_add_f32 v63, v63, v67\n" \
"v_cvt_pk_bf16_f32 v184, v60, v61\n" \
"v_cvt_pk_bf16_f32 v185, v62, v63\n" \
"ds_write_b64 v217, v[184:185] offset:0\n" \
"v_permlane32_swap_b32 v68, v72\n" \
"v_permlane32_swap_b32 v69, v73\n" \
"v_permlane32_swap_b32 v70, v74\n" \
"v_permlane32_swap_b32 v71, v75\n" \
"v_add_f32 v68, v68, v72\n" \
"v_add_f32 v69, v69, v73\n" \
"v_add_f32 v70, v70, v74\n" \
"v_add_f32 v71, v71, v75\n" \
"v_cvt_pk_bf16_f32 v186, v68, v69\n" \
"v_cvt_pk_bf16_f32 v187, v70, v71\n" \
"ds_write_b64 v217, v[186:187] offset:512\n" \
"ds_read_b64_tr_b16 v[0:1], v218 offset:0\n" \
"ds_read_b64_tr_b16 v[2:3], v218 offset:64\n" \
"s_add_u32 s43, s40, 0\n" \
"s_mul_i32 s43, s43, 114688\n" \
"s_waitcnt lgkmcnt(0)\n" \
"buffer_store_dwordx4 v[0:3], v219, s[16:19], s43 offen sc1\n" \
"s_waitcnt vmcnt(5)\n" \
"s_barrier\n" \
"ds_read_b64_tr_b16 v[92:93], v213\n" \
"ds_read_b64_tr_b16 v[94:95], v213 offset:64\n" \
"ds_read_b32 v84, v252 offset:16\n" \
"v_mov_b32 v245, v245\n" \
"s_nop 2\n" \
"v_mov_b32 v245, v245\n" \
"s_nop 2\n" \
"v_mov_b32 v245, v245\n" \
"s_waitcnt lgkmcnt(1)\n" \
"v_mfma_f32_16x16x32_bf16 v[68:71], v[92:95], v[28:31], 0\n" \
"s_waitcnt lgkmcnt(0)\n" \
"v_xor_b32 v85, s29, v84\n" \
"v_mfma_f32_16x16x32_bf16 v[76:79], v[92:95], v[36:39], 0\n" \
"v_add_u32 v84, v84, v210\n" \
"v_add_u32 v85, v85, v210\n" \
"v_mfma_f32_16x16x32_bf16 v[72:75], v[92:95], v[32:35], 0\n" \
"s_nop 1\n" \
"v_mfma_f32_16x16x32_bf16 v[80:83], v[92:95], v[40:43], 0\n" \
"v_cvt_pk_bf16_f32 v96, v68, v69\n" \
"v_cvt_pk_bf16_f32 v97, v70, v71\n" \
"v_cvt_pk_bf16_f32 v98, v76, v77\n" \
"v_cvt_pk_bf16_f32 v99, v78, v79\n" \
"s_mov_b64 exec, s[2:3]\n" \
"v_cvt_pk_bf16_f32 v96, v72, v73\n" \
"v_cvt_pk_bf16_f32 v97, v74, v75\n" \
"s_nop 0\n" \
"v_cvt_pk_bf16_f32 v98, v80, v81\n" \
"v_cvt_pk_bf16_f32 v99, v82, v83\n" \
"s_mov_b64 exec, -1\n" \
"s_nop 1\n" \
"v_permlane16_swap_b32 v96, v98\n" \
"v_permlane16_swap_b32 v97, v99\n" \
"buffer_store_dwordx4 v[96:99], v84, s[4:7], 0 offen sc1\n" \
"s_barrier\n" \
"buffer_store_dwordx4 v[96:99], v85, s[4:7], 0 offen sc1\n" \
"s_nop 15\n" \
"s_nop 15\n" \
"s_nop 15\n" \
"s_nop 15\n" \
"s_nop 15\n" \
"buffer_store_dwordx4 v[100:103], v214, s[4:7], s49 offen sc1\n" \
"s_xor_b32 s43, s49, s29\n" \
"buffer_store_dwordx4 v[100:103], v214, s[4:7], s43 offen sc1\n" \
"s_waitcnt vmcnt(4)\n" \
"v_mov_b32 v28, v44\n" \
"v_mov_b32 v29, v45\n" \
"v_mov_b32 v30, v46\n" \
"v_mov_b32 v31, v47\n" \
"v_mov_b32 v32, v48\n" \
"v_mov_b32 v33, v49\n" \
"v_mov_b32 v34, v50\n" \
"v_mov_b32 v35, v51\n" \
"v_mov_b32 v36, v52\n" \
"v_mov_b32 v37, v53\n" \
"v_mov_b32 v38, v54\n" \
"v_mov_b32 v39, v55\n" \
"v_mov_b32 v40, v56\n" \
"v_mov_b32 v41, v57\n" \
"v_mov_b32 v42, v58\n" \
"v_mov_b32 v43, v59\n" \
"s_add_u32 s40, s40, 1\n" \
"s_waitcnt lgkmcnt(0)\n" \
"s_barrier\n" \
"s_cmp_eq_u32 s40, 0\n" \
"s_cbranch_scc1 .LX1_wet\n" \
"s_cmp_lt_u32 s40, 63\n" \
"s_cbranch_scc1 .LX1_loop\n" \
"s_add_u32 s43, s30, s40\n" \
"s_add_u32 s43, s43, -1\n" \
"v_mov_b32 v241, s43\n" \
"buffer_store_dword v241, v220, s[4:7], 0 offen sc1\n" \
"ds_read_b128 v[0:3], v216 offset:0\n" \
"ds_read_b128 v[4:7], v216 offset:1024\n" \
"ds_read_b128 v[8:11], v216 offset:2048\n" \
"ds_read_b128 v[12:15], v216 offset:3072\n" \
"ds_read_b128 v[16:19], v216 offset:4096\n" \
"ds_read_b128 v[20:23], v216 offset:5120\n" \
"ds_read_b128 v[24:27], v216 offset:6144\n" \
"v_mov_b32 v60, 0\n" \
"v_mov_b32 v61, 0\n" \
"v_mov_b32 v62, 0\n" \
"v_mov_b32 v63, 0\n" \
"v_mov_b32 v64, 0\n" \
"v_mov_b32 v65, 0\n" \
"v_mov_b32 v66, 0\n" \
"v_mov_b32 v67, 0\n" \
"v_mov_b32 v68, 0\n" \
"v_mov_b32 v69, 0\n" \
"v_mov_b32 v70, 0\n" \
"v_mov_b32 v71, 0\n" \
"v_mov_b32 v72, 0\n" \
"v_mov_b32 v73, 0\n" \
"v_mov_b32 v74, 0\n" \
"v_mov_b32 v75, 0\n" \
"s_nop 1\n" \
"s_waitcnt lgkmcnt(6)\n" \
"v_smfmac_f32_16x16x64_bf16 v[60:63], v[0:3], a[0:7], v200\n" \
"v_smfmac_f32_16x16x64_bf16 v[64:67], v[0:3], a[56:63], v200\n" \
"v_smfmac_f32_16x16x64_bf16 v[68:71], v[0:3], a[112:119], v200\n" \
"v_smfmac_f32_16x16x64_bf16 v[72:75], v[0:3], a[168:175], v200\n" \
"s_waitcnt lgkmcnt(5)\n" \
"v_smfmac_f32_16x16x64_bf16 v[60:63], v[4:7], a[8:15], v200\n" \
"v_smfmac_f32_16x16x64_bf16 v[64:67], v[4:7], a[64:71], v200\n" \
"v_smfmac_f32_16x16x64_bf16 v[68:71], v[4:7], a[120:127], v200\n" \
"v_smfmac_f32_16x16x64_bf16 v[72:75], v[4:7], a[176:183], v200\n" \
"s_waitcnt lgkmcnt(4)\n" \
"v_smfmac_f32_16x16x64_bf16 v[60:63], v[8:11], a[16:23], v200\n" \
"v_smfmac_f32_16x16x64_bf16 v[64:67], v[8:11], a[72:79], v200\n" \
"v_smfmac_f32_16x16x64_bf16 v[68:71], v[8:11], a[128:135], v200\n" \
"v_smfmac_f32_16x16x64_bf16 v[72:75], v[8:11], a[184:191], v200\n" \
"s_waitcnt lgkmcnt(3)\n" \
"v_smfmac_f32_16x16x64_bf16 v[60:63], v[12:15], a[24:31], v200\n" \
"v_smfmac_f32_16x16x64_bf16 v[64:67], v[12:15], a[80:87], v200\n" \
"v_smfmac_f32_16x16x64_bf16 v[68:71], v[12:15], a[136:143], v200\n" \
"v_smfmac_f32_16x16x64_bf16 v[72:75], v[12:15], a[192:199], v200\n" \
"s_waitcnt lgkmcnt(2)\n" \
"v_smfmac_f32_16x16x64_bf16 v[60:63], v[16:19], a[32:39], v200\n" \
"v_smfmac_f32_16x16x64_bf16 v[64:67], v[16:19], a[88:95], v200\n" \
"v_smfmac_f32_16x16x64_bf16 v[68:71], v[16:19], a[144:151], v200\n" \
"v_smfmac_f32_16x16x64_bf16 v[72:75], v[16:19], a[200:207], v200\n" \
"s_waitcnt lgkmcnt(1)\n" \
"v_smfmac_f32_16x16x64_bf16 v[60:63], v[20:23], a[40:47], v200\n" \
"v_smfmac_f32_16x16x64_bf16 v[64:67], v[20:23], a[96:103], v200\n" \
"v_smfmac_f32_16x16x64_bf16 v[68:71], v[20:23], a[152:159], v200\n" \
"v_smfmac_f32_16x16x64_bf16 v[72:75], v[20:23], a[208:215], v200\n" \
"s_waitcnt lgkmcnt(0)\n" \
"v_smfmac_f32_16x16x64_bf16 v[60:63], v[24:27], a[48:55], v200\n" \
"v_smfmac_f32_16x16x64_bf16 v[64:67], v[24:27], a[104:111], v200\n" \
"v_smfmac_f32_16x16x64_bf16 v[68:71], v[24:27], a[160:167], v200\n" \
"v_smfmac_f32_16x16x64_bf16 v[72:75], v[24:27], a[216:223], v200\n" \
"s_nop 7\n" \
"v_permlane32_swap_b32 v60, v64\n" \
"v_permlane32_swap_b32 v61, v65\n" \
"v_permlane32_swap_b32 v62, v66\n" \
"v_permlane32_swap_b32 v63, v67\n" \
"v_add_f32 v60, v60, v64\n" \
"v_add_f32 v61, v61, v65\n" \
"v_add_f32 v62, v62, v66\n" \
"v_add_f32 v63, v63, v67\n" \
"v_cvt_pk_bf16_f32 v184, v60, v61\n" \
"v_cvt_pk_bf16_f32 v185, v62, v63\n" \
"ds_write_b64 v217, v[184:185] offset:0\n" \
"v_permlane32_swap_b32 v68, v72\n" \
"v_permlane32_swap_b32 v69, v73\n" \
"v_permlane32_swap_b32 v70, v74\n" \
"v_permlane32_swap_b32 v71, v75\n" \
"v_add_f32 v68, v68, v72\n" \
"v_add_f32 v69, v69, v73\n" \
"v_add_f32 v70, v70, v74\n" \
"v_add_f32 v71, v71, v75\n" \
"v_cvt_pk_bf16_f32 v186, v68, v69\n" \
"v_cvt_pk_bf16_f32 v187, v70, v71\n" \
"ds_write_b64 v217, v[186:187] offset:512\n" \
"ds_read_b64_tr_b16 v[0:1], v218 offset:0\n" \
"ds_read_b64_tr_b16 v[2:3], v218 offset:64\n" \
"s_add_u32 s43, s40, 0\n" \
"s_mul_i32 s43, s43, 114688\n" \
"s_waitcnt lgkmcnt(0)\n" \
"buffer_store_dwordx4 v[0:3], v219, s[16:19], s43 offen sc1\n" \
"s_waitcnt vmcnt(0)\n" \
"s_barrier\n" \
"s_add_u32 s43, s30, s40\n" \
"v_mov_b32 v241, s43\n" \
"buffer_store_dword v241, v220, s[4:7], 0 offen sc1\n" \
"ds_read_b128 v[0:3], v216 offset:7168\n" \
"ds_read_b128 v[4:7], v216 offset:8192\n" \
"ds_read_b128 v[8:11], v216 offset:9216\n" \
"ds_read_b128 v[12:15], v216 offset:10240\n" \
"ds_read_b128 v[16:19], v216 offset:11264\n" \
"ds_read_b128 v[20:23], v216 offset:12288\n" \
"ds_read_b128 v[24:27], v216 offset:13312\n" \
"v_mov_b32 v60, 0\n" \
"v_mov_b32 v61, 0\n" \
"v_mov_b32 v62, 0\n" \
"v_mov_b32 v63, 0\n" \
"v_mov_b32 v64, 0\n" \
"v_mov_b32 v65, 0\n" \
"v_mov_b32 v66, 0\n" \
"v_mov_b32 v67, 0\n" \
"v_mov_b32 v68, 0\n" \
"v_mov_b32 v69, 0\n" \
"v_mov_b32 v70, 0\n" \
"v_mov_b32 v71, 0\n" \
"v_mov_b32 v72, 0\n" \
"v_mov_b32 v73, 0\n" \
"v_mov_b32 v74, 0\n" \
"v_mov_b32 v75, 0\n" \
"s_nop 1\n" \
"s_waitcnt lgkmcnt(6)\n" \
"v_smfmac_f32_16x16x64_bf16 v[60:63], v[0:3], a[0:7], v200\n" \
"v_smfmac_f32_16x16x64_bf16 v[64:67], v[0:3], a[56:63], v200\n" \
"v_smfmac_f32_16x16x64_bf16 v[68:71], v[0:3], a[112:119], v200\n" \
"v_smfmac_f32_16x16x64_bf16 v[72:75], v[0:3], a[168:175], v200\n" \
"s_waitcnt lgkmcnt(5)\n" \
"v_smfmac_f32_16x16x64_bf16 v[60:63], v[4:7], a[8:15], v200\n" \
"v_smfmac_f32_16x16x64_bf16 v[64:67], v[4:7], a[64:71], v200\n" \
"v_smfmac_f32_16x16x64_bf16 v[68:71], v[4:7], a[120:127], v200\n" \
"v_smfmac_f32_16x16x64_bf16 v[72:75], v[4:7], a[176:183], v200\n" \
"s_waitcnt lgkmcnt(4)\n" \
"v_smfmac_f32_16x16x64_bf16 v[60:63], v[8:11], a[16:23], v200\n" \
"v_smfmac_f32_16x16x64_bf16 v[64:67], v[8:11], a[72:79], v200\n" \
"v_smfmac_f32_16x16x64_bf16 v[68:71], v[8:11], a[128:135], v200\n" \
"v_smfmac_f32_16x16x64_bf16 v[72:75], v[8:11], a[184:191], v200\n" \
"s_waitcnt lgkmcnt(3)\n" \
"v_smfmac_f32_16x16x64_bf16 v[60:63], v[12:15], a[24:31], v200\n" \
"v_smfmac_f32_16x16x64_bf16 v[64:67], v[12:15], a[80:87], v200\n" \
"v_smfmac_f32_16x16x64_bf16 v[68:71], v[12:15], a[136:143], v200\n" \
"v_smfmac_f32_16x16x64_bf16 v[72:75], v[12:15], a[192:199], v200\n" \
"s_waitcnt lgkmcnt(2)\n" \
"v_smfmac_f32_16x16x64_bf16 v[60:63], v[16:19], a[32:39], v200\n" \
"v_smfmac_f32_16x16x64_bf16 v[64:67], v[16:19], a[88:95], v200\n" \
"v_smfmac_f32_16x16x64_bf16 v[68:71], v[16:19], a[144:151], v200\n" \
"v_smfmac_f32_16x16x64_bf16 v[72:75], v[16:19], a[200:207], v200\n" \
"s_waitcnt lgkmcnt(1)\n" \
"v_smfmac_f32_16x16x64_bf16 v[60:63], v[20:23], a[40:47], v200\n" \
"v_smfmac_f32_16x16x64_bf16 v[64:67], v[20:23], a[96:103], v200\n" \
"v_smfmac_f32_16x16x64_bf16 v[68:71], v[20:23], a[152:159], v200\n" \
"v_smfmac_f32_16x16x64_bf16 v[72:75], v[20:23], a[208:215], v200\n" \
"s_waitcnt lgkmcnt(0)\n" \
"v_smfmac_f32_16x16x64_bf16 v[60:63], v[24:27], a[48:55], v200\n" \
"v_smfmac_f32_16x16x64_bf16 v[64:67], v[24:27], a[104:111], v200\n" \
"v_smfmac_f32_16x16x64_bf16 v[68:71], v[24:27], a[160:167], v200\n" \
"v_smfmac_f32_16x16x64_bf16 v[72:75], v[24:27], a[216:223], v200\n" \
"s_nop 7\n" \
"v_permlane32_swap_b32 v60, v64\n" \
"v_permlane32_swap_b32 v61, v65\n" \
"v_permlane32_swap_b32 v62, v66\n" \
"v_permlane32_swap_b32 v63, v67\n" \
"v_add_f32 v60, v60, v64\n" \
"v_add_f32 v61, v61, v65\n" \
"v_add_f32 v62, v62, v66\n" \
"v_add_f32 v63, v63, v67\n" \
"v_cvt_pk_bf16_f32 v184, v60, v61\n" \
"v_cvt_pk_bf16_f32 v185, v62, v63\n" \
"ds_write_b64 v217, v[184:185] offset:0\n" \
"v_permlane32_swap_b32 v68, v72\n" \
"v_permlane32_swap_b32 v69, v73\n" \
"v_permlane32_swap_b32 v70, v74\n" \
"v_permlane32_swap_b32 v71, v75\n" \
"v_add_f32 v68, v68, v72\n" \
"v_add_f32 v69, v69, v73\n" \
"v_add_f32 v70, v70, v74\n" \
"v_add_f32 v71, v71, v75\n" \
"v_cvt_pk_bf16_f32 v186, v68, v69\n" \
"v_cvt_pk_bf16_f32 v187, v70, v71\n" \
"ds_write_b64 v217, v[186:187] offset:512\n" \
"ds_read_b64_tr_b16 v[0:1], v218 offset:0\n" \
"ds_read_b64_tr_b16 v[2:3], v218 offset:64\n" \
"s_add_u32 s43, s40, 1\n" \
"s_mul_i32 s43, s43, 114688\n" \
"s_waitcnt lgkmcnt(0)\n" \
"buffer_store_dwordx4 v[0:3], v219, s[16:19], s43 offen sc1\n" \
"s_waitcnt vmcnt(0)\n" \
"s_barrier\n" \
"s_add_u32 s43, s30, s40\n" \
"s_add_u32 s43, s43, 1\n" \
"v_mov_b32 v241, s43\n" \
"buffer_store_dword v241, v220, s[4:7], 0 offen sc1\n" \
".LX1_end:\n" \
"s_endpgm\n" \
".p2align 6\n" \
"fk_role_X2:\n" \
"ds_read_b32 v200, v255 offset:0\n" \
"ds_read_b32 v201, v255 offset:256\n" \
"ds_read_b32 v202, v255 offset:512\n" \
"ds_read_b32 v203, v255 offset:768\n" \
"ds_read_b32 v204, v255 offset:1024\n" \
"ds_read_b32 v205, v255 offset:1280\n" \
"ds_read_b32 v206, v255 offset:1536\n" \
"ds_read_b32 v207, v255 offset:1792\n" \
"ds_read_b32 v208, v255 offset:2048\n" \
"ds_read_b32 v209, v255 offset:2304\n" \
"ds_read_b32 v210, v255 offset:2560\n" \
"ds_read_b32 v211, v255 offset:2816\n" \
"ds_read_b32 v212, v255 offset:3072\n" \
"ds_read_b32 v213, v255 offset:3328\n" \
"ds_read_b32 v214, v255 offset:3584\n" \
"ds_read_b32 v215, v255 offset:3840\n" \
"ds_read_b32 v216, v255 offset:4096\n" \
"ds_read_b32 v217, v255 offset:4352\n" \
"ds_read_b32 v218, v255 offset:4608\n" \
"ds_read_b32 v219, v255 offset:4864\n" \
"ds_read_b32 v220, v255 offset:5120\n" \
"ds_read_b32 v221, v255 offset:5376\n" \
"ds_read_b32 v254, v255 offset:7936\n" \
"s_waitcnt lgkmcnt(0)\n" \
"v_readlane_b32 s4, v254, 0\n" \
"v_readlane_b32 s5, v254, 1\n" \
"v_readlane_b32 s6, v254, 2\n" \
"v_readlane_b32 s7, v254, 3\n" \
"v_readlane_b32 s8, v254, 4\n" \
"v_readlane_b32 s9, v254, 5\n" \
"v_readlane_b32 s10, v254, 6\n" \
"v_readlane_b32 s11, v254, 7\n" \
"v_readlane_b32 s12, v254, 8\n" \
"v_readlane_b32 s13, v254, 9\n" \
"v_readlane_b32 s14, v254, 10\n" \
"v_readlane_b32 s15, v254, 11\n" \
"v_readlane_b32 s16, v254, 12\n" \
"v_readlane_b32 s17, v254, 13\n" \
"v_readlane_b32 s18, v254, 14\n" \
"v_readlane_b32 s19, v254, 15\n" \
"v_readlane_b32 s20, v254, 16\n" \
"v_readlane_b32 s21, v254, 17\n" \
"v_readlane_b32 s22, v254, 18\n" \
"v_readlane_b32 s23, v254, 19\n" \
"v_readlane_b32 s24, v254, 20\n" \
"v_readlane_b32 s25, v254, 21\n" \
"v_readlane_b32 s26, v254, 22\n" \
"v_readlane_b32 s27, v254, 23\n" \
"v_readlane_b32 s28, v254, 24\n" \
"v_readlane_b32 s29, v254, 25\n" \
"v_readlane_b32 s30, v254, 26\n" \
"v_readlane_b32 s31, v254, 27\n" \
"v_readlane_b32 s32, v254, 28\n" \
"v_readlane_b32 s33, v254, 29\n" \
"v_readlane_b32 s34, v254, 30\n" \
"v_readlane_b32 s35, v254, 31\n" \
"v_readlane_b32 s36, v254, 32\n" \
"v_readlane_b32 s37, v254, 33\n" \
"v_readlane_b32 s38, v254, 34\n" \
"v_readlane_b32 s39, v254, 35\n" \
"s_mov_b32 s40, 0\n" \
"s_mov_b32 s51, 0\n" \
"s_nop 0\n" \
"v_mov_b32 v252, s38\n" \
"s_nop 4\n" \
"v_mov_b32 v100, -1\n" \
"v_mov_b32 v101, -1\n" \
"v_mov_b32 v102, -1\n" \
"v_mov_b32 v103, -1\n" \
"s_mov_b32 s35, -2\n" \
"s_mov_b32 s2, 0\n" \
"s_mov_b32 s3, -1\n" \
"v_writelane_b32 v251, s6, 0\n" \
"v_writelane_b32 v251, s10, 1\n" \
"v_writelane_b32 v251, s14, 2\n" \
"v_writelane_b32 v251, s18, 3\n" \
"s_mov_b32 s6, 0\n" \
"s_mov_b32 s10, 0\n" \
"s_mov_b32 s14, 0\n" \
"s_mov_b32 s18, 0\n" \
"s_mov_b32 s40, -1\n" \
"s_branch .LX2_pre\n" \
".LX2_wet:\n" \
"s_waitcnt vmcnt(0)\n" \
"v_readlane_b32 s6, v251, 0\n" \
"v_readlane_b32 s10, v251, 1\n" \
"v_readlane_b32 s14, v251, 2\n" \
"v_readlane_b32 s18, v251, 3\n" \
"s_nop 4\n" \
"s_add_u32 s43, s30, 1\n" \
"s_mov_b32 s50, 0\n" \
".LX2_w81l:\n" \
"buffer_load_dword v240, v204, s[4:7], 0 offen sc1\n" \
"s_waitcnt vmcnt(0)\n" \
".LX2_w81c:\n" \
"v_subrev_u32 v241, s43, v240\n" \
"v_cmp_gt_u32 vcc, 0x100, v241\n" \
"s_cmp_eq_u64 vcc, -1\n" \
"s_cbranch_scc1 .LX2_w81d\n" \
"s_sleep 2\n" \
"s_add_u32 s50, s50, 1\n" \
"s_cmp_lt_u32 s50, 0x20000\n" \
"s_cbranch_scc1 .LX2_w81l\n" \
"s_branch .LX2_end\n" \
".LX2_w81d:\n" \
"v_readlane_b32 s43, v202, 0\n" \
"s_nop 4\n" \
"buffer_load_dwordx4 a[0:3], v201, s[8:11], s43 offen sc1\n" \
"buffer_load_dwordx4 a[4:7], v201, s[8:11], s43 offen offset:1024 sc1\n" \
"v_readlane_b32 s43, v202, 7\n" \
"s_nop 4\n" \
"buffer_load_dwordx4 a[56:59], v201, s[8:11], s43 offen sc1\n" \
"buffer_load_dwordx4 a[60:63], v201, s[8:11], s43 offen offset:1024 sc1\n" \
"v_readlane_b32 s43, v202, 14\n" \
"s_nop 4\n" \
"buffer_load_dwordx4 a[112:115], v201, s[8:11], s43 offen sc1\n" \
"buffer_load_dwordx4 a[116:119], v201, s[8:11], s43 offen offset:1024 sc1\n" \
"v_readlane_b32 s43, v202, 21\n" \
"s_nop 4\n" \
"buffer_load_dwordx4 a[168:171], v201, s[8:11], s43 offen sc1\n" \
"buffer_load_dwordx4 a[172:175], v201, s[8:11], s43 offen offset:1024 sc1\n" \
"v_readlane_b32 s43, v202, 28\n" \
"s_nop 4\n" \
"buffer_load_dwordx4 a[224:227], v201, s[8:11], s43 offen sc1\n" \
"buffer_load_dwordx4 a[228:231], v201, s[8:11], s43 offen offset:1024 sc1\n" \
"v_readlane_b32 s43, v202, 35\n" \
"s_nop 4\n" \
"buffer_load_dwordx4 v[128:131], v201, s[8:11], s43 offen sc1\n" \
"buffer_load_dwordx4 v[132:135], v201, s[8:11], s43 offen offset:1024 sc1\n" \
"s_add_u32 s43, s30, 2\n" \
"s_mov_b32 s50, 0\n" \
".LX2_w82l:\n" \
"buffer_load_dword v240, v204, s[4:7], 0 offen sc1\n" \
"s_waitcnt vmcnt(0)\n" \
".LX2_w82c:\n" \
"v_subrev_u32 v241, s43, v240\n" \
"v_cmp_gt_u32 vcc, 0x100, v241\n" \
"s_cmp_eq_u64 vcc, -1\n" \
"s_cbranch_scc1 .LX2_w82d\n" \
"s_sleep 2\n" \
"s_add_u32 s50, s50, 1\n" \
"s_cmp_lt_u32 s50, 0x20000\n" \
"s_cbranch_scc1 .LX2_w82l\n" \
"s_branch .LX2_end\n" \
".LX2_w82d:\n" \
"v_readlane_b32 s43, v202, 1\n" \
"s_nop 4\n" \
"buffer_load_dwordx4 a[8:11], v201, s[8:11], s43 offen sc1\n" \
"buffer_load_dwordx4 a[12:15], v201, s[8:11], s43 offen offset:1024 sc1\n" \
"v_readlane_b32 s43, v202, 8\n" \
"s_nop 4\n" \
"buffer_load_dwordx4 a[64:67], v201, s[8:11], s43 offen sc1\n" \
"buffer_load_dwordx4 a[68:71], v201, s[8:11], s43 offen offset:1024 sc1\n" \
"v_readlane_b32 s43, v202, 15\n" \
"s_nop 4\n" \
"buffer_load_dwordx4 a[120:123], v201, s[8:11], s43 offen sc1\n" \
"buffer_load_dwordx4 a[124:127], v201, s[8:11], s43 offen offset:1024 sc1\n" \
"v_readlane_b32 s43, v202, 22\n" \
"s_nop 4\n" \
"buffer_load_dwordx4 a[176:179], v201, s[8:11], s43 offen sc1\n" \
"buffer_load_dwordx4 a[180:183], v201, s[8:11], s43 offen offset:1024 sc1\n" \
"v_readlane_b32 s43, v202, 29\n" \
"s_nop 4\n" \
"buffer_load_dwordx4 a[232:235], v201, s[8:11], s43 offen sc1\n" \
"buffer_load_dwordx4 a[236:239], v201, s[8:11], s43 offen offset:1024 sc1\n" \
"v_readlane_b32 s43, v202, 36\n" \
"s_nop 4\n" \
"buffer_load_dwordx4 v[136:139], v201, s[8:11], s43 offen sc1\n" \
"buffer_load_dwordx4 v[140:143], v201, s[8:11], s43 offen offset:1024 sc1\n" \
"s_add_u32 s43, s30, 3\n" \
"s_mov_b32 s50, 0\n" \
".LX2_w83l:\n" \
"buffer_load_dword v240, v204, s[4:7], 0 offen sc1\n" \
"s_waitcnt vmcnt(0)\n" \
".LX2_w83c:\n" \
"v_subrev_u32 v241, s43, v240\n" \
"v_cmp_gt_u32 vcc, 0x100, v241\n" \
"s_cmp_eq_u64 vcc, -1\n" \
"s_cbranch_scc1 .LX2_w83d\n" \
"s_sleep 2\n" \
"s_add_u32 s50, s50, 1\n" \
"s_cmp_lt_u32 s50, 0x20000\n" \
"s_cbranch_scc1 .LX2_w83l\n" \
"s_branch .LX2_end\n" \
".LX2_w83d:\n" \
"v_readlane_b32 s43, v202, 2\n" \
"s_nop 4\n" \
"buffer_load_dwordx4 a[16:19], v201, s[8:11], s43 offen sc1\n" \
"buffer_load_dwordx4 a[20:23], v201, s[8:11], s43 offen offset:1024 sc1\n" \
"v_readlane_b32 s43, v202, 9\n" \
"s_nop 4\n" \
"buffer_load_dwordx4 a[72:75], v201, s[8:11], s43 offen sc1\n" \
"buffer_load_dwordx4 a[76:79], v201, s[8:11], s43 offen offset:1024 sc1\n" \
"v_readlane_b32 s43, v202, 16\n" \
"s_nop 4\n" \
"buffer_load_dwordx4 a[128:131], v201, s[8:11], s43 offen sc1\n" \
"buffer_load_dwordx4 a[132:135], v201, s[8:11], s43 offen offset:1024 sc1\n" \
"v_readlane_b32 s43, v202, 23\n" \
"s_nop 4\n" \
"buffer_load_dwordx4 a[184:187], v201, s[8:11], s43 offen sc1\n" \
"buffer_load_dwordx4 a[188:191], v201, s[8:11], s43 offen offset:1024 sc1\n" \
"v_readlane_b32 s43, v202, 30\n" \
"s_nop 4\n" \
"buffer_load_dwordx4 a[240:243], v201, s[8:11], s43 offen sc1\n" \
"buffer_load_dwordx4 a[244:247], v201, s[8:11], s43 offen offset:1024 sc1\n" \
"v_readlane_b32 s43, v202, 37\n" \
"s_nop 4\n" \
"buffer_load_dwordx4 v[144:147], v201, s[8:11], s43 offen sc1\n" \
"buffer_load_dwordx4 v[148:151], v201, s[8:11], s43 offen offset:1024 sc1\n" \
"s_add_u32 s43, s30, 4\n" \
"s_mov_b32 s50, 0\n" \
".LX2_w84l:\n" \
"buffer_load_dword v240, v204, s[4:7], 0 offen sc1\n" \
"s_waitcnt vmcnt(0)\n" \
".LX2_w84c:\n" \
"v_subrev_u32 v241, s43, v240\n" \
"v_cmp_gt_u32 vcc, 0x100, v241\n" \
"s_cmp_eq_u64 vcc, -1\n" \
"s_cbranch_scc1 .LX2_w84d\n" \
"s_sleep 2\n" \
"s_add_u32 s50, s50, 1\n" \
"s_cmp_lt_u32 s50, 0x20000\n" \
"s_cbranch_scc1 .LX2_w84l\n" \
"s_branch .LX2_end\n" \
".LX2_w84d:\n" \
"v_readlane_b32 s43, v202, 3\n" \
"s_nop 4\n" \
"buffer_load_dwordx4 a[24:27], v201, s[8:11], s43 offen sc1\n" \
"buffer_load_dwordx4 a[28:31], v201, s[8:11], s43 offen offset:1024 sc1\n" \
"v_readlane_b32 s43, v202, 10\n" \
"s_nop 4\n" \
"buffer_load_dwordx4 a[80:83], v201, s[8:11], s43 offen sc1\n" \
"buffer_load_dwordx4 a[84:87], v201, s[8:11], s43 offen offset:1024 sc1\n" \
"v_readlane_b32 s43, v202, 17\n" \
"s_nop 4\n" \
"buffer_load_dwordx4 a[136:139], v201, s[8:11], s43 offen sc1\n" \
"buffer_load_dwordx4 a[140:143], v201, s[8:11], s43 offen offset:1024 sc1\n" \
"v_readlane_b32 s43, v202, 24\n" \
"s_nop 4\n" \
"buffer_load_dwordx4 a[192:195], v201, s[8:11], s43 offen sc1\n" \
"buffer_load_dwordx4 a[196:199], v201, s[8:11], s43 offen offset:1024 sc1\n" \
"v_readlane_b32 s43, v202, 31\n" \
"s_nop 4\n" \
"buffer_load_dwordx4 a[248:251], v201, s[8:11], s43 offen sc1\n" \
"buffer_load_dwordx4 a[252:255], v201, s[8:11], s43 offen offset:1024 sc1\n" \
"v_readlane_b32 s43, v202, 38\n" \
"s_nop 4\n" \
"buffer_load_dwordx4 v[152:155], v201, s[8:11], s43 offen sc1\n" \
"buffer_load_dwordx4 v[156:159], v201, s[8:11], s43 offen offset:1024 sc1\n" \
"s_add_u32 s43, s30, 5\n" \
"s_mov_b32 s50, 0\n" \
".LX2_w85l:\n" \
"buffer_load_dword v240, v204, s[4:7], 0 offen sc1\n" \
"s_waitcnt vmcnt(0)\n" \
".LX2_w85c:\n" \
"v_subrev_u32 v241, s43, v240\n" \
"v_cmp_gt_u32 vcc, 0x100, v241\n" \
"s_cmp_eq_u64 vcc, -1\n" \
"s_cbranch_scc1 .LX2_w85d\n" \
"s_sleep 2\n" \
"s_add_u32 s50, s50, 1\n" \
"s_cmp_lt_u32 s50, 0x20000\n" \
"s_cbranch_scc1 .LX2_w85l\n" \
"s_branch .LX2_end\n" \
".LX2_w85d:\n" \
"v_readlane_b32 s43, v202, 4\n" \
"s_nop 4\n" \
"buffer_load_dwordx4 a[32:35], v201, s[8:11], s43 offen sc1\n" \
"buffer_load_dwordx4 a[36:39], v201, s[8:11], s43 offen offset:1024 sc1\n" \
"v_readlane_b32 s43, v202, 11\n" \
"s_nop 4\n" \
"buffer_load_dwordx4 a[88:91], v201, s[8:11], s43 offen sc1\n" \
"buffer_load_dwordx4 a[92:95], v201, s[8:11], s43 offen offset:1024 sc1\n" \
"v_readlane_b32 s43, v202, 18\n" \
"s_nop 4\n" \
"buffer_load_dwordx4 a[144:147], v201, s[8:11], s43 offen sc1\n" \
"buffer_load_dwordx4 a[148:151], v201, s[8:11], s43 offen offset:1024 sc1\n" \
"v_readlane_b32 s43, v202, 25\n" \
"s_nop 4\n" \
"buffer_load_dwordx4 a[200:203], v201, s[8:11], s43 offen sc1\n" \
"buffer_load_dwordx4 a[204:207], v201, s[8:11], s43 offen offset:1024 sc1\n" \
"v_readlane_b32 s43, v202, 32\n" \
"s_nop 4\n" \
"buffer_load_dwordx4 v[104:107], v201, s[8:11], s43 offen sc1\n" \
"buffer_load_dwordx4 v[108:111], v201, s[8:11], s43 offen offset:1024 sc1\n" \
"v_readlane_b32 s43, v202, 39\n" \
"s_nop 4\n" \
"buffer_load_dwordx4 v[160:163], v201, s[8:11], s43 offen sc1\n" \
"buffer_load_dwordx4 v[164:167], v201, s[8:11], s43 offen offset:1024 sc1\n" \
"s_add_u32 s43, s30, 6\n" \
"s_mov_b32 s50, 0\n" \
".LX2_w86l:\n" \
"buffer_load_dword v240, v204, s[4:7], 0 offen sc1\n" \
"s_waitcnt vmcnt(0)\n" \
".LX2_w86c:\n" \
"v_subrev_u32 v241, s43, v240\n" \
"v_cmp_gt_u32 vcc, 0x100, v241\n" \
"s_cmp_eq_u64 vcc, -1\n" \
"s_cbranch_scc1 .LX2_w86d\n" \
"s_sleep 2\n" \
"s_add_u32 s50, s50, 1\n" \
"s_cmp_lt_u32 s50, 0x20000\n" \
"s_cbranch_scc1 .LX2_w86l\n" \
"s_branch .LX2_end\n" \
".LX2_w86d:\n" \
"v_readlane_b32 s43, v202, 5\n" \
"s_nop 4\n" \
"buffer_load_dwordx4 a[40:43], v201, s[8:11], s43 offen sc1\n" \
"buffer_load_dwordx4 a[44:47], v201, s[8:11], s43 offen offset:1024 sc1\n" \
"v_readlane_b32 s43, v202, 12\n" \
"s_nop 4\n" \
"buffer_load_dwordx4 a[96:99], v201, s[8:11], s43 offen sc1\n" \
"buffer_load_dwordx4 a[100:103], v201, s[8:11], s43 offen offset:1024 sc1\n" \
"v_readlane_b32 s43, v202, 19\n" \
"s_nop 4\n" \
"buffer_load_dwordx4 a[152:155], v201, s[8:11], s43 offen sc1\n" \
"buffer_load_dwordx4 a[156:159], v201, s[8:11], s43 offen offset:1024 sc1\n" \
"v_readlane_b32 s43, v202, 26\n" \
"s_nop 4\n" \
"buffer_load_dwordx4 a[208:211], v201, s[8:11], s43 offen sc1\n" \
"buffer_load_dwordx4 a[212:215], v201, s[8:11], s43 offen offset:1024 sc1\n" \
"v_readlane_b32 s43, v202, 33\n" \
"s_nop 4\n" \
"buffer_load_dwordx4 v[112:115], v201, s[8:11], s43 offen sc1\n" \
"buffer_load_dwordx4 v[116:119], v201, s[8:11], s43 offen offset:1024 sc1\n" \
"v_readlane_b32 s43, v202, 40\n" \
"s_nop 4\n" \
"buffer_load_dwordx4 v[168:171], v201, s[8:11], s43 offen sc1\n" \
"buffer_load_dwordx4 v[172:175], v201, s[8:11], s43 offen offset:1024 sc1\n" \
"s_add_u32 s43, s30, 7\n" \
"s_mov_b32 s50, 0\n" \
".LX2_w87l:\n" \
"buffer_load_dword v240, v204, s[4:7], 0 offen sc1\n" \
"s_waitcnt vmcnt(0)\n" \
".LX2_w87c:\n" \
"v_subrev_u32 v241, s43, v240\n" \
"v_cmp_gt_u32 vcc, 0x100, v241\n" \
"s_cmp_eq_u64 vcc, -1\n" \
"s_cbranch_scc1 .LX2_w87d\n" \
"s_sleep 2\n" \
"s_add_u32 s50, s50, 1\n" \
"s_cmp_lt_u32 s50, 0x20000\n" \
"s_cbranch_scc1 .LX2_w87l\n" \
"s_branch .LX2_end\n" \
".LX2_w87d:\n" \
"v_readlane_b32 s43, v202, 6\n" \
"s_nop 4\n" \
"buffer_load_dwordx4 a[48:51], v201, s[8:11], s43 offen sc1\n" \
"buffer_load_dwordx4 a[52:55], v201, s[8:11], s43 offen offset:1024 sc1\n" \
"v_readlane_b32 s43, v202, 13\n" \
"s_nop 4\n" \
"buffer_load_dwordx4 a[104:107], v201, s[8:11], s43 offen sc1\n" \
"buffer_load_dwordx4 a[108:111], v201, s[8:11], s43 offen offset:1024 sc1\n" \
"v_readlane_b32 s43, v202, 20\n" \
"s_nop 4\n" \
"buffer_load_dwordx4 a[160:163], v201, s[8:11], s43 offen sc1\n" \
"buffer_load_dwordx4 a[164:167], v201, s[8:11], s43 offen offset:1024 sc1\n" \
"v_readlane_b32 s43, v202, 27\n" \
"s_nop 4\n" \
"buffer_load_dwordx4 a[216:219], v201, s[8:11], s43 offen sc1\n" \
"buffer_load_dwordx4 a[220:223], v201, s[8:11], s43 offen offset:1024 sc1\n" \
"v_readlane_b32 s43, v202, 34\n" \
"s_nop 4\n" \
"buffer_load_dwordx4 v[120:123], v201, s[8:11], s43 offen sc1\n" \
"buffer_load_dwordx4 v[124:127], v201, s[8:11], s43 offen offset:1024 sc1\n" \
"v_readlane_b32 s43, v202, 41\n" \
"s_nop 4\n" \
"buffer_load_dwordx4 v[176:179], v201, s[8:11], s43 offen sc1\n" \
"buffer_load_dwordx4 v[180:183], v201, s[8:11], s43 offen offset:1024 sc1\n" \
"s_add_u32 s43, s30, 9\n" \
"s_mov_b32 s50, 0\n" \
".LX2_w88l:\n" \
"buffer_load_dword v240, v215, s[4:7], 0 offen sc1\n" \
"s_waitcnt vmcnt(0)\n" \
".LX2_w88c:\n" \
"v_subrev_u32 v241, s43, v240\n" \
"v_cmp_gt_u32 vcc, 0x100, v241\n" \
"s_cmp_eq_u64 vcc, -1\n" \
"s_cbranch_scc1 .LX2_w88d\n" \
"s_sleep 2\n" \
"s_add_u32 s50, s50, 1\n" \
"s_cmp_lt_u32 s50, 0x20000\n" \
"s_cbranch_scc1 .LX2_w88l\n" \
"s_branch .LX2_end\n" \
".LX2_w88d:\n" \
"s_add_u32 s50, s40, 0\n" \
"s_mul_i32 s50, s50, 458752\n" \
"s_add_u32 s50, s50, s34\n" \
"buffer_load_dwordx4 v[28:31], v201, s[12:15], s50 offen sc1\n" \
"buffer_load_dwordx4 v[32:35], v201, s[12:15], s50 offen offset:1024 sc1\n" \
"buffer_load_dwordx4 v[36:39], v201, s[12:15], s50 offen offset:2048 sc1\n" \
"buffer_load_dwordx4 v[40:43], v201, s[12:15], s50 offen offset:3072 sc1\n" \
"s_add_u32 s43, s30, 1\n" \
"s_mov_b32 s50, 0\n" \
".LX2_w89l:\n" \
"buffer_load_dword v240, v205, s[4:7], 0 offen sc1\n" \
"s_waitcnt vmcnt(0)\n" \
".LX2_w89c:\n" \
"v_subrev_u32 v241, s43, v240\n" \
"v_cmp_gt_u32 vcc, 0x100, v241\n" \
"s_cmp_eq_u64 vcc, -1\n" \
"s_cbranch_scc1 .LX2_w89d\n" \
"s_sleep 2\n" \
"s_add_u32 s50, s50, 1\n" \
"s_cmp_lt_u32 s50, 0x20000\n" \
"s_cbranch_scc1 .LX2_w89l\n" \
"s_branch .LX2_end\n" \
".LX2_w89d:\n" \
"s_add_u32 s43, s30, 1\n" \
"s_mov_b32 s50, 0\n" \
".LX2_w90l:\n" \
"buffer_load_dword v240, v206, s[4:7], 0 offen sc1\n" \
"s_waitcnt vmcnt(0)\n" \
".LX2_w90c:\n" \
"v_subrev_u32 v241, s43, v240\n" \
"v_cmp_gt_u32 vcc, 0x100, v241\n" \
"s_cmp_eq_u64 vcc, -1\n" \
"s_cbranch_scc1 .LX2_w90d\n" \
"s_sleep 2\n" \
"s_add_u32 s50, s50, 1\n" \
"s_cmp_lt_u32 s50, 0x20000\n" \
"s_cbranch_scc1 .LX2_w90l\n" \
"s_branch .LX2_end\n" \
".LX2_w90d:\n" \
".LX2_pre:\n" \
"s_barrier\n" \
".LX2_loop:\n" \
"s_add_u32 s43, s40, 2\n" \
"s_and_b32 s43, s43, 3\n" \
"s_bitcmp1_b32 s43, 0\n" \
"s_cselect_b32 s49, s32, s31\n" \
"s_lshl_b32 s43, s43, 19\n" \
"s_add_u32 s49, s49, s43\n" \
"ds_read_b128 v[0:3], v216 offset:0\n" \
"ds_read_b128 v[4:7], v216 offset:1024\n" \
"ds_read_b128 v[8:11], v216 offset:2048\n" \
"ds_read_b128 v[12:15], v216 offset:3072\n" \
"ds_read_b128 v[16:19], v216 offset:4096\n" \
"ds_read_b128 v[20:23], v216 offset:5120\n" \
"ds_read_b128 v[24:27], v216 offset:6144\n" \
"v_mov_b32 v60, 0\n" \
"v_mov_b32 v61, 0\n" \
"v_mov_b32 v62, 0\n" \
"v_mov_b32 v63, 0\n" \
"v_mov_b32 v64, 0\n" \
"v_mov_b32 v65, 0\n" \
"v_mov_b32 v66, 0\n" \
"v_mov_b32 v67, 0\n" \
"v_mov_b32 v68, 0\n" \
"v_mov_b32 v69, 0\n" \
"v_mov_b32 v70, 0\n" \
"v_mov_b32 v71, 0\n" \
"v_mov_b32 v72, 0\n" \
"v_mov_b32 v73, 0\n" \
"v_mov_b32 v74, 0\n" \
"v_mov_b32 v75, 0\n" \
"v_mov_b32 v76, 0\n" \
"v_mov_b32 v77, 0\n" \
"v_mov_b32 v78, 0\n" \
"v_mov_b32 v79, 0\n" \
"v_mov_b32 v80, 0\n" \
"v_mov_b32 v81, 0\n" \
"v_mov_b32 v82, 0\n" \
"v_mov_b32 v83, 0\n" \
"s_nop 1\n" \
"s_waitcnt lgkmcnt(6)\n" \
"v_smfmac_f32_16x16x64_bf16 v[60:63], v[0:3], a[0:7], v200\n" \
"v_smfmac_f32_16x16x64_bf16 v[64:67], v[0:3], a[56:63], v200\n" \
"v_smfmac_f32_16x16x64_bf16 v[68:71], v[0:3], a[112:119], v200\n" \
"v_smfmac_f32_16x16x64_bf16 v[72:75], v[0:3], a[168:175], v200\n" \
"v_smfmac_f32_16x16x64_bf16 v[76:79], v[0:3], a[224:231], v200\n" \
"v_smfmac_f32_16x16x64_bf16 v[80:83], v[0:3], v[128:135], v200\n" \
"s_waitcnt lgkmcnt(5)\n" \
"v_smfmac_f32_16x16x64_bf16 v[60:63], v[4:7], a[8:15], v200\n" \
"v_smfmac_f32_16x16x64_bf16 v[64:67], v[4:7], a[64:71], v200\n" \
"v_smfmac_f32_16x16x64_bf16 v[68:71], v[4:7], a[120:127], v200\n" \
"v_smfmac_f32_16x16x64_bf16 v[72:75], v[4:7], a[176:183], v200\n" \
"v_smfmac_f32_16x16x64_bf16 v[76:79], v[4:7], a[232:239], v200\n" \
"v_smfmac_f32_16x16x64_bf16 v[80:83], v[4:7], v[136:143], v200\n" \
"s_waitcnt lgkmcnt(4)\n" \
"v_smfmac_f32_16x16x64_bf16 v[60:63], v[8:11], a[16:23], v200\n" \
"v_smfmac_f32_16x16x64_bf16 v[64:67], v[8:11], a[72:79], v200\n" \
"v_smfmac_f32_16x16x64_bf16 v[68:71], v[8:11], a[128:135], v200\n" \
"v_smfmac_f32_16x16x64_bf16 v[72:75], v[8:11], a[184:191], v200\n" \
"v_smfmac_f32_16x16x64_bf16 v[76:79], v[8:11], a[240:247], v200\n" \
"v_smfmac_f32_16x16x64_bf16 v[80:83], v[8:11], v[144:151], v200\n" \
"s_waitcnt lgkmcnt(3)\n" \
"v_smfmac_f32_16x16x64_bf16 v[60:63], v[12:15], a[24:31], v200\n" \
"v_smfmac_f32_16x16x64_bf16 v[64:67], v[12:15], a[80:87], v200\n" \
"v_smfmac_f32_16x16x64_bf16 v[68:71], v[12:15], a[136:143], v200\n" \
"v_smfmac_f32_16x16x64_bf16 v[72:75], v[12:15], a[192:199], v200\n" \
"v_smfmac_f32_16x16x64_bf16 v[76:79], v[12:15], a[248:255], v200\n" \
"v_smfmac_f32_16x16x64_bf16 v[80:83], v[12:15], v[152:159], v200\n" \
"s_waitcnt lgkmcnt(2)\n" \
"v_smfmac_f32_16x16x64_bf16 v[60:63], v[16:19], a[32:39], v200\n" \
"v_smfmac_f32_16x16x64_bf16 v[64:67], v[16:19], a[88:95], v200\n" \
"v_smfmac_f32_16x16x64_bf16 v[68:71], v[16:19], a[144:151], v200\n" \
"v_smfmac_f32_16x16x64_bf16 v[72:75], v[16:19], a[200:207], v200\n" \
"s_barrier\n" \
"s_add_u32 s50, s40, 1\n" \
"s_mul_i32 s50, s50, 458752\n" \
"s_add_u32 s50, s50, s34\n" \
"buffer_load_dwordx4 v[44:47], v201, s[12:15], s50 offen sc1\n" \
"buffer_load_dwordx4 v[48:51], v201, s[12:15], s50 offen offset:1024 sc1\n" \
"buffer_load_dwordx4 v[52:55], v201, s[12:15], s50 offen offset:2048 sc1\n" \
"buffer_load_dwordx4 v[56:59], v201, s[12:15], s50 offen offset:3072 sc1\n" \
"buffer_load_dword v240, v215, s[4:7], 0 offen sc1\n" \
"v_smfmac_f32_16x16x64_bf16 v[76:79], v[16:19], v[104:111], v200\n" \
"v_smfmac_f32_16x16x64_bf16 v[80:83], v[16:19], v[160:167], v200\n" \
"s_waitcnt lgkmcnt(1)\n" \
"v_smfmac_f32_16x16x64_bf16 v[60:63], v[20:23], a[40:47], v200\n" \
"v_smfmac_f32_16x16x64_bf16 v[64:67], v[20:23], a[96:103], v200\n" \
"v_smfmac_f32_16x16x64_bf16 v[68:71], v[20:23], a[152:159], v200\n" \
"v_smfmac_f32_16x16x64_bf16 v[72:75], v[20:23], a[208:215], v200\n" \
"v_smfmac_f32_16x16x64_bf16 v[76:79], v[20:23], v[112:119], v200\n" \
"v_smfmac_f32_16x16x64_bf16 v[80:83], v[20:23], v[168:175], v200\n" \
"s_waitcnt lgkmcnt(0)\n" \
"v_smfmac_f32_16x16x64_bf16 v[60:63], v[24:27], a[48:55], v200\n" \
"v_smfmac_f32_16x16x64_bf16 v[64:67], v[24:27], a[104:111], v200\n" \
"v_smfmac_f32_16x16x64_bf16 v[68:71], v[24:27], a[160:167], v200\n" \
"v_smfmac_f32_16x16x64_bf16 v[72:75], v[24:27], a[216:223], v200\n" \
"v_smfmac_f32_16x16x64_bf16 v[76:79], v[24:27], v[120:127], v200\n" \
"v_smfmac_f32_16x16x64_bf16 v[80:83], v[24:27], v[176:183], v200\n" \
"s_nop 7\n" \
"v_permlane32_swap_b32 v60, v64\n" \
"v_permlane32_swap_b32 v61, v65\n" \
"v_permlane32_swap_b32 v62, v66\n" \
"v_permlane32_swap_b32 v63, v67\n" \
"v_add_f32 v60, v60, v64\n" \
"v_add_f32 v61, v61, v65\n" \
"v_add_f32 v62, v62, v66\n" \
"v_add_f32 v63, v63, v67\n" \
"v_cvt_pk_bf16_f32 v184, v60, v61\n" \
"v_cvt_pk_bf16_f32 v185, v62, v63\n" \
"ds_write_b64 v217, v[184:185] offset:0\n" \
"v_permlane32_swap_b32 v68, v72\n" \
"v_permlane32_swap_b32 v69, v73\n" \
"v_permlane32_swap_b32 v70, v74\n" \
"v_permlane32_swap_b32 v71, v75\n" \
"v_add_f32 v68, v68, v72\n" \
"v_add_f32 v69, v69, v73\n" \
"v_add_f32 v70, v70, v74\n" \
"v_add_f32 v71, v71, v75\n" \
"v_cvt_pk_bf16_f32 v186, v68, v69\n" \
"v_cvt_pk_bf16_f32 v187, v70, v71\n" \
"ds_write_b64 v217, v[186:187] offset:512\n" \
"v_permlane32_swap_b32 v76, v80\n" \
"v_permlane32_swap_b32 v77, v81\n" \
"v_permlane32_swap_b32 v78, v82\n" \
"v_permlane32_swap_b32 v79, v83\n" \
"v_add_f32 v76, v76, v80\n" \
"v_add_f32 v77, v77, v81\n" \
"v_add_f32 v78, v78, v82\n" \
"v_add_f32 v79, v79, v83\n" \
"v_cvt_pk_bf16_f32 v188, v76, v77\n" \
"v_cvt_pk_bf16_f32 v189, v78, v79\n" \
"ds_write_b64 v217, v[188:189] offset:1024\n" \
"ds_read_b64_tr_b16 v[0:1], v218 offset:0\n" \
"ds_read_b64_tr_b16 v[2:3], v218 offset:64\n" \
"ds_read_b64_tr_b16 v[4:5], v218 offset:1024\n" \
"ds_read_b64_tr_b16 v[6:7], v218 offset:1088\n" \
"s_add_u32 s43, s40, 0\n" \
"s_mul_i32 s43, s43, 114688\n" \
"s_waitcnt lgkmcnt(0)\n" \
"buffer_store_dwordx4 v[0:3], v219, s[16:19], s43 offen sc1\n" \
"buffer_store_dwordx4 v[4:7], v221, s[16:19], s43 offen offset:1024 sc1\n" \
"s_waitcnt vmcnt(7)\n" \
"s_barrier\n" \
"ds_read_b64_tr_b16 v[92:93], v213\n" \
"ds_read_b64_tr_b16 v[94:95], v213 offset:64\n" \
"ds_read_b32 v84, v252 offset:16\n" \
"v_mov_b32 v245, v245\n" \
"s_nop 2\n" \
"v_mov_b32 v245, v245\n" \
"s_nop 2\n" \
"v_mov_b32 v245, v245\n" \
"s_waitcnt lgkmcnt(1)\n" \
"v_mfma_f32_16x16x32_bf16 v[68:71], v[92:95], v[28:31], 0\n" \
"s_waitcnt lgkmcnt(0)\n" \
"v_xor_b32 v85, s29, v84\n" \
"v_mfma_f32_16x16x32_bf16 v[76:79], v[92:95], v[36:39], 0\n" \
"v_add_u32 v84, v84, v210\n" \
"v_add_u32 v85, v85, v210\n" \
"v_mfma_f32_16x16x32_bf16 v[72:75], v[92:95], v[32:35], 0\n" \
"s_nop 1\n" \
"v_mfma_f32_16x16x32_bf16 v[80:83], v[92:95], v[40:43], 0\n" \
"v_cvt_pk_bf16_f32 v96, v68, v69\n" \
"v_cvt_pk_bf16_f32 v97, v70, v71\n" \
"v_cvt_pk_bf16_f32 v98, v76, v77\n" \
"v_cvt_pk_bf16_f32 v99, v78, v79\n" \
"s_mov_b64 exec, s[2:3]\n" \
"v_cvt_pk_bf16_f32 v96, v72, v73\n" \
"v_cvt_pk_bf16_f32 v97, v74, v75\n" \
"s_nop 0\n" \
"v_cvt_pk_bf16_f32 v98, v80, v81\n" \
"v_cvt_pk_bf16_f32 v99, v82, v83\n" \
"s_mov_b64 exec, -1\n" \
"s_nop 1\n" \
"v_permlane16_swap_b32 v96, v98\n" \
"v_permlane16_swap_b32 v97, v99\n" \
"buffer_store_dwordx4 v[96:99], v84, s[4:7], 0 offen sc1\n" \
"s_barrier\n" \
"buffer_store_dwordx4 v[96:99], v85, s[4:7], 0 offen sc1\n" \
"s_nop 15\n" \
"s_nop 15\n" \
"s_nop 15\n" \
"s_nop 15\n" \
"s_nop 15\n" \
"buffer_store_dwordx4 v[100:103], v214, s[4:7], s49 offen sc1\n" \
"s_xor_b32 s43, s49, s29\n" \
"buffer_store_dwordx4 v[100:103], v214, s[4:7], s43 offen sc1\n" \
"s_waitcnt vmcnt(4)\n" \
"v_mov_b32 v28, v44\n" \
"v_mov_b32 v29, v45\n" \
"v_mov_b32 v30, v46\n" \
"v_mov_b32 v31, v47\n" \
"v_mov_b32 v32, v48\n" \
"v_mov_b32 v33, v49\n" \
"v_mov_b32 v34, v50\n" \
"v_mov_b32 v35, v51\n" \
"v_mov_b32 v36, v52\n" \
"v_mov_b32 v37, v53\n" \
"v_mov_b32 v38, v54\n" \
"v_mov_b32 v39, v55\n" \
"v_mov_b32 v40, v56\n" \
"v_mov_b32 v41, v57\n" \
"v_mov_b32 v42, v58\n" \
"v_mov_b32 v43, v59\n" \
"s_cmp_eq_u32 s40, -1\n" \
"s_cbranch_scc1 .LX2_nogu\n" \
"s_add_u32 s36, s40, 10\n" \
"s_min_u32 s36, s36, 71\n" \
"s_add_u32 s43, s30, s36\n" \
"s_mov_b32 s50, 0\n" \
"s_branch .LX2_w91c\n" \
".LX2_w91l:\n" \
"buffer_load_dword v240, v215, s[4:7], 0 offen sc1\n" \
"s_waitcnt vmcnt(0)\n" \
".LX2_w91c:\n" \
"v_subrev_u32 v241, s43, v240\n" \
"v_cmp_gt_u32 vcc, 0x100, v241\n" \
"s_cmp_eq_u64 vcc, -1\n" \
"s_cbranch_scc1 .LX2_w91d\n" \
"s_sleep 2\n" \
"s_add_u32 s50, s50, 1\n" \
"s_cmp_lt_u32 s50, 0x20000\n" \
"s_cbranch_scc1 .LX2_w91l\n" \
"s_branch .LX2_end\n" \
".LX2_w91d:\n" \
".LX2_nogu:\n" \
"s_add_u32 s40, s40, 1\n" \
"s_waitcnt lgkmcnt(0)\n" \
"s_barrier\n" \
"s_cmp_eq_u32 s40, 0\n" \
"s_cbranch_scc1 .LX2_wet\n" \
"s_cmp_lt_u32 s40, 63\n" \
"s_cbranch_scc1 .LX2_loop\n" \
"ds_read_b128 v[0:3], v216 offset:0\n" \
"ds_read_b128 v[4:7], v216 offset:1024\n" \
"ds_read_b128 v[8:11], v216 offset:2048\n" \
"ds_read_b128 v[12:15], v216 offset:3072\n" \
"ds_read_b128 v[16:19], v216 offset:4096\n" \
"ds_read_b128 v[20:23], v216 offset:5120\n" \
"ds_read_b128 v[24:27], v216 offset:6144\n" \
"v_mov_b32 v60, 0\n" \
"v_mov_b32 v61, 0\n" \
"v_mov_b32 v62, 0\n" \
"v_mov_b32 v63, 0\n" \
"v_mov_b32 v64, 0\n" \
"v_mov_b32 v65, 0\n" \
"v_mov_b32 v66, 0\n" \
"v_mov_b32 v67, 0\n" \
"v_mov_b32 v68, 0\n" \
"v_mov_b32 v69, 0\n" \
"v_mov_b32 v70, 0\n" \
"v_mov_b32 v71, 0\n" \
"v_mov_b32 v72, 0\n" \
"v_mov_b32 v73, 0\n" \
"v_mov_b32 v74, 0\n" \
"v_mov_b32 v75, 0\n" \
"v_mov_b32 v76, 0\n" \
"v_mov_b32 v77, 0\n" \
"v_mov_b32 v78, 0\n" \
"v_mov_b32 v79, 0\n" \
"v_mov_b32 v80, 0\n" \
"v_mov_b32 v81, 0\n" \
"v_mov_b32 v82, 0\n" \
"v_mov_b32 v83, 0\n" \
"s_nop 1\n" \
"s_waitcnt lgkmcnt(6)\n" \
"v_smfmac_f32_16x16x64_bf16 v[60:63], v[0:3], a[0:7], v200\n" \
"v_smfmac_f32_16x16x64_bf16 v[64:67], v[0:3], a[56:63], v200\n" \
"v_smfmac_f32_16x16x64_bf16 v[68:71], v[0:3], a[112:119], v200\n" \
"v_smfmac_f32_16x16x64_bf16 v[72:75], v[0:3], a[168:175], v200\n" \
"v_smfmac_f32_16x16x64_bf16 v[76:79], v[0:3], a[224:231], v200\n" \
"v_smfmac_f32_16x16x64_bf16 v[80:83], v[0:3], v[128:135], v200\n" \
"s_waitcnt lgkmcnt(5)\n" \
"v_smfmac_f32_16x16x64_bf16 v[60:63], v[4:7], a[8:15], v200\n" \
"v_smfmac_f32_16x16x64_bf16 v[64:67], v[4:7], a[64:71], v200\n" \
"v_smfmac_f32_16x16x64_bf16 v[68:71], v[4:7], a[120:127], v200\n" \
"v_smfmac_f32_16x16x64_bf16 v[72:75], v[4:7], a[176:183], v200\n" \
"v_smfmac_f32_16x16x64_bf16 v[76:79], v[4:7], a[232:239], v200\n" \
"v_smfmac_f32_16x16x64_bf16 v[80:83], v[4:7], v[136:143], v200\n" \
"s_waitcnt lgkmcnt(4)\n" \
"v_smfmac_f32_16x16x64_bf16 v[60:63], v[8:11], a[16:23], v200\n" \
"v_smfmac_f32_16x16x64_bf16 v[64:67], v[8:11], a[72:79], v200\n" \
"v_smfmac_f32_16x16x64_bf16 v[68:71], v[8:11], a[128:135], v200\n" \
"v_smfmac_f32_16x16x64_bf16 v[72:75], v[8:11], a[184:191], v200\n" \
"v_smfmac_f32_16x16x64_bf16 v[76:79], v[8:11], a[240:247], v200\n" \
"v_smfmac_f32_16x16x64_bf16 v[80:83], v[8:11], v[144:151], v200\n" \
"s_waitcnt lgkmcnt(3)\n" \
"v_smfmac_f32_16x16x64_bf16 v[60:63], v[12:15], a[24:31], v200\n" \
"v_smfmac_f32_16x16x64_bf16 v[64:67], v[12:15], a[80:87], v200\n" \
"v_smfmac_f32_16x16x64_bf16 v[68:71], v[12:15], a[136:143], v200\n" \
"v_smfmac_f32_16x16x64_bf16 v[72:75], v[12:15], a[192:199], v200\n" \
"v_smfmac_f32_16x16x64_bf16 v[76:79], v[12:15], a[248:255], v200\n" \
"v_smfmac_f32_16x16x64_bf16 v[80:83], v[12:15], v[152:159], v200\n" \
"s_waitcnt lgkmcnt(2)\n" \
"v_smfmac_f32_16x16x64_bf16 v[60:63], v[16:19], a[32:39], v200\n" \
"v_smfmac_f32_16x16x64_bf16 v[64:67], v[16:19], a[88:95], v200\n" \
"v_smfmac_f32_16x16x64_bf16 v[68:71], v[16:19], a[144:151], v200\n" \
"v_smfmac_f32_16x16x64_bf16 v[72:75], v[16:19], a[200:207], v200\n" \
"v_smfmac_f32_16x16x64_bf16 v[76:79], v[16:19], v[104:111], v200\n" \
"v_smfmac_f32_16x16x64_bf16 v[80:83], v[16:19], v[160:167], v200\n" \
"s_waitcnt lgkmcnt(1)\n" \
"v_smfmac_f32_16x16x64_bf16 v[60:63], v[20:23], a[40:47], v200\n" \
"v_smfmac_f32_16x16x64_bf16 v[64:67], v[20:23], a[96:103], v200\n" \
"v_smfmac_f32_16x16x64_bf16 v[68:71], v[20:23], a[152:159], v200\n" \
"v_smfmac_f32_16x16x64_bf16 v[72:75], v[20:23], a[208:215], v200\n" \
"v_smfmac_f32_16x16x64_bf16 v[76:79], v[20:23], v[112:119], v200\n" \
"v_smfmac_f32_16x16x64_bf16 v[80:83], v[20:23], v[168:175], v200\n" \
"s_waitcnt lgkmcnt(0)\n" \
"v_smfmac_f32_16x16x64_bf16 v[60:63], v[24:27], a[48:55], v200\n" \
"v_smfmac_f32_16x16x64_bf16 v[64:67], v[24:27], a[104:111], v200\n" \
"v_smfmac_f32_16x16x64_bf16 v[68:71], v[24:27], a[160:167], v200\n" \
"v_smfmac_f32_16x16x64_bf16 v[72:75], v[24:27], a[216:223], v200\n" \
"v_smfmac_f32_16x16x64_bf16 v[76:79], v[24:27], v[120:127], v200\n" \
"v_smfmac_f32_16x16x64_bf16 v[80:83], v[24:27], v[176:183], v200\n" \
"s_nop 7\n" \
"v_permlane32_swap_b32 v60, v64\n" \
"v_permlane32_swap_b32 v61, v65\n" \
"v_permlane32_swap_b32 v62, v66\n" \
"v_permlane32_swap_b32 v63, v67\n" \
"v_add_f32 v60, v60, v64\n" \
"v_add_f32 v61, v61, v65\n" \
"v_add_f32 v62, v62, v66\n" \
"v_add_f32 v63, v63, v67\n" \
"v_cvt_pk_bf16_f32 v184, v60, v61\n" \
"v_cvt_pk_bf16_f32 v185, v62, v63\n" \
"ds_write_b64 v217, v[184:185] offset:0\n" \
"v_permlane32_swap_b32 v68, v72\n" \
"v_permlane32_swap_b32 v69, v73\n" \
"v_permlane32_swap_b32 v70, v74\n" \
"v_permlane32_swap_b32 v71, v75\n" \
"v_add_f32 v68, v68, v72\n" \
"v_add_f32 v69, v69, v73\n" \
"v_add_f32 v70, v70, v74\n" \
"v_add_f32 v71, v71, v75\n" \
"v_cvt_pk_bf16_f32 v186, v68, v69\n" \
"v_cvt_pk_bf16_f32 v187, v70, v71\n" \
"ds_write_b64 v217, v[186:187] offset:512\n" \
"v_permlane32_swap_b32 v76, v80\n" \
"v_permlane32_swap_b32 v77, v81\n" \
"v_permlane32_swap_b32 v78, v82\n" \
"v_permlane32_swap_b32 v79, v83\n" \
"v_add_f32 v76, v76, v80\n" \
"v_add_f32 v77, v77, v81\n" \
"v_add_f32 v78, v78, v82\n" \
"v_add_f32 v79, v79, v83\n" \
"v_cvt_pk_bf16_f32 v188, v76, v77\n" \
"v_cvt_pk_bf16_f32 v189, v78, v79\n" \
"ds_write_b64 v217, v[188:189] offset:1024\n" \
"ds_read_b64_tr_b16 v[0:1], v218 offset:0\n" \
"ds_read_b64_tr_b16 v[2:3], v218 offset:64\n" \
"ds_read_b64_tr_b16 v[4:5], v218 offset:1024\n" \
"ds_read_b64_tr_b16 v[6:7], v218 offset:1088\n" \
"s_add_u32 s43, s40, 0\n" \
"s_mul_i32 s43, s43, 114688\n" \
"s_waitcnt lgkmcnt(0)\n" \
"buffer_store_dwordx4 v[0:3], v219, s[16:19], s43 offen sc1\n" \
"buffer_store_dwordx4 v[4:7], v221, s[16:19], s43 offen offset:1024 sc1\n" \
"s_waitcnt vmcnt(0)\n" \
"s_barrier\n" \
"ds_read_b128 v[0:3], v216 offset:7168\n" \
"ds_read_b128 v[4:7], v216 offset:8192\n" \
"ds_read_b128 v[8:11], v216 offset:9216\n" \
"ds_read_b128 v[12:15], v216 offset:10240\n" \
"ds_read_b128 v[16:19], v216 offset:11264\n" \
"ds_read_b128 v[20:23], v216 offset:12288\n" \
"ds_read_b128 v[24:27], v216 offset:13312\n" \
"v_mov_b32 v60, 0\n" \
"v_mov_b32 v61, 0\n" \
"v_mov_b32 v62, 0\n" \
"v_mov_b32 v63, 0\n" \
"v_mov_b32 v64, 0\n" \
"v_mov_b32 v65, 0\n" \
"v_mov_b32 v66, 0\n" \
"v_mov_b32 v67, 0\n" \
"v_mov_b32 v68, 0\n" \
"v_mov_b32 v69, 0\n" \
"v_mov_b32 v70, 0\n" \
"v_mov_b32 v71, 0\n" \
"v_mov_b32 v72, 0\n" \
"v_mov_b32 v73, 0\n" \
"v_mov_b32 v74, 0\n" \
"v_mov_b32 v75, 0\n" \
"v_mov_b32 v76, 0\n" \
"v_mov_b32 v77, 0\n" \
"v_mov_b32 v78, 0\n" \
"v_mov_b32 v79, 0\n" \
"v_mov_b32 v80, 0\n" \
"v_mov_b32 v81, 0\n" \
"v_mov_b32 v82, 0\n" \
"v_mov_b32 v83, 0\n" \
"s_nop 1\n" \
"s_waitcnt lgkmcnt(6)\n" \
"v_smfmac_f32_16x16x64_bf16 v[60:63], v[0:3], a[0:7], v200\n" \
"v_smfmac_f32_16x16x64_bf16 v[64:67], v[0:3], a[56:63], v200\n" \
"v_smfmac_f32_16x16x64_bf16 v[68:71], v[0:3], a[112:119], v200\n" \
"v_smfmac_f32_16x16x64_bf16 v[72:75], v[0:3], a[168:175], v200\n" \
"v_smfmac_f32_16x16x64_bf16 v[76:79], v[0:3], a[224:231], v200\n" \
"v_smfmac_f32_16x16x64_bf16 v[80:83], v[0:3], v[128:135], v200\n" \
"s_waitcnt lgkmcnt(5)\n" \
"v_smfmac_f32_16x16x64_bf16 v[60:63], v[4:7], a[8:15], v200\n" \
"v_smfmac_f32_16x16x64_bf16 v[64:67], v[4:7], a[64:71], v200\n" \
"v_smfmac_f32_16x16x64_bf16 v[68:71], v[4:7], a[120:127], v200\n" \
"v_smfmac_f32_16x16x64_bf16 v[72:75], v[4:7], a[176:183], v200\n" \
"v_smfmac_f32_16x16x64_bf16 v[76:79], v[4:7], a[232:239], v200\n" \
"v_smfmac_f32_16x16x64_bf16 v[80:83], v[4:7], v[136:143], v200\n" \
"s_waitcnt lgkmcnt(4)\n" \
"v_smfmac_f32_16x16x64_bf16 v[60:63], v[8:11], a[16:23], v200\n" \
"v_smfmac_f32_16x16x64_bf16 v[64:67], v[8:11], a[72:79], v200\n" \
"v_smfmac_f32_16x16x64_bf16 v[68:71], v[8:11], a[128:135], v200\n" \
"v_smfmac_f32_16x16x64_bf16 v[72:75], v[8:11], a[184:191], v200\n" \
"v_smfmac_f32_16x16x64_bf16 v[76:79], v[8:11], a[240:247], v200\n" \
"v_smfmac_f32_16x16x64_bf16 v[80:83], v[8:11], v[144:151], v200\n" \
"s_waitcnt lgkmcnt(3)\n" \
"v_smfmac_f32_16x16x64_bf16 v[60:63], v[12:15], a[24:31], v200\n" \
"v_smfmac_f32_16x16x64_bf16 v[64:67], v[12:15], a[80:87], v200\n" \
"v_smfmac_f32_16x16x64_bf16 v[68:71], v[12:15], a[136:143], v200\n" \
"v_smfmac_f32_16x16x64_bf16 v[72:75], v[12:15], a[192:199], v200\n" \
"v_smfmac_f32_16x16x64_bf16 v[76:79], v[12:15], a[248:255], v200\n" \
"v_smfmac_f32_16x16x64_bf16 v[80:83], v[12:15], v[152:159], v200\n" \
"s_waitcnt lgkmcnt(2)\n" \
"v_smfmac_f32_16x16x64_bf16 v[60:63], v[16:19], a[32:39], v200\n" \
"v_smfmac_f32_16x16x64_bf16 v[64:67], v[16:19], a[88:95], v200\n" \
"v_smfmac_f32_16x16x64_bf16 v[68:71], v[16:19], a[144:151], v200\n" \
"v_smfmac_f32_16x16x64_bf16 v[72:75], v[16:19], a[200:207], v200\n" \
"v_smfmac_f32_16x16x64_bf16 v[76:79], v[16:19], v[104:111], v200\n" \
"v_smfmac_f32_16x16x64_bf16 v[80:83], v[16:19], v[160:167], v200\n" \
"s_waitcnt lgkmcnt(1)\n" \
"v_smfmac_f32_16x16x64_bf16 v[60:63], v[20:23], a[40:47], v200\n" \
"v_smfmac_f32_16x16x64_bf16 v[64:67], v[20:23], a[96:103], v200\n" \
"v_smfmac_f32_16x16x64_bf16 v[68:71], v[20:23], a[152:159], v200\n" \
"v_smfmac_f32_16x16x64_bf16 v[72:75], v[20:23], a[208:215], v200\n" \
"v_smfmac_f32_16x16x64_bf16 v[76:79], v[20:23], v[112:119], v200\n" \
"v_smfmac_f32_16x16x64_bf16 v[80:83], v[20:23], v[168:175], v200\n" \
"s_waitcnt lgkmcnt(0)\n" \
"v_smfmac_f32_16x16x64_bf16 v[60:63], v[24:27], a[48:55], v200\n" \
"v_smfmac_f32_16x16x64_bf16 v[64:67], v[24:27], a[104:111], v200\n" \
"v_smfmac_f32_16x16x64_bf16 v[68:71], v[24:27], a[160:167], v200\n" \
"v_smfmac_f32_16x16x64_bf16 v[72:75], v[24:27], a[216:223], v200\n" \
"v_smfmac_f32_16x16x64_bf16 v[76:79], v[24:27], v[120:127], v200\n" \
"v_smfmac_f32_16x16x64_bf16 v[80:83], v[24:27], v[176:183], v200\n" \
"s_nop 7\n" \
"v_permlane32_swap_b32 v60, v64\n" \
"v_permlane32_swap_b32 v61, v65\n" \
"v_permlane32_swap_b32 v62, v66\n" \
"v_permlane32_swap_b32 v63, v67\n" \
"v_add_f32 v60, v60, v64\n" \
"v_add_f32 v61, v61, v65\n" \
"v_add_f32 v62, v62, v66\n" \
"v_add_f32 v63, v63, v67\n" \
"v_cvt_pk_bf16_f32 v184, v60, v61\n" \
"v_cvt_pk_bf16_f32 v185, v62, v63\n" \
"ds_write_b64 v217, v[184:185] offset:0\n" \
"v_permlane32_swap_b32 v68, v72\n" \
"v_permlane32_swap_b32 v69, v73\n" \
"v_permlane32_swap_b32 v70, v74\n" \
"v_permlane32_swap_b32 v71, v75\n" \
"v_add_f32 v68, v68, v72\n" \
"v_add_f32 v69, v69, v73\n" \
"v_add_f32 v70, v70, v74\n" \
"v_add_f32 v71, v71, v75\n" \
"v_cvt_pk_bf16_f32 v186, v68, v69\n" \
"v_cvt_pk_bf16_f32 v187, v70, v71\n" \
"ds_write_b64 v217, v[186:187] offset:512\n" \
"v_permlane32_swap_b32 v76, v80\n" \
"v_permlane32_swap_b32 v77, v81\n" \
"v_permlane32_swap_b32 v78, v82\n" \
"v_permlane32_swap_b32 v79, v83\n" \
"v_add_f32 v76, v76, v80\n" \
"v_add_f32 v77, v77, v81\n" \
"v_add_f32 v78, v78, v82\n" \
"v_add_f32 v79, v79, v83\n" \
"v_cvt_pk_bf16_f32 v188, v76, v77\n" \
"v_cvt_pk_bf16_f32 v189, v78, v79\n" \
"ds_write_b64 v217, v[188:189] offset:1024\n" \
"ds_read_b64_tr_b16 v[0:1], v218 offset:0\n" \
"ds_read_b64_tr_b16 v[2:3], v218 offset:64\n" \
"ds_read_b64_tr_b16 v[4:5], v218 offset:1024\n" \
"ds_read_b64_tr_b16 v[6:7], v218 offset:1088\n" \
"s_add_u32 s43, s40, 1\n" \
"s_mul_i32 s43, s43, 114688\n" \
"s_waitcnt lgkmcnt(0)\n" \
"buffer_store_dwordx4 v[0:3], v219, s[16:19], s43 offen sc1\n" \
"buffer_store_dwordx4 v[4:7], v221, s[16:19], s43 offen offset:1024 sc1\n" \
"s_waitcnt vmcnt(0)\n" \
"s_barrier\n" \
".LX2_end:\n" \
"s_endpgm\n" \
".p2align 6\n" \
"fk_role_X3:\n" \
"ds_read_b32 v200, v255 offset:0\n" \
"ds_read_b32 v201, v255 offset:256\n" \
"ds_read_b32 v202, v255 offset:512\n" \
"ds_read_b32 v203, v255 offset:768\n" \
"ds_read_b32 v204, v255 offset:1024\n" \
"ds_read_b32 v205, v255 offset:1280\n" \
"ds_read_b32 v206, v255 offset:1536\n" \
"ds_read_b32 v207, v255 offset:1792\n" \
"ds_read_b32 v208, v255 offset:2048\n" \
"ds_read_b32 v209, v255 offset:2304\n" \
"ds_read_b32 v210, v255 offset:2560\n" \
"ds_read_b32 v211, v255 offset:2816\n" \
"ds_read_b32 v212, v255 offset:3072\n" \
"ds_read_b32 v213, v255 offset:3328\n" \
"ds_read_b32 v214, v255 offset:3584\n" \
"ds_read_b32 v215, v255 offset:3840\n" \
"ds_read_b32 v216, v255 offset:4096\n" \
"ds_read_b32 v217, v255 offset:4352\n" \
"ds_read_b32 v218, v255 offset:4608\n" \
"ds_read_b32 v219, v255 offset:4864\n" \
"ds_read_b32 v220, v255 offset:5120\n" \
"ds_read_b32 v221, v255 offset:5376\n" \
"ds_read_b32 v254, v255 offset:7936\n" \
"s_waitcnt lgkmcnt(0)\n" \
"v_readlane_b32 s4, v254, 0\n" \
"v_readlane_b32 s5, v254, 1\n" \
"v_readlane_b32 s6, v254, 2\n" \
"v_readlane_b32 s7, v254, 3\n" \
"v_readlane_b32 s8, v254, 4\n" \
"v_readlane_b32 s9, v254, 5\n" \
"v_readlane_b32 s10, v254, 6\n" \
"v_readlane_b32 s11, v254, 7\n" \
"v_readlane_b32 s12, v254, 8\n" \
"v_readlane_b32 s13, v254, 9\n" \
"v_readlane_b32 s14, v254, 10\n" \
"v_readlane_b32 s15, v254, 11\n" \
"v_readlane_b32 s16, v254, 12\n" \
"v_readlane_b32 s17, v254, 13\n" \
"v_readlane_b32 s18, v254, 14\n" \
"v_readlane_b32 s19, v254, 15\n" \
"v_readlane_b32 s20, v254, 16\n" \
"v_readlane_b32 s21, v254, 17\n" \
"v_readlane_b32 s22, v254, 18\n" \
"v_readlane_b32 s23, v254, 19\n" \
"v_readlane_b32 s24, v254, 20\n" \
"v_readlane_b32 s25, v254, 21\n" \
"v_readlane_b32 s26, v254, 22\n" \
"v_readlane_b32 s27, v254, 23\n" \
"v_readlane_b32 s28, v254, 24\n" \
"v_readlane_b32 s29, v254, 25\n" \
"v_readlane_b32 s30, v254, 26\n" \
"v_readlane_b32 s31, v254, 27\n" \
"v_readlane_b32 s32, v254, 28\n" \
"v_readlane_b32 s33, v254, 29\n" \
"v_readlane_b32 s34, v254, 30\n" \
"v_readlane_b32 s35, v254, 31\n" \
"v_readlane_b32 s36, v254, 32\n" \
"v_readlane_b32 s37, v254, 33\n" \
"v_readlane_b32 s38, v254, 34\n" \
"v_readlane_b32 s39, v254, 35\n" \
"s_mov_b32 s40, 0\n" \
"s_mov_b32 s51, 0\n" \
"s_nop 0\n" \
"v_mov_b32 v252, s38\n" \
"s_nop 4\n" \
"v_mov_b32 v100, -1\n" \
"v_mov_b32 v101, -1\n" \
"v_mov_b32 v102, -1\n" \
"v_mov_b32 v103, -1\n" \
"s_mov_b32 s35, -2\n" \
"s_mov_b32 s2, 0\n" \
"s_mov_b32 s3, -1\n" \
"v_writelane_b32 v251, s6, 0\n" \
"v_writelane_b32 v251, s10, 1\n" \
"v_writelane_b32 v251, s14, 2\n" \
"v_writelane_b32 v251, s18, 3\n" \
"s_mov_b32 s6, 0\n" \
"s_mov_b32 s10, 0\n" \
"s_mov_b32 s14, 0\n" \
"s_mov_b32 s18, 0\n" \
"s_mov_b32 s40, -1\n" \
"s_branch .LX3_pre\n" \
".LX3_wet:\n" \
"s_waitcnt vmcnt(0)\n" \
"v_readlane_b32 s6, v251, 0\n" \
"v_readlane_b32 s10, v251, 1\n" \
"v_readlane_b32 s14, v251, 2\n" \
"v_readlane_b32 s18, v251, 3\n" \
"s_nop 4\n" \
"s_add_u32 s43, s30, 9\n" \
"s_mov_b32 s50, 0\n" \
".LX3_w92l:\n" \
"buffer_load_dword v240, v215, s[4:7], 0 offen sc1\n" \
"s_waitcnt vmcnt(0)\n" \
".LX3_w92c:\n" \
"v_subrev_u32 v241, s43, v240\n" \
"v_cmp_gt_u32 vcc, 0x100, v241\n" \
"s_cmp_eq_u64 vcc, -1\n" \
"s_cbranch_scc1 .LX3_w92d\n" \
"s_sleep 2\n" \
"s_add_u32 s50, s50, 1\n" \
"s_cmp_lt_u32 s50, 0x20000\n" \
"s_cbranch_scc1 .LX3_w92l\n" \
"s_branch .LX3_end\n" \
".LX3_w92d:\n" \
"s_add_u32 s43, s40, 0\n" \
"s_mul_i32 s43, s43, 458752\n" \
"s_add_u32 s43, s43, s33\n" \
"buffer_load_dwordx4 a[0:3], v201, s[8:11], s43 offen sc1\n" \
"buffer_load_dwordx4 a[4:7], v201, s[8:11], s43 offen offset:1024 sc1\n" \
"buffer_load_dwordx4 a[8:11], v201, s[8:11], s43 offen offset:2048 sc1\n" \
"buffer_load_dwordx4 a[12:15], v201, s[8:11], s43 offen offset:3072 sc1\n" \
"s_add_u32 s43, s43, 4096\n" \
"buffer_load_dwordx4 a[16:19], v201, s[8:11], s43 offen sc1\n" \
"buffer_load_dwordx4 a[20:23], v201, s[8:11], s43 offen offset:1024 sc1\n" \
"buffer_load_dwordx4 a[24:27], v201, s[8:11], s43 offen offset:2048 sc1\n" \
"buffer_load_dwordx4 a[28:31], v201, s[8:11], s43 offen offset:3072 sc1\n" \
"s_add_u32 s43, s43, 4096\n" \
"buffer_load_dwordx4 a[32:35], v201, s[8:11], s43 offen sc1\n" \
"buffer_load_dwordx4 a[36:39], v201, s[8:11], s43 offen offset:1024 sc1\n" \
"buffer_load_dwordx4 a[40:43], v201, s[8:11], s43 offen offset:2048 sc1\n" \
"buffer_load_dwordx4 a[44:47], v201, s[8:11], s43 offen offset:3072 sc1\n" \
"s_add_u32 s43, s43, 4096\n" \
"buffer_load_dwordx4 a[48:51], v201, s[8:11], s43 offen sc1\n" \
"buffer_load_dwordx4 a[52:55], v201, s[8:11], s43 offen offset:1024 sc1\n" \
"buffer_load_dwordx4 a[56:59], v201, s[8:11], s43 offen offset:2048 sc1\n" \
"buffer_load_dwordx4 a[60:63], v201, s[8:11], s43 offen offset:3072 sc1\n" \
"s_add_u32 s43, s43, 4096\n" \
"buffer_load_dwordx4 a[64:67], v201, s[8:11], s43 offen sc1\n" \
"buffer_load_dwordx4 a[68:71], v201, s[8:11], s43 offen offset:1024 sc1\n" \
"buffer_load_dwordx4 a[72:75], v201, s[8:11], s43 offen offset:2048 sc1\n" \
"buffer_load_dwordx4 a[76:79], v201, s[8:11], s43 offen offset:3072 sc1\n" \
"s_add_u32 s43, s43, 4096\n" \
"buffer_load_dwordx4 a[80:83], v201, s[8:11], s43 offen sc1\n" \
"buffer_load_dwordx4 a[84:87], v201, s[8:11], s43 offen offset:1024 sc1\n" \
"buffer_load_dwordx4 a[88:91], v201, s[8:11], s43 offen offset:2048 sc1\n" \
"buffer_load_dwordx4 a[92:95], v201, s[8:11], s43 offen offset:3072 sc1\n" \
"s_add_u32 s43, s43, 4096\n" \
"buffer_load_dwordx4 a[96:99], v201, s[8:11], s43 offen sc1\n" \
"buffer_load_dwordx4 a[100:103], v201, s[8:11], s43 offen offset:1024 sc1\n" \
"buffer_load_dwordx4 a[104:107], v201, s[8:11], s43 offen offset:2048 sc1\n" \
"buffer_load_dwordx4 a[108:111], v201, s[8:11], s43 offen offset:3072 sc1\n" \
"s_add_u32 s50, s40, 0\n" \
"s_mul_i32 s50, s50, 458752\n" \
"s_add_u32 s50, s50, s34\n" \
"buffer_load_dwordx4 v[28:31], v201, s[12:15], s50 offen sc1\n" \
"buffer_load_dwordx4 v[32:35], v201, s[12:15], s50 offen offset:1024 sc1\n" \
"s_add_u32 s43, s30, 1\n" \
"s_mov_b32 s50, 0\n" \
".LX3_w93l:\n" \
"buffer_load_dword v240, v205, s[4:7], 0 offen sc1\n" \
"s_waitcnt vmcnt(0)\n" \
".LX3_w93c:\n" \
"v_subrev_u32 v241, s43, v240\n" \
"v_cmp_gt_u32 vcc, 0x100, v241\n" \
"s_cmp_eq_u64 vcc, -1\n" \
"s_cbranch_scc1 .LX3_w93d\n" \
"s_sleep 2\n" \
"s_add_u32 s50, s50, 1\n" \
"s_cmp_lt_u32 s50, 0x20000\n" \
"s_cbranch_scc1 .LX3_w93l\n" \
"s_branch .LX3_end\n" \
".LX3_w93d:\n" \
"s_add_u32 s43, s30, 1\n" \
"s_mov_b32 s50, 0\n" \
".LX3_w94l:\n" \
"buffer_load_dword v240, v206, s[4:7], 0 offen sc1\n" \
"s_waitcnt vmcnt(0)\n" \
".LX3_w94c:\n" \
"v_subrev_u32 v241, s43, v240\n" \
"v_cmp_gt_u32 vcc, 0x100, v241\n" \
"s_cmp_eq_u64 vcc, -1\n" \
"s_cbranch_scc1 .LX3_w94d\n" \
"s_sleep 2\n" \
"s_add_u32 s50, s50, 1\n" \
"s_cmp_lt_u32 s50, 0x20000\n" \
"s_cbranch_scc1 .LX3_w94l\n" \
"s_branch .LX3_end\n" \
".LX3_w94d:\n" \
".LX3_pre:\n" \
"s_barrier\n" \
".LX3_loop:\n" \
"s_add_u32 s43, s40, 2\n" \
"s_and_b32 s43, s43, 3\n" \
"s_bitcmp1_b32 s43, 0\n" \
"s_cselect_b32 s49, s32, s31\n" \
"s_lshl_b32 s43, s43, 19\n" \
"s_add_u32 s49, s49, s43\n" \
"ds_read_b96 v[248:250], v252\n" \
"s_waitcnt lgkmcnt(0)\n" \
"v_readfirstlane_b32 s44, v248\n" \
"v_readfirstlane_b32 s45, v249\n" \
"v_readfirstlane_b32 s46, v250\n" \
"s_mul_i32 s42, s25, s46\n" \
"s_lshr_b32 s42, s42, 12\n" \
"s_add_u32 s43, s40, 0\n" \
"s_and_b32 s43, s43, 3\n" \
"s_bitcmp1_b32 s43, 0\n" \
"s_cselect_b32 s47, s32, s31\n" \
"s_lshl_b32 s43, s43, 19\n" \
"s_add_u32 s47, s47, s43\n" \
"s_sub_u32 s43, s24, s44\n" \
"s_add_u32 s43, s43, s28\n" \
"s_max_i32 s43, s43, 0xfffd0000\n" \
"s_min_i32 s43, s43, 0x4000\n" \
"s_mul_i32 s43, s43, s46\n" \
"s_ashr_i32 s43, s43, 12\n" \
"s_add_u32 s41, s45, s43\n" \
"s_branch .LX3_a1\n" \
".LX3_retrya:\n" \
"s_add_u32 s51, s51, 1\n" \
"s_cmp_gt_u32 s51, 0x20000\n" \
"s_cbranch_scc1 .LX3_end\n" \
".LX3_a0:\n" \
"s_add_u32 s41, s41, s42\n" \
"s_add_u32 s24, s24, s25\n" \
"s_xor_b32 s31, s31, s29\n" \
"s_xor_b32 s32, s32, s29\n" \
"s_xor_b32 s47, s47, s29\n" \
"s_xor_b32 s48, s48, s29\n" \
"s_add_u32 s39, s39, 1\n" \
"buffer_store_dwordx4 v[100:103], v214, s[4:7], s47 offen sc1\n" \
"buffer_store_dwordx4 v[100:103], v217, s[4:7], s47 offen sc1\n" \
"buffer_store_dwordx4 v[100:103], v218, s[4:7], s47 offen sc1\n" \
"buffer_store_dwordx4 v[100:103], v219, s[4:7], s47 offen sc1\n" \
".p2align 6\n" \
".LX3_a1:\n" \
"s_memtime vcc\n" \
"s_waitcnt lgkmcnt(0)\n" \
"s_sub_u32 s43, s41, vcc_lo\n" \
"s_cmp_gt_i32 s43, 128\n" \
"s_cbranch_scc1 .LX3_a1\n" \
"s_cmp_lt_i32 s43, s37\n" \
"s_cbranch_scc1 .LX3_a0\n" \
"s_max_i32 s43, s43, 0\n" \
"s_and_b32 s43, s43, -4\n" \
"s_getpc_b64 vcc\n" \
".LX3_a3:\n" \
"s_sub_u32 s43, .LX3_a4-.LX3_a3, s43\n" \
"s_add_u32 vcc_lo, vcc_lo, s43\n" \
"s_addc_u32 vcc_hi, vcc_hi, 0\n" \
"s_setpc_b64 vcc\n" \
".rept 32\n" \
"s_nop 0\n" \
".endr\n" \
".LX3_a4:\n" \
"buffer_load_dwordx4 v[0:3], v208, s[4:7], s47 offen sc1\n" \
"buffer_load_dwordx4 v[4:7], v208, s[4:7], s47 offen offset:1024 sc1\n" \
"buffer_load_dwordx4 v[8:11], v208, s[4:7], s47 offen offset:2048 sc1\n" \
"buffer_load_dwordx4 v[12:15], v208, s[4:7], s47 offen offset:3072 sc1\n" \
"buffer_load_dwordx4 v[16:19], v209, s[4:7], s47 offen sc1\n" \
"buffer_load_dwordx4 v[20:23], v209, s[4:7], s47 offen offset:1024 sc1\n" \
"buffer_load_dwordx4 v[24:27], v209, s[4:7], s47 offen offset:2048 sc1\n" \
"s_cmp_eq_u32 s35, s40\n" \
"s_cbranch_scc1 .LX3_nb\n" \
"s_barrier\n" \
".LX3_nb:\n" \
"s_mov_b32 s35, s40\n" \
"v_mov_b32 v96, s47\n" \
"ds_write_b32 v252, v96 offset:16\n" \
"v_mov_b32 v60, 0\n" \
"v_mov_b32 v61, 0\n" \
"v_mov_b32 v62, 0\n" \
"v_mov_b32 v63, 0\n" \
"v_mov_b32 v64, 0\n" \
"v_mov_b32 v65, 0\n" \
"v_mov_b32 v66, 0\n" \
"v_mov_b32 v67, 0\n" \
"s_lshl_b32 s43, s40, 7\n" \
"buffer_load_dwordx4 v[236:239], v207, s[4:7], s43 offen sc1\n" \
"s_add_u32 s50, s40, 1\n" \
"s_mul_i32 s50, s50, 458752\n" \
"s_add_u32 s50, s50, s34\n" \
"buffer_load_dwordx4 v[44:47], v201, s[12:15], s50 offen sc1\n" \
"buffer_load_dwordx4 v[48:51], v201, s[12:15], s50 offen offset:1024 sc1\n" \
"s_waitcnt vmcnt(9)\n" \
"v_smfmac_f32_16x16x64_bf16 v[60:63], v[0:3], a[0:7], v200\n" \
"v_smfmac_f32_16x16x64_bf16 v[64:67], v[0:3], a[56:63], v200\n" \
"s_waitcnt vmcnt(8)\n" \
"v_smfmac_f32_16x16x64_bf16 v[60:63], v[4:7], a[8:15], v200\n" \
"v_smfmac_f32_16x16x64_bf16 v[64:67], v[4:7], a[64:71], v200\n" \
"s_waitcnt vmcnt(7)\n" \
"v_smfmac_f32_16x16x64_bf16 v[60:63], v[8:11], a[16:23], v200\n" \
"v_smfmac_f32_16x16x64_bf16 v[64:67], v[8:11], a[72:79], v200\n" \
"s_waitcnt vmcnt(6)\n" \
"v_smfmac_f32_16x16x64_bf16 v[60:63], v[12:15], a[24:31], v200\n" \
"v_smfmac_f32_16x16x64_bf16 v[64:67], v[12:15], a[80:87], v200\n" \
"s_waitcnt vmcnt(5)\n" \
"v_smfmac_f32_16x16x64_bf16 v[60:63], v[16:19], a[32:39], v200\n" \
"v_smfmac_f32_16x16x64_bf16 v[64:67], v[16:19], a[88:95], v200\n" \
"s_waitcnt vmcnt(4)\n" \
"v_smfmac_f32_16x16x64_bf16 v[60:63], v[20:23], a[40:47], v200\n" \
"v_smfmac_f32_16x16x64_bf16 v[64:67], v[20:23], a[96:103], v200\n" \
"s_waitcnt vmcnt(3)\n" \
"v_smfmac_f32_16x16x64_bf16 v[60:63], v[24:27], a[48:55], v200\n" \
"v_smfmac_f32_16x16x64_bf16 v[64:67], v[24:27], a[104:111], v200\n" \
"s_nop 2\n" \
"v_max3_u32 v84, v0, v0, v1\n" \
"v_max3_u32 v84, v84, v2, v3\n" \
"v_max3_u32 v84, v84, v4, v5\n" \
"v_max3_u32 v84, v84, v6, v7\n" \
"v_max3_u32 v84, v84, v8, v9\n" \
"v_permlane32_swap_b32 v60, v64\n" \
"v_permlane32_swap_b32 v61, v65\n" \
"v_permlane32_swap_b32 v62, v66\n" \
"v_permlane32_swap_b32 v63, v67\n" \
"v_pk_add_f32 v[60:61], v[60:61], v[64:65]\n" \
"v_pk_add_f32 v[62:63], v[62:63], v[66:67]\n" \
"v_max3_u32 v84, v84, v10, v11\n" \
"v_max_f32_dpp v88, v60, v60 row_ror:8 row_mask:0xf bank_mask:0x3\n" \
"v_max_f32_dpp v89, v61, v61 row_ror:8 row_mask:0xf bank_mask:0x3\n" \
"v_max_f32_dpp v88, v62, v62 row_ror:8 row_mask:0xf bank_mask:0xc\n" \
"v_max_f32_dpp v89, v63, v63 row_ror:8 row_mask:0xf bank_mask:0xc\n" \
"v_max3_u32 v84, v84, v12, v13\n" \
"v_max_f32_dpp v90, v88, v88 row_shl:4 row_mask:0xf bank_mask:0x5\n" \
"v_max_f32_dpp v90, v89, v89 row_shr:4 row_mask:0xf bank_mask:0xa\n" \
"v_max3_u32 v84, v84, v14, v15\n" \
"v_max3_u32 v84, v84, v16, v17\n" \
"v_max_f32_dpp v88, v90, v90 quad_perm:[1,0,3,2] row_mask:0xf bank_mask:0xf\n" \
"v_max3_u32 v84, v84, v18, v19\n" \
"v_max3_u32 v84, v84, v20, v21\n" \
"v_max_f32_dpp v86, v88, v88 quad_perm:[2,3,0,1] row_mask:0xf bank_mask:0xf\n" \
"v_max_f32_dpp v89, v88, v88 quad_perm:[2,3,0,1] row_mask:0xf bank_mask:0xf\n" \
"v_max3_u32 v84, v84, v22, v23\n" \
"v_max3_u32 v84, v84, v24, v25\n" \
"v_permlane32_swap_b32 v86, v89\n" \
"v_max_f32 v86, v86, v89\n" \
"v_max3_u32 v84, v84, v26, v27\n" \
"v_cmp_eq_u32 vcc, -1, v84\n" \
"v_subrev_f32_dpp v60, v86, v60 row_newbcast:0 row_mask:0xf bank_mask:0xf\n" \
"v_subrev_f32_dpp v61, v86, v61 row_newbcast:4 row_mask:0xf bank_mask:0xf\n" \
"v_subrev_f32_dpp v62, v86, v62 row_newbcast:8 row_mask:0xf bank_mask:0xf\n" \
"v_subrev_f32_dpp v63, v86, v63 row_newbcast:12 row_mask:0xf bank_mask:0xf\n" \
"v_exp_f32 v60, v60\n" \
"v_exp_f32 v61, v61\n" \
"v_exp_f32 v62, v62\n" \
"v_exp_f32 v63, v63\n" \
"v_cvt_pk_bf16_f32 v92, v60, v61\n" \
"v_cvt_pk_bf16_f32 v93, v62, v63\n" \
"ds_write_b64 v212, v[92:93]\n" \
"s_cbranch_vccnz .LX3_retrya\n" \
"v_add_f32_dpp v88, v60, v60 row_ror:8 row_mask:0xf bank_mask:0x3\n" \
"v_add_f32_dpp v89, v61, v61 row_ror:8 row_mask:0xf bank_mask:0x3\n" \
"v_add_f32_dpp v88, v62, v62 row_ror:8 row_mask:0xf bank_mask:0xc\n" \
"v_add_f32_dpp v89, v63, v63 row_ror:8 row_mask:0xf bank_mask:0xc\n" \
"v_mov_b32 v245, v245\n" \
"v_add_f32_dpp v90, v88, v88 row_shl:4 row_mask:0xf bank_mask:0x5\n" \
"v_add_f32_dpp v90, v89, v89 row_shr:4 row_mask:0xf bank_mask:0xa\n" \
"v_mov_b32 v245, v245\n" \
"v_mov_b32 v245, v245\n" \
"v_add_f32_dpp v88, v90, v90 quad_perm:[1,0,3,2] row_mask:0xf bank_mask:0xf\n" \
"v_mov_b32 v245, v245\n" \
"v_mov_b32 v245, v245\n" \
"v_add_f32_dpp v87, v88, v88 quad_perm:[2,3,0,1] row_mask:0xf bank_mask:0xf\n" \
"buffer_store_dwordx2 v[86:87], v211, s[4:7], s47 offen sc1\n" \
"s_nop 0\n" \
"v_mov_b32 v245, v245\n" \
"s_waitcnt lgkmcnt(0)\n" \
"s_barrier\n" \
"ds_read_b64_tr_b16 v[92:93], v213\n" \
"ds_read_b64_tr_b16 v[94:95], v213 offset:64\n" \
"ds_read_b32 v84, v252 offset:16\n" \
"v_mov_b32 v245, v245\n" \
"s_nop 2\n" \
"v_mov_b32 v245, v245\n" \
"s_nop 2\n" \
"v_mov_b32 v245, v245\n" \
"s_waitcnt lgkmcnt(1)\n" \
"v_mfma_f32_16x16x32_bf16 v[68:71], v[92:95], v[28:31], 0\n" \
"s_waitcnt lgkmcnt(0)\n" \
"v_xor_b32 v85, s29, v84\n" \
"v_mfma_f32_16x16x32_bf16 v[72:75], v[92:95], v[32:35], 0\n" \
"v_add_u32 v84, v84, v210\n" \
"v_add_u32 v85, v85, v210\n" \
"v_mov_b32 v245, v245\n" \
"v_mov_b32 v245, v245\n" \
"v_mov_b32 v245, v245\n" \
"v_cvt_pk_bf16_f32 v96, v68, v69\n" \
"v_cvt_pk_bf16_f32 v97, v70, v71\n" \
"s_mov_b64 exec, s[2:3]\n" \
"v_cvt_pk_bf16_f32 v96, v72, v73\n" \
"v_cvt_pk_bf16_f32 v97, v74, v75\n" \
"s_mov_b64 exec, -1\n" \
"v_mov_b32 v98, v96\n" \
"v_mov_b32 v99, v97\n" \
"s_nop 1\n" \
"v_permlane16_swap_b32 v96, v98\n" \
"v_permlane16_swap_b32 v97, v99\n" \
"buffer_store_dwordx4 v[96:99], v84, s[4:7], 0 offen sc1\n" \
"s_barrier\n" \
"buffer_store_dwordx4 v[96:99], v85, s[4:7], 0 offen sc1\n" \
"s_xor_b32 s43, s47, s29\n" \
"buffer_store_dwordx2 v[86:87], v211, s[4:7], s43 offen sc1\n" \
"ds_write_b128 v216, v[0:3] offset:0\n" \
"ds_write_b128 v216, v[4:7] offset:1024\n" \
"ds_write_b128 v216, v[8:11] offset:2048\n" \
"ds_write_b128 v216, v[12:15] offset:3072\n" \
"ds_write_b128 v216, v[16:19] offset:4096\n" \
"ds_write_b128 v216, v[20:23] offset:5120\n" \
"ds_write_b128 v216, v[24:27] offset:6144\n" \
"s_add_u32 s43, s40, 1\n" \
"s_mul_i32 s43, s43, 458752\n" \
"s_add_u32 s43, s43, s33\n" \
"buffer_load_dwordx4 a[0:3], v201, s[8:11], s43 offen sc1\n" \
"buffer_load_dwordx4 a[4:7], v201, s[8:11], s43 offen offset:1024 sc1\n" \
"buffer_load_dwordx4 a[8:11], v201, s[8:11], s43 offen offset:2048 sc1\n" \
"buffer_load_dwordx4 a[12:15], v201, s[8:11], s43 offen offset:3072 sc1\n" \
"s_add_u32 s43, s43, 4096\n" \
"buffer_load_dwordx4 a[16:19], v201, s[8:11], s43 offen sc1\n" \
"buffer_load_dwordx4 a[20:23], v201, s[8:11], s43 offen offset:1024 sc1\n" \
"buffer_load_dwordx4 a[24:27], v201, s[8:11], s43 offen offset:2048 sc1\n" \
"buffer_load_dwordx4 a[28:31], v201, s[8:11], s43 offen offset:3072 sc1\n" \
"s_add_u32 s43, s43, 4096\n" \
"buffer_load_dwordx4 a[32:35], v201, s[8:11], s43 offen sc1\n" \
"buffer_load_dwordx4 a[36:39], v201, s[8:11], s43 offen offset:1024 sc1\n" \
"buffer_load_dwordx4 a[40:43], v201, s[8:11], s43 offen offset:2048 sc1\n" \
"buffer_load_dwordx4 a[44:47], v201, s[8:11], s43 offen offset:3072 sc1\n" \
"s_add_u32 s43, s43, 4096\n" \
"buffer_load_dwordx4 a[48:51], v201, s[8:11], s43 offen sc1\n" \
"buffer_load_dwordx4 a[52:55], v201, s[8:11], s43 offen offset:1024 sc1\n" \
"buffer_load_dwordx4 a[56:59], v201, s[8:11], s43 offen offset:2048 sc1\n" \
"buffer_load_dwordx4 a[60:63], v201, s[8:11], s43 offen offset:3072 sc1\n" \
"s_add_u32 s43, s43, 4096\n" \
"buffer_load_dwordx4 a[64:67], v201, s[8:11], s43 offen sc1\n" \
"buffer_load_dwordx4 a[68:71], v201, s[8:11], s43 offen offset:1024 sc1\n" \
"buffer_load_dwordx4 a[72:75], v201, s[8:11], s43 offen offset:2048 sc1\n" \
"buffer_load_dwordx4 a[76:79], v201, s[8:11], s43 offen offset:3072 sc1\n" \
"s_add_u32 s43, s43, 4096\n" \
"buffer_load_dwordx4 a[80:83], v201, s[8:11], s43 offen sc1\n" \
"buffer_load_dwordx4 a[84:87], v201, s[8:11], s43 offen offset:1024 sc1\n" \
"buffer_load_dwordx4 a[88:91], v201, s[8:11], s43 offen offset:2048 sc1\n" \
"buffer_load_dwordx4 a[92:95], v201, s[8:11], s43 offen offset:3072 sc1\n" \
"s_add_u32 s43, s43, 4096\n" \
"buffer_load_dwordx4 a[96:99], v201, s[8:11], s43 offen sc1\n" \
"buffer_load_dwordx4 a[100:103], v201, s[8:11], s43 offen offset:1024 sc1\n" \
"buffer_load_dwordx4 a[104:107], v201, s[8:11], s43 offen offset:2048 sc1\n" \
"buffer_load_dwordx4 a[108:111], v201, s[8:11], s43 offen offset:3072 sc1\n" \
"buffer_store_dwordx4 v[100:103], v214, s[4:7], s49 offen sc1\n" \
"s_xor_b32 s43, s49, s29\n" \
"buffer_store_dwordx4 v[100:103], v214, s[4:7], s43 offen sc1\n" \
"s_waitcnt vmcnt(34)\n" \
"v_mov_b32 v28, v44\n" \
"v_mov_b32 v29, v45\n" \
"v_mov_b32 v30, v46\n" \
"v_mov_b32 v31, v47\n" \
"v_mov_b32 v32, v48\n" \
"v_mov_b32 v33, v49\n" \
"v_mov_b32 v34, v50\n" \
"v_mov_b32 v35, v51\n" \
"v_max_u32 v236, v236, v238\n" \
"v_max_u32 v237, v237, v239\n" \
"v_and_b32 v236, 0xfff, v236\n" \
"v_and_b32 v237, 0xfff, v237\n" \
"s_nop 0\n" \
"v_readfirstlane_b32 s26, v236\n" \
"v_readfirstlane_b32 s27, v237\n" \
"s_add_u32 s24, s24, s26\n" \
"s_add_u32 s24, s24, s27\n" \
"s_add_u32 s40, s40, 1\n" \
"s_waitcnt lgkmcnt(0)\n" \
"s_barrier\n" \
"s_cmp_eq_u32 s40, 0\n" \
"s_cbranch_scc1 .LX3_wet\n" \
"s_cmp_lt_u32 s40, 63\n" \
"s_cbranch_scc1 .LX3_loop\n" \
"ds_read_b96 v[248:250], v252\n" \
"s_waitcnt lgkmcnt(0)\n" \
"v_readfirstlane_b32 s44, v248\n" \
"v_readfirstlane_b32 s45, v249\n" \
"v_readfirstlane_b32 s46, v250\n" \
"s_mul_i32 s42, s25, s46\n" \
"s_lshr_b32 s42, s42, 12\n" \
"s_add_u32 s43, s40, 0\n" \
"s_and_b32 s43, s43, 3\n" \
"s_bitcmp1_b32 s43, 0\n" \
"s_cselect_b32 s47, s32, s31\n" \
"s_lshl_b32 s43, s43, 19\n" \
"s_add_u32 s47, s47, s43\n" \
"s_sub_u32 s43, s24, s44\n" \
"s_add_u32 s43, s43, s28\n" \
"s_max_i32 s43, s43, 0xfffd0000\n" \
"s_min_i32 s43, s43, 0x4000\n" \
"s_mul_i32 s43, s43, s46\n" \
"s_ashr_i32 s43, s43, 12\n" \
"s_add_u32 s41, s45, s43\n" \
"s_branch .LX3_z1\n" \
".LX3_retryz:\n" \
"s_add_u32 s51, s51, 1\n" \
"s_cmp_gt_u32 s51, 0x20000\n" \
"s_cbranch_scc1 .LX3_end\n" \
".LX3_z0:\n" \
"s_add_u32 s41, s41, s42\n" \
"s_add_u32 s24, s24, s25\n" \
"s_xor_b32 s31, s31, s29\n" \
"s_xor_b32 s32, s32, s29\n" \
"s_xor_b32 s47, s47, s29\n" \
"s_xor_b32 s48, s48, s29\n" \
"s_add_u32 s39, s39, 1\n" \
".p2align 6\n" \
".LX3_z1:\n" \
"s_memtime vcc\n" \
"s_waitcnt lgkmcnt(0)\n" \
"s_sub_u32 s43, s41, vcc_lo\n" \
"s_cmp_gt_i32 s43, 128\n" \
"s_cbranch_scc1 .LX3_z1\n" \
"s_cmp_lt_i32 s43, s37\n" \
"s_cbranch_scc1 .LX3_z0\n" \
"s_max_i32 s43, s43, 0\n" \
"s_and_b32 s43, s43, -4\n" \
"s_getpc_b64 vcc\n" \
".LX3_z3:\n" \
"s_sub_u32 s43, .LX3_z4-.LX3_z3, s43\n" \
"s_add_u32 vcc_lo, vcc_lo, s43\n" \
"s_addc_u32 vcc_hi, vcc_hi, 0\n" \
"s_setpc_b64 vcc\n" \
".rept 32\n" \
"s_nop 0\n" \
".endr\n" \
".LX3_z4:\n" \
"buffer_load_dwordx4 v[0:3], v208, s[4:7], s47 offen sc1\n" \
"buffer_load_dwordx4 v[4:7], v208, s[4:7], s47 offen offset:1024 sc1\n" \
"buffer_load_dwordx4 v[8:11], v208, s[4:7], s47 offen offset:2048 sc1\n" \
"buffer_load_dwordx4 v[12:15], v208, s[4:7], s47 offen offset:3072 sc1\n" \
"buffer_load_dwordx4 v[16:19], v209, s[4:7], s47 offen sc1\n" \
"buffer_load_dwordx4 v[20:23], v209, s[4:7], s47 offen offset:1024 sc1\n" \
"buffer_load_dwordx4 v[24:27], v209, s[4:7], s47 offen offset:2048 sc1\n" \
"s_waitcnt vmcnt(0)\n" \
"ds_write_b128 v216, v[0:3] offset:7168\n" \
"ds_write_b128 v216, v[4:7] offset:8192\n" \
"ds_write_b128 v216, v[8:11] offset:9216\n" \
"ds_write_b128 v216, v[12:15] offset:10240\n" \
"ds_write_b128 v216, v[16:19] offset:11264\n" \
"ds_write_b128 v216, v[20:23] offset:12288\n" \
"ds_write_b128 v216, v[24:27] offset:13312\n" \
"v_max3_u32 v84, v0, v0, v1\n" \
"v_max3_u32 v84, v84, v2, v3\n" \
"v_max3_u32 v84, v84, v4, v5\n" \
"v_max3_u32 v84, v84, v6, v7\n" \
"v_max3_u32 v84, v84, v8, v9\n" \
"v_max3_u32 v84, v84, v10, v11\n" \
"v_max3_u32 v84, v84, v12, v13\n" \
"v_max3_u32 v84, v84, v14, v15\n" \
"v_max3_u32 v84, v84, v16, v17\n" \
"v_max3_u32 v84, v84, v18, v19\n" \
"v_max3_u32 v84, v84, v20, v21\n" \
"v_max3_u32 v84, v84, v22, v23\n" \
"v_max3_u32 v84, v84, v24, v25\n" \
"v_max3_u32 v84, v84, v26, v27\n" \
"v_cmp_eq_u32 vcc, -1, v84\n" \
"s_cbranch_vccnz .LX3_retryz\n" \
"s_waitcnt lgkmcnt(0)\n" \
"s_barrier\n" \
"s_barrier\n" \
".LX3_end:\n" \
"s_endpgm\n"
typedef __attribute__((ext_vector_type(16))) float f16v;
DI u4 pack8(const f16v& x, int s, float sc) { return (u4){pk2(x[8 * s] * sc, x[8 * s + 1] * sc), pk2(x[8 * s + 2] * sc, x[8 * s + 3] * sc), pk2(x[8 * s + 4] * sc, x[8 * s + 5] * sc), pk2(x[8 * s + 6] * sc, x[8 * s + 7] * sc)}; }
DI u4 pack8(const f16v& x, int s) { return (u4){pk2(x[8 * s], x[8 * s + 1]), pk2(x[8 * s + 2], x[8 * s + 3]), pk2(x[8 * s + 4], x[8 * s + 5]), pk2(x[8 * s + 6], x[8 * s + 7])}; }
DI void dma_pair(u4 srd, int v0, int v1, int soff, unsigned m) {
    asm volatile("s_mov_b32 m0, %4\n\ts_nop 0\n\tbuffer_load_dwordx4 %0, %2, %3 offen lds\n\tbuffer_load_dwordx4 %1, %2, %3 offen offset:1024 lds" :: "v"(v0), "v"(v1), "s"(srd), "s"(soff), "s"(m) : "memory");
}
DI void dma_one(u4 srd, int v0, int soff, unsigned m) {
    asm volatile("s_mov_b32 m0, %3\n\ts_nop 0\n\tbuffer_load_dwordx4 %0, %1, %2 offen lds" :: "v"(v0), "s"(srd), "s"(soff), "s"(m) : "memory");
}
DI void mV_va(f16v& c, u4 x, u4 y) { asm volatile("v_mfma_f32_32x32x16_bf16 %0, %1, %2, %0" : "+v"(c) : "v"(x), "a"(y)); }
DI void mV_va0(f16v& c, u4 x, u4 y) { asm volatile("v_mfma_f32_32x32x16_bf16 %0, %1, %2, 0" : "=&v"(c) : "v"(x), "a"(y)); }
DI void mV_av(f16v& c, u4 x, u4 y) { asm volatile("v_mfma_f32_32x32x16_bf16 %0, %1, %2, %0" : "+v"(c) : "a"(x), "v"(y)); }
DI void mV_av0(f16v& c, u4 x, u4 y) { asm volatile("v_mfma_f32_32x32x16_bf16 %0, %1, %2, 0" : "=&v"(c) : "a"(x), "v"(y)); }
DI void mV_vv(f16v& c, u4 x, u4 y) { asm volatile("v_mfma_f32_32x32x16_bf16 %0, %1, %2, %0" : "+v"(c) : "v"(x), "v"(y)); }
DI void mV_vv0(f16v& c, u4 x, u4 y) { asm volatile("v_mfma_f32_32x32x16_bf16 %0, %1, %2, 0" : "=&v"(c) : "v"(x), "v"(y)); }
template <int N, int PD, class RD, class MM, class EX> DI void pipe(RD rd, MM mm, EX ex) {
    u4 f[PD];
#pragma unroll
    for (int j = 0; j < PD; ++j) f[j] = rd(j);
    SB();
#pragma unroll
    for (int j = 0; j < N; ++j) { mm(j, f[j % PD]); if (j + PD < N) f[j % PD] = rd(j + PD); ex(j); SB(); }
}
DI u4 sld128(u4 srd, int voff, int soff) { u4 v; asm volatile("s_nop 4\n\tbuffer_load_dwordx4 %0, %1, %2, %3 offen sc1" : "=v"(v) : "v"(voff), "s"(srd), "s"(soff) : "memory"); return v; }
DI u2 sld64(u4 srd, int voff, int soff) { u2 v; asm volatile("s_nop 4\n\tbuffer_load_dwordx2 %0, %1, %2, %3 offen sc1" : "=v"(v) : "v"(voff), "s"(srd), "s"(soff) : "memory"); return v; }
DI void sst128(u4 v, u4 srd, int voff, int soff) { asm volatile("s_nop 4\n\tbuffer_store_dwordx4 %0, %1, %2, %3 offen sc1\n\ts_nop 1" :: "v"(v), "v"(voff), "s"(srd), "s"(soff) : "memory"); }
DI void sst64(u2 v, u4 srd, int voff, int soff) { asm volatile("s_nop 4\n\tbuffer_store_dwordx2 %0, %1, %2, %3 offen sc1" :: "v"(v), "v"(voff), "s"(srd), "s"(soff) : "memory"); }
DI void sst32(unsigned v, u4 srd, int voff, int soff) { asm volatile("s_nop 4\n\tbuffer_store_dword %0, %1, %2, %3 offen sc1" :: "v"(v), "v"(voff), "s"(srd), "s"(soff) : "memory"); }
DI unsigned sld32(u4 srd, int voff, int soff) { unsigned v; asm volatile("s_nop 4\n\tbuffer_load_dword %0, %1, %2, %3 offen sc1" : "=v"(v) : "v"(voff), "s"(srd), "s"(soff) : "memory"); return v; }
template <int N> DI void waitvm() { asm volatile("s_waitcnt vmcnt(%0)" :: "i"(N) : "memory"); }
DI int swzr(int row) { return ((row & 1) << 2) | (row & 8) | ((row >> 1) & 3); }
#define SLICE 32768
#define CSCALE (LOG2E * 0.0625f)

struct SideCtx {
    char LAS* sm; unsigned smb; int lane, wave;
    int vr0, vr1, vc0, vc1;
};
DI int fresh_lane() { int l; asm volatile("v_mbcnt_lo_u32_b32 %0, -1, 0\n\tv_mbcnt_hi_u32_b32 %0, -1, %0" : "=v"(l)); __builtin_assume(l >= 0 && l < 64); return l; }
DI SideCtx make_ctx(int wave) {
    SideCtx c; c.sm = (char LAS*)smem_; c.smb = (unsigned)(size_t)c.sm; c.lane = fresh_lane(); c.wave = wave;
    asm volatile("" : "+s"(c.wave), "+s"(c.smb));
    const int l = c.lane;
    { int row = 4 * c.wave + (l >> 5); c.vr0 = row * 8192 + (((l & 31) ^ swzr(row)) * 16); row += 2; c.vr1 = row * 8192 + (((l & 31) ^ swzr(row)) * 16) - 1024; }
    { int row = l >> 3; c.vc0 = row * 896 + (((l & 7) ^ (((row >> 1) & 1) << 2)) * 16); c.vc1 = c.vc0 + 8 * 896 - 1024; }
    return c;
}
DI void dma_rows_q(const SideCtx& c, u4 srd, int h, int r0, int slot, int g) { dma_pair(srd, c.vr0, c.vr1, (r0 + 16 * g) * 8192 + h * 512, c.smb + slot * SLICE + (8 * g + 2 * c.wave) * 1024); }
DI void dma_cols_q(const SideCtx& c, u4 srd, int h, int c0, int slot, int g) { const int k = 8 * c.wave + 2 * g; dma_pair(srd, c.vc0, c.vc1, (h * 256 + 8 * k) * 896 + c0 * 2, c.smb + slot * SLICE + k * 1024); }
DI void dma_rows(const SideCtx& c, u4 srd, int h, int r0, int slot) {
#pragma unroll
    for (int g = 0; g < 4; ++g) dma_rows_q(c, srd, h, r0, slot, g);
}
DI void dma_cols(const SideCtx& c, u4 srd, int h, int c0, int slot) {
#pragma unroll
    for (int g = 0; g < 4; ++g) dma_cols_q(c, srd, h, c0, slot, g);
}
DI u4 rd_rows(const SideCtx& c, int slot, int ks, int tau) {
    const int l = c.lane, hi = l >> 5, ro = l & 31;
    const int rowc = 32 * (ro >> 4) + 4 * ((ro >> 2) & 1) + 8 * ((ro >> 1) & 1) + 2 * ((ro >> 3) & 1) + (ro & 1);
    const int ch = (2 * (ks & 7) + hi) ^ swzr(rowc);
    return *(const u4 LAS*)(c.sm + (rowc * 512 + ch * 16) + (slot * SLICE + tau * 8192 + (ks >> 3) * 256));
}
DI u4 tr_cols(const SideCtx& c, int slot, int ks, int nt) {
    const int l = c.lane, hi = l >> 5, blk = (l >> 4) & 1, r = (l & 15) >> 2, p = l & 3;
    u2 o[2];
#pragma unroll
    for (int t = 0; t < 2; ++t) {
        const int ch = (4 * nt + 2 * blk + (p >> 1)) ^ (((r >> 1) & 1) << 2);
        o[t] = trread(c.sm + ((8 * hi + r) * 128 + ch * 16 + 8 * (p & 1)) + (slot * SLICE + ks * 2048 + t * 512));
    }
    return (u4){o[0][0], o[0][1], o[1][0], o[1][1]};
}
#define PF_AHEAD 12
#define NSEG1 8
DI void set_flag(const SideCtx& c, u4 rhb, int off, unsigned val) {
    waitvm<0>(); bar();
    if (c.wave == 0) sst32(val, rhb, c.lane == 0 ? off : OOB, 0);
}
DI void wait_prog(u4 rhb, int voff_or_oob, unsigned want) {
    unsigned sp = 0;
#pragma nounroll
    for (;;) {
        unsigned v = sld32(rhb, voff_or_oob, 0); WAITV(0, "+v"(v));
        if (__all((v - want < 256u) | (voff_or_oob >= OOB))) break;
        asm volatile("s_sleep 4" ::: "memory");
        if (++sp > SPIN_MAX) __builtin_amdgcn_endpgm();
    }
}
#define MFW32(x, y) asm volatile("s_nop 11" : "+v"(x), "+v"(y))
DI int perm16(int r) { return 4 * ((r >> 2) & 1) + 8 * ((r >> 1) & 1) + 2 * ((r >> 3) & 1) + (r & 1); }
DI void w_first(int wave, int type, int h, int sub) {
    CArgs& a = get_args();
    const SideCtx c = make_ctx(wave);
    h = __builtin_amdgcn_readfirstlane(h); sub = __builtin_amdgcn_readfirstlane(sub); asm volatile("" : "+s"(h), "+s"(sub));
    const u4 rw1 = mksrd(type ? a.Wv : a.Wk, 448 * 8192), rw2 = mksrd(type ? a.Wo : a.Wq, 448 * 8192);
    const int c1 = sub == 3 ? 384 : sub * 128 + 64;
    if (type) { dma_cols(c, rw2, h, sub * 128, 2); dma_cols(c, rw2, h, c1, 3); }
    else { dma_rows(c, rw2, h, sub * 128, 2); dma_rows(c, rw2, h, c1, 3); }
    dma_rows(c, rw1, h, 0, 0); dma_rows(c, rw1, h, 64, 1);
}
template <int N> struct IntC { static constexpr int value = N; };
template <int TYPE>
DI void wgu_task(unsigned tag, int vb, int wave, int h, int sub) {
    CArgs& a = get_args();
    const SideCtx c = make_ctx(wave);
    const int lane = c.lane, hi = lane >> 5, l31 = lane & 31, w = c.wave;
    h = __builtin_amdgcn_readfirstlane(h); sub = __builtin_amdgcn_readfirstlane(sub); asm volatile("" : "+s"(h), "+s"(sub));
    const u4 rw1 = mksrd(TYPE ? a.Wv : a.Wk, 448 * 8192), rx = mksrd(a.X, 2048 * 896), rhb = mksrd(a.HB, HB_SZ);
    const int o_fl = (w == 0 && lane == 0) ? O_FW + ((h * 2 + TYPE) * 4 + sub) * 128 : OOB;
    int xo[7];
#pragma unroll
    for (int k = 0; k < 7; ++k) { const int n = 64 * (7 * (w & 1) + k) + lane, row = (n * 1171) >> 16, pos = n - 56 * row; xo[k] = (row + 16 * (w >> 1)) * 896 + ((pos ^ ((row >> 1) & 7)) * 16); }
    auto xdma = [&](int s, int reg, int k) { dma_one(rx, xo[k], s * 28672, c.smb + reg + (7 * w + k) * 1024); };
    const int m = sub * 4 + w;
    u4 wk[28];
    f16v d[2][2];
    {
        u4 kf[16];
        waitvm<16>(); bar();
        const int rowp = 16 * (l31 >> 4) + perm16(l31 & 15);
#pragma unroll
        for (int ks = 0; ks < 16; ++ks)
            kf[ks] = TYPE ? tr_cols(c, 2 + (w >> 1), ks, w & 1) : *(const u4 LAS*)(c.sm + 2 * SLICE + w * 16384 + (rowp * 512 + ((2 * (ks & 7) + hi) ^ swzr(rowp)) * 16) + (ks >> 3) * 256);
        bar();
#pragma unroll
        for (int i = 0; i < PF_AHEAD / 4; ++i) dma_one(rx, lane < 56 ? (vb >> 3) * 896 + lane * 16 : OOB, (4 * i + w) * 28672, c.smb + 4 * SLICE + 28672 + w * 1024);
        dma_rows(c, rw1, h, 128, 2);
        const u4 rout = mksrd(TYPE ? a.WvoF : a.WqkF, 28 * 16 * 14336);
        const int t16 = 2 * m + (l31 >> 4);
        const int o_st = m > 13 ? OOB : (TYPE ? t16 * 16 + h : h * 28 + t16) * 14336 + (hi * 16 + (TYPE ? l31 & 15 : perm16(l31 & 15))) * 16;
        auto epi = [&](int v, int e) {
            const u4 p = TYPE ? pack8(d[v & 1][e & 1], e >> 1) : pack8(d[v & 1][e & 1], e >> 1, CSCALE);
            st128(p, rout, o_st, (v * 2 + (e >> 1)) * 1024 + (e & 1) * 512);
            const auto s0 = __builtin_amdgcn_permlane32_swap(p[0], p[1], false, false), s1 = __builtin_amdgcn_permlane32_swap(p[2], p[3], false, false);
            wk[4 * v + e] = (u4){s0[0], s1[0], s0[1], s1[1]};
            asm volatile("" : "+a"(wk[4 * v + e]));
        };
#pragma unroll
        for (int u = 0; u < 7; ++u) {
            if (u == 0) waitvm<16 + PF_AHEAD / 4>(); else if (u == 1) waitvm<16 + PF_AHEAD / 4>(); else if (u == 4) waitvm<21>(); else if (u == 6) waitvm<19>(); else waitvm<20>();
            bar();
            if (u >= 3) st32(tag + u - 2, rhb, o_fl, 0);
            pipe<32, 4>([&](int j) { return rd_rows(c, u & 3, j >> 1, j & 1); },
                        [&](int j, u4 f) { if (j < 2) mV_vv0(d[u & 1][j & 1], f, kf[j >> 1]); else mV_vv(d[u & 1][j & 1], f, kf[j >> 1]); },
                        [&](int j) { if (u > 0 && j >= 4 && j < 16 && (j - 4) % 3 == 0) { if (j == 4) asm volatile("s_nop 3" : "+v"(d[(u - 1) & 1][0]), "+v"(d[(u - 1) & 1][1])); epi(u - 1, (j - 4) / 3); }
                                     if (u < 4) { if (j >= 15 && (j & 3) == 3 && j < 31) dma_rows_q(c, rw1, h, 64 * (u + 3), (u + 3) & 3, (j - 15) >> 2); }
                                     else if (j >= 15 && (j & 1) && j < 29) xdma(u - 4, (u == 4 ? 4 : u == 5 ? 3 : 0) * SLICE, (j - 15) >> 1); });
        }
        MFW32(d[0][0], d[0][1]);
#pragma unroll
        for (int e = 0; e < 4; ++e) epi(6, e);
    }
    const u4 rout = mksrd(TYPE ? a.UF : a.GF, 64 * 16 * 28 * 1024);
    auto o_out = [&](int mm) { return mm > 13 ? OOB : h * 28672 + (TYPE ? (2 * mm + (l31 >> 4)) * 1024 : mm * 1024 + (l31 >> 4) * 14336) + (hi * 16 + (l31 & 15)) * 16; };
    const int o_st = o_out(m);
    const char LAS* xb[4];
#pragma unroll
    for (int k = 0; k < 4; ++k) xb[k] = c.sm + l31 * 896 + ((2 * k + hi) ^ ((l31 >> 1) & 7)) * 16;
    const int o_pf = (w == 1 && lane < 56) ? (vb >> 3) * 896 + lane * 16 : OOB;
    unsigned ring = 0x21034;
    auto mm1 = [&](f16v& acc, const u4& wt, u4 f, bool first) __attribute__((always_inline)) {
        if (TYPE) { if (first) mV_va0(acc, f, wt); else mV_va(acc, f, wt); } else { if (first) mV_av0(acc, wt, f); else mV_av(acc, wt, f); } };
    auto seg = [&](auto first_, auto par_, int i) __attribute__((always_inline)) {
        constexpr int FI = decltype(first_)::value, PAR = decltype(par_)::value;
        if (FI == 0) waitvm<23>(); else if (FI == 1) waitvm<9>(); else waitvm<18>();
        bar();
        st32(FI == 0 ? tag + 5 : FI == 1 ? tag + 7 : tag + 5 + i, rhb, o_fl, 0);
        dma_one(rx, o_pf, (i + PF_AHEAD) * 28672, c.smb + 4 * SLICE + 28672 + w * 1024);
        const int r0 = (ring & 7) * SLICE, r3 = ((ring >> 12) & 7) * SLICE;
        ring = (ring >> 4) | ((ring & 7) << 16);
        pipe<28, 4>([&](int k) { return *(const u4 LAS*)(xb[k & 3] + r0 + (k >> 2) * 128); },
                    [&](int k, u4 f) { mm1(d[PAR][0], wk[k], f, k == 0); },
                    [&](int k) { if (FI != 0 && (k == 2 || k == 4)) { if (k == 2) asm volatile("s_nop 5" : "+v"(d[PAR ^ 1][0])); st128(pack8(d[PAR ^ 1][0], k == 4), rout, o_st, (i - 1) * 458752 + (k == 4) * 512); }
                                 if (k >= 6 && k % 3 == 0 && k < 27) xdma(i + 3, r3, k / 3 - 2); });
    };
    seg(IntC<0>{}, IntC<0>{}, 0); seg(IntC<1>{}, IntC<1>{}, 1);
#pragma nounroll
    for (int i = 2; i < NSEG1; i += 2) { seg(IntC<2>{}, IntC<0>{}, i); seg(IntC<2>{}, IntC<1>{}, i + 1); }
    asm volatile("s_nop 11" : "+v"(d[1][0]));
    sst128(pack8(d[1][0], 0), rout, o_st, (NSEG1 - 1) * 458752); sst128(pack8(d[1][0], 1), rout, o_st, (NSEG1 - 1) * 458752 + 512);
    if (sub & 1) { set_flag(c, rhb, O_FW + ((h * 2 + TYPE) * 4 + sub) * 128, tag + 7 + NSEG); return; }
    wait_prog(rhb, lane < 2 ? O_FW + ((h * 2 + TYPE) * 4 + 1 + 2 * lane) * 128 : OOB, tag + 7);
    bar();
    if (w == 0) sst32(tag + 7 + NSEG1, rhb, o_fl, 0);
    const int pfo = lane < 56 ? ((vb >> 3 & 7) + (vb >> 7) * 8) * 1792 + lane * 16 : OOB;
    if (w == 3) {
        int xp[7];
#pragma unroll
        for (int k = 0; k < 7; ++k) { const int n = 64 * k + lane, row = (n * 1171) >> 16, pos = n - 56 * row; xp[k] = row * 896 + ((pos ^ ((row >> 1) & 7)) * 16); xo[k] -= 16 * 896; }
        auto segl = [&](auto first_, auto par_, int i) __attribute__((always_inline)) {
            constexpr int FI = decltype(first_)::value, PAR = decltype(par_)::value;
            waitvm<62>();
            bar();
            const int r0 = (ring & 7) * SLICE, r3 = ((ring >> 12) & 7) * SLICE;
            ring = (ring >> 4) | ((ring & 7) << 16);
            pipe<28, 4>([&](int k) { return *(const u4 LAS*)(xb[k & 3] + r0 + (k >> 2) * 128); },
                        [&](int k, u4 f) { mm1(d[PAR][0], wk[k], f, k == 0); },
                        [&](int k) { if (k == 0) { if (FI) { asm volatile("s_nop 9" : "+v"(d[PAR ^ 1][0])); st128(pack8(d[PAR ^ 1][0], 0), rout, o_st, (i - 1) * 458752); st128(pack8(d[PAR ^ 1][0], 1), rout, o_st, (i - 1) * 458752 + 512); }
                                                   dma_pair(rx, pfo, pfo - 128, (i + PF_AHEAD) * 28672, c.smb + 4 * SLICE + 28672); }
                                     dma_one(rx, (k / 7) & 1 ? xo[k % 7] : xp[k % 7], (i + 3) * 28672 + (k / 14) * 14336, c.smb + r3 + k * 1024); });
        };
        segl(IntC<0>{}, IntC<0>{}, NSEG1); segl(IntC<1>{}, IntC<1>{}, NSEG1 + 1);
#pragma nounroll
        for (int i = NSEG1 + 2; i < NSEG; i += 2) { segl(IntC<1>{}, IntC<0>{}, i); segl(IntC<1>{}, IntC<1>{}, i + 1); }
        asm volatile("s_nop 11" : "+v"(d[1][0]));
    } else {
        const int m2 = sub == 0 ? 4 + w : w == 2 ? 7 : 12 + w, t16 = 2 * m2 + (l31 >> 4);
        const int o_s2 = o_out(m2);
        u4 w2[28];
        {
            const u4 rin = mksrd(TYPE ? a.WvoF : a.WqkF, 28 * 16 * 14336);
            const int o_in = (TYPE ? t16 * 16 + h : h * 28 + t16) * 14336 + (hi * 16 + (TYPE ? l31 & 15 : perm16(l31 & 15))) * 16;
#pragma unroll
            for (int k = 0; k < 28; ++k) w2[k] = sld128(rin, o_in, k * 512);
            waitvm<0>();
#pragma unroll
            for (int k = 0; k < 28; ++k) {
                PIN(w2[k]);
                const auto s0 = __builtin_amdgcn_permlane32_swap(w2[k][0], w2[k][1], false, false), s1 = __builtin_amdgcn_permlane32_swap(w2[k][2], w2[k][3], false, false);
                w2[k] = (u4){s0[0], s1[0], s0[1], s1[1]};
                asm volatile("" : "+a"(w2[k]));
            }
        }
        auto seg2 = [&](auto first_, auto par_, int i) __attribute__((always_inline)) {
            constexpr int FI = decltype(first_)::value, PAR = decltype(par_)::value;
            waitvm<5>();
            bar();
            st32(tag + (i < NSEG1 + 2 ? 7 + NSEG1 : 5 + i), rhb, o_fl, 0);
            const int r0 = (ring & 7) * SLICE;
            ring = (ring >> 4) | ((ring & 7) << 16);
            pipe<28, 3>([&](int k) { return *(const u4 LAS*)(xb[k & 3] + r0 + (k >> 2) * 128); },
                        [&](int k, u4 f) { mm1(d[PAR][0], wk[k], f, k == 0); mm1(d[PAR][1], w2[k], f, k == 0); },
                        [&](int k) { if (FI && k >= 2 && k < 10 && (k & 1) == 0) { if (k == 2) asm volatile("s_nop 3" : "+v"(d[PAR ^ 1][0]), "+v"(d[PAR ^ 1][1]));
                                         st128(pack8(d[PAR ^ 1][k >= 6], (k >> 1) & 1), rout, k >= 6 ? o_s2 : o_st, (i - 1) * 458752 + ((k >> 1) & 1) * 512); } });
        };
        seg2(IntC<0>{}, IntC<0>{}, NSEG1); seg2(IntC<1>{}, IntC<1>{}, NSEG1 + 1);
#pragma nounroll
        for (int i = NSEG1 + 2; i < NSEG; i += 2) { seg2(IntC<1>{}, IntC<0>{}, i); seg2(IntC<1>{}, IntC<1>{}, i + 1); }
        MFW32(d[1][0], d[1][1]);
        sst128(pack8(d[1][1], 0), rout, o_s2, 63 * 458752); sst128(pack8(d[1][1], 1), rout, o_s2, 63 * 458752 + 512);
    }
    sst128(pack8(d[1][0], 0), rout, o_st, 63 * 458752); sst128(pack8(d[1][0], 1), rout, o_st, 63 * 458752 + 512);
    set_flag(c, rhb, O_FW + ((h * 2 + TYPE) * 4 + sub) * 128, tag + 7 + NSEG);
}

DI void x_image(const SideCtx& c, u4 rx, int s) {
#pragma unroll
    for (int k = 0; k < 7; ++k) {
        const int n = 64 * (7 * (c.wave & 1) + k) + c.lane, row = (n * 1171) >> 16, pos = n - 56 * row;
        dma_one(rx, row * 896 + ((pos ^ ((row >> 1) & 7)) * 16), (s * 32 + 16 * (c.wave >> 1)) * 896, c.smb + 40960 + (7 * c.wave + k) * 1024);
    }
}
DI void e_task(unsigned tag, int wave, int s, int hg) {
    CArgs& a = get_args();
    const SideCtx c = make_ctx(wave);
    const int lane = c.lane, hi = lane >> 5, l31 = lane & 31;
    s = __builtin_amdgcn_readfirstlane(s); hg = __builtin_amdgcn_readfirstlane(hg); asm volatile("" : "+s"(s), "+s"(hg));
    const int sh = s * 16 + 4 * hg + c.wave;
    const u4 rhb = mksrd(a.HB, HB_SZ), rg = mksrd(a.GF, 64 * 16 * 28 * 1024);
    bar();
    x_image(c, mksrd(a.X, 2048 * 896), s);
    wait_prog(rhb, lane < 4 ? O_FW + ((4 * hg + c.wave) * 8 + lane) * 128 : OOB, tag + 8 + s);
    u4 g[28];
    const int o_g = (l31 >> 4) * 14336 + (hi * 16 + (l31 & 15)) * 16;
#pragma unroll
    for (int k = 0; k < 28; ++k) asm volatile("buffer_load_dwordx4 %0, %1, %2, %3 offen sc1" : "=a"(g[k]) : "v"(o_g), "s"(rg), "s"(sh * 28672 + k * 512) : "memory");
    bar();
    f16v sx;
    pipe<28, 4>([&](int k) { const char LAS* b = c.sm + 40960 + l31 * 896 + hi * 8 + (k >> 2) * 128;
                             const u2 c0 = *(const u2 LAS*)(b + ((2 * (k & 3)) ^ ((l31 >> 1) & 7)) * 16), c1 = *(const u2 LAS*)(b + ((2 * (k & 3) + 1) ^ ((l31 >> 1) & 7)) * 16);
                             return (u4){c0[0], c0[1], c1[0], c1[1]}; },
                [&](int k, u4 f) { if (k == 0) WAITV(21, "+a"(g[0]), "+a"(g[1]), "+a"(g[2]), "+a"(g[3]), "+a"(g[4]), "+a"(g[5]), "+a"(g[6]));
                                   if (k == 7) WAITV(14, "+a"(g[7]), "+a"(g[8]), "+a"(g[9]), "+a"(g[10]), "+a"(g[11]), "+a"(g[12]), "+a"(g[13]));
                                   if (k == 14) WAITV(7, "+a"(g[14]), "+a"(g[15]), "+a"(g[16]), "+a"(g[17]), "+a"(g[18]), "+a"(g[19]), "+a"(g[20]));
                                   if (k == 21) WAITV(0, "+a"(g[21]), "+a"(g[22]), "+a"(g[23]), "+a"(g[24]), "+a"(g[25]), "+a"(g[26]), "+a"(g[27]));
                                   u4 y = {f[0], f[2], f[1], f[3]}; asm volatile("s_nop 1" : "+v"(y));
                                   if (k == 0) mV_av0(sx, g[k], y); else mV_av(sx, g[k], y); },
                [&](int) {});
    asm volatile("s_nop 11" : "+v"(sx));
    float m = -3.0e38f;
#pragma unroll
    for (int j = 0; j < 16; ++j) { const int key = (j & 3) + 8 * (j >> 2) + 4 * hi; if (key > l31) sx[j] = -INFINITY; m = fmaxf(m, sx[j]); }
    m = x32max(m);
    float z = 0.f;
#pragma unroll
    for (int j = 0; j < 16; ++j) { sx[j] = __builtin_amdgcn_exp2f(sx[j] - m); z += sx[j]; }
    z = x32add(z);
    const u4 rex = mksrd(a.EX, 64 * 16 * 2048), rmz = mksrd(a.MZX, 64 * 16 * 256);
    sst128(pack8(sx, 0), rex, lane * 16, sh * 2048); sst128(pack8(sx, 1), rex, lane * 16, sh * 2048 + 1024);
    sst64((u2){__float_as_uint(m), __float_as_uint(z)}, rmz, lane < 32 ? lane * 8 : OOB, sh * 256);
    set_flag(c, rhb, O_FE + (s * 4 + hg) * 4, tag + 1);
}
#define P_XSLP 0
DI void slp(int n) {
#pragma nounroll
    for (int i = 0; i < n; i += 8) asm volatile("s_sleep 8");
}
DI void x_task(unsigned tag, int wave, int s, int cb) {
    CArgs& a = get_args();
    const SideCtx c = make_ctx(wave);
    const int lane = c.lane, hi = lane >> 5, l31 = lane & 31, w = c.wave;
    s = __builtin_amdgcn_readfirstlane(s); cb = __builtin_amdgcn_readfirstlane(cb); asm volatile("" : "+s"(s), "+s"(cb));
    const u4 rhb = mksrd(a.HB, HB_SZ);
    const u4 ru = mksrd(a.UF, 64 * 16 * 28 * 1024), rex = mksrd(a.EX, 64 * 16 * 2048), rmz = mksrd(a.MZX, 64 * 16 * 256);
    const u4 rum = mksrd(a.UM, 65 * 28 * 4096), rtf = mksrd(a.TF, 65 * 32 * 3584), rx = mksrd(a.X, 2048 * 896);
    bar();
    x_image(c, rx, s);
    wait_prog(rhb, lane < 16 ? O_FW + (((4 * w + (lane >> 2)) * 2 + 1) * 4 + (lane & 3)) * 128 : lane == 16 ? O_FE + (s * 4 + w) * 4 : OOB, lane < 16 ? tag + 8 + s : tag + 1);
    u4 uf[4][2][2], ex[4][2]; u2 mz[4];
    const int o_u = (l31 >> 4) * 1024 + hi * 256 + (l31 & 15) * 16;
#pragma unroll
    for (int k = 0; k < 4; ++k) {
        const int sh = s * 16 + 4 * w + k;
#pragma unroll
        for (int sg = 0; sg < 2; ++sg) {
            ex[k][sg] = sld128(rex, lane * 16, sh * 2048 + sg * 1024);
#pragma unroll
            for (int nt = 0; nt < 2; ++nt) uf[k][sg][nt] = sld128(ru, o_u, (sh * 28 + 4 * cb + 2 * nt) * 1024 + sg * 512);
        }
        mz[k] = sld64(rmz, l31 * 8, sh * 256);
        asm volatile("s_sleep %0" :: "i"(P_XSLP));
    }
    waitvm<0>();
    f16v rr[4][2];
#pragma unroll
    for (int k = 0; k < 4; ++k) {
        asm volatile("" : "+v"(mz[k]), "+v"(ex[k][0]), "+v"(ex[k][1]), "+v"(uf[k][0][0]), "+v"(uf[k][0][1]), "+v"(uf[k][1][0]), "+v"(uf[k][1][1]));
        mV_vv0(rr[k][0], uf[k][0][0], ex[k][0]); mV_vv0(rr[k][1], uf[k][0][1], ex[k][0]);
        mV_vv(rr[k][0], uf[k][1][0], ex[k][1]); mV_vv(rr[k][1], uf[k][1][1], ex[k][1]);
        MFW32(rr[k][0], rr[k][1]);
    }
    bar();
    wait_prog(rhb, lane < 8 ? O_FT + (8 * w + lane) * 128 : lane < 12 ? O_FT + (24 + 4 * cb + lane) * 128 : OOB, tag + s + 1);
    u4 tf[28]; u4 um[2][2];
    const int o_tf = (l31 >> 3) * 7168 + hi * 128 + (l31 & 7) * 16, o_um = (l31 >> 4) * 4096 + hi * 256 + (l31 & 15) * 16;
#pragma unroll
    for (int k = 0; k < 28; ++k) {
        asm volatile("buffer_load_dwordx4 %0, %1, %2, %3 offen sc1" : "=a"(tf[k]) : "v"(o_tf), "s"(rtf), "s"(((s + 1) * 32 + 8 * w) * 3584 + k * 256) : "memory");
        if (k % 7 == 6) slp(s < 58 ? P_XSLP : 0);
    }
#pragma unroll
    for (int k = 0; k < 4; ++k) um[k >> 1][k & 1] = ld128(rum, o_um, (s * 28 + 28 + 4 * cb + 2 * (k & 1)) * 4096 + w * 1024 + (k >> 1) * 512);
    f16v sm_;
    pipe<28, 4>([&](int k) { return *(const u4 LAS*)(c.sm + 40960 + l31 * 896 + (((2 * (k & 3) + hi) ^ ((l31 >> 1) & 7)) * 16) + (k >> 2) * 128); },
                [&](int k, u4 f) { if (k == 0) WAITV(25, "+a"(tf[0]), "+a"(tf[1]), "+a"(tf[2]), "+a"(tf[3]), "+a"(tf[4]), "+a"(tf[5]), "+a"(tf[6]));
                                   if (k == 7) WAITV(18, "+a"(tf[7]), "+a"(tf[8]), "+a"(tf[9]), "+a"(tf[10]), "+a"(tf[11]), "+a"(tf[12]), "+a"(tf[13]));
                                   if (k == 14) WAITV(11, "+a"(tf[14]), "+a"(tf[15]), "+a"(tf[16]), "+a"(tf[17]), "+a"(tf[18]), "+a"(tf[19]), "+a"(tf[20]));
                                   if (k == 21) WAITV(4, "+a"(tf[21]), "+a"(tf[22]), "+a"(tf[23]), "+a"(tf[24]), "+a"(tf[25]), "+a"(tf[26]), "+a"(tf[27]));
                                   if (k == 0) mV_av0(sm_, tf[k], f); else mV_av(sm_, tf[k], f); },
                [&](int) {});
    waitvm<0>();
    asm volatile("s_nop 11" : "+v"(sm_));
#pragma unroll
    for (int k = 0; k < 4; ++k) PIN(um[k >> 1][k & 1]);
    u2 pm[4]; float scx[4];
#pragma unroll
    for (int k = 0; k < 4; ++k) {
        const f4 sc = {sm_[4 * k], sm_[4 * k + 1], sm_[4 * k + 2], sm_[4 * k + 3]};
        const float mx = __uint_as_float(mz[k][0]);
        float m = fmaxf(x32max(fmaxf(fmaxf(sc[0], sc[1]), fmaxf(sc[2], sc[3]))), mx);
        float e[4];
#pragma unroll
        for (int j = 0; j < 4; ++j) e[j] = __builtin_amdgcn_exp2f(sc[j] - m);
        const float cx = __builtin_amdgcn_exp2f(mx - m);
        const float z = x32add((e[0] + e[1]) + (e[2] + e[3])) + cx * __uint_as_float(mz[k][1]);
        const float inv = __builtin_amdgcn_rcpf(z);
        scx[k] = cx * inv;
        pm[k] = (u2){pk2(e[0] * inv, e[1] * inv), pk2(e[2] * inv, e[3] * inv)};
    }
    f16v d0, d1;
    {
        u4 p0 = {pm[0][0], pm[0][1], pm[2][0], pm[2][1]}, p1 = {pm[1][0], pm[1][1], pm[3][0], pm[3][1]};
        asm volatile("s_nop 1\n\tv_mfma_f32_32x32x16_bf16 %0, %2, %6, 0\n\tv_mfma_f32_32x32x16_bf16 %1, %3, %6, 0\n\tv_mfma_f32_32x32x16_bf16 %0, %4, %7, %0\n\tv_mfma_f32_32x32x16_bf16 %1, %5, %7, %1"
                     : "=&v"(d0), "=&v"(d1) : "v"(um[0][0]), "v"(um[0][1]), "v"(um[1][0]), "v"(um[1][1]), "v"(p0), "v"(p1));
    }
    f16v a0 = (rr[0][0] * scx[0] + rr[1][0] * scx[1]) + (rr[2][0] * scx[2] + rr[3][0] * scx[3]);
    f16v a1 = (rr[0][1] * scx[0] + rr[1][1] * scx[1]) + (rr[2][1] * scx[2] + rr[3][1] * scx[3]);
    asm volatile("s_nop 11" : "+v"(d0), "+v"(d1), "+v"(a0), "+v"(a1));
    a0 += d0; a1 += d1;
#pragma unroll
    for (int g = 0; g < 4; ++g) {
        *(f4 LAS*)(c.sm + w * 8704 + l31 * 272 + (8 * g + 4 * hi) * 4) = (f4){a0[4 * g], a0[4 * g + 1], a0[4 * g + 2], a0[4 * g + 3]};
        *(f4 LAS*)(c.sm + w * 8704 + l31 * 272 + 128 + (8 * g + 4 * hi) * 4) = (f4){a1[4 * g], a1[4 * g + 1], a1[4 * g + 2], a1[4 * g + 3]};
    }
    bar();
    {
        const int t = w * 8 + (lane >> 3), c8 = lane & 7;
        f4 s0 = {0.f, 0.f, 0.f, 0.f}, s1 = s0;
#pragma unroll
        for (int p = 0; p < 4; ++p) { s0 += *(const f4 LAS*)(c.sm + p * 8704 + t * 272 + c8 * 32); s1 += *(const f4 LAS*)(c.sm + p * 8704 + t * 272 + c8 * 32 + 16); }
        sst128((u4){pk2(s0[0], s0[1]), pk2(s0[2], s0[3]), pk2(s1[0], s1[1]), pk2(s1[2], s1[3])}, mksrd(a.out, 2048 * 1792), t * 1792 + c8 * 16, s * 32 * 1792 + 896 + cb * 128);
    }
}

#define P_ASLP 8
#define PA_UT 0
#define PA_TU 33792
#define PA_KH (PA_TU + 35456)
#define PA_KL (PA_KH + 7680)
#define PA_KT (PA_KL + 7680)
#define PA_RED (PA_KT + 12288)
DI float bf2f(u16 x) { return __uint_as_float((unsigned)x << 16); }
DI void parta_task(unsigned tag, int wave, int task) {
    CArgs& a = get_args();
    const SideCtx c = make_ctx(wave);
    char LAS* sm = c.sm;
    const int lane = c.lane, w = c.wave, tid = w * 64 + lane, hi = lane >> 5, l31 = lane & 31, l15 = lane & 15, kk = lane >> 4;
    task = __builtin_amdgcn_readfirstlane(task); asm volatile("" : "+s"(task));
    const int d0 = task * 4;
    const u4 rpa = mksrd(a.PA, 56 * 32768);
    bar();
#pragma unroll
    for (int i = 0; i < 8; ++i) {
        const int l = tid + 256 * i;
        const u2 v = *(const u2*)(a.Au + (size_t)l * 448 + d0);
#pragma unroll
        for (int ch = 0; ch < 4; ++ch) *(u16 LAS*)(sm + PA_UT + ch * 4224 + (2048 - l) * 2) = (u16)(v[ch >> 1] >> (16 * (ch & 1)));
    }
    { const int i = tid, ch = i >> 6, m = i & 63; *(u16 LAS*)(sm + PA_UT + ch * 4224 + (m == 0 ? 0 : (2048 + m) * 2)) = 0; }
    for (int i = tid; i < 2 * 31 * 5; i += 256) { const int x = i / 155, r = i % 155; *(u4 LAS*)(sm + (x ? PA_KL : PA_KH) + r * 16) = (u4){0u, 0u, 0u, 0u}; }
    int toff[8];
#pragma unroll
    for (int j = 0; j < 8; ++j) {
        const int p = (int)bfr((float)(tid * 8 + j));
        int col, ar = (p & 255) >> 4;
        if (p < 256) col = p & 15; else if (p < 512) col = 16 + ((p & 15) >> 1); else if (p < 1024) col = 24 + ((p - 512) >> 8) * 4 + ((p & 15) >> 2);
        else if (p < 2048) col = 32 + ((p - 1024) >> 8) * 2 + ((p & 15) >> 3); else col = 40;
        toff[j] = (col * 16 + ar) * 4;
    }
    float qpos[3];
    qpos[0] = (float)l15;
    qpos[1] = l15 < 8 ? (float)(256 + 2 * l15) : (float)(512 + ((l15 - 8) >> 2) * 256 + (l15 & 3) * 4);
    qpos[2] = l15 < 8 ? (float)(1024 + (l15 >> 1) * 256 + (l15 & 1) * 8) : 2048.f;
    const float ppos = (float)(16 * l15);
    {
        {
            const int d = d0 + w;
            const float dt = bfr(expf(bf2f(a.Alogdt[d])));
            char LAS* pt = sm + PA_RED + w * 9216;
#pragma unroll
            for (int i = 0; i < 4; ++i) {
                const int n = lane + 64 * i;
                const float ar = -bfr(expf(bf2f(a.AlogA[d * 256 + n])));
                *(f4 LAS*)(pt + n * 16) = (f4){ar * dt * LOG2E, bf2f(a.Aim[d * 256 + n]) * dt * 0.15915494309189535f, 2.f * bf2f(a.ACr[d * 256 + n]) * dt, 2.f * bf2f(a.ACi[d * 256 + n]) * dt};
            }
            f4 acc[3] = {{0.f, 0.f, 0.f, 0.f}, {0.f, 0.f, 0.f, 0.f}, {0.f, 0.f, 0.f, 0.f}};
#pragma unroll 2
            for (int st = 0; st < 64; ++st) {
                const f4 z = *(const f4 LAS*)(pt + (4 * st + kk) * 16);
                float er, ei;
                { const float ex = __builtin_amdgcn_exp2f(z[0] * ppos); const float rv = __builtin_amdgcn_fractf(z[1] * ppos);
                  er = ex * __builtin_amdgcn_cosf(rv); ei = ex * __builtin_amdgcn_sinf(rv); }
                const float are = z[2] * er - z[3] * ei, aim = -(z[2] * ei + z[3] * er);
#pragma unroll
                for (int t = 0; t < 3; ++t) {
                    const float ex = __builtin_amdgcn_exp2f(z[0] * qpos[t]); const float rv = __builtin_amdgcn_fractf(z[1] * qpos[t]);
                    const float br = ex * __builtin_amdgcn_cosf(rv), bi = ex * __builtin_amdgcn_sinf(rv);
                    acc[t] = __builtin_amdgcn_mfma_f32_16x16x4f32(are, br, acc[t], 0, 0, 0);
                    acc[t] = __builtin_amdgcn_mfma_f32_16x16x4f32(aim, bi, acc[t], 0, 0, 0);
                }
            }
#pragma unroll
            for (int t = 0; t < 3; ++t) *(f4 LAS*)(sm + PA_KT + w * 3072 + ((t * 16 + l15) * 16 + 4 * kk) * 4) = acc[t];
        }
        bar();
#pragma nounroll
        for (int cc = 0; cc < 4; ++cc) {
            const int ch = cc;
            {
                float kv[8];
#pragma unroll
                for (int j = 0; j < 8; ++j) kv[j] = *(const float LAS*)(sm + PA_KT + cc * 3072 + toff[j]);
                u4 h4, l4;
#pragma unroll
                for (int j = 0; j < 4; ++j) { h4[j] = pk2(kv[2 * j], kv[2 * j + 1]);
                    l4[j] = pk2(kv[2 * j] - __uint_as_float(h4[j] << 16), kv[2 * j + 1] - __uint_as_float(h4[j] & 0xffff0000u)); }
                const int o = (31 + (tid >> 2)) * 80 + (tid & 3) * 16;
                *(u4 LAS*)(sm + PA_KH + o) = h4; *(u4 LAS*)(sm + PA_KL + o) = l4;
            }
            for (int i = tid; i < 262; i += 256) {
                const u4 x0 = *(const u4 LAS*)(sm + PA_UT + ch * 4224 + i * 16), x1 = *(const u4 LAS*)(sm + PA_UT + ch * 4224 + i * 16 + 16);
                const unsigned r[8] = {x0[0], x0[1], x0[2], x0[3], x1[0], x1[1], x1[2], x1[3]};
#pragma unroll
                for (int cp = 0; cp < 8; ++cp) {
                    u4 o;
#pragma unroll
                    for (int j = 0; j < 4; ++j) o[j] = (cp & 1) ? __builtin_amdgcn_alignbit(r[(cp >> 1) + j + 1], r[(cp >> 1) + j], 16) : r[(cp >> 1) + j];
                    *(u4 LAS*)(sm + PA_TU + (cp * 276 + (cp ? 1 : 0) + i) * 16) = o;
                }
            }
            bar();
            {
                f16v y0, y1;
                const int m00 = 2048 - l31 + 8 * hi, cp = m00 & 7;
                const char LAS* ab = sm + PA_TU + (cp * 276 + (cp ? 1 : 0) + (m00 >> 3)) * 16 - 63 * 64;
                const char LAS* bb = sm + PA_KH + l31 * 80 + hi * 16;
                const char LAS* a1w = ab - w * 64; const char LAS* b1w = bb - w * 80;
                const char LAS* a2w = ab + w * 64; const char LAS* b2w = bb + w * 80;
                u4 fr[2][4];
                auto rdg = [&](int g, int sl) __attribute__((always_inline)) {
                    const int i = (g & 15) >> 1, ks = g & 1;
                    if (g < 16) { fr[sl][0] = *(const u4 LAS*)(b1w + (31 - 4 * i) * 80 + ks * 32); fr[sl][2] = *(const u4 LAS*)(a1w + (63 - 4 * i) * 64 + ks * 32);
                                  fr[sl][3] = *(const u4 LAS*)(a1w + (31 - 4 * i) * 64 + ks * 32); fr[sl][1] = *(const u4 LAS*)(b1w + 7680 + (31 - 4 * i) * 80 + ks * 32); }
                    else { fr[sl][0] = *(const u4 LAS*)(b2w + (32 + 4 * i) * 80 + ks * 32); fr[sl][3] = *(const u4 LAS*)(a2w + (32 + 4 * i) * 64 + ks * 32);
                           fr[sl][1] = *(const u4 LAS*)(b2w + 7680 + (32 + 4 * i) * 80 + ks * 32); }
                };
                rdg(0, 0); SB();
#pragma unroll
                for (int g = 0; g < 32; ++g) {
                    const int sl = g & 1;
                    if (g + 1 < 32) rdg(g + 1, sl ^ 1);
                    SB();
                    if (g == 0) { mV_vv0(y0, fr[sl][2], fr[sl][0]); mV_vv0(y1, fr[sl][3], fr[sl][0]); }
                    else { if (g < 16) mV_vv(y0, fr[sl][2], fr[sl][0]); mV_vv(y1, fr[sl][3], fr[sl][0]); }
                    if (g < 16) mV_vv(y0, fr[sl][2], fr[sl][1]);
                    mV_vv(y1, fr[sl][3], fr[sl][1]);
                    SB();
                }
                MFW32(y0, y1);
#pragma unroll
                for (int g = 0; g < 4; ++g) {
                    *(f4 LAS*)(sm + PA_RED + w * 9216 + l31 * 144 + (8 * g + 4 * hi) * 4) = (f4){y0[4 * g], y0[4 * g + 1], y0[4 * g + 2], y0[4 * g + 3]};
                    *(f4 LAS*)(sm + PA_RED + w * 9216 + (32 + l31) * 144 + (8 * g + 4 * hi) * 4) = (f4){y1[4 * g], y1[4 * g + 1], y1[4 * g + 2], y1[4 * g + 3]};
                }
            }
            bar();
            {
                f4 s0 = {0.f, 0.f, 0.f, 0.f}, s1 = s0;
#pragma unroll
                for (int p = 0; p < 4; ++p) { const char LAS* q_ = sm + PA_RED + p * 9216 + (tid >> 2) * 144 + (tid & 3) * 32; s0 += *(const f4 LAS*)q_; s1 += *(const f4 LAS*)(q_ + 16); }
                const float dsk = bf2f(a.ADsk[d0 + ch]);
                float yv[8];
#pragma unroll
                for (int j = 0; j < 8; ++j) yv[j] = bfr(j < 4 ? s0[j & 3] : s1[j & 3]) + bfr(bf2f(*(const u16 LAS*)(sm + PA_UT + ch * 4224 + (2048 - (tid * 8 + j)) * 2)) * dsk);
                sst128((u4){pk2(yv[0], yv[1]), pk2(yv[2], yv[3]), pk2(yv[4], yv[5]), pk2(yv[6], yv[7])}, rpa, tid * 16, (d0 + ch) * 4096);
            }
            bar();
        }
    }
    set_flag(c, mksrd(a.HB, HB_SZ), O_FPA + task * 4, tag + 1);
}
DI void pa_out(unsigned tag, int wave, int t) {
    CArgs& a = get_args();
    const SideCtx c = make_ctx(wave);
    const int lane = c.lane, tid = c.wave * 64 + lane;
    t = __builtin_amdgcn_readfirstlane(t); asm volatile("" : "+s"(t));
    const u4 rpa = mksrd(a.PA, 56 * 32768), ro = mksrd(a.out, 2048 * 1792);
    wait_prog(mksrd(a.HB, HB_SZ), lane < 16 ? O_FPA + (t & ~7) * 8 + lane * 4 : OOB, tag + 1);
    u4 v[8];
#pragma unroll
    for (int i = 0; i < 8; ++i) v[i] = sld128(rpa, (tid >> 5) * 4096 + (tid & 31) * 16, ((t >> 3) * 64 + i * 8) * 4096 + (t & 7) * 512);
    WAITV(0, "+v"(v[0]), "+v"(v[1]), "+v"(v[2]), "+v"(v[3]), "+v"(v[4]), "+v"(v[5]), "+v"(v[6]), "+v"(v[7]));
    bar();
#pragma unroll
    for (int i = 0; i < 8; ++i) *(u4 LAS*)(c.sm + (i * 8 + (tid >> 5)) * 528 + (tid & 31) * 16) = v[i];
    bar();
#pragma unroll
    for (int k = 0; k < 8; ++k) {
        const char LAS* p = c.sm + (tid & 7) * 8 * 528 + ((tid >> 3) + 32 * k) * 2;
        u4 o;
#pragma unroll
        for (int j = 0; j < 4; ++j) o[j] = (unsigned)*(const u16 LAS*)(p + (2 * j) * 528) | ((unsigned)*(const u16 LAS*)(p + (2 * j + 1) * 528) << 16);
        sst128(o, ro, (tid >> 3) * 1792 + (tid & 7) * 16, ((t & 7) * 256 + 32 * k) * 1792 + (t >> 3) * 128);
        asm volatile("s_sleep %0" :: "i"(P_ASLP));
    }
}
#define L_TAB 0
#define L_CLK 32768
#define L_MEM 33792
#define L_WRK 49152
DI void edge(unsigned& ce, unsigned& ne) {
    const unsigned n0 = rt_now(); u64 t, c; int n = 0;
#pragma nounroll
    do { asm volatile("s_memrealtime %0\n\ts_memtime %1\n\ts_waitcnt lgkmcnt(0)" : "=&s"(t), "=&s"(c) :: "memory"); } while ((unsigned)t == n0 && ++n < 64);
    ce = (unsigned)c - 30u; ne = (unsigned)t;
}
DI unsigned srdw(const void* p, unsigned bytes, int i) { const u64 x = (u64)p; return i == 0 ? (unsigned)x : i == 1 ? (unsigned)(x >> 32) & 0xffffu : i == 2 ? bytes : 0x00020000u; }
#define RUN(r) asm volatile("v_mov_b32 v255, %0\n\ts_getpc_b64 vcc\n.Lfk_from_" #r ":\n\ts_add_u32 vcc_lo, vcc_lo, fk_role_" #r "-.Lfk_from_" #r "\n\ts_addc_u32 vcc_hi, vcc_hi, 0\n\ts_setpc_b64 vcc" :: "v"(tab) : "memory", "vcc", "scc", "s99", "v255", "a255")
DI unsigned probe(u4 r, int id, int xh) {
    const int lane = fresh_lane(), o = xh * (2 << 20) + O_PROBE + id * 128; unsigned best = 1016;
#pragma nounroll
    for (int rep = 0; rep < 2; ++rep) {
        sst32(rep, r, lane < 4 ? o + lane * 8192 : OOB, 0); waitvm<0>();
#pragma nounroll
        for (int k = 0; k < 4; ++k) {
            const int kk = (k + id) & 3, ol = lane == 0 ? o + kk * 8192 : OOB;
            const unsigned t0 = now32(); unsigned v = sld32(r, ol, 0); WAITV(0, "+v"(v));
            const unsigned d = now32() - t0;
            if (lane == kk) best = min(best, d);
        }
    }
    return best;
}
DI void chain_role(unsigned tag, int vb, int W, int role, int id, int xh, unsigned best) {
    CArgs& a = get_args();
    char LAS* sm = (char LAS*)smem_;
    const unsigned smb = (unsigned)(size_t)sm;
    const int lane = fresh_lane(), q = lane >> 4, l15 = lane & 15, i8 = lane & 7, hb = (lane >> 3) & 1;
    unsigned LAS* tb = (unsigned LAS*)(sm + L_TAB + W * 8192) + lane;
    const u4 r = mksrd(a.HB, HB_SZ);
    unsigned c0, n0, c1, n1; edge(c0, n0);
    if (role == 0) {
        if (W == 3) sst32(tag + 1 + (best >> 2), r, lane < 4 ? O_PAR + id * 16 + lane * 4 : OOB, 0);
    } else {
        unsigned v = sld32(mksrd(role == 1 ? a.UM : W == 0 ? a.GF : W == 1 ? a.UF : a.TF, role == 1 ? 65 * 28 * 4096 : W < 2 ? 64 * 16 * 28 * 1024 : 65 * 32 * 3584), lane << 19, 0); WAITV(0, "+v"(v));
    }
    const int cp = xh * 32768;
    const void *p1, *p2 = a.UF; unsigned x1 = 0, x2 = 0, po = 0; int poi, fw, wk;
    if (role == 0) {
        const int part = id & 1, h = id >> 1, rr = l15 >> 2, c = lane & 3;
        const int bq = q == 0 ? 0 : q == 1 ? 528 : q == 2 ? 1152 : 1680;
        tb[8 * 64] = O_MEM + cp + lane * 16 + part * 4096; tb[9 * 64] = O_MEM + cp + lane * 16 + (1 - part) * 4096; tb[10 * 64] = O_MEM + cp + lane * 16 + 3072;
        tb[11 * 64] = smb + L_WRK + bq + l15 * 8 + (W == 3 ? 3 * 2304 + part * 256 : 0); tb[14 * 64] = smb + L_WRK + bq + l15 * 8 + 3 * 2304 + (1 - part) * 256;
        tb[12 * 64] = smb + L_WRK + (c == 0 ? 0 : c == 2 ? 528 : c == 1 ? 1152 : 1680) + (16 * (q >> 1) + 4 * (q & 1) + 8 * (rr >> 1) + (rr & 1)) * 8;
        const int op = O_SMM + id * 256 + hb * 128 + (4 * (q & 1) + 2 * (q >> 1)) * 16 + i8 * 4;
        tb[13 * 64] = op + cp; tb[15 * 64] = op + (cp ^ 32768);
        int tile, chunk;
        if (W < 3) {
            const int i = lane < 28 ? lane : 0;
            const int p = i < 8 ? i : i < 19 ? (int)(0x43424130210ull >> (4 * (i - 8))) & 7 : 5 + (i - 19) / 3, k = i < 8 ? 0 : i < 19 ? (int)(0x32221212111ull >> (4 * (i - 8))) & 7 : 1 + (i - 19) % 3;
            const int n = 3 * (p & 3) + W;
            tile = 4 * (4 * (p < 4 ? part : 1 - part) + (n >> 2)) + (n & 3); chunk = k < 3 ? 4 * part + (W + k) % 3 : 3;
        } else {
            const int p = lane < 2 ? 2 + lane : (lane - 2) & 3, k = lane < 2 || lane >= 14 ? 3 : (lane - 2) >> 2;
            tile = 12 + (p < 2 ? 2 * part + p : 2 * (1 - part) + p - 2); chunk = k < 3 ? 4 * part + k : 3;
        }
        wk = ((h * 28 + tile) * 7 + chunk) * 2048; fw = h * 8 + (lane & 3);
        poi = (W == 0 && q < 2) ? O_SMM + q * 32768 + id * 256 + l15 * 16 : OOB;
        p1 = a.WqkF; x1 = 4 * part + 1;
    } else if (role == 1) {
        const int hc = 4 * W + ((q & 1) << 1) + (q >> 1), k = i8 - 4 * hb;
        const int ocp = W == 0 ? O_MEM : W == 3 ? O_MEM + 32768 : O_MEMX + (W - 1) * 32768;
        tb[8 * 64] = O_MEM + cp + lane * 16; tb[9 * 64] = O_MEM + cp + lane * 16 + 4096;
        tb[10 * 64] = O_SMM + cp + hc * 512 + l15 * 16; tb[11 * 64] = O_MZ + hc * 128 + l15 * 8;
        tb[12 * 64] = RXW(id >> 1) + (id & 1) * 4096 + hc * 256 + l15 * 16;
        tb[13 * 64] = ocp + id * 256 + lane * 4;
        tb[14 * 64] = k == 0 ? 0xffffu : k == 1 ? 0xffff0000u : 0u; tb[15 * 64] = k == 2 ? 0xffffu : k == 3 ? 0xffff0000u : 0u;
        tb[16 * 64] = smb + L_WRK + W * 512 + lane * 8;
        tb[17 * 64] = smb + L_WRK + ((lane & 32) + (lane & 2) * 8 + (lane & 1) * 8 + ((lane >> 2) & 7)) * 8;
        tb[18 * 64] = id * 4096 + W * 1024 + lane * 16;
        tb[19 * 64] = san(*(const unsigned*)(a.mem0 + ((lane >> 2) & 7) * 448 + id * 16 + (lane >> 5) * 8 + (lane & 3) * 2));
        tb[20 * 64] = smb + L_MEM + lane * 16;
        tb[21 * 64] = lane == 0 ? O_FT + (32 + id) * 128 : OOB;
        tb[23 * 64] = (q == 0 ? O_MEM : q == 3 ? O_MEM + 32768 : O_MEMX + (q - 1) * 32768) + id * 256 + l15 * 16;
        const bool lead = id == 14 * xh;
        tb[22 * 64] = lane != 0 ? OOB : W == 2 ? (lead ? O_REC + xh * 8 : OOB) : id == 0 ? O_EPOCH : OOB;
        if (W == 3) sst64((u2){REC_A, REC_B}, r, lead && lane < 3 ? O_REC + lane * 128 + xh * 8 : OOB, 0);
        const int ord = ((W == 0 ? 0x6543210 : W == 1 ? 0x3106542 : W == 2 ? 0x4321065 : 0x6542103) >> (4 * (lane % 7))) & 7;
        wk = ((id * 16 + 4 * W + (lane / 7 & 3)) * 7 + ord) * 2048; fw = (4 * W + (lane & 3)) * 8 + 4 + (id >> 3);
        poi = q == 0 ? ocp + id * 256 + l15 * 16 : OOB;
        p1 = a.WvoF; p2 = a.UM; po = OFF_P;
    } else {
        const int h = id >> 1, half = id & 1, pt = 4 * W + q;
        tb[8 * 64] = O_MEMX + cp + lane * 16; tb[9 * 64] = O_MEMX + cp + lane * 16 + 4096;
        tb[10 * 64] = (W < 3 || (q & 1) == 0) ? RXW(half * 7 + 2 * W + (q & 1)) + (q >> 1) * 4096 + h * 256 + l15 * 16 : OOB;
        tb[11 * 64] = ((lane & 3) == 0 && half == 0) ? O_MZ + h * 128 + (lane >> 2) * 8 : OOB;
        tb[12 * 64] = smb + L_WRK + lane * 8;
        tb[13 * 64] = smb + L_WRK + ((q >> 1) * 32 + (lane & 1) * 16 + 4 * (q & 1) + (l15 >> 2)) * 8;
        poi = pt < 14 ? RXW(half * 7 + (pt >> 1)) + (pt & 1) * 4096 + h * 256 + l15 * 16 : (pt == 14 && half == 0 && l15 < 8) ? O_MZ + h * 128 + l15 * 16 : OOB;
        tb[14 * 64] = poi;
        tb[15 * 64] = O_FW + (h * 8 + i8) * 128;
        tb[16 * 64] = smb + L_MEM + lane * 16;
        tb[17 * 64] = smb + L_WRK + 1024 + W * 2048 + ((q >> 1) * 16 + l15) * 16 + (q & 1) * 8;
        tb[18 * 64] = smb + L_WRK + 1024 + W * 2048 + (8 * q + (l15 >> 2) + 32 * ((lane >> 1) & 1)) * 16 + (lane & 1) * 8;
        const int otf = id * 3584 + W * 1024 + (4 * hb + q) * 128 + i8 * 16;
        tb[19 * 64] = otf; tb[21 * 64] = hb ? OOB : otf;
        tb[20 * 64] = lane == 0 ? O_FT + id * 128 : OOB;
        if (W == 3)
            for (int k = 0; k < 3; ++k) tb[(17 + k) * 64] = RXW(half * 7 + 2 * k + (q >> 1)) + (q & 1) * 4096 + h * 256 + l15 * 16;
        wk = ((h * 28 + half * 14 + 4 * W) * 7 + (lane < 42 ? lane : 0)) * 2048; fw = h * 8 + (lane & 3);
        p1 = W == 3 ? a.GF : a.WqkF; x1 = h * 28 * 1024; x2 = (h * 28 + half * 14 + 4 * W) * 1024; po = OFF_X;
    }
    tb[0 * 64] = (lane & 8) ? 0xEEEE : 0x4444; tb[1 * 64] = lane * 16; tb[2 * 64] = wk; tb[3 * 64] = ((vb >> 3) * 16 + (vb & 3) * 4 + W) * 1280 + lane * 4;
    tb[4 * 64] = O_FW + fw * 128; tb[5 * 64] = O_RDY + lane * 4; tb[6 * 64] = O_RDY + 256 + lane * 4; tb[7 * 64] = O_REC;
    wait_prog(r, O_PAR + lane * 4, tag + 1); wait_prog(r, O_PAR + 256 + lane * 4, tag + 1);
    unsigned la = sld32(r, O_PAR + lane * 4, 0), lb = sld32(r, O_PAR + 256 + lane * 4, 0); WAITV(0, "+v"(la), "+v"(lb));
    la = ((la - tag) << 2) | (lane & 3); lb = ((lb - tag) << 2) | (lane & 3);
#pragma unroll
    for (int sft = 32; sft >= 4; sft >>= 1) { la = min(la, (unsigned)__shfl_xor(la, sft)); lb = min(lb, (unsigned)__shfl_xor(lb, sft)); }
    la = min(la, (unsigned)__shfl_xor(la, 1)); la = min(la, (unsigned)__shfl_xor(la, 2)); lb = min(lb, (unsigned)__shfl_xor(lb, 1)); lb = min(lb, (unsigned)__shfl_xor(lb, 2));
    const unsigned pa = __builtin_amdgcn_readfirstlane(la) & 3, pb = __builtin_amdgcn_readfirstlane(lb) & 3;
    const unsigned dx = (1u << 21) | ((pa ^ pb) << 13);
#pragma unroll
    for (int i = 0; i < 8; ++i) sst128((u4){FF, FF, FF, FF}, r, poi, (((pa << 13) ^ ((i & 1) << 14)) + (i & 3) * 524288) ^ (i & 4 ? dx : 0u));
    edge(c1, n1);
    const unsigned f = min(max((unsigned)((float)(c1 - c0) * 252.f / (float)(n1 - n0)), 0x800u), 0x2000u);
    const unsigned k0 = n1 * 16u / LAT_R, sl0 = (pa << 13) ^ ((k0 & 1) ? dx : 0u);
    const unsigned sz1 = (role == 2 && W == 3) ? 64 * 16 * 28 * 1024 : 28 * 16 * 14336, sz2 = role == 1 ? 65 * 28 * 4096 : 64 * 16 * 28 * 1024;
    unsigned sv;
    if (lane < 4) sv = srdw(a.HB, HB_SZ, lane); else if (lane < 8) sv = srdw(p1, sz1, lane - 4); else if (lane < 12) sv = srdw(p2, sz2, lane - 8); else if (lane < 16) sv = srdw(a.TF, 65 * 32 * 3584, lane - 12);
    else if (lane < 20) sv = srdw(a.TS, 512 * 1280, lane - 16);
    else sv = lane == 20 ? k0 * LAT_R : lane == 21 ? LAT_R : lane == 24 ? po : lane == 25 ? dx : lane == 26 ? tag : lane == 27 ? sl0 : lane == 28 ? sl0 ^ 16384u : lane == 29 ? x1 : lane == 30 ? x2
            : lane == 33 ? (unsigned)TOL_C : lane == 34 ? smb + L_CLK : 0u;
    tb[31 * 64] = sv;
    if (W == 0 && lane < 2) *(u4 LAS*)(sm + L_CLK + lane * 16) = lane == 0 ? (u4){n1 * 16u, c1, f, 0u} : (u4){0u, 0u, 0u, 0u};
    waitvm<0>(); bar();
    if (W == 0) sst32(tag + 1, r, lane == 0 ? O_RDY + ((vb & 3) * 32 + (vb >> 3)) * 4 : OOB, 0);
    const unsigned tab = smb + L_TAB + W * 8192 + lane * 4;
    if (role == 0) { if (W == 0) RUN(S0); else if (W == 1) RUN(S1); else if (W == 2) RUN(S2); else RUN(S3); }
    else if (role == 1) { if (W == 0) RUN(P0); else if (W == 1) RUN(P1); else if (W == 2) RUN(P2); else RUN(P3); }
    else { if (W == 0) RUN(X0); else if (W == 1) RUN(X1); else if (W == 2) RUN(X2); else RUN(X3); }
}

#define NPA_FAR 10
#define PAO_AT 118
__global__ __launch_bounds__(256, 1) void fused_kernel(Args a_) {
    CArgs& a = get_args();
    const int wave = __builtin_amdgcn_readfirstlane(threadIdx.x >> 6);
    const u4 rhb = mksrd(a.HB, HB_SZ);
    unsigned ep = 0;
    if (wave == 0) ep = ld32(rhb, O_EPOCH, 0);
    else {
        u64 pc; unsigned len;
        asm volatile("s_getpc_b64 %0\nfk_code_lo:\n\ts_mov_b32 %1, fk_code_hi-fk_code_lo" : "=s"(pc), "=s"(len));
        const u4 rc = mksrd((const void*)pc, len & ~15u);
        const int vo = (((blockIdx.x >> 3) * 3 + wave - 1) * 2) * 1024 + (threadIdx.x & 63) * 16;
#pragma unroll
        for (int i = 0; i < 2; ++i) dma_one(rc, vo + i * 1024, 0, (unsigned)(size_t)(char LAS*)smem_ + 147456 + (wave * 3 + i) * 1024);
    }
    const int xcc = __builtin_amdgcn_s_getreg((31 << 11) | 20) & 7;
    int vb = xcc + (blockIdx.x & ~7);
    unsigned tag, best = 1016;
    if ((vb & 0x85) == 1 && wave == 3) best = probe(rhb, vb >> 3, (vb >> 1) & 1);
#pragma nounroll
    for (int pass = 0;; ++pass) {
        if (vb & 4) w_first(wave, (vb >> 5) & 1, 4 * (vb & 3) + ((vb >> 3) & 3), vb >> 6); else waitvm<0>();
        if (pass) break;
        if (wave == 0) {
            WAITV(28, "+v"(ep));
            if (threadIdx.x == 0) {
                const unsigned tg = ep + 0x9E3779B4u;
                unsigned slot = (unsigned)xcc * 32u + (blockIdx.x >> 3);
                for (int k = 0; k < 256; ++k, slot = (slot + 1u) & 255u)
                    if (__hip_atomic_exchange((unsigned*)(a.HB + O_CNT) + slot * 32u, tg, __ATOMIC_RELAXED, __HIP_MEMORY_SCOPE_AGENT) != tg) break;
                *(u2 LAS*)(smem_ + 147456) = (u2){(slot >> 5) + 8u * (slot & 31u), tg};
            }
        }
        bar();
        const int got = __builtin_amdgcn_readfirstlane(*(const unsigned LAS*)(smem_ + 147456));
        tag = __builtin_amdgcn_readfirstlane(*(const unsigned LAS*)(smem_ + 147460));
        if (got == vb) break;
        vb = got; best = 1016; waitvm<0>(); bar();
    }
    const int res = vb & 7, idx = vb >> 3;
    int j;
    const int xq = res & 3;
    unsigned* const tick = (unsigned*)(a.HB + O_TICK) + xq * 32;
    if (res < 4) {
        const int xh = res >> 1;
        if (idx < 16 || ((res & 1) && idx < 30)) { chain_role(tag, vb, wave, (res & 1) ? idx >> 4 : 2, idx < 16 ? xh * 16 + idx : xh * 14 + idx - 16, xh, best); return; }
        if (wave == 0) sst32(tag + 1, rhb, fresh_lane() == 0 ? O_RDY + (res * 32 + idx) * 4 : OOB, 0);
        j = -2;
    } else {
        if (idx == 0 && wave == 0) sst32(0u, rhb, fresh_lane() == 0 ? O_TICK + xq * 128 : OOB, 0);
        if (idx & 4) wgu_task<1>(tag, vb, wave, 4 * xq + (idx & 3), idx >> 3); else wgu_task<0>(tag, vb, wave, 4 * xq + (idx & 3), idx >> 3);
        wait_prog(rhb, fresh_lane() == 0 ? O_FW + xq * 32 * 128 : OOB, tag + 1);
        j = 0;
    }
    int round = 0;
#pragma nounroll
    for (; round < 256; ++round) {
        if (j >= 0) {
            if (wave == 0 && fresh_lane() == 0) *(unsigned LAS*)(smem_ + 160 * 1024 - 16) = __hip_atomic_fetch_add(tick, 1u, __ATOMIC_RELAXED, __HIP_MEMORY_SCOPE_AGENT);
            bar();
            j = __builtin_amdgcn_readfirstlane(*(const unsigned LAS*)(smem_ + 160 * 1024 - 16));
            if ((unsigned)j >= NPA_FAR + 190u) break;
        }
        int k = j - NPA_FAR;
        if (k < 0) {
            parta_task(tag, wave, j < 0 ? 2 * ((res & 1) ? (res >> 1) * 2 + idx + 2 : (res >> 1) * 16 + idx - 16) + j + 2 : 72 + xq * NPA_FAR + j);
            if (j < 0 && ++j == 0) break;
            continue;
        }
        if (k >= PAO_AT && k < PAO_AT + 14) { pa_out(tag, wave, (k - PAO_AT) * 4 + xq); bar(); continue; }
        if (k >= PAO_AT) k -= 14;
        const int g = k < 162 ? (k + 14) / 11 : 16 + (k - 162) / 7, o = k < 162 ? (k + 14) % 11 : (k - 162) % 7;
        if (k < 8 || o >= 7) e_task(tag, wave, 4 * (k < 8 ? k >> 2 : g) + xq, k < 8 ? k & 3 : o - 7);
        else { x_task(tag, wave, 4 * (g - 2) + xq, o); bar(); }
    }
}
__device__ __attribute__((used, noinline)) void fused_kernel_roles() { asm volatile("s_endpgm\n" ASM_ROLES "fk_code_hi:"); }

#define WS_SKIP 0
extern "C" void kernel_launch(void* const* d_in, const int* in_sizes, int n_in, void* d_out, int out_size, void* d_ws, size_t ws_size, hipStream_t stream) {
    Args a;
    a.Au = (const u16*)d_in[0]; a.AlogA = (const u16*)d_in[1]; a.Aim = (const u16*)d_in[2]; a.ACr = (const u16*)d_in[3]; a.ACi = (const u16*)d_in[4];
    a.Alogdt = (const u16*)d_in[5]; a.ADsk = (const u16*)d_in[6]; a.X = (const u16*)d_in[7]; a.mem0 = (const u16*)d_in[8];
    a.Wq = (const u16*)d_in[9]; a.Wk = (const u16*)d_in[10]; a.Wv = (const u16*)d_in[11]; a.Wo = (const u16*)d_in[12];
    a.out = (u16*)d_out;
    char* base = (char*)(((size_t)d_ws + WS_SKIP + (2u << 20) - 1) & ~(size_t)((2u << 20) - 1));
    a.HB = base;
    size_t off = HB_SZ;
    auto alloc = [&](size_t n) { char* p = base + off; off += (n + 4095) & ~(size_t)4095; return p; };
    a.WvoF = (u16*)alloc(28 * 16 * 14336); a.WqkF = (u16*)alloc(28 * 16 * 14336); a.GF = (u16*)alloc((size_t)64 * 16 * 28 * 1024); a.UF = (u16*)alloc((size_t)64 * 16 * 28 * 1024);
    a.EX = (u16*)alloc(64 * 16 * 2048); a.UM = (u16*)alloc(65 * 28 * 4096); a.MZX = (float*)alloc(64 * 16 * 256); a.TF = (u16*)alloc(65 * 32 * 3584); a.PA = (u16*)alloc(56 * 32768);
    a.TS = (unsigned*)alloc(768 * 1280);
    (void)hipFuncSetAttribute((const void*)fused_kernel, hipFuncAttributeMaxDynamicSharedMemorySize, 160 * 1024);
    fused_kernel<<<256, 256, 160 * 1024, stream>>>(a);
}
```

```cpp
#include <hip/hip_runtime.h>
#include <stdint.h>
typedef unsigned short u16;
typedef unsigned long long u64;
typedef __attribute__((ext_vector_type(8))) __bf16 bf8;
typedef __attribute__((ext_vector_type(16))) __bf16 bf16v;
typedef __attribute__((ext_vector_type(2))) __bf16 bf2;
typedef __attribute__((ext_vector_type(2))) float f2;
typedef __attribute__((ext_vector_type(4))) float f4;
typedef __attribute__((ext_vector_type(4))) unsigned u4;
typedef __attribute__((ext_vector_type(2))) unsigned u2;
typedef __attribute__((ext_vector_type(8))) unsigned u8v;
typedef __attribute__((ext_vector_type(4))) short s4;
#define LAS __attribute__((address_space(3)))
#define DI __device__ __forceinline__
#define SB() __builtin_amdgcn_sched_barrier(0)

DI unsigned pk2(float a, float b) { f2 v = {a, b}; return __builtin_bit_cast(unsigned, __builtin_convertvector(v, bf2)); }
DI float bfr(float a) { return __uint_as_float(pk2(a, a) & 0xffff0000u); }
DI unsigned san(unsigned x) { return x < 0xFFFFFFFEu ? x : 0xFFFFFFFEu; }
DI void bar() { asm volatile("s_waitcnt lgkmcnt(0)\n\ts_barrier" ::: "memory"); SB(); }
DI float x32max(float a) { auto r = __builtin_amdgcn_permlane32_swap(__float_as_uint(a), __float_as_uint(a), false, false); return fmaxf(__uint_as_float(r[0]), __uint_as_float(r[1])); }
DI float x32add(float a) { auto r = __builtin_amdgcn_permlane32_swap(__float_as_uint(a), __float_as_uint(a), false, false); return __uint_as_float(r[0]) + __uint_as_float(r[1]); }
DI u2 trread(const char LAS* p) { return __builtin_bit_cast(u2, __builtin_amdgcn_ds_read_tr16_b64_v4i16((LAS s4*)p)); }
DI u4 mksrd(const void* p, unsigned bytes) { u64 x = (u64)p; return (u4){(unsigned)x, (unsigned)(x >> 32) & 0xffffu, bytes, 0x00020000u}; }
DI u4 ld128(u4 srd, int voff, int soff) { u4 v; asm volatile("buffer_load_dwordx4 %0, %1, %2, %3 offen sc1" : "=v"(v) : "v"(voff), "s"(srd), "s"(soff) : "memory"); return v; }
DI unsigned ld32(u4 srd, int voff, int soff) { unsigned v; asm volatile("buffer_load_dword %0, %1, %2, %3 offen sc1" : "=v"(v) : "v"(voff), "s"(srd), "s"(soff) : "memory"); return v; }
DI void st128(u4 v, u4 srd, int voff, int soff) { asm volatile("buffer_store_dwordx4 %0, %1, %2, %3 offen sc1\n\ts_nop 1" :: "v"(v), "v"(voff), "s"(srd), "s"(soff) : "memory"); }
DI void st32(unsigned v, u4 srd, int voff, int soff) { asm volatile("buffer_store_dword %0, %1, %2, %3 offen sc1" :: "v"(v), "v"(voff), "s"(srd), "s"(soff) : "memory"); }
#define PIN(x) asm volatile("" : "+v"(x))
#define WAITV(n, ...) asm volatile("s_waitcnt vmcnt(" #n ")" : __VA_ARGS__ :: "memory")

#define NSEG 64
#define O_MEM 0
#define O_SMM (2 * 32768)
#define O_MZ (4 * 32768)
#define O_MEMX 16384
#define RXW(n) ((n) < 7 ? (5 + (n)) * 32768 : 16384 + ((n) - 5) * 32768)
#define O_PROBE (15 * 32768)
#define O_FL (4 << 20)
#define O_PAR O_FL
#define O_RDY (O_FL + 512)
#define O_FPA (O_FL + 1024)
#define O_FE (O_FL + 2048)
#define O_FW (O_FL + 4096)
#define O_FT (O_FW + 16384)
#define O_REC (O_FT + 8192)
#define O_CNT (O_REC + 16384)
#define O_EPOCH (O_CNT + 32768)
#define O_TICK (O_EPOCH + 128)
#define HB_SZ (5 << 20)
#define SPIN_MAX (1u << 17)
#define LOG2E 1.4426950408889634f
#define FF 0xFFFFFFFFu
#define OOB 0x7f000000

struct Args {
    const u16 *Au, *AlogA, *Aim, *ACr, *ACi, *Alogdt, *ADsk, *X, *mem0, *Wq, *Wk, *Wv, *Wo;
    u16* out;
    char* HB;
    u16 *WvoF, *WqkF, *GF, *UF, *EX, *UM, *TF, *PA; float *MZX;
    unsigned* TS;
};
typedef const Args __attribute__((address_space(4))) CArgs;
DI CArgs& get_args() { CArgs* p = (CArgs*)__builtin_amdgcn_kernarg_segment_ptr(); asm volatile("" : "+s"(p)); return *p; }
extern __shared__ __attribute__((aligned(16))) char smem_[];
DI unsigned now32() { u64 t; asm volatile("s_memtime %0\n\ts_waitcnt lgkmcnt(0)" : "=s"(t) :: "memory"); return (unsigned)t; }
DI unsigned rt_now() { u64 t; asm volatile("s_memrealtime %0\n\ts_waitcnt lgkmcnt(0)" : "=s"(t) :: "memory"); return (unsigned)t; }
#define TNOW() 0
#define REC_A 1145
#define REC_B 930
#define LAT_R 1200u
#define OFF_P 60
#define OFF_X 20
#define TOL_C -100
#define ASM_ROLES \
".p2align 6\n" \
"fk_role_S0:\n" \
"ds_read_b32 v200, v255 offset:0\n" \
"ds_read_b32 v201, v255 offset:256\n" \
"ds_read_b32 v202, v255 offset:512\n" \
"ds_read_b32 v203, v255 offset:768\n" \
"ds_read_b32 v204, v255 offset:1024\n" \
"ds_read_b32 v205, v255 offset:1280\n" \
"ds_read_b32 v206, v255 offset:1536\n" \
"ds_read_b32 v207, v255 offset:1792\n" \
"ds_read_b32 v208, v255 offset:2048\n" \
"ds_read_b32 v209, v255 offset:2304\n" \
"ds_read_b32 v210, v255 offset:2560\n" \
"ds_read_b32 v211, v255 offset:2816\n" \
"ds_read_b32 v212, v255 offset:3072\n" \
"ds_read_b32 v213, v255 offset:3328\n" \
"ds_read_b32 v214, v255 offset:3584\n" \
"ds_read_b32 v215, v255 offset:3840\n" \
"ds_read_b32 v254, v255 offset:7936\n" \
"s_waitcnt lgkmcnt(0)\n" \
"v_readlane_b32 s4, v254, 0\n" \
"v_readlane_b32 s5, v254, 1\n" \
"v_readlane_b32 s6, v254, 2\n" \
"v_readlane_b32 s7, v254, 3\n" \
"v_readlane_b32 s8, v254, 4\n" \
"v_readlane_b32 s9, v254, 5\n" \
"v_readlane_b32 s10, v254, 6\n" \
"v_readlane_b32 s11, v254, 7\n" \
"v_readlane_b32 s12, v254, 8\n" \
"v_readlane_b32 s13, v254, 9\n" \
"v_readlane_b32 s14, v254, 10\n" \
"v_readlane_b32 s15, v254, 11\n" \
"v_readlane_b32 s16, v254, 12\n" \
"v_readlane_b32 s17, v254, 13\n" \
"v_readlane_b32 s18, v254, 14\n" \
"v_readlane_b32 s19, v254, 15\n" \
"v_readlane_b32 s20, v254, 16\n" \
"v_readlane_b32 s21, v254, 17\n" \
"v_readlane_b32 s22, v254, 18\n" \
"v_readlane_b32 s23, v254, 19\n" \
"v_readlane_b32 s24, v254, 20\n" \
"v_readlane_b32 s25, v254, 21\n" \
"v_readlane_b32 s26, v254, 22\n" \
"v_readlane_b32 s27, v254, 23\n" \
"v_readlane_b32 s28, v254, 24\n" \
"v_readlane_b32 s29, v254, 25\n" \
"v_readlane_b32 s30, v254, 26\n" \
"v_readlane_b32 s31, v254, 27\n" \
"v_readlane_b32 s32, v254, 28\n" \
"v_readlane_b32 s33, v254, 29\n" \
"v_readlane_b32 s34, v254, 30\n" \
"v_readlane_b32 s35, v254, 31\n" \
"v_readlane_b32 s36, v254, 32\n" \
"v_readlane_b32 s37, v254, 33\n" \
"v_readlane_b32 s38, v254, 34\n" \
"v_readlane_b32 s39, v254, 35\n" \
"s_mov_b32 s40, 0\n" \
"s_mov_b32 s51, 0\n" \
"s_nop 0\n" \
"v_mov_b32 v252, s38\n" \
"s_nop 4\n" \
"v_mov_b32 v188, -1\n" \
"s_mov_b32 s28, s33\n" \
"s_mov_b32 s33, -1\n" \
"v_writelane_b32 v251, s6, 0\n" \
"v_writelane_b32 v251, s10, 1\n" \
"v_writelane_b32 v251, s14, 2\n" \
"v_writelane_b32 v251, s18, 3\n" \
"s_mov_b32 s6, 0\n" \
"s_mov_b32 s10, 0\n" \
"s_mov_b32 s14, 0\n" \
"s_mov_b32 s18, 0\n" \
"s_mov_b32 s40, -1\n" \
"s_branch .LS0_pre\n" \
".LS0_wet:\n" \
"s_waitcnt vmcnt(0)\n" \
"v_readlane_b32 s6, v251, 0\n" \
"v_readlane_b32 s10, v251, 1\n" \
"v_readlane_b32 s14, v251, 2\n" \
"v_readlane_b32 s18, v251, 3\n" \
"s_nop 4\n" \
"ds_read_b32 v232, v255 offset:7424\n" \
"ds_read_b32 v233, v255 offset:7680\n" \
"v_lshrrev_b32 v234, 4, v201\n" \
"v_and_b32 v234, 3, v234\n" \
"v_subrev_u32 v230, 512, v205\n" \
"s_add_u32 s43, s30, 1\n" \
"s_mov_b32 s50, 0\n" \
".LS0_w1l:\n" \
"buffer_load_dword v240, v230, s[4:7], 0 offen sc1\n" \
"s_waitcnt vmcnt(0)\n" \
".LS0_w1c:\n" \
"v_subrev_u32 v241, s43, v240\n" \
"v_cmp_gt_u32 vcc, 0x100, v241\n" \
"s_cmp_eq_u64 vcc, -1\n" \
"s_cbranch_scc1 .LS0_w1d\n" \
"s_sleep 2\n" \
"s_add_u32 s50, s50, 1\n" \
"s_cmp_lt_u32 s50, 0x20000\n" \
"s_cbranch_scc1 .LS0_w1l\n" \
"s_branch .LS0_end\n" \
".LS0_w1d:\n" \
"v_lshl_or_b32 v241, v241, 2, v234\n" \
"s_nop 1\n" \
"v_min_u32_dpp v241, v241, v241 row_ror:4 row_mask:0xf bank_mask:0xf\n" \
"s_nop 1\n" \
"v_min_u32_dpp v241, v241, v241 row_ror:8 row_mask:0xf bank_mask:0xf\n" \
"s_nop 1\n" \
"v_min_u32_dpp v241, v241, v241 quad_perm:[1,0,3,2] row_mask:0xf bank_mask:0xf\n" \
"s_nop 1\n" \
"v_min_u32_dpp v241, v241, v241 quad_perm:[2,3,0,1] row_mask:0xf bank_mask:0xf\n" \
"s_nop 0\n" \
"v_readlane_b32 s31, v241, 0\n" \
"v_readlane_b32 s43, v241, 16\n" \
"s_min_u32 s31, s31, s43\n" \
"v_readlane_b32 s43, v241, 32\n" \
"s_min_u32 s31, s31, s43\n" \
"v_readlane_b32 s43, v241, 48\n" \
"s_min_u32 s31, s31, s43\n" \
"v_subrev_u32 v230, 256, v205\n" \
"s_add_u32 s43, s30, 1\n" \
"s_mov_b32 s50, 0\n" \
".LS0_w2l:\n" \
"buffer_load_dword v240, v230, s[4:7], 0 offen sc1\n" \
"s_waitcnt vmcnt(0)\n" \
".LS0_w2c:\n" \
"v_subrev_u32 v241, s43, v240\n" \
"v_cmp_gt_u32 vcc, 0x100, v241\n" \
"s_cmp_eq_u64 vcc, -1\n" \
"s_cbranch_scc1 .LS0_w2d\n" \
"s_sleep 2\n" \
"s_add_u32 s50, s50, 1\n" \
"s_cmp_lt_u32 s50, 0x20000\n" \
"s_cbranch_scc1 .LS0_w2l\n" \
"s_branch .LS0_end\n" \
".LS0_w2d:\n" \
"v_lshl_or_b32 v241, v241, 2, v234\n" \
"s_nop 1\n" \
"v_min_u32_dpp v241, v241, v241 row_ror:4 row_mask:0xf bank_mask:0xf\n" \
"s_nop 1\n" \
"v_min_u32_dpp v241, v241, v241 row_ror:8 row_mask:0xf bank_mask:0xf\n" \
"s_nop 1\n" \
"v_min_u32_dpp v241, v241, v241 quad_perm:[1,0,3,2] row_mask:0xf bank_mask:0xf\n" \
"s_nop 1\n" \
"v_min_u32_dpp v241, v241, v241 quad_perm:[2,3,0,1] row_mask:0xf bank_mask:0xf\n" \
"s_nop 0\n" \
"v_readlane_b32 s32, v241, 0\n" \
"v_readlane_b32 s43, v241, 16\n" \
"s_min_u32 s32, s32, s43\n" \
"v_readlane_b32 s43, v241, 32\n" \
"s_min_u32 s32, s32, s43\n" \
"v_readlane_b32 s43, v241, 48\n" \
"s_min_u32 s32, s32, s43\n" \
"s_xor_b32 s29, s31, s32\n" \
"s_and_b32 s29, s29, 3\n" \
"s_lshl_b32 s29, s29, 13\n" \
"s_bitset1_b32 s29, 21\n" \
"s_and_b32 s31, s31, 3\n" \
"s_lshl_b32 s31, s31, 13\n" \
"s_bitcmp1_b32 s39, 0\n" \
"s_cselect_b32 s43, s29, 0\n" \
"s_xor_b32 s31, s31, s43\n" \
"s_xor_b32 s32, s31, 0x4000\n" \
"s_mov_b32 s39, 0\n" \
"v_mov_b32 v224, -1\n" \
"v_mov_b32 v225, -1\n" \
"v_mov_b32 v226, -1\n" \
"v_mov_b32 v227, -1\n" \
"s_waitcnt lgkmcnt(0)\n" \
"s_add_u32 s50, s31, 0\n" \
"buffer_store_dwordx4 v[224:227], v232, s[4:7], s50 offen sc1\n" \
"s_xor_b32 s43, s50, s29\n" \
"buffer_store_dwordx4 v[224:227], v232, s[4:7], s43 offen sc1\n" \
"s_add_u32 s50, s32, 524288\n" \
"buffer_store_dwordx4 v[224:227], v232, s[4:7], s50 offen sc1\n" \
"s_xor_b32 s43, s50, s29\n" \
"buffer_store_dwordx4 v[224:227], v232, s[4:7], s43 offen sc1\n" \
"s_add_u32 s50, s31, 1048576\n" \
"buffer_store_dwordx4 v[224:227], v232, s[4:7], s50 offen sc1\n" \
"s_xor_b32 s43, s50, s29\n" \
"buffer_store_dwordx4 v[224:227], v232, s[4:7], s43 offen sc1\n" \
"s_add_u32 s50, s32, 1572864\n" \
"buffer_store_dwordx4 v[224:227], v232, s[4:7], s50 offen sc1\n" \
"s_xor_b32 s43, s50, s29\n" \
"buffer_store_dwordx4 v[224:227], v232, s[4:7], s43 offen sc1\n" \
"s_waitcnt vmcnt(0)\n" \
"s_barrier\n" \
"s_add_u32 s43, s30, s40\n" \
"s_add_u32 s43, s43, 1\n" \
"v_mov_b32 v241, s43\n" \
"buffer_store_dword v241, v233, s[4:7], 0 offen sc1\n" \
"s_mov_b32 s33, s28\n" \
"s_add_u32 s34, s33, 2\n" \
"s_add_u32 s36, s33, 4\n" \
"s_add_u32 s43, s30, s33\n" \
"s_mov_b32 s50, 0\n" \
".LS0_w3l:\n" \
"buffer_load_dword v240, v204, s[4:7], 0 offen sc1\n" \
"s_waitcnt vmcnt(0)\n" \
".LS0_w3c:\n" \
"v_subrev_u32 v241, s43, v240\n" \
"v_cmp_gt_u32 vcc, 0x100, v241\n" \
"s_cmp_eq_u64 vcc, -1\n" \
"s_cbranch_scc1 .LS0_w3d\n" \
"s_sleep 2\n" \
"s_add_u32 s50, s50, 1\n" \
"s_cmp_lt_u32 s50, 0x20000\n" \
"s_cbranch_scc1 .LS0_w3l\n" \
"s_branch .LS0_end\n" \
".LS0_w3d:\n" \
"v_readlane_b32 s43, v202, 0\n" \
"s_nop 4\n" \
"buffer_load_dwordx4 a[0:3], v201, s[8:11], s43 offen sc1\n" \
"buffer_load_dwordx4 a[4:7], v201, s[8:11], s43 offen offset:1024 sc1\n" \
"v_readlane_b32 s43, v202, 1\n" \
"s_nop 4\n" \
"buffer_load_dwordx4 a[8:11], v201, s[8:11], s43 offen sc1\n" \
"buffer_load_dwordx4 a[12:15], v201, s[8:11], s43 offen offset:1024 sc1\n" \
"v_readlane_b32 s43, v202, 2\n" \
"s_nop 4\n" \
"buffer_load_dwordx4 a[16:19], v201, s[8:11], s43 offen sc1\n" \
"buffer_load_dwordx4 a[20:23], v201, s[8:11], s43 offen offset:1024 sc1\n" \
"v_readlane_b32 s43, v202, 3\n" \
"s_nop 4\n" \
"buffer_load_dwordx4 a[24:27], v201, s[8:11], s43 offen sc1\n" \
"buffer_load_dwordx4 a[28:31], v201, s[8:11], s43 offen offset:1024 sc1\n" \
"v_readlane_b32 s43, v202, 4\n" \
"s_nop 4\n" \
"buffer_load_dwordx4 a[32:35], v201, s[8:11], s43 offen sc1\n" \
"buffer_load_dwordx4 a[36:39], v201, s[8:11], s43 offen offset:1024 sc1\n" \
"v_readlane_b32 s43, v202, 5\n" \
"s_nop 4\n" \
"buffer_load_dwordx4 a[40:43], v201, s[8:11], s43 offen sc1\n" \
"buffer_load_dwordx4 a[44:47], v201, s[8:11], s43 offen offset:1024 sc1\n" \
"v_readlane_b32 s43, v202, 6\n" \
"s_nop 4\n" \
"buffer_load_dwordx4 a[48:51], v201, s[8:11], s43 offen sc1\n" \
"buffer_load_dwordx4 a[52:55], v201, s[8:11], s43 offen offset:1024 sc1\n" \
"v_readlane_b32 s43, v202, 7\n" \
"s_nop 4\n" \
"buffer_load_dwordx4 a[56:59], v201, s[8:11], s43 offen sc1\n" \
"buffer_load_dwordx4 a[60:63], v201, s[8:11], s43 offen offset:1024 sc1\n" \
"s_add_u32 s43, s30, s34\n" \
"s_mov_b32 s50, 0\n" \
"s_branch .LS0_w4c\n" \
".LS0_w4l:\n" \
"buffer_load_dword v240, v204, s[4:7], 0 offen sc1\n" \
"s_waitcnt vmcnt(0)\n" \
".LS0_w4c:\n" \
"v_subrev_u32 v241, s43, v240\n" \
"v_cmp_gt_u32 vcc, 0x100, v241\n" \
"s_cmp_eq_u64 vcc, -1\n" \
"s_cbranch_scc1 .LS0_w4d\n" \
"s_sleep 2\n" \
"s_add_u32 s50, s50, 1\n" \
"s_cmp_lt_u32 s50, 0x20000\n" \
"s_cbranch_scc1 .LS0_w4l\n" \
"s_branch .LS0_end\n" \
".LS0_w4d:\n" \
"v_readlane_b32 s43, v202, 8\n" \
"s_nop 4\n" \
"buffer_load_dwordx4 a[64:67], v201, s[8:11], s43 offen sc1\n" \
"buffer_load_dwordx4 a[68:71], v201, s[8:11], s43 offen offset:1024 sc1\n" \
"v_readlane_b32 s43, v202, 9\n" \
"s_nop 4\n" \
"buffer_load_dwordx4 a[72:75], v201, s[8:11], s43 offen sc1\n" \
"buffer_load_dwordx4 a[76:79], v201, s[8:11], s43 offen offset:1024 sc1\n" \
"v_readlane_b32 s43, v202, 10\n" \
"s_nop 4\n" \
"buffer_load_dwordx4 a[80:83], v201, s[8:11], s43 offen sc1\n" \
"buffer_load_dwordx4 a[84:87], v201, s[8:11], s43 offen offset:1024 sc1\n" \
"v_readlane_b32 s43, v202, 12\n" \
"s_nop 4\n" \
"buffer_load_dwordx4 a[96:99], v201, s[8:11], s43 offen sc1\n" \
"buffer_load_dwordx4 a[100:103], v201, s[8:11], s43 offen offset:1024 sc1\n" \
"v_readlane_b32 s43, v202, 14\n" \
"s_nop 4\n" \
"buffer_load_dwordx4 a[112:115], v201, s[8:11], s43 offen sc1\n" \
"buffer_load_dwordx4 a[116:119], v201, s[8:11], s43 offen offset:1024 sc1\n" \
"v_readlane_b32 s43, v202, 19\n" \
"s_nop 4\n" \
"buffer_load_dwordx4 a[152:155], v201, s[8:11], s43 offen sc1\n" \
"buffer_load_dwordx4 a[156:159], v201, s[8:11], s43 offen offset:1024 sc1\n" \
"v_readlane_b32 s43, v202, 22\n" \
"s_nop 4\n" \
"buffer_load_dwordx4 a[176:179], v201, s[8:11], s43 offen sc1\n" \
"buffer_load_dwordx4 a[180:183], v201, s[8:11], s43 offen offset:1024 sc1\n" \
"v_readlane_b32 s43, v202, 25\n" \
"s_nop 4\n" \
"buffer_load_dwordx4 a[200:203], v201, s[8:11], s43 offen sc1\n" \
"buffer_load_dwordx4 a[204:207], v201, s[8:11], s43 offen offset:1024 sc1\n" \
"s_add_u32 s43, s30, s36\n" \
"s_mov_b32 s50, 0\n" \
"s_branch .LS0_w5c\n" \
".LS0_w5l:\n" \
"buffer_load_dword v240, v204, s[4:7], 0 offen sc1\n" \
"s_waitcnt vmcnt(0)\n" \
".LS0_w5c:\n" \
"v_subrev_u32 v241, s43, v240\n" \
"v_cmp_gt_u32 vcc, 0x100, v241\n" \
"s_cmp_eq_u64 vcc, -1\n" \
"s_cbranch_scc1 .LS0_w5d\n" \
"s_sleep 2\n" \
"s_add_u32 s50, s50, 1\n" \
"s_cmp_lt_u32 s50, 0x20000\n" \
"s_cbranch_scc1 .LS0_w5l\n" \
"s_branch .LS0_end\n" \
".LS0_w5d:\n" \
"v_readlane_b32 s43, v202, 11\n" \
"s_nop 4\n" \
"buffer_load_dwordx4 a[88:91], v201, s[8:11], s43 offen sc1\n" \
"buffer_load_dwordx4 a[92:95], v201, s[8:11], s43 offen offset:1024 sc1\n" \
"v_readlane_b32 s43, v202, 13\n" \
"s_nop 4\n" \
"buffer_load_dwordx4 a[104:107], v201, s[8:11], s43 offen sc1\n" \
"buffer_load_dwordx4 a[108:111], v201, s[8:11], s43 offen offset:1024 sc1\n" \
"v_readlane_b32 s43, v202, 15\n" \
"s_nop 4\n" \
"buffer_load_dwordx4 a[120:123], v201, s[8:11], s43 offen sc1\n" \
"buffer_load_dwordx4 a[124:127], v201, s[8:11], s43 offen offset:1024 sc1\n" \
"v_readlane_b32 s43, v202, 16\n" \
"s_nop 4\n" \
"buffer_load_dwordx4 a[128:131], v201, s[8:11], s43 offen sc1\n" \
"buffer_load_dwordx4 a[132:135], v201, s[8:11], s43 offen offset:1024 sc1\n" \
"v_readlane_b32 s43, v202, 17\n" \
"s_nop 4\n" \
"buffer_load_dwordx4 a[136:139], v201, s[8:11], s43 offen sc1\n" \
"buffer_load_dwordx4 a[140:143], v201, s[8:11], s43 offen offset:1024 sc1\n" \
"v_readlane_b32 s43, v202, 20\n" \
"s_nop 4\n" \
"buffer_load_dwordx4 a[160:163], v201, s[8:11], s43 offen sc1\n" \
"buffer_load_dwordx4 a[164:167], v201, s[8:11], s43 offen offset:1024 sc1\n" \
"v_readlane_b32 s43, v202, 23\n" \
"s_nop 4\n" \
"buffer_load_dwordx4 a[184:187], v201, s[8:11], s43 offen sc1\n" \
"buffer_load_dwordx4 a[188:191], v201, s[8:11], s43 offen offset:1024 sc1\n" \
"v_readlane_b32 s43, v202, 26\n" \
"s_nop 4\n" \
"buffer_load_dwordx4 a[208:211], v201, s[8:11], s43 offen sc1\n" \
"buffer_load_dwordx4 a[212:215], v201, s[8:11], s43 offen offset:1024 sc1\n" \
"s_add_u32 s43, s30, 7\n" \
"s_mov_b32 s50, 0\n" \
"s_branch .LS0_w6c\n" \
".LS0_w6l:\n" \
"buffer_load_dword v240, v204, s[4:7], 0 offen sc1\n" \
"s_waitcnt vmcnt(0)\n" \
".LS0_w6c:\n" \
"v_subrev_u32 v241, s43, v240\n" \
"v_cmp_gt_u32 vcc, 0x100, v241\n" \
"s_cmp_eq_u64 vcc, -1\n" \
"s_cbranch_scc1 .LS0_w6d\n" \
"s_sleep 2\n" \
"s_add_u32 s50, s50, 1\n" \
"s_cmp_lt_u32 s50, 0x20000\n" \
"s_cbranch_scc1 .LS0_w6l\n" \
"s_branch .LS0_end\n" \
".LS0_w6d:\n" \
"v_readlane_b32 s43, v202, 18\n" \
"s_nop 4\n" \
"buffer_load_dwordx4 a[144:147], v201, s[8:11], s43 offen sc1\n" \
"buffer_load_dwordx4 a[148:151], v201, s[8:11], s43 offen offset:1024 sc1\n" \
"v_readlane_b32 s43, v202, 21\n" \
"s_nop 4\n" \
"buffer_load_dwordx4 a[168:171], v201, s[8:11], s43 offen sc1\n" \
"buffer_load_dwordx4 a[172:175], v201, s[8:11], s43 offen offset:1024 sc1\n" \
"v_readlane_b32 s43, v202, 24\n" \
"s_nop 4\n" \
"buffer_load_dwordx4 a[192:195], v201, s[8:11], s43 offen sc1\n" \
"buffer_load_dwordx4 a[196:199], v201, s[8:11], s43 offen offset:1024 sc1\n" \
"v_readlane_b32 s43, v202, 27\n" \
"s_nop 4\n" \
"buffer_load_dwordx4 a[216:219], v201, s[8:11], s43 offen sc1\n" \
"buffer_load_dwordx4 a[220:223], v201, s[8:11], s43 offen offset:1024 sc1\n" \
"s_mov_b32 s33, -1\n" \
"s_add_u32 s43, s30, 1\n" \
"s_mov_b32 s50, 0\n" \
".LS0_w7l:\n" \
"buffer_load_dword v240, v205, s[4:7], 0 offen sc1\n" \
"s_waitcnt vmcnt(0)\n" \
".LS0_w7c:\n" \
"v_subrev_u32 v241, s43, v240\n" \
"v_cmp_gt_u32 vcc, 0x100, v241\n" \
"s_cmp_eq_u64 vcc, -1\n" \
"s_cbranch_scc1 .LS0_w7d\n" \
"s_sleep 2\n" \
"s_add_u32 s50, s50, 1\n" \
"s_cmp_lt_u32 s50, 0x20000\n" \
"s_cbranch_scc1 .LS0_w7l\n" \
"s_branch .LS0_end\n" \
".LS0_w7d:\n" \
"s_add_u32 s43, s30, 1\n" \
"s_mov_b32 s50, 0\n" \
".LS0_w8l:\n" \
"buffer_load_dword v240, v206, s[4:7], 0 offen sc1\n" \
"s_waitcnt vmcnt(0)\n" \
".LS0_w8c:\n" \
"v_subrev_u32 v241, s43, v240\n" \
"v_cmp_gt_u32 vcc, 0x100, v241\n" \
"s_cmp_eq_u64 vcc, -1\n" \
"s_cbranch_scc1 .LS0_w8d\n" \
"s_sleep 2\n" \
"s_add_u32 s50, s50, 1\n" \
"s_cmp_lt_u32 s50, 0x20000\n" \
"s_cbranch_scc1 .LS0_w8l\n" \
"s_branch .LS0_end\n" \
".LS0_w8d:\n" \
".LS0_pre:\n" \
"s_barrier\n" \
"ds_read_b96 v[248:250], v252\n" \
"s_waitcnt lgkmcnt(0)\n" \
"v_readfirstlane_b32 s44, v248\n" \
"v_readfirstlane_b32 s45, v249\n" \
"v_readfirstlane_b32 s46, v250\n" \
"s_mul_i32 s42, s25, s46\n" \
"s_lshr_b32 s42, s42, 12\n" \
".LS0_cu:\n" \
"s_sub_u32 s43, s24, s44\n" \
"s_cmp_lt_i32 s43, 0\n" \
"s_cbranch_scc0 .LS0_cud\n" \
"s_add_u32 s24, s24, s25\n" \
"s_xor_b32 s31, s31, s29\n" \
"s_xor_b32 s32, s32, s29\n" \
"s_xor_b32 s47, s47, s29\n" \
"s_add_u32 s39, s39, 1\n" \
"s_branch .LS0_cu\n" \
".LS0_cud:\n" \
".LS0_loop:\n" \
"s_add_u32 s43, s40, 0\n" \
"s_and_b32 s43, s43, 3\n" \
"s_bitcmp1_b32 s43, 0\n" \
"s_cselect_b32 s47, s32, s31\n" \
"s_lshl_b32 s43, s43, 19\n" \
"s_add_u32 s47, s47, s43\n" \
"s_sub_u32 s43, s24, s44\n" \
"s_max_i32 s43, s43, 0xfffd0000\n" \
"s_min_i32 s43, s43, 0x4000\n" \
"s_mul_i32 s43, s43, s46\n" \
"s_ashr_i32 s43, s43, 12\n" \
"s_add_u32 s41, s45, s43\n" \
"s_branch .LS0_a1\n" \
".LS0_retry:\n" \
"s_add_u32 s51, s51, 1\n" \
"s_cmp_gt_u32 s51, 0x20000\n" \
"s_cbranch_scc1 .LS0_end\n" \
".LS0_a0:\n" \
"s_add_u32 s41, s41, s42\n" \
"s_add_u32 s24, s24, s25\n" \
"s_xor_b32 s31, s31, s29\n" \
"s_xor_b32 s32, s32, s29\n" \
"s_xor_b32 s47, s47, s29\n" \
"s_xor_b32 s48, s48, s29\n" \
"s_add_u32 s39, s39, 1\n" \
".p2align 6\n" \
".LS0_a1:\n" \
"s_memtime vcc\n" \
"s_waitcnt lgkmcnt(0)\n" \
"s_sub_u32 s43, s41, vcc_lo\n" \
"s_cmp_gt_i32 s43, 128\n" \
"s_cbranch_scc1 .LS0_a1\n" \
"s_cmp_lt_i32 s43, s37\n" \
"s_cbranch_scc1 .LS0_a0\n" \
"s_max_i32 s43, s43, 0\n" \
"s_and_b32 s43, s43, -4\n" \
"s_getpc_b64 vcc\n" \
".LS0_a3:\n" \
"s_sub_u32 s43, .LS0_a4-.LS0_a3, s43\n" \
"s_add_u32 vcc_lo, vcc_lo, s43\n" \
"s_addc_u32 vcc_hi, vcc_hi, 0\n" \
"s_setpc_b64 vcc\n" \
".rept 32\n" \
"s_nop 0\n" \
".endr\n" \
".LS0_a4:\n" \
"buffer_load_dwordx4 v[0:3], v208, s[4:7], s47 offen sc1\n" \
"s_nop 9\n" \
"buffer_load_dwordx4 v[4:7], v208, s[4:7], s47 offen offset:1024 sc1\n" \
"s_nop 9\n" \
"buffer_load_dwordx4 v[8:11], v208, s[4:7], s47 offen offset:2048 sc1\n" \
"s_nop 9\n" \
"buffer_load_dwordx4 v[12:15], v210, s[4:7], s47 offen sc1\n" \
"s_add_u32 s35, s35, 1\n" \
"v_mov_b32 v28, 0\n" \
"v_mov_b32 v29, 0\n" \
"v_mov_b32 v30, 0\n" \
"v_mov_b32 v31, 0\n" \
"v_mov_b32 v32, 0\n" \
"v_mov_b32 v33, 0\n" \
"v_mov_b32 v34, 0\n" \
"v_mov_b32 v35, 0\n" \
"v_mov_b32 v36, 0\n" \
"v_mov_b32 v37, 0\n" \
"v_mov_b32 v38, 0\n" \
"v_mov_b32 v39, 0\n" \
"v_mov_b32 v40, 0\n" \
"v_mov_b32 v41, 0\n" \
"v_mov_b32 v42, 0\n" \
"v_mov_b32 v43, 0\n" \
"v_mov_b32 v44, 0\n" \
"v_mov_b32 v45, 0\n" \
"v_mov_b32 v46, 0\n" \
"v_mov_b32 v47, 0\n" \
"v_mov_b32 v48, 0\n" \
"v_mov_b32 v49, 0\n" \
"v_mov_b32 v50, 0\n" \
"v_mov_b32 v51, 0\n" \
"v_mov_b32 v52, 0\n" \
"v_mov_b32 v53, 0\n" \
"v_mov_b32 v54, 0\n" \
"v_mov_b32 v55, 0\n" \
"v_mov_b32 v56, 0\n" \
"v_mov_b32 v57, 0\n" \
"v_mov_b32 v58, 0\n" \
"v_mov_b32 v59, 0\n" \
"s_waitcnt vmcnt(3)\n" \
"v_smfmac_f32_16x16x64_bf16 v[28:31], v[0:3], a[0:7], v200\n" \
"v_smfmac_f32_16x16x64_bf16 v[32:35], v[0:3], a[8:15], v200\n" \
"v_smfmac_f32_16x16x64_bf16 v[36:39], v[0:3], a[16:23], v200\n" \
"v_smfmac_f32_16x16x64_bf16 v[40:43], v[0:3], a[24:31], v200\n" \
"v_smfmac_f32_16x16x64_bf16 v[44:47], v[0:3], a[32:39], v200\n" \
"v_smfmac_f32_16x16x64_bf16 v[48:51], v[0:3], a[40:47], v200\n" \
"v_smfmac_f32_16x16x64_bf16 v[52:55], v[0:3], a[48:55], v200\n" \
"v_smfmac_f32_16x16x64_bf16 v[56:59], v[0:3], a[56:63], v200\n" \
"s_waitcnt vmcnt(2)\n" \
"v_smfmac_f32_16x16x64_bf16 v[28:31], v[4:7], a[64:71], v200\n" \
"v_smfmac_f32_16x16x64_bf16 v[32:35], v[4:7], a[72:79], v200\n" \
"v_smfmac_f32_16x16x64_bf16 v[36:39], v[4:7], a[80:87], v200\n" \
"s_waitcnt vmcnt(1)\n" \
"v_smfmac_f32_16x16x64_bf16 v[28:31], v[8:11], a[88:95], v200\n" \
"s_nop 2\n" \
"v_smfmac_f32_16x16x64_bf16 v[40:43], v[4:7], a[96:103], v200\n" \
"s_nop 2\n" \
"v_smfmac_f32_16x16x64_bf16 v[32:35], v[8:11], a[104:111], v200\n" \
"s_nop 0\n" \
"v_cvt_pk_bf16_f32 v128, v28, v29\n" \
"v_cvt_pk_bf16_f32 v129, v30, v31\n" \
"v_smfmac_f32_16x16x64_bf16 v[44:47], v[4:7], a[112:119], v200\n" \
"ds_write_b64 v211, v[128:129] offset:0\n" \
"s_nop 1\n" \
"v_smfmac_f32_16x16x64_bf16 v[36:39], v[8:11], a[120:127], v200\n" \
"s_nop 0\n" \
"v_cvt_pk_bf16_f32 v130, v32, v33\n" \
"v_cvt_pk_bf16_f32 v131, v34, v35\n" \
"v_smfmac_f32_16x16x64_bf16 v[44:47], v[8:11], a[128:135], v200\n" \
"ds_write_b64 v211, v[130:131] offset:384\n" \
"s_nop 1\n" \
"v_smfmac_f32_16x16x64_bf16 v[40:43], v[8:11], a[136:143], v200\n" \
"s_waitcnt vmcnt(0)\n" \
"v_cvt_pk_bf16_f32 v132, v36, v37\n" \
"v_cvt_pk_bf16_f32 v133, v38, v39\n" \
"v_smfmac_f32_16x16x64_bf16 v[44:47], v[12:15], a[144:151], v200\n" \
"ds_write_b64 v211, v[132:133] offset:2560\n" \
"s_nop 1\n" \
"v_smfmac_f32_16x16x64_bf16 v[48:51], v[4:7], a[152:159], v200\n" \
"s_nop 0\n" \
"v_cvt_pk_bf16_f32 v134, v40, v41\n" \
"v_cvt_pk_bf16_f32 v135, v42, v43\n" \
"v_smfmac_f32_16x16x64_bf16 v[48:51], v[8:11], a[160:167], v200\n" \
"ds_write_b64 v211, v[134:135] offset:4736\n" \
"v_cvt_pk_bf16_f32 v136, v44, v45\n" \
"v_cvt_pk_bf16_f32 v137, v46, v47\n" \
"v_smfmac_f32_16x16x64_bf16 v[48:51], v[12:15], a[168:175], v200\n" \
"ds_write_b64 v211, v[136:137] offset:9216\n" \
"s_nop 1\n" \
"v_smfmac_f32_16x16x64_bf16 v[52:55], v[4:7], a[176:183], v200\n" \
"s_nop 2\n" \
"v_smfmac_f32_16x16x64_bf16 v[52:55], v[8:11], a[184:191], v200\n" \
"s_nop 0\n" \
"v_cvt_pk_bf16_f32 v138, v48, v49\n" \
"v_cvt_pk_bf16_f32 v139, v50, v51\n" \
"v_smfmac_f32_16x16x64_bf16 v[52:55], v[12:15], a[192:199], v200\n" \
"ds_write_b64 v211, v[138:139] offset:9600\n" \
"s_waitcnt lgkmcnt(2)\n" \
"s_barrier\n" \
"v_smfmac_f32_16x16x64_bf16 v[56:59], v[4:7], a[200:207], v200\n" \
"s_nop 2\n" \
"v_smfmac_f32_16x16x64_bf16 v[56:59], v[8:11], a[208:215], v200\n" \
"s_nop 2\n" \
"v_smfmac_f32_16x16x64_bf16 v[56:59], v[12:15], a[216:223], v200\n" \
"v_cvt_pk_bf16_f32 v140, v52, v53\n" \
"v_cvt_pk_bf16_f32 v141, v54, v55\n" \
"s_nop 1\n" \
"ds_write_b64 v211, v[140:141] offset:11776\n" \
"s_nop 2\n" \
"v_cvt_pk_bf16_f32 v142, v56, v57\n" \
"v_cvt_pk_bf16_f32 v143, v58, v59\n" \
"ds_write_b64 v211, v[142:143] offset:13952\n" \
"s_waitcnt lgkmcnt(2)\n" \
"s_barrier\n" \
"s_waitcnt lgkmcnt(1)\n" \
"s_barrier\n" \
"s_waitcnt lgkmcnt(0)\n" \
"s_barrier\n" \
"s_nop 3\n" \
"ds_read_b32 v64, v252 offset:20\n" \
"s_waitcnt lgkmcnt(0)\n" \
"v_readfirstlane_b32 s43, v64\n" \
"s_cmp_eq_u32 s43, s35\n" \
"s_cbranch_scc1 .LS0_retry\n" \
"s_nop 15\n" \
"v_max3_u32 v64, v0, v0, v1\n" \
"v_max3_u32 v64, v64, v2, v3\n" \
"v_max3_u32 v64, v64, v4, v5\n" \
"v_max3_u32 v64, v64, v6, v7\n" \
"v_max3_u32 v64, v64, v8, v9\n" \
"v_max3_u32 v64, v64, v10, v11\n" \
"v_max3_u32 v64, v64, v12, v13\n" \
"v_max3_u32 v64, v64, v14, v15\n" \
"v_cmp_eq_u32 vcc, -1, v64\n" \
"s_cbranch_vccnz .LS0_veto\n" \
".LS0_back:\n" \
"s_waitcnt lgkmcnt(0)\n" \
"s_barrier\n" \
"ds_read_b32 v64, v252 offset:16\n" \
"s_waitcnt lgkmcnt(0)\n" \
"v_readfirstlane_b32 s43, v64\n" \
"s_cmp_eq_u32 s43, s35\n" \
"s_cbranch_scc1 .LS0_retry\n" \
"s_waitcnt lgkmcnt(0)\n" \
"s_barrier\n" \
"ds_read_b64 v[246:247], v252 offset:24\n" \
"ds_read_b96 v[248:250], v252\n" \
"s_waitcnt lgkmcnt(0)\n" \
"v_readfirstlane_b32 s44, v248\n" \
"v_readfirstlane_b32 s45, v249\n" \
"v_readfirstlane_b32 s46, v250\n" \
"v_readfirstlane_b32 s26, v246\n" \
"v_readfirstlane_b32 s27, v247\n" \
"s_mul_i32 s42, s25, s46\n" \
"s_lshr_b32 s42, s42, 12\n" \
"s_add_u32 s40, s40, 1\n" \
"s_add_u32 s24, s24, s26\n" \
"s_add_u32 s24, s24, s27\n" \
"s_cmp_eq_u32 s40, 0\n" \
"s_cbranch_scc1 .LS0_wet\n" \
"s_cmp_lt_u32 s40, 63\n" \
"s_cbranch_scc1 .LS0_loop\n" \
".LS0_end:\n" \
"s_endpgm\n" \
".LS0_veto:\n" \
"v_mov_b32 v64, s35\n" \
"ds_write_b32 v252, v64 offset:16\n" \
"s_branch .LS0_back\n" \
".p2align 6\n" \
"fk_role_S1:\n" \
"ds_read_b32 v200, v255 offset:0\n" \
"ds_read_b32 v201, v255 offset:256\n" \
"ds_read_b32 v202, v255 offset:512\n" \
"ds_read_b32 v203, v255 offset:768\n" \
"ds_read_b32 v204, v255 offset:1024\n" \
"ds_read_b32 v205, v255 offset:1280\n" \
"ds_read_b32 v206, v255 offset:1536\n" \
"ds_read_b32 v207, v255 offset:1792\n" \
"ds_read_b32 v208, v255 offset:2048\n" \
"ds_read_b32 v209, v255 offset:2304\n" \
"ds_read_b32 v210, v255 offset:2560\n" \
"ds_read_b32 v211, v255 offset:2816\n" \
"ds_read_b32 v212, v255 offset:3072\n" \
"ds_read_b32 v213, v255 offset:3328\n" \
"ds_read_b32 v214, v255 offset:3584\n" \
"ds_read_b32 v215, v255 offset:3840\n" \
"ds_read_b32 v254, v255 offset:7936\n" \
"s_waitcnt lgkmcnt(0)\n" \
"v_readlane_b32 s4, v254, 0\n" \
"v_readlane_b32 s5, v254, 1\n" \
"v_readlane_b32 s6, v254, 2\n" \
"v_readlane_b32 s7, v254, 3\n" \
"v_readlane_b32 s8, v254, 4\n" \
"v_readlane_b32 s9, v254, 5\n" \
"v_readlane_b32 s10, v254, 6\n" \
"v_readlane_b32 s11, v254, 7\n" \
"v_readlane_b32 s12, v254, 8\n" \
"v_readlane_b32 s13, v254, 9\n" \
"v_readlane_b32 s14, v254, 10\n" \
"v_readlane_b32 s15, v254, 11\n" \
"v_readlane_b32 s16, v254, 12\n" \
"v_readlane_b32 s17, v254, 13\n" \
"v_readlane_b32 s18, v254, 14\n" \
"v_readlane_b32 s19, v254, 15\n" \
"v_readlane_b32 s20, v254, 16\n" \
"v_readlane_b32 s21, v254, 17\n" \
"v_readlane_b32 s22, v254, 18\n" \
"v_readlane_b32 s23, v254, 19\n" \
"v_readlane_b32 s24, v254, 20\n" \
"v_readlane_b32 s25, v254, 21\n" \
"v_readlane_b32 s26, v254, 22\n" \
"v_readlane_b32 s27, v254, 23\n" \
"v_readlane_b32 s28, v254, 24\n" \
"v_readlane_b32 s29, v254, 25\n" \
"v_readlane_b32 s30, v254, 26\n" \
"v_readlane_b32 s31, v254, 27\n" \
"v_readlane_b32 s32, v254, 28\n" \
"v_readlane_b32 s33, v254, 29\n" \
"v_readlane_b32 s34, v254, 30\n" \
"v_readlane_b32 s35, v254, 31\n" \
"v_readlane_b32 s36, v254, 32\n" \
"v_readlane_b32 s37, v254, 33\n" \
"v_readlane_b32 s38, v254, 34\n" \
"v_readlane_b32 s39, v254, 35\n" \
"s_mov_b32 s40, 0\n" \
"s_mov_b32 s51, 0\n" \
"s_nop 0\n" \
"v_mov_b32 v252, s38\n" \
"s_nop 4\n" \
"v_mov_b32 v188, -1\n" \
"s_mov_b32 s28, s33\n" \
"s_mov_b32 s33, -1\n" \
"v_writelane_b32 v251, s6, 0\n" \
"v_writelane_b32 v251, s10, 1\n" \
"v_writelane_b32 v251, s14, 2\n" \
"v_writelane_b32 v251, s18, 3\n" \
"s_mov_b32 s6, 0\n" \
"s_mov_b32 s10, 0\n" \
"s_mov_b32 s14, 0\n" \
"s_mov_b32 s18, 0\n" \
"s_mov_b32 s40, -1\n" \
"s_branch .LS1_pre\n" \
".LS1_wet:\n" \
"s_waitcnt vmcnt(0)\n" \
"v_readlane_b32 s6, v251, 0\n" \
"v_readlane_b32 s10, v251, 1\n" \
"v_readlane_b32 s14, v251, 2\n" \
"v_readlane_b32 s18, v251, 3\n" \
"s_nop 4\n" \
"ds_read_b32 v232, v255 offset:7424\n" \
"ds_read_b32 v233, v255 offset:7680\n" \
"v_lshrrev_b32 v234, 4, v201\n" \
"v_and_b32 v234, 3, v234\n" \
"v_subrev_u32 v230, 512, v205\n" \
"s_add_u32 s43, s30, 1\n" \
"s_mov_b32 s50, 0\n" \
".LS1_w9l:\n" \
"buffer_load_dword v240, v230, s[4:7], 0 offen sc1\n" \
"s_waitcnt vmcnt(0)\n" \
".LS1_w9c:\n" \
"v_subrev_u32 v241, s43, v240\n" \
"v_cmp_gt_u32 vcc, 0x100, v241\n" \
"s_cmp_eq_u64 vcc, -1\n" \
"s_cbranch_scc1 .LS1_w9d\n" \
"s_sleep 2\n" \
"s_add_u32 s50, s50, 1\n" \
"s_cmp_lt_u32 s50, 0x20000\n" \
"s_cbranch_scc1 .LS1_w9l\n" \
"s_branch .LS1_end\n" \
".LS1_w9d:\n" \
"v_lshl_or_b32 v241, v241, 2, v234\n" \
"s_nop 1\n" \
"v_min_u32_dpp v241, v241, v241 row_ror:4 row_mask:0xf bank_mask:0xf\n" \
"s_nop 1\n" \
"v_min_u32_dpp v241, v241, v241 row_ror:8 row_mask:0xf bank_mask:0xf\n" \
"s_nop 1\n" \
"v_min_u32_dpp v241, v241, v241 quad_perm:[1,0,3,2] row_mask:0xf bank_mask:0xf\n" \
"s_nop 1\n" \
"v_min_u32_dpp v241, v241, v241 quad_perm:[2,3,0,1] row_mask:0xf bank_mask:0xf\n" \
"s_nop 0\n" \
"v_readlane_b32 s31, v241, 0\n" \
"v_readlane_b32 s43, v241, 16\n" \
"s_min_u32 s31, s31, s43\n" \
"v_readlane_b32 s43, v241, 32\n" \
"s_min_u32 s31, s31, s43\n" \
"v_readlane_b32 s43, v241, 48\n" \
"s_min_u32 s31, s31, s43\n" \
"v_subrev_u32 v230, 256, v205\n" \
"s_add_u32 s43, s30, 1\n" \
"s_mov_b32 s50, 0\n" \
".LS1_w10l:\n" \
"buffer_load_dword v240, v230, s[4:7], 0 offen sc1\n" \
"s_waitcnt vmcnt(0)\n" \
".LS1_w10c:\n" \
"v_subrev_u32 v241, s43, v240\n" \
"v_cmp_gt_u32 vcc, 0x100, v241\n" \
"s_cmp_eq_u64 vcc, -1\n" \
"s_cbranch_scc1 .LS1_w10d\n" \
"s_sleep 2\n" \
"s_add_u32 s50, s50, 1\n" \
"s_cmp_lt_u32 s50, 0x20000\n" \
"s_cbranch_scc1 .LS1_w10l\n" \
"s_branch .LS1_end\n" \
".LS1_w10d:\n" \
"v_lshl_or_b32 v241, v241, 2, v234\n" \
"s_nop 1\n" \
"v_min_u32_dpp v241, v241, v241 row_ror:4 row_mask:0xf bank_mask:0xf\n" \
"s_nop 1\n" \
"v_min_u32_dpp v241, v241, v241 row_ror:8 row_mask:0xf bank_mask:0xf\n" \
"s_nop 1\n" \
"v_min_u32_dpp v241, v241, v241 quad_perm:[1,0,3,2] row_mask:0xf bank_mask:0xf\n" \
"s_nop 1\n" \
"v_min_u32_dpp v241, v241, v241 quad_perm:[2,3,0,1] row_mask:0xf bank_mask:0xf\n" \
"s_nop 0\n" \
"v_readlane_b32 s32, v241, 0\n" \
"v_readlane_b32 s43, v241, 16\n" \
"s_min_u32 s32, s32, s43\n" \
"v_readlane_b32 s43, v241, 32\n" \
"s_min_u32 s32, s32, s43\n" \
"v_readlane_b32 s43, v241, 48\n" \
"s_min_u32 s32, s32, s43\n" \
"s_xor_b32 s29, s31, s32\n" \
"s_and_b32 s29, s29, 3\n" \
"s_lshl_b32 s29, s29, 13\n" \
"s_bitset1_b32 s29, 21\n" \
"s_and_b32 s31, s31, 3\n" \
"s_lshl_b32 s31, s31, 13\n" \
"s_bitcmp1_b32 s39, 0\n" \
"s_cselect_b32 s43, s29, 0\n" \
"s_xor_b32 s31, s31, s43\n" \
"s_xor_b32 s32, s31, 0x4000\n" \
"s_mov_b32 s39, 0\n" \
"v_mov_b32 v224, -1\n" \
"v_mov_b32 v225, -1\n" \
"v_mov_b32 v226, -1\n" \
"v_mov_b32 v227, -1\n" \
"s_waitcnt lgkmcnt(0)\n" \
"s_add_u32 s50, s31, 0\n" \
"buffer_store_dwordx4 v[224:227], v232, s[4:7], s50 offen sc1\n" \
"s_xor_b32 s43, s50, s29\n" \
"buffer_store_dwordx4 v[224:227], v232, s[4:7], s43 offen sc1\n" \
"s_add_u32 s50, s32, 524288\n" \
"buffer_store_dwordx4 v[224:227], v232, s[4:7], s50 offen sc1\n" \
"s_xor_b32 s43, s50, s29\n" \
"buffer_store_dwordx4 v[224:227], v232, s[4:7], s43 offen sc1\n" \
"s_add_u32 s50, s31, 1048576\n" \
"buffer_store_dwordx4 v[224:227], v232, s[4:7], s50 offen sc1\n" \
"s_xor_b32 s43, s50, s29\n" \
"buffer_store_dwordx4 v[224:227], v232, s[4:7], s43 offen sc1\n" \
"s_add_u32 s50, s32, 1572864\n" \
"buffer_store_dwordx4 v[224:227], v232, s[4:7], s50 offen sc1\n" \
"s_xor_b32 s43, s50, s29\n" \
"buffer_store_dwordx4 v[224:227], v232, s[4:7], s43 offen sc1\n" \
"s_waitcnt vmcnt(0)\n" \
"s_barrier\n" \
"s_add_u32 s43, s30, s40\n" \
"s_add_u32 s43, s43, 1\n" \
"v_mov_b32 v241, s43\n" \
"buffer_store_dword v241, v233, s[4:7], 0 offen sc1\n" \
"s_mov_b32 s33, s28\n" \
"s_add_u32 s34, s33, 2\n" \
"s_add_u32 s36, s33, 4\n" \
"s_add_u32 s43, s30, s33\n" \
"s_mov_b32 s50, 0\n" \
".LS1_w11l:\n" \
"buffer_load_dword v240, v204, s[4:7], 0 offen sc1\n" \
"s_waitcnt vmcnt(0)\n" \
".LS1_w11c:\n" \
"v_subrev_u32 v241, s43, v240\n" \
"v_cmp_gt_u32 vcc, 0x100, v241\n" \
"s_cmp_eq_u64 vcc, -1\n" \
"s_cbranch_scc1 .LS1_w11d\n" \
"s_sleep 2\n" \
"s_add_u32 s50, s50, 1\n" \
"s_cmp_lt_u32 s50, 0x20000\n" \
"s_cbranch_scc1 .LS1_w11l\n" \
"s_branch .LS1_end\n" \
".LS1_w11d:\n" \
"v_readlane_b32 s43, v202, 11\n" \
"s_nop 4\n" \
"buffer_load_dwordx4 a[88:91], v201, s[8:11], s43 offen sc1\n" \
"buffer_load_dwordx4 a[92:95], v201, s[8:11], s43 offen offset:1024 sc1\n" \
"v_readlane_b32 s43, v202, 13\n" \
"s_nop 4\n" \
"buffer_load_dwordx4 a[104:107], v201, s[8:11], s43 offen sc1\n" \
"buffer_load_dwordx4 a[108:111], v201, s[8:11], s43 offen offset:1024 sc1\n" \
"v_readlane_b32 s43, v202, 15\n" \
"s_nop 4\n" \
"buffer_load_dwordx4 a[120:123], v201, s[8:11], s43 offen sc1\n" \
"buffer_load_dwordx4 a[124:127], v201, s[8:11], s43 offen offset:1024 sc1\n" \
"v_readlane_b32 s43, v202, 16\n" \
"s_nop 4\n" \
"buffer_load_dwordx4 a[128:131], v201, s[8:11], s43 offen sc1\n" \
"buffer_load_dwordx4 a[132:135], v201, s[8:11], s43 offen offset:1024 sc1\n" \
"v_readlane_b32 s43, v202, 17\n" \
"s_nop 4\n" \
"buffer_load_dwordx4 a[136:139], v201, s[8:11], s43 offen sc1\n" \
"buffer_load_dwordx4 a[140:143], v201, s[8:11], s43 offen offset:1024 sc1\n" \
"v_readlane_b32 s43, v202, 20\n" \
"s_nop 4\n" \
"buffer_load_dwordx4 a[160:163], v201, s[8:11], s43 offen sc1\n" \
"buffer_load_dwordx4 a[164:167], v201, s[8:11], s43 offen offset:1024 sc1\n" \
"v_readlane_b32 s43, v202, 23\n" \
"s_nop 4\n" \
"buffer_load_dwordx4 a[184:187], v201, s[8:11], s43 offen sc1\n" \
"buffer_load_dwordx4 a[188:191], v201, s[8:11], s43 offen offset:1024 sc1\n" \
"v_readlane_b32 s43, v202, 26\n" \
"s_nop 4\n" \
"buffer_load_dwordx4 a[208:211], v201, s[8:11], s43 offen sc1\n" \
"buffer_load_dwordx4 a[212:215], v201, s[8:11], s43 offen offset:1024 sc1\n" \
"s_add_u32 s43, s30, s34\n" \
"s_mov_b32 s50, 0\n" \
"s_branch .LS1_w12c\n" \
".LS1_w12l:\n" \
"buffer_load_dword v240, v204, s[4:7], 0 offen sc1\n" \
"s_waitcnt vmcnt(0)\n" \
".LS1_w12c:\n" \
"v_subrev_u32 v241, s43, v240\n" \
"v_cmp_gt_u32 vcc, 0x100, v241\n" \
"s_cmp_eq_u64 vcc, -1\n" \
"s_cbranch_scc1 .LS1_w12d\n" \
"s_sleep 2\n" \
"s_add_u32 s50, s50, 1\n" \
"s_cmp_lt_u32 s50, 0x20000\n" \
"s_cbranch_scc1 .LS1_w12l\n" \
"s_branch .LS1_end\n" \
".LS1_w12d:\n" \
"v_readlane_b32 s43, v202, 0\n" \
"s_nop 4\n" \
"buffer_load_dwordx4 a[0:3], v201, s[8:11], s43 offen sc1\n" \
"buffer_load_dwordx4 a[4:7], v201, s[8:11], s43 offen offset:1024 sc1\n" \
"v_readlane_b32 s43, v202, 1\n" \
"s_nop 4\n" \
"buffer_load_dwordx4 a[8:11], v201, s[8:11], s43 offen sc1\n" \
"buffer_load_dwordx4 a[12:15], v201, s[8:11], s43 offen offset:1024 sc1\n" \
"v_readlane_b32 s43, v202, 2\n" \
"s_nop 4\n" \
"buffer_load_dwordx4 a[16:19], v201, s[8:11], s43 offen sc1\n" \
"buffer_load_dwordx4 a[20:23], v201, s[8:11], s43 offen offset:1024 sc1\n" \
"v_readlane_b32 s43, v202, 3\n" \
"s_nop 4\n" \
"buffer_load_dwordx4 a[24:27], v201, s[8:11], s43 offen sc1\n" \
"buffer_load_dwordx4 a[28:31], v201, s[8:11], s43 offen offset:1024 sc1\n" \
"v_readlane_b32 s43, v202, 4\n" \
"s_nop 4\n" \
"buffer_load_dwordx4 a[32:35], v201, s[8:11], s43 offen sc1\n" \
"buffer_load_dwordx4 a[36:39], v201, s[8:11], s43 offen offset:1024 sc1\n" \
"v_readlane_b32 s43, v202, 5\n" \
"s_nop 4\n" \
"buffer_load_dwordx4 a[40:43], v201, s[8:11], s43 offen sc1\n" \
"buffer_load_dwordx4 a[44:47], v201, s[8:11], s43 offen offset:1024 sc1\n" \
"v_readlane_b32 s43, v202, 6\n" \
"s_nop 4\n" \
"buffer_load_dwordx4 a[48:51], v201, s[8:11], s43 offen sc1\n" \
"buffer_load_dwordx4 a[52:55], v201, s[8:11], s43 offen offset:1024 sc1\n" \
"v_readlane_b32 s43, v202, 7\n" \
"s_nop 4\n" \
"buffer_load_dwordx4 a[56:59], v201, s[8:11], s43 offen sc1\n" \
"buffer_load_dwordx4 a[60:63], v201, s[8:11], s43 offen offset:1024 sc1\n" \
"s_add_u32 s43, s30, s36\n" \
"s_mov_b32 s50, 0\n" \
"s_branch .LS1_w13c\n" \
".LS1_w13l:\n" \
"buffer_load_dword v240, v204, s[4:7], 0 offen sc1\n" \
"s_waitcnt vmcnt(0)\n" \
".LS1_w13c:\n" \
"v_subrev_u32 v241, s43, v240\n" \
"v_cmp_gt_u32 vcc, 0x100, v241\n" \
"s_cmp_eq_u64 vcc, -1\n" \
"s_cbranch_scc1 .LS1_w13d\n" \
"s_sleep 2\n" \
"s_add_u32 s50, s50, 1\n" \
"s_cmp_lt_u32 s50, 0x20000\n" \
"s_cbranch_scc1 .LS1_w13l\n" \
"s_branch .LS1_end\n" \
".LS1_w13d:\n" \
"v_readlane_b32 s43, v202, 8\n" \
"s_nop 4\n" \
"buffer_load_dwordx4 a[64:67], v201, s[8:11], s43 offen sc1\n" \
"buffer_load_dwordx4 a[68:71], v201, s[8:11], s43 offen offset:1024 sc1\n" \
"v_readlane_b32 s43, v202, 9\n" \
"s_nop 4\n" \
"buffer_load_dwordx4 a[72:75], v201, s[8:11], s43 offen sc1\n" \
"buffer_load_dwordx4 a[76:79], v201, s[8:11], s43 offen offset:1024 sc1\n" \
"v_readlane_b32 s43, v202, 10\n" \
"s_nop 4\n" \
"buffer_load_dwordx4 a[80:83], v201, s[8:11], s43 offen sc1\n" \
"buffer_load_dwordx4 a[84:87], v201, s[8:11], s43 offen offset:1024 sc1\n" \
"v_readlane_b32 s43, v202, 12\n" \
"s_nop 4\n" \
"buffer_load_dwordx4 a[96:99], v201, s[8:11], s43 offen sc1\n" \
"buffer_load_dwordx4 a[100:103], v201, s[8:11], s43 offen offset:1024 sc1\n" \
"v_readlane_b32 s43, v202, 14\n" \
"s_nop 4\n" \
"buffer_load_dwordx4 a[112:115], v201, s[8:11], s43 offen sc1\n" \
"buffer_load_dwordx4 a[116:119], v201, s[8:11], s43 offen offset:1024 sc1\n" \
"v_readlane_b32 s43, v202, 19\n" \
"s_nop 4\n" \
"buffer_load_dwordx4 a[152:155], v201, s[8:11], s43 offen sc1\n" \
"buffer_load_dwordx4 a[156:159], v201, s[8:11], s43 offen offset:1024 sc1\n" \
"v_readlane_b32 s43, v202, 22\n" \
"s_nop 4\n" \
"buffer_load_dwordx4 a[176:179], v201, s[8:11], s43 offen sc1\n" \
"buffer_load_dwordx4 a[180:183], v201, s[8:11], s43 offen offset:1024 sc1\n" \
"v_readlane_b32 s43, v202, 25\n" \
"s_nop 4\n" \
"buffer_load_dwordx4 a[200:203], v201, s[8:11], s43 offen sc1\n" \
"buffer_load_dwordx4 a[204:207], v201, s[8:11], s43 offen offset:1024 sc1\n" \
"s_add_u32 s43, s30, 7\n" \
"s_mov_b32 s50, 0\n" \
"s_branch .LS1_w14c\n" \
".LS1_w14l:\n" \
"buffer_load_dword v240, v204, s[4:7], 0 offen sc1\n" \
"s_waitcnt vmcnt(0)\n" \
".LS1_w14c:\n" \
"v_subrev_u32 v241, s43, v240\n" \
"v_cmp_gt_u32 vcc, 0x100, v241\n" \
"s_cmp_eq_u64 vcc, -1\n" \
"s_cbranch_scc1 .LS1_w14d\n" \
"s_sleep 2\n" \
"s_add_u32 s50, s50, 1\n" \
"s_cmp_lt_u32 s50, 0x20000\n" \
"s_cbranch_scc1 .LS1_w14l\n" \
"s_branch .LS1_end\n" \
".LS1_w14d:\n" \
"v_readlane_b32 s43, v202, 18\n" \
"s_nop 4\n" \
"buffer_load_dwordx4 a[144:147], v201, s[8:11], s43 offen sc1\n" \
"buffer_load_dwordx4 a[148:151], v201, s[8:11], s43 offen offset:1024 sc1\n" \
"v_readlane_b32 s43, v202, 21\n" \
"s_nop 4\n" \
"buffer_load_dwordx4 a[168:171], v201, s[8:11], s43 offen sc1\n" \
"buffer_load_dwordx4 a[172:175], v201, s[8:11], s43 offen offset:1024 sc1\n" \
"v_readlane_b32 s43, v202, 24\n" \
"s_nop 4\n" \
"buffer_load_dwordx4 a[192:195], v201, s[8:11], s43 offen sc1\n" \
"buffer_load_dwordx4 a[196:199], v201, s[8:11], s43 offen offset:1024 sc1\n" \
"v_readlane_b32 s43, v202, 27\n" \
"s_nop 4\n" \
"buffer_load_dwordx4 a[216:219], v201, s[8:11], s43 offen sc1\n" \
"buffer_load_dwordx4 a[220:223], v201, s[8:11], s43 offen offset:1024 sc1\n" \
"s_mov_b32 s33, -1\n" \
"s_add_u32 s43, s30, 1\n" \
"s_mov_b32 s50, 0\n" \
".LS1_w15l:\n" \
"buffer_load_dword v240, v205, s[4:7], 0 offen sc1\n" \
"s_waitcnt vmcnt(0)\n" \
".LS1_w15c:\n" \
"v_subrev_u32 v241, s43, v240\n" \
"v_cmp_gt_u32 vcc, 0x100, v241\n" \
"s_cmp_eq_u64 vcc, -1\n" \
"s_cbranch_scc1 .LS1_w15d\n" \
"s_sleep 2\n" \
"s_add_u32 s50, s50, 1\n" \
"s_cmp_lt_u32 s50, 0x20000\n" \
"s_cbranch_scc1 .LS1_w15l\n" \
"s_branch .LS1_end\n" \
".LS1_w15d:\n" \
"s_add_u32 s43, s30, 1\n" \
"s_mov_b32 s50, 0\n" \
".LS1_w16l:\n" \
"buffer_load_dword v240, v206, s[4:7], 0 offen sc1\n" \
"s_waitcnt vmcnt(0)\n" \
".LS1_w16c:\n" \
"v_subrev_u32 v241, s43, v240\n" \
"v_cmp_gt_u32 vcc, 0x100, v241\n" \
"s_cmp_eq_u64 vcc, -1\n" \
"s_cbranch_scc1 .LS1_w16d\n" \
"s_sleep 2\n" \
"s_add_u32 s50, s50, 1\n" \
"s_cmp_lt_u32 s50, 0x20000\n" \
"s_cbranch_scc1 .LS1_w16l\n" \
"s_branch .LS1_end\n" \
".LS1_w16d:\n" \
".LS1_pre:\n" \
"s_barrier\n" \
"ds_read_b96 v[248:250], v252\n" \
"s_waitcnt lgkmcnt(0)\n" \
"v_readfirstlane_b32 s44, v248\n" \
"v_readfirstlane_b32 s45, v249\n" \
"v_readfirstlane_b32 s46, v250\n" \
"s_mul_i32 s42, s25, s46\n" \
"s_lshr_b32 s42, s42, 12\n" \
".LS1_cu:\n" \
"s_sub_u32 s43, s24, s44\n" \
"s_cmp_lt_i32 s43, 0\n" \
"s_cbranch_scc0 .LS1_cud\n" \
"s_add_u32 s24, s24, s25\n" \
"s_xor_b32 s31, s31, s29\n" \
"s_xor_b32 s32, s32, s29\n" \
"s_xor_b32 s47, s47, s29\n" \
"s_add_u32 s39, s39, 1\n" \
"s_branch .LS1_cu\n" \
".LS1_cud:\n" \
".LS1_loop:\n" \
"s_add_u32 s43, s40, 0\n" \
"s_and_b32 s43, s43, 3\n" \
"s_bitcmp1_b32 s43, 0\n" \
"s_cselect_b32 s47, s32, s31\n" \
"s_lshl_b32 s43, s43, 19\n" \
"s_add_u32 s47, s47, s43\n" \
"s_sub_u32 s43, s24, s44\n" \
"s_max_i32 s43, s43, 0xfffd0000\n" \
"s_min_i32 s43, s43, 0x4000\n" \
"s_mul_i32 s43, s43, s46\n" \
"s_ashr_i32 s43, s43, 12\n" \
"s_add_u32 s41, s45, s43\n" \
"s_add_u32 s41, s41, 4\n" \
"s_branch .LS1_a1\n" \
".LS1_retry:\n" \
"s_add_u32 s51, s51, 1\n" \
"s_cmp_gt_u32 s51, 0x20000\n" \
"s_cbranch_scc1 .LS1_end\n" \
".LS1_a0:\n" \
"s_add_u32 s41, s41, s42\n" \
"s_add_u32 s24, s24, s25\n" \
"s_xor_b32 s31, s31, s29\n" \
"s_xor_b32 s32, s32, s29\n" \
"s_xor_b32 s47, s47, s29\n" \
"s_xor_b32 s48, s48, s29\n" \
"s_add_u32 s39, s39, 1\n" \
".p2align 6\n" \
".LS1_a1:\n" \
"s_memtime vcc\n" \
"s_waitcnt lgkmcnt(0)\n" \
"s_sub_u32 s43, s41, vcc_lo\n" \
"s_cmp_gt_i32 s43, 128\n" \
"s_cbranch_scc1 .LS1_a1\n" \
"s_cmp_lt_i32 s43, s37\n" \
"s_cbranch_scc1 .LS1_a0\n" \
"s_max_i32 s43, s43, 0\n" \
"s_and_b32 s43, s43, -4\n" \
"s_getpc_b64 vcc\n" \
".LS1_a3:\n" \
"s_sub_u32 s43, .LS1_a4-.LS1_a3, s43\n" \
"s_add_u32 vcc_lo, vcc_lo, s43\n" \
"s_addc_u32 vcc_hi, vcc_hi, 0\n" \
"s_setpc_b64 vcc\n" \
".rept 32\n" \
"s_nop 0\n" \
".endr\n" \
".LS1_a4:\n" \
"buffer_load_dwordx4 v[0:3], v208, s[4:7], s47 offen offset:1024 sc1\n" \
"s_nop 9\n" \
"buffer_load_dwordx4 v[4:7], v208, s[4:7], s47 offen offset:2048 sc1\n" \
"s_nop 9\n" \
"buffer_load_dwordx4 v[8:11], v208, s[4:7], s47 offen sc1\n" \
"s_nop 9\n" \
"buffer_load_dwordx4 v[12:15], v210, s[4:7], s47 offen sc1\n" \
"s_add_u32 s35, s35, 1\n" \
"v_mov_b32 v28, 0\n" \
"v_mov_b32 v29, 0\n" \
"v_mov_b32 v30, 0\n" \
"v_mov_b32 v31, 0\n" \
"v_mov_b32 v32, 0\n" \
"v_mov_b32 v33, 0\n" \
"v_mov_b32 v34, 0\n" \
"v_mov_b32 v35, 0\n" \
"v_mov_b32 v36, 0\n" \
"v_mov_b32 v37, 0\n" \
"v_mov_b32 v38, 0\n" \
"v_mov_b32 v39, 0\n" \
"v_mov_b32 v40, 0\n" \
"v_mov_b32 v41, 0\n" \
"v_mov_b32 v42, 0\n" \
"v_mov_b32 v43, 0\n" \
"v_mov_b32 v44, 0\n" \
"v_mov_b32 v45, 0\n" \
"v_mov_b32 v46, 0\n" \
"v_mov_b32 v47, 0\n" \
"v_mov_b32 v48, 0\n" \
"v_mov_b32 v49, 0\n" \
"v_mov_b32 v50, 0\n" \
"v_mov_b32 v51, 0\n" \
"v_mov_b32 v52, 0\n" \
"v_mov_b32 v53, 0\n" \
"v_mov_b32 v54, 0\n" \
"v_mov_b32 v55, 0\n" \
"v_mov_b32 v56, 0\n" \
"v_mov_b32 v57, 0\n" \
"v_mov_b32 v58, 0\n" \
"v_mov_b32 v59, 0\n" \
"s_waitcnt vmcnt(3)\n" \
"v_smfmac_f32_16x16x64_bf16 v[28:31], v[0:3], a[0:7], v200\n" \
"v_smfmac_f32_16x16x64_bf16 v[32:35], v[0:3], a[8:15], v200\n" \
"v_smfmac_f32_16x16x64_bf16 v[36:39], v[0:3], a[16:23], v200\n" \
"v_smfmac_f32_16x16x64_bf16 v[40:43], v[0:3], a[24:31], v200\n" \
"v_smfmac_f32_16x16x64_bf16 v[44:47], v[0:3], a[32:39], v200\n" \
"v_smfmac_f32_16x16x64_bf16 v[48:51], v[0:3], a[40:47], v200\n" \
"v_smfmac_f32_16x16x64_bf16 v[52:55], v[0:3], a[48:55], v200\n" \
"v_smfmac_f32_16x16x64_bf16 v[56:59], v[0:3], a[56:63], v200\n" \
"s_waitcnt vmcnt(2)\n" \
"v_smfmac_f32_16x16x64_bf16 v[28:31], v[4:7], a[64:71], v200\n" \
"v_smfmac_f32_16x16x64_bf16 v[32:35], v[4:7], a[72:79], v200\n" \
"v_smfmac_f32_16x16x64_bf16 v[36:39], v[4:7], a[80:87], v200\n" \
"s_waitcnt vmcnt(1)\n" \
"v_smfmac_f32_16x16x64_bf16 v[28:31], v[8:11], a[88:95], v200\n" \
"s_nop 2\n" \
"v_smfmac_f32_16x16x64_bf16 v[40:43], v[4:7], a[96:103], v200\n" \
"s_nop 2\n" \
"v_smfmac_f32_16x16x64_bf16 v[32:35], v[8:11], a[104:111], v200\n" \
"s_nop 0\n" \
"v_cvt_pk_bf16_f32 v128, v28, v29\n" \
"v_cvt_pk_bf16_f32 v129, v30, v31\n" \
"v_smfmac_f32_16x16x64_bf16 v[44:47], v[4:7], a[112:119], v200\n" \
"ds_write_b64 v211, v[128:129] offset:128\n" \
"s_nop 1\n" \
"v_smfmac_f32_16x16x64_bf16 v[36:39], v[8:11], a[120:127], v200\n" \
"s_nop 0\n" \
"v_cvt_pk_bf16_f32 v130, v32, v33\n" \
"v_cvt_pk_bf16_f32 v131, v34, v35\n" \
"v_smfmac_f32_16x16x64_bf16 v[44:47], v[8:11], a[128:135], v200\n" \
"ds_write_b64 v211, v[130:131] offset:2304\n" \
"s_nop 1\n" \
"v_smfmac_f32_16x16x64_bf16 v[40:43], v[8:11], a[136:143], v200\n" \
"s_waitcnt vmcnt(0)\n" \
"v_cvt_pk_bf16_f32 v132, v36, v37\n" \
"v_cvt_pk_bf16_f32 v133, v38, v39\n" \
"v_smfmac_f32_16x16x64_bf16 v[44:47], v[12:15], a[144:151], v200\n" \
"ds_write_b64 v211, v[132:133] offset:2688\n" \
"s_nop 1\n" \
"v_smfmac_f32_16x16x64_bf16 v[48:51], v[4:7], a[152:159], v200\n" \
"s_nop 0\n" \
"v_cvt_pk_bf16_f32 v134, v40, v41\n" \
"v_cvt_pk_bf16_f32 v135, v42, v43\n" \
"v_smfmac_f32_16x16x64_bf16 v[48:51], v[8:11], a[160:167], v200\n" \
"ds_write_b64 v211, v[134:135] offset:4864\n" \
"v_cvt_pk_bf16_f32 v136, v44, v45\n" \
"v_cvt_pk_bf16_f32 v137, v46, v47\n" \
"v_smfmac_f32_16x16x64_bf16 v[48:51], v[12:15], a[168:175], v200\n" \
"ds_write_b64 v211, v[136:137] offset:9344\n" \
"s_nop 1\n" \
"v_smfmac_f32_16x16x64_bf16 v[52:55], v[4:7], a[176:183], v200\n" \
"s_nop 2\n" \
"v_smfmac_f32_16x16x64_bf16 v[52:55], v[8:11], a[184:191], v200\n" \
"s_nop 0\n" \
"v_cvt_pk_bf16_f32 v138, v48, v49\n" \
"v_cvt_pk_bf16_f32 v139, v50, v51\n" \
"v_smfmac_f32_16x16x64_bf16 v[52:55], v[12:15], a[192:199], v200\n" \
"ds_write_b64 v211, v[138:139] offset:11520\n" \
"s_waitcnt lgkmcnt(2)\n" \
"s_barrier\n" \
"v_smfmac_f32_16x16x64_bf16 v[56:59], v[4:7], a[200:207], v200\n" \
"s_nop 2\n" \
"v_smfmac_f32_16x16x64_bf16 v[56:59], v[8:11], a[208:215], v200\n" \
"s_nop 2\n" \
"v_smfmac_f32_16x16x64_bf16 v[56:59], v[12:15], a[216:223], v200\n" \
"v_cvt_pk_bf16_f32 v140, v52, v53\n" \
"v_cvt_pk_bf16_f32 v141, v54, v55\n" \
"s_nop 1\n" \
"ds_write_b64 v211, v[140:141] offset:11904\n" \
"s_nop 2\n" \
"v_cvt_pk_bf16_f32 v142, v56, v57\n" \
"v_cvt_pk_bf16_f32 v143, v58, v59\n" \
"ds_write_b64 v211, v[142:143] offset:14080\n" \
"s_waitcnt lgkmcnt(2)\n" \
"s_barrier\n" \
"s_waitcnt lgkmcnt(1)\n" \
"s_barrier\n" \
"s_waitcnt lgkmcnt(0)\n" \
"s_barrier\n" \
"s_nop 3\n" \
"ds_read_b32 v64, v252 offset:20\n" \
"s_waitcnt lgkmcnt(0)\n" \
"v_readfirstlane_b32 s43, v64\n" \
"s_cmp_eq_u32 s43, s35\n" \
"s_cbranch_scc1 .LS1_retry\n" \
"s_nop 15\n" \
"v_max3_u32 v64, v0, v0, v1\n" \
"v_max3_u32 v64, v64, v2, v3\n" \
"v_max3_u32 v64, v64, v4, v5\n" \
"v_max3_u32 v64, v64, v6, v7\n" \
"v_max3_u32 v64, v64, v8, v9\n" \
"v_max3_u32 v64, v64, v10, v11\n" \
"v_max3_u32 v64, v64, v12, v13\n" \
"v_max3_u32 v64, v64, v14, v15\n" \
"v_cmp_eq_u32 vcc, -1, v64\n" \
"s_cbranch_vccnz .LS1_veto\n" \
".LS1_back:\n" \
"s_waitcnt lgkmcnt(0)\n" \
"s_barrier\n" \
"ds_read_b32 v64, v252 offset:16\n" \
"s_waitcnt lgkmcnt(0)\n" \
"v_readfirstlane_b32 s43, v64\n" \
"s_cmp_eq_u32 s43, s35\n" \
"s_cbranch_scc1 .LS1_retry\n" \
"s_waitcnt lgkmcnt(0)\n" \
"s_barrier\n" \
"ds_read_b64 v[246:247], v252 offset:24\n" \
"ds_read_b96 v[248:250], v252\n" \
"s_waitcnt lgkmcnt(0)\n" \
"v_readfirstlane_b32 s44, v248\n" \
"v_readfirstlane_b32 s45, v249\n" \
"v_readfirstlane_b32 s46, v250\n" \
"v_readfirstlane_b32 s26, v246\n" \
"v_readfirstlane_b32 s27, v247\n" \
"s_mul_i32 s42, s25, s46\n" \
"s_lshr_b32 s42, s42, 12\n" \
"s_add_u32 s40, s40, 1\n" \
"s_add_u32 s24, s24, s26\n" \
"s_add_u32 s24, s24, s27\n" \
"s_cmp_eq_u32 s40, 0\n" \
"s_cbranch_scc1 .LS1_wet\n" \
"s_cmp_lt_u32 s40, 63\n" \
"s_cbranch_scc1 .LS1_loop\n" \
".LS1_end:\n" \
"s_endpgm\n" \
".LS1_veto:\n" \
"v_mov_b32 v64, s35\n" \
"ds_write_b32 v252, v64 offset:16\n" \
"s_branch .LS1_back\n" \
".p2align 6\n" \
"fk_role_S2:\n" \
"ds_read_b32 v200, v255 offset:0\n" \
"ds_read_b32 v201, v255 offset:256\n" \
"ds_read_b32 v202, v255 offset:512\n" \
"ds_read_b32 v203, v255 offset:768\n" \
"ds_read_b32 v204, v255 offset:1024\n" \
"ds_read_b32 v205, v255 offset:1280\n" \
"ds_read_b32 v206, v255 offset:1536\n" \
"ds_read_b32 v207, v255 offset:1792\n" \
"ds_read_b32 v208, v255 offset:2048\n" \
"ds_read_b32 v209, v255 offset:2304\n" \
"ds_read_b32 v210, v255 offset:2560\n" \
"ds_read_b32 v211, v255 offset:2816\n" \
"ds_read_b32 v212, v255 offset:3072\n" \
"ds_read_b32 v213, v255 offset:3328\n" \
"ds_read_b32 v214, v255 offset:3584\n" \
"ds_read_b32 v215, v255 offset:3840\n" \
"ds_read_b32 v254, v255 offset:7936\n" \
"s_waitcnt lgkmcnt(0)\n" \
"v_readlane_b32 s4, v254, 0\n" \
"v_readlane_b32 s5, v254, 1\n" \
"v_readlane_b32 s6, v254, 2\n" \
"v_readlane_b32 s7, v254, 3\n" \
"v_readlane_b32 s8, v254, 4\n" \
"v_readlane_b32 s9, v254, 5\n" \
"v_readlane_b32 s10, v254, 6\n" \
"v_readlane_b32 s11, v254, 7\n" \
"v_readlane_b32 s12, v254, 8\n" \
"v_readlane_b32 s13, v254, 9\n" \
"v_readlane_b32 s14, v254, 10\n" \
"v_readlane_b32 s15, v254, 11\n" \
"v_readlane_b32 s16, v254, 12\n" \
"v_readlane_b32 s17, v254, 13\n" \
"v_readlane_b32 s18, v254, 14\n" \
"v_readlane_b32 s19, v254, 15\n" \
"v_readlane_b32 s20, v254, 16\n" \
"v_readlane_b32 s21, v254, 17\n" \
"v_readlane_b32 s22, v254, 18\n" \
"v_readlane_b32 s23, v254, 19\n" \
"v_readlane_b32 s24, v254, 20\n" \
"v_readlane_b32 s25, v254, 21\n" \
"v_readlane_b32 s26, v254, 22\n" \
"v_readlane_b32 s27, v254, 23\n" \
"v_readlane_b32 s28, v254, 24\n" \
"v_readlane_b32 s29, v254, 25\n" \
"v_readlane_b32 s30, v254, 26\n" \
"v_readlane_b32 s31, v254, 27\n" \
"v_readlane_b32 s32, v254, 28\n" \
"v_readlane_b32 s33, v254, 29\n" \
"v_readlane_b32 s34, v254, 30\n" \
"v_readlane_b32 s35, v254, 31\n" \
"v_readlane_b32 s36, v254, 32\n" \
"v_readlane_b32 s37, v254, 33\n" \
"v_readlane_b32 s38, v254, 34\n" \
"v_readlane_b32 s39, v254, 35\n" \
"s_mov_b32 s40, 0\n" \
"s_mov_b32 s51, 0\n" \
"s_nop 0\n" \
"v_mov_b32 v252, s38\n" \
"s_nop 4\n" \
"v_mov_b32 v188, -1\n" \
"s_mov_b32 s28, s33\n" \
"s_mov_b32 s33, -1\n" \
"v_writelane_b32 v251, s6, 0\n" \
"v_writelane_b32 v251, s10, 1\n" \
"v_writelane_b32 v251, s14, 2\n" \
"v_writelane_b32 v251, s18, 3\n" \
"s_mov_b32 s6, 0\n" \
"s_mov_b32 s10, 0\n" \
"s_mov_b32 s14, 0\n" \
"s_mov_b32 s18, 0\n" \
"s_mov_b32 s40, -1\n" \
"s_branch .LS2_pre\n" \
".LS2_wet:\n" \
"s_waitcnt vmcnt(0)\n" \
"v_readlane_b32 s6, v251, 0\n" \
"v_readlane_b32 s10, v251, 1\n" \
"v_readlane_b32 s14, v251, 2\n" \
"v_readlane_b32 s18, v251, 3\n" \
"s_nop 4\n" \
"ds_read_b32 v232, v255 offset:7424\n" \
"ds_read_b32 v233, v255 offset:7680\n" \
"v_lshrrev_b32 v234, 4, v201\n" \
"v_and_b32 v234, 3, v234\n" \
"v_subrev_u32 v230, 512, v205\n" \
"s_add_u32 s43, s30, 1\n" \
"s_mov_b32 s50, 0\n" \
".LS2_w17l:\n" \
"buffer_load_dword v240, v230, s[4:7], 0 offen sc1\n" \
"s_waitcnt vmcnt(0)\n" \
".LS2_w17c:\n" \
"v_subrev_u32 v241, s43, v240\n" \
"v_cmp_gt_u32 vcc, 0x100, v241\n" \
"s_cmp_eq_u64 vcc, -1\n" \
"s_cbranch_scc1 .LS2_w17d\n" \
"s_sleep 2\n" \
"s_add_u32 s50, s50, 1\n" \
"s_cmp_lt_u32 s50, 0x20000\n" \
"s_cbranch_scc1 .LS2_w17l\n" \
"s_branch .LS2_end\n" \
".LS2_w17d:\n" \
"v_lshl_or_b32 v241, v241, 2, v234\n" \
"s_nop 1\n" \
"v_min_u32_dpp v241, v241, v241 row_ror:4 row_mask:0xf bank_mask:0xf\n" \
"s_nop 1\n" \
"v_min_u32_dpp v241, v241, v241 row_ror:8 row_mask:0xf bank_mask:0xf\n" \
"s_nop 1\n" \
"v_min_u32_dpp v241, v241, v241 quad_perm:[1,0,3,2] row_mask:0xf bank_mask:0xf\n" \
"s_nop 1\n" \
"v_min_u32_dpp v241, v241, v241 quad_perm:[2,3,0,1] row_mask:0xf bank_mask:0xf\n" \
"s_nop 0\n" \
"v_readlane_b32 s31, v241, 0\n" \
"v_readlane_b32 s43, v241, 16\n" \
"s_min_u32 s31, s31, s43\n" \
"v_readlane_b32 s43, v241, 32\n" \
"s_min_u32 s31, s31, s43\n" \
"v_readlane_b32 s43, v241, 48\n" \
"s_min_u32 s31, s31, s43\n" \
"v_subrev_u32 v230, 256, v205\n" \
"s_add_u32 s43, s30, 1\n" \
"s_mov_b32 s50, 0\n" \
".LS2_w18l:\n" \
"buffer_load_dword v240, v230, s[4:7], 0 offen sc1\n" \
"s_waitcnt vmcnt(0)\n" \
".LS2_w18c:\n" \
"v_subrev_u32 v241, s43, v240\n" \
"v_cmp_gt_u32 vcc, 0x100, v241\n" \
"s_cmp_eq_u64 vcc, -1\n" \
"s_cbranch_scc1 .LS2_w18d\n" \
"s_sleep 2\n" \
"s_add_u32 s50, s50, 1\n" \
"s_cmp_lt_u32 s50, 0x20000\n" \
"s_cbranch_scc1 .LS2_w18l\n" \
"s_branch .LS2_end\n" \
".LS2_w18d:\n" \
"v_lshl_or_b32 v241, v241, 2, v234\n" \
"s_nop 1\n" \
"v_min_u32_dpp v241, v241, v241 row_ror:4 row_mask:0xf bank_mask:0xf\n" \
"s_nop 1\n" \
"v_min_u32_dpp v241, v241, v241 row_ror:8 row_mask:0xf bank_mask:0xf\n" \
"s_nop 1\n" \
"v_min_u32_dpp v241, v241, v241 quad_perm:[1,0,3,2] row_mask:0xf bank_mask:0xf\n" \
"s_nop 1\n" \
"v_min_u32_dpp v241, v241, v241 quad_perm:[2,3,0,1] row_mask:0xf bank_mask:0xf\n" \
"s_nop 0\n" \
"v_readlane_b32 s32, v241, 0\n" \
"v_readlane_b32 s43, v241, 16\n" \
"s_min_u32 s32, s32, s43\n" \
"v_readlane_b32 s43, v241, 32\n" \
"s_min_u32 s32, s32, s43\n" \
"v_readlane_b32 s43, v241, 48\n" \
"s_min_u32 s32, s32, s43\n" \
"s_xor_b32 s29, s31, s32\n" \
"s_and_b32 s29, s29, 3\n" \
"s_lshl_b32 s29, s29, 13\n" \
"s_bitset1_b32 s29, 21\n" \
"s_and_b32 s31, s31, 3\n" \
"s_lshl_b32 s31, s31, 13\n" \
"s_bitcmp1_b32 s39, 0\n" \
"s_cselect_b32 s43, s29, 0\n" \
"s_xor_b32 s31, s31, s43\n" \
"s_xor_b32 s32, s31, 0x4000\n" \
"s_mov_b32 s39, 0\n" \
"v_mov_b32 v224, -1\n" \
"v_mov_b32 v225, -1\n" \
"v_mov_b32 v226, -1\n" \
"v_mov_b32 v227, -1\n" \
"s_waitcnt lgkmcnt(0)\n" \
"s_add_u32 s50, s31, 0\n" \
"buffer_store_dwordx4 v[224:227], v232, s[4:7], s50 offen sc1\n" \
"s_xor_b32 s43, s50, s29\n" \
"buffer_store_dwordx4 v[224:227], v232, s[4:7], s43 offen sc1\n" \
"s_add_u32 s50, s32, 524288\n" \
"buffer_store_dwordx4 v[224:227], v232, s[4:7], s50 offen sc1\n" \
"s_xor_b32 s43, s50, s29\n" \
"buffer_store_dwordx4 v[224:227], v232, s[4:7], s43 offen sc1\n" \
"s_add_u32 s50, s31, 1048576\n" \
"buffer_store_dwordx4 v[224:227], v232, s[4:7], s50 offen sc1\n" \
"s_xor_b32 s43, s50, s29\n" \
"buffer_store_dwordx4 v[224:227], v232, s[4:7], s43 offen sc1\n" \
"s_add_u32 s50, s32, 1572864\n" \
"buffer_store_dwordx4 v[224:227], v232, s[4:7], s50 offen sc1\n" \
"s_xor_b32 s43, s50, s29\n" \
"buffer_store_dwordx4 v[224:227], v232, s[4:7], s43 offen sc1\n" \
"s_waitcnt vmcnt(0)\n" \
"s_barrier\n" \
"s_add_u32 s43, s30, s40\n" \
"s_add_u32 s43, s43, 1\n" \
"v_mov_b32 v241, s43\n" \
"buffer_store_dword v241, v233, s[4:7], 0 offen sc1\n" \
"s_mov_b32 s33, s28\n" \
"s_add_u32 s34, s33, 2\n" \
"s_add_u32 s36, s33, 4\n" \
"s_add_u32 s43, s30, s33\n" \
"s_mov_b32 s50, 0\n" \
".LS2_w19l:\n" \
"buffer_load_dword v240, v204, s[4:7], 0 offen sc1\n" \
"s_waitcnt vmcnt(0)\n" \
".LS2_w19c:\n" \
"v_subrev_u32 v241, s43, v240\n" \
"v_cmp_gt_u32 vcc, 0x100, v241\n" \
"s_cmp_eq_u64 vcc, -1\n" \
"s_cbranch_scc1 .LS2_w19d\n" \
"s_sleep 2\n" \
"s_add_u32 s50, s50, 1\n" \
"s_cmp_lt_u32 s50, 0x20000\n" \
"s_cbranch_scc1 .LS2_w19l\n" \
"s_branch .LS2_end\n" \
".LS2_w19d:\n" \
"v_readlane_b32 s43, v202, 8\n" \
"s_nop 4\n" \
"buffer_load_dwordx4 a[64:67], v201, s[8:11], s43 offen sc1\n" \
"buffer_load_dwordx4 a[68:71], v201, s[8:11], s43 offen offset:1024 sc1\n" \
"v_readlane_b32 s43, v202, 9\n" \
"s_nop 4\n" \
"buffer_load_dwordx4 a[72:75], v201, s[8:11], s43 offen sc1\n" \
"buffer_load_dwordx4 a[76:79], v201, s[8:11], s43 offen offset:1024 sc1\n" \
"v_readlane_b32 s43, v202, 10\n" \
"s_nop 4\n" \
"buffer_load_dwordx4 a[80:83], v201, s[8:11], s43 offen sc1\n" \
"buffer_load_dwordx4 a[84:87], v201, s[8:11], s43 offen offset:1024 sc1\n" \
"v_readlane_b32 s43, v202, 12\n" \
"s_nop 4\n" \
"buffer_load_dwordx4 a[96:99], v201, s[8:11], s43 offen sc1\n" \
"buffer_load_dwordx4 a[100:103], v201, s[8:11], s43 offen offset:1024 sc1\n" \
"v_readlane_b32 s43, v202, 14\n" \
"s_nop 4\n" \
"buffer_load_dwordx4 a[112:115], v201, s[8:11], s43 offen sc1\n" \
"buffer_load_dwordx4 a[116:119], v201, s[8:11], s43 offen offset:1024 sc1\n" \
"v_readlane_b32 s43, v202, 19\n" \
"s_nop 4\n" \
"buffer_load_dwordx4 a[152:155], v201, s[8:11], s43 offen sc1\n" \
"buffer_load_dwordx4 a[156:159], v201, s[8:11], s43 offen offset:1024 sc1\n" \
"v_readlane_b32 s43, v202, 22\n" \
"s_nop 4\n" \
"buffer_load_dwordx4 a[176:179], v201, s[8:11], s43 offen sc1\n" \
"buffer_load_dwordx4 a[180:183], v201, s[8:11], s43 offen offset:1024 sc1\n" \
"v_readlane_b32 s43, v202, 25\n" \
"s_nop 4\n" \
"buffer_load_dwordx4 a[200:203], v201, s[8:11], s43 offen sc1\n" \
"buffer_load_dwordx4 a[204:207], v201, s[8:11], s43 offen offset:1024 sc1\n" \
"s_add_u32 s43, s30, s34\n" \
"s_mov_b32 s50, 0\n" \
"s_branch .LS2_w20c\n" \
".LS2_w20l:\n" \
"buffer_load_dword v240, v204, s[4:7], 0 offen sc1\n" \
"s_waitcnt vmcnt(0)\n" \
".LS2_w20c:\n" \
"v_subrev_u32 v241, s43, v240\n" \
"v_cmp_gt_u32 vcc, 0x100, v241\n" \
"s_cmp_eq_u64 vcc, -1\n" \
"s_cbranch_scc1 .LS2_w20d\n" \
"s_sleep 2\n" \
"s_add_u32 s50, s50, 1\n" \
"s_cmp_lt_u32 s50, 0x20000\n" \
"s_cbranch_scc1 .LS2_w20l\n" \
"s_branch .LS2_end\n" \
".LS2_w20d:\n" \
"v_readlane_b32 s43, v202, 11\n" \
"s_nop 4\n" \
"buffer_load_dwordx4 a[88:91], v201, s[8:11], s43 offen sc1\n" \
"buffer_load_dwordx4 a[92:95], v201, s[8:11], s43 offen offset:1024 sc1\n" \
"v_readlane_b32 s43, v202, 13\n" \
"s_nop 4\n" \
"buffer_load_dwordx4 a[104:107], v201, s[8:11], s43 offen sc1\n" \
"buffer_load_dwordx4 a[108:111], v201, s[8:11], s43 offen offset:1024 sc1\n" \
"v_readlane_b32 s43, v202, 15\n" \
"s_nop 4\n" \
"buffer_load_dwordx4 a[120:123], v201, s[8:11], s43 offen sc1\n" \
"buffer_load_dwordx4 a[124:127], v201, s[8:11], s43 offen offset:1024 sc1\n" \
"v_readlane_b32 s43, v202, 16\n" \
"s_nop 4\n" \
"buffer_load_dwordx4 a[128:131], v201, s[8:11], s43 offen sc1\n" \
"buffer_load_dwordx4 a[132:135], v201, s[8:11], s43 offen offset:1024 sc1\n" \
"v_readlane_b32 s43, v202, 17\n" \
"s_nop 4\n" \
"buffer_load_dwordx4 a[136:139], v201, s[8:11], s43 offen sc1\n" \
"buffer_load_dwordx4 a[140:143], v201, s[8:11], s43 offen offset:1024 sc1\n" \
"v_readlane_b32 s43, v202, 20\n" \
"s_nop 4\n" \
"buffer_load_dwordx4 a[160:163], v201, s[8:11], s43 offen sc1\n" \
"buffer_load_dwordx4 a[164:167], v201, s[8:11], s43 offen offset:1024 sc1\n" \
"v_readlane_b32 s43, v202, 23\n" \
"s_nop 4\n" \
"buffer_load_dwordx4 a[184:187], v201, s[8:11], s43 offen sc1\n" \
"buffer_load_dwordx4 a[188:191], v201, s[8:11], s43 offen offset:1024 sc1\n" \
"v_readlane_b32 s43, v202, 26\n" \
"s_nop 4\n" \
"buffer_load_dwordx4 a[208:211], v201, s[8:11], s43 offen sc1\n" \
"buffer_load_dwordx4 a[212:215], v201, s[8:11], s43 offen offset:1024 sc1\n" \
"s_add_u32 s43, s30, s36\n" \
"s_mov_b32 s50, 0\n" \
"s_branch .LS2_w21c\n" \
".LS2_w21l:\n" \
"buffer_load_dword v240, v204, s[4:7], 0 offen sc1\n" \
"s_waitcnt vmcnt(0)\n" \
".LS2_w21c:\n" \
"v_subrev_u32 v241, s43, v240\n" \
"v_cmp_gt_u32 vcc, 0x100, v241\n" \
"s_cmp_eq_u64 vcc, -1\n" \
"s_cbranch_scc1 .LS2_w21d\n" \
"s_sleep 2\n" \
"s_add_u32 s50, s50, 1\n" \
"s_cmp_lt_u32 s50, 0x20000\n" \
"s_cbranch_scc1 .LS2_w21l\n" \
"s_branch .LS2_end\n" \
".LS2_w21d:\n" \
"v_readlane_b32 s43, v202, 0\n" \
"s_nop 4\n" \
"buffer_load_dwordx4 a[0:3], v201, s[8:11], s43 offen sc1\n" \
"buffer_load_dwordx4 a[4:7], v201, s[8:11], s43 offen offset:1024 sc1\n" \
"v_readlane_b32 s43, v202, 1\n" \
"s_nop 4\n" \
"buffer_load_dwordx4 a[8:11], v201, s[8:11], s43 offen sc1\n" \
"buffer_load_dwordx4 a[12:15], v201, s[8:11], s43 offen offset:1024 sc1\n" \
"v_readlane_b32 s43, v202, 2\n" \
"s_nop 4\n" \
"buffer_load_dwordx4 a[16:19], v201, s[8:11], s43 offen sc1\n" \
"buffer_load_dwordx4 a[20:23], v201, s[8:11], s43 offen offset:1024 sc1\n" \
"v_readlane_b32 s43, v202, 3\n" \
"s_nop 4\n" \
"buffer_load_dwordx4 a[24:27], v201, s[8:11], s43 offen sc1\n" \
"buffer_load_dwordx4 a[28:31], v201, s[8:11], s43 offen offset:1024 sc1\n" \
"v_readlane_b32 s43, v202, 4\n" \
"s_nop 4\n" \
"buffer_load_dwordx4 a[32:35], v201, s[8:11], s43 offen sc1\n" \
"buffer_load_dwordx4 a[36:39], v201, s[8:11], s43 offen offset:1024 sc1\n" \
"v_readlane_b32 s43, v202, 5\n" \
"s_nop 4\n" \
"buffer_load_dwordx4 a[40:43], v201, s[8:11], s43 offen sc1\n" \
"buffer_load_dwordx4 a[44:47], v201, s[8:11], s43 offen offset:1024 sc1\n" \
"v_readlane_b32 s43, v202, 6\n" \
"s_nop 4\n" \
"buffer_load_dwordx4 a[48:51], v201, s[8:11], s43 offen sc1\n" \
"buffer_load_dwordx4 a[52:55], v201, s[8:11], s43 offen offset:1024 sc1\n" \
"v_readlane_b32 s43, v202, 7\n" \
"s_nop 4\n" \
"buffer_load_dwordx4 a[56:59], v201, s[8:11], s43 offen sc1\n" \
"buffer_load_dwordx4 a[60:63], v201, s[8:11], s43 offen offset:1024 sc1\n" \
"s_add_u32 s43, s30, 7\n" \
"s_mov_b32 s50, 0\n" \
"s_branch .LS2_w22c\n" \
".LS2_w22l:\n" \
"buffer_load_dword v240, v204, s[4:7], 0 offen sc1\n" \
"s_waitcnt vmcnt(0)\n" \
".LS2_w22c:\n" \
"v_subrev_u32 v241, s43, v240\n" \
"v_cmp_gt_u32 vcc, 0x100, v241\n" \
"s_cmp_eq_u64 vcc, -1\n" \
"s_cbranch_scc1 .LS2_w22d\n" \
"s_sleep 2\n" \
"s_add_u32 s50, s50, 1\n" \
"s_cmp_lt_u32 s50, 0x20000\n" \
"s_cbranch_scc1 .LS2_w22l\n" \
"s_branch .LS2_end\n" \
".LS2_w22d:\n" \
"v_readlane_b32 s43, v202, 18\n" \
"s_nop 4\n" \
"buffer_load_dwordx4 a[144:147], v201, s[8:11], s43 offen sc1\n" \
"buffer_load_dwordx4 a[148:151], v201, s[8:11], s43 offen offset:1024 sc1\n" \
"v_readlane_b32 s43, v202, 21\n" \
"s_nop 4\n" \
"buffer_load_dwordx4 a[168:171], v201, s[8:11], s43 offen sc1\n" \
"buffer_load_dwordx4 a[172:175], v201, s[8:11], s43 offen offset:1024 sc1\n" \
"v_readlane_b32 s43, v202, 24\n" \
"s_nop 4\n" \
"buffer_load_dwordx4 a[192:195], v201, s[8:11], s43 offen sc1\n" \
"buffer_load_dwordx4 a[196:199], v201, s[8:11], s43 offen offset:1024 sc1\n" \
"v_readlane_b32 s43, v202, 27\n" \
"s_nop 4\n" \
"buffer_load_dwordx4 a[216:219], v201, s[8:11], s43 offen sc1\n" \
"buffer_load_dwordx4 a[220:223], v201, s[8:11], s43 offen offset:1024 sc1\n" \
"s_mov_b32 s33, -1\n" \
"ds_read_b96 v[248:250], v252\n" \
"s_waitcnt lgkmcnt(0)\n" \
"v_readfirstlane_b32 s44, v248\n" \
"v_readfirstlane_b32 s45, v249\n" \
"v_readfirstlane_b32 s46, v250\n" \
"s_mul_i32 s42, s25, s46\n" \
"s_lshr_b32 s42, s42, 12\n" \
"s_mov_b32 s26, s44\n" \
"s_mov_b32 s27, s45\n" \
".p2align 3\n" \
"s_memtime s[54:55]\n" \
"s_memrealtime s[56:57]\n" \
"s_memtime s[58:59]\n" \
"s_memrealtime s[60:61]\n" \
"s_nop 0\n" \
"s_nop 0\n" \
"s_memtime s[62:63]\n" \
"s_memrealtime s[64:65]\n" \
"s_memtime s[66:67]\n" \
"s_memrealtime s[68:69]\n" \
"s_memtime s[70:71]\n" \
"s_memrealtime s[72:73]\n" \
"s_memtime s[74:75]\n" \
"s_memrealtime s[76:77]\n" \
"s_nop 0\n" \
"s_nop 0\n" \
"s_memtime s[78:79]\n" \
"s_memrealtime s[80:81]\n" \
"s_memtime s[82:83]\n" \
"s_memrealtime s[84:85]\n" \
"s_memtime s[86:87]\n" \
"s_memrealtime s[88:89]\n" \
"s_memtime s[90:91]\n" \
"s_memrealtime s[92:93]\n" \
"s_nop 0\n" \
"s_nop 0\n" \
"s_memtime s[94:95]\n" \
"s_memrealtime s[96:97]\n" \
"s_memtime s[98:99]\n" \
"s_mov_b32 s52, 0\n" \
"s_mov_b32 s53, 4096\n" \
"s_waitcnt lgkmcnt(0)\n" \
"s_cmp_lg_u32 s96, s56\n" \
"s_cselect_b32 s52, s90, s52\n" \
"s_cselect_b32 s53, s98, s53\n" \
"s_cmp_lg_u32 s92, s56\n" \
"s_cselect_b32 s52, s86, s52\n" \
"s_cselect_b32 s53, s94, s53\n" \
"s_cmp_lg_u32 s88, s56\n" \
"s_cselect_b32 s52, s82, s52\n" \
"s_cselect_b32 s53, s90, s53\n" \
"s_cmp_lg_u32 s84, s56\n" \
"s_cselect_b32 s52, s78, s52\n" \
"s_cselect_b32 s53, s86, s53\n" \
"s_cmp_lg_u32 s80, s56\n" \
"s_cselect_b32 s52, s74, s52\n" \
"s_cselect_b32 s53, s82, s53\n" \
"s_cmp_lg_u32 s76, s56\n" \
"s_cselect_b32 s52, s70, s52\n" \
"s_cselect_b32 s53, s78, s53\n" \
"s_cmp_lg_u32 s72, s56\n" \
"s_cselect_b32 s52, s66, s52\n" \
"s_cselect_b32 s53, s74, s53\n" \
"s_cmp_lg_u32 s68, s56\n" \
"s_cselect_b32 s52, s62, s52\n" \
"s_cselect_b32 s53, s70, s53\n" \
"s_cmp_lg_u32 s64, s56\n" \
"s_cselect_b32 s52, s58, s52\n" \
"s_cselect_b32 s53, s66, s53\n" \
"s_cmp_lg_u32 s60, s56\n" \
"s_cselect_b32 s52, s54, s52\n" \
"s_cselect_b32 s53, s62, s53\n" \
"s_sub_u32 s53, s53, s52\n" \
"s_lshr_b32 s55, s53, 1\n" \
"s_add_u32 s52, s52, s55\n" \
"s_add_u32 s55, s56, 4\n" \
"s_lshl_b32 s55, s55, 4\n" \
"s_cmp_le_u32 s53, 40\n" \
"s_cselect_b32 s44, s55, s44\n" \
"s_cselect_b32 s45, s52, s45\n" \
"v_mov_b32 v248, s44\n" \
"v_mov_b32 v249, s45\n" \
"v_mov_b32 v250, s46\n" \
"s_mov_b64 exec, 1\n" \
"ds_write_b96 v252, v[248:250]\n" \
"s_mov_b64 exec, -1\n" \
"s_sub_u32 s26, s44, s26\n" \
"s_sub_u32 s27, s45, s27\n" \
"v_cvt_f32_u32 v248, s26\n" \
"v_cvt_f32_u32 v249, s27\n" \
"v_rcp_f32 v248, v248\n" \
"v_mul_f32 v249, 0x457ae148, v249\n" \
"v_mul_f32 v248, v248, v249\n" \
"v_cvt_u32_f32 v248, v248\n" \
"s_nop 0\n" \
"v_readfirstlane_b32 s27, v248\n" \
"s_max_u32 s27, s27, 0x800\n" \
"s_min_u32 s27, s27, 0x2000\n" \
"s_cmp_gt_u32 s26, 1600\n" \
"s_cselect_b32 s46, s27, s46\n" \
".p2align 3\n" \
"s_memtime s[54:55]\n" \
"s_memrealtime s[56:57]\n" \
"s_memtime s[58:59]\n" \
"s_memrealtime s[60:61]\n" \
"s_nop 0\n" \
"s_nop 0\n" \
"s_memtime s[62:63]\n" \
"s_memrealtime s[64:65]\n" \
"s_memtime s[66:67]\n" \
"s_memrealtime s[68:69]\n" \
"s_memtime s[70:71]\n" \
"s_memrealtime s[72:73]\n" \
"s_memtime s[74:75]\n" \
"s_memrealtime s[76:77]\n" \
"s_nop 0\n" \
"s_nop 0\n" \
"s_memtime s[78:79]\n" \
"s_memrealtime s[80:81]\n" \
"s_memtime s[82:83]\n" \
"s_memrealtime s[84:85]\n" \
"s_memtime s[86:87]\n" \
"s_memrealtime s[88:89]\n" \
"s_memtime s[90:91]\n" \
"s_memrealtime s[92:93]\n" \
"s_nop 0\n" \
"s_nop 0\n" \
"s_memtime s[94:95]\n" \
"s_memrealtime s[96:97]\n" \
"s_memtime s[98:99]\n" \
"s_mov_b32 s52, 0\n" \
"s_mov_b32 s53, 4096\n" \
"s_waitcnt lgkmcnt(0)\n" \
"s_cmp_lg_u32 s96, s56\n" \
"s_cselect_b32 s52, s90, s52\n" \
"s_cselect_b32 s53, s98, s53\n" \
"s_cmp_lg_u32 s92, s56\n" \
"s_cselect_b32 s52, s86, s52\n" \
"s_cselect_b32 s53, s94, s53\n" \
"s_cmp_lg_u32 s88, s56\n" \
"s_cselect_b32 s52, s82, s52\n" \
"s_cselect_b32 s53, s90, s53\n" \
"s_cmp_lg_u32 s84, s56\n" \
"s_cselect_b32 s52, s78, s52\n" \
"s_cselect_b32 s53, s86, s53\n" \
"s_cmp_lg_u32 s80, s56\n" \
"s_cselect_b32 s52, s74, s52\n" \
"s_cselect_b32 s53, s82, s53\n" \
"s_cmp_lg_u32 s76, s56\n" \
"s_cselect_b32 s52, s70, s52\n" \
"s_cselect_b32 s53, s78, s53\n" \
"s_cmp_lg_u32 s72, s56\n" \
"s_cselect_b32 s52, s66, s52\n" \
"s_cselect_b32 s53, s74, s53\n" \
"s_cmp_lg_u32 s68, s56\n" \
"s_cselect_b32 s52, s62, s52\n" \
"s_cselect_b32 s53, s70, s53\n" \
"s_cmp_lg_u32 s64, s56\n" \
"s_cselect_b32 s52, s58, s52\n" \
"s_cselect_b32 s53, s66, s53\n" \
"s_cmp_lg_u32 s60, s56\n" \
"s_cselect_b32 s52, s54, s52\n" \
"s_cselect_b32 s53, s62, s53\n" \
"s_sub_u32 s53, s53, s52\n" \
"s_lshr_b32 s55, s53, 1\n" \
"s_add_u32 s52, s52, s55\n" \
"s_add_u32 s55, s56, 4\n" \
"s_lshl_b32 s55, s55, 4\n" \
"s_cmp_le_u32 s53, 40\n" \
"s_cselect_b32 s44, s55, s44\n" \
"s_cselect_b32 s45, s52, s45\n" \
"v_mov_b32 v248, s44\n" \
"v_mov_b32 v249, s45\n" \
"v_mov_b32 v250, s46\n" \
"s_mov_b64 exec, 1\n" \
"ds_write_b96 v252, v[248:250]\n" \
"s_mov_b64 exec, -1\n" \
"s_waitcnt lgkmcnt(0)\n" \
"s_add_u32 s43, s30, 1\n" \
"s_mov_b32 s50, 0\n" \
".LS2_w23l:\n" \
"buffer_load_dword v240, v205, s[4:7], 0 offen sc1\n" \
"s_waitcnt vmcnt(0)\n" \
".LS2_w23c:\n" \
"v_subrev_u32 v241, s43, v240\n" \
"v_cmp_gt_u32 vcc, 0x100, v241\n" \
"s_cmp_eq_u64 vcc, -1\n" \
"s_cbranch_scc1 .LS2_w23d\n" \
"s_sleep 2\n" \
"s_add_u32 s50, s50, 1\n" \
"s_cmp_lt_u32 s50, 0x20000\n" \
"s_cbranch_scc1 .LS2_w23l\n" \
"s_branch .LS2_end\n" \
".LS2_w23d:\n" \
"s_add_u32 s43, s30, 1\n" \
"s_mov_b32 s50, 0\n" \
".LS2_w24l:\n" \
"buffer_load_dword v240, v206, s[4:7], 0 offen sc1\n" \
"s_waitcnt vmcnt(0)\n" \
".LS2_w24c:\n" \
"v_subrev_u32 v241, s43, v240\n" \
"v_cmp_gt_u32 vcc, 0x100, v241\n" \
"s_cmp_eq_u64 vcc, -1\n" \
"s_cbranch_scc1 .LS2_w24d\n" \
"s_sleep 2\n" \
"s_add_u32 s50, s50, 1\n" \
"s_cmp_lt_u32 s50, 0x20000\n" \
"s_cbranch_scc1 .LS2_w24l\n" \
"s_branch .LS2_end\n" \
".LS2_w24d:\n" \
".LS2_pre:\n" \
"s_barrier\n" \
"ds_read_b96 v[248:250], v252\n" \
"s_waitcnt lgkmcnt(0)\n" \
"v_readfirstlane_b32 s44, v248\n" \
"v_readfirstlane_b32 s45, v249\n" \
"v_readfirstlane_b32 s46, v250\n" \
"s_mul_i32 s42, s25, s46\n" \
"s_lshr_b32 s42, s42, 12\n" \
".LS2_cu:\n" \
"s_sub_u32 s43, s24, s44\n" \
"s_cmp_lt_i32 s43, 0\n" \
"s_cbranch_scc0 .LS2_cud\n" \
"s_add_u32 s24, s24, s25\n" \
"s_xor_b32 s31, s31, s29\n" \
"s_xor_b32 s32, s32, s29\n" \
"s_xor_b32 s47, s47, s29\n" \
"s_add_u32 s39, s39, 1\n" \
"s_branch .LS2_cu\n" \
".LS2_cud:\n" \
".LS2_loop:\n" \
"s_add_u32 s43, s40, 0\n" \
"s_and_b32 s43, s43, 3\n" \
"s_bitcmp1_b32 s43, 0\n" \
"s_cselect_b32 s47, s32, s31\n" \
"s_lshl_b32 s43, s43, 19\n" \
"s_add_u32 s47, s47, s43\n" \
"s_sub_u32 s43, s24, s44\n" \
"s_max_i32 s43, s43, 0xfffd0000\n" \
"s_min_i32 s43, s43, 0x4000\n" \
"s_mul_i32 s43, s43, s46\n" \
"s_ashr_i32 s43, s43, 12\n" \
"s_add_u32 s41, s45, s43\n" \
"s_add_u32 s41, s41, 8\n" \
"s_branch .LS2_a1\n" \
".LS2_retry:\n" \
"s_add_u32 s51, s51, 1\n" \
"s_cmp_gt_u32 s51, 0x20000\n" \
"s_cbranch_scc1 .LS2_end\n" \
".LS2_a0:\n" \
"s_add_u32 s41, s41, s42\n" \
"s_add_u32 s24, s24, s25\n" \
"s_xor_b32 s31, s31, s29\n" \
"s_xor_b32 s32, s32, s29\n" \
"s_xor_b32 s47, s47, s29\n" \
"s_xor_b32 s48, s48, s29\n" \
"s_add_u32 s39, s39, 1\n" \
".p2align 6\n" \
".LS2_a1:\n" \
"s_memtime vcc\n" \
"s_waitcnt lgkmcnt(0)\n" \
"s_sub_u32 s43, s41, vcc_lo\n" \
"s_cmp_gt_i32 s43, 128\n" \
"s_cbranch_scc1 .LS2_a1\n" \
"s_cmp_lt_i32 s43, s37\n" \
"s_cbranch_scc1 .LS2_a0\n" \
"s_max_i32 s43, s43, 0\n" \
"s_and_b32 s43, s43, -4\n" \
"s_getpc_b64 vcc\n" \
".LS2_a3:\n" \
"s_sub_u32 s43, .LS2_a4-.LS2_a3, s43\n" \
"s_add_u32 vcc_lo, vcc_lo, s43\n" \
"s_addc_u32 vcc_hi, vcc_hi, 0\n" \
"s_setpc_b64 vcc\n" \
".rept 32\n" \
"s_nop 0\n" \
".endr\n" \
".LS2_a4:\n" \
"buffer_load_dwordx4 v[0:3], v208, s[4:7], s47 offen offset:2048 sc1\n" \
"s_nop 9\n" \
"buffer_load_dwordx4 v[4:7], v208, s[4:7], s47 offen sc1\n" \
"s_nop 9\n" \
"buffer_load_dwordx4 v[8:11], v208, s[4:7], s47 offen offset:1024 sc1\n" \
"s_nop 9\n" \
"buffer_load_dwordx4 v[12:15], v210, s[4:7], s47 offen sc1\n" \
"s_add_u32 s35, s35, 1\n" \
"v_mov_b32 v28, 0\n" \
"v_mov_b32 v29, 0\n" \
"v_mov_b32 v30, 0\n" \
"v_mov_b32 v31, 0\n" \
"v_mov_b32 v32, 0\n" \
"v_mov_b32 v33, 0\n" \
"v_mov_b32 v34, 0\n" \
"v_mov_b32 v35, 0\n" \
"v_mov_b32 v36, 0\n" \
"v_mov_b32 v37, 0\n" \
"v_mov_b32 v38, 0\n" \
"v_mov_b32 v39, 0\n" \
"v_mov_b32 v40, 0\n" \
"v_mov_b32 v41, 0\n" \
"v_mov_b32 v42, 0\n" \
"v_mov_b32 v43, 0\n" \
"v_mov_b32 v44, 0\n" \
"v_mov_b32 v45, 0\n" \
"v_mov_b32 v46, 0\n" \
"v_mov_b32 v47, 0\n" \
"v_mov_b32 v48, 0\n" \
"v_mov_b32 v49, 0\n" \
"v_mov_b32 v50, 0\n" \
"v_mov_b32 v51, 0\n" \
"v_mov_b32 v52, 0\n" \
"v_mov_b32 v53, 0\n" \
"v_mov_b32 v54, 0\n" \
"v_mov_b32 v55, 0\n" \
"v_mov_b32 v56, 0\n" \
"v_mov_b32 v57, 0\n" \
"v_mov_b32 v58, 0\n" \
"v_mov_b32 v59, 0\n" \
"s_waitcnt vmcnt(3)\n" \
"v_smfmac_f32_16x16x64_bf16 v[28:31], v[0:3], a[0:7], v200\n" \
"v_smfmac_f32_16x16x64_bf16 v[32:35], v[0:3], a[8:15], v200\n" \
"v_smfmac_f32_16x16x64_bf16 v[36:39], v[0:3], a[16:23], v200\n" \
"v_smfmac_f32_16x16x64_bf16 v[40:43], v[0:3], a[24:31], v200\n" \
"v_smfmac_f32_16x16x64_bf16 v[44:47], v[0:3], a[32:39], v200\n" \
"v_smfmac_f32_16x16x64_bf16 v[48:51], v[0:3], a[40:47], v200\n" \
"v_smfmac_f32_16x16x64_bf16 v[52:55], v[0:3], a[48:55], v200\n" \
"v_smfmac_f32_16x16x64_bf16 v[56:59], v[0:3], a[56:63], v200\n" \
"s_waitcnt vmcnt(2)\n" \
"v_smfmac_f32_16x16x64_bf16 v[28:31], v[4:7], a[64:71], v200\n" \
"v_smfmac_f32_16x16x64_bf16 v[32:35], v[4:7], a[72:79], v200\n" \
"v_smfmac_f32_16x16x64_bf16 v[36:39], v[4:7], a[80:87], v200\n" \
"s_waitcnt vmcnt(1)\n" \
"v_smfmac_f32_16x16x64_bf16 v[28:31], v[8:11], a[88:95], v200\n" \
"s_nop 2\n" \
"v_smfmac_f32_16x16x64_bf16 v[40:43], v[4:7], a[96:103], v200\n" \
"s_nop 2\n" \
"v_smfmac_f32_16x16x64_bf16 v[32:35], v[8:11], a[104:111], v200\n" \
"s_nop 0\n" \
"v_cvt_pk_bf16_f32 v128, v28, v29\n" \
"v_cvt_pk_bf16_f32 v129, v30, v31\n" \
"v_smfmac_f32_16x16x64_bf16 v[44:47], v[4:7], a[112:119], v200\n" \
"ds_write_b64 v211, v[128:129] offset:256\n" \
"s_nop 1\n" \
"v_smfmac_f32_16x16x64_bf16 v[36:39], v[8:11], a[120:127], v200\n" \
"s_nop 0\n" \
"v_cvt_pk_bf16_f32 v130, v32, v33\n" \
"v_cvt_pk_bf16_f32 v131, v34, v35\n" \
"v_smfmac_f32_16x16x64_bf16 v[44:47], v[8:11], a[128:135], v200\n" \
"ds_write_b64 v211, v[130:131] offset:2432\n" \
"s_nop 1\n" \
"v_smfmac_f32_16x16x64_bf16 v[40:43], v[8:11], a[136:143], v200\n" \
"s_waitcnt vmcnt(0)\n" \
"v_cvt_pk_bf16_f32 v132, v36, v37\n" \
"v_cvt_pk_bf16_f32 v133, v38, v39\n" \
"v_smfmac_f32_16x16x64_bf16 v[44:47], v[12:15], a[144:151], v200\n" \
"ds_write_b64 v211, v[132:133] offset:4608\n" \
"s_nop 1\n" \
"v_smfmac_f32_16x16x64_bf16 v[48:51], v[4:7], a[152:159], v200\n" \
"s_nop 0\n" \
"v_cvt_pk_bf16_f32 v134, v40, v41\n" \
"v_cvt_pk_bf16_f32 v135, v42, v43\n" \
"v_smfmac_f32_16x16x64_bf16 v[48:51], v[8:11], a[160:167], v200\n" \
"ds_write_b64 v211, v[134:135] offset:4992\n" \
"v_cvt_pk_bf16_f32 v136, v44, v45\n" \
"v_cvt_pk_bf16_f32 v137, v46, v47\n" \
"v_smfmac_f32_16x16x64_bf16 v[48:51], v[12:15], a[168:175], v200\n" \
"ds_write_b64 v211, v[136:137] offset:9472\n" \
"s_nop 1\n" \
"v_smfmac_f32_16x16x64_bf16 v[52:55], v[4:7], a[176:183], v200\n" \
"s_nop 2\n" \
"v_smfmac_f32_16x16x64_bf16 v[52:55], v[8:11], a[184:191], v200\n" \
"s_nop 0\n" \
"v_cvt_pk_bf16_f32 v138, v48, v49\n" \
"v_cvt_pk_bf16_f32 v139, v50, v51\n" \
"v_smfmac_f32_16x16x64_bf16 v[52:55], v[12:15], a[192:199], v200\n" \
"ds_write_b64 v211, v[138:139] offset:11648\n" \
"s_waitcnt lgkmcnt(2)\n" \
"s_barrier\n" \
"v_smfmac_f32_16x16x64_bf16 v[56:59], v[4:7], a[200:207], v200\n" \
"s_nop 2\n" \
"v_smfmac_f32_16x16x64_bf16 v[56:59], v[8:11], a[208:215], v200\n" \
"s_nop 2\n" \
"v_smfmac_f32_16x16x64_bf16 v[56:59], v[12:15], a[216:223], v200\n" \
"v_cvt_pk_bf16_f32 v140, v52, v53\n" \
"v_cvt_pk_bf16_f32 v141, v54, v55\n" \
"s_nop 1\n" \
"ds_write_b64 v211, v[140:141] offset:13824\n" \
"s_nop 2\n" \
"v_cvt_pk_bf16_f32 v142, v56, v57\n" \
"v_cvt_pk_bf16_f32 v143, v58, v59\n" \
"ds_write_b64 v211, v[142:143] offset:14208\n" \
"s_waitcnt lgkmcnt(2)\n" \
"s_barrier\n" \
"s_waitcnt lgkmcnt(1)\n" \
"s_barrier\n" \
"s_waitcnt lgkmcnt(0)\n" \
"s_barrier\n" \
"s_nop 3\n" \
"ds_read_b32 v64, v252 offset:20\n" \
"s_waitcnt lgkmcnt(0)\n" \
"v_readfirstlane_b32 s43, v64\n" \
"s_cmp_eq_u32 s43, s35\n" \
"s_cbranch_scc1 .LS2_retry\n" \
"s_nop 15\n" \
"v_max3_u32 v64, v0, v0, v1\n" \
"v_max3_u32 v64, v64, v2, v3\n" \
"v_max3_u32 v64, v64, v4, v5\n" \
"v_max3_u32 v64, v64, v6, v7\n" \
"v_max3_u32 v64, v64, v8, v9\n" \
"v_max3_u32 v64, v64, v10, v11\n" \
"v_max3_u32 v64, v64, v12, v13\n" \
"v_max3_u32 v64, v64, v14, v15\n" \
"v_cmp_eq_u32 vcc, -1, v64\n" \
"s_cbranch_vccnz .LS2_veto\n" \
".LS2_back:\n" \
"s_waitcnt lgkmcnt(0)\n" \
"s_barrier\n" \
"ds_read_b32 v64, v252 offset:16\n" \
"s_waitcnt lgkmcnt(0)\n" \
"v_readfirstlane_b32 s43, v64\n" \
"s_cmp_eq_u32 s43, s35\n" \
"s_cbranch_scc1 .LS2_retry\n" \
".p2align 3\n" \
"s_memtime s[54:55]\n" \
"s_memrealtime s[56:57]\n" \
"s_memtime s[58:59]\n" \
"s_memrealtime s[60:61]\n" \
"s_nop 0\n" \
"s_nop 0\n" \
"s_memtime s[62:63]\n" \
"s_memrealtime s[64:65]\n" \
"s_memtime s[66:67]\n" \
"s_memrealtime s[68:69]\n" \
"s_memtime s[70:71]\n" \
"s_memrealtime s[72:73]\n" \
"s_memtime s[74:75]\n" \
"s_memrealtime s[76:77]\n" \
"s_nop 0\n" \
"s_nop 0\n" \
"s_memtime s[78:79]\n" \
"s_memrealtime s[80:81]\n" \
"s_memtime s[82:83]\n" \
"s_memrealtime s[84:85]\n" \
"s_memtime s[86:87]\n" \
"s_memrealtime s[88:89]\n" \
"s_memtime s[90:91]\n" \
"s_memrealtime s[92:93]\n" \
"s_nop 0\n" \
"s_nop 0\n" \
"s_memtime s[94:95]\n" \
"s_memrealtime s[96:97]\n" \
"s_memtime s[98:99]\n" \
"s_mov_b32 s52, 0\n" \
"s_mov_b32 s53, 4096\n" \
"s_waitcnt lgkmcnt(0)\n" \
"s_cmp_lg_u32 s96, s56\n" \
"s_cselect_b32 s52, s90, s52\n" \
"s_cselect_b32 s53, s98, s53\n" \
"s_cmp_lg_u32 s92, s56\n" \
"s_cselect_b32 s52, s86, s52\n" \
"s_cselect_b32 s53, s94, s53\n" \
"s_cmp_lg_u32 s88, s56\n" \
"s_cselect_b32 s52, s82, s52\n" \
"s_cselect_b32 s53, s90, s53\n" \
"s_cmp_lg_u32 s84, s56\n" \
"s_cselect_b32 s52, s78, s52\n" \
"s_cselect_b32 s53, s86, s53\n" \
"s_cmp_lg_u32 s80, s56\n" \
"s_cselect_b32 s52, s74, s52\n" \
"s_cselect_b32 s53, s82, s53\n" \
"s_cmp_lg_u32 s76, s56\n" \
"s_cselect_b32 s52, s70, s52\n" \
"s_cselect_b32 s53, s78, s53\n" \
"s_cmp_lg_u32 s72, s56\n" \
"s_cselect_b32 s52, s66, s52\n" \
"s_cselect_b32 s53, s74, s53\n" \
"s_cmp_lg_u32 s68, s56\n" \
"s_cselect_b32 s52, s62, s52\n" \
"s_cselect_b32 s53, s70, s53\n" \
"s_cmp_lg_u32 s64, s56\n" \
"s_cselect_b32 s52, s58, s52\n" \
"s_cselect_b32 s53, s66, s53\n" \
"s_cmp_lg_u32 s60, s56\n" \
"s_cselect_b32 s52, s54, s52\n" \
"s_cselect_b32 s53, s62, s53\n" \
"s_sub_u32 s53, s53, s52\n" \
"s_lshr_b32 s55, s53, 1\n" \
"s_add_u32 s52, s52, s55\n" \
"s_add_u32 s55, s56, 4\n" \
"s_lshl_b32 s55, s55, 4\n" \
"s_sub_u32 s59, s55, s44\n" \
"s_cmp_lt_u32 s59, 2600\n" \
"s_cselect_b32 s63, 1, 2\n" \
"s_mul_i32 s59, s59, s46\n" \
"s_ashr_i32 s59, s59, 12\n" \
"s_add_u32 s59, s59, s45\n" \
"s_sub_u32 s59, s52, s59\n" \
"s_max_i32 s59, s59, -96\n" \
"s_min_i32 s59, s59, 96\n" \
"s_ashr_i32 s63, s59, s63\n" \
"s_add_u32 s63, s63, s46\n" \
"s_max_i32 s63, s63, 0x800\n" \
"s_min_i32 s63, s63, 0x2000\n" \
"s_cmp_le_u32 s53, 40\n" \
"s_cselect_b32 s44, s55, s44\n" \
"s_cselect_b32 s45, s52, s45\n" \
"s_cselect_b32 s46, s63, s46\n" \
"v_mov_b32 v248, s44\n" \
"v_mov_b32 v249, s45\n" \
"v_mov_b32 v250, s46\n" \
"s_mov_b64 exec, 1\n" \
"ds_write_b96 v252, v[248:250]\n" \
"s_mov_b64 exec, -1\n" \
"s_waitcnt lgkmcnt(0)\n" \
"s_barrier\n" \
"ds_read_b64 v[246:247], v252 offset:24\n" \
"ds_read_b96 v[248:250], v252\n" \
"s_waitcnt lgkmcnt(0)\n" \
"v_readfirstlane_b32 s44, v248\n" \
"v_readfirstlane_b32 s45, v249\n" \
"v_readfirstlane_b32 s46, v250\n" \
"v_readfirstlane_b32 s26, v246\n" \
"v_readfirstlane_b32 s27, v247\n" \
"s_mul_i32 s42, s25, s46\n" \
"s_lshr_b32 s42, s42, 12\n" \
"s_add_u32 s40, s40, 1\n" \
"s_add_u32 s24, s24, s26\n" \
"s_add_u32 s24, s24, s27\n" \
"s_cmp_eq_u32 s40, 0\n" \
"s_cbranch_scc1 .LS2_wet\n" \
"s_cmp_lt_u32 s40, 63\n" \
"s_cbranch_scc1 .LS2_loop\n" \
".LS2_end:\n" \
"s_endpgm\n" \
".LS2_veto:\n" \
"v_mov_b32 v64, s35\n" \
"ds_write_b32 v252, v64 offset:16\n" \
"s_branch .LS2_back\n" \
".p2align 6\n" \
"fk_role_S3:\n" \
"ds_read_b32 v200, v255 offset:0\n" \
"ds_read_b32 v201, v255 offset:256\n" \
"ds_read_b32 v202, v255 offset:512\n" \
"ds_read_b32 v203, v255 offset:768\n" \
"ds_read_b32 v204, v255 offset:1024\n" \
"ds_read_b32 v205, v255 offset:1280\n" \
"ds_read_b32 v206, v255 offset:1536\n" \
"ds_read_b32 v207, v255 offset:1792\n" \
"ds_read_b32 v208, v255 offset:2048\n" \
"ds_read_b32 v209, v255 offset:2304\n" \
"ds_read_b32 v210, v255 offset:2560\n" \
"ds_read_b32 v211, v255 offset:2816\n" \
"ds_read_b32 v212, v255 offset:3072\n" \
"ds_read_b32 v213, v255 offset:3328\n" \
"ds_read_b32 v214, v255 offset:3584\n" \
"ds_read_b32 v215, v255 offset:3840\n" \
"ds_read_b32 v254, v255 offset:7936\n" \
"s_waitcnt lgkmcnt(0)\n" \
"v_readlane_b32 s4, v254, 0\n" \
"v_readlane_b32 s5, v254, 1\n" \
"v_readlane_b32 s6, v254, 2\n" \
"v_readlane_b32 s7, v254, 3\n" \
"v_readlane_b32 s8, v254, 4\n" \
"v_readlane_b32 s9, v254, 5\n" \
"v_readlane_b32 s10, v254, 6\n" \
"v_readlane_b32 s11, v254, 7\n" \
"v_readlane_b32 s12, v254, 8\n" \
"v_readlane_b32 s13, v254, 9\n" \
"v_readlane_b32 s14, v254, 10\n" \
"v_readlane_b32 s15, v254, 11\n" \
"v_readlane_b32 s16, v254, 12\n" \
"v_readlane_b32 s17, v254, 13\n" \
"v_readlane_b32 s18, v254, 14\n" \
"v_readlane_b32 s19, v254, 15\n" \
"v_readlane_b32 s20, v254, 16\n" \
"v_readlane_b32 s21, v254, 17\n" \
"v_readlane_b32 s22, v254, 18\n" \
"v_readlane_b32 s23, v254, 19\n" \
"v_readlane_b32 s24, v254, 20\n" \
"v_readlane_b32 s25, v254, 21\n" \
"v_readlane_b32 s26, v254, 22\n" \
"v_readlane_b32 s27, v254, 23\n" \
"v_readlane_b32 s28, v254, 24\n" \
"v_readlane_b32 s29, v254, 25\n" \
"v_readlane_b32 s30, v254, 26\n" \
"v_readlane_b32 s31, v254, 27\n" \
"v_readlane_b32 s32, v254, 28\n" \
"v_readlane_b32 s33, v254, 29\n" \
"v_readlane_b32 s34, v254, 30\n" \
"v_readlane_b32 s35, v254, 31\n" \
"v_readlane_b32 s36, v254, 32\n" \
"v_readlane_b32 s37, v254, 33\n" \
"v_readlane_b32 s38, v254, 34\n" \
"v_readlane_b32 s39, v254, 35\n" \
"s_mov_b32 s40, 0\n" \
"s_mov_b32 s51, 0\n" \
"s_nop 0\n" \
"v_mov_b32 v252, s38\n" \
"s_nop 4\n" \
"v_mov_b32 v188, -1\n" \
"s_mov_b32 s28, s33\n" \
"s_mov_b32 s33, -1\n" \
"v_writelane_b32 v251, s6, 0\n" \
"v_writelane_b32 v251, s10, 1\n" \
"v_writelane_b32 v251, s14, 2\n" \
"v_writelane_b32 v251, s18, 3\n" \
"s_mov_b32 s6, 0\n" \
"s_mov_b32 s10, 0\n" \
"s_mov_b32 s14, 0\n" \
"s_mov_b32 s18, 0\n" \
"s_mov_b32 s40, -1\n" \
"s_branch .LS3_pre\n" \
".LS3_wet:\n" \
"s_waitcnt vmcnt(0)\n" \
"v_readlane_b32 s6, v251, 0\n" \
"v_readlane_b32 s10, v251, 1\n" \
"v_readlane_b32 s14, v251, 2\n" \
"v_readlane_b32 s18, v251, 3\n" \
"s_nop 4\n" \
"ds_read_b32 v232, v255 offset:7424\n" \
"ds_read_b32 v233, v255 offset:7680\n" \
"v_lshrrev_b32 v234, 4, v201\n" \
"v_and_b32 v234, 3, v234\n" \
"v_subrev_u32 v230, 512, v205\n" \
"s_add_u32 s43, s30, 1\n" \
"s_mov_b32 s50, 0\n" \
".LS3_w25l:\n" \
"buffer_load_dword v240, v230, s[4:7], 0 offen sc1\n" \
"s_waitcnt vmcnt(0)\n" \
".LS3_w25c:\n" \
"v_subrev_u32 v241, s43, v240\n" \
"v_cmp_gt_u32 vcc, 0x100, v241\n" \
"s_cmp_eq_u64 vcc, -1\n" \
"s_cbranch_scc1 .LS3_w25d\n" \
"s_sleep 2\n" \
"s_add_u32 s50, s50, 1\n" \
"s_cmp_lt_u32 s50, 0x20000\n" \
"s_cbranch_scc1 .LS3_w25l\n" \
"s_branch .LS3_end\n" \
".LS3_w25d:\n" \
"v_lshl_or_b32 v241, v241, 2, v234\n" \
"s_nop 1\n" \
"v_min_u32_dpp v241, v241, v241 row_ror:4 row_mask:0xf bank_mask:0xf\n" \
"s_nop 1\n" \
"v_min_u32_dpp v241, v241, v241 row_ror:8 row_mask:0xf bank_mask:0xf\n" \
"s_nop 1\n" \
"v_min_u32_dpp v241, v241, v241 quad_perm:[1,0,3,2] row_mask:0xf bank_mask:0xf\n" \
"s_nop 1\n" \
"v_min_u32_dpp v241, v241, v241 quad_perm:[2,3,0,1] row_mask:0xf bank_mask:0xf\n" \
"s_nop 0\n" \
"v_readlane_b32 s31, v241, 0\n" \
"v_readlane_b32 s43, v241, 16\n" \
"s_min_u32 s31, s31, s43\n" \
"v_readlane_b32 s43, v241, 32\n" \
"s_min_u32 s31, s31, s43\n" \
"v_readlane_b32 s43, v241, 48\n" \
"s_min_u32 s31, s31, s43\n" \
"v_subrev_u32 v230, 256, v205\n" \
"s_add_u32 s43, s30, 1\n" \
"s_mov_b32 s50, 0\n" \
".LS3_w26l:\n" \
"buffer_load_dword v240, v230, s[4:7], 0 offen sc1\n" \
"s_waitcnt vmcnt(0)\n" \
".LS3_w26c:\n" \
"v_subrev_u32 v241, s43, v240\n" \
"v_cmp_gt_u32 vcc, 0x100, v241\n" \
"s_cmp_eq_u64 vcc, -1\n" \
"s_cbranch_scc1 .LS3_w26d\n" \
"s_sleep 2\n" \
"s_add_u32 s50, s50, 1\n" \
"s_cmp_lt_u32 s50, 0x20000\n" \
"s_cbranch_scc1 .LS3_w26l\n" \
"s_branch .LS3_end\n" \
".LS3_w26d:\n" \
"v_lshl_or_b32 v241, v241, 2, v234\n" \
"s_nop 1\n" \
"v_min_u32_dpp v241, v241, v241 row_ror:4 row_mask:0xf bank_mask:0xf\n" \
"s_nop 1\n" \
"v_min_u32_dpp v241, v241, v241 row_ror:8 row_mask:0xf bank_mask:0xf\n" \
"s_nop 1\n" \
"v_min_u32_dpp v241, v241, v241 quad_perm:[1,0,3,2] row_mask:0xf bank_mask:0xf\n" \
"s_nop 1\n" \
"v_min_u32_dpp v241, v241, v241 quad_perm:[2,3,0,1] row_mask:0xf bank_mask:0xf\n" \
"s_nop 0\n" \
"v_readlane_b32 s32, v241, 0\n" \
"v_readlane_b32 s43, v241, 16\n" \
"s_min_u32 s32, s32, s43\n" \
"v_readlane_b32 s43, v241, 32\n" \
"s_min_u32 s32, s32, s43\n" \
"v_readlane_b32 s43, v241, 48\n" \
"s_min_u32 s32, s32, s43\n" \
"s_xor_b32 s29, s31, s32\n" \
"s_and_b32 s29, s29, 3\n" \
"s_lshl_b32 s29, s29, 13\n" \
"s_bitset1_b32 s29, 21\n" \
"s_and_b32 s31, s31, 3\n" \
"s_lshl_b32 s31, s31, 13\n" \
"s_bitcmp1_b32 s39, 0\n" \
"s_cselect_b32 s43, s29, 0\n" \
"s_xor_b32 s31, s31, s43\n" \
"s_xor_b32 s32, s31, 0x4000\n" \
"s_mov_b32 s39, 0\n" \
"v_mov_b32 v224, -1\n" \
"v_mov_b32 v225, -1\n" \
"v_mov_b32 v226, -1\n" \
"v_mov_b32 v227, -1\n" \
"s_waitcnt lgkmcnt(0)\n" \
"s_add_u32 s50, s31, 0\n" \
"buffer_store_dwordx4 v[224:227], v232, s[4:7], s50 offen sc1\n" \
"s_xor_b32 s43, s50, s29\n" \
"buffer_store_dwordx4 v[224:227], v232, s[4:7], s43 offen sc1\n" \
"s_add_u32 s50, s32, 524288\n" \
"buffer_store_dwordx4 v[224:227], v232, s[4:7], s50 offen sc1\n" \
"s_xor_b32 s43, s50, s29\n" \
"buffer_store_dwordx4 v[224:227], v232, s[4:7], s43 offen sc1\n" \
"s_add_u32 s50, s31, 1048576\n" \
"buffer_store_dwordx4 v[224:227], v232, s[4:7], s50 offen sc1\n" \
"s_xor_b32 s43, s50, s29\n" \
"buffer_store_dwordx4 v[224:227], v232, s[4:7], s43 offen sc1\n" \
"s_add_u32 s50, s32, 1572864\n" \
"buffer_store_dwordx4 v[224:227], v232, s[4:7], s50 offen sc1\n" \
"s_xor_b32 s43, s50, s29\n" \
"buffer_store_dwordx4 v[224:227], v232, s[4:7], s43 offen sc1\n" \
"s_waitcnt vmcnt(0)\n" \
"s_barrier\n" \
"s_add_u32 s43, s30, s40\n" \
"s_add_u32 s43, s43, 1\n" \
"v_mov_b32 v241, s43\n" \
"buffer_store_dword v241, v233, s[4:7], 0 offen sc1\n" \
"s_mov_b32 s33, s28\n" \
"s_add_u32 s34, s33, 2\n" \
"s_add_u32 s36, s33, 4\n" \
"s_add_u32 s43, s30, s33\n" \
"s_mov_b32 s50, 0\n" \
".LS3_w27l:\n" \
"buffer_load_dword v240, v204, s[4:7], 0 offen sc1\n" \
"s_waitcnt vmcnt(0)\n" \
".LS3_w27c:\n" \
"v_subrev_u32 v241, s43, v240\n" \
"v_cmp_gt_u32 vcc, 0x100, v241\n" \
"s_cmp_eq_u64 vcc, -1\n" \
"s_cbranch_scc1 .LS3_w27d\n" \
"s_sleep 2\n" \
"s_add_u32 s50, s50, 1\n" \
"s_cmp_lt_u32 s50, 0x20000\n" \
"s_cbranch_scc1 .LS3_w27l\n" \
"s_branch .LS3_end\n" \
".LS3_w27d:\n" \
"v_readlane_b32 s43, v202, 2\n" \
"s_nop 4\n" \
"buffer_load_dwordx4 a[16:19], v201, s[8:11], s43 offen sc1\n" \
"buffer_load_dwordx4 a[20:23], v201, s[8:11], s43 offen offset:1024 sc1\n" \
"v_readlane_b32 s43, v202, 3\n" \
"s_nop 4\n" \
"buffer_load_dwordx4 a[24:27], v201, s[8:11], s43 offen sc1\n" \
"buffer_load_dwordx4 a[28:31], v201, s[8:11], s43 offen offset:1024 sc1\n" \
"v_readlane_b32 s43, v202, 4\n" \
"s_nop 4\n" \
"buffer_load_dwordx4 a[32:35], v201, s[8:11], s43 offen sc1\n" \
"buffer_load_dwordx4 a[36:39], v201, s[8:11], s43 offen offset:1024 sc1\n" \
"v_readlane_b32 s43, v202, 5\n" \
"s_nop 4\n" \
"buffer_load_dwordx4 a[40:43], v201, s[8:11], s43 offen sc1\n" \
"buffer_load_dwordx4 a[44:47], v201, s[8:11], s43 offen offset:1024 sc1\n" \
"s_add_u32 s43, s30, s34\n" \
"s_mov_b32 s50, 0\n" \
"s_branch .LS3_w28c\n" \
".LS3_w28l:\n" \
"buffer_load_dword v240, v204, s[4:7], 0 offen sc1\n" \
"s_waitcnt vmcnt(0)\n" \
".LS3_w28c:\n" \
"v_subrev_u32 v241, s43, v240\n" \
"v_cmp_gt_u32 vcc, 0x100, v241\n" \
"s_cmp_eq_u64 vcc, -1\n" \
"s_cbranch_scc1 .LS3_w28d\n" \
"s_sleep 2\n" \
"s_add_u32 s50, s50, 1\n" \
"s_cmp_lt_u32 s50, 0x20000\n" \
"s_cbranch_scc1 .LS3_w28l\n" \
"s_branch .LS3_end\n" \
".LS3_w28d:\n" \
"v_readlane_b32 s43, v202, 6\n" \
"s_nop 4\n" \
"buffer_load_dwordx4 a[48:51], v201, s[8:11], s43 offen sc1\n" \
"buffer_load_dwordx4 a[52:55], v201, s[8:11], s43 offen offset:1024 sc1\n" \
"v_readlane_b32 s43, v202, 7\n" \
"s_nop 4\n" \
"buffer_load_dwordx4 a[56:59], v201, s[8:11], s43 offen sc1\n" \
"buffer_load_dwordx4 a[60:63], v201, s[8:11], s43 offen offset:1024 sc1\n" \
"v_readlane_b32 s43, v202, 8\n" \
"s_nop 4\n" \
"buffer_load_dwordx4 a[64:67], v201, s[8:11], s43 offen sc1\n" \
"buffer_load_dwordx4 a[68:71], v201, s[8:11], s43 offen offset:1024 sc1\n" \
"v_readlane_b32 s43, v202, 9\n" \
"s_nop 4\n" \
"buffer_load_dwordx4 a[72:75], v201, s[8:11], s43 offen sc1\n" \
"buffer_load_dwordx4 a[76:79], v201, s[8:11], s43 offen offset:1024 sc1\n" \
"s_add_u32 s43, s30, s36\n" \
"s_mov_b32 s50, 0\n" \
"s_branch .LS3_w29c\n" \
".LS3_w29l:\n" \
"buffer_load_dword v240, v204, s[4:7], 0 offen sc1\n" \
"s_waitcnt vmcnt(0)\n" \
".LS3_w29c:\n" \
"v_subrev_u32 v241, s43, v240\n" \
"v_cmp_gt_u32 vcc, 0x100, v241\n" \
"s_cmp_eq_u64 vcc, -1\n" \
"s_cbranch_scc1 .LS3_w29d\n" \
"s_sleep 2\n" \
"s_add_u32 s50, s50, 1\n" \
"s_cmp_lt_u32 s50, 0x20000\n" \
"s_cbranch_scc1 .LS3_w29l\n" \
"s_branch .LS3_end\n" \
".LS3_w29d:\n" \
"v_readlane_b32 s43, v202, 10\n" \
"s_nop 4\n" \
"buffer_load_dwordx4 a[80:83], v201, s[8:11], s43 offen sc1\n" \
"buffer_load_dwordx4 a[84:87], v201, s[8:11], s43 offen offset:1024 sc1\n" \
"v_readlane_b32 s43, v202, 11\n" \
"s_nop 4\n" \
"buffer_load_dwordx4 a[88:91], v201, s[8:11], s43 offen sc1\n" \
"buffer_load_dwordx4 a[92:95], v201, s[8:11], s43 offen offset:1024 sc1\n" \
"v_readlane_b32 s43, v202, 12\n" \
"s_nop 4\n" \
"buffer_load_dwordx4 a[96:99], v201, s[8:11], s43 offen sc1\n" \
"buffer_load_dwordx4 a[100:103], v201, s[8:11], s43 offen offset:1024 sc1\n" \
"v_readlane_b32 s43, v202, 13\n" \
"s_nop 4\n" \
"buffer_load_dwordx4 a[104:107], v201, s[8:11], s43 offen sc1\n" \
"buffer_load_dwordx4 a[108:111], v201, s[8:11], s43 offen offset:1024 sc1\n" \
"s_add_u32 s43, s30, 7\n" \
"s_mov_b32 s50, 0\n" \
"s_branch .LS3_w30c\n" \
".LS3_w30l:\n" \
"buffer_load_dword v240, v204, s[4:7], 0 offen sc1\n" \
"s_waitcnt vmcnt(0)\n" \
".LS3_w30c:\n" \
"v_subrev_u32 v241, s43, v240\n" \
"v_cmp_gt_u32 vcc, 0x100, v241\n" \
"s_cmp_eq_u64 vcc, -1\n" \
"s_cbranch_scc1 .LS3_w30d\n" \
"s_sleep 2\n" \
"s_add_u32 s50, s50, 1\n" \
"s_cmp_lt_u32 s50, 0x20000\n" \
"s_cbranch_scc1 .LS3_w30l\n" \
"s_branch .LS3_end\n" \
".LS3_w30d:\n" \
"v_readlane_b32 s43, v202, 0\n" \
"s_nop 4\n" \
"buffer_load_dwordx4 a[0:3], v201, s[8:11], s43 offen sc1\n" \
"buffer_load_dwordx4 a[4:7], v201, s[8:11], s43 offen offset:1024 sc1\n" \
"v_readlane_b32 s43, v202, 1\n" \
"s_nop 4\n" \
"buffer_load_dwordx4 a[8:11], v201, s[8:11], s43 offen sc1\n" \
"buffer_load_dwordx4 a[12:15], v201, s[8:11], s43 offen offset:1024 sc1\n" \
"s_mov_b32 s33, -1\n" \
"s_add_u32 s43, s30, 1\n" \
"s_mov_b32 s50, 0\n" \
".LS3_w31l:\n" \
"buffer_load_dword v240, v205, s[4:7], 0 offen sc1\n" \
"s_waitcnt vmcnt(0)\n" \
".LS3_w31c:\n" \
"v_subrev_u32 v241, s43, v240\n" \
"v_cmp_gt_u32 vcc, 0x100, v241\n" \
"s_cmp_eq_u64 vcc, -1\n" \
"s_cbranch_scc1 .LS3_w31d\n" \
"s_sleep 2\n" \
"s_add_u32 s50, s50, 1\n" \
"s_cmp_lt_u32 s50, 0x20000\n" \
"s_cbranch_scc1 .LS3_w31l\n" \
"s_branch .LS3_end\n" \
".LS3_w31d:\n" \
"s_add_u32 s43, s30, 1\n" \
"s_mov_b32 s50, 0\n" \
".LS3_w32l:\n" \
"buffer_load_dword v240, v206, s[4:7], 0 offen sc1\n" \
"s_waitcnt vmcnt(0)\n" \
".LS3_w32c:\n" \
"v_subrev_u32 v241, s43, v240\n" \
"v_cmp_gt_u32 vcc, 0x100, v241\n" \
"s_cmp_eq_u64 vcc, -1\n" \
"s_cbranch_scc1 .LS3_w32d\n" \
"s_sleep 2\n" \
"s_add_u32 s50, s50, 1\n" \
"s_cmp_lt_u32 s50, 0x20000\n" \
"s_cbranch_scc1 .LS3_w32l\n" \
"s_branch .LS3_end\n" \
".LS3_w32d:\n" \
".LS3_pre:\n" \
"s_barrier\n" \
"ds_read_b96 v[248:250], v252\n" \
"s_waitcnt lgkmcnt(0)\n" \
"v_readfirstlane_b32 s44, v248\n" \
"v_readfirstlane_b32 s45, v249\n" \
"v_readfirstlane_b32 s46, v250\n" \
"s_mul_i32 s42, s25, s46\n" \
"s_lshr_b32 s42, s42, 12\n" \
".LS3_cu:\n" \
"s_sub_u32 s43, s24, s44\n" \
"s_cmp_lt_i32 s43, 0\n" \
"s_cbranch_scc0 .LS3_cud\n" \
"s_add_u32 s24, s24, s25\n" \
"s_xor_b32 s31, s31, s29\n" \
"s_xor_b32 s32, s32, s29\n" \
"s_xor_b32 s47, s47, s29\n" \
"s_add_u32 s39, s39, 1\n" \
"s_branch .LS3_cu\n" \
".LS3_cud:\n" \
".LS3_loop:\n" \
"s_add_u32 s43, s40, 0\n" \
"s_and_b32 s43, s43, 3\n" \
"s_bitcmp1_b32 s43, 0\n" \
"s_cselect_b32 s47, s32, s31\n" \
"s_lshl_b32 s43, s43, 19\n" \
"s_add_u32 s47, s47, s43\n" \
"s_add_u32 s43, s40, 2\n" \
"s_and_b32 s43, s43, 3\n" \
"s_bitcmp1_b32 s43, 0\n" \
"s_cselect_b32 s49, s32, s31\n" \
"s_lshl_b32 s43, s43, 19\n" \
"s_add_u32 s49, s49, s43\n" \
"s_sub_u32 s43, s24, s44\n" \
"s_max_i32 s43, s43, 0xfffd0000\n" \
"s_min_i32 s43, s43, 0x4000\n" \
"s_mul_i32 s43, s43, s46\n" \
"s_ashr_i32 s43, s43, 12\n" \
"s_add_u32 s41, s45, s43\n" \
"s_add_u32 s41, s41, 12\n" \
"s_branch .LS3_a1\n" \
".LS3_retry:\n" \
"s_add_u32 s51, s51, 1\n" \
"s_cmp_gt_u32 s51, 0x20000\n" \
"s_cbranch_scc1 .LS3_end\n" \
".LS3_a0:\n" \
"s_add_u32 s41, s41, s42\n" \
"s_add_u32 s24, s24, s25\n" \
"s_xor_b32 s31, s31, s29\n" \
"s_xor_b32 s32, s32, s29\n" \
"s_xor_b32 s47, s47, s29\n" \
"s_xor_b32 s48, s48, s29\n" \
"s_add_u32 s39, s39, 1\n" \
"buffer_store_dword v188, v213, s[4:7], s47 offen sc1\n" \
"buffer_store_dword v188, v215, s[4:7], s47 offen sc1\n" \
".p2align 6\n" \
".LS3_a1:\n" \
"s_memtime vcc\n" \
"s_waitcnt lgkmcnt(0)\n" \
"s_sub_u32 s43, s41, vcc_lo\n" \
"s_cmp_gt_i32 s43, 128\n" \
"s_cbranch_scc1 .LS3_a1\n" \
"s_cmp_lt_i32 s43, s37\n" \
"s_cbranch_scc1 .LS3_a0\n" \
"s_max_i32 s43, s43, 0\n" \
"s_and_b32 s43, s43, -4\n" \
"s_getpc_b64 vcc\n" \
".LS3_a3:\n" \
"s_sub_u32 s43, .LS3_a4-.LS3_a3, s43\n" \
"s_add_u32 vcc_lo, vcc_lo, s43\n" \
"s_addc_u32 vcc_hi, vcc_hi, 0\n" \
"s_setpc_b64 vcc\n" \
".rept 32\n" \
"s_nop 0\n" \
".endr\n" \
".LS3_a4:\n" \
"buffer_load_dwordx4 v[12:15], v210, s[4:7], s47 offen sc1\n" \
"s_nop 9\n" \
"buffer_load_dwordx4 v[0:3], v208, s[4:7], s47 offen sc1\n" \
"s_nop 9\n" \
"buffer_load_dwordx4 v[4:7], v208, s[4:7], s47 offen offset:1024 sc1\n" \
"s_nop 9\n" \
"buffer_load_dwordx4 v[8:11], v208, s[4:7], s47 offen offset:2048 sc1\n" \
"s_nop 9\n" \
"buffer_load_dwordx4 v[16:19], v209, s[4:7], s47 offen sc1\n" \
"s_nop 3\n" \
"buffer_load_dwordx4 v[20:23], v209, s[4:7], s47 offen offset:1024 sc1\n" \
"s_nop 3\n" \
"buffer_load_dwordx4 v[24:27], v209, s[4:7], s47 offen offset:2048 sc1\n" \
"s_lshl_b32 s43, s40, 7\n" \
"buffer_load_dwordx4 v[236:239], v207, s[4:7], s43 offen sc1\n" \
"s_add_u32 s35, s35, 1\n" \
"v_mov_b32 v28, 0\n" \
"v_mov_b32 v29, 0\n" \
"v_mov_b32 v30, 0\n" \
"v_mov_b32 v31, 0\n" \
"v_mov_b32 v32, 0\n" \
"v_mov_b32 v33, 0\n" \
"v_mov_b32 v34, 0\n" \
"v_mov_b32 v35, 0\n" \
"v_mov_b32 v36, 0\n" \
"v_mov_b32 v37, 0\n" \
"v_mov_b32 v38, 0\n" \
"v_mov_b32 v39, 0\n" \
"v_mov_b32 v40, 0\n" \
"v_mov_b32 v41, 0\n" \
"v_mov_b32 v42, 0\n" \
"v_mov_b32 v43, 0\n" \
"v_mov_b32 v68, 0\n" \
"v_mov_b32 v69, 0\n" \
"v_mov_b32 v70, 0\n" \
"v_mov_b32 v71, 0\n" \
"s_waitcnt vmcnt(7)\n" \
"v_smfmac_f32_16x16x64_bf16 v[36:39], v[12:15], a[0:7], v200\n" \
"v_smfmac_f32_16x16x64_bf16 v[40:43], v[12:15], a[8:15], v200\n" \
"v_max3_u32 v64, v12, v12, v13\n" \
"v_max3_u32 v64, v64, v14, v15\n" \
"s_waitcnt vmcnt(6)\n" \
"v_smfmac_f32_16x16x64_bf16 v[28:31], v[0:3], a[16:23], v200\n" \
"v_smfmac_f32_16x16x64_bf16 v[32:35], v[0:3], a[24:31], v200\n" \
"v_smfmac_f32_16x16x64_bf16 v[36:39], v[0:3], a[32:39], v200\n" \
"v_smfmac_f32_16x16x64_bf16 v[40:43], v[0:3], a[40:47], v200\n" \
"v_max3_u32 v64, v64, v0, v1\n" \
"v_max3_u32 v64, v64, v2, v3\n" \
"s_waitcnt vmcnt(5)\n" \
"v_smfmac_f32_16x16x64_bf16 v[28:31], v[4:7], a[48:55], v200\n" \
"v_smfmac_f32_16x16x64_bf16 v[32:35], v[4:7], a[56:63], v200\n" \
"v_smfmac_f32_16x16x64_bf16 v[36:39], v[4:7], a[64:71], v200\n" \
"v_smfmac_f32_16x16x64_bf16 v[40:43], v[4:7], a[72:79], v200\n" \
"v_max3_u32 v64, v64, v4, v5\n" \
"v_max3_u32 v64, v64, v6, v7\n" \
"s_waitcnt vmcnt(4)\n" \
"v_smfmac_f32_16x16x64_bf16 v[28:31], v[8:11], a[80:87], v200\n" \
"v_smfmac_f32_16x16x64_bf16 v[32:35], v[8:11], a[88:95], v200\n" \
"v_smfmac_f32_16x16x64_bf16 v[36:39], v[8:11], a[96:103], v200\n" \
"v_smfmac_f32_16x16x64_bf16 v[40:43], v[8:11], a[104:111], v200\n" \
"v_max3_u32 v64, v64, v8, v9\n" \
"v_max3_u32 v64, v64, v10, v11\n" \
"s_nop 2\n" \
"v_cvt_pk_bf16_f32 v128, v28, v29\n" \
"v_cvt_pk_bf16_f32 v129, v30, v31\n" \
"v_cvt_pk_bf16_f32 v130, v32, v33\n" \
"v_cvt_pk_bf16_f32 v131, v34, v35\n" \
"ds_write_b64 v211, v[128:129] offset:0\n" \
"v_cvt_pk_bf16_f32 v132, v36, v37\n" \
"v_cvt_pk_bf16_f32 v133, v38, v39\n" \
"ds_write_b64 v211, v[130:131] offset:128\n" \
"v_cvt_pk_bf16_f32 v134, v40, v41\n" \
"v_cvt_pk_bf16_f32 v135, v42, v43\n" \
"ds_write_b64 v214, v[132:133] offset:0\n" \
"ds_write_b64 v214, v[134:135] offset:128\n" \
"ds_read_b64_tr_b16 v[96:97], v212 offset:6912\n" \
"ds_read_b64_tr_b16 v[98:99], v212 offset:6928\n" \
"ds_read_b64_tr_b16 v[100:101], v212 offset:7168\n" \
"ds_read_b64_tr_b16 v[102:103], v212 offset:7184\n" \
"s_waitcnt vmcnt(1)\n" \
"v_max3_u32 v64, v64, v16, v17\n" \
"v_max3_u32 v64, v64, v18, v19\n" \
"v_max3_u32 v64, v64, v20, v21\n" \
"v_max3_u32 v64, v64, v22, v23\n" \
"v_max3_u32 v64, v64, v24, v25\n" \
"v_max3_u32 v64, v64, v26, v27\n" \
"v_cmp_eq_u32 vcc, -1, v64\n" \
"s_barrier\n" \
"ds_read_b64_tr_b16 v[72:73], v212 offset:0\n" \
"ds_read_b64_tr_b16 v[74:75], v212 offset:16\n" \
"ds_read_b64_tr_b16 v[76:77], v212 offset:256\n" \
"ds_read_b64_tr_b16 v[78:79], v212 offset:272\n" \
"ds_read_b64_tr_b16 v[80:81], v212 offset:2304\n" \
"ds_read_b64_tr_b16 v[82:83], v212 offset:2320\n" \
"ds_read_b64_tr_b16 v[84:85], v212 offset:2560\n" \
"ds_read_b64_tr_b16 v[86:87], v212 offset:2576\n" \
"s_cbranch_vccnz .LS3_veto\n" \
"s_waitcnt lgkmcnt(8)\n" \
"v_smfmac_f32_16x16x64_bf16 v[68:71], v[12:15], v[96:103], v200\n" \
"ds_read_b64_tr_b16 v[88:89], v212 offset:4608\n" \
"ds_read_b64_tr_b16 v[90:91], v212 offset:4624\n" \
"ds_read_b64_tr_b16 v[92:93], v212 offset:4864\n" \
"ds_read_b64_tr_b16 v[94:95], v212 offset:4880\n" \
"s_waitcnt lgkmcnt(8)\n" \
"v_smfmac_f32_16x16x64_bf16 v[68:71], v[0:3], v[72:79], v200\n" \
"s_waitcnt lgkmcnt(4)\n" \
"v_smfmac_f32_16x16x64_bf16 v[68:71], v[4:7], v[80:87], v200\n" \
"s_waitcnt lgkmcnt(0)\n" \
"v_smfmac_f32_16x16x64_bf16 v[68:71], v[8:11], v[88:95], v200\n" \
"s_barrier\n" \
"ds_read_b64_tr_b16 v[104:105], v212 offset:9216\n" \
"ds_read_b64_tr_b16 v[106:107], v212 offset:9232\n" \
"ds_read_b64_tr_b16 v[108:109], v212 offset:9472\n" \
"ds_read_b64_tr_b16 v[110:111], v212 offset:9488\n" \
"s_nop 2\n" \
"v_cmp_u_f32 vcc, v68, v68\n" \
"s_barrier\n" \
"ds_read_b64_tr_b16 v[112:113], v212 offset:11520\n" \
"ds_read_b64_tr_b16 v[114:115], v212 offset:11536\n" \
"ds_read_b64_tr_b16 v[116:117], v212 offset:11776\n" \
"ds_read_b64_tr_b16 v[118:119], v212 offset:11792\n" \
"s_cbranch_vccnz .LS3_veto2\n" \
".LS3_veto2r:\n" \
"s_barrier\n" \
"ds_read_b64_tr_b16 v[120:121], v212 offset:13824\n" \
"ds_read_b64_tr_b16 v[122:123], v212 offset:13840\n" \
"ds_read_b64_tr_b16 v[124:125], v212 offset:14080\n" \
"ds_read_b64_tr_b16 v[126:127], v212 offset:14096\n" \
"s_waitcnt lgkmcnt(8)\n" \
"v_smfmac_f32_16x16x64_bf16 v[68:71], v[16:19], v[104:111], v200\n" \
"s_waitcnt lgkmcnt(4)\n" \
"v_smfmac_f32_16x16x64_bf16 v[68:71], v[20:23], v[112:119], v200\n" \
"s_waitcnt lgkmcnt(0)\n" \
"v_smfmac_f32_16x16x64_bf16 v[68:71], v[24:27], v[120:127], v200\n" \
"s_nop 7\n" \
"v_add_f32_dpp v128, v68, v68 row_shl:4 row_mask:0xf bank_mask:0x5\n" \
"v_add_f32_dpp v129, v70, v70 row_shl:4 row_mask:0xf bank_mask:0x5\n" \
"v_add_f32_dpp v128, v69, v69 row_shr:4 row_mask:0xf bank_mask:0xa\n" \
"v_add_f32_dpp v129, v71, v71 row_shr:4 row_mask:0xf bank_mask:0xa\n" \
"s_nop 1\n" \
"v_permlane32_swap_b32 v128, v129\n" \
"v_add_f32 v128, v128, v129\n" \
"buffer_store_dword v128, v213, s[4:7], s47 offen sc1\n" \
"buffer_store_dword v128, v215, s[4:7], s47 offen sc1\n" \
"s_xor_b32 s43, s47, s29\n" \
"buffer_store_dword v128, v213, s[4:7], s43 offen sc1\n" \
"buffer_store_dword v128, v215, s[4:7], s43 offen sc1\n" \
"s_waitcnt lgkmcnt(0)\n" \
"s_barrier\n" \
"ds_read_b32 v64, v252 offset:16\n" \
"s_waitcnt lgkmcnt(0)\n" \
"v_readfirstlane_b32 s43, v64\n" \
"s_cmp_eq_u32 s43, s35\n" \
"s_cbranch_scc1 .LS3_retry\n" \
"v_cmp_u_f32 vcc, v128, v128\n" \
"s_cbranch_vccnz .LS3_nan\n" \
".LS3_nanr:\n" \
"s_waitcnt vmcnt(4)\n" \
"v_max_u32 v236, v236, v238\n" \
"v_max_u32 v237, v237, v239\n" \
"v_and_b32 v236, 0xfff, v236\n" \
"v_and_b32 v237, 0xfff, v237\n" \
"s_mov_b64 exec, 1\n" \
"ds_write_b64 v252, v[236:237] offset:24\n" \
"s_mov_b64 exec, -1\n" \
"buffer_store_dword v188, v213, s[4:7], s49 offen sc1\n" \
"buffer_store_dword v188, v215, s[4:7], s49 offen sc1\n" \
"s_xor_b32 s43, s49, s29\n" \
"buffer_store_dword v188, v213, s[4:7], s43 offen sc1\n" \
"buffer_store_dword v188, v215, s[4:7], s43 offen sc1\n" \
"s_waitcnt lgkmcnt(0)\n" \
"s_barrier\n" \
"ds_read_b64 v[246:247], v252 offset:24\n" \
"ds_read_b96 v[248:250], v252\n" \
"s_waitcnt lgkmcnt(0)\n" \
"v_readfirstlane_b32 s44, v248\n" \
"v_readfirstlane_b32 s45, v249\n" \
"v_readfirstlane_b32 s46, v250\n" \
"v_readfirstlane_b32 s26, v246\n" \
"v_readfirstlane_b32 s27, v247\n" \
"s_mul_i32 s42, s25, s46\n" \
"s_lshr_b32 s42, s42, 12\n" \
"s_add_u32 s40, s40, 1\n" \
"s_add_u32 s24, s24, s26\n" \
"s_add_u32 s24, s24, s27\n" \
"s_cmp_eq_u32 s40, 0\n" \
"s_cbranch_scc1 .LS3_wet\n" \
"s_cmp_lt_u32 s40, 63\n" \
"s_cbranch_scc1 .LS3_loop\n" \
".LS3_end:\n" \
"s_endpgm\n" \
".LS3_nan:\n" \
"v_cmp_u_f32 vcc, v128, v128\n" \
"v_mov_b32 v129, 0x7fc00001\n" \
"s_nop 1\n" \
"v_cndmask_b32 v128, v128, v129, vcc\n" \
"buffer_store_dword v128, v213, s[4:7], s47 offen sc1\n" \
"buffer_store_dword v128, v215, s[4:7], s47 offen sc1\n" \
"s_xor_b32 s43, s47, s29\n" \
"buffer_store_dword v128, v213, s[4:7], s43 offen sc1\n" \
"buffer_store_dword v128, v215, s[4:7], s43 offen sc1\n" \
"s_branch .LS3_nanr\n" \
".LS3_veto:\n" \
"v_mov_b32 v64, s35\n" \
"ds_write_b32 v252, v64 offset:20\n" \
"s_barrier\n" \
"s_barrier\n" \
"s_waitcnt lgkmcnt(0)\n" \
"s_barrier\n" \
"s_branch .LS3_retry\n" \
".LS3_veto2:\n" \
"s_cmp_eq_u32 s33, s40\n" \
"s_cbranch_scc1 .LS3_veto2r\n" \
"s_mov_b32 s33, s40\n" \
"v_mov_b32 v64, s35\n" \
"ds_write_b32 v252, v64 offset:20\n" \
"s_waitcnt lgkmcnt(0)\n" \
"s_barrier\n" \
"s_branch .LS3_retry\n" \
".p2align 6\n" \
"fk_role_P0:\n" \
"ds_read_b32 v200, v255 offset:0\n" \
"ds_read_b32 v201, v255 offset:256\n" \
"ds_read_b32 v202, v255 offset:512\n" \
"ds_read_b32 v203, v255 offset:768\n" \
"ds_read_b32 v204, v255 offset:1024\n" \
"ds_read_b32 v205, v255 offset:1280\n" \
"ds_read_b32 v206, v255 offset:1536\n" \
"ds_read_b32 v207, v255 offset:1792\n" \
"ds_read_b32 v208, v255 offset:2048\n" \
"ds_read_b32 v209, v255 offset:2304\n" \
"ds_read_b32 v210, v255 offset:2560\n" \
"ds_read_b32 v211, v255 offset:2816\n" \
"ds_read_b32 v212, v255 offset:3072\n" \
"ds_read_b32 v213, v255 offset:3328\n" \
"ds_read_b32 v214, v255 offset:3584\n" \
"ds_read_b32 v215, v255 offset:3840\n" \
"ds_read_b32 v216, v255 offset:4096\n" \
"ds_read_b32 v217, v255 offset:4352\n" \
"ds_read_b32 v218, v255 offset:4608\n" \
"ds_read_b32 v219, v255 offset:4864\n" \
"ds_read_b32 v220, v255 offset:5120\n" \
"ds_read_b32 v221, v255 offset:5376\n" \
"ds_read_b32 v222, v255 offset:5632\n" \
"ds_read_b32 v223, v255 offset:5888\n" \
"ds_read_b32 v254, v255 offset:7936\n" \
"s_waitcnt lgkmcnt(0)\n" \
"v_readlane_b32 s4, v254, 0\n" \
"v_readlane_b32 s5, v254, 1\n" \
"v_readlane_b32 s6, v254, 2\n" \
"v_readlane_b32 s7, v254, 3\n" \
"v_readlane_b32 s8, v254, 4\n" \
"v_readlane_b32 s9, v254, 5\n" \
"v_readlane_b32 s10, v254, 6\n" \
"v_readlane_b32 s11, v254, 7\n" \
"v_readlane_b32 s12, v254, 8\n" \
"v_readlane_b32 s13, v254, 9\n" \
"v_readlane_b32 s14, v254, 10\n" \
"v_readlane_b32 s15, v254, 11\n" \
"v_readlane_b32 s16, v254, 12\n" \
"v_readlane_b32 s17, v254, 13\n" \
"v_readlane_b32 s18, v254, 14\n" \
"v_readlane_b32 s19, v254, 15\n" \
"v_readlane_b32 s20, v254, 16\n" \
"v_readlane_b32 s21, v254, 17\n" \
"v_readlane_b32 s22, v254, 18\n" \
"v_readlane_b32 s23, v254, 19\n" \
"v_readlane_b32 s24, v254, 20\n" \
"v_readlane_b32 s25, v254, 21\n" \
"v_readlane_b32 s26, v254, 22\n" \
"v_readlane_b32 s27, v254, 23\n" \
"v_readlane_b32 s28, v254, 24\n" \
"v_readlane_b32 s29, v254, 25\n" \
"v_readlane_b32 s30, v254, 26\n" \
"v_readlane_b32 s31, v254, 27\n" \
"v_readlane_b32 s32, v254, 28\n" \
"v_readlane_b32 s33, v254, 29\n" \
"v_readlane_b32 s34, v254, 30\n" \
"v_readlane_b32 s35, v254, 31\n" \
"v_readlane_b32 s36, v254, 32\n" \
"v_readlane_b32 s37, v254, 33\n" \
"v_readlane_b32 s38, v254, 34\n" \
"v_readlane_b32 s39, v254, 35\n" \
"s_mov_b32 s40, 0\n" \
"s_mov_b32 s51, 0\n" \
"s_nop 0\n" \
"v_mov_b32 v252, s38\n" \
"s_nop 4\n" \
"v_mov_b32 v112, -1\n" \
"v_mov_b32 v113, -1\n" \
"v_mov_b32 v114, -1\n" \
"v_mov_b32 v115, -1\n" \
"v_mov_b32 v109, 0x7fc00001\n" \
"v_writelane_b32 v251, s6, 0\n" \
"v_writelane_b32 v251, s10, 1\n" \
"v_writelane_b32 v251, s14, 2\n" \
"v_writelane_b32 v251, s18, 3\n" \
"s_mov_b32 s6, 0\n" \
"s_mov_b32 s10, 0\n" \
"s_mov_b32 s14, 0\n" \
"s_mov_b32 s18, 0\n" \
"s_mov_b32 s40, -1\n" \
"s_branch .LP0_pre\n" \
".LP0_wet:\n" \
"s_waitcnt vmcnt(0)\n" \
"v_readlane_b32 s6, v251, 0\n" \
"v_readlane_b32 s10, v251, 1\n" \
"v_readlane_b32 s14, v251, 2\n" \
"v_readlane_b32 s18, v251, 3\n" \
"s_nop 4\n" \
"ds_read_b32 v232, v255 offset:7424\n" \
"ds_read_b32 v233, v255 offset:7680\n" \
"v_lshrrev_b32 v234, 4, v201\n" \
"v_and_b32 v234, 3, v234\n" \
"v_subrev_u32 v230, 512, v205\n" \
"s_add_u32 s43, s30, 1\n" \
"s_mov_b32 s50, 0\n" \
".LP0_w33l:\n" \
"buffer_load_dword v240, v230, s[4:7], 0 offen sc1\n" \
"s_waitcnt vmcnt(0)\n" \
".LP0_w33c:\n" \
"v_subrev_u32 v241, s43, v240\n" \
"v_cmp_gt_u32 vcc, 0x100, v241\n" \
"s_cmp_eq_u64 vcc, -1\n" \
"s_cbranch_scc1 .LP0_w33d\n" \
"s_sleep 2\n" \
"s_add_u32 s50, s50, 1\n" \
"s_cmp_lt_u32 s50, 0x20000\n" \
"s_cbranch_scc1 .LP0_w33l\n" \
"s_branch .LP0_end\n" \
".LP0_w33d:\n" \
"v_lshl_or_b32 v241, v241, 2, v234\n" \
"s_nop 1\n" \
"v_min_u32_dpp v241, v241, v241 row_ror:4 row_mask:0xf bank_mask:0xf\n" \
"s_nop 1\n" \
"v_min_u32_dpp v241, v241, v241 row_ror:8 row_mask:0xf bank_mask:0xf\n" \
"s_nop 1\n" \
"v_min_u32_dpp v241, v241, v241 quad_perm:[1,0,3,2] row_mask:0xf bank_mask:0xf\n" \
"s_nop 1\n" \
"v_min_u32_dpp v241, v241, v241 quad_perm:[2,3,0,1] row_mask:0xf bank_mask:0xf\n" \
"s_nop 0\n" \
"v_readlane_b32 s31, v241, 0\n" \
"v_readlane_b32 s43, v241, 16\n" \
"s_min_u32 s31, s31, s43\n" \
"v_readlane_b32 s43, v241, 32\n" \
"s_min_u32 s31, s31, s43\n" \
"v_readlane_b32 s43, v241, 48\n" \
"s_min_u32 s31, s31, s43\n" \
"v_subrev_u32 v230, 256, v205\n" \
"s_add_u32 s43, s30, 1\n" \
"s_mov_b32 s50, 0\n" \
".LP0_w34l:\n" \
"buffer_load_dword v240, v230, s[4:7], 0 offen sc1\n" \
"s_waitcnt vmcnt(0)\n" \
".LP0_w34c:\n" \
"v_subrev_u32 v241, s43, v240\n" \
"v_cmp_gt_u32 vcc, 0x100, v241\n" \
"s_cmp_eq_u64 vcc, -1\n" \
"s_cbranch_scc1 .LP0_w34d\n" \
"s_sleep 2\n" \
"s_add_u32 s50, s50, 1\n" \
"s_cmp_lt_u32 s50, 0x20000\n" \
"s_cbranch_scc1 .LP0_w34l\n" \
"s_branch .LP0_end\n" \
".LP0_w34d:\n" \
"v_lshl_or_b32 v241, v241, 2, v234\n" \
"s_nop 1\n" \
"v_min_u32_dpp v241, v241, v241 row_ror:4 row_mask:0xf bank_mask:0xf\n" \
"s_nop 1\n" \
"v_min_u32_dpp v241, v241, v241 row_ror:8 row_mask:0xf bank_mask:0xf\n" \
"s_nop 1\n" \
"v_min_u32_dpp v241, v241, v241 quad_perm:[1,0,3,2] row_mask:0xf bank_mask:0xf\n" \
"s_nop 1\n" \
"v_min_u32_dpp v241, v241, v241 quad_perm:[2,3,0,1] row_mask:0xf bank_mask:0xf\n" \
"s_nop 0\n" \
"v_readlane_b32 s32, v241, 0\n" \
"v_readlane_b32 s43, v241, 16\n" \
"s_min_u32 s32, s32, s43\n" \
"v_readlane_b32 s43, v241, 32\n" \
"s_min_u32 s32, s32, s43\n" \
"v_readlane_b32 s43, v241, 48\n" \
"s_min_u32 s32, s32, s43\n" \
"s_xor_b32 s29, s31, s32\n" \
"s_and_b32 s29, s29, 3\n" \
"s_lshl_b32 s29, s29, 13\n" \
"s_bitset1_b32 s29, 21\n" \
"s_and_b32 s31, s31, 3\n" \
"s_lshl_b32 s31, s31, 13\n" \
"s_bitcmp1_b32 s39, 0\n" \
"s_cselect_b32 s43, s29, 0\n" \
"s_xor_b32 s31, s31, s43\n" \
"s_xor_b32 s32, s31, 0x4000\n" \
"s_mov_b32 s39, 0\n" \
"v_mov_b32 v224, -1\n" \
"v_mov_b32 v225, -1\n" \
"v_mov_b32 v226, -1\n" \
"v_mov_b32 v227, -1\n" \
"s_waitcnt lgkmcnt(0)\n" \
"s_add_u32 s50, s31, 0\n" \
"buffer_store_dwordx4 v[224:227], v232, s[4:7], s50 offen sc1\n" \
"s_xor_b32 s43, s50, s29\n" \
"buffer_store_dwordx4 v[224:227], v232, s[4:7], s43 offen sc1\n" \
"s_add_u32 s50, s32, 524288\n" \
"buffer_store_dwordx4 v[224:227], v232, s[4:7], s50 offen sc1\n" \
"s_xor_b32 s43, s50, s29\n" \
"buffer_store_dwordx4 v[224:227], v232, s[4:7], s43 offen sc1\n" \
"s_add_u32 s50, s31, 1048576\n" \
"buffer_store_dwordx4 v[224:227], v232, s[4:7], s50 offen sc1\n" \
"s_xor_b32 s43, s50, s29\n" \
"buffer_store_dwordx4 v[224:227], v232, s[4:7], s43 offen sc1\n" \
"s_add_u32 s50, s32, 1572864\n" \
"buffer_store_dwordx4 v[224:227], v232, s[4:7], s50 offen sc1\n" \
"s_xor_b32 s43, s50, s29\n" \
"buffer_store_dwordx4 v[224:227], v232, s[4:7], s43 offen sc1\n" \
"s_waitcnt vmcnt(0)\n" \
"s_barrier\n" \
"s_add_u32 s43, s30, s40\n" \
"s_add_u32 s43, s43, 1\n" \
"v_mov_b32 v241, s43\n" \
"buffer_store_dword v241, v233, s[4:7], 0 offen sc1\n" \
"s_mov_b32 s33, 40\n" \
"s_mov_b32 s34, 0\n" \
"s_add_u32 s43, s30, 1\n" \
"s_mov_b32 s50, 0\n" \
".LP0_w35l:\n" \
"buffer_load_dword v240, v205, s[4:7], 0 offen sc1\n" \
"s_waitcnt vmcnt(0)\n" \
".LP0_w35c:\n" \
"v_subrev_u32 v241, s43, v240\n" \
"v_cmp_gt_u32 vcc, 0x100, v241\n" \
"s_cmp_eq_u64 vcc, -1\n" \
"s_cbranch_scc1 .LP0_w35d\n" \
"s_sleep 2\n" \
"s_add_u32 s50, s50, 1\n" \
"s_cmp_lt_u32 s50, 0x20000\n" \
"s_cbranch_scc1 .LP0_w35l\n" \
"s_branch .LP0_end\n" \
".LP0_w35d:\n" \
"s_add_u32 s43, s30, 1\n" \
"s_mov_b32 s50, 0\n" \
".LP0_w36l:\n" \
"buffer_load_dword v240, v206, s[4:7], 0 offen sc1\n" \
"s_waitcnt vmcnt(0)\n" \
".LP0_w36c:\n" \
"v_subrev_u32 v241, s43, v240\n" \
"v_cmp_gt_u32 vcc, 0x100, v241\n" \
"s_cmp_eq_u64 vcc, -1\n" \
"s_cbranch_scc1 .LP0_w36d\n" \
"s_sleep 2\n" \
"s_add_u32 s50, s50, 1\n" \
"s_cmp_lt_u32 s50, 0x20000\n" \
"s_cbranch_scc1 .LP0_w36l\n" \
"s_branch .LP0_end\n" \
".LP0_w36d:\n" \
"s_add_u32 s43, s40, 0\n" \
"s_and_b32 s43, s43, 3\n" \
"s_bitcmp1_b32 s43, 0\n" \
"s_cselect_b32 s47, s32, s31\n" \
"s_lshl_b32 s43, s43, 19\n" \
"s_add_u32 s47, s47, s43\n" \
"buffer_store_dword v219, v213, s[4:7], s47 offen sc1\n" \
"s_xor_b32 s43, s47, s29\n" \
"buffer_store_dword v219, v213, s[4:7], s43 offen sc1\n" \
"s_add_u32 s43, s30, 1\n" \
"s_mov_b32 s50, 0\n" \
".LP0_w37l:\n" \
"buffer_load_dword v240, v204, s[4:7], 0 offen sc1\n" \
"s_waitcnt vmcnt(0)\n" \
".LP0_w37c:\n" \
"v_subrev_u32 v241, s43, v240\n" \
"v_cmp_gt_u32 vcc, 0x100, v241\n" \
"s_cmp_eq_u64 vcc, -1\n" \
"s_cbranch_scc1 .LP0_w37d\n" \
"s_sleep 2\n" \
"s_add_u32 s50, s50, 1\n" \
"s_cmp_lt_u32 s50, 0x20000\n" \
"s_cbranch_scc1 .LP0_w37l\n" \
"s_branch .LP0_end\n" \
".LP0_w37d:\n" \
"v_readlane_b32 s43, v202, 0\n" \
"s_nop 4\n" \
"buffer_load_dwordx4 a[0:3], v201, s[8:11], s43 offen sc1\n" \
"buffer_load_dwordx4 a[4:7], v201, s[8:11], s43 offen offset:1024 sc1\n" \
"v_readlane_b32 s43, v202, 7\n" \
"s_nop 4\n" \
"buffer_load_dwordx4 a[56:59], v201, s[8:11], s43 offen sc1\n" \
"buffer_load_dwordx4 a[60:63], v201, s[8:11], s43 offen offset:1024 sc1\n" \
"v_readlane_b32 s43, v202, 14\n" \
"s_nop 4\n" \
"buffer_load_dwordx4 a[112:115], v201, s[8:11], s43 offen sc1\n" \
"buffer_load_dwordx4 a[116:119], v201, s[8:11], s43 offen offset:1024 sc1\n" \
"v_readlane_b32 s43, v202, 21\n" \
"s_nop 4\n" \
"buffer_load_dwordx4 a[168:171], v201, s[8:11], s43 offen sc1\n" \
"buffer_load_dwordx4 a[172:175], v201, s[8:11], s43 offen offset:1024 sc1\n" \
"s_add_u32 s43, s30, 2\n" \
"s_mov_b32 s50, 0\n" \
"s_branch .LP0_w38c\n" \
".LP0_w38l:\n" \
"buffer_load_dword v240, v204, s[4:7], 0 offen sc1\n" \
"s_waitcnt vmcnt(0)\n" \
".LP0_w38c:\n" \
"v_subrev_u32 v241, s43, v240\n" \
"v_cmp_gt_u32 vcc, 0x100, v241\n" \
"s_cmp_eq_u64 vcc, -1\n" \
"s_cbranch_scc1 .LP0_w38d\n" \
"s_sleep 2\n" \
"s_add_u32 s50, s50, 1\n" \
"s_cmp_lt_u32 s50, 0x20000\n" \
"s_cbranch_scc1 .LP0_w38l\n" \
"s_branch .LP0_end\n" \
".LP0_w38d:\n" \
"v_readlane_b32 s43, v202, 4\n" \
"s_nop 4\n" \
"buffer_load_dwordx4 a[32:35], v201, s[8:11], s43 offen sc1\n" \
"buffer_load_dwordx4 a[36:39], v201, s[8:11], s43 offen offset:1024 sc1\n" \
"v_readlane_b32 s43, v202, 11\n" \
"s_nop 4\n" \
"buffer_load_dwordx4 a[88:91], v201, s[8:11], s43 offen sc1\n" \
"buffer_load_dwordx4 a[92:95], v201, s[8:11], s43 offen offset:1024 sc1\n" \
"v_readlane_b32 s43, v202, 18\n" \
"s_nop 4\n" \
"buffer_load_dwordx4 a[144:147], v201, s[8:11], s43 offen sc1\n" \
"buffer_load_dwordx4 a[148:151], v201, s[8:11], s43 offen offset:1024 sc1\n" \
"v_readlane_b32 s43, v202, 25\n" \
"s_nop 4\n" \
"buffer_load_dwordx4 a[200:203], v201, s[8:11], s43 offen sc1\n" \
"buffer_load_dwordx4 a[204:207], v201, s[8:11], s43 offen offset:1024 sc1\n" \
"s_add_u32 s43, s30, 3\n" \
"s_mov_b32 s50, 0\n" \
"s_branch .LP0_w39c\n" \
".LP0_w39l:\n" \
"buffer_load_dword v240, v204, s[4:7], 0 offen sc1\n" \
"s_waitcnt vmcnt(0)\n" \
".LP0_w39c:\n" \
"v_subrev_u32 v241, s43, v240\n" \
"v_cmp_gt_u32 vcc, 0x100, v241\n" \
"s_cmp_eq_u64 vcc, -1\n" \
"s_cbranch_scc1 .LP0_w39d\n" \
"s_sleep 2\n" \
"s_add_u32 s50, s50, 1\n" \
"s_cmp_lt_u32 s50, 0x20000\n" \
"s_cbranch_scc1 .LP0_w39l\n" \
"s_branch .LP0_end\n" \
".LP0_w39d:\n" \
"v_readlane_b32 s43, v202, 1\n" \
"s_nop 4\n" \
"buffer_load_dwordx4 a[8:11], v201, s[8:11], s43 offen sc1\n" \
"buffer_load_dwordx4 a[12:15], v201, s[8:11], s43 offen offset:1024 sc1\n" \
"v_readlane_b32 s43, v202, 8\n" \
"s_nop 4\n" \
"buffer_load_dwordx4 a[64:67], v201, s[8:11], s43 offen sc1\n" \
"buffer_load_dwordx4 a[68:71], v201, s[8:11], s43 offen offset:1024 sc1\n" \
"v_readlane_b32 s43, v202, 15\n" \
"s_nop 4\n" \
"buffer_load_dwordx4 a[120:123], v201, s[8:11], s43 offen sc1\n" \
"buffer_load_dwordx4 a[124:127], v201, s[8:11], s43 offen offset:1024 sc1\n" \
"v_readlane_b32 s43, v202, 22\n" \
"s_nop 4\n" \
"buffer_load_dwordx4 a[176:179], v201, s[8:11], s43 offen sc1\n" \
"buffer_load_dwordx4 a[180:183], v201, s[8:11], s43 offen offset:1024 sc1\n" \
"s_add_u32 s43, s30, 4\n" \
"s_mov_b32 s50, 0\n" \
"s_branch .LP0_w40c\n" \
".LP0_w40l:\n" \
"buffer_load_dword v240, v204, s[4:7], 0 offen sc1\n" \
"s_waitcnt vmcnt(0)\n" \
".LP0_w40c:\n" \
"v_subrev_u32 v241, s43, v240\n" \
"v_cmp_gt_u32 vcc, 0x100, v241\n" \
"s_cmp_eq_u64 vcc, -1\n" \
"s_cbranch_scc1 .LP0_w40d\n" \
"s_sleep 2\n" \
"s_add_u32 s50, s50, 1\n" \
"s_cmp_lt_u32 s50, 0x20000\n" \
"s_cbranch_scc1 .LP0_w40l\n" \
"s_branch .LP0_end\n" \
".LP0_w40d:\n" \
"v_readlane_b32 s43, v202, 5\n" \
"s_nop 4\n" \
"buffer_load_dwordx4 a[40:43], v201, s[8:11], s43 offen sc1\n" \
"buffer_load_dwordx4 a[44:47], v201, s[8:11], s43 offen offset:1024 sc1\n" \
"v_readlane_b32 s43, v202, 12\n" \
"s_nop 4\n" \
"buffer_load_dwordx4 a[96:99], v201, s[8:11], s43 offen sc1\n" \
"buffer_load_dwordx4 a[100:103], v201, s[8:11], s43 offen offset:1024 sc1\n" \
"v_readlane_b32 s43, v202, 19\n" \
"s_nop 4\n" \
"buffer_load_dwordx4 a[152:155], v201, s[8:11], s43 offen sc1\n" \
"buffer_load_dwordx4 a[156:159], v201, s[8:11], s43 offen offset:1024 sc1\n" \
"v_readlane_b32 s43, v202, 26\n" \
"s_nop 4\n" \
"buffer_load_dwordx4 a[208:211], v201, s[8:11], s43 offen sc1\n" \
"buffer_load_dwordx4 a[212:215], v201, s[8:11], s43 offen offset:1024 sc1\n" \
"s_add_u32 s43, s30, 5\n" \
"s_mov_b32 s50, 0\n" \
"s_branch .LP0_w41c\n" \
".LP0_w41l:\n" \
"buffer_load_dword v240, v204, s[4:7], 0 offen sc1\n" \
"s_waitcnt vmcnt(0)\n" \
".LP0_w41c:\n" \
"v_subrev_u32 v241, s43, v240\n" \
"v_cmp_gt_u32 vcc, 0x100, v241\n" \
"s_cmp_eq_u64 vcc, -1\n" \
"s_cbranch_scc1 .LP0_w41d\n" \
"s_sleep 2\n" \
"s_add_u32 s50, s50, 1\n" \
"s_cmp_lt_u32 s50, 0x20000\n" \
"s_cbranch_scc1 .LP0_w41l\n" \
"s_branch .LP0_end\n" \
".LP0_w41d:\n" \
"v_readlane_b32 s43, v202, 2\n" \
"s_nop 4\n" \
"buffer_load_dwordx4 a[16:19], v201, s[8:11], s43 offen sc1\n" \
"buffer_load_dwordx4 a[20:23], v201, s[8:11], s43 offen offset:1024 sc1\n" \
"v_readlane_b32 s43, v202, 9\n" \
"s_nop 4\n" \
"buffer_load_dwordx4 a[72:75], v201, s[8:11], s43 offen sc1\n" \
"buffer_load_dwordx4 a[76:79], v201, s[8:11], s43 offen offset:1024 sc1\n" \
"v_readlane_b32 s43, v202, 16\n" \
"s_nop 4\n" \
"buffer_load_dwordx4 a[128:131], v201, s[8:11], s43 offen sc1\n" \
"buffer_load_dwordx4 a[132:135], v201, s[8:11], s43 offen offset:1024 sc1\n" \
"v_readlane_b32 s43, v202, 23\n" \
"s_nop 4\n" \
"buffer_load_dwordx4 a[184:187], v201, s[8:11], s43 offen sc1\n" \
"buffer_load_dwordx4 a[188:191], v201, s[8:11], s43 offen offset:1024 sc1\n" \
"s_add_u32 s43, s30, 6\n" \
"s_mov_b32 s50, 0\n" \
"s_branch .LP0_w42c\n" \
".LP0_w42l:\n" \
"buffer_load_dword v240, v204, s[4:7], 0 offen sc1\n" \
"s_waitcnt vmcnt(0)\n" \
".LP0_w42c:\n" \
"v_subrev_u32 v241, s43, v240\n" \
"v_cmp_gt_u32 vcc, 0x100, v241\n" \
"s_cmp_eq_u64 vcc, -1\n" \
"s_cbranch_scc1 .LP0_w42d\n" \
"s_sleep 2\n" \
"s_add_u32 s50, s50, 1\n" \
"s_cmp_lt_u32 s50, 0x20000\n" \
"s_cbranch_scc1 .LP0_w42l\n" \
"s_branch .LP0_end\n" \
".LP0_w42d:\n" \
"v_readlane_b32 s43, v202, 6\n" \
"s_nop 4\n" \
"buffer_load_dwordx4 a[48:51], v201, s[8:11], s43 offen sc1\n" \
"buffer_load_dwordx4 a[52:55], v201, s[8:11], s43 offen offset:1024 sc1\n" \
"v_readlane_b32 s43, v202, 13\n" \
"s_nop 4\n" \
"buffer_load_dwordx4 a[104:107], v201, s[8:11], s43 offen sc1\n" \
"buffer_load_dwordx4 a[108:111], v201, s[8:11], s43 offen offset:1024 sc1\n" \
"v_readlane_b32 s43, v202, 20\n" \
"s_nop 4\n" \
"buffer_load_dwordx4 a[160:163], v201, s[8:11], s43 offen sc1\n" \
"buffer_load_dwordx4 a[164:167], v201, s[8:11], s43 offen offset:1024 sc1\n" \
"v_readlane_b32 s43, v202, 27\n" \
"s_nop 4\n" \
"buffer_load_dwordx4 a[216:219], v201, s[8:11], s43 offen sc1\n" \
"buffer_load_dwordx4 a[220:223], v201, s[8:11], s43 offen offset:1024 sc1\n" \
"s_add_u32 s43, s30, 7\n" \
"s_mov_b32 s50, 0\n" \
"s_branch .LP0_w43c\n" \
".LP0_w43l:\n" \
"buffer_load_dword v240, v204, s[4:7], 0 offen sc1\n" \
"s_waitcnt vmcnt(0)\n" \
".LP0_w43c:\n" \
"v_subrev_u32 v241, s43, v240\n" \
"v_cmp_gt_u32 vcc, 0x100, v241\n" \
"s_cmp_eq_u64 vcc, -1\n" \
"s_cbranch_scc1 .LP0_w43d\n" \
"s_sleep 2\n" \
"s_add_u32 s50, s50, 1\n" \
"s_cmp_lt_u32 s50, 0x20000\n" \
"s_cbranch_scc1 .LP0_w43l\n" \
"s_branch .LP0_end\n" \
".LP0_w43d:\n" \
"v_readlane_b32 s43, v202, 3\n" \
"s_nop 4\n" \
"buffer_load_dwordx4 a[24:27], v201, s[8:11], s43 offen sc1\n" \
"buffer_load_dwordx4 a[28:31], v201, s[8:11], s43 offen offset:1024 sc1\n" \
"v_readlane_b32 s43, v202, 10\n" \
"s_nop 4\n" \
"buffer_load_dwordx4 a[80:83], v201, s[8:11], s43 offen sc1\n" \
"buffer_load_dwordx4 a[84:87], v201, s[8:11], s43 offen offset:1024 sc1\n" \
"v_readlane_b32 s43, v202, 17\n" \
"s_nop 4\n" \
"buffer_load_dwordx4 a[136:139], v201, s[8:11], s43 offen sc1\n" \
"buffer_load_dwordx4 a[140:143], v201, s[8:11], s43 offen offset:1024 sc1\n" \
"v_readlane_b32 s43, v202, 24\n" \
"s_nop 4\n" \
"buffer_load_dwordx4 a[192:195], v201, s[8:11], s43 offen sc1\n" \
"buffer_load_dwordx4 a[196:199], v201, s[8:11], s43 offen offset:1024 sc1\n" \
".LP0_pre:\n" \
"s_waitcnt vmcnt(0)\n" \
"s_barrier\n" \
"ds_read_b96 v[248:250], v252\n" \
"s_waitcnt lgkmcnt(0)\n" \
"v_readfirstlane_b32 s44, v248\n" \
"v_readfirstlane_b32 s45, v249\n" \
"v_readfirstlane_b32 s46, v250\n" \
"s_mul_i32 s42, s25, s46\n" \
"s_lshr_b32 s42, s42, 12\n" \
".LP0_cu:\n" \
"s_sub_u32 s43, s24, s44\n" \
"s_cmp_lt_i32 s43, 0\n" \
"s_cbranch_scc0 .LP0_cud\n" \
"s_add_u32 s24, s24, s25\n" \
"s_xor_b32 s31, s31, s29\n" \
"s_xor_b32 s32, s32, s29\n" \
"s_xor_b32 s47, s47, s29\n" \
"s_add_u32 s39, s39, 1\n" \
"s_branch .LP0_cu\n" \
".LP0_cud:\n" \
"s_mov_b32 s3, s46\n" \
"s_add_u32 s43, s40, 1\n" \
"s_and_b32 s43, s43, 3\n" \
"s_bitcmp1_b32 s43, 0\n" \
"s_cselect_b32 s48, s32, s31\n" \
"s_lshl_b32 s43, s43, 19\n" \
"s_add_u32 s48, s48, s43\n" \
"s_add_u32 s43, s40, 2\n" \
"s_and_b32 s43, s43, 3\n" \
"s_bitcmp1_b32 s43, 0\n" \
"s_cselect_b32 s49, s32, s31\n" \
"s_lshl_b32 s43, s43, 19\n" \
"s_add_u32 s49, s49, s43\n" \
"s_sub_u32 s43, s24, s44\n" \
"s_add_u32 s43, s43, s28\n" \
"s_max_i32 s43, s43, 0xfffd0000\n" \
"s_min_i32 s43, s43, 0x4000\n" \
"s_mul_i32 s43, s43, s46\n" \
"s_ashr_i32 s43, s43, 12\n" \
"s_add_u32 s41, s45, s43\n" \
"s_lshr_b32 s35, s42, 1\n" \
"s_sub_u32 s35, 0, s35\n" \
".LP0_loop:\n" \
"s_branch .LP0_m1\n" \
".LP0_retry1:\n" \
"s_add_u32 s51, s51, 1\n" \
"s_cmp_gt_u32 s51, 0x20000\n" \
"s_cbranch_scc1 .LP0_end\n" \
".LP0_m0:\n" \
"s_add_u32 s41, s41, s42\n" \
"s_add_u32 s24, s24, s25\n" \
"s_xor_b32 s31, s31, s29\n" \
"s_xor_b32 s32, s32, s29\n" \
"s_xor_b32 s47, s47, s29\n" \
"s_xor_b32 s48, s48, s29\n" \
"s_add_u32 s39, s39, 1\n" \
"s_bitcmp1_b32 s31, 21\n" \
"s_cselect_b32 s43, s29, 0\n" \
"v_mov_b32 v116, s43\n" \
"ds_write_b32 v252, v116 offset:16\n" \
".LP0_m1:\n" \
"s_memtime vcc\n" \
"s_waitcnt lgkmcnt(0)\n" \
"s_sub_u32 s43, s41, vcc_lo\n" \
"s_cmp_gt_i32 s43, 0\n" \
"s_cbranch_scc1 .LP0_m1\n" \
"s_cmp_lt_i32 s43, s35\n" \
"s_cbranch_scc1 .LP0_m0\n" \
"buffer_load_dwordx4 v[0:3], v208, s[4:7], s47 offen sc1\n" \
"buffer_load_dwordx4 v[4:7], v208, s[4:7], s47 offen offset:1024 sc1\n" \
"s_nop 15\n" \
"s_nop 13\n" \
"s_mul_i32 s50, s40, 114688\n" \
"buffer_store_dwordx4 v[68:71], v218, s[12:15], s50 offen sc1\n" \
"buffer_store_dword v112, v213, s[4:7], s49 offen sc1\n" \
"s_xor_b32 s43, s49, s29\n" \
"buffer_store_dword v112, v213, s[4:7], s43 offen sc1\n" \
"s_add_u32 s43, s30, s40\n" \
"s_add_u32 s43, s43, -1\n" \
"v_mov_b32 v241, s43\n" \
"buffer_store_dword v241, v221, s[4:7], 0 offen sc1\n" \
"s_bitcmp1_b32 s31, 21\n" \
"s_cselect_b32 s43, s29, 0\n" \
"v_mov_b32 v116, s43\n" \
"ds_write_b32 v252, v116 offset:16\n" \
"v_mov_b32 v28, 0\n" \
"v_mov_b32 v29, 0\n" \
"v_mov_b32 v30, 0\n" \
"v_mov_b32 v31, 0\n" \
"v_mov_b32 v32, 0\n" \
"v_mov_b32 v33, 0\n" \
"v_mov_b32 v34, 0\n" \
"v_mov_b32 v35, 0\n" \
"v_mov_b32 v36, 0\n" \
"v_mov_b32 v37, 0\n" \
"v_mov_b32 v38, 0\n" \
"v_mov_b32 v39, 0\n" \
"v_mov_b32 v40, 0\n" \
"v_mov_b32 v41, 0\n" \
"v_mov_b32 v42, 0\n" \
"v_mov_b32 v43, 0\n" \
"s_waitcnt vmcnt(5)\n" \
"ds_write_b128 v220, v[0:3] offset:0\n" \
"s_waitcnt vmcnt(4)\n" \
"ds_write_b128 v220, v[4:7] offset:1024\n" \
"v_max3_u32 v44, v0, v0, v1\n" \
"v_max3_u32 v44, v44, v2, v3\n" \
"v_max3_u32 v44, v44, v4, v5\n" \
"v_max3_u32 v44, v44, v6, v7\n" \
"v_cmp_eq_u32 vcc, -1, v44\n" \
"v_smfmac_f32_16x16x64_bf16 v[28:31], v[0:3], a[0:7], v200\n" \
"v_smfmac_f32_16x16x64_bf16 v[32:35], v[0:3], a[56:63], v200\n" \
"s_cbranch_vccnz .LP0_retry1\n" \
"s_waitcnt lgkmcnt(0)\n" \
"s_barrier\n" \
"ds_read_b128 v[8:11], v220 offset:2048\n" \
"ds_read_b128 v[12:15], v220 offset:3072\n" \
"ds_read_b128 v[16:19], v220 offset:4096\n" \
"ds_read_b128 v[20:23], v220 offset:5120\n" \
"ds_read_b128 v[24:27], v220 offset:6144\n" \
"ds_read_b64 v[246:247], v252 offset:24\n" \
"ds_read_b96 v[248:250], v252\n" \
"v_smfmac_f32_16x16x64_bf16 v[36:39], v[0:3], a[112:119], v200\n" \
"v_smfmac_f32_16x16x64_bf16 v[40:43], v[0:3], a[168:175], v200\n" \
"v_smfmac_f32_16x16x64_bf16 v[28:31], v[4:7], a[8:15], v200\n" \
"v_smfmac_f32_16x16x64_bf16 v[32:35], v[4:7], a[64:71], v200\n" \
"v_smfmac_f32_16x16x64_bf16 v[36:39], v[4:7], a[120:127], v200\n" \
"v_smfmac_f32_16x16x64_bf16 v[40:43], v[4:7], a[176:183], v200\n" \
"s_waitcnt lgkmcnt(6)\n" \
"v_smfmac_f32_16x16x64_bf16 v[28:31], v[8:11], a[16:23], v200\n" \
"v_smfmac_f32_16x16x64_bf16 v[32:35], v[8:11], a[72:79], v200\n" \
"v_smfmac_f32_16x16x64_bf16 v[36:39], v[8:11], a[128:135], v200\n" \
"v_smfmac_f32_16x16x64_bf16 v[40:43], v[8:11], a[184:191], v200\n" \
"s_waitcnt lgkmcnt(0)\n" \
"v_readfirstlane_b32 s44, v248\n" \
"v_readfirstlane_b32 s45, v249\n" \
"v_smfmac_f32_16x16x64_bf16 v[28:31], v[12:15], a[24:31], v200\n" \
"v_readfirstlane_b32 s46, v250\n" \
"v_readfirstlane_b32 s26, v246\n" \
"v_readfirstlane_b32 s27, v247\n" \
"v_smfmac_f32_16x16x64_bf16 v[32:35], v[12:15], a[80:87], v200\n" \
"s_mul_i32 s42, s25, s46\n" \
"s_lshr_b32 s42, s42, 12\n" \
"s_add_u32 s24, s24, s26\n" \
"v_smfmac_f32_16x16x64_bf16 v[36:39], v[12:15], a[136:143], v200\n" \
"s_sub_u32 s43, s24, s44\n" \
"s_max_i32 s43, s43, 0xfffd0000\n" \
"s_min_i32 s43, s43, 0x4000\n" \
"v_smfmac_f32_16x16x64_bf16 v[40:43], v[12:15], a[192:199], v200\n" \
"s_mul_i32 s43, s43, s46\n" \
"s_ashr_i32 s43, s43, 12\n" \
"s_add_u32 s41, s45, s43\n" \
"s_cmp_eq_u32 s40, 63\n" \
"s_cbranch_scc1 .LP0_last\n" \
"s_branch .LP0_z1\n" \
".LP0_z0:\n" \
"s_add_u32 s41, s41, s42\n" \
"s_add_u32 s24, s24, s25\n" \
"s_xor_b32 s31, s31, s29\n" \
"s_xor_b32 s32, s32, s29\n" \
"s_xor_b32 s47, s47, s29\n" \
"s_xor_b32 s48, s48, s29\n" \
"s_add_u32 s39, s39, 1\n" \
"buffer_store_dwordx4 v[112:115], v223, s[4:7], s48 offen sc1\n" \
"s_bitcmp1_b32 s31, 21\n" \
"s_cselect_b32 s43, s29, 0\n" \
"v_mov_b32 v116, s43\n" \
"ds_write_b32 v252, v116 offset:16\n" \
".LP0_z1:\n" \
"s_memtime vcc\n" \
"s_waitcnt lgkmcnt(0)\n" \
"s_sub_u32 s43, s41, vcc_lo\n" \
"s_cmp_gt_i32 s43, 200\n" \
"s_cbranch_scc1 .LP0_z1\n" \
"s_cmp_lt_i32 s43, s37\n" \
"s_cbranch_scc1 .LP0_z0\n" \
"buffer_load_dwordx2 v[64:65], v211, s[4:7], s47 offen sc1\n" \
".p2align 6\n" \
".LP0_b1:\n" \
"s_memtime vcc\n" \
"s_waitcnt lgkmcnt(0)\n" \
"s_sub_u32 s43, s41, vcc_lo\n" \
"s_cmp_gt_i32 s43, 128\n" \
"s_cbranch_scc1 .LP0_b1\n" \
"s_cmp_lt_i32 s43, s37\n" \
"s_cbranch_scc1 .LP0_z0\n" \
"s_max_i32 s43, s43, 0\n" \
"s_and_b32 s43, s43, -4\n" \
"s_getpc_b64 vcc\n" \
".LP0_b3:\n" \
"s_sub_u32 s43, .LP0_b4-.LP0_b3, s43\n" \
"s_add_u32 vcc_lo, vcc_lo, s43\n" \
"s_addc_u32 vcc_hi, vcc_hi, 0\n" \
"s_setpc_b64 vcc\n" \
".rept 32\n" \
"s_nop 0\n" \
".endr\n" \
".LP0_b4:\n" \
"buffer_load_dwordx4 v[56:59], v210, s[4:7], s47 offen sc1\n" \
"buffer_load_dwordx4 v[60:63], v210, s[4:7], s47 offen offset:256 sc1\n" \
"v_smfmac_f32_16x16x64_bf16 v[28:31], v[16:19], a[32:39], v200\n" \
"v_smfmac_f32_16x16x64_bf16 v[32:35], v[16:19], a[88:95], v200\n" \
"v_smfmac_f32_16x16x64_bf16 v[36:39], v[16:19], a[144:151], v200\n" \
"v_smfmac_f32_16x16x64_bf16 v[40:43], v[16:19], a[200:207], v200\n" \
"v_smfmac_f32_16x16x64_bf16 v[28:31], v[20:23], a[40:47], v200\n" \
"v_smfmac_f32_16x16x64_bf16 v[32:35], v[20:23], a[96:103], v200\n" \
"buffer_load_dwordx4 v[50:53], v212, s[4:7], s47 offen sc1\n" \
"v_smfmac_f32_16x16x64_bf16 v[36:39], v[20:23], a[152:159], v200\n" \
"v_smfmac_f32_16x16x64_bf16 v[40:43], v[20:23], a[208:215], v200\n" \
"v_smfmac_f32_16x16x64_bf16 v[28:31], v[24:27], a[48:55], v200\n" \
"v_smfmac_f32_16x16x64_bf16 v[32:35], v[24:27], a[104:111], v200\n" \
"v_smfmac_f32_16x16x64_bf16 v[36:39], v[24:27], a[160:167], v200\n" \
"v_smfmac_f32_16x16x64_bf16 v[40:43], v[24:27], a[216:223], v200\n" \
"s_nop 2\n" \
"v_mov_b32 v245, v245\n" \
"v_mov_b32 v245, v245\n" \
"v_mov_b32 v245, v245\n" \
"v_permlane32_swap_b32 v28, v32\n" \
"v_permlane32_swap_b32 v29, v33\n" \
"v_permlane32_swap_b32 v30, v34\n" \
"v_permlane32_swap_b32 v31, v35\n" \
"v_add_f32 v28, v28, v32\n" \
"v_add_f32 v29, v29, v33\n" \
"v_add_f32 v30, v30, v34\n" \
"v_add_f32 v31, v31, v35\n" \
"v_cvt_pk_bf16_f32 v68, v28, v29\n" \
"v_cvt_pk_bf16_f32 v69, v30, v31\n" \
"v_permlane32_swap_b32 v36, v40\n" \
"v_permlane32_swap_b32 v37, v41\n" \
"v_permlane32_swap_b32 v38, v42\n" \
"v_permlane32_swap_b32 v39, v43\n" \
"v_add_f32 v36, v36, v40\n" \
"v_add_f32 v37, v37, v41\n" \
"v_add_f32 v38, v38, v42\n" \
"v_add_f32 v39, v39, v43\n" \
"v_cvt_pk_bf16_f32 v70, v36, v37\n" \
"v_cvt_pk_bf16_f32 v71, v38, v39\n" \
"v_mov_b32 v48, v68\n" \
"v_mov_b32 v49, v69\n" \
"v_mov_b32 v54, v70\n" \
"v_mov_b32 v55, v71\n" \
"s_nop 1\n" \
"v_permlane16_swap_b32 v48, v54\n" \
"v_permlane16_swap_b32 v49, v55\n" \
".LP0_wait:\n" \
"s_waitcnt vmcnt(1)\n" \
"v_pk_add_f32 v[72:73], v[56:57], v[60:61]\n" \
"v_pk_add_f32 v[74:75], v[58:59], v[62:63]\n" \
"v_max3_f32 v77, v72, v73, v64\n" \
"v_max3_f32 v77, v77, v74, v75\n" \
"s_nop 1\n" \
"v_max_f32_dpp v76, v77, v77 row_ror:8 row_mask:0xf bank_mask:0xf\n" \
"v_pk_add_f32 v[72:73], v[72:73], v[76:77] op_sel_hi:[1,0] neg_lo:[0,1] neg_hi:[0,1]\n" \
"v_pk_add_f32 v[74:75], v[74:75], v[76:77] op_sel_hi:[1,0] neg_lo:[0,1] neg_hi:[0,1]\n" \
"v_sub_f32 v78, v64, v76\n" \
"v_exp_f32 v72, v72\n" \
"v_exp_f32 v73, v73\n" \
"v_exp_f32 v74, v74\n" \
"v_exp_f32 v75, v75\n" \
"v_exp_f32 v78, v78\n" \
"v_pk_add_f32 v[80:81], v[72:73], v[74:75]\n" \
"v_fma_f32 v80, v78, v65, v80\n" \
"v_add_f32 v81, v80, v81\n" \
"s_nop 1\n" \
"v_add_f32_dpp v80, v81, v81 row_ror:8 row_mask:0xf bank_mask:0xf\n" \
"v_rcp_f32 v80, v80\n" \
"s_nop 0\n" \
"v_mul_f32 v78, v78, v80\n" \
"v_pk_mul_f32 v[72:73], v[72:73], v[80:81] op_sel_hi:[1,0]\n" \
"v_pk_mul_f32 v[74:75], v[74:75], v[80:81] op_sel_hi:[1,0]\n" \
"v_cvt_pk_bf16_f32 v78, v78, v78\n" \
"v_cvt_pk_bf16_f32 v82, v72, v73\n" \
"v_cvt_pk_bf16_f32 v83, v74, v75\n" \
"v_and_b32 v84, v78, v214\n" \
"v_and_b32 v85, v78, v215\n" \
"v_cvt_pk_bf16_f32 v86, v72, v73\n" \
"v_cvt_pk_bf16_f32 v87, v74, v75\n" \
"s_waitcnt vmcnt(0)\n" \
"v_mfma_f32_16x16x32_bf16 v[88:91], v[48:51], v[82:85], 0\n" \
"v_mfma_f32_16x16x32_bf16 v[92:95], v[52:55], v[84:87], 0\n" \
"s_nop 2\n" \
"v_mov_b32 v245, v245\n" \
"s_nop 2\n" \
"v_mov_b32 v245, v245\n" \
"v_add_f32_dpp v96, v92, v88 row_ror:8 row_mask:0xf bank_mask:0x3\n" \
"v_add_f32_dpp v97, v93, v89 row_ror:8 row_mask:0xf bank_mask:0x3\n" \
"v_add_f32_dpp v96, v90, v94 row_ror:8 row_mask:0xf bank_mask:0xc\n" \
"v_add_f32_dpp v97, v91, v95 row_ror:8 row_mask:0xf bank_mask:0xc\n" \
"ds_write_b64 v216, v[96:97]\n" \
"v_cmp_u_f32 vcc, v96, v97\n" \
"s_bitcmp1_b32 s31, 21\n" \
"s_cselect_b32 s18, s29, 0\n" \
"s_xor_b32 s18, s18, s48\n" \
"v_mov_b32 v245, v245\n" \
"s_xor_b32 s19, s18, s29\n" \
"s_nop 0\n" \
"s_nop 0\n" \
"v_mov_b32 v245, v245\n" \
"s_nop 0\n" \
"s_nop 0\n" \
"s_nop 0\n" \
"v_mov_b32 v245, v245\n" \
"s_cbranch_vccnz .LP0_cls\n" \
".LP0_clsr:\n" \
"s_waitcnt lgkmcnt(0)\n" \
"s_barrier\n" \
"ds_read_b64 v[100:101], v217 offset:0\n" \
"ds_read_b64 v[102:103], v217 offset:512\n" \
"ds_read_b32 v116, v252 offset:16\n" \
"ds_read_b64 v[104:105], v217 offset:1024\n" \
"ds_read_b64 v[106:107], v217 offset:1536\n" \
"v_mov_b32 v245, v245\n" \
"v_mov_b32 v245, v245\n" \
"s_add_u32 s40, s40, 1\n" \
"s_add_u32 s24, s24, s27\n" \
"s_mov_b32 s47, s48\n" \
"v_mov_b32 v245, v245\n" \
"s_waitcnt lgkmcnt(2)\n" \
"v_xad_u32 v117, v116, s18, v213\n" \
"v_pk_add_f32 v[100:101], v[100:101], v[102:103]\n" \
"s_waitcnt lgkmcnt(0)\n" \
"v_pk_add_f32 v[104:105], v[104:105], v[106:107]\n" \
"v_pk_add_f32 v[100:101], v[100:101], v[104:105]\n" \
"v_cvt_pk_bf16_f32 v96, v100, v101\n" \
"buffer_store_dword v96, v117, s[4:7], 0 offen sc1\n" \
"s_add_u32 s43, s40, 1\n" \
"s_and_b32 s43, s43, 3\n" \
"s_bitcmp1_b32 s43, 0\n" \
"s_cselect_b32 s48, s32, s31\n" \
"s_lshl_b32 s43, s43, 19\n" \
"s_add_u32 s48, s48, s43\n" \
"s_add_u32 s43, s40, 2\n" \
"s_and_b32 s43, s43, 3\n" \
"s_bitcmp1_b32 s43, 0\n" \
"s_cselect_b32 s49, s32, s31\n" \
"s_lshl_b32 s43, s43, 19\n" \
"s_add_u32 s49, s49, s43\n" \
"s_sub_u32 s43, s24, s44\n" \
"s_add_u32 s43, s43, s28\n" \
"s_max_i32 s43, s43, 0xfffd0000\n" \
"s_min_i32 s43, s43, 0x4000\n" \
"s_mul_i32 s43, s43, s46\n" \
"s_ashr_i32 s43, s43, 12\n" \
"s_add_u32 s41, s45, s43\n" \
"s_lshr_b32 s35, s42, 1\n" \
"s_sub_u32 s35, 0, s35\n" \
"v_xad_u32 v116, v116, s19, v213\n" \
"buffer_store_dword v96, v116, s[4:7], 0 offen sc1\n" \
"s_cmp_eq_u32 s40, 0\n" \
"s_cbranch_scc1 .LP0_wet\n" \
"s_branch .LP0_loop\n" \
"s_branch .LP0_y1\n" \
".LP0_retry2:\n" \
"s_add_u32 s51, s51, 1\n" \
"s_cmp_gt_u32 s51, 0x20000\n" \
"s_cbranch_scc1 .LP0_end\n" \
".LP0_y0:\n" \
"s_add_u32 s41, s41, s42\n" \
"s_add_u32 s24, s24, s25\n" \
"s_xor_b32 s31, s31, s29\n" \
"s_xor_b32 s32, s32, s29\n" \
"s_xor_b32 s47, s47, s29\n" \
"s_xor_b32 s48, s48, s29\n" \
"s_add_u32 s39, s39, 1\n" \
"buffer_store_dwordx4 v[112:115], v223, s[4:7], s48 offen sc1\n" \
"s_bitcmp1_b32 s31, 21\n" \
"s_cselect_b32 s43, s29, 0\n" \
"v_mov_b32 v116, s43\n" \
"ds_write_b32 v252, v116 offset:16\n" \
".LP0_y1:\n" \
"s_memtime vcc\n" \
"s_waitcnt lgkmcnt(0)\n" \
"s_sub_u32 s43, s41, vcc_lo\n" \
"s_cmp_gt_i32 s43, 200\n" \
"s_cbranch_scc1 .LP0_y1\n" \
"s_cmp_lt_i32 s43, s37\n" \
"s_cbranch_scc1 .LP0_y0\n" \
"buffer_load_dwordx2 v[64:65], v211, s[4:7], s47 offen sc1\n" \
".p2align 6\n" \
".LP0_c1:\n" \
"s_memtime vcc\n" \
"s_waitcnt lgkmcnt(0)\n" \
"s_sub_u32 s43, s41, vcc_lo\n" \
"s_cmp_gt_i32 s43, 128\n" \
"s_cbranch_scc1 .LP0_c1\n" \
"s_cmp_lt_i32 s43, s37\n" \
"s_cbranch_scc1 .LP0_y0\n" \
"s_max_i32 s43, s43, 0\n" \
"s_and_b32 s43, s43, -4\n" \
"s_getpc_b64 vcc\n" \
".LP0_c3:\n" \
"s_sub_u32 s43, .LP0_c4-.LP0_c3, s43\n" \
"s_add_u32 vcc_lo, vcc_lo, s43\n" \
"s_addc_u32 vcc_hi, vcc_hi, 0\n" \
"s_setpc_b64 vcc\n" \
".rept 32\n" \
"s_nop 0\n" \
".endr\n" \
".LP0_c4:\n" \
"buffer_load_dwordx4 v[56:59], v210, s[4:7], s47 offen sc1\n" \
"buffer_load_dwordx4 v[60:63], v210, s[4:7], s47 offen offset:256 sc1\n" \
"s_nop 2\n" \
"v_mov_b32 v245, v245\n" \
"s_nop 2\n" \
"v_mov_b32 v245, v245\n" \
"s_nop 2\n" \
"v_mov_b32 v245, v245\n" \
"s_nop 2\n" \
"v_mov_b32 v245, v245\n" \
"s_nop 2\n" \
"v_mov_b32 v245, v245\n" \
"s_nop 2\n" \
"v_mov_b32 v245, v245\n" \
"buffer_load_dwordx4 v[50:53], v212, s[4:7], s47 offen sc1\n" \
"s_nop 2\n" \
"v_mov_b32 v245, v245\n" \
"s_nop 2\n" \
"v_mov_b32 v245, v245\n" \
"s_nop 2\n" \
"v_mov_b32 v245, v245\n" \
"s_nop 2\n" \
"v_mov_b32 v245, v245\n" \
"s_nop 2\n" \
"v_mov_b32 v245, v245\n" \
"s_nop 2\n" \
"v_mov_b32 v245, v245\n" \
"s_nop 2\n" \
"v_mov_b32 v245, v245\n" \
"s_nop 2\n" \
"v_mov_b32 v245, v245\n" \
"s_nop 2\n" \
"v_mov_b32 v245, v245\n" \
"s_nop 2\n" \
"v_mov_b32 v245, v245\n" \
"s_nop 2\n" \
"v_mov_b32 v245, v245\n" \
"s_nop 2\n" \
"v_mov_b32 v245, v245\n" \
"s_nop 2\n" \
"v_mov_b32 v245, v245\n" \
"s_nop 2\n" \
"v_mov_b32 v245, v245\n" \
"s_nop 2\n" \
"v_mov_b32 v245, v245\n" \
"s_nop 2\n" \
"v_mov_b32 v245, v245\n" \
"s_nop 2\n" \
"v_mov_b32 v245, v245\n" \
"s_branch .LP0_wait\n" \
".LP0_cls:\n" \
"s_waitcnt vmcnt(0)\n" \
"v_max3_u32 v44, v64, v64, v65\n" \
"v_max3_u32 v44, v44, v50, v51\n" \
"v_max3_u32 v44, v44, v52, v53\n" \
"v_cmp_eq_u32 vcc, -1, v44\n" \
"s_cbranch_vccnz .LP0_retry2\n" \
"v_cmp_u_f32 vcc, v56, v56\n" \
"v_cmp_ne_u32 s[52:53], v56, v109\n" \
"s_and_b64 vcc, vcc, s[52:53]\n" \
"s_cbranch_vccnz .LP0_retry2\n" \
"v_cmp_u_f32 vcc, v57, v57\n" \
"v_cmp_ne_u32 s[52:53], v57, v109\n" \
"s_and_b64 vcc, vcc, s[52:53]\n" \
"s_cbranch_vccnz .LP0_retry2\n" \
"v_cmp_u_f32 vcc, v58, v58\n" \
"v_cmp_ne_u32 s[52:53], v58, v109\n" \
"s_and_b64 vcc, vcc, s[52:53]\n" \
"s_cbranch_vccnz .LP0_retry2\n" \
"v_cmp_u_f32 vcc, v59, v59\n" \
"v_cmp_ne_u32 s[52:53], v59, v109\n" \
"s_and_b64 vcc, vcc, s[52:53]\n" \
"s_cbranch_vccnz .LP0_retry2\n" \
"v_cmp_u_f32 vcc, v60, v60\n" \
"v_cmp_ne_u32 s[52:53], v60, v109\n" \
"s_and_b64 vcc, vcc, s[52:53]\n" \
"s_cbranch_vccnz .LP0_retry2\n" \
"v_cmp_u_f32 vcc, v61, v61\n" \
"v_cmp_ne_u32 s[52:53], v61, v109\n" \
"s_and_b64 vcc, vcc, s[52:53]\n" \
"s_cbranch_vccnz .LP0_retry2\n" \
"v_cmp_u_f32 vcc, v62, v62\n" \
"v_cmp_ne_u32 s[52:53], v62, v109\n" \
"s_and_b64 vcc, vcc, s[52:53]\n" \
"s_cbranch_vccnz .LP0_retry2\n" \
"v_cmp_u_f32 vcc, v63, v63\n" \
"v_cmp_ne_u32 s[52:53], v63, v109\n" \
"s_and_b64 vcc, vcc, s[52:53]\n" \
"s_cbranch_vccnz .LP0_retry2\n" \
"s_branch .LP0_clsr\n" \
".LP0_last:\n" \
"v_smfmac_f32_16x16x64_bf16 v[28:31], v[16:19], a[32:39], v200\n" \
"v_smfmac_f32_16x16x64_bf16 v[32:35], v[16:19], a[88:95], v200\n" \
"v_smfmac_f32_16x16x64_bf16 v[36:39], v[16:19], a[144:151], v200\n" \
"v_smfmac_f32_16x16x64_bf16 v[40:43], v[16:19], a[200:207], v200\n" \
"v_smfmac_f32_16x16x64_bf16 v[28:31], v[20:23], a[40:47], v200\n" \
"v_smfmac_f32_16x16x64_bf16 v[32:35], v[20:23], a[96:103], v200\n" \
"v_smfmac_f32_16x16x64_bf16 v[36:39], v[20:23], a[152:159], v200\n" \
"v_smfmac_f32_16x16x64_bf16 v[40:43], v[20:23], a[208:215], v200\n" \
"v_smfmac_f32_16x16x64_bf16 v[28:31], v[24:27], a[48:55], v200\n" \
"v_smfmac_f32_16x16x64_bf16 v[32:35], v[24:27], a[104:111], v200\n" \
"v_smfmac_f32_16x16x64_bf16 v[36:39], v[24:27], a[160:167], v200\n" \
"v_smfmac_f32_16x16x64_bf16 v[40:43], v[24:27], a[216:223], v200\n" \
"s_waitcnt vmcnt(0)\n" \
"s_nop 7\n" \
"v_permlane32_swap_b32 v28, v32\n" \
"v_permlane32_swap_b32 v29, v33\n" \
"v_permlane32_swap_b32 v30, v34\n" \
"v_permlane32_swap_b32 v31, v35\n" \
"v_add_f32 v28, v28, v32\n" \
"v_add_f32 v29, v29, v33\n" \
"v_add_f32 v30, v30, v34\n" \
"v_add_f32 v31, v31, v35\n" \
"v_cvt_pk_bf16_f32 v68, v28, v29\n" \
"v_cvt_pk_bf16_f32 v69, v30, v31\n" \
"v_permlane32_swap_b32 v36, v40\n" \
"v_permlane32_swap_b32 v37, v41\n" \
"v_permlane32_swap_b32 v38, v42\n" \
"v_permlane32_swap_b32 v39, v43\n" \
"v_add_f32 v36, v36, v40\n" \
"v_add_f32 v37, v37, v41\n" \
"v_add_f32 v38, v38, v42\n" \
"v_add_f32 v39, v39, v43\n" \
"v_cvt_pk_bf16_f32 v70, v36, v37\n" \
"v_cvt_pk_bf16_f32 v71, v38, v39\n" \
"s_mov_b32 s50, 7340032\n" \
"buffer_store_dwordx4 v[68:71], v218, s[12:15], s50 offen sc1\n" \
"s_waitcnt vmcnt(0)\n" \
"s_barrier\n" \
"s_add_u32 s43, s30, s40\n" \
"s_add_u32 s43, s43, 1\n" \
"v_mov_b32 v241, s43\n" \
"buffer_store_dword v241, v221, s[4:7], 0 offen sc1\n" \
".LP0_end:\n" \
"s_endpgm\n" \
".p2align 6\n" \
"fk_role_P1:\n" \
"ds_read_b32 v200, v255 offset:0\n" \
"ds_read_b32 v201, v255 offset:256\n" \
"ds_read_b32 v202, v255 offset:512\n" \
"ds_read_b32 v203, v255 offset:768\n" \
"ds_read_b32 v204, v255 offset:1024\n" \
"ds_read_b32 v205, v255 offset:1280\n" \
"ds_read_b32 v206, v255 offset:1536\n" \
"ds_read_b32 v207, v255 offset:1792\n" \
"ds_read_b32 v208, v255 offset:2048\n" \
"ds_read_b32 v209, v255 offset:2304\n" \
"ds_read_b32 v210, v255 offset:2560\n" \
"ds_read_b32 v211, v255 offset:2816\n" \
"ds_read_b32 v212, v255 offset:3072\n" \
"ds_read_b32 v213, v255 offset:3328\n" \
"ds_read_b32 v214, v255 offset:3584\n" \
"ds_read_b32 v215, v255 offset:3840\n" \
"ds_read_b32 v216, v255 offset:4096\n" \
"ds_read_b32 v217, v255 offset:4352\n" \
"ds_read_b32 v218, v255 offset:4608\n" \
"ds_read_b32 v219, v255 offset:4864\n" \
"ds_read_b32 v220, v255 offset:5120\n" \
"ds_read_b32 v221, v255 offset:5376\n" \
"ds_read_b32 v222, v255 offset:5632\n" \
"ds_read_b32 v223, v255 offset:5888\n" \
"ds_read_b32 v254, v255 offset:7936\n" \
"s_waitcnt lgkmcnt(0)\n" \
"v_readlane_b32 s4, v254, 0\n" \
"v_readlane_b32 s5, v254, 1\n" \
"v_readlane_b32 s6, v254, 2\n" \
"v_readlane_b32 s7, v254, 3\n" \
"v_readlane_b32 s8, v254, 4\n" \
"v_readlane_b32 s9, v254, 5\n" \
"v_readlane_b32 s10, v254, 6\n" \
"v_readlane_b32 s11, v254, 7\n" \
"v_readlane_b32 s12, v254, 8\n" \
"v_readlane_b32 s13, v254, 9\n" \
"v_readlane_b32 s14, v254, 10\n" \
"v_readlane_b32 s15, v254, 11\n" \
"v_readlane_b32 s16, v254, 12\n" \
"v_readlane_b32 s17, v254, 13\n" \
"v_readlane_b32 s18, v254, 14\n" \
"v_readlane_b32 s19, v254, 15\n" \
"v_readlane_b32 s20, v254, 16\n" \
"v_readlane_b32 s21, v254, 17\n" \
"v_readlane_b32 s22, v254, 18\n" \
"v_readlane_b32 s23, v254, 19\n" \
"v_readlane_b32 s24, v254, 20\n" \
"v_readlane_b32 s25, v254, 21\n" \
"v_readlane_b32 s26, v254, 22\n" \
"v_readlane_b32 s27, v254, 23\n" \
"v_readlane_b32 s28, v254, 24\n" \
"v_readlane_b32 s29, v254, 25\n" \
"v_readlane_b32 s30, v254, 26\n" \
"v_readlane_b32 s31, v254, 27\n" \
"v_readlane_b32 s32, v254, 28\n" \
"v_readlane_b32 s33, v254, 29\n" \
"v_readlane_b32 s34, v254, 30\n" \
"v_readlane_b32 s35, v254, 31\n" \
"v_readlane_b32 s36, v254, 32\n" \
"v_readlane_b32 s37, v254, 33\n" \
"v_readlane_b32 s38, v254, 34\n" \
"v_readlane_b32 s39, v254, 35\n" \
"s_mov_b32 s40, 0\n" \
"s_mov_b32 s51, 0\n" \
"s_nop 0\n" \
"v_mov_b32 v252, s38\n" \
"s_nop 4\n" \
"v_mov_b32 v112, -1\n" \
"v_mov_b32 v113, -1\n" \
"v_mov_b32 v114, -1\n" \
"v_mov_b32 v115, -1\n" \
"v_mov_b32 v109, 0x7fc00001\n" \
"v_writelane_b32 v251, s6, 0\n" \
"v_writelane_b32 v251, s10, 1\n" \
"v_writelane_b32 v251, s14, 2\n" \
"v_writelane_b32 v251, s18, 3\n" \
"s_mov_b32 s6, 0\n" \
"s_mov_b32 s10, 0\n" \
"s_mov_b32 s14, 0\n" \
"s_mov_b32 s18, 0\n" \
"s_mov_b32 s40, -1\n" \
"s_branch .LP1_pre\n" \
".LP1_wet:\n" \
"s_waitcnt vmcnt(0)\n" \
"v_readlane_b32 s6, v251, 0\n" \
"v_readlane_b32 s10, v251, 1\n" \
"v_readlane_b32 s14, v251, 2\n" \
"v_readlane_b32 s18, v251, 3\n" \
"s_nop 4\n" \
"ds_read_b32 v232, v255 offset:7424\n" \
"ds_read_b32 v233, v255 offset:7680\n" \
"v_lshrrev_b32 v234, 4, v201\n" \
"v_and_b32 v234, 3, v234\n" \
"v_subrev_u32 v230, 512, v205\n" \
"s_add_u32 s43, s30, 1\n" \
"s_mov_b32 s50, 0\n" \
".LP1_w44l:\n" \
"buffer_load_dword v240, v230, s[4:7], 0 offen sc1\n" \
"s_waitcnt vmcnt(0)\n" \
".LP1_w44c:\n" \
"v_subrev_u32 v241, s43, v240\n" \
"v_cmp_gt_u32 vcc, 0x100, v241\n" \
"s_cmp_eq_u64 vcc, -1\n" \
"s_cbranch_scc1 .LP1_w44d\n" \
"s_sleep 2\n" \
"s_add_u32 s50, s50, 1\n" \
"s_cmp_lt_u32 s50, 0x20000\n" \
"s_cbranch_scc1 .LP1_w44l\n" \
"s_branch .LP1_end\n" \
".LP1_w44d:\n" \
"v_lshl_or_b32 v241, v241, 2, v234\n" \
"s_nop 1\n" \
"v_min_u32_dpp v241, v241, v241 row_ror:4 row_mask:0xf bank_mask:0xf\n" \
"s_nop 1\n" \
"v_min_u32_dpp v241, v241, v241 row_ror:8 row_mask:0xf bank_mask:0xf\n" \
"s_nop 1\n" \
"v_min_u32_dpp v241, v241, v241 quad_perm:[1,0,3,2] row_mask:0xf bank_mask:0xf\n" \
"s_nop 1\n" \
"v_min_u32_dpp v241, v241, v241 quad_perm:[2,3,0,1] row_mask:0xf bank_mask:0xf\n" \
"s_nop 0\n" \
"v_readlane_b32 s31, v241, 0\n" \
"v_readlane_b32 s43, v241, 16\n" \
"s_min_u32 s31, s31, s43\n" \
"v_readlane_b32 s43, v241, 32\n" \
"s_min_u32 s31, s31, s43\n" \
"v_readlane_b32 s43, v241, 48\n" \
"s_min_u32 s31, s31, s43\n" \
"v_subrev_u32 v230, 256, v205\n" \
"s_add_u32 s43, s30, 1\n" \
"s_mov_b32 s50, 0\n" \
".LP1_w45l:\n" \
"buffer_load_dword v240, v230, s[4:7], 0 offen sc1\n" \
"s_waitcnt vmcnt(0)\n" \
".LP1_w45c:\n" \
"v_subrev_u32 v241, s43, v240\n" \
"v_cmp_gt_u32 vcc, 0x100, v241\n" \
"s_cmp_eq_u64 vcc, -1\n" \
"s_cbranch_scc1 .LP1_w45d\n" \
"s_sleep 2\n" \
"s_add_u32 s50, s50, 1\n" \
"s_cmp_lt_u32 s50, 0x20000\n" \
"s_cbranch_scc1 .LP1_w45l\n" \
"s_branch .LP1_end\n" \
".LP1_w45d:\n" \
"v_lshl_or_b32 v241, v241, 2, v234\n" \
"s_nop 1\n" \
"v_min_u32_dpp v241, v241, v241 row_ror:4 row_mask:0xf bank_mask:0xf\n" \
"s_nop 1\n" \
"v_min_u32_dpp v241, v241, v241 row_ror:8 row_mask:0xf bank_mask:0xf\n" \
"s_nop 1\n" \
"v_min_u32_dpp v241, v241, v241 quad_perm:[1,0,3,2] row_mask:0xf bank_mask:0xf\n" \
"s_nop 1\n" \
"v_min_u32_dpp v241, v241, v241 quad_perm:[2,3,0,1] row_mask:0xf bank_mask:0xf\n" \
"s_nop 0\n" \
"v_readlane_b32 s32, v241, 0\n" \
"v_readlane_b32 s43, v241, 16\n" \
"s_min_u32 s32, s32, s43\n" \
"v_readlane_b32 s43, v241, 32\n" \
"s_min_u32 s32, s32, s43\n" \
"v_readlane_b32 s43, v241, 48\n" \
"s_min_u32 s32, s32, s43\n" \
"s_xor_b32 s29, s31, s32\n" \
"s_and_b32 s29, s29, 3\n" \
"s_lshl_b32 s29, s29, 13\n" \
"s_bitset1_b32 s29, 21\n" \
"s_and_b32 s31, s31, 3\n" \
"s_lshl_b32 s31, s31, 13\n" \
"s_bitcmp1_b32 s39, 0\n" \
"s_cselect_b32 s43, s29, 0\n" \
"s_xor_b32 s31, s31, s43\n" \
"s_xor_b32 s32, s31, 0x4000\n" \
"s_mov_b32 s39, 0\n" \
"v_mov_b32 v224, -1\n" \
"v_mov_b32 v225, -1\n" \
"v_mov_b32 v226, -1\n" \
"v_mov_b32 v227, -1\n" \
"s_waitcnt lgkmcnt(0)\n" \
"s_add_u32 s50, s31, 0\n" \
"buffer_store_dwordx4 v[224:227], v232, s[4:7], s50 offen sc1\n" \
"s_xor_b32 s43, s50, s29\n" \
"buffer_store_dwordx4 v[224:227], v232, s[4:7], s43 offen sc1\n" \
"s_add_u32 s50, s32, 524288\n" \
"buffer_store_dwordx4 v[224:227], v232, s[4:7], s50 offen sc1\n" \
"s_xor_b32 s43, s50, s29\n" \
"buffer_store_dwordx4 v[224:227], v232, s[4:7], s43 offen sc1\n" \
"s_add_u32 s50, s31, 1048576\n" \
"buffer_store_dwordx4 v[224:227], v232, s[4:7], s50 offen sc1\n" \
"s_xor_b32 s43, s50, s29\n" \
"buffer_store_dwordx4 v[224:227], v232, s[4:7], s43 offen sc1\n" \
"s_add_u32 s50, s32, 1572864\n" \
"buffer_store_dwordx4 v[224:227], v232, s[4:7], s50 offen sc1\n" \
"s_xor_b32 s43, s50, s29\n" \
"buffer_store_dwordx4 v[224:227], v232, s[4:7], s43 offen sc1\n" \
"s_waitcnt vmcnt(0)\n" \
"s_barrier\n" \
"s_add_u32 s43, s30, s40\n" \
"s_add_u32 s43, s43, 1\n" \
"v_mov_b32 v241, s43\n" \
"buffer_store_dword v241, v233, s[4:7], 0 offen sc1\n" \
"s_mov_b32 s33, 40\n" \
"s_mov_b32 s34, 0\n" \
"s_add_u32 s43, s30, 1\n" \
"s_mov_b32 s50, 0\n" \
".LP1_w46l:\n" \
"buffer_load_dword v240, v205, s[4:7], 0 offen sc1\n" \
"s_waitcnt vmcnt(0)\n" \
".LP1_w46c:\n" \
"v_subrev_u32 v241, s43, v240\n" \
"v_cmp_gt_u32 vcc, 0x100, v241\n" \
"s_cmp_eq_u64 vcc, -1\n" \
"s_cbranch_scc1 .LP1_w46d\n" \
"s_sleep 2\n" \
"s_add_u32 s50, s50, 1\n" \
"s_cmp_lt_u32 s50, 0x20000\n" \
"s_cbranch_scc1 .LP1_w46l\n" \
"s_branch .LP1_end\n" \
".LP1_w46d:\n" \
"s_add_u32 s43, s30, 1\n" \
"s_mov_b32 s50, 0\n" \
".LP1_w47l:\n" \
"buffer_load_dword v240, v206, s[4:7], 0 offen sc1\n" \
"s_waitcnt vmcnt(0)\n" \
".LP1_w47c:\n" \
"v_subrev_u32 v241, s43, v240\n" \
"v_cmp_gt_u32 vcc, 0x100, v241\n" \
"s_cmp_eq_u64 vcc, -1\n" \
"s_cbranch_scc1 .LP1_w47d\n" \
"s_sleep 2\n" \
"s_add_u32 s50, s50, 1\n" \
"s_cmp_lt_u32 s50, 0x20000\n" \
"s_cbranch_scc1 .LP1_w47l\n" \
"s_branch .LP1_end\n" \
".LP1_w47d:\n" \
"s_add_u32 s43, s40, 0\n" \
"s_and_b32 s43, s43, 3\n" \
"s_bitcmp1_b32 s43, 0\n" \
"s_cselect_b32 s47, s32, s31\n" \
"s_lshl_b32 s43, s43, 19\n" \
"s_add_u32 s47, s47, s43\n" \
"buffer_store_dword v219, v213, s[4:7], s47 offen sc1\n" \
"s_xor_b32 s43, s47, s29\n" \
"buffer_store_dword v219, v213, s[4:7], s43 offen sc1\n" \
"s_add_u32 s43, s30, 1\n" \
"s_mov_b32 s50, 0\n" \
".LP1_w48l:\n" \
"buffer_load_dword v240, v204, s[4:7], 0 offen sc1\n" \
"s_waitcnt vmcnt(0)\n" \
".LP1_w48c:\n" \
"v_subrev_u32 v241, s43, v240\n" \
"v_cmp_gt_u32 vcc, 0x100, v241\n" \
"s_cmp_eq_u64 vcc, -1\n" \
"s_cbranch_scc1 .LP1_w48d\n" \
"s_sleep 2\n" \
"s_add_u32 s50, s50, 1\n" \
"s_cmp_lt_u32 s50, 0x20000\n" \
"s_cbranch_scc1 .LP1_w48l\n" \
"s_branch .LP1_end\n" \
".LP1_w48d:\n" \
"v_readlane_b32 s43, v202, 4\n" \
"s_nop 4\n" \
"buffer_load_dwordx4 a[32:35], v201, s[8:11], s43 offen sc1\n" \
"buffer_load_dwordx4 a[36:39], v201, s[8:11], s43 offen offset:1024 sc1\n" \
"v_readlane_b32 s43, v202, 11\n" \
"s_nop 4\n" \
"buffer_load_dwordx4 a[88:91], v201, s[8:11], s43 offen sc1\n" \
"buffer_load_dwordx4 a[92:95], v201, s[8:11], s43 offen offset:1024 sc1\n" \
"v_readlane_b32 s43, v202, 18\n" \
"s_nop 4\n" \
"buffer_load_dwordx4 a[144:147], v201, s[8:11], s43 offen sc1\n" \
"buffer_load_dwordx4 a[148:151], v201, s[8:11], s43 offen offset:1024 sc1\n" \
"v_readlane_b32 s43, v202, 25\n" \
"s_nop 4\n" \
"buffer_load_dwordx4 a[200:203], v201, s[8:11], s43 offen sc1\n" \
"buffer_load_dwordx4 a[204:207], v201, s[8:11], s43 offen offset:1024 sc1\n" \
"s_add_u32 s43, s30, 2\n" \
"s_mov_b32 s50, 0\n" \
"s_branch .LP1_w49c\n" \
".LP1_w49l:\n" \
"buffer_load_dword v240, v204, s[4:7], 0 offen sc1\n" \
"s_waitcnt vmcnt(0)\n" \
".LP1_w49c:\n" \
"v_subrev_u32 v241, s43, v240\n" \
"v_cmp_gt_u32 vcc, 0x100, v241\n" \
"s_cmp_eq_u64 vcc, -1\n" \
"s_cbranch_scc1 .LP1_w49d\n" \
"s_sleep 2\n" \
"s_add_u32 s50, s50, 1\n" \
"s_cmp_lt_u32 s50, 0x20000\n" \
"s_cbranch_scc1 .LP1_w49l\n" \
"s_branch .LP1_end\n" \
".LP1_w49d:\n" \
"v_readlane_b32 s43, v202, 1\n" \
"s_nop 4\n" \
"buffer_load_dwordx4 a[8:11], v201, s[8:11], s43 offen sc1\n" \
"buffer_load_dwordx4 a[12:15], v201, s[8:11], s43 offen offset:1024 sc1\n" \
"v_readlane_b32 s43, v202, 8\n" \
"s_nop 4\n" \
"buffer_load_dwordx4 a[64:67], v201, s[8:11], s43 offen sc1\n" \
"buffer_load_dwordx4 a[68:71], v201, s[8:11], s43 offen offset:1024 sc1\n" \
"v_readlane_b32 s43, v202, 15\n" \
"s_nop 4\n" \
"buffer_load_dwordx4 a[120:123], v201, s[8:11], s43 offen sc1\n" \
"buffer_load_dwordx4 a[124:127], v201, s[8:11], s43 offen offset:1024 sc1\n" \
"v_readlane_b32 s43, v202, 22\n" \
"s_nop 4\n" \
"buffer_load_dwordx4 a[176:179], v201, s[8:11], s43 offen sc1\n" \
"buffer_load_dwordx4 a[180:183], v201, s[8:11], s43 offen offset:1024 sc1\n" \
"s_add_u32 s43, s30, 3\n" \
"s_mov_b32 s50, 0\n" \
"s_branch .LP1_w50c\n" \
".LP1_w50l:\n" \
"buffer_load_dword v240, v204, s[4:7], 0 offen sc1\n" \
"s_waitcnt vmcnt(0)\n" \
".LP1_w50c:\n" \
"v_subrev_u32 v241, s43, v240\n" \
"v_cmp_gt_u32 vcc, 0x100, v241\n" \
"s_cmp_eq_u64 vcc, -1\n" \
"s_cbranch_scc1 .LP1_w50d\n" \
"s_sleep 2\n" \
"s_add_u32 s50, s50, 1\n" \
"s_cmp_lt_u32 s50, 0x20000\n" \
"s_cbranch_scc1 .LP1_w50l\n" \
"s_branch .LP1_end\n" \
".LP1_w50d:\n" \
"v_readlane_b32 s43, v202, 5\n" \
"s_nop 4\n" \
"buffer_load_dwordx4 a[40:43], v201, s[8:11], s43 offen sc1\n" \
"buffer_load_dwordx4 a[44:47], v201, s[8:11], s43 offen offset:1024 sc1\n" \
"v_readlane_b32 s43, v202, 12\n" \
"s_nop 4\n" \
"buffer_load_dwordx4 a[96:99], v201, s[8:11], s43 offen sc1\n" \
"buffer_load_dwordx4 a[100:103], v201, s[8:11], s43 offen offset:1024 sc1\n" \
"v_readlane_b32 s43, v202, 19\n" \
"s_nop 4\n" \
"buffer_load_dwordx4 a[152:155], v201, s[8:11], s43 offen sc1\n" \
"buffer_load_dwordx4 a[156:159], v201, s[8:11], s43 offen offset:1024 sc1\n" \
"v_readlane_b32 s43, v202, 26\n" \
"s_nop 4\n" \
"buffer_load_dwordx4 a[208:211], v201, s[8:11], s43 offen sc1\n" \
"buffer_load_dwordx4 a[212:215], v201, s[8:11], s43 offen offset:1024 sc1\n" \
"s_add_u32 s43, s30, 4\n" \
"s_mov_b32 s50, 0\n" \
"s_branch .LP1_w51c\n" \
".LP1_w51l:\n" \
"buffer_load_dword v240, v204, s[4:7], 0 offen sc1\n" \
"s_waitcnt vmcnt(0)\n" \
".LP1_w51c:\n" \
"v_subrev_u32 v241, s43, v240\n" \
"v_cmp_gt_u32 vcc, 0x100, v241\n" \
"s_cmp_eq_u64 vcc, -1\n" \
"s_cbranch_scc1 .LP1_w51d\n" \
"s_sleep 2\n" \
"s_add_u32 s50, s50, 1\n" \
"s_cmp_lt_u32 s50, 0x20000\n" \
"s_cbranch_scc1 .LP1_w51l\n" \
"s_branch .LP1_end\n" \
".LP1_w51d:\n" \
"v_readlane_b32 s43, v202, 2\n" \
"s_nop 4\n" \
"buffer_load_dwordx4 a[16:19], v201, s[8:11], s43 offen sc1\n" \
"buffer_load_dwordx4 a[20:23], v201, s[8:11], s43 offen offset:1024 sc1\n" \
"v_readlane_b32 s43, v202, 9\n" \
"s_nop 4\n" \
"buffer_load_dwordx4 a[72:75], v201, s[8:11], s43 offen sc1\n" \
"buffer_load_dwordx4 a[76:79], v201, s[8:11], s43 offen offset:1024 sc1\n" \
"v_readlane_b32 s43, v202, 16\n" \
"s_nop 4\n" \
"buffer_load_dwordx4 a[128:131], v201, s[8:11], s43 offen sc1\n" \
"buffer_load_dwordx4 a[132:135], v201, s[8:11], s43 offen offset:1024 sc1\n" \
"v_readlane_b32 s43, v202, 23\n" \
"s_nop 4\n" \
"buffer_load_dwordx4 a[184:187], v201, s[8:11], s43 offen sc1\n" \
"buffer_load_dwordx4 a[188:191], v201, s[8:11], s43 offen offset:1024 sc1\n" \
"s_add_u32 s43, s30, 5\n" \
"s_mov_b32 s50, 0\n" \
"s_branch .LP1_w52c\n" \
".LP1_w52l:\n" \
"buffer_load_dword v240, v204, s[4:7], 0 offen sc1\n" \
"s_waitcnt vmcnt(0)\n" \
".LP1_w52c:\n" \
"v_subrev_u32 v241, s43, v240\n" \
"v_cmp_gt_u32 vcc, 0x100, v241\n" \
"s_cmp_eq_u64 vcc, -1\n" \
"s_cbranch_scc1 .LP1_w52d\n" \
"s_sleep 2\n" \
"s_add_u32 s50, s50, 1\n" \
"s_cmp_lt_u32 s50, 0x20000\n" \
"s_cbranch_scc1 .LP1_w52l\n" \
"s_branch .LP1_end\n" \
".LP1_w52d:\n" \
"v_readlane_b32 s43, v202, 0\n" \
"s_nop 4\n" \
"buffer_load_dwordx4 a[0:3], v201, s[8:11], s43 offen sc1\n" \
"buffer_load_dwordx4 a[4:7], v201, s[8:11], s43 offen offset:1024 sc1\n" \
"v_readlane_b32 s43, v202, 7\n" \
"s_nop 4\n" \
"buffer_load_dwordx4 a[56:59], v201, s[8:11], s43 offen sc1\n" \
"buffer_load_dwordx4 a[60:63], v201, s[8:11], s43 offen offset:1024 sc1\n" \
"v_readlane_b32 s43, v202, 14\n" \
"s_nop 4\n" \
"buffer_load_dwordx4 a[112:115], v201, s[8:11], s43 offen sc1\n" \
"buffer_load_dwordx4 a[116:119], v201, s[8:11], s43 offen offset:1024 sc1\n" \
"v_readlane_b32 s43, v202, 21\n" \
"s_nop 4\n" \
"buffer_load_dwordx4 a[168:171], v201, s[8:11], s43 offen sc1\n" \
"buffer_load_dwordx4 a[172:175], v201, s[8:11], s43 offen offset:1024 sc1\n" \
"s_add_u32 s43, s30, 6\n" \
"s_mov_b32 s50, 0\n" \
"s_branch .LP1_w53c\n" \
".LP1_w53l:\n" \
"buffer_load_dword v240, v204, s[4:7], 0 offen sc1\n" \
"s_waitcnt vmcnt(0)\n" \
".LP1_w53c:\n" \
"v_subrev_u32 v241, s43, v240\n" \
"v_cmp_gt_u32 vcc, 0x100, v241\n" \
"s_cmp_eq_u64 vcc, -1\n" \
"s_cbranch_scc1 .LP1_w53d\n" \
"s_sleep 2\n" \
"s_add_u32 s50, s50, 1\n" \
"s_cmp_lt_u32 s50, 0x20000\n" \
"s_cbranch_scc1 .LP1_w53l\n" \
"s_branch .LP1_end\n" \
".LP1_w53d:\n" \
"v_readlane_b32 s43, v202, 3\n" \
"s_nop 4\n" \
"buffer_load_dwordx4 a[24:27], v201, s[8:11], s43 offen sc1\n" \
"buffer_load_dwordx4 a[28:31], v201, s[8:11], s43 offen offset:1024 sc1\n" \
"v_readlane_b32 s43, v202, 10\n" \
"s_nop 4\n" \
"buffer_load_dwordx4 a[80:83], v201, s[8:11], s43 offen sc1\n" \
"buffer_load_dwordx4 a[84:87], v201, s[8:11], s43 offen offset:1024 sc1\n" \
"v_readlane_b32 s43, v202, 17\n" \
"s_nop 4\n" \
"buffer_load_dwordx4 a[136:139], v201, s[8:11], s43 offen sc1\n" \
"buffer_load_dwordx4 a[140:143], v201, s[8:11], s43 offen offset:1024 sc1\n" \
"v_readlane_b32 s43, v202, 24\n" \
"s_nop 4\n" \
"buffer_load_dwordx4 a[192:195], v201, s[8:11], s43 offen sc1\n" \
"buffer_load_dwordx4 a[196:199], v201, s[8:11], s43 offen offset:1024 sc1\n" \
"s_add_u32 s43, s30, 7\n" \
"s_mov_b32 s50, 0\n" \
"s_branch .LP1_w54c\n" \
".LP1_w54l:\n" \
"buffer_load_dword v240, v204, s[4:7], 0 offen sc1\n" \
"s_waitcnt vmcnt(0)\n" \
".LP1_w54c:\n" \
"v_subrev_u32 v241, s43, v240\n" \
"v_cmp_gt_u32 vcc, 0x100, v241\n" \
"s_cmp_eq_u64 vcc, -1\n" \
"s_cbranch_scc1 .LP1_w54d\n" \
"s_sleep 2\n" \
"s_add_u32 s50, s50, 1\n" \
"s_cmp_lt_u32 s50, 0x20000\n" \
"s_cbranch_scc1 .LP1_w54l\n" \
"s_branch .LP1_end\n" \
".LP1_w54d:\n" \
"v_readlane_b32 s43, v202, 6\n" \
"s_nop 4\n" \
"buffer_load_dwordx4 a[48:51], v201, s[8:11], s43 offen sc1\n" \
"buffer_load_dwordx4 a[52:55], v201, s[8:11], s43 offen offset:1024 sc1\n" \
"v_readlane_b32 s43, v202, 13\n" \
"s_nop 4\n" \
"buffer_load_dwordx4 a[104:107], v201, s[8:11], s43 offen sc1\n" \
"buffer_load_dwordx4 a[108:111], v201, s[8:11], s43 offen offset:1024 sc1\n" \
"v_readlane_b32 s43, v202, 20\n" \
"s_nop 4\n" \
"buffer_load_dwordx4 a[160:163], v201, s[8:11], s43 offen sc1\n" \
"buffer_load_dwordx4 a[164:167], v201, s[8:11], s43 offen offset:1024 sc1\n" \
"v_readlane_b32 s43, v202, 27\n" \
"s_nop 4\n" \
"buffer_load_dwordx4 a[216:219], v201, s[8:11], s43 offen sc1\n" \
"buffer_load_dwordx4 a[220:223], v201, s[8:11], s43 offen offset:1024 sc1\n" \
".LP1_pre:\n" \
"s_lshl_b32 s43, s40, 7\n" \
"buffer_load_dwordx4 v[236:239], v207, s[4:7], s43 offen sc1\n" \
"s_waitcnt vmcnt(0)\n" \
"s_barrier\n" \
"ds_read_b96 v[248:250], v252\n" \
"s_waitcnt lgkmcnt(0)\n" \
"v_readfirstlane_b32 s44, v248\n" \
"v_readfirstlane_b32 s45, v249\n" \
"v_readfirstlane_b32 s46, v250\n" \
"s_mul_i32 s42, s25, s46\n" \
"s_lshr_b32 s42, s42, 12\n" \
".LP1_cu:\n" \
"s_sub_u32 s43, s24, s44\n" \
"s_cmp_lt_i32 s43, 0\n" \
"s_cbranch_scc0 .LP1_cud\n" \
"s_add_u32 s24, s24, s25\n" \
"s_xor_b32 s31, s31, s29\n" \
"s_xor_b32 s32, s32, s29\n" \
"s_xor_b32 s47, s47, s29\n" \
"s_add_u32 s39, s39, 1\n" \
"s_branch .LP1_cu\n" \
".LP1_cud:\n" \
"s_mov_b32 s3, s46\n" \
"s_add_u32 s43, s40, 1\n" \
"s_and_b32 s43, s43, 3\n" \
"s_bitcmp1_b32 s43, 0\n" \
"s_cselect_b32 s48, s32, s31\n" \
"s_lshl_b32 s43, s43, 19\n" \
"s_add_u32 s48, s48, s43\n" \
"s_add_u32 s43, s40, 2\n" \
"s_and_b32 s43, s43, 3\n" \
"s_bitcmp1_b32 s43, 0\n" \
"s_cselect_b32 s49, s32, s31\n" \
"s_lshl_b32 s43, s43, 19\n" \
"s_add_u32 s49, s49, s43\n" \
"s_sub_u32 s43, s24, s44\n" \
"s_add_u32 s43, s43, s28\n" \
"s_max_i32 s43, s43, 0xfffd0000\n" \
"s_min_i32 s43, s43, 0x4000\n" \
"s_mul_i32 s43, s43, s46\n" \
"s_ashr_i32 s43, s43, 12\n" \
"s_add_u32 s41, s45, s43\n" \
"s_lshr_b32 s35, s42, 1\n" \
"s_sub_u32 s35, 0, s35\n" \
"v_max_u32 v236, v236, v238\n" \
"v_max_u32 v237, v237, v239\n" \
"v_and_b32 v236, 0xfff, v236\n" \
"v_and_b32 v237, 0xfff, v237\n" \
"s_mov_b64 exec, 1\n" \
"ds_write_b64 v252, v[236:237] offset:24\n" \
"s_mov_b64 exec, -1\n" \
".LP1_loop:\n" \
"s_branch .LP1_m1\n" \
".LP1_retry1:\n" \
"s_add_u32 s51, s51, 1\n" \
"s_cmp_gt_u32 s51, 0x20000\n" \
"s_cbranch_scc1 .LP1_end\n" \
".LP1_m0:\n" \
"s_add_u32 s41, s41, s42\n" \
"s_add_u32 s24, s24, s25\n" \
"s_xor_b32 s31, s31, s29\n" \
"s_xor_b32 s32, s32, s29\n" \
"s_xor_b32 s47, s47, s29\n" \
"s_xor_b32 s48, s48, s29\n" \
"s_add_u32 s39, s39, 1\n" \
"s_bitcmp1_b32 s31, 21\n" \
"s_cselect_b32 s43, s29, 0\n" \
"v_mov_b32 v116, s43\n" \
"ds_write_b32 v252, v116 offset:16\n" \
".LP1_m1:\n" \
"s_memtime vcc\n" \
"s_waitcnt lgkmcnt(0)\n" \
"s_sub_u32 s43, s41, vcc_lo\n" \
"s_cmp_gt_i32 s43, 0\n" \
"s_cbranch_scc1 .LP1_m1\n" \
"s_cmp_lt_i32 s43, s35\n" \
"s_cbranch_scc1 .LP1_m0\n" \
"buffer_load_dwordx4 v[0:3], v208, s[4:7], s47 offen offset:2048 sc1\n" \
"buffer_load_dwordx4 v[4:7], v209, s[4:7], s47 offen sc1\n" \
"s_nop 15\n" \
"s_nop 13\n" \
"s_mul_i32 s50, s40, 114688\n" \
"buffer_store_dwordx4 v[68:71], v218, s[12:15], s50 offen sc1\n" \
"buffer_store_dword v112, v213, s[4:7], s49 offen sc1\n" \
"s_xor_b32 s43, s49, s29\n" \
"buffer_store_dword v112, v213, s[4:7], s43 offen sc1\n" \
"s_lshl_b32 s43, s40, 7\n" \
"buffer_load_dwordx4 v[236:239], v207, s[4:7], s43 offen offset:128 sc1\n" \
"v_mov_b32 v28, 0\n" \
"v_mov_b32 v29, 0\n" \
"v_mov_b32 v30, 0\n" \
"v_mov_b32 v31, 0\n" \
"v_mov_b32 v32, 0\n" \
"v_mov_b32 v33, 0\n" \
"v_mov_b32 v34, 0\n" \
"v_mov_b32 v35, 0\n" \
"v_mov_b32 v36, 0\n" \
"v_mov_b32 v37, 0\n" \
"v_mov_b32 v38, 0\n" \
"v_mov_b32 v39, 0\n" \
"v_mov_b32 v40, 0\n" \
"v_mov_b32 v41, 0\n" \
"v_mov_b32 v42, 0\n" \
"v_mov_b32 v43, 0\n" \
"s_waitcnt vmcnt(5)\n" \
"ds_write_b128 v220, v[0:3] offset:2048\n" \
"s_waitcnt vmcnt(4)\n" \
"ds_write_b128 v220, v[4:7] offset:4096\n" \
"v_max3_u32 v44, v0, v0, v1\n" \
"v_max3_u32 v44, v44, v2, v3\n" \
"v_max3_u32 v44, v44, v4, v5\n" \
"v_max3_u32 v44, v44, v6, v7\n" \
"v_cmp_eq_u32 vcc, -1, v44\n" \
"v_smfmac_f32_16x16x64_bf16 v[28:31], v[0:3], a[0:7], v200\n" \
"v_smfmac_f32_16x16x64_bf16 v[32:35], v[0:3], a[56:63], v200\n" \
"s_cbranch_vccnz .LP1_retry1\n" \
"s_waitcnt lgkmcnt(0)\n" \
"s_barrier\n" \
"ds_read_b128 v[8:11], v220 offset:5120\n" \
"ds_read_b128 v[12:15], v220 offset:6144\n" \
"ds_read_b128 v[16:19], v220 offset:0\n" \
"ds_read_b128 v[20:23], v220 offset:1024\n" \
"ds_read_b128 v[24:27], v220 offset:3072\n" \
"ds_read_b64 v[246:247], v252 offset:24\n" \
"ds_read_b96 v[248:250], v252\n" \
"v_smfmac_f32_16x16x64_bf16 v[36:39], v[0:3], a[112:119], v200\n" \
"v_smfmac_f32_16x16x64_bf16 v[40:43], v[0:3], a[168:175], v200\n" \
"v_smfmac_f32_16x16x64_bf16 v[28:31], v[4:7], a[8:15], v200\n" \
"v_smfmac_f32_16x16x64_bf16 v[32:35], v[4:7], a[64:71], v200\n" \
"v_smfmac_f32_16x16x64_bf16 v[36:39], v[4:7], a[120:127], v200\n" \
"v_smfmac_f32_16x16x64_bf16 v[40:43], v[4:7], a[176:183], v200\n" \
"s_waitcnt lgkmcnt(6)\n" \
"v_smfmac_f32_16x16x64_bf16 v[28:31], v[8:11], a[16:23], v200\n" \
"v_smfmac_f32_16x16x64_bf16 v[32:35], v[8:11], a[72:79], v200\n" \
"v_smfmac_f32_16x16x64_bf16 v[36:39], v[8:11], a[128:135], v200\n" \
"v_smfmac_f32_16x16x64_bf16 v[40:43], v[8:11], a[184:191], v200\n" \
"s_waitcnt lgkmcnt(0)\n" \
"v_readfirstlane_b32 s44, v248\n" \
"v_readfirstlane_b32 s45, v249\n" \
"v_smfmac_f32_16x16x64_bf16 v[28:31], v[12:15], a[24:31], v200\n" \
"v_readfirstlane_b32 s46, v250\n" \
"v_readfirstlane_b32 s26, v246\n" \
"v_readfirstlane_b32 s27, v247\n" \
"v_smfmac_f32_16x16x64_bf16 v[32:35], v[12:15], a[80:87], v200\n" \
"s_mul_i32 s42, s25, s46\n" \
"s_lshr_b32 s42, s42, 12\n" \
"s_add_u32 s24, s24, s26\n" \
"v_smfmac_f32_16x16x64_bf16 v[36:39], v[12:15], a[136:143], v200\n" \
"s_sub_u32 s43, s24, s44\n" \
"s_max_i32 s43, s43, 0xfffd0000\n" \
"s_min_i32 s43, s43, 0x4000\n" \
"v_smfmac_f32_16x16x64_bf16 v[40:43], v[12:15], a[192:199], v200\n" \
"s_mul_i32 s43, s43, s46\n" \
"s_ashr_i32 s43, s43, 12\n" \
"s_add_u32 s41, s45, s43\n" \
"s_cmp_eq_u32 s40, 63\n" \
"s_cbranch_scc1 .LP1_last\n" \
"s_branch .LP1_z1\n" \
".LP1_z0:\n" \
"s_add_u32 s41, s41, s42\n" \
"s_add_u32 s24, s24, s25\n" \
"s_xor_b32 s31, s31, s29\n" \
"s_xor_b32 s32, s32, s29\n" \
"s_xor_b32 s47, s47, s29\n" \
"s_xor_b32 s48, s48, s29\n" \
"s_add_u32 s39, s39, 1\n" \
"buffer_store_dwordx4 v[112:115], v223, s[4:7], s48 offen sc1\n" \
"s_bitcmp1_b32 s31, 21\n" \
"s_cselect_b32 s43, s29, 0\n" \
"v_mov_b32 v116, s43\n" \
"ds_write_b32 v252, v116 offset:16\n" \
".LP1_z1:\n" \
"s_memtime vcc\n" \
"s_waitcnt lgkmcnt(0)\n" \
"s_sub_u32 s43, s41, vcc_lo\n" \
"s_cmp_gt_i32 s43, 200\n" \
"s_cbranch_scc1 .LP1_z1\n" \
"s_cmp_lt_i32 s43, s37\n" \
"s_cbranch_scc1 .LP1_z0\n" \
"buffer_load_dwordx2 v[64:65], v211, s[4:7], s47 offen sc1\n" \
".p2align 6\n" \
".LP1_b1:\n" \
"s_memtime vcc\n" \
"s_waitcnt lgkmcnt(0)\n" \
"s_sub_u32 s43, s41, vcc_lo\n" \
"s_cmp_gt_i32 s43, 128\n" \
"s_cbranch_scc1 .LP1_b1\n" \
"s_cmp_lt_i32 s43, s37\n" \
"s_cbranch_scc1 .LP1_z0\n" \
"s_max_i32 s43, s43, 0\n" \
"s_and_b32 s43, s43, -4\n" \
"s_getpc_b64 vcc\n" \
".LP1_b3:\n" \
"s_sub_u32 s43, .LP1_b4-.LP1_b3, s43\n" \
"s_add_u32 vcc_lo, vcc_lo, s43\n" \
"s_addc_u32 vcc_hi, vcc_hi, 0\n" \
"s_setpc_b64 vcc\n" \
".rept 32\n" \
"s_nop 0\n" \
".endr\n" \
".LP1_b4:\n" \
"buffer_load_dwordx4 v[56:59], v210, s[4:7], s47 offen sc1\n" \
"buffer_load_dwordx4 v[60:63], v210, s[4:7], s47 offen offset:256 sc1\n" \
"v_smfmac_f32_16x16x64_bf16 v[28:31], v[16:19], a[32:39], v200\n" \
"v_smfmac_f32_16x16x64_bf16 v[32:35], v[16:19], a[88:95], v200\n" \
"v_smfmac_f32_16x16x64_bf16 v[36:39], v[16:19], a[144:151], v200\n" \
"v_smfmac_f32_16x16x64_bf16 v[40:43], v[16:19], a[200:207], v200\n" \
"v_smfmac_f32_16x16x64_bf16 v[28:31], v[20:23], a[40:47], v200\n" \
"v_smfmac_f32_16x16x64_bf16 v[32:35], v[20:23], a[96:103], v200\n" \
"buffer_load_dwordx4 v[50:53], v212, s[4:7], s47 offen sc1\n" \
"v_smfmac_f32_16x16x64_bf16 v[36:39], v[20:23], a[152:159], v200\n" \
"v_smfmac_f32_16x16x64_bf16 v[40:43], v[20:23], a[208:215], v200\n" \
"v_smfmac_f32_16x16x64_bf16 v[28:31], v[24:27], a[48:55], v200\n" \
"v_smfmac_f32_16x16x64_bf16 v[32:35], v[24:27], a[104:111], v200\n" \
"v_smfmac_f32_16x16x64_bf16 v[36:39], v[24:27], a[160:167], v200\n" \
"v_smfmac_f32_16x16x64_bf16 v[40:43], v[24:27], a[216:223], v200\n" \
"s_nop 2\n" \
"v_mov_b32 v245, v245\n" \
"v_mov_b32 v245, v245\n" \
"v_mov_b32 v245, v245\n" \
"v_permlane32_swap_b32 v28, v32\n" \
"v_permlane32_swap_b32 v29, v33\n" \
"v_permlane32_swap_b32 v30, v34\n" \
"v_permlane32_swap_b32 v31, v35\n" \
"v_add_f32 v28, v28, v32\n" \
"v_add_f32 v29, v29, v33\n" \
"v_add_f32 v30, v30, v34\n" \
"v_add_f32 v31, v31, v35\n" \
"v_cvt_pk_bf16_f32 v68, v28, v29\n" \
"v_cvt_pk_bf16_f32 v69, v30, v31\n" \
"v_permlane32_swap_b32 v36, v40\n" \
"v_permlane32_swap_b32 v37, v41\n" \
"v_permlane32_swap_b32 v38, v42\n" \
"v_permlane32_swap_b32 v39, v43\n" \
"v_add_f32 v36, v36, v40\n" \
"v_add_f32 v37, v37, v41\n" \
"v_add_f32 v38, v38, v42\n" \
"v_add_f32 v39, v39, v43\n" \
"v_cvt_pk_bf16_f32 v70, v36, v37\n" \
"v_cvt_pk_bf16_f32 v71, v38, v39\n" \
"v_mov_b32 v48, v68\n" \
"v_mov_b32 v49, v69\n" \
"v_mov_b32 v54, v70\n" \
"v_mov_b32 v55, v71\n" \
"s_nop 1\n" \
"v_permlane16_swap_b32 v48, v54\n" \
"v_permlane16_swap_b32 v49, v55\n" \
".LP1_wait:\n" \
"s_waitcnt vmcnt(1)\n" \
"v_pk_add_f32 v[72:73], v[56:57], v[60:61]\n" \
"v_pk_add_f32 v[74:75], v[58:59], v[62:63]\n" \
"v_max3_f32 v77, v72, v73, v64\n" \
"v_max3_f32 v77, v77, v74, v75\n" \
"s_nop 1\n" \
"v_max_f32_dpp v76, v77, v77 row_ror:8 row_mask:0xf bank_mask:0xf\n" \
"v_pk_add_f32 v[72:73], v[72:73], v[76:77] op_sel_hi:[1,0] neg_lo:[0,1] neg_hi:[0,1]\n" \
"v_pk_add_f32 v[74:75], v[74:75], v[76:77] op_sel_hi:[1,0] neg_lo:[0,1] neg_hi:[0,1]\n" \
"v_sub_f32 v78, v64, v76\n" \
"v_exp_f32 v72, v72\n" \
"v_exp_f32 v73, v73\n" \
"v_exp_f32 v74, v74\n" \
"v_exp_f32 v75, v75\n" \
"v_exp_f32 v78, v78\n" \
"v_pk_add_f32 v[80:81], v[72:73], v[74:75]\n" \
"v_fma_f32 v80, v78, v65, v80\n" \
"v_add_f32 v81, v80, v81\n" \
"s_nop 1\n" \
"v_add_f32_dpp v80, v81, v81 row_ror:8 row_mask:0xf bank_mask:0xf\n" \
"v_rcp_f32 v80, v80\n" \
"s_nop 0\n" \
"v_mul_f32 v78, v78, v80\n" \
"v_pk_mul_f32 v[72:73], v[72:73], v[80:81] op_sel_hi:[1,0]\n" \
"v_pk_mul_f32 v[74:75], v[74:75], v[80:81] op_sel_hi:[1,0]\n" \
"v_cvt_pk_bf16_f32 v78, v78, v78\n" \
"v_cvt_pk_bf16_f32 v82, v72, v73\n" \
"v_cvt_pk_bf16_f32 v83, v74, v75\n" \
"v_and_b32 v84, v78, v214\n" \
"v_and_b32 v85, v78, v215\n" \
"v_cvt_pk_bf16_f32 v86, v72, v73\n" \
"v_cvt_pk_bf16_f32 v87, v74, v75\n" \
"s_waitcnt vmcnt(0)\n" \
"v_mfma_f32_16x16x32_bf16 v[88:91], v[48:51], v[82:85], 0\n" \
"v_mfma_f32_16x16x32_bf16 v[92:95], v[52:55], v[84:87], 0\n" \
"s_nop 2\n" \
"v_mov_b32 v245, v245\n" \
"s_nop 2\n" \
"v_mov_b32 v245, v245\n" \
"v_add_f32_dpp v96, v92, v88 row_ror:8 row_mask:0xf bank_mask:0x3\n" \
"v_add_f32_dpp v97, v93, v89 row_ror:8 row_mask:0xf bank_mask:0x3\n" \
"v_add_f32_dpp v96, v90, v94 row_ror:8 row_mask:0xf bank_mask:0xc\n" \
"v_add_f32_dpp v97, v91, v95 row_ror:8 row_mask:0xf bank_mask:0xc\n" \
"ds_write_b64 v216, v[96:97]\n" \
"v_cmp_u_f32 vcc, v96, v97\n" \
"s_bitcmp1_b32 s31, 21\n" \
"s_cselect_b32 s18, s29, 0\n" \
"s_xor_b32 s18, s18, s48\n" \
"v_mov_b32 v245, v245\n" \
"s_xor_b32 s19, s18, s29\n" \
"s_nop 0\n" \
"s_nop 0\n" \
"v_mov_b32 v245, v245\n" \
"s_nop 0\n" \
"s_nop 0\n" \
"s_nop 0\n" \
"v_mov_b32 v245, v245\n" \
"s_cbranch_vccnz .LP1_cls\n" \
".LP1_clsr:\n" \
"s_waitcnt lgkmcnt(0)\n" \
"s_barrier\n" \
"ds_read_b64 v[100:101], v217 offset:0\n" \
"ds_read_b64 v[102:103], v217 offset:512\n" \
"ds_read_b32 v116, v252 offset:16\n" \
"ds_read_b64 v[104:105], v217 offset:1024\n" \
"ds_read_b64 v[106:107], v217 offset:1536\n" \
"v_mov_b32 v245, v245\n" \
"v_mov_b32 v245, v245\n" \
"s_add_u32 s40, s40, 1\n" \
"s_add_u32 s24, s24, s27\n" \
"s_mov_b32 s47, s48\n" \
"v_mov_b32 v245, v245\n" \
"s_waitcnt lgkmcnt(2)\n" \
"v_xad_u32 v117, v116, s18, v213\n" \
"v_pk_add_f32 v[100:101], v[100:101], v[102:103]\n" \
"s_waitcnt lgkmcnt(0)\n" \
"v_pk_add_f32 v[104:105], v[104:105], v[106:107]\n" \
"v_pk_add_f32 v[100:101], v[100:101], v[104:105]\n" \
"v_cvt_pk_bf16_f32 v96, v100, v101\n" \
"s_add_u32 s43, s40, 1\n" \
"s_and_b32 s43, s43, 3\n" \
"s_bitcmp1_b32 s43, 0\n" \
"s_cselect_b32 s48, s32, s31\n" \
"s_lshl_b32 s43, s43, 19\n" \
"s_add_u32 s48, s48, s43\n" \
"s_add_u32 s43, s40, 2\n" \
"s_and_b32 s43, s43, 3\n" \
"buffer_store_dword v96, v117, s[4:7], 0 offen sc1\n" \
"s_bitcmp1_b32 s43, 0\n" \
"s_cselect_b32 s49, s32, s31\n" \
"s_lshl_b32 s43, s43, 19\n" \
"s_add_u32 s49, s49, s43\n" \
"s_sub_u32 s43, s24, s44\n" \
"s_add_u32 s43, s43, s28\n" \
"s_max_i32 s43, s43, 0xfffd0000\n" \
"s_min_i32 s43, s43, 0x4000\n" \
"s_mul_i32 s43, s43, s46\n" \
"s_ashr_i32 s43, s43, 12\n" \
"s_add_u32 s41, s45, s43\n" \
"s_lshr_b32 s35, s42, 1\n" \
"s_sub_u32 s35, 0, s35\n" \
"v_max_u32 v236, v236, v238\n" \
"v_max_u32 v237, v237, v239\n" \
"v_and_b32 v236, 0xfff, v236\n" \
"v_and_b32 v237, 0xfff, v237\n" \
"s_mov_b64 exec, 1\n" \
"ds_write_b64 v252, v[236:237] offset:24\n" \
"s_mov_b64 exec, -1\n" \
"v_xad_u32 v116, v116, s19, v213\n" \
"buffer_store_dword v96, v116, s[4:7], 0 offen sc1\n" \
"s_cmp_eq_u32 s40, 0\n" \
"s_cbranch_scc1 .LP1_wet\n" \
"s_branch .LP1_loop\n" \
"s_branch .LP1_y1\n" \
".LP1_retry2:\n" \
"s_add_u32 s51, s51, 1\n" \
"s_cmp_gt_u32 s51, 0x20000\n" \
"s_cbranch_scc1 .LP1_end\n" \
".LP1_y0:\n" \
"s_add_u32 s41, s41, s42\n" \
"s_add_u32 s24, s24, s25\n" \
"s_xor_b32 s31, s31, s29\n" \
"s_xor_b32 s32, s32, s29\n" \
"s_xor_b32 s47, s47, s29\n" \
"s_xor_b32 s48, s48, s29\n" \
"s_add_u32 s39, s39, 1\n" \
"buffer_store_dwordx4 v[112:115], v223, s[4:7], s48 offen sc1\n" \
"s_bitcmp1_b32 s31, 21\n" \
"s_cselect_b32 s43, s29, 0\n" \
"v_mov_b32 v116, s43\n" \
"ds_write_b32 v252, v116 offset:16\n" \
".LP1_y1:\n" \
"s_memtime vcc\n" \
"s_waitcnt lgkmcnt(0)\n" \
"s_sub_u32 s43, s41, vcc_lo\n" \
"s_cmp_gt_i32 s43, 200\n" \
"s_cbranch_scc1 .LP1_y1\n" \
"s_cmp_lt_i32 s43, s37\n" \
"s_cbranch_scc1 .LP1_y0\n" \
"buffer_load_dwordx2 v[64:65], v211, s[4:7], s47 offen sc1\n" \
".p2align 6\n" \
".LP1_c1:\n" \
"s_memtime vcc\n" \
"s_waitcnt lgkmcnt(0)\n" \
"s_sub_u32 s43, s41, vcc_lo\n" \
"s_cmp_gt_i32 s43, 128\n" \
"s_cbranch_scc1 .LP1_c1\n" \
"s_cmp_lt_i32 s43, s37\n" \
"s_cbranch_scc1 .LP1_y0\n" \
"s_max_i32 s43, s43, 0\n" \
"s_and_b32 s43, s43, -4\n" \
"s_getpc_b64 vcc\n" \
".LP1_c3:\n" \
"s_sub_u32 s43, .LP1_c4-.LP1_c3, s43\n" \
"s_add_u32 vcc_lo, vcc_lo, s43\n" \
"s_addc_u32 vcc_hi, vcc_hi, 0\n" \
"s_setpc_b64 vcc\n" \
".rept 32\n" \
"s_nop 0\n" \
".endr\n" \
".LP1_c4:\n" \
"buffer_load_dwordx4 v[56:59], v210, s[4:7], s47 offen sc1\n" \
"buffer_load_dwordx4 v[60:63], v210, s[4:7], s47 offen offset:256 sc1\n" \
"s_nop 2\n" \
"v_mov_b32 v245, v245\n" \
"s_nop 2\n" \
"v_mov_b32 v245, v245\n" \
"s_nop 2\n" \
"v_mov_b32 v245, v245\n" \
"s_nop 2\n" \
"v_mov_b32 v245, v245\n" \
"s_nop 2\n" \
"v_mov_b32 v245, v245\n" \
"s_nop 2\n" \
"v_mov_b32 v245, v245\n" \
"buffer_load_dwordx4 v[50:53], v212, s[4:7], s47 offen sc1\n" \
"s_nop 2\n" \
"v_mov_b32 v245, v245\n" \
"s_nop 2\n" \
"v_mov_b32 v245, v245\n" \
"s_nop 2\n" \
"v_mov_b32 v245, v245\n" \
"s_nop 2\n" \
"v_mov_b32 v245, v245\n" \
"s_nop 2\n" \
"v_mov_b32 v245, v245\n" \
"s_nop 2\n" \
"v_mov_b32 v245, v245\n" \
"s_nop 2\n" \
"v_mov_b32 v245, v245\n" \
"s_nop 2\n" \
"v_mov_b32 v245, v245\n" \
"s_nop 2\n" \
"v_mov_b32 v245, v245\n" \
"s_nop 2\n" \
"v_mov_b32 v245, v245\n" \
"s_nop 2\n" \
"v_mov_b32 v245, v245\n" \
"s_nop 2\n" \
"v_mov_b32 v245, v245\n" \
"s_nop 2\n" \
"v_mov_b32 v245, v245\n" \
"s_nop 2\n" \
"v_mov_b32 v245, v245\n" \
"s_nop 2\n" \
"v_mov_b32 v245, v245\n" \
"s_nop 2\n" \
"v_mov_b32 v245, v245\n" \
"s_nop 2\n" \
"v_mov_b32 v245, v245\n" \
"s_branch .LP1_wait\n" \
".LP1_cls:\n" \
"s_waitcnt vmcnt(0)\n" \
"v_max3_u32 v44, v64, v64, v65\n" \
"v_max3_u32 v44, v44, v50, v51\n" \
"v_max3_u32 v44, v44, v52, v53\n" \
"v_cmp_eq_u32 vcc, -1, v44\n" \
"s_cbranch_vccnz .LP1_retry2\n" \
"v_cmp_u_f32 vcc, v56, v56\n" \
"v_cmp_ne_u32 s[52:53], v56, v109\n" \
"s_and_b64 vcc, vcc, s[52:53]\n" \
"s_cbranch_vccnz .LP1_retry2\n" \
"v_cmp_u_f32 vcc, v57, v57\n" \
"v_cmp_ne_u32 s[52:53], v57, v109\n" \
"s_and_b64 vcc, vcc, s[52:53]\n" \
"s_cbranch_vccnz .LP1_retry2\n" \
"v_cmp_u_f32 vcc, v58, v58\n" \
"v_cmp_ne_u32 s[52:53], v58, v109\n" \
"s_and_b64 vcc, vcc, s[52:53]\n" \
"s_cbranch_vccnz .LP1_retry2\n" \
"v_cmp_u_f32 vcc, v59, v59\n" \
"v_cmp_ne_u32 s[52:53], v59, v109\n" \
"s_and_b64 vcc, vcc, s[52:53]\n" \
"s_cbranch_vccnz .LP1_retry2\n" \
"v_cmp_u_f32 vcc, v60, v60\n" \
"v_cmp_ne_u32 s[52:53], v60, v109\n" \
"s_and_b64 vcc, vcc, s[52:53]\n" \
"s_cbranch_vccnz .LP1_retry2\n" \
"v_cmp_u_f32 vcc, v61, v61\n" \
"v_cmp_ne_u32 s[52:53], v61, v109\n" \
"s_and_b64 vcc, vcc, s[52:53]\n" \
"s_cbranch_vccnz .LP1_retry2\n" \
"v_cmp_u_f32 vcc, v62, v62\n" \
"v_cmp_ne_u32 s[52:53], v62, v109\n" \
"s_and_b64 vcc, vcc, s[52:53]\n" \
"s_cbranch_vccnz .LP1_retry2\n" \
"v_cmp_u_f32 vcc, v63, v63\n" \
"v_cmp_ne_u32 s[52:53], v63, v109\n" \
"s_and_b64 vcc, vcc, s[52:53]\n" \
"s_cbranch_vccnz .LP1_retry2\n" \
"s_branch .LP1_clsr\n" \
".LP1_last:\n" \
"v_smfmac_f32_16x16x64_bf16 v[28:31], v[16:19], a[32:39], v200\n" \
"v_smfmac_f32_16x16x64_bf16 v[32:35], v[16:19], a[88:95], v200\n" \
"v_smfmac_f32_16x16x64_bf16 v[36:39], v[16:19], a[144:151], v200\n" \
"v_smfmac_f32_16x16x64_bf16 v[40:43], v[16:19], a[200:207], v200\n" \
"v_smfmac_f32_16x16x64_bf16 v[28:31], v[20:23], a[40:47], v200\n" \
"v_smfmac_f32_16x16x64_bf16 v[32:35], v[20:23], a[96:103], v200\n" \
"v_smfmac_f32_16x16x64_bf16 v[36:39], v[20:23], a[152:159], v200\n" \
"v_smfmac_f32_16x16x64_bf16 v[40:43], v[20:23], a[208:215], v200\n" \
"v_smfmac_f32_16x16x64_bf16 v[28:31], v[24:27], a[48:55], v200\n" \
"v_smfmac_f32_16x16x64_bf16 v[32:35], v[24:27], a[104:111], v200\n" \
"v_smfmac_f32_16x16x64_bf16 v[36:39], v[24:27], a[160:167], v200\n" \
"v_smfmac_f32_16x16x64_bf16 v[40:43], v[24:27], a[216:223], v200\n" \
"s_waitcnt vmcnt(0)\n" \
"s_nop 7\n" \
"v_permlane32_swap_b32 v28, v32\n" \
"v_permlane32_swap_b32 v29, v33\n" \
"v_permlane32_swap_b32 v30, v34\n" \
"v_permlane32_swap_b32 v31, v35\n" \
"v_add_f32 v28, v28, v32\n" \
"v_add_f32 v29, v29, v33\n" \
"v_add_f32 v30, v30, v34\n" \
"v_add_f32 v31, v31, v35\n" \
"v_cvt_pk_bf16_f32 v68, v28, v29\n" \
"v_cvt_pk_bf16_f32 v69, v30, v31\n" \
"v_permlane32_swap_b32 v36, v40\n" \
"v_permlane32_swap_b32 v37, v41\n" \
"v_permlane32_swap_b32 v38, v42\n" \
"v_permlane32_swap_b32 v39, v43\n" \
"v_add_f32 v36, v36, v40\n" \
"v_add_f32 v37, v37, v41\n" \
"v_add_f32 v38, v38, v42\n" \
"v_add_f32 v39, v39, v43\n" \
"v_cvt_pk_bf16_f32 v70, v36, v37\n" \
"v_cvt_pk_bf16_f32 v71, v38, v39\n" \
"s_mov_b32 s50, 7340032\n" \
"buffer_store_dwordx4 v[68:71], v218, s[12:15], s50 offen sc1\n" \
"s_waitcnt vmcnt(0)\n" \
"s_barrier\n" \
"v_mov_b32 v241, s30\n" \
"buffer_store_dword v241, v222, s[4:7], 0 offen sc1\n" \
".LP1_end:\n" \
"s_endpgm\n" \
".p2align 6\n" \
"fk_role_P2:\n" \
"ds_read_b32 v200, v255 offset:0\n" \
"ds_read_b32 v201, v255 offset:256\n" \
"ds_read_b32 v202, v255 offset:512\n" \
"ds_read_b32 v203, v255 offset:768\n" \
"ds_read_b32 v204, v255 offset:1024\n" \
"ds_read_b32 v205, v255 offset:1280\n" \
"ds_read_b32 v206, v255 offset:1536\n" \
"ds_read_b32 v207, v255 offset:1792\n" \
"ds_read_b32 v208, v255 offset:2048\n" \
"ds_read_b32 v209, v255 offset:2304\n" \
"ds_read_b32 v210, v255 offset:2560\n" \
"ds_read_b32 v211, v255 offset:2816\n" \
"ds_read_b32 v212, v255 offset:3072\n" \
"ds_read_b32 v213, v255 offset:3328\n" \
"ds_read_b32 v214, v255 offset:3584\n" \
"ds_read_b32 v215, v255 offset:3840\n" \
"ds_read_b32 v216, v255 offset:4096\n" \
"ds_read_b32 v217, v255 offset:4352\n" \
"ds_read_b32 v218, v255 offset:4608\n" \
"ds_read_b32 v219, v255 offset:4864\n" \
"ds_read_b32 v220, v255 offset:5120\n" \
"ds_read_b32 v221, v255 offset:5376\n" \
"ds_read_b32 v222, v255 offset:5632\n" \
"ds_read_b32 v223, v255 offset:5888\n" \
"ds_read_b32 v254, v255 offset:7936\n" \
"s_waitcnt lgkmcnt(0)\n" \
"v_readlane_b32 s4, v254, 0\n" \
"v_readlane_b32 s5, v254, 1\n" \
"v_readlane_b32 s6, v254, 2\n" \
"v_readlane_b32 s7, v254, 3\n" \
"v_readlane_b32 s8, v254, 4\n" \
"v_readlane_b32 s9, v254, 5\n" \
"v_readlane_b32 s10, v254, 6\n" \
"v_readlane_b32 s11, v254, 7\n" \
"v_readlane_b32 s12, v254, 8\n" \
"v_readlane_b32 s13, v254, 9\n" \
"v_readlane_b32 s14, v254, 10\n" \
"v_readlane_b32 s15, v254, 11\n" \
"v_readlane_b32 s16, v254, 12\n" \
"v_readlane_b32 s17, v254, 13\n" \
"v_readlane_b32 s18, v254, 14\n" \
"v_readlane_b32 s19, v254, 15\n" \
"v_readlane_b32 s20, v254, 16\n" \
"v_readlane_b32 s21, v254, 17\n" \
"v_readlane_b32 s22, v254, 18\n" \
"v_readlane_b32 s23, v254, 19\n" \
"v_readlane_b32 s24, v254, 20\n" \
"v_readlane_b32 s25, v254, 21\n" \
"v_readlane_b32 s26, v254, 22\n" \
"v_readlane_b32 s27, v254, 23\n" \
"v_readlane_b32 s28, v254, 24\n" \
"v_readlane_b32 s29, v254, 25\n" \
"v_readlane_b32 s30, v254, 26\n" \
"v_readlane_b32 s31, v254, 27\n" \
"v_readlane_b32 s32, v254, 28\n" \
"v_readlane_b32 s33, v254, 29\n" \
"v_readlane_b32 s34, v254, 30\n" \
"v_readlane_b32 s35, v254, 31\n" \
"v_readlane_b32 s36, v254, 32\n" \
"v_readlane_b32 s37, v254, 33\n" \
"v_readlane_b32 s38, v254, 34\n" \
"v_readlane_b32 s39, v254, 35\n" \
"s_mov_b32 s40, 0\n" \
"s_mov_b32 s51, 0\n" \
"s_nop 0\n" \
"v_mov_b32 v252, s38\n" \
"s_nop 4\n" \
"v_mov_b32 v112, -1\n" \
"v_mov_b32 v113, -1\n" \
"v_mov_b32 v114, -1\n" \
"v_mov_b32 v115, -1\n" \
"v_mov_b32 v109, 0x7fc00001\n" \
"v_writelane_b32 v251, s6, 0\n" \
"v_writelane_b32 v251, s10, 1\n" \
"v_writelane_b32 v251, s14, 2\n" \
"v_writelane_b32 v251, s18, 3\n" \
"s_mov_b32 s6, 0\n" \
"s_mov_b32 s10, 0\n" \
"s_mov_b32 s14, 0\n" \
"s_mov_b32 s18, 0\n" \
"s_mov_b32 s40, -1\n" \
"s_branch .LP2_pre\n" \
".LP2_wet:\n" \
"s_waitcnt vmcnt(0)\n" \
"v_readlane_b32 s6, v251, 0\n" \
"v_readlane_b32 s10, v251, 1\n" \
"v_readlane_b32 s14, v251, 2\n" \
"v_readlane_b32 s18, v251, 3\n" \
"s_nop 4\n" \
"ds_read_b32 v232, v255 offset:7424\n" \
"ds_read_b32 v233, v255 offset:7680\n" \
"v_lshrrev_b32 v234, 4, v201\n" \
"v_and_b32 v234, 3, v234\n" \
"v_subrev_u32 v230, 512, v205\n" \
"s_add_u32 s43, s30, 1\n" \
"s_mov_b32 s50, 0\n" \
".LP2_w55l:\n" \
"buffer_load_dword v240, v230, s[4:7], 0 offen sc1\n" \
"s_waitcnt vmcnt(0)\n" \
".LP2_w55c:\n" \
"v_subrev_u32 v241, s43, v240\n" \
"v_cmp_gt_u32 vcc, 0x100, v241\n" \
"s_cmp_eq_u64 vcc, -1\n" \
"s_cbranch_scc1 .LP2_w55d\n" \
"s_sleep 2\n" \
"s_add_u32 s50, s50, 1\n" \
"s_cmp_lt_u32 s50, 0x20000\n" \
"s_cbranch_scc1 .LP2_w55l\n" \
"s_branch .LP2_end\n" \
".LP2_w55d:\n" \
"v_lshl_or_b32 v241, v241, 2, v234\n" \
"s_nop 1\n" \
"v_min_u32_dpp v241, v241, v241 row_ror:4 row_mask:0xf bank_mask:0xf\n" \
"s_nop 1\n" \
"v_min_u32_dpp v241, v241, v241 row_ror:8 row_mask:0xf bank_mask:0xf\n" \
"s_nop 1\n" \
"v_min_u32_dpp v241, v241, v241 quad_perm:[1,0,3,2] row_mask:0xf bank_mask:0xf\n" \
"s_nop 1\n" \
"v_min_u32_dpp v241, v241, v241 quad_perm:[2,3,0,1] row_mask:0xf bank_mask:0xf\n" \
"s_nop 0\n" \
"v_readlane_b32 s31, v241, 0\n" \
"v_readlane_b32 s43, v241, 16\n" \
"s_min_u32 s31, s31, s43\n" \
"v_readlane_b32 s43, v241, 32\n" \
"s_min_u32 s31, s31, s43\n" \
"v_readlane_b32 s43, v241, 48\n" \
"s_min_u32 s31, s31, s43\n" \
"v_subrev_u32 v230, 256, v205\n" \
"s_add_u32 s43, s30, 1\n" \
"s_mov_b32 s50, 0\n" \
".LP2_w56l:\n" \
"buffer_load_dword v240, v230, s[4:7], 0 offen sc1\n" \
"s_waitcnt vmcnt(0)\n" \
".LP2_w56c:\n" \
"v_subrev_u32 v241, s43, v240\n" \
"v_cmp_gt_u32 vcc, 0x100, v241\n" \
"s_cmp_eq_u64 vcc, -1\n" \
"s_cbranch_scc1 .LP2_w56d\n" \
"s_sleep 2\n" \
"s_add_u32 s50, s50, 1\n" \
"s_cmp_lt_u32 s50, 0x20000\n" \
"s_cbranch_scc1 .LP2_w56l\n" \
"s_branch .LP2_end\n" \
".LP2_w56d:\n" \
"v_lshl_or_b32 v241, v241, 2, v234\n" \
"s_nop 1\n" \
"v_min_u32_dpp v241, v241, v241 row_ror:4 row_mask:0xf bank_mask:0xf\n" \
"s_nop 1\n" \
"v_min_u32_dpp v241, v241, v241 row_ror:8 row_mask:0xf bank_mask:0xf\n" \
"s_nop 1\n" \
"v_min_u32_dpp v241, v241, v241 quad_perm:[1,0,3,2] row_mask:0xf bank_mask:0xf\n" \
"s_nop 1\n" \
"v_min_u32_dpp v241, v241, v241 quad_perm:[2,3,0,1] row_mask:0xf bank_mask:0xf\n" \
"s_nop 0\n" \
"v_readlane_b32 s32, v241, 0\n" \
"v_readlane_b32 s43, v241, 16\n" \
"s_min_u32 s32, s32, s43\n" \
"v_readlane_b32 s43, v241, 32\n" \
"s_min_u32 s32, s32, s43\n" \
"v_readlane_b32 s43, v241, 48\n" \
"s_min_u32 s32, s32, s43\n" \
"s_xor_b32 s29, s31, s32\n" \
"s_and_b32 s29, s29, 3\n" \
"s_lshl_b32 s29, s29, 13\n" \
"s_bitset1_b32 s29, 21\n" \
"s_and_b32 s31, s31, 3\n" \
"s_lshl_b32 s31, s31, 13\n" \
"s_bitcmp1_b32 s39, 0\n" \
"s_cselect_b32 s43, s29, 0\n" \
"s_xor_b32 s31, s31, s43\n" \
"s_xor_b32 s32, s31, 0x4000\n" \
"s_mov_b32 s39, 0\n" \
"v_mov_b32 v224, -1\n" \
"v_mov_b32 v225, -1\n" \
"v_mov_b32 v226, -1\n" \
"v_mov_b32 v227, -1\n" \
"s_waitcnt lgkmcnt(0)\n" \
"s_add_u32 s50, s31, 0\n" \
"buffer_store_dwordx4 v[224:227], v232, s[4:7], s50 offen sc1\n" \
"s_xor_b32 s43, s50, s29\n" \
"buffer_store_dwordx4 v[224:227], v232, s[4:7], s43 offen sc1\n" \
"s_add_u32 s50, s32, 524288\n" \
"buffer_store_dwordx4 v[224:227], v232, s[4:7], s50 offen sc1\n" \
"s_xor_b32 s43, s50, s29\n" \
"buffer_store_dwordx4 v[224:227], v232, s[4:7], s43 offen sc1\n" \
"s_add_u32 s50, s31, 1048576\n" \
"buffer_store_dwordx4 v[224:227], v232, s[4:7], s50 offen sc1\n" \
"s_xor_b32 s43, s50, s29\n" \
"buffer_store_dwordx4 v[224:227], v232, s[4:7], s43 offen sc1\n" \
"s_add_u32 s50, s32, 1572864\n" \
"buffer_store_dwordx4 v[224:227], v232, s[4:7], s50 offen sc1\n" \
"s_xor_b32 s43, s50, s29\n" \
"buffer_store_dwordx4 v[224:227], v232, s[4:7], s43 offen sc1\n" \
"s_waitcnt vmcnt(0)\n" \
"s_barrier\n" \
"s_add_u32 s43, s30, s40\n" \
"s_add_u32 s43, s43, 1\n" \
"v_mov_b32 v241, s43\n" \
"buffer_store_dword v241, v233, s[4:7], 0 offen sc1\n" \
"s_mov_b32 s33, 40\n" \
"s_mov_b32 s34, 0\n" \
"s_add_u32 s43, s30, 1\n" \
"s_mov_b32 s50, 0\n" \
".LP2_w57l:\n" \
"buffer_load_dword v240, v205, s[4:7], 0 offen sc1\n" \
"s_waitcnt vmcnt(0)\n" \
".LP2_w57c:\n" \
"v_subrev_u32 v241, s43, v240\n" \
"v_cmp_gt_u32 vcc, 0x100, v241\n" \
"s_cmp_eq_u64 vcc, -1\n" \
"s_cbranch_scc1 .LP2_w57d\n" \
"s_sleep 2\n" \
"s_add_u32 s50, s50, 1\n" \
"s_cmp_lt_u32 s50, 0x20000\n" \
"s_cbranch_scc1 .LP2_w57l\n" \
"s_branch .LP2_end\n" \
".LP2_w57d:\n" \
"s_add_u32 s43, s30, 1\n" \
"s_mov_b32 s50, 0\n" \
".LP2_w58l:\n" \
"buffer_load_dword v240, v206, s[4:7], 0 offen sc1\n" \
"s_waitcnt vmcnt(0)\n" \
".LP2_w58c:\n" \
"v_subrev_u32 v241, s43, v240\n" \
"v_cmp_gt_u32 vcc, 0x100, v241\n" \
"s_cmp_eq_u64 vcc, -1\n" \
"s_cbranch_scc1 .LP2_w58d\n" \
"s_sleep 2\n" \
"s_add_u32 s50, s50, 1\n" \
"s_cmp_lt_u32 s50, 0x20000\n" \
"s_cbranch_scc1 .LP2_w58l\n" \
"s_branch .LP2_end\n" \
".LP2_w58d:\n" \
"s_add_u32 s43, s40, 0\n" \
"s_and_b32 s43, s43, 3\n" \
"s_bitcmp1_b32 s43, 0\n" \
"s_cselect_b32 s47, s32, s31\n" \
"s_lshl_b32 s43, s43, 19\n" \
"s_add_u32 s47, s47, s43\n" \
"buffer_store_dword v219, v213, s[4:7], s47 offen sc1\n" \
"s_xor_b32 s43, s47, s29\n" \
"buffer_store_dword v219, v213, s[4:7], s43 offen sc1\n" \
"s_add_u32 s43, s30, 1\n" \
"s_mov_b32 s50, 0\n" \
".LP2_w59l:\n" \
"buffer_load_dword v240, v204, s[4:7], 0 offen sc1\n" \
"s_waitcnt vmcnt(0)\n" \
".LP2_w59c:\n" \
"v_subrev_u32 v241, s43, v240\n" \
"v_cmp_gt_u32 vcc, 0x100, v241\n" \
"s_cmp_eq_u64 vcc, -1\n" \
"s_cbranch_scc1 .LP2_w59d\n" \
"s_sleep 2\n" \
"s_add_u32 s50, s50, 1\n" \
"s_cmp_lt_u32 s50, 0x20000\n" \
"s_cbranch_scc1 .LP2_w59l\n" \
"s_branch .LP2_end\n" \
".LP2_w59d:\n" \
"v_readlane_b32 s43, v202, 2\n" \
"s_nop 4\n" \
"buffer_load_dwordx4 a[16:19], v201, s[8:11], s43 offen sc1\n" \
"buffer_load_dwordx4 a[20:23], v201, s[8:11], s43 offen offset:1024 sc1\n" \
"v_readlane_b32 s43, v202, 9\n" \
"s_nop 4\n" \
"buffer_load_dwordx4 a[72:75], v201, s[8:11], s43 offen sc1\n" \
"buffer_load_dwordx4 a[76:79], v201, s[8:11], s43 offen offset:1024 sc1\n" \
"v_readlane_b32 s43, v202, 16\n" \
"s_nop 4\n" \
"buffer_load_dwordx4 a[128:131], v201, s[8:11], s43 offen sc1\n" \
"buffer_load_dwordx4 a[132:135], v201, s[8:11], s43 offen offset:1024 sc1\n" \
"v_readlane_b32 s43, v202, 23\n" \
"s_nop 4\n" \
"buffer_load_dwordx4 a[184:187], v201, s[8:11], s43 offen sc1\n" \
"buffer_load_dwordx4 a[188:191], v201, s[8:11], s43 offen offset:1024 sc1\n" \
"s_add_u32 s43, s30, 2\n" \
"s_mov_b32 s50, 0\n" \
"s_branch .LP2_w60c\n" \
".LP2_w60l:\n" \
"buffer_load_dword v240, v204, s[4:7], 0 offen sc1\n" \
"s_waitcnt vmcnt(0)\n" \
".LP2_w60c:\n" \
"v_subrev_u32 v241, s43, v240\n" \
"v_cmp_gt_u32 vcc, 0x100, v241\n" \
"s_cmp_eq_u64 vcc, -1\n" \
"s_cbranch_scc1 .LP2_w60d\n" \
"s_sleep 2\n" \
"s_add_u32 s50, s50, 1\n" \
"s_cmp_lt_u32 s50, 0x20000\n" \
"s_cbranch_scc1 .LP2_w60l\n" \
"s_branch .LP2_end\n" \
".LP2_w60d:\n" \
"v_readlane_b32 s43, v202, 6\n" \
"s_nop 4\n" \
"buffer_load_dwordx4 a[48:51], v201, s[8:11], s43 offen sc1\n" \
"buffer_load_dwordx4 a[52:55], v201, s[8:11], s43 offen offset:1024 sc1\n" \
"v_readlane_b32 s43, v202, 13\n" \
"s_nop 4\n" \
"buffer_load_dwordx4 a[104:107], v201, s[8:11], s43 offen sc1\n" \
"buffer_load_dwordx4 a[108:111], v201, s[8:11], s43 offen offset:1024 sc1\n" \
"v_readlane_b32 s43, v202, 20\n" \
"s_nop 4\n" \
"buffer_load_dwordx4 a[160:163], v201, s[8:11], s43 offen sc1\n" \
"buffer_load_dwordx4 a[164:167], v201, s[8:11], s43 offen offset:1024 sc1\n" \
"v_readlane_b32 s43, v202, 27\n" \
"s_nop 4\n" \
"buffer_load_dwordx4 a[216:219], v201, s[8:11], s43 offen sc1\n" \
"buffer_load_dwordx4 a[220:223], v201, s[8:11], s43 offen offset:1024 sc1\n" \
"s_add_u32 s43, s30, 3\n" \
"s_mov_b32 s50, 0\n" \
"s_branch .LP2_w61c\n" \
".LP2_w61l:\n" \
"buffer_load_dword v240, v204, s[4:7], 0 offen sc1\n" \
"s_waitcnt vmcnt(0)\n" \
".LP2_w61c:\n" \
"v_subrev_u32 v241, s43, v240\n" \
"v_cmp_gt_u32 vcc, 0x100, v241\n" \
"s_cmp_eq_u64 vcc, -1\n" \
"s_cbranch_scc1 .LP2_w61d\n" \
"s_sleep 2\n" \
"s_add_u32 s50, s50, 1\n" \
"s_cmp_lt_u32 s50, 0x20000\n" \
"s_cbranch_scc1 .LP2_w61l\n" \
"s_branch .LP2_end\n" \
".LP2_w61d:\n" \
"v_readlane_b32 s43, v202, 3\n" \
"s_nop 4\n" \
"buffer_load_dwordx4 a[24:27], v201, s[8:11], s43 offen sc1\n" \
"buffer_load_dwordx4 a[28:31], v201, s[8:11], s43 offen offset:1024 sc1\n" \
"v_readlane_b32 s43, v202, 10\n" \
"s_nop 4\n" \
"buffer_load_dwordx4 a[80:83], v201, s[8:11], s43 offen sc1\n" \
"buffer_load_dwordx4 a[84:87], v201, s[8:11], s43 offen offset:1024 sc1\n" \
"v_readlane_b32 s43, v202, 17\n" \
"s_nop 4\n" \
"buffer_load_dwordx4 a[136:139], v201, s[8:11], s43 offen sc1\n" \
"buffer_load_dwordx4 a[140:143], v201, s[8:11], s43 offen offset:1024 sc1\n" \
"v_readlane_b32 s43, v202, 24\n" \
"s_nop 4\n" \
"buffer_load_dwordx4 a[192:195], v201, s[8:11], s43 offen sc1\n" \
"buffer_load_dwordx4 a[196:199], v201, s[8:11], s43 offen offset:1024 sc1\n" \
"s_add_u32 s43, s30, 4\n" \
"s_mov_b32 s50, 0\n" \
"s_branch .LP2_w62c\n" \
".LP2_w62l:\n" \
"buffer_load_dword v240, v204, s[4:7], 0 offen sc1\n" \
"s_waitcnt vmcnt(0)\n" \
".LP2_w62c:\n" \
"v_subrev_u32 v241, s43, v240\n" \
"v_cmp_gt_u32 vcc, 0x100, v241\n" \
"s_cmp_eq_u64 vcc, -1\n" \
"s_cbranch_scc1 .LP2_w62d\n" \
"s_sleep 2\n" \
"s_add_u32 s50, s50, 1\n" \
"s_cmp_lt_u32 s50, 0x20000\n" \
"s_cbranch_scc1 .LP2_w62l\n" \
"s_branch .LP2_end\n" \
".LP2_w62d:\n" \
"v_readlane_b32 s43, v202, 0\n" \
"s_nop 4\n" \
"buffer_load_dwordx4 a[0:3], v201, s[8:11], s43 offen sc1\n" \
"buffer_load_dwordx4 a[4:7], v201, s[8:11], s43 offen offset:1024 sc1\n" \
"v_readlane_b32 s43, v202, 7\n" \
"s_nop 4\n" \
"buffer_load_dwordx4 a[56:59], v201, s[8:11], s43 offen sc1\n" \
"buffer_load_dwordx4 a[60:63], v201, s[8:11], s43 offen offset:1024 sc1\n" \
"v_readlane_b32 s43, v202, 14\n" \
"s_nop 4\n" \
"buffer_load_dwordx4 a[112:115], v201, s[8:11], s43 offen sc1\n" \
"buffer_load_dwordx4 a[116:119], v201, s[8:11], s43 offen offset:1024 sc1\n" \
"v_readlane_b32 s43, v202, 21\n" \
"s_nop 4\n" \
"buffer_load_dwordx4 a[168:171], v201, s[8:11], s43 offen sc1\n" \
"buffer_load_dwordx4 a[172:175], v201, s[8:11], s43 offen offset:1024 sc1\n" \
"s_add_u32 s43, s30, 5\n" \
"s_mov_b32 s50, 0\n" \
"s_branch .LP2_w63c\n" \
".LP2_w63l:\n" \
"buffer_load_dword v240, v204, s[4:7], 0 offen sc1\n" \
"s_waitcnt vmcnt(0)\n" \
".LP2_w63c:\n" \
"v_subrev_u32 v241, s43, v240\n" \
"v_cmp_gt_u32 vcc, 0x100, v241\n" \
"s_cmp_eq_u64 vcc, -1\n" \
"s_cbranch_scc1 .LP2_w63d\n" \
"s_sleep 2\n" \
"s_add_u32 s50, s50, 1\n" \
"s_cmp_lt_u32 s50, 0x20000\n" \
"s_cbranch_scc1 .LP2_w63l\n" \
"s_branch .LP2_end\n" \
".LP2_w63d:\n" \
"v_readlane_b32 s43, v202, 4\n" \
"s_nop 4\n" \
"buffer_load_dwordx4 a[32:35], v201, s[8:11], s43 offen sc1\n" \
"buffer_load_dwordx4 a[36:39], v201, s[8:11], s43 offen offset:1024 sc1\n" \
"v_readlane_b32 s43, v202, 11\n" \
"s_nop 4\n" \
"buffer_load_dwordx4 a[88:91], v201, s[8:11], s43 offen sc1\n" \
"buffer_load_dwordx4 a[92:95], v201, s[8:11], s43 offen offset:1024 sc1\n" \
"v_readlane_b32 s43, v202, 18\n" \
"s_nop 4\n" \
"buffer_load_dwordx4 a[144:147], v201, s[8:11], s43 offen sc1\n" \
"buffer_load_dwordx4 a[148:151], v201, s[8:11], s43 offen offset:1024 sc1\n" \
"v_readlane_b32 s43, v202, 25\n" \
"s_nop 4\n" \
"buffer_load_dwordx4 a[200:203], v201, s[8:11], s43 offen sc1\n" \
"buffer_load_dwordx4 a[204:207], v201, s[8:11], s43 offen offset:1024 sc1\n" \
"s_add_u32 s43, s30, 6\n" \
"s_mov_b32 s50, 0\n" \
"s_branch .LP2_w64c\n" \
".LP2_w64l:\n" \
"buffer_load_dword v240, v204, s[4:7], 0 offen sc1\n" \
"s_waitcnt vmcnt(0)\n" \
".LP2_w64c:\n" \
"v_subrev_u32 v241, s43, v240\n" \
"v_cmp_gt_u32 vcc, 0x100, v241\n" \
"s_cmp_eq_u64 vcc, -1\n" \
"s_cbranch_scc1 .LP2_w64d\n" \
"s_sleep 2\n" \
"s_add_u32 s50, s50, 1\n" \
"s_cmp_lt_u32 s50, 0x20000\n" \
"s_cbranch_scc1 .LP2_w64l\n" \
"s_branch .LP2_end\n" \
".LP2_w64d:\n" \
"v_readlane_b32 s43, v202, 1\n" \
"s_nop 4\n" \
"buffer_load_dwordx4 a[8:11], v201, s[8:11], s43 offen sc1\n" \
"buffer_load_dwordx4 a[12:15], v201, s[8:11], s43 offen offset:1024 sc1\n" \
"v_readlane_b32 s43, v202, 8\n" \
"s_nop 4\n" \
"buffer_load_dwordx4 a[64:67], v201, s[8:11], s43 offen sc1\n" \
"buffer_load_dwordx4 a[68:71], v201, s[8:11], s43 offen offset:1024 sc1\n" \
"v_readlane_b32 s43, v202, 15\n" \
"s_nop 4\n" \
"buffer_load_dwordx4 a[120:123], v201, s[8:11], s43 offen sc1\n" \
"buffer_load_dwordx4 a[124:127], v201, s[8:11], s43 offen offset:1024 sc1\n" \
"v_readlane_b32 s43, v202, 22\n" \
"s_nop 4\n" \
"buffer_load_dwordx4 a[176:179], v201, s[8:11], s43 offen sc1\n" \
"buffer_load_dwordx4 a[180:183], v201, s[8:11], s43 offen offset:1024 sc1\n" \
"s_add_u32 s43, s30, 7\n" \
"s_mov_b32 s50, 0\n" \
"s_branch .LP2_w65c\n" \
".LP2_w65l:\n" \
"buffer_load_dword v240, v204, s[4:7], 0 offen sc1\n" \
"s_waitcnt vmcnt(0)\n" \
".LP2_w65c:\n" \
"v_subrev_u32 v241, s43, v240\n" \
"v_cmp_gt_u32 vcc, 0x100, v241\n" \
"s_cmp_eq_u64 vcc, -1\n" \
"s_cbranch_scc1 .LP2_w65d\n" \
"s_sleep 2\n" \
"s_add_u32 s50, s50, 1\n" \
"s_cmp_lt_u32 s50, 0x20000\n" \
"s_cbranch_scc1 .LP2_w65l\n" \
"s_branch .LP2_end\n" \
".LP2_w65d:\n" \
"v_readlane_b32 s43, v202, 5\n" \
"s_nop 4\n" \
"buffer_load_dwordx4 a[40:43], v201, s[8:11], s43 offen sc1\n" \
"buffer_load_dwordx4 a[44:47], v201, s[8:11], s43 offen offset:1024 sc1\n" \
"v_readlane_b32 s43, v202, 12\n" \
"s_nop 4\n" \
"buffer_load_dwordx4 a[96:99], v201, s[8:11], s43 offen sc1\n" \
"buffer_load_dwordx4 a[100:103], v201, s[8:11], s43 offen offset:1024 sc1\n" \
"v_readlane_b32 s43, v202, 19\n" \
"s_nop 4\n" \
"buffer_load_dwordx4 a[152:155], v201, s[8:11], s43 offen sc1\n" \
"buffer_load_dwordx4 a[156:159], v201, s[8:11], s43 offen offset:1024 sc1\n" \
"v_readlane_b32 s43, v202, 26\n" \
"s_nop 4\n" \
"buffer_load_dwordx4 a[208:211], v201, s[8:11], s43 offen sc1\n" \
"buffer_load_dwordx4 a[212:215], v201, s[8:11], s43 offen offset:1024 sc1\n" \
".LP2_pre:\n" \
"s_waitcnt vmcnt(0)\n" \
"s_barrier\n" \
"ds_read_b96 v[248:250], v252\n" \
"s_waitcnt lgkmcnt(0)\n" \
"v_readfirstlane_b32 s44, v248\n" \
"v_readfirstlane_b32 s45, v249\n" \
"v_readfirstlane_b32 s46, v250\n" \
"s_mul_i32 s42, s25, s46\n" \
"s_lshr_b32 s42, s42, 12\n" \
".LP2_cu:\n" \
"s_sub_u32 s43, s24, s44\n" \
"s_cmp_lt_i32 s43, 0\n" \
"s_cbranch_scc0 .LP2_cud\n" \
"s_add_u32 s24, s24, s25\n" \
"s_xor_b32 s31, s31, s29\n" \
"s_xor_b32 s32, s32, s29\n" \
"s_xor_b32 s47, s47, s29\n" \
"s_add_u32 s39, s39, 1\n" \
"s_branch .LP2_cu\n" \
".LP2_cud:\n" \
"s_mov_b32 s3, s46\n" \
"s_add_u32 s43, s40, 1\n" \
"s_and_b32 s43, s43, 3\n" \
"s_bitcmp1_b32 s43, 0\n" \
"s_cselect_b32 s48, s32, s31\n" \
"s_lshl_b32 s43, s43, 19\n" \
"s_add_u32 s48, s48, s43\n" \
"s_add_u32 s43, s40, 2\n" \
"s_and_b32 s43, s43, 3\n" \
"s_bitcmp1_b32 s43, 0\n" \
"s_cselect_b32 s49, s32, s31\n" \
"s_lshl_b32 s43, s43, 19\n" \
"s_add_u32 s49, s49, s43\n" \
"s_sub_u32 s43, s24, s44\n" \
"s_add_u32 s43, s43, s28\n" \
"s_max_i32 s43, s43, 0xfffd0000\n" \
"s_min_i32 s43, s43, 0x4000\n" \
"s_mul_i32 s43, s43, s46\n" \
"s_ashr_i32 s43, s43, 12\n" \
"s_add_u32 s41, s45, s43\n" \
"s_lshr_b32 s35, s42, 1\n" \
"s_sub_u32 s35, 0, s35\n" \
".LP2_loop:\n" \
"s_branch .LP2_m1\n" \
".LP2_retry1:\n" \
"s_add_u32 s51, s51, 1\n" \
"s_cmp_gt_u32 s51, 0x20000\n" \
"s_cbranch_scc1 .LP2_end\n" \
".LP2_m0:\n" \
"s_add_u32 s41, s41, s42\n" \
"s_add_u32 s24, s24, s25\n" \
"s_xor_b32 s31, s31, s29\n" \
"s_xor_b32 s32, s32, s29\n" \
"s_xor_b32 s47, s47, s29\n" \
"s_xor_b32 s48, s48, s29\n" \
"s_add_u32 s39, s39, 1\n" \
"s_bitcmp1_b32 s31, 21\n" \
"s_cselect_b32 s43, s29, 0\n" \
"v_mov_b32 v116, s43\n" \
"ds_write_b32 v252, v116 offset:16\n" \
".LP2_m1:\n" \
"s_memtime vcc\n" \
"s_waitcnt lgkmcnt(0)\n" \
"s_sub_u32 s43, s41, vcc_lo\n" \
"s_cmp_gt_i32 s43, 0\n" \
"s_cbranch_scc1 .LP2_m1\n" \
"s_cmp_lt_i32 s43, s35\n" \
"s_cbranch_scc1 .LP2_m0\n" \
"buffer_load_dwordx4 v[0:3], v209, s[4:7], s47 offen offset:1024 sc1\n" \
"buffer_load_dwordx4 v[4:7], v209, s[4:7], s47 offen offset:2048 sc1\n" \
"s_cmp_lg_u32 s39, s34\n" \
"s_cselect_b32 s43, 24, -3\n" \
"s_add_u32 s33, s33, s43\n" \
"s_max_i32 s33, s33, 0\n" \
"s_min_i32 s33, s33, 60\n" \
"s_mov_b32 s34, s39\n" \
"s_add_u32 s3, s3, 6\n" \
"s_min_u32 s3, s3, s46\n" \
"s_sub_u32 s43, 6042, s3\n" \
"s_mov_b32 s50, s33\n" \
"s_mul_i32 s36, s43, 185\n" \
"s_ashr_i32 s36, s36, 10\n" \
"s_add_u32 s36, s36, s50\n" \
"s_add_u32 s36, s36, 1045\n" \
"v_mov_b32 v110, s36\n" \
"s_mul_i32 s36, s43, 148\n" \
"s_ashr_i32 s36, s36, 10\n" \
"s_add_u32 s36, s36, s50\n" \
"s_add_u32 s36, s36, 830\n" \
"v_mov_b32 v111, s36\n" \
"s_nop 15\n" \
"s_nop 13\n" \
"s_mul_i32 s50, s40, 114688\n" \
"buffer_store_dwordx4 v[68:71], v218, s[12:15], s50 offen sc1\n" \
"buffer_store_dword v112, v213, s[4:7], s49 offen sc1\n" \
"s_xor_b32 s43, s49, s29\n" \
"buffer_store_dword v112, v213, s[4:7], s43 offen sc1\n" \
"s_lshl_b32 s43, s40, 7\n" \
"buffer_store_dwordx2 v[110:111], v222, s[4:7], s43 offen offset:384 sc1\n" \
"v_mov_b32 v28, 0\n" \
"v_mov_b32 v29, 0\n" \
"v_mov_b32 v30, 0\n" \
"v_mov_b32 v31, 0\n" \
"v_mov_b32 v32, 0\n" \
"v_mov_b32 v33, 0\n" \
"v_mov_b32 v34, 0\n" \
"v_mov_b32 v35, 0\n" \
"v_mov_b32 v36, 0\n" \
"v_mov_b32 v37, 0\n" \
"v_mov_b32 v38, 0\n" \
"v_mov_b32 v39, 0\n" \
"v_mov_b32 v40, 0\n" \
"v_mov_b32 v41, 0\n" \
"v_mov_b32 v42, 0\n" \
"v_mov_b32 v43, 0\n" \
"s_waitcnt vmcnt(5)\n" \
"ds_write_b128 v220, v[0:3] offset:5120\n" \
"s_waitcnt vmcnt(4)\n" \
"ds_write_b128 v220, v[4:7] offset:6144\n" \
"v_max3_u32 v44, v0, v0, v1\n" \
"v_max3_u32 v44, v44, v2, v3\n" \
"v_max3_u32 v44, v44, v4, v5\n" \
"v_max3_u32 v44, v44, v6, v7\n" \
"v_cmp_eq_u32 vcc, -1, v44\n" \
"v_smfmac_f32_16x16x64_bf16 v[28:31], v[0:3], a[0:7], v200\n" \
"v_smfmac_f32_16x16x64_bf16 v[32:35], v[0:3], a[56:63], v200\n" \
"s_cbranch_vccnz .LP2_retry1\n" \
"s_waitcnt lgkmcnt(0)\n" \
"s_barrier\n" \
"ds_read_b128 v[8:11], v220 offset:0\n" \
"ds_read_b128 v[12:15], v220 offset:1024\n" \
"ds_read_b128 v[16:19], v220 offset:2048\n" \
"ds_read_b128 v[20:23], v220 offset:3072\n" \
"ds_read_b128 v[24:27], v220 offset:4096\n" \
"ds_read_b64 v[246:247], v252 offset:24\n" \
"ds_read_b96 v[248:250], v252\n" \
"v_smfmac_f32_16x16x64_bf16 v[36:39], v[0:3], a[112:119], v200\n" \
"v_smfmac_f32_16x16x64_bf16 v[40:43], v[0:3], a[168:175], v200\n" \
"v_smfmac_f32_16x16x64_bf16 v[28:31], v[4:7], a[8:15], v200\n" \
"v_smfmac_f32_16x16x64_bf16 v[32:35], v[4:7], a[64:71], v200\n" \
"v_smfmac_f32_16x16x64_bf16 v[36:39], v[4:7], a[120:127], v200\n" \
"v_smfmac_f32_16x16x64_bf16 v[40:43], v[4:7], a[176:183], v200\n" \
"s_waitcnt lgkmcnt(6)\n" \
"v_smfmac_f32_16x16x64_bf16 v[28:31], v[8:11], a[16:23], v200\n" \
"v_smfmac_f32_16x16x64_bf16 v[32:35], v[8:11], a[72:79], v200\n" \
"v_smfmac_f32_16x16x64_bf16 v[36:39], v[8:11], a[128:135], v200\n" \
"v_smfmac_f32_16x16x64_bf16 v[40:43], v[8:11], a[184:191], v200\n" \
"s_waitcnt lgkmcnt(0)\n" \
"v_readfirstlane_b32 s44, v248\n" \
"v_readfirstlane_b32 s45, v249\n" \
"v_smfmac_f32_16x16x64_bf16 v[28:31], v[12:15], a[24:31], v200\n" \
"v_readfirstlane_b32 s46, v250\n" \
"v_readfirstlane_b32 s26, v246\n" \
"v_readfirstlane_b32 s27, v247\n" \
"v_smfmac_f32_16x16x64_bf16 v[32:35], v[12:15], a[80:87], v200\n" \
"s_mul_i32 s42, s25, s46\n" \
"s_lshr_b32 s42, s42, 12\n" \
"s_add_u32 s24, s24, s26\n" \
"v_smfmac_f32_16x16x64_bf16 v[36:39], v[12:15], a[136:143], v200\n" \
"s_sub_u32 s43, s24, s44\n" \
"s_max_i32 s43, s43, 0xfffd0000\n" \
"s_min_i32 s43, s43, 0x4000\n" \
"v_smfmac_f32_16x16x64_bf16 v[40:43], v[12:15], a[192:199], v200\n" \
"s_mul_i32 s43, s43, s46\n" \
"s_ashr_i32 s43, s43, 12\n" \
"s_add_u32 s41, s45, s43\n" \
"s_cmp_eq_u32 s40, 63\n" \
"s_cbranch_scc1 .LP2_last\n" \
"s_branch .LP2_z1\n" \
".LP2_z0:\n" \
"s_add_u32 s41, s41, s42\n" \
"s_add_u32 s24, s24, s25\n" \
"s_xor_b32 s31, s31, s29\n" \
"s_xor_b32 s32, s32, s29\n" \
"s_xor_b32 s47, s47, s29\n" \
"s_xor_b32 s48, s48, s29\n" \
"s_add_u32 s39, s39, 1\n" \
"buffer_store_dwordx4 v[112:115], v223, s[4:7], s48 offen sc1\n" \
"s_bitcmp1_b32 s31, 21\n" \
"s_cselect_b32 s43, s29, 0\n" \
"v_mov_b32 v116, s43\n" \
"ds_write_b32 v252, v116 offset:16\n" \
".LP2_z1:\n" \
"s_memtime vcc\n" \
"s_waitcnt lgkmcnt(0)\n" \
"s_sub_u32 s43, s41, vcc_lo\n" \
"s_cmp_gt_i32 s43, 200\n" \
"s_cbranch_scc1 .LP2_z1\n" \
"s_cmp_lt_i32 s43, s37\n" \
"s_cbranch_scc1 .LP2_z0\n" \
"buffer_load_dwordx2 v[64:65], v211, s[4:7], s47 offen sc1\n" \
".p2align 6\n" \
".LP2_b1:\n" \
"s_memtime vcc\n" \
"s_waitcnt lgkmcnt(0)\n" \
"s_sub_u32 s43, s41, vcc_lo\n" \
"s_cmp_gt_i32 s43, 128\n" \
"s_cbranch_scc1 .LP2_b1\n" \
"s_cmp_lt_i32 s43, s37\n" \
"s_cbranch_scc1 .LP2_z0\n" \
"s_max_i32 s43, s43, 0\n" \
"s_and_b32 s43, s43, -4\n" \
"s_getpc_b64 vcc\n" \
".LP2_b3:\n" \
"s_sub_u32 s43, .LP2_b4-.LP2_b3, s43\n" \
"s_add_u32 vcc_lo, vcc_lo, s43\n" \
"s_addc_u32 vcc_hi, vcc_hi, 0\n" \
"s_setpc_b64 vcc\n" \
".rept 32\n" \
"s_nop 0\n" \
".endr\n" \
".LP2_b4:\n" \
"buffer_load_dwordx4 v[56:59], v210, s[4:7], s47 offen sc1\n" \
"buffer_load_dwordx4 v[60:63], v210, s[4:7], s47 offen offset:256 sc1\n" \
"v_smfmac_f32_16x16x64_bf16 v[28:31], v[16:19], a[32:39], v200\n" \
"v_smfmac_f32_16x16x64_bf16 v[32:35], v[16:19], a[88:95], v200\n" \
"v_smfmac_f32_16x16x64_bf16 v[36:39], v[16:19], a[144:151], v200\n" \
"v_smfmac_f32_16x16x64_bf16 v[40:43], v[16:19], a[200:207], v200\n" \
"v_smfmac_f32_16x16x64_bf16 v[28:31], v[20:23], a[40:47], v200\n" \
"v_smfmac_f32_16x16x64_bf16 v[32:35], v[20:23], a[96:103], v200\n" \
"buffer_load_dwordx4 v[50:53], v212, s[4:7], s47 offen sc1\n" \
"v_smfmac_f32_16x16x64_bf16 v[36:39], v[20:23], a[152:159], v200\n" \
"v_smfmac_f32_16x16x64_bf16 v[40:43], v[20:23], a[208:215], v200\n" \
"v_smfmac_f32_16x16x64_bf16 v[28:31], v[24:27], a[48:55], v200\n" \
"v_smfmac_f32_16x16x64_bf16 v[32:35], v[24:27], a[104:111], v200\n" \
"v_smfmac_f32_16x16x64_bf16 v[36:39], v[24:27], a[160:167], v200\n" \
"v_smfmac_f32_16x16x64_bf16 v[40:43], v[24:27], a[216:223], v200\n" \
"s_nop 2\n" \
"v_mov_b32 v245, v245\n" \
"v_mov_b32 v245, v245\n" \
"v_mov_b32 v245, v245\n" \
"v_permlane32_swap_b32 v28, v32\n" \
"v_permlane32_swap_b32 v29, v33\n" \
"v_permlane32_swap_b32 v30, v34\n" \
"v_permlane32_swap_b32 v31, v35\n" \
"v_add_f32 v28, v28, v32\n" \
"v_add_f32 v29, v29, v33\n" \
"v_add_f32 v30, v30, v34\n" \
"v_add_f32 v31, v31, v35\n" \
"v_cvt_pk_bf16_f32 v68, v28, v29\n" \
"v_cvt_pk_bf16_f32 v69, v30, v31\n" \
"v_permlane32_swap_b32 v36, v40\n" \
"v_permlane32_swap_b32 v37, v41\n" \
"v_permlane32_swap_b32 v38, v42\n" \
"v_permlane32_swap_b32 v39, v43\n" \
"v_add_f32 v36, v36, v40\n" \
"v_add_f32 v37, v37, v41\n" \
"v_add_f32 v38, v38, v42\n" \
"v_add_f32 v39, v39, v43\n" \
"v_cvt_pk_bf16_f32 v70, v36, v37\n" \
"v_cvt_pk_bf16_f32 v71, v38, v39\n" \
"v_mov_b32 v48, v68\n" \
"v_mov_b32 v49, v69\n" \
"v_mov_b32 v54, v70\n" \
"v_mov_b32 v55, v71\n" \
"s_nop 1\n" \
"v_permlane16_swap_b32 v48, v54\n" \
"v_permlane16_swap_b32 v49, v55\n" \
".LP2_wait:\n" \
"s_waitcnt vmcnt(1)\n" \
"v_pk_add_f32 v[72:73], v[56:57], v[60:61]\n" \
"v_pk_add_f32 v[74:75], v[58:59], v[62:63]\n" \
"v_max3_f32 v77, v72, v73, v64\n" \
"v_max3_f32 v77, v77, v74, v75\n" \
"s_nop 1\n" \
"v_max_f32_dpp v76, v77, v77 row_ror:8 row_mask:0xf bank_mask:0xf\n" \
"v_pk_add_f32 v[72:73], v[72:73], v[76:77] op_sel_hi:[1,0] neg_lo:[0,1] neg_hi:[0,1]\n" \
"v_pk_add_f32 v[74:75], v[74:75], v[76:77] op_sel_hi:[1,0] neg_lo:[0,1] neg_hi:[0,1]\n" \
"v_sub_f32 v78, v64, v76\n" \
"v_exp_f32 v72, v72\n" \
"v_exp_f32 v73, v73\n" \
"v_exp_f32 v74, v74\n" \
"v_exp_f32 v75, v75\n" \
"v_exp_f32 v78, v78\n" \
"v_pk_add_f32 v[80:81], v[72:73], v[74:75]\n" \
"v_fma_f32 v80, v78, v65, v80\n" \
"v_add_f32 v81, v80, v81\n" \
"s_nop 1\n" \
"v_add_f32_dpp v80, v81, v81 row_ror:8 row_mask:0xf bank_mask:0xf\n" \
"v_rcp_f32 v80, v80\n" \
"s_nop 0\n" \
"v_mul_f32 v78, v78, v80\n" \
"v_pk_mul_f32 v[72:73], v[72:73], v[80:81] op_sel_hi:[1,0]\n" \
"v_pk_mul_f32 v[74:75], v[74:75], v[80:81] op_sel_hi:[1,0]\n" \
"v_cvt_pk_bf16_f32 v78, v78, v78\n" \
"v_cvt_pk_bf16_f32 v82, v72, v73\n" \
"v_cvt_pk_bf16_f32 v83, v74, v75\n" \
"v_and_b32 v84, v78, v214\n" \
"v_and_b32 v85, v78, v215\n" \
"v_cvt_pk_bf16_f32 v86, v72, v73\n" \
"v_cvt_pk_bf16_f32 v87, v74, v75\n" \
"s_waitcnt vmcnt(0)\n" \
"v_mfma_f32_16x16x32_bf16 v[88:91], v[48:51], v[82:85], 0\n" \
"v_mfma_f32_16x16x32_bf16 v[92:95], v[52:55], v[84:87], 0\n" \
"s_nop 2\n" \
"v_mov_b32 v245, v245\n" \
"s_nop 2\n" \
"v_mov_b32 v245, v245\n" \
"v_add_f32_dpp v96, v92, v88 row_ror:8 row_mask:0xf bank_mask:0x3\n" \
"v_add_f32_dpp v97, v93, v89 row_ror:8 row_mask:0xf bank_mask:0x3\n" \
"v_add_f32_dpp v96, v90, v94 row_ror:8 row_mask:0xf bank_mask:0xc\n" \
"v_add_f32_dpp v97, v91, v95 row_ror:8 row_mask:0xf bank_mask:0xc\n" \
"ds_write_b64 v216, v[96:97]\n" \
"v_cmp_u_f32 vcc, v96, v97\n" \
"s_bitcmp1_b32 s31, 21\n" \
"s_cselect_b32 s18, s29, 0\n" \
"s_xor_b32 s18, s18, s48\n" \
"v_mov_b32 v245, v245\n" \
"s_xor_b32 s19, s18, s29\n" \
"s_nop 0\n" \
"s_nop 0\n" \
"v_mov_b32 v245, v245\n" \
"s_nop 0\n" \
"s_nop 0\n" \
"s_nop 0\n" \
"v_mov_b32 v245, v245\n" \
"s_cbranch_vccnz .LP2_cls\n" \
".LP2_clsr:\n" \
"s_waitcnt lgkmcnt(0)\n" \
"s_barrier\n" \
"ds_read_b64 v[100:101], v217 offset:0\n" \
"ds_read_b64 v[102:103], v217 offset:512\n" \
"ds_read_b32 v116, v252 offset:16\n" \
"ds_read_b64 v[104:105], v217 offset:1024\n" \
"ds_read_b64 v[106:107], v217 offset:1536\n" \
"v_mov_b32 v245, v245\n" \
"v_mov_b32 v245, v245\n" \
"s_add_u32 s40, s40, 1\n" \
"s_add_u32 s24, s24, s27\n" \
"s_mov_b32 s47, s48\n" \
"v_mov_b32 v245, v245\n" \
"s_waitcnt lgkmcnt(2)\n" \
"v_xad_u32 v117, v116, s18, v213\n" \
"v_pk_add_f32 v[100:101], v[100:101], v[102:103]\n" \
"s_waitcnt lgkmcnt(0)\n" \
"v_pk_add_f32 v[104:105], v[104:105], v[106:107]\n" \
"v_pk_add_f32 v[100:101], v[100:101], v[104:105]\n" \
"v_cvt_pk_bf16_f32 v96, v100, v101\n" \
"s_add_u32 s43, s40, 1\n" \
"s_and_b32 s43, s43, 3\n" \
"s_bitcmp1_b32 s43, 0\n" \
"s_cselect_b32 s48, s32, s31\n" \
"s_lshl_b32 s43, s43, 19\n" \
"s_add_u32 s48, s48, s43\n" \
"s_add_u32 s43, s40, 2\n" \
"s_and_b32 s43, s43, 3\n" \
"buffer_store_dword v96, v117, s[4:7], 0 offen sc1\n" \
"s_bitcmp1_b32 s43, 0\n" \
"s_cselect_b32 s49, s32, s31\n" \
"s_lshl_b32 s43, s43, 19\n" \
"s_add_u32 s49, s49, s43\n" \
"s_sub_u32 s43, s24, s44\n" \
"s_add_u32 s43, s43, s28\n" \
"s_max_i32 s43, s43, 0xfffd0000\n" \
"s_min_i32 s43, s43, 0x4000\n" \
"s_mul_i32 s43, s43, s46\n" \
"s_ashr_i32 s43, s43, 12\n" \
"s_add_u32 s41, s45, s43\n" \
"s_lshr_b32 s35, s42, 1\n" \
"s_sub_u32 s35, 0, s35\n" \
"s_nop 6\n" \
"v_xad_u32 v116, v116, s19, v213\n" \
"buffer_store_dword v96, v116, s[4:7], 0 offen sc1\n" \
"s_cmp_eq_u32 s40, 0\n" \
"s_cbranch_scc1 .LP2_wet\n" \
"s_branch .LP2_loop\n" \
"s_branch .LP2_y1\n" \
".LP2_retry2:\n" \
"s_add_u32 s51, s51, 1\n" \
"s_cmp_gt_u32 s51, 0x20000\n" \
"s_cbranch_scc1 .LP2_end\n" \
".LP2_y0:\n" \
"s_add_u32 s41, s41, s42\n" \
"s_add_u32 s24, s24, s25\n" \
"s_xor_b32 s31, s31, s29\n" \
"s_xor_b32 s32, s32, s29\n" \
"s_xor_b32 s47, s47, s29\n" \
"s_xor_b32 s48, s48, s29\n" \
"s_add_u32 s39, s39, 1\n" \
"buffer_store_dwordx4 v[112:115], v223, s[4:7], s48 offen sc1\n" \
"s_bitcmp1_b32 s31, 21\n" \
"s_cselect_b32 s43, s29, 0\n" \
"v_mov_b32 v116, s43\n" \
"ds_write_b32 v252, v116 offset:16\n" \
".LP2_y1:\n" \
"s_memtime vcc\n" \
"s_waitcnt lgkmcnt(0)\n" \
"s_sub_u32 s43, s41, vcc_lo\n" \
"s_cmp_gt_i32 s43, 200\n" \
"s_cbranch_scc1 .LP2_y1\n" \
"s_cmp_lt_i32 s43, s37\n" \
"s_cbranch_scc1 .LP2_y0\n" \
"buffer_load_dwordx2 v[64:65], v211, s[4:7], s47 offen sc1\n" \
".p2align 6\n" \
".LP2_c1:\n" \
"s_memtime vcc\n" \
"s_waitcnt lgkmcnt(0)\n" \
"s_sub_u32 s43, s41, vcc_lo\n" \
"s_cmp_gt_i32 s43, 128\n" \
"s_cbranch_scc1 .LP2_c1\n" \
"s_cmp_lt_i32 s43, s37\n" \
"s_cbranch_scc1 .LP2_y0\n" \
"s_max_i32 s43, s43, 0\n" \
"s_and_b32 s43, s43, -4\n" \
"s_getpc_b64 vcc\n" \
".LP2_c3:\n" \
"s_sub_u32 s43, .LP2_c4-.LP2_c3, s43\n" \
"s_add_u32 vcc_lo, vcc_lo, s43\n" \
"s_addc_u32 vcc_hi, vcc_hi, 0\n" \
"s_setpc_b64 vcc\n" \
".rept 32\n" \
"s_nop 0\n" \
".endr\n" \
".LP2_c4:\n" \
"buffer_load_dwordx4 v[56:59], v210, s[4:7], s47 offen sc1\n" \
"buffer_load_dwordx4 v[60:63], v210, s[4:7], s47 offen offset:256 sc1\n" \
"s_nop 2\n" \
"v_mov_b32 v245, v245\n" \
"s_nop 2\n" \
"v_mov_b32 v245, v245\n" \
"s_nop 2\n" \
"v_mov_b32 v245, v245\n" \
"s_nop 2\n" \
"v_mov_b32 v245, v245\n" \
"s_nop 2\n" \
"v_mov_b32 v245, v245\n" \
"s_nop 2\n" \
"v_mov_b32 v245, v245\n" \
"buffer_load_dwordx4 v[50:53], v212, s[4:7], s47 offen sc1\n" \
"s_nop 2\n" \
"v_mov_b32 v245, v245\n" \
"s_nop 2\n" \
"v_mov_b32 v245, v245\n" \
"s_nop 2\n" \
"v_mov_b32 v245, v245\n" \
"s_nop 2\n" \
"v_mov_b32 v245, v245\n" \
"s_nop 2\n" \
"v_mov_b32 v245, v245\n" \
"s_nop 2\n" \
"v_mov_b32 v245, v245\n" \
"s_nop 2\n" \
"v_mov_b32 v245, v245\n" \
"s_nop 2\n" \
"v_mov_b32 v245, v245\n" \
"s_nop 2\n" \
"v_mov_b32 v245, v245\n" \
"s_nop 2\n" \
"v_mov_b32 v245, v245\n" \
"s_nop 2\n" \
"v_mov_b32 v245, v245\n" \
"s_nop 2\n" \
"v_mov_b32 v245, v245\n" \
"s_nop 2\n" \
"v_mov_b32 v245, v245\n" \
"s_nop 2\n" \
"v_mov_b32 v245, v245\n" \
"s_nop 2\n" \
"v_mov_b32 v245, v245\n" \
"s_nop 2\n" \
"v_mov_b32 v245, v245\n" \
"s_nop 2\n" \
"v_mov_b32 v245, v245\n" \
"s_branch .LP2_wait\n" \
".LP2_cls:\n" \
"s_waitcnt vmcnt(0)\n" \
"v_max3_u32 v44, v64, v64, v65\n" \
"v_max3_u32 v44, v44, v50, v51\n" \
"v_max3_u32 v44, v44, v52, v53\n" \
"v_cmp_eq_u32 vcc, -1, v44\n" \
"s_cbranch_vccnz .LP2_retry2\n" \
"v_cmp_u_f32 vcc, v56, v56\n" \
"v_cmp_ne_u32 s[52:53], v56, v109\n" \
"s_and_b64 vcc, vcc, s[52:53]\n" \
"s_cbranch_vccnz .LP2_retry2\n" \
"v_cmp_u_f32 vcc, v57, v57\n" \
"v_cmp_ne_u32 s[52:53], v57, v109\n" \
"s_and_b64 vcc, vcc, s[52:53]\n" \
"s_cbranch_vccnz .LP2_retry2\n" \
"v_cmp_u_f32 vcc, v58, v58\n" \
"v_cmp_ne_u32 s[52:53], v58, v109\n" \
"s_and_b64 vcc, vcc, s[52:53]\n" \
"s_cbranch_vccnz .LP2_retry2\n" \
"v_cmp_u_f32 vcc, v59, v59\n" \
"v_cmp_ne_u32 s[52:53], v59, v109\n" \
"s_and_b64 vcc, vcc, s[52:53]\n" \
"s_cbranch_vccnz .LP2_retry2\n" \
"v_cmp_u_f32 vcc, v60, v60\n" \
"v_cmp_ne_u32 s[52:53], v60, v109\n" \
"s_and_b64 vcc, vcc, s[52:53]\n" \
"s_cbranch_vccnz .LP2_retry2\n" \
"v_cmp_u_f32 vcc, v61, v61\n" \
"v_cmp_ne_u32 s[52:53], v61, v109\n" \
"s_and_b64 vcc, vcc, s[52:53]\n" \
"s_cbranch_vccnz .LP2_retry2\n" \
"v_cmp_u_f32 vcc, v62, v62\n" \
"v_cmp_ne_u32 s[52:53], v62, v109\n" \
"s_and_b64 vcc, vcc, s[52:53]\n" \
"s_cbranch_vccnz .LP2_retry2\n" \
"v_cmp_u_f32 vcc, v63, v63\n" \
"v_cmp_ne_u32 s[52:53], v63, v109\n" \
"s_and_b64 vcc, vcc, s[52:53]\n" \
"s_cbranch_vccnz .LP2_retry2\n" \
"s_branch .LP2_clsr\n" \
".LP2_last:\n" \
"v_smfmac_f32_16x16x64_bf16 v[28:31], v[16:19], a[32:39], v200\n" \
"v_smfmac_f32_16x16x64_bf16 v[32:35], v[16:19], a[88:95], v200\n" \
"v_smfmac_f32_16x16x64_bf16 v[36:39], v[16:19], a[144:151], v200\n" \
"v_smfmac_f32_16x16x64_bf16 v[40:43], v[16:19], a[200:207], v200\n" \
"v_smfmac_f32_16x16x64_bf16 v[28:31], v[20:23], a[40:47], v200\n" \
"v_smfmac_f32_16x16x64_bf16 v[32:35], v[20:23], a[96:103], v200\n" \
"v_smfmac_f32_16x16x64_bf16 v[36:39], v[20:23], a[152:159], v200\n" \
"v_smfmac_f32_16x16x64_bf16 v[40:43], v[20:23], a[208:215], v200\n" \
"v_smfmac_f32_16x16x64_bf16 v[28:31], v[24:27], a[48:55], v200\n" \
"v_smfmac_f32_16x16x64_bf16 v[32:35], v[24:27], a[104:111], v200\n" \
"v_smfmac_f32_16x16x64_bf16 v[36:39], v[24:27], a[160:167], v200\n" \
"v_smfmac_f32_16x16x64_bf16 v[40:43], v[24:27], a[216:223], v200\n" \
"s_waitcnt vmcnt(0)\n" \
"s_nop 7\n" \
"v_permlane32_swap_b32 v28, v32\n" \
"v_permlane32_swap_b32 v29, v33\n" \
"v_permlane32_swap_b32 v30, v34\n" \
"v_permlane32_swap_b32 v31, v35\n" \
"v_add_f32 v28, v28, v32\n" \
"v_add_f32 v29, v29, v33\n" \
"v_add_f32 v30, v30, v34\n" \
"v_add_f32 v31, v31, v35\n" \
"v_cvt_pk_bf16_f32 v68, v28, v29\n" \
"v_cvt_pk_bf16_f32 v69, v30, v31\n" \
"v_permlane32_swap_b32 v36, v40\n" \
"v_permlane32_swap_b32 v37, v41\n" \
"v_permlane32_swap_b32 v38, v42\n" \
"v_permlane32_swap_b32 v39, v43\n" \
"v_add_f32 v36, v36, v40\n" \
"v_add_f32 v37, v37, v41\n" \
"v_add_f32 v38, v38, v42\n" \
"v_add_f32 v39, v39, v43\n" \
"v_cvt_pk_bf16_f32 v70, v36, v37\n" \
"v_cvt_pk_bf16_f32 v71, v38, v39\n" \
"s_mov_b32 s50, 7340032\n" \
"buffer_store_dwordx4 v[68:71], v218, s[12:15], s50 offen sc1\n" \
"s_waitcnt vmcnt(0)\n" \
"s_barrier\n" \
".LP2_end:\n" \
"s_endpgm\n" \
".p2align 6\n" \
"fk_role_P3:\n" \
"ds_read_b32 v200, v255 offset:0\n" \
"ds_read_b32 v201, v255 offset:256\n" \
"ds_read_b32 v202, v255 offset:512\n" \
"ds_read_b32 v203, v255 offset:768\n" \
"ds_read_b32 v204, v255 offset:1024\n" \
"ds_read_b32 v205, v255 offset:1280\n" \
"ds_read_b32 v206, v255 offset:1536\n" \
"ds_read_b32 v207, v255 offset:1792\n" \
"ds_read_b32 v208, v255 offset:2048\n" \
"ds_read_b32 v209, v255 offset:2304\n" \
"ds_read_b32 v210, v255 offset:2560\n" \
"ds_read_b32 v211, v255 offset:2816\n" \
"ds_read_b32 v212, v255 offset:3072\n" \
"ds_read_b32 v213, v255 offset:3328\n" \
"ds_read_b32 v214, v255 offset:3584\n" \
"ds_read_b32 v215, v255 offset:3840\n" \
"ds_read_b32 v216, v255 offset:4096\n" \
"ds_read_b32 v217, v255 offset:4352\n" \
"ds_read_b32 v218, v255 offset:4608\n" \
"ds_read_b32 v219, v255 offset:4864\n" \
"ds_read_b32 v220, v255 offset:5120\n" \
"ds_read_b32 v221, v255 offset:5376\n" \
"ds_read_b32 v222, v255 offset:5632\n" \
"ds_read_b32 v223, v255 offset:5888\n" \
"ds_read_b32 v254, v255 offset:7936\n" \
"s_waitcnt lgkmcnt(0)\n" \
"v_readlane_b32 s4, v254, 0\n" \
"v_readlane_b32 s5, v254, 1\n" \
"v_readlane_b32 s6, v254, 2\n" \
"v_readlane_b32 s7, v254, 3\n" \
"v_readlane_b32 s8, v254, 4\n" \
"v_readlane_b32 s9, v254, 5\n" \
"v_readlane_b32 s10, v254, 6\n" \
"v_readlane_b32 s11, v254, 7\n" \
"v_readlane_b32 s12, v254, 8\n" \
"v_readlane_b32 s13, v254, 9\n" \
"v_readlane_b32 s14, v254, 10\n" \
"v_readlane_b32 s15, v254, 11\n" \
"v_readlane_b32 s16, v254, 12\n" \
"v_readlane_b32 s17, v254, 13\n" \
"v_readlane_b32 s18, v254, 14\n" \
"v_readlane_b32 s19, v254, 15\n" \
"v_readlane_b32 s20, v254, 16\n" \
"v_readlane_b32 s21, v254, 17\n" \
"v_readlane_b32 s22, v254, 18\n" \
"v_readlane_b32 s23, v254, 19\n" \
"v_readlane_b32 s24, v254, 20\n" \
"v_readlane_b32 s25, v254, 21\n" \
"v_readlane_b32 s26, v254, 22\n" \
"v_readlane_b32 s27, v254, 23\n" \
"v_readlane_b32 s28, v254, 24\n" \
"v_readlane_b32 s29, v254, 25\n" \
"v_readlane_b32 s30, v254, 26\n" \
"v_readlane_b32 s31, v254, 27\n" \
"v_readlane_b32 s32, v254, 28\n" \
"v_readlane_b32 s33, v254, 29\n" \
"v_readlane_b32 s34, v254, 30\n" \
"v_readlane_b32 s35, v254, 31\n" \
"v_readlane_b32 s36, v254, 32\n" \
"v_readlane_b32 s37, v254, 33\n" \
"v_readlane_b32 s38, v254, 34\n" \
"v_readlane_b32 s39, v254, 35\n" \
"s_mov_b32 s40, 0\n" \
"s_mov_b32 s51, 0\n" \
"s_nop 0\n" \
"v_mov_b32 v252, s38\n" \
"s_nop 4\n" \
"v_mov_b32 v112, -1\n" \
"v_mov_b32 v113, -1\n" \
"v_mov_b32 v114, -1\n" \
"v_mov_b32 v115, -1\n" \
"v_mov_b32 v109, 0x7fc00001\n" \
"v_writelane_b32 v251, s6, 0\n" \
"v_writelane_b32 v251, s10, 1\n" \
"v_writelane_b32 v251, s14, 2\n" \
"v_writelane_b32 v251, s18, 3\n" \
"s_mov_b32 s6, 0\n" \
"s_mov_b32 s10, 0\n" \
"s_mov_b32 s14, 0\n" \
"s_mov_b32 s18, 0\n" \
"s_mov_b32 s40, -1\n" \
"s_branch .LP3_pre\n" \
".LP3_wet:\n" \
"s_waitcnt vmcnt(0)\n" \
"v_readlane_b32 s6, v251, 0\n" \
"v_readlane_b32 s10, v251, 1\n" \
"v_readlane_b32 s14, v251, 2\n" \
"v_readlane_b32 s18, v251, 3\n" \
"s_nop 4\n" \
"ds_read_b32 v232, v255 offset:7424\n" \
"ds_read_b32 v233, v255 offset:7680\n" \
"v_lshrrev_b32 v234, 4, v201\n" \
"v_and_b32 v234, 3, v234\n" \
"v_subrev_u32 v230, 512, v205\n" \
"s_add_u32 s43, s30, 1\n" \
"s_mov_b32 s50, 0\n" \
".LP3_w66l:\n" \
"buffer_load_dword v240, v230, s[4:7], 0 offen sc1\n" \
"s_waitcnt vmcnt(0)\n" \
".LP3_w66c:\n" \
"v_subrev_u32 v241, s43, v240\n" \
"v_cmp_gt_u32 vcc, 0x100, v241\n" \
"s_cmp_eq_u64 vcc, -1\n" \
"s_cbranch_scc1 .LP3_w66d\n" \
"s_sleep 2\n" \
"s_add_u32 s50, s50, 1\n" \
"s_cmp_lt_u32 s50, 0x20000\n" \
"s_cbranch_scc1 .LP3_w66l\n" \
"s_branch .LP3_end\n" \
".LP3_w66d:\n" \
"v_lshl_or_b32 v241, v241, 2, v234\n" \
"s_nop 1\n" \
"v_min_u32_dpp v241, v241, v241 row_ror:4 row_mask:0xf bank_mask:0xf\n" \
"s_nop 1\n" \
"v_min_u32_dpp v241, v241, v241 row_ror:8 row_mask:0xf bank_mask:0xf\n" \
"s_nop 1\n" \
"v_min_u32_dpp v241, v241, v241 quad_perm:[1,0,3,2] row_mask:0xf bank_mask:0xf\n" \
"s_nop 1\n" \
"v_min_u32_dpp v241, v241, v241 quad_perm:[2,3,0,1] row_mask:0xf bank_mask:0xf\n" \
"s_nop 0\n" \
"v_readlane_b32 s31, v241, 0\n" \
"v_readlane_b32 s43, v241, 16\n" \
"s_min_u32 s31, s31, s43\n" \
"v_readlane_b32 s43, v241, 32\n" \
"s_min_u32 s31, s31, s43\n" \
"v_readlane_b32 s43, v241, 48\n" \
"s_min_u32 s31, s31, s43\n" \
"v_subrev_u32 v230, 256, v205\n" \
"s_add_u32 s43, s30, 1\n" \
"s_mov_b32 s50, 0\n" \
".LP3_w67l:\n" \
"buffer_load_dword v240, v230, s[4:7], 0 offen sc1\n" \
"s_waitcnt vmcnt(0)\n" \
".LP3_w67c:\n" \
"v_subrev_u32 v241, s43, v240\n" \
"v_cmp_gt_u32 vcc, 0x100, v241\n" \
"s_cmp_eq_u64 vcc, -1\n" \
"s_cbranch_scc1 .LP3_w67d\n" \
"s_sleep 2\n" \
"s_add_u32 s50, s50, 1\n" \
"s_cmp_lt_u32 s50, 0x20000\n" \
"s_cbranch_scc1 .LP3_w67l\n" \
"s_branch .LP3_end\n" \
".LP3_w67d:\n" \
"v_lshl_or_b32 v241, v241, 2, v234\n" \
"s_nop 1\n" \
"v_min_u32_dpp v241, v241, v241 row_ror:4 row_mask:0xf bank_mask:0xf\n" \
"s_nop 1\n" \
"v_min_u32_dpp v241, v241, v241 row_ror:8 row_mask:0xf bank_mask:0xf\n" \
"s_nop 1\n" \
"v_min_u32_dpp v241, v241, v241 quad_perm:[1,0,3,2] row_mask:0xf bank_mask:0xf\n" \
"s_nop 1\n" \
"v_min_u32_dpp v241, v241, v241 quad_perm:[2,3,0,1] row_mask:0xf bank_mask:0xf\n" \
"s_nop 0\n" \
"v_readlane_b32 s32, v241, 0\n" \
"v_readlane_b32 s43, v241, 16\n" \
"s_min_u32 s32, s32, s43\n" \
"v_readlane_b32 s43, v241, 32\n" \
"s_min_u32 s32, s32, s43\n" \
"v_readlane_b32 s43, v241, 48\n" \
"s_min_u32 s32, s32, s43\n" \
"s_xor_b32 s29, s31, s32\n" \
"s_and_b32 s29, s29, 3\n" \
"s_lshl_b32 s29, s29, 13\n" \
"s_bitset1_b32 s29, 21\n" \
"s_and_b32 s31, s31, 3\n" \
"s_lshl_b32 s31, s31, 13\n" \
"s_bitcmp1_b32 s39, 0\n" \
"s_cselect_b32 s43, s29, 0\n" \
"s_xor_b32 s31, s31, s43\n" \
"s_xor_b32 s32, s31, 0x4000\n" \
"s_mov_b32 s39, 0\n" \
"v_mov_b32 v224, -1\n" \
"v_mov_b32 v225, -1\n" \
"v_mov_b32 v226, -1\n" \
"v_mov_b32 v227, -1\n" \
"s_waitcnt lgkmcnt(0)\n" \
"s_add_u32 s50, s31, 0\n" \
"buffer_store_dwordx4 v[224:227], v232, s[4:7], s50 offen sc1\n" \
"s_xor_b32 s43, s50, s29\n" \
"buffer_store_dwordx4 v[224:227], v232, s[4:7], s43 offen sc1\n" \
"s_add_u32 s50, s32, 524288\n" \
"buffer_store_dwordx4 v[224:227], v232, s[4:7], s50 offen sc1\n" \
"s_xor_b32 s43, s50, s29\n" \
"buffer_store_dwordx4 v[224:227], v232, s[4:7], s43 offen sc1\n" \
"s_add_u32 s50, s31, 1048576\n" \
"buffer_store_dwordx4 v[224:227], v232, s[4:7], s50 offen sc1\n" \
"s_xor_b32 s43, s50, s29\n" \
"buffer_store_dwordx4 v[224:227], v232, s[4:7], s43 offen sc1\n" \
"s_add_u32 s50, s32, 1572864\n" \
"buffer_store_dwordx4 v[224:227], v232, s[4:7], s50 offen sc1\n" \
"s_xor_b32 s43, s50, s29\n" \
"buffer_store_dwordx4 v[224:227], v232, s[4:7], s43 offen sc1\n" \
"s_waitcnt vmcnt(0)\n" \
"s_barrier\n" \
"s_add_u32 s43, s30, s40\n" \
"s_add_u32 s43, s43, 1\n" \
"v_mov_b32 v241, s43\n" \
"buffer_store_dword v241, v233, s[4:7], 0 offen sc1\n" \
"s_mov_b32 s33, 40\n" \
"s_mov_b32 s34, 0\n" \
"s_add_u32 s43, s30, 1\n" \
"s_mov_b32 s50, 0\n" \
".LP3_w68l:\n" \
"buffer_load_dword v240, v205, s[4:7], 0 offen sc1\n" \
"s_waitcnt vmcnt(0)\n" \
".LP3_w68c:\n" \
"v_subrev_u32 v241, s43, v240\n" \
"v_cmp_gt_u32 vcc, 0x100, v241\n" \
"s_cmp_eq_u64 vcc, -1\n" \
"s_cbranch_scc1 .LP3_w68d\n" \
"s_sleep 2\n" \
"s_add_u32 s50, s50, 1\n" \
"s_cmp_lt_u32 s50, 0x20000\n" \
"s_cbranch_scc1 .LP3_w68l\n" \
"s_branch .LP3_end\n" \
".LP3_w68d:\n" \
"s_add_u32 s43, s30, 1\n" \
"s_mov_b32 s50, 0\n" \
".LP3_w69l:\n" \
"buffer_load_dword v240, v206, s[4:7], 0 offen sc1\n" \
"s_waitcnt vmcnt(0)\n" \
".LP3_w69c:\n" \
"v_subrev_u32 v241, s43, v240\n" \
"v_cmp_gt_u32 vcc, 0x100, v241\n" \
"s_cmp_eq_u64 vcc, -1\n" \
"s_cbranch_scc1 .LP3_w69d\n" \
"s_sleep 2\n" \
"s_add_u32 s50, s50, 1\n" \
"s_cmp_lt_u32 s50, 0x20000\n" \
"s_cbranch_scc1 .LP3_w69l\n" \
"s_branch .LP3_end\n" \
".LP3_w69d:\n" \
"s_add_u32 s43, s40, 0\n" \
"s_and_b32 s43, s43, 3\n" \
"s_bitcmp1_b32 s43, 0\n" \
"s_cselect_b32 s47, s32, s31\n" \
"s_lshl_b32 s43, s43, 19\n" \
"s_add_u32 s47, s47, s43\n" \
"buffer_store_dword v219, v213, s[4:7], s47 offen sc1\n" \
"s_xor_b32 s43, s47, s29\n" \
"buffer_store_dword v219, v213, s[4:7], s43 offen sc1\n" \
"s_add_u32 s43, s30, 1\n" \
"s_mov_b32 s50, 0\n" \
".LP3_w70l:\n" \
"buffer_load_dword v240, v204, s[4:7], 0 offen sc1\n" \
"s_waitcnt vmcnt(0)\n" \
".LP3_w70c:\n" \
"v_subrev_u32 v241, s43, v240\n" \
"v_cmp_gt_u32 vcc, 0x100, v241\n" \
"s_cmp_eq_u64 vcc, -1\n" \
"s_cbranch_scc1 .LP3_w70d\n" \
"s_sleep 2\n" \
"s_add_u32 s50, s50, 1\n" \
"s_cmp_lt_u32 s50, 0x20000\n" \
"s_cbranch_scc1 .LP3_w70l\n" \
"s_branch .LP3_end\n" \
".LP3_w70d:\n" \
"v_readlane_b32 s43, v202, 1\n" \
"s_nop 4\n" \
"buffer_load_dwordx4 a[8:11], v201, s[8:11], s43 offen sc1\n" \
"buffer_load_dwordx4 a[12:15], v201, s[8:11], s43 offen offset:1024 sc1\n" \
"v_readlane_b32 s43, v202, 8\n" \
"s_nop 4\n" \
"buffer_load_dwordx4 a[64:67], v201, s[8:11], s43 offen sc1\n" \
"buffer_load_dwordx4 a[68:71], v201, s[8:11], s43 offen offset:1024 sc1\n" \
"v_readlane_b32 s43, v202, 15\n" \
"s_nop 4\n" \
"buffer_load_dwordx4 a[120:123], v201, s[8:11], s43 offen sc1\n" \
"buffer_load_dwordx4 a[124:127], v201, s[8:11], s43 offen offset:1024 sc1\n" \
"v_readlane_b32 s43, v202, 22\n" \
"s_nop 4\n" \
"buffer_load_dwordx4 a[176:179], v201, s[8:11], s43 offen sc1\n" \
"buffer_load_dwordx4 a[180:183], v201, s[8:11], s43 offen offset:1024 sc1\n" \
"s_add_u32 s43, s30, 2\n" \
"s_mov_b32 s50, 0\n" \
"s_branch .LP3_w71c\n" \
".LP3_w71l:\n" \
"buffer_load_dword v240, v204, s[4:7], 0 offen sc1\n" \
"s_waitcnt vmcnt(0)\n" \
".LP3_w71c:\n" \
"v_subrev_u32 v241, s43, v240\n" \
"v_cmp_gt_u32 vcc, 0x100, v241\n" \
"s_cmp_eq_u64 vcc, -1\n" \
"s_cbranch_scc1 .LP3_w71d\n" \
"s_sleep 2\n" \
"s_add_u32 s50, s50, 1\n" \
"s_cmp_lt_u32 s50, 0x20000\n" \
"s_cbranch_scc1 .LP3_w71l\n" \
"s_branch .LP3_end\n" \
".LP3_w71d:\n" \
"v_readlane_b32 s43, v202, 4\n" \
"s_nop 4\n" \
"buffer_load_dwordx4 a[32:35], v201, s[8:11], s43 offen sc1\n" \
"buffer_load_dwordx4 a[36:39], v201, s[8:11], s43 offen offset:1024 sc1\n" \
"v_readlane_b32 s43, v202, 11\n" \
"s_nop 4\n" \
"buffer_load_dwordx4 a[88:91], v201, s[8:11], s43 offen sc1\n" \
"buffer_load_dwordx4 a[92:95], v201, s[8:11], s43 offen offset:1024 sc1\n" \
"v_readlane_b32 s43, v202, 18\n" \
"s_nop 4\n" \
"buffer_load_dwordx4 a[144:147], v201, s[8:11], s43 offen sc1\n" \
"buffer_load_dwordx4 a[148:151], v201, s[8:11], s43 offen offset:1024 sc1\n" \
"v_readlane_b32 s43, v202, 25\n" \
"s_nop 4\n" \
"buffer_load_dwordx4 a[200:203], v201, s[8:11], s43 offen sc1\n" \
"buffer_load_dwordx4 a[204:207], v201, s[8:11], s43 offen offset:1024 sc1\n" \
"s_add_u32 s43, s30, 3\n" \
"s_mov_b32 s50, 0\n" \
"s_branch .LP3_w72c\n" \
".LP3_w72l:\n" \
"buffer_load_dword v240, v204, s[4:7], 0 offen sc1\n" \
"s_waitcnt vmcnt(0)\n" \
".LP3_w72c:\n" \
"v_subrev_u32 v241, s43, v240\n" \
"v_cmp_gt_u32 vcc, 0x100, v241\n" \
"s_cmp_eq_u64 vcc, -1\n" \
"s_cbranch_scc1 .LP3_w72d\n" \
"s_sleep 2\n" \
"s_add_u32 s50, s50, 1\n" \
"s_cmp_lt_u32 s50, 0x20000\n" \
"s_cbranch_scc1 .LP3_w72l\n" \
"s_branch .LP3_end\n" \
".LP3_w72d:\n" \
"v_readlane_b32 s43, v202, 2\n" \
"s_nop 4\n" \
"buffer_load_dwordx4 a[16:19], v201, s[8:11], s43 offen sc1\n" \
"buffer_load_dwordx4 a[20:23], v201, s[8:11], s43 offen offset:1024 sc1\n" \
"v_readlane_b32 s43, v202, 9\n" \
"s_nop 4\n" \
"buffer_load_dwordx4 a[72:75], v201, s[8:11], s43 offen sc1\n" \
"buffer_load_dwordx4 a[76:79], v201, s[8:11], s43 offen offset:1024 sc1\n" \
"v_readlane_b32 s43, v202, 16\n" \
"s_nop 4\n" \
"buffer_load_dwordx4 a[128:131], v201, s[8:11], s43 offen sc1\n" \
"buffer_load_dwordx4 a[132:135], v201, s[8:11], s43 offen offset:1024 sc1\n" \
"v_readlane_b32 s43, v202, 23\n" \
"s_nop 4\n" \
"buffer_load_dwordx4 a[184:187], v201, s[8:11], s43 offen sc1\n" \
"buffer_load_dwordx4 a[188:191], v201, s[8:11], s43 offen offset:1024 sc1\n" \
"s_add_u32 s43, s30, 4\n" \
"s_mov_b32 s50, 0\n" \
"s_branch .LP3_w73c\n" \
".LP3_w73l:\n" \
"buffer_load_dword v240, v204, s[4:7], 0 offen sc1\n" \
"s_waitcnt vmcnt(0)\n" \
".LP3_w73c:\n" \
"v_subrev_u32 v241, s43, v240\n" \
"v_cmp_gt_u32 vcc, 0x100, v241\n" \
"s_cmp_eq_u64 vcc, -1\n" \
"s_cbranch_scc1 .LP3_w73d\n" \
"s_sleep 2\n" \
"s_add_u32 s50, s50, 1\n" \
"s_cmp_lt_u32 s50, 0x20000\n" \
"s_cbranch_scc1 .LP3_w73l\n" \
"s_branch .LP3_end\n" \
".LP3_w73d:\n" \
"v_readlane_b32 s43, v202, 5\n" \
"s_nop 4\n" \
"buffer_load_dwordx4 a[40:43], v201, s[8:11], s43 offen sc1\n" \
"buffer_load_dwordx4 a[44:47], v201, s[8:11], s43 offen offset:1024 sc1\n" \
"v_readlane_b32 s43, v202, 12\n" \
"s_nop 4\n" \
"buffer_load_dwordx4 a[96:99], v201, s[8:11], s43 offen sc1\n" \
"buffer_load_dwordx4 a[100:103], v201, s[8:11], s43 offen offset:1024 sc1\n" \
"v_readlane_b32 s43, v202, 19\n" \
"s_nop 4\n" \
"buffer_load_dwordx4 a[152:155], v201, s[8:11], s43 offen sc1\n" \
"buffer_load_dwordx4 a[156:159], v201, s[8:11], s43 offen offset:1024 sc1\n" \
"v_readlane_b32 s43, v202, 26\n" \
"s_nop 4\n" \
"buffer_load_dwordx4 a[208:211], v201, s[8:11], s43 offen sc1\n" \
"buffer_load_dwordx4 a[212:215], v201, s[8:11], s43 offen offset:1024 sc1\n" \
"s_add_u32 s43, s30, 5\n" \
"s_mov_b32 s50, 0\n" \
"s_branch .LP3_w74c\n" \
".LP3_w74l:\n" \
"buffer_load_dword v240, v204, s[4:7], 0 offen sc1\n" \
"s_waitcnt vmcnt(0)\n" \
".LP3_w74c:\n" \
"v_subrev_u32 v241, s43, v240\n" \
"v_cmp_gt_u32 vcc, 0x100, v241\n" \
"s_cmp_eq_u64 vcc, -1\n" \
"s_cbranch_scc1 .LP3_w74d\n" \
"s_sleep 2\n" \
"s_add_u32 s50, s50, 1\n" \
"s_cmp_lt_u32 s50, 0x20000\n" \
"s_cbranch_scc1 .LP3_w74l\n" \
"s_branch .LP3_end\n" \
".LP3_w74d:\n" \
"v_readlane_b32 s43, v202, 3\n" \
"s_nop 4\n" \
"buffer_load_dwordx4 a[24:27], v201, s[8:11], s43 offen sc1\n" \
"buffer_load_dwordx4 a[28:31], v201, s[8:11], s43 offen offset:1024 sc1\n" \
"v_readlane_b32 s43, v202, 10\n" \
"s_nop 4\n" \
"buffer_load_dwordx4 a[80:83], v201, s[8:11], s43 offen sc1\n" \
"buffer_load_dwordx4 a[84:87], v201, s[8:11], s43 offen offset:1024 sc1\n" \
"v_readlane_b32 s43, v202, 17\n" \
"s_nop 4\n" \
"buffer_load_dwordx4 a[136:139], v201, s[8:11], s43 offen sc1\n" \
"buffer_load_dwordx4 a[140:143], v201, s[8:11], s43 offen offset:1024 sc1\n" \
"v_readlane_b32 s43, v202, 24\n" \
"s_nop 4\n" \
"buffer_load_dwordx4 a[192:195], v201, s[8:11], s43 offen sc1\n" \
"buffer_load_dwordx4 a[196:199], v201, s[8:11], s43 offen offset:1024 sc1\n" \
"s_add_u32 s43, s30, 6\n" \
"s_mov_b32 s50, 0\n" \
"s_branch .LP3_w75c\n" \
".LP3_w75l:\n" \
"buffer_load_dword v240, v204, s[4:7], 0 offen sc1\n" \
"s_waitcnt vmcnt(0)\n" \
".LP3_w75c:\n" \
"v_subrev_u32 v241, s43, v240\n" \
"v_cmp_gt_u32 vcc, 0x100, v241\n" \
"s_cmp_eq_u64 vcc, -1\n" \
"s_cbranch_scc1 .LP3_w75d\n" \
"s_sleep 2\n" \
"s_add_u32 s50, s50, 1\n" \
"s_cmp_lt_u32 s50, 0x20000\n" \
"s_cbranch_scc1 .LP3_w75l\n" \
"s_branch .LP3_end\n" \
".LP3_w75d:\n" \
"v_readlane_b32 s43, v202, 6\n" \
"s_nop 4\n" \
"buffer_load_dwordx4 a[48:51], v201, s[8:11], s43 offen sc1\n" \
"buffer_load_dwordx4 a[52:55], v201, s[8:11], s43 offen offset:1024 sc1\n" \
"v_readlane_b32 s43, v202, 13\n" \
"s_nop 4\n" \
"buffer_load_dwordx4 a[104:107], v201, s[8:11], s43 offen sc1\n" \
"buffer_load_dwordx4 a[108:111], v201, s[8:11], s43 offen offset:1024 sc1\n" \
"v_readlane_b32 s43, v202, 20\n" \
"s_nop 4\n" \
"buffer_load_dwordx4 a[160:163], v201, s[8:11], s43 offen sc1\n" \
"buffer_load_dwordx4 a[164:167], v201, s[8:11], s43 offen offset:1024 sc1\n" \
"v_readlane_b32 s43, v202, 27\n" \
"s_nop 4\n" \
"buffer_load_dwordx4 a[216:219], v201, s[8:11], s43 offen sc1\n" \
"buffer_load_dwordx4 a[220:223], v201, s[8:11], s43 offen offset:1024 sc1\n" \
"s_add_u32 s43, s30, 7\n" \
"s_mov_b32 s50, 0\n" \
"s_branch .LP3_w76c\n" \
".LP3_w76l:\n" \
"buffer_load_dword v240, v204, s[4:7], 0 offen sc1\n" \
"s_waitcnt vmcnt(0)\n" \
".LP3_w76c:\n" \
"v_subrev_u32 v241, s43, v240\n" \
"v_cmp_gt_u32 vcc, 0x100, v241\n" \
"s_cmp_eq_u64 vcc, -1\n" \
"s_cbranch_scc1 .LP3_w76d\n" \
"s_sleep 2\n" \
"s_add_u32 s50, s50, 1\n" \
"s_cmp_lt_u32 s50, 0x20000\n" \
"s_cbranch_scc1 .LP3_w76l\n" \
"s_branch .LP3_end\n" \
".LP3_w76d:\n" \
"v_readlane_b32 s43, v202, 0\n" \
"s_nop 4\n" \
"buffer_load_dwordx4 a[0:3], v201, s[8:11], s43 offen sc1\n" \
"buffer_load_dwordx4 a[4:7], v201, s[8:11], s43 offen offset:1024 sc1\n" \
"v_readlane_b32 s43, v202, 7\n" \
"s_nop 4\n" \
"buffer_load_dwordx4 a[56:59], v201, s[8:11], s43 offen sc1\n" \
"buffer_load_dwordx4 a[60:63], v201, s[8:11], s43 offen offset:1024 sc1\n" \
"v_readlane_b32 s43, v202, 14\n" \
"s_nop 4\n" \
"buffer_load_dwordx4 a[112:115], v201, s[8:11], s43 offen sc1\n" \
"buffer_load_dwordx4 a[116:119], v201, s[8:11], s43 offen offset:1024 sc1\n" \
"v_readlane_b32 s43, v202, 21\n" \
"s_nop 4\n" \
"buffer_load_dwordx4 a[168:171], v201, s[8:11], s43 offen sc1\n" \
"buffer_load_dwordx4 a[172:175], v201, s[8:11], s43 offen offset:1024 sc1\n" \
"ds_read_b96 v[248:250], v252\n" \
"s_waitcnt lgkmcnt(0)\n" \
"v_readfirstlane_b32 s44, v248\n" \
"v_readfirstlane_b32 s45, v249\n" \
"v_readfirstlane_b32 s46, v250\n" \
"s_mul_i32 s42, s25, s46\n" \
"s_lshr_b32 s42, s42, 12\n" \
"s_mov_b32 s26, s44\n" \
"s_mov_b32 s27, s45\n" \
".p2align 3\n" \
"s_memtime s[54:55]\n" \
"s_memrealtime s[56:57]\n" \
"s_memtime s[58:59]\n" \
"s_memrealtime s[60:61]\n" \
"s_nop 0\n" \
"s_nop 0\n" \
"s_memtime s[62:63]\n" \
"s_memrealtime s[64:65]\n" \
"s_memtime s[66:67]\n" \
"s_memrealtime s[68:69]\n" \
"s_memtime s[70:71]\n" \
"s_memrealtime s[72:73]\n" \
"s_memtime s[74:75]\n" \
"s_memrealtime s[76:77]\n" \
"s_nop 0\n" \
"s_nop 0\n" \
"s_memtime s[78:79]\n" \
"s_memrealtime s[80:81]\n" \
"s_memtime s[82:83]\n" \
"s_memrealtime s[84:85]\n" \
"s_memtime s[86:87]\n" \
"s_memrealtime s[88:89]\n" \
"s_memtime s[90:91]\n" \
"s_memrealtime s[92:93]\n" \
"s_nop 0\n" \
"s_nop 0\n" \
"s_memtime s[94:95]\n" \
"s_memrealtime s[96:97]\n" \
"s_memtime s[98:99]\n" \
"s_mov_b32 s52, 0\n" \
"s_mov_b32 s53, 4096\n" \
"s_waitcnt lgkmcnt(0)\n" \
"s_cmp_lg_u32 s96, s56\n" \
"s_cselect_b32 s52, s90, s52\n" \
"s_cselect_b32 s53, s98, s53\n" \
"s_cmp_lg_u32 s92, s56\n" \
"s_cselect_b32 s52, s86, s52\n" \
"s_cselect_b32 s53, s94, s53\n" \
"s_cmp_lg_u32 s88, s56\n" \
"s_cselect_b32 s52, s82, s52\n" \
"s_cselect_b32 s53, s90, s53\n" \
"s_cmp_lg_u32 s84, s56\n" \
"s_cselect_b32 s52, s78, s52\n" \
"s_cselect_b32 s53, s86, s53\n" \
"s_cmp_lg_u32 s80, s56\n" \
"s_cselect_b32 s52, s74, s52\n" \
"s_cselect_b32 s53, s82, s53\n" \
"s_cmp_lg_u32 s76, s56\n" \
"s_cselect_b32 s52, s70, s52\n" \
"s_cselect_b32 s53, s78, s53\n" \
"s_cmp_lg_u32 s72, s56\n" \
"s_cselect_b32 s52, s66, s52\n" \
"s_cselect_b32 s53, s74, s53\n" \
"s_cmp_lg_u32 s68, s56\n" \
"s_cselect_b32 s52, s62, s52\n" \
"s_cselect_b32 s53, s70, s53\n" \
"s_cmp_lg_u32 s64, s56\n" \
"s_cselect_b32 s52, s58, s52\n" \
"s_cselect_b32 s53, s66, s53\n" \
"s_cmp_lg_u32 s60, s56\n" \
"s_cselect_b32 s52, s54, s52\n" \
"s_cselect_b32 s53, s62, s53\n" \
"s_sub_u32 s53, s53, s52\n" \
"s_lshr_b32 s55, s53, 1\n" \
"s_add_u32 s52, s52, s55\n" \
"s_add_u32 s55, s56, 4\n" \
"s_lshl_b32 s55, s55, 4\n" \
"s_cmp_le_u32 s53, 40\n" \
"s_cselect_b32 s44, s55, s44\n" \
"s_cselect_b32 s45, s52, s45\n" \
"v_mov_b32 v248, s44\n" \
"v_mov_b32 v249, s45\n" \
"v_mov_b32 v250, s46\n" \
"s_mov_b64 exec, 1\n" \
"ds_write_b96 v252, v[248:250]\n" \
"s_mov_b64 exec, -1\n" \
"s_sub_u32 s26, s44, s26\n" \
"s_sub_u32 s27, s45, s27\n" \
"v_cvt_f32_u32 v248, s26\n" \
"v_cvt_f32_u32 v249, s27\n" \
"v_rcp_f32 v248, v248\n" \
"v_mul_f32 v249, 0x457ae148, v249\n" \
"v_mul_f32 v248, v248, v249\n" \
"v_cvt_u32_f32 v248, v248\n" \
"s_nop 0\n" \
"v_readfirstlane_b32 s27, v248\n" \
"s_max_u32 s27, s27, 0x800\n" \
"s_min_u32 s27, s27, 0x2000\n" \
"s_cmp_gt_u32 s26, 1600\n" \
"s_cselect_b32 s46, s27, s46\n" \
".p2align 3\n" \
"s_memtime s[54:55]\n" \
"s_memrealtime s[56:57]\n" \
"s_memtime s[58:59]\n" \
"s_memrealtime s[60:61]\n" \
"s_nop 0\n" \
"s_nop 0\n" \
"s_memtime s[62:63]\n" \
"s_memrealtime s[64:65]\n" \
"s_memtime s[66:67]\n" \
"s_memrealtime s[68:69]\n" \
"s_memtime s[70:71]\n" \
"s_memrealtime s[72:73]\n" \
"s_memtime s[74:75]\n" \
"s_memrealtime s[76:77]\n" \
"s_nop 0\n" \
"s_nop 0\n" \
"s_memtime s[78:79]\n" \
"s_memrealtime s[80:81]\n" \
"s_memtime s[82:83]\n" \
"s_memrealtime s[84:85]\n" \
"s_memtime s[86:87]\n" \
"s_memrealtime s[88:89]\n" \
"s_memtime s[90:91]\n" \
"s_memrealtime s[92:93]\n" \
"s_nop 0\n" \
"s_nop 0\n" \
"s_memtime s[94:95]\n" \
"s_memrealtime s[96:97]\n" \
"s_memtime s[98:99]\n" \
"s_mov_b32 s52, 0\n" \
"s_mov_b32 s53, 4096\n" \
"s_waitcnt lgkmcnt(0)\n" \
"s_cmp_lg_u32 s96, s56\n" \
"s_cselect_b32 s52, s90, s52\n" \
"s_cselect_b32 s53, s98, s53\n" \
"s_cmp_lg_u32 s92, s56\n" \
"s_cselect_b32 s52, s86, s52\n" \
"s_cselect_b32 s53, s94, s53\n" \
"s_cmp_lg_u32 s88, s56\n" \
"s_cselect_b32 s52, s82, s52\n" \
"s_cselect_b32 s53, s90, s53\n" \
"s_cmp_lg_u32 s84, s56\n" \
"s_cselect_b32 s52, s78, s52\n" \
"s_cselect_b32 s53, s86, s53\n" \
"s_cmp_lg_u32 s80, s56\n" \
"s_cselect_b32 s52, s74, s52\n" \
"s_cselect_b32 s53, s82, s53\n" \
"s_cmp_lg_u32 s76, s56\n" \
"s_cselect_b32 s52, s70, s52\n" \
"s_cselect_b32 s53, s78, s53\n" \
"s_cmp_lg_u32 s72, s56\n" \
"s_cselect_b32 s52, s66, s52\n" \
"s_cselect_b32 s53, s74, s53\n" \
"s_cmp_lg_u32 s68, s56\n" \
"s_cselect_b32 s52, s62, s52\n" \
"s_cselect_b32 s53, s70, s53\n" \
"s_cmp_lg_u32 s64, s56\n" \
"s_cselect_b32 s52, s58, s52\n" \
"s_cselect_b32 s53, s66, s53\n" \
"s_cmp_lg_u32 s60, s56\n" \
"s_cselect_b32 s52, s54, s52\n" \
"s_cselect_b32 s53, s62, s53\n" \
"s_sub_u32 s53, s53, s52\n" \
"s_lshr_b32 s55, s53, 1\n" \
"s_add_u32 s52, s52, s55\n" \
"s_add_u32 s55, s56, 4\n" \
"s_lshl_b32 s55, s55, 4\n" \
"s_cmp_le_u32 s53, 40\n" \
"s_cselect_b32 s44, s55, s44\n" \
"s_cselect_b32 s45, s52, s45\n" \
"v_mov_b32 v248, s44\n" \
"v_mov_b32 v249, s45\n" \
"v_mov_b32 v250, s46\n" \
"s_mov_b64 exec, 1\n" \
"ds_write_b96 v252, v[248:250]\n" \
"s_mov_b64 exec, -1\n" \
"s_waitcnt lgkmcnt(0)\n" \
".LP3_pre:\n" \
"s_waitcnt vmcnt(0)\n" \
"s_barrier\n" \
"ds_read_b96 v[248:250], v252\n" \
"s_waitcnt lgkmcnt(0)\n" \
"v_readfirstlane_b32 s44, v248\n" \
"v_readfirstlane_b32 s45, v249\n" \
"v_readfirstlane_b32 s46, v250\n" \
"s_mul_i32 s42, s25, s46\n" \
"s_lshr_b32 s42, s42, 12\n" \
".LP3_cu:\n" \
"s_sub_u32 s43, s24, s44\n" \
"s_cmp_lt_i32 s43, 0\n" \
"s_cbranch_scc0 .LP3_cud\n" \
"s_add_u32 s24, s24, s25\n" \
"s_xor_b32 s31, s31, s29\n" \
"s_xor_b32 s32, s32, s29\n" \
"s_xor_b32 s47, s47, s29\n" \
"s_add_u32 s39, s39, 1\n" \
"s_branch .LP3_cu\n" \
".LP3_cud:\n" \
"s_mov_b32 s3, s46\n" \
"s_add_u32 s43, s40, 1\n" \
"s_and_b32 s43, s43, 3\n" \
"s_bitcmp1_b32 s43, 0\n" \
"s_cselect_b32 s48, s32, s31\n" \
"s_lshl_b32 s43, s43, 19\n" \
"s_add_u32 s48, s48, s43\n" \
"s_add_u32 s43, s40, 2\n" \
"s_and_b32 s43, s43, 3\n" \
"s_bitcmp1_b32 s43, 0\n" \
"s_cselect_b32 s49, s32, s31\n" \
"s_lshl_b32 s43, s43, 19\n" \
"s_add_u32 s49, s49, s43\n" \
"s_sub_u32 s43, s24, s44\n" \
"s_add_u32 s43, s43, s28\n" \
"s_max_i32 s43, s43, 0xfffd0000\n" \
"s_min_i32 s43, s43, 0x4000\n" \
"s_mul_i32 s43, s43, s46\n" \
"s_ashr_i32 s43, s43, 12\n" \
"s_add_u32 s41, s45, s43\n" \
"s_lshr_b32 s35, s42, 1\n" \
"s_sub_u32 s35, 0, s35\n" \
".LP3_loop:\n" \
"s_branch .LP3_m1\n" \
".LP3_retry1:\n" \
"s_add_u32 s51, s51, 1\n" \
"s_cmp_gt_u32 s51, 0x20000\n" \
"s_cbranch_scc1 .LP3_end\n" \
".LP3_m0:\n" \
"s_add_u32 s41, s41, s42\n" \
"s_add_u32 s24, s24, s25\n" \
"s_xor_b32 s31, s31, s29\n" \
"s_xor_b32 s32, s32, s29\n" \
"s_xor_b32 s47, s47, s29\n" \
"s_xor_b32 s48, s48, s29\n" \
"s_add_u32 s39, s39, 1\n" \
"s_bitcmp1_b32 s31, 21\n" \
"s_cselect_b32 s43, s29, 0\n" \
"v_mov_b32 v116, s43\n" \
"ds_write_b32 v252, v116 offset:16\n" \
".LP3_m1:\n" \
"s_memtime vcc\n" \
"s_waitcnt lgkmcnt(0)\n" \
"s_sub_u32 s43, s41, vcc_lo\n" \
"s_cmp_gt_i32 s43, 0\n" \
"s_cbranch_scc1 .LP3_m1\n" \
"s_cmp_lt_i32 s43, s35\n" \
"s_cbranch_scc1 .LP3_m0\n" \
"buffer_load_dwordx4 v[0:3], v208, s[4:7], s47 offen offset:3072 sc1\n" \
".p2align 3\n" \
"s_memtime s[54:55]\n" \
"s_memrealtime s[56:57]\n" \
"s_memtime s[58:59]\n" \
"s_memrealtime s[60:61]\n" \
"s_nop 0\n" \
"s_nop 0\n" \
"s_memtime s[62:63]\n" \
"s_memrealtime s[64:65]\n" \
"s_memtime s[66:67]\n" \
"s_memrealtime s[68:69]\n" \
"s_memtime s[70:71]\n" \
"s_memrealtime s[72:73]\n" \
"s_memtime s[74:75]\n" \
"s_memrealtime s[76:77]\n" \
"s_nop 0\n" \
"s_nop 0\n" \
"s_memtime s[78:79]\n" \
"s_memrealtime s[80:81]\n" \
"s_memtime s[82:83]\n" \
"s_memrealtime s[84:85]\n" \
"s_memtime s[86:87]\n" \
"s_memrealtime s[88:89]\n" \
"s_memtime s[90:91]\n" \
"s_memrealtime s[92:93]\n" \
"s_nop 0\n" \
"s_nop 0\n" \
"s_memtime s[94:95]\n" \
"s_memrealtime s[96:97]\n" \
"s_memtime s[98:99]\n" \
"s_mov_b32 s52, 0\n" \
"s_mov_b32 s53, 4096\n" \
"s_waitcnt lgkmcnt(0)\n" \
"s_cmp_lg_u32 s96, s56\n" \
"s_cselect_b32 s52, s90, s52\n" \
"s_cselect_b32 s53, s98, s53\n" \
"s_cmp_lg_u32 s92, s56\n" \
"s_cselect_b32 s52, s86, s52\n" \
"s_cselect_b32 s53, s94, s53\n" \
"s_cmp_lg_u32 s88, s56\n" \
"s_cselect_b32 s52, s82, s52\n" \
"s_cselect_b32 s53, s90, s53\n" \
"s_cmp_lg_u32 s84, s56\n" \
"s_cselect_b32 s52, s78, s52\n" \
"s_cselect_b32 s53, s86, s53\n" \
"s_cmp_lg_u32 s80, s56\n" \
"s_cselect_b32 s52, s74, s52\n" \
"s_cselect_b32 s53, s82, s53\n" \
"s_cmp_lg_u32 s76, s56\n" \
"s_cselect_b32 s52, s70, s52\n" \
"s_cselect_b32 s53, s78, s53\n" \
"s_cmp_lg_u32 s72, s56\n" \
"s_cselect_b32 s52, s66, s52\n" \
"s_cselect_b32 s53, s74, s53\n" \
"s_cmp_lg_u32 s68, s56\n" \
"s_cselect_b32 s52, s62, s52\n" \
"s_cselect_b32 s53, s70, s53\n" \
"s_cmp_lg_u32 s64, s56\n" \
"s_cselect_b32 s52, s58, s52\n" \
"s_cselect_b32 s53, s66, s53\n" \
"s_cmp_lg_u32 s60, s56\n" \
"s_cselect_b32 s52, s54, s52\n" \
"s_cselect_b32 s53, s62, s53\n" \
"s_sub_u32 s53, s53, s52\n" \
"s_lshr_b32 s55, s53, 1\n" \
"s_add_u32 s52, s52, s55\n" \
"s_add_u32 s55, s56, 4\n" \
"s_lshl_b32 s55, s55, 4\n" \
"s_sub_u32 s59, s55, s44\n" \
"s_cmp_lt_u32 s59, 2600\n" \
"s_cselect_b32 s63, 1, 2\n" \
"s_mul_i32 s59, s59, s46\n" \
"s_ashr_i32 s59, s59, 12\n" \
"s_add_u32 s59, s59, s45\n" \
"s_sub_u32 s59, s52, s59\n" \
"s_max_i32 s59, s59, -96\n" \
"s_min_i32 s59, s59, 96\n" \
"s_ashr_i32 s63, s59, s63\n" \
"s_add_u32 s63, s63, s46\n" \
"s_max_i32 s63, s63, 0x800\n" \
"s_min_i32 s63, s63, 0x2000\n" \
"s_cmp_le_u32 s53, 40\n" \
"s_cselect_b32 s44, s55, s44\n" \
"s_cselect_b32 s45, s52, s45\n" \
"s_cselect_b32 s46, s63, s46\n" \
"v_mov_b32 v248, s44\n" \
"v_mov_b32 v249, s45\n" \
"v_mov_b32 v250, s46\n" \
"s_mov_b64 exec, 1\n" \
"ds_write_b96 v252, v[248:250]\n" \
"s_mov_b64 exec, -1\n" \
"v_mov_b32 v28, 0\n" \
"v_mov_b32 v29, 0\n" \
"v_mov_b32 v30, 0\n" \
"v_mov_b32 v31, 0\n" \
"v_mov_b32 v32, 0\n" \
"v_mov_b32 v33, 0\n" \
"v_mov_b32 v34, 0\n" \
"v_mov_b32 v35, 0\n" \
"v_mov_b32 v36, 0\n" \
"v_mov_b32 v37, 0\n" \
"v_mov_b32 v38, 0\n" \
"v_mov_b32 v39, 0\n" \
"v_mov_b32 v40, 0\n" \
"v_mov_b32 v41, 0\n" \
"v_mov_b32 v42, 0\n" \
"v_mov_b32 v43, 0\n" \
"s_waitcnt vmcnt(0)\n" \
"ds_write_b128 v220, v[0:3] offset:3072\n" \
"v_max3_u32 v44, v0, v0, v1\n" \
"v_max3_u32 v44, v44, v2, v3\n" \
"v_cmp_eq_u32 vcc, -1, v44\n" \
"v_smfmac_f32_16x16x64_bf16 v[28:31], v[0:3], a[0:7], v200\n" \
"v_smfmac_f32_16x16x64_bf16 v[32:35], v[0:3], a[56:63], v200\n" \
"s_cbranch_vccnz .LP3_retry1\n" \
"s_waitcnt lgkmcnt(0)\n" \
"s_barrier\n" \
"ds_read_b128 v[4:7], v220 offset:0\n" \
"ds_read_b128 v[8:11], v220 offset:1024\n" \
"ds_read_b128 v[12:15], v220 offset:2048\n" \
"ds_read_b128 v[16:19], v220 offset:4096\n" \
"ds_read_b128 v[20:23], v220 offset:5120\n" \
"ds_read_b128 v[24:27], v220 offset:6144\n" \
"ds_read_b64 v[246:247], v252 offset:24\n" \
"ds_read_b96 v[248:250], v252\n" \
"s_mul_i32 s50, s40, 114688\n" \
"buffer_store_dwordx4 v[68:71], v218, s[12:15], s50 offen sc1\n" \
"buffer_store_dword v112, v213, s[4:7], s49 offen sc1\n" \
"s_xor_b32 s43, s49, s29\n" \
"buffer_store_dword v112, v213, s[4:7], s43 offen sc1\n" \
"v_smfmac_f32_16x16x64_bf16 v[36:39], v[0:3], a[112:119], v200\n" \
"v_smfmac_f32_16x16x64_bf16 v[40:43], v[0:3], a[168:175], v200\n" \
"s_waitcnt lgkmcnt(7)\n" \
"v_smfmac_f32_16x16x64_bf16 v[28:31], v[4:7], a[8:15], v200\n" \
"v_smfmac_f32_16x16x64_bf16 v[32:35], v[4:7], a[64:71], v200\n" \
"v_smfmac_f32_16x16x64_bf16 v[36:39], v[4:7], a[120:127], v200\n" \
"v_smfmac_f32_16x16x64_bf16 v[40:43], v[4:7], a[176:183], v200\n" \
"s_waitcnt lgkmcnt(6)\n" \
"v_smfmac_f32_16x16x64_bf16 v[28:31], v[8:11], a[16:23], v200\n" \
"v_smfmac_f32_16x16x64_bf16 v[32:35], v[8:11], a[72:79], v200\n" \
"v_smfmac_f32_16x16x64_bf16 v[36:39], v[8:11], a[128:135], v200\n" \
"v_smfmac_f32_16x16x64_bf16 v[40:43], v[8:11], a[184:191], v200\n" \
"s_waitcnt lgkmcnt(0)\n" \
"v_readfirstlane_b32 s44, v248\n" \
"v_readfirstlane_b32 s45, v249\n" \
"v_smfmac_f32_16x16x64_bf16 v[28:31], v[12:15], a[24:31], v200\n" \
"v_readfirstlane_b32 s46, v250\n" \
"v_readfirstlane_b32 s26, v246\n" \
"v_readfirstlane_b32 s27, v247\n" \
"v_smfmac_f32_16x16x64_bf16 v[32:35], v[12:15], a[80:87], v200\n" \
"s_mul_i32 s42, s25, s46\n" \
"s_lshr_b32 s42, s42, 12\n" \
"s_add_u32 s24, s24, s26\n" \
"v_smfmac_f32_16x16x64_bf16 v[36:39], v[12:15], a[136:143], v200\n" \
"s_sub_u32 s43, s24, s44\n" \
"s_max_i32 s43, s43, 0xfffd0000\n" \
"s_min_i32 s43, s43, 0x4000\n" \
"v_smfmac_f32_16x16x64_bf16 v[40:43], v[12:15], a[192:199], v200\n" \
"s_mul_i32 s43, s43, s46\n" \
"s_ashr_i32 s43, s43, 12\n" \
"s_add_u32 s41, s45, s43\n" \
"s_cmp_eq_u32 s40, 63\n" \
"s_cbranch_scc1 .LP3_last\n" \
"s_branch .LP3_z1\n" \
".LP3_z0:\n" \
"s_add_u32 s41, s41, s42\n" \
"s_add_u32 s24, s24, s25\n" \
"s_xor_b32 s31, s31, s29\n" \
"s_xor_b32 s32, s32, s29\n" \
"s_xor_b32 s47, s47, s29\n" \
"s_xor_b32 s48, s48, s29\n" \
"s_add_u32 s39, s39, 1\n" \
"buffer_store_dwordx4 v[112:115], v223, s[4:7], s48 offen sc1\n" \
"s_bitcmp1_b32 s31, 21\n" \
"s_cselect_b32 s43, s29, 0\n" \
"v_mov_b32 v116, s43\n" \
"ds_write_b32 v252, v116 offset:16\n" \
".LP3_z1:\n" \
"s_memtime vcc\n" \
"s_waitcnt lgkmcnt(0)\n" \
"s_sub_u32 s43, s41, vcc_lo\n" \
"s_cmp_gt_i32 s43, 200\n" \
"s_cbranch_scc1 .LP3_z1\n" \
"s_cmp_lt_i32 s43, s37\n" \
"s_cbranch_scc1 .LP3_z0\n" \
"buffer_load_dwordx2 v[64:65], v211, s[4:7], s47 offen sc1\n" \
".p2align 6\n" \
".LP3_b1:\n" \
"s_memtime vcc\n" \
"s_waitcnt lgkmcnt(0)\n" \
"s_sub_u32 s43, s41, vcc_lo\n" \
"s_cmp_gt_i32 s43, 128\n" \
"s_cbranch_scc1 .LP3_b1\n" \
"s_cmp_lt_i32 s43, s37\n" \
"s_cbranch_scc1 .LP3_z0\n" \
"s_max_i32 s43, s43, 0\n" \
"s_and_b32 s43, s43, -4\n" \
"s_getpc_b64 vcc\n" \
".LP3_b3:\n" \
"s_sub_u32 s43, .LP3_b4-.LP3_b3, s43\n" \
"s_add_u32 vcc_lo, vcc_lo, s43\n" \
"s_addc_u32 vcc_hi, vcc_hi, 0\n" \
"s_setpc_b64 vcc\n" \
".rept 32\n" \
"s_nop 0\n" \
".endr\n" \
".LP3_b4:\n" \
"buffer_load_dwordx4 v[56:59], v210, s[4:7], s47 offen sc1\n" \
"buffer_load_dwordx4 v[60:63], v210, s[4:7], s47 offen offset:256 sc1\n" \
"v_smfmac_f32_16x16x64_bf16 v[28:31], v[16:19], a[32:39], v200\n" \
"v_smfmac_f32_16x16x64_bf16 v[32:35], v[16:19], a[88:95], v200\n" \
"v_smfmac_f32_16x16x64_bf16 v[36:39], v[16:19], a[144:151], v200\n" \
"v_smfmac_f32_16x16x64_bf16 v[40:43], v[16:19], a[200:207], v200\n" \
"v_smfmac_f32_16x16x64_bf16 v[28:31], v[20:23], a[40:47], v200\n" \
"v_smfmac_f32_16x16x64_bf16 v[32:35], v[20:23], a[96:103], v200\n" \
"buffer_load_dwordx4 v[50:53], v212, s[4:7], s47 offen sc1\n" \
"v_smfmac_f32_16x16x64_bf16 v[36:39], v[20:23], a[152:159], v200\n" \
"v_smfmac_f32_16x16x64_bf16 v[40:43], v[20:23], a[208:215], v200\n" \
"v_smfmac_f32_16x16x64_bf16 v[28:31], v[24:27], a[48:55], v200\n" \
"v_smfmac_f32_16x16x64_bf16 v[32:35], v[24:27], a[104:111], v200\n" \
"v_smfmac_f32_16x16x64_bf16 v[36:39], v[24:27], a[160:167], v200\n" \
"v_smfmac_f32_16x16x64_bf16 v[40:43], v[24:27], a[216:223], v200\n" \
"s_nop 2\n" \
"v_mov_b32 v245, v245\n" \
"v_mov_b32 v245, v245\n" \
"v_mov_b32 v245, v245\n" \
"v_permlane32_swap_b32 v28, v32\n" \
"v_permlane32_swap_b32 v29, v33\n" \
"v_permlane32_swap_b32 v30, v34\n" \
"v_permlane32_swap_b32 v31, v35\n" \
"v_add_f32 v28, v28, v32\n" \
"v_add_f32 v29, v29, v33\n" \
"v_add_f32 v30, v30, v34\n" \
"v_add_f32 v31, v31, v35\n" \
"v_cvt_pk_bf16_f32 v68, v28, v29\n" \
"v_cvt_pk_bf16_f32 v69, v30, v31\n" \
"v_permlane32_swap_b32 v36, v40\n" \
"v_permlane32_swap_b32 v37, v41\n" \
"v_permlane32_swap_b32 v38, v42\n" \
"v_permlane32_swap_b32 v39, v43\n" \
"v_add_f32 v36, v36, v40\n" \
"v_add_f32 v37, v37, v41\n" \
"v_add_f32 v38, v38, v42\n" \
"v_add_f32 v39, v39, v43\n" \
"v_cvt_pk_bf16_f32 v70, v36, v37\n" \
"v_cvt_pk_bf16_f32 v71, v38, v39\n" \
"v_mov_b32 v48, v68\n" \
"v_mov_b32 v49, v69\n" \
"v_mov_b32 v54, v70\n" \
"v_mov_b32 v55, v71\n" \
"s_nop 1\n" \
"v_permlane16_swap_b32 v48, v54\n" \
"v_permlane16_swap_b32 v49, v55\n" \
".LP3_wait:\n" \
"s_waitcnt vmcnt(1)\n" \
"v_pk_add_f32 v[72:73], v[56:57], v[60:61]\n" \
"v_pk_add_f32 v[74:75], v[58:59], v[62:63]\n" \
"v_max3_f32 v77, v72, v73, v64\n" \
"v_max3_f32 v77, v77, v74, v75\n" \
"s_nop 1\n" \
"v_max_f32_dpp v76, v77, v77 row_ror:8 row_mask:0xf bank_mask:0xf\n" \
"v_pk_add_f32 v[72:73], v[72:73], v[76:77] op_sel_hi:[1,0] neg_lo:[0,1] neg_hi:[0,1]\n" \
"v_pk_add_f32 v[74:75], v[74:75], v[76:77] op_sel_hi:[1,0] neg_lo:[0,1] neg_hi:[0,1]\n" \
"v_sub_f32 v78, v64, v76\n" \
"v_exp_f32 v72, v72\n" \
"v_exp_f32 v73, v73\n" \
"v_exp_f32 v74, v74\n" \
"v_exp_f32 v75, v75\n" \
"v_exp_f32 v78, v78\n" \
"v_pk_add_f32 v[80:81], v[72:73], v[74:75]\n" \
"v_fma_f32 v80, v78, v65, v80\n" \
"v_add_f32 v81, v80, v81\n" \
"s_nop 1\n" \
"v_add_f32_dpp v80, v81, v81 row_ror:8 row_mask:0xf bank_mask:0xf\n" \
"v_rcp_f32 v80, v80\n" \
"s_nop 0\n" \
"v_mul_f32 v78, v78, v80\n" \
"v_pk_mul_f32 v[72:73], v[72:73], v[80:81] op_sel_hi:[1,0]\n" \
"v_pk_mul_f32 v[74:75], v[74:75], v[80:81] op_sel_hi:[1,0]\n" \
"v_cvt_pk_bf16_f32 v78, v78, v78\n" \
"v_cvt_pk_bf16_f32 v82, v72, v73\n" \
"v_cvt_pk_bf16_f32 v83, v74, v75\n" \
"v_and_b32 v84, v78, v214\n" \
"v_and_b32 v85, v78, v215\n" \
"v_cvt_pk_bf16_f32 v86, v72, v73\n" \
"v_cvt_pk_bf16_f32 v87, v74, v75\n" \
"s_waitcnt vmcnt(0)\n" \
"v_mfma_f32_16x16x32_bf16 v[88:91], v[48:51], v[82:85], 0\n" \
"v_mfma_f32_16x16x32_bf16 v[92:95], v[52:55], v[84:87], 0\n" \
"s_nop 2\n" \
"v_mov_b32 v245, v245\n" \
"s_nop 2\n" \
"v_mov_b32 v245, v245\n" \
"v_add_f32_dpp v96, v92, v88 row_ror:8 row_mask:0xf bank_mask:0x3\n" \
"v_add_f32_dpp v97, v93, v89 row_ror:8 row_mask:0xf bank_mask:0x3\n" \
"v_add_f32_dpp v96, v90, v94 row_ror:8 row_mask:0xf bank_mask:0xc\n" \
"v_add_f32_dpp v97, v91, v95 row_ror:8 row_mask:0xf bank_mask:0xc\n" \
"ds_write_b64 v216, v[96:97]\n" \
"v_cmp_u_f32 vcc, v96, v97\n" \
"s_bitcmp1_b32 s31, 21\n" \
"s_cselect_b32 s18, s29, 0\n" \
"s_xor_b32 s18, s18, s48\n" \
"v_mov_b32 v245, v245\n" \
"s_xor_b32 s19, s18, s29\n" \
"s_nop 0\n" \
"s_nop 0\n" \
"v_mov_b32 v245, v245\n" \
"s_nop 0\n" \
"s_nop 0\n" \
"s_nop 0\n" \
"v_mov_b32 v245, v245\n" \
"s_cbranch_vccnz .LP3_cls\n" \
".LP3_clsr:\n" \
"s_waitcnt lgkmcnt(0)\n" \
"s_barrier\n" \
"ds_read_b64 v[100:101], v217 offset:0\n" \
"ds_read_b64 v[102:103], v217 offset:512\n" \
"ds_read_b32 v116, v252 offset:16\n" \
"ds_read_b64 v[104:105], v217 offset:1024\n" \
"ds_read_b64 v[106:107], v217 offset:1536\n" \
"v_mov_b32 v245, v245\n" \
"v_mov_b32 v245, v245\n" \
"s_add_u32 s40, s40, 1\n" \
"s_add_u32 s24, s24, s27\n" \
"s_mov_b32 s47, s48\n" \
"v_mov_b32 v245, v245\n" \
"s_waitcnt lgkmcnt(2)\n" \
"v_xad_u32 v117, v116, s18, v213\n" \
"v_pk_add_f32 v[100:101], v[100:101], v[102:103]\n" \
"s_waitcnt lgkmcnt(0)\n" \
"v_pk_add_f32 v[104:105], v[104:105], v[106:107]\n" \
"v_pk_add_f32 v[100:101], v[100:101], v[104:105]\n" \
"v_cvt_pk_bf16_f32 v96, v100, v101\n" \
"buffer_store_dword v96, v117, s[4:7], 0 offen sc1\n" \
"s_add_u32 s43, s40, 1\n" \
"s_and_b32 s43, s43, 3\n" \
"s_bitcmp1_b32 s43, 0\n" \
"s_cselect_b32 s48, s32, s31\n" \
"s_lshl_b32 s43, s43, 19\n" \
"s_add_u32 s48, s48, s43\n" \
"s_add_u32 s43, s40, 2\n" \
"s_and_b32 s43, s43, 3\n" \
"s_bitcmp1_b32 s43, 0\n" \
"s_cselect_b32 s49, s32, s31\n" \
"s_lshl_b32 s43, s43, 19\n" \
"s_add_u32 s49, s49, s43\n" \
"s_sub_u32 s43, s24, s44\n" \
"s_add_u32 s43, s43, s28\n" \
"s_max_i32 s43, s43, 0xfffd0000\n" \
"s_min_i32 s43, s43, 0x4000\n" \
"s_mul_i32 s43, s43, s46\n" \
"s_ashr_i32 s43, s43, 12\n" \
"s_add_u32 s41, s45, s43\n" \
"s_lshr_b32 s35, s42, 1\n" \
"s_sub_u32 s35, 0, s35\n" \
"v_xad_u32 v116, v116, s19, v213\n" \
"buffer_store_dword v96, v116, s[4:7], 0 offen sc1\n" \
"s_cmp_eq_u32 s40, 0\n" \
"s_cbranch_scc1 .LP3_wet\n" \
"s_branch .LP3_loop\n" \
"s_branch .LP3_y1\n" \
".LP3_retry2:\n" \
"s_add_u32 s51, s51, 1\n" \
"s_cmp_gt_u32 s51, 0x20000\n" \
"s_cbranch_scc1 .LP3_end\n" \
".LP3_y0:\n" \
"s_add_u32 s41, s41, s42\n" \
"s_add_u32 s24, s24, s25\n" \
"s_xor_b32 s31, s31, s29\n" \
"s_xor_b32 s32, s32, s29\n" \
"s_xor_b32 s47, s47, s29\n" \
"s_xor_b32 s48, s48, s29\n" \
"s_add_u32 s39, s39, 1\n" \
"buffer_store_dwordx4 v[112:115], v223, s[4:7], s48 offen sc1\n" \
"s_bitcmp1_b32 s31, 21\n" \
"s_cselect_b32 s43, s29, 0\n" \
"v_mov_b32 v116, s43\n" \
"ds_write_b32 v252, v116 offset:16\n" \
".LP3_y1:\n" \
"s_memtime vcc\n" \
"s_waitcnt lgkmcnt(0)\n" \
"s_sub_u32 s43, s41, vcc_lo\n" \
"s_cmp_gt_i32 s43, 200\n" \
"s_cbranch_scc1 .LP3_y1\n" \
"s_cmp_lt_i32 s43, s37\n" \
"s_cbranch_scc1 .LP3_y0\n" \
"buffer_load_dwordx2 v[64:65], v211, s[4:7], s47 offen sc1\n" \
".p2align 6\n" \
".LP3_c1:\n" \
"s_memtime vcc\n" \
"s_waitcnt lgkmcnt(0)\n" \
"s_sub_u32 s43, s41, vcc_lo\n" \
"s_cmp_gt_i32 s43, 128\n" \
"s_cbranch_scc1 .LP3_c1\n" \
"s_cmp_lt_i32 s43, s37\n" \
"s_cbranch_scc1 .LP3_y0\n" \
"s_max_i32 s43, s43, 0\n" \
"s_and_b32 s43, s43, -4\n" \
"s_getpc_b64 vcc\n" \
".LP3_c3:\n" \
"s_sub_u32 s43, .LP3_c4-.LP3_c3, s43\n" \
"s_add_u32 vcc_lo, vcc_lo, s43\n" \
"s_addc_u32 vcc_hi, vcc_hi, 0\n" \
"s_setpc_b64 vcc\n" \
".rept 32\n" \
"s_nop 0\n" \
".endr\n" \
".LP3_c4:\n" \
"buffer_load_dwordx4 v[56:59], v210, s[4:7], s47 offen sc1\n" \
"buffer_load_dwordx4 v[60:63], v210, s[4:7], s47 offen offset:256 sc1\n" \
"s_nop 2\n" \
"v_mov_b32 v245, v245\n" \
"s_nop 2\n" \
"v_mov_b32 v245, v245\n" \
"s_nop 2\n" \
"v_mov_b32 v245, v245\n" \
"s_nop 2\n" \
"v_mov_b32 v245, v245\n" \
"s_nop 2\n" \
"v_mov_b32 v245, v245\n" \
"s_nop 2\n" \
"v_mov_b32 v245, v245\n" \
"buffer_load_dwordx4 v[50:53], v212, s[4:7], s47 offen sc1\n" \
"s_nop 2\n" \
"v_mov_b32 v245, v245\n" \
"s_nop 2\n" \
"v_mov_b32 v245, v245\n" \
"s_nop 2\n" \
"v_mov_b32 v245, v245\n" \
"s_nop 2\n" \
"v_mov_b32 v245, v245\n" \
"s_nop 2\n" \
"v_mov_b32 v245, v245\n" \
"s_nop 2\n" \
"v_mov_b32 v245, v245\n" \
"s_nop 2\n" \
"v_mov_b32 v245, v245\n" \
"s_nop 2\n" \
"v_mov_b32 v245, v245\n" \
"s_nop 2\n" \
"v_mov_b32 v245, v245\n" \
"s_nop 2\n" \
"v_mov_b32 v245, v245\n" \
"s_nop 2\n" \
"v_mov_b32 v245, v245\n" \
"s_nop 2\n" \
"v_mov_b32 v245, v245\n" \
"s_nop 2\n" \
"v_mov_b32 v245, v245\n" \
"s_nop 2\n" \
"v_mov_b32 v245, v245\n" \
"s_nop 2\n" \
"v_mov_b32 v245, v245\n" \
"s_nop 2\n" \
"v_mov_b32 v245, v245\n" \
"s_nop 2\n" \
"v_mov_b32 v245, v245\n" \
"s_branch .LP3_wait\n" \
".LP3_cls:\n" \
"s_waitcnt vmcnt(0)\n" \
"v_max3_u32 v44, v64, v64, v65\n" \
"v_max3_u32 v44, v44, v50, v51\n" \
"v_max3_u32 v44, v44, v52, v53\n" \
"v_cmp_eq_u32 vcc, -1, v44\n" \
"s_cbranch_vccnz .LP3_retry2\n" \
"v_cmp_u_f32 vcc, v56, v56\n" \
"v_cmp_ne_u32 s[52:53], v56, v109\n" \
"s_and_b64 vcc, vcc, s[52:53]\n" \
"s_cbranch_vccnz .LP3_retry2\n" \
"v_cmp_u_f32 vcc, v57, v57\n" \
"v_cmp_ne_u32 s[52:53], v57, v109\n" \
"s_and_b64 vcc, vcc, s[52:53]\n" \
"s_cbranch_vccnz .LP3_retry2\n" \
"v_cmp_u_f32 vcc, v58, v58\n" \
"v_cmp_ne_u32 s[52:53], v58, v109\n" \
"s_and_b64 vcc, vcc, s[52:53]\n" \
"s_cbranch_vccnz .LP3_retry2\n" \
"v_cmp_u_f32 vcc, v59, v59\n" \
"v_cmp_ne_u32 s[52:53], v59, v109\n" \
"s_and_b64 vcc, vcc, s[52:53]\n" \
"s_cbranch_vccnz .LP3_retry2\n" \
"v_cmp_u_f32 vcc, v60, v60\n" \
"v_cmp_ne_u32 s[52:53], v60, v109\n" \
"s_and_b64 vcc, vcc, s[52:53]\n" \
"s_cbranch_vccnz .LP3_retry2\n" \
"v_cmp_u_f32 vcc, v61, v61\n" \
"v_cmp_ne_u32 s[52:53], v61, v109\n" \
"s_and_b64 vcc, vcc, s[52:53]\n" \
"s_cbranch_vccnz .LP3_retry2\n" \
"v_cmp_u_f32 vcc, v62, v62\n" \
"v_cmp_ne_u32 s[52:53], v62, v109\n" \
"s_and_b64 vcc, vcc, s[52:53]\n" \
"s_cbranch_vccnz .LP3_retry2\n" \
"v_cmp_u_f32 vcc, v63, v63\n" \
"v_cmp_ne_u32 s[52:53], v63, v109\n" \
"s_and_b64 vcc, vcc, s[52:53]\n" \
"s_cbranch_vccnz .LP3_retry2\n" \
"s_branch .LP3_clsr\n" \
".LP3_last:\n" \
"v_smfmac_f32_16x16x64_bf16 v[28:31], v[16:19], a[32:39], v200\n" \
"v_smfmac_f32_16x16x64_bf16 v[32:35], v[16:19], a[88:95], v200\n" \
"v_smfmac_f32_16x16x64_bf16 v[36:39], v[16:19], a[144:151], v200\n" \
"v_smfmac_f32_16x16x64_bf16 v[40:43], v[16:19], a[200:207], v200\n" \
"v_smfmac_f32_16x16x64_bf16 v[28:31], v[20:23], a[40:47], v200\n" \
"v_smfmac_f32_16x16x64_bf16 v[32:35], v[20:23], a[96:103], v200\n" \
"v_smfmac_f32_16x16x64_bf16 v[36:39], v[20:23], a[152:159], v200\n" \
"v_smfmac_f32_16x16x64_bf16 v[40:43], v[20:23], a[208:215], v200\n" \
"v_smfmac_f32_16x16x64_bf16 v[28:31], v[24:27], a[48:55], v200\n" \
"v_smfmac_f32_16x16x64_bf16 v[32:35], v[24:27], a[104:111], v200\n" \
"v_smfmac_f32_16x16x64_bf16 v[36:39], v[24:27], a[160:167], v200\n" \
"v_smfmac_f32_16x16x64_bf16 v[40:43], v[24:27], a[216:223], v200\n" \
"s_waitcnt vmcnt(0)\n" \
"s_nop 7\n" \
"v_permlane32_swap_b32 v28, v32\n" \
"v_permlane32_swap_b32 v29, v33\n" \
"v_permlane32_swap_b32 v30, v34\n" \
"v_permlane32_swap_b32 v31, v35\n" \
"v_add_f32 v28, v28, v32\n" \
"v_add_f32 v29, v29, v33\n" \
"v_add_f32 v30, v30, v34\n" \
"v_add_f32 v31, v31, v35\n" \
"v_cvt_pk_bf16_f32 v68, v28, v29\n" \
"v_cvt_pk_bf16_f32 v69, v30, v31\n" \
"v_permlane32_swap_b32 v36, v40\n" \
"v_permlane32_swap_b32 v37, v41\n" \
"v_permlane32_swap_b32 v38, v42\n" \
"v_permlane32_swap_b32 v39, v43\n" \
"v_add_f32 v36, v36, v40\n" \
"v_add_f32 v37, v37, v41\n" \
"v_add_f32 v38, v38, v42\n" \
"v_add_f32 v39, v39, v43\n" \
"v_cvt_pk_bf16_f32 v70, v36, v37\n" \
"v_cvt_pk_bf16_f32 v71, v38, v39\n" \
"s_mov_b32 s50, 7340032\n" \
"buffer_store_dwordx4 v[68:71], v218, s[12:15], s50 offen sc1\n" \
"s_waitcnt vmcnt(0)\n" \
"s_barrier\n" \
".LP3_end:\n" \
"s_endpgm\n" \
".p2align 6\n" \
"fk_role_X0:\n" \
"ds_read_b32 v200, v255 offset:0\n" \
"ds_read_b32 v201, v255 offset:256\n" \
"ds_read_b32 v202, v255 offset:512\n" \
"ds_read_b32 v203, v255 offset:768\n" \
"ds_read_b32 v204, v255 offset:1024\n" \
"ds_read_b32 v205, v255 offset:1280\n" \
"ds_read_b32 v206, v255 offset:1536\n" \
"ds_read_b32 v207, v255 offset:1792\n" \
"ds_read_b32 v208, v255 offset:2048\n" \
"ds_read_b32 v209, v255 offset:2304\n" \
"ds_read_b32 v210, v255 offset:2560\n" \
"ds_read_b32 v211, v255 offset:2816\n" \
"ds_read_b32 v212, v255 offset:3072\n" \
"ds_read_b32 v213, v255 offset:3328\n" \
"ds_read_b32 v214, v255 offset:3584\n" \
"ds_read_b32 v215, v255 offset:3840\n" \
"ds_read_b32 v216, v255 offset:4096\n" \
"ds_read_b32 v217, v255 offset:4352\n" \
"ds_read_b32 v218, v255 offset:4608\n" \
"ds_read_b32 v219, v255 offset:4864\n" \
"ds_read_b32 v220, v255 offset:5120\n" \
"ds_read_b32 v221, v255 offset:5376\n" \
"ds_read_b32 v254, v255 offset:7936\n" \
"s_waitcnt lgkmcnt(0)\n" \
"v_readlane_b32 s4, v254, 0\n" \
"v_readlane_b32 s5, v254, 1\n" \
"v_readlane_b32 s6, v254, 2\n" \
"v_readlane_b32 s7, v254, 3\n" \
"v_readlane_b32 s8, v254, 4\n" \
"v_readlane_b32 s9, v254, 5\n" \
"v_readlane_b32 s10, v254, 6\n" \
"v_readlane_b32 s11, v254, 7\n" \
"v_readlane_b32 s12, v254, 8\n" \
"v_readlane_b32 s13, v254, 9\n" \
"v_readlane_b32 s14, v254, 10\n" \
"v_readlane_b32 s15, v254, 11\n" \
"v_readlane_b32 s16, v254, 12\n" \
"v_readlane_b32 s17, v254, 13\n" \
"v_readlane_b32 s18, v254, 14\n" \
"v_readlane_b32 s19, v254, 15\n" \
"v_readlane_b32 s20, v254, 16\n" \
"v_readlane_b32 s21, v254, 17\n" \
"v_readlane_b32 s22, v254, 18\n" \
"v_readlane_b32 s23, v254, 19\n" \
"v_readlane_b32 s24, v254, 20\n" \
"v_readlane_b32 s25, v254, 21\n" \
"v_readlane_b32 s26, v254, 22\n" \
"v_readlane_b32 s27, v254, 23\n" \
"v_readlane_b32 s28, v254, 24\n" \
"v_readlane_b32 s29, v254, 25\n" \
"v_readlane_b32 s30, v254, 26\n" \
"v_readlane_b32 s31, v254, 27\n" \
"v_readlane_b32 s32, v254, 28\n" \
"v_readlane_b32 s33, v254, 29\n" \
"v_readlane_b32 s34, v254, 30\n" \
"v_readlane_b32 s35, v254, 31\n" \
"v_readlane_b32 s36, v254, 32\n" \
"v_readlane_b32 s37, v254, 33\n" \
"v_readlane_b32 s38, v254, 34\n" \
"v_readlane_b32 s39, v254, 35\n" \
"s_mov_b32 s40, 0\n" \
"s_mov_b32 s51, 0\n" \
"s_nop 0\n" \
"v_mov_b32 v252, s38\n" \
"s_nop 4\n" \
"v_mov_b32 v100, -1\n" \
"v_mov_b32 v101, -1\n" \
"v_mov_b32 v102, -1\n" \
"v_mov_b32 v103, -1\n" \
"s_mov_b32 s35, -2\n" \
"s_mov_b32 s2, 0\n" \
"s_mov_b32 s3, -1\n" \
"v_writelane_b32 v251, s6, 0\n" \
"v_writelane_b32 v251, s10, 1\n" \
"v_writelane_b32 v251, s14, 2\n" \
"v_writelane_b32 v251, s18, 3\n" \
"s_mov_b32 s6, 0\n" \
"s_mov_b32 s10, 0\n" \
"s_mov_b32 s14, 0\n" \
"s_mov_b32 s18, 0\n" \
"s_mov_b32 s40, -1\n" \
"s_branch .LX0_pre\n" \
".LX0_wet:\n" \
"s_waitcnt vmcnt(0)\n" \
"v_readlane_b32 s6, v251, 0\n" \
"v_readlane_b32 s10, v251, 1\n" \
"v_readlane_b32 s14, v251, 2\n" \
"v_readlane_b32 s18, v251, 3\n" \
"s_nop 4\n" \
"ds_read_b32 v232, v255 offset:7424\n" \
"ds_read_b32 v233, v255 offset:7680\n" \
"v_lshrrev_b32 v234, 4, v201\n" \
"v_and_b32 v234, 3, v234\n" \
"v_subrev_u32 v230, 512, v205\n" \
"s_add_u32 s43, s30, 1\n" \
"s_mov_b32 s50, 0\n" \
".LX0_w77l:\n" \
"buffer_load_dword v240, v230, s[4:7], 0 offen sc1\n" \
"s_waitcnt vmcnt(0)\n" \
".LX0_w77c:\n" \
"v_subrev_u32 v241, s43, v240\n" \
"v_cmp_gt_u32 vcc, 0x100, v241\n" \
"s_cmp_eq_u64 vcc, -1\n" \
"s_cbranch_scc1 .LX0_w77d\n" \
"s_sleep 2\n" \
"s_add_u32 s50, s50, 1\n" \
"s_cmp_lt_u32 s50, 0x20000\n" \
"s_cbranch_scc1 .LX0_w77l\n" \
"s_branch .LX0_end\n" \
".LX0_w77d:\n" \
"v_lshl_or_b32 v241, v241, 2, v234\n" \
"s_nop 1\n" \
"v_min_u32_dpp v241, v241, v241 row_ror:4 row_mask:0xf bank_mask:0xf\n" \
"s_nop 1\n" \
"v_min_u32_dpp v241, v241, v241 row_ror:8 row_mask:0xf bank_mask:0xf\n" \
"s_nop 1\n" \
"v_min_u32_dpp v241, v241, v241 quad_perm:[1,0,3,2] row_mask:0xf bank_mask:0xf\n" \
"s_nop 1\n" \
"v_min_u32_dpp v241, v241, v241 quad_perm:[2,3,0,1] row_mask:0xf bank_mask:0xf\n" \
"s_nop 0\n" \
"v_readlane_b32 s31, v241, 0\n" \
"v_readlane_b32 s43, v241, 16\n" \
"s_min_u32 s31, s31, s43\n" \
"v_readlane_b32 s43, v241, 32\n" \
"s_min_u32 s31, s31, s43\n" \
"v_readlane_b32 s43, v241, 48\n" \
"s_min_u32 s31, s31, s43\n" \
"v_subrev_u32 v230, 256, v205\n" \
"s_add_u32 s43, s30, 1\n" \
"s_mov_b32 s50, 0\n" \
".LX0_w78l:\n" \
"buffer_load_dword v240, v230, s[4:7], 0 offen sc1\n" \
"s_waitcnt vmcnt(0)\n" \
".LX0_w78c:\n" \
"v_subrev_u32 v241, s43, v240\n" \
"v_cmp_gt_u32 vcc, 0x100, v241\n" \
"s_cmp_eq_u64 vcc, -1\n" \
"s_cbranch_scc1 .LX0_w78d\n" \
"s_sleep 2\n" \
"s_add_u32 s50, s50, 1\n" \
"s_cmp_lt_u32 s50, 0x20000\n" \
"s_cbranch_scc1 .LX0_w78l\n" \
"s_branch .LX0_end\n" \
".LX0_w78d:\n" \
"v_lshl_or_b32 v241, v241, 2, v234\n" \
"s_nop 1\n" \
"v_min_u32_dpp v241, v241, v241 row_ror:4 row_mask:0xf bank_mask:0xf\n" \
"s_nop 1\n" \
"v_min_u32_dpp v241, v241, v241 row_ror:8 row_mask:0xf bank_mask:0xf\n" \
"s_nop 1\n" \
"v_min_u32_dpp v241, v241, v241 quad_perm:[1,0,3,2] row_mask:0xf bank_mask:0xf\n" \
"s_nop 1\n" \
"v_min_u32_dpp v241, v241, v241 quad_perm:[2,3,0,1] row_mask:0xf bank_mask:0xf\n" \
"s_nop 0\n" \
"v_readlane_b32 s32, v241, 0\n" \
"v_readlane_b32 s43, v241, 16\n" \
"s_min_u32 s32, s32, s43\n" \
"v_readlane_b32 s43, v241, 32\n" \
"s_min_u32 s32, s32, s43\n" \
"v_readlane_b32 s43, v241, 48\n" \
"s_min_u32 s32, s32, s43\n" \
"s_xor_b32 s29, s31, s32\n" \
"s_and_b32 s29, s29, 3\n" \
"s_lshl_b32 s29, s29, 13\n" \
"s_bitset1_b32 s29, 21\n" \
"s_and_b32 s31, s31, 3\n" \
"s_lshl_b32 s31, s31, 13\n" \
"s_bitcmp1_b32 s39, 0\n" \
"s_cselect_b32 s43, s29, 0\n" \
"s_xor_b32 s31, s31, s43\n" \
"s_xor_b32 s32, s31, 0x4000\n" \
"s_mov_b32 s39, 0\n" \
"v_mov_b32 v224, -1\n" \
"v_mov_b32 v225, -1\n" \
"v_mov_b32 v226, -1\n" \
"v_mov_b32 v227, -1\n" \
"s_waitcnt lgkmcnt(0)\n" \
"s_add_u32 s50, s31, 0\n" \
"buffer_store_dwordx4 v[224:227], v232, s[4:7], s50 offen sc1\n" \
"s_xor_b32 s43, s50, s29\n" \
"buffer_store_dwordx4 v[224:227], v232, s[4:7], s43 offen sc1\n" \
"s_add_u32 s50, s32, 524288\n" \
"buffer_store_dwordx4 v[224:227], v232, s[4:7], s50 offen sc1\n" \
"s_xor_b32 s43, s50, s29\n" \
"buffer_store_dwordx4 v[224:227], v232, s[4:7], s43 offen sc1\n" \
"s_add_u32 s50, s31, 1048576\n" \
"buffer_store_dwordx4 v[224:227], v232, s[4:7], s50 offen sc1\n" \
"s_xor_b32 s43, s50, s29\n" \
"buffer_store_dwordx4 v[224:227], v232, s[4:7], s43 offen sc1\n" \
"s_add_u32 s50, s32, 1572864\n" \
"buffer_store_dwordx4 v[224:227], v232, s[4:7], s50 offen sc1\n" \
"s_xor_b32 s43, s50, s29\n" \
"buffer_store_dwordx4 v[224:227], v232, s[4:7], s43 offen sc1\n" \
"s_waitcnt vmcnt(0)\n" \
"s_barrier\n" \
"s_add_u32 s43, s30, s40\n" \
"s_add_u32 s43, s43, 1\n" \
"v_mov_b32 v241, s43\n" \
"buffer_store_dword v241, v233, s[4:7], 0 offen sc1\n" \
"s_add_u32 s43, s30, 1\n" \
"s_mov_b32 s50, 0\n" \
".LX0_w79l:\n" \
"buffer_load_dword v240, v204, s[4:7], 0 offen sc1\n" \
"s_waitcnt vmcnt(0)\n" \
".LX0_w79c:\n" \
"v_subrev_u32 v241, s43, v240\n" \
"v_cmp_gt_u32 vcc, 0x100, v241\n" \
"s_cmp_eq_u64 vcc, -1\n" \
"s_cbranch_scc1 .LX0_w79d\n" \
"s_sleep 2\n" \
"s_add_u32 s50, s50, 1\n" \
"s_cmp_lt_u32 s50, 0x20000\n" \
"s_cbranch_scc1 .LX0_w79l\n" \
"s_branch .LX0_end\n" \
".LX0_w79d:\n" \
"v_readlane_b32 s43, v202, 0\n" \
"s_nop 4\n" \
"buffer_load_dwordx4 a[0:3], v201, s[8:11], s43 offen sc1\n" \
"buffer_load_dwordx4 a[4:7], v201, s[8:11], s43 offen offset:1024 sc1\n" \
"v_readlane_b32 s43, v202, 7\n" \
"s_nop 4\n" \
"buffer_load_dwordx4 a[56:59], v201, s[8:11], s43 offen sc1\n" \
"buffer_load_dwordx4 a[60:63], v201, s[8:11], s43 offen offset:1024 sc1\n" \
"v_readlane_b32 s43, v202, 14\n" \
"s_nop 4\n" \
"buffer_load_dwordx4 a[112:115], v201, s[8:11], s43 offen sc1\n" \
"buffer_load_dwordx4 a[116:119], v201, s[8:11], s43 offen offset:1024 sc1\n" \
"v_readlane_b32 s43, v202, 21\n" \
"s_nop 4\n" \
"buffer_load_dwordx4 a[168:171], v201, s[8:11], s43 offen sc1\n" \
"buffer_load_dwordx4 a[172:175], v201, s[8:11], s43 offen offset:1024 sc1\n" \
"s_add_u32 s43, s30, 2\n" \
"s_mov_b32 s50, 0\n" \
"s_branch .LX0_w80c\n" \
".LX0_w80l:\n" \
"buffer_load_dword v240, v204, s[4:7], 0 offen sc1\n" \
"s_waitcnt vmcnt(0)\n" \
".LX0_w80c:\n" \
"v_subrev_u32 v241, s43, v240\n" \
"v_cmp_gt_u32 vcc, 0x100, v241\n" \
"s_cmp_eq_u64 vcc, -1\n" \
"s_cbranch_scc1 .LX0_w80d\n" \
"s_sleep 2\n" \
"s_add_u32 s50, s50, 1\n" \
"s_cmp_lt_u32 s50, 0x20000\n" \
"s_cbranch_scc1 .LX0_w80l\n" \
"s_branch .LX0_end\n" \
".LX0_w80d:\n" \
"v_readlane_b32 s43, v202, 4\n" \
"s_nop 4\n" \
"buffer_load_dwordx4 a[32:35], v201, s[8:11], s43 offen sc1\n" \
"buffer_load_dwordx4 a[36:39], v201, s[8:11], s43 offen offset:1024 sc1\n" \
"v_readlane_b32 s43, v202, 11\n" \
"s_nop 4\n" \
"buffer_load_dwordx4 a[88:91], v201, s[8:11], s43 offen sc1\n" \
"buffer_load_dwordx4 a[92:95], v201, s[8:11], s43 offen offset:1024 sc1\n" \
"v_readlane_b32 s43, v202, 18\n" \
"s_nop 4\n" \
"buffer_load_dwordx4 a[144:147], v201, s[8:11], s43 offen sc1\n" \
"buffer_load_dwordx4 a[148:151], v201, s[8:11], s43 offen offset:1024 sc1\n" \
"v_readlane_b32 s43, v202, 25\n" \
"s_nop 4\n" \
"buffer_load_dwordx4 a[200:203], v201, s[8:11], s43 offen sc1\n" \
"buffer_load_dwordx4 a[204:207], v201, s[8:11], s43 offen offset:1024 sc1\n" \
"s_add_u32 s43, s30, 3\n" \
"s_mov_b32 s50, 0\n" \
"s_branch .LX0_w81c\n" \
".LX0_w81l:\n" \
"buffer_load_dword v240, v204, s[4:7], 0 offen sc1\n" \
"s_waitcnt vmcnt(0)\n" \
".LX0_w81c:\n" \
"v_subrev_u32 v241, s43, v240\n" \
"v_cmp_gt_u32 vcc, 0x100, v241\n" \
"s_cmp_eq_u64 vcc, -1\n" \
"s_cbranch_scc1 .LX0_w81d\n" \
"s_sleep 2\n" \
"s_add_u32 s50, s50, 1\n" \
"s_cmp_lt_u32 s50, 0x20000\n" \
"s_cbranch_scc1 .LX0_w81l\n" \
"s_branch .LX0_end\n" \
".LX0_w81d:\n" \
"v_readlane_b32 s43, v202, 1\n" \
"s_nop 4\n" \
"buffer_load_dwordx4 a[8:11], v201, s[8:11], s43 offen sc1\n" \
"buffer_load_dwordx4 a[12:15], v201, s[8:11], s43 offen offset:1024 sc1\n" \
"v_readlane_b32 s43, v202, 8\n" \
"s_nop 4\n" \
"buffer_load_dwordx4 a[64:67], v201, s[8:11], s43 offen sc1\n" \
"buffer_load_dwordx4 a[68:71], v201, s[8:11], s43 offen offset:1024 sc1\n" \
"v_readlane_b32 s43, v202, 15\n" \
"s_nop 4\n" \
"buffer_load_dwordx4 a[120:123], v201, s[8:11], s43 offen sc1\n" \
"buffer_load_dwordx4 a[124:127], v201, s[8:11], s43 offen offset:1024 sc1\n" \
"v_readlane_b32 s43, v202, 22\n" \
"s_nop 4\n" \
"buffer_load_dwordx4 a[176:179], v201, s[8:11], s43 offen sc1\n" \
"buffer_load_dwordx4 a[180:183], v201, s[8:11], s43 offen offset:1024 sc1\n" \
"s_add_u32 s43, s30, 4\n" \
"s_mov_b32 s50, 0\n" \
"s_branch .LX0_w82c\n" \
".LX0_w82l:\n" \
"buffer_load_dword v240, v204, s[4:7], 0 offen sc1\n" \
"s_waitcnt vmcnt(0)\n" \
".LX0_w82c:\n" \
"v_subrev_u32 v241, s43, v240\n" \
"v_cmp_gt_u32 vcc, 0x100, v241\n" \
"s_cmp_eq_u64 vcc, -1\n" \
"s_cbranch_scc1 .LX0_w82d\n" \
"s_sleep 2\n" \
"s_add_u32 s50, s50, 1\n" \
"s_cmp_lt_u32 s50, 0x20000\n" \
"s_cbranch_scc1 .LX0_w82l\n" \
"s_branch .LX0_end\n" \
".LX0_w82d:\n" \
"v_readlane_b32 s43, v202, 5\n" \
"s_nop 4\n" \
"buffer_load_dwordx4 a[40:43], v201, s[8:11], s43 offen sc1\n" \
"buffer_load_dwordx4 a[44:47], v201, s[8:11], s43 offen offset:1024 sc1\n" \
"v_readlane_b32 s43, v202, 12\n" \
"s_nop 4\n" \
"buffer_load_dwordx4 a[96:99], v201, s[8:11], s43 offen sc1\n" \
"buffer_load_dwordx4 a[100:103], v201, s[8:11], s43 offen offset:1024 sc1\n" \
"v_readlane_b32 s43, v202, 19\n" \
"s_nop 4\n" \
"buffer_load_dwordx4 a[152:155], v201, s[8:11], s43 offen sc1\n" \
"buffer_load_dwordx4 a[156:159], v201, s[8:11], s43 offen offset:1024 sc1\n" \
"v_readlane_b32 s43, v202, 26\n" \
"s_nop 4\n" \
"buffer_load_dwordx4 a[208:211], v201, s[8:11], s43 offen sc1\n" \
"buffer_load_dwordx4 a[212:215], v201, s[8:11], s43 offen offset:1024 sc1\n" \
"s_add_u32 s43, s30, 5\n" \
"s_mov_b32 s50, 0\n" \
"s_branch .LX0_w83c\n" \
".LX0_w83l:\n" \
"buffer_load_dword v240, v204, s[4:7], 0 offen sc1\n" \
"s_waitcnt vmcnt(0)\n" \
".LX0_w83c:\n" \
"v_subrev_u32 v241, s43, v240\n" \
"v_cmp_gt_u32 vcc, 0x100, v241\n" \
"s_cmp_eq_u64 vcc, -1\n" \
"s_cbranch_scc1 .LX0_w83d\n" \
"s_sleep 2\n" \
"s_add_u32 s50, s50, 1\n" \
"s_cmp_lt_u32 s50, 0x20000\n" \
"s_cbranch_scc1 .LX0_w83l\n" \
"s_branch .LX0_end\n" \
".LX0_w83d:\n" \
"v_readlane_b32 s43, v202, 2\n" \
"s_nop 4\n" \
"buffer_load_dwordx4 a[16:19], v201, s[8:11], s43 offen sc1\n" \
"buffer_load_dwordx4 a[20:23], v201, s[8:11], s43 offen offset:1024 sc1\n" \
"v_readlane_b32 s43, v202, 9\n" \
"s_nop 4\n" \
"buffer_load_dwordx4 a[72:75], v201, s[8:11], s43 offen sc1\n" \
"buffer_load_dwordx4 a[76:79], v201, s[8:11], s43 offen offset:1024 sc1\n" \
"v_readlane_b32 s43, v202, 16\n" \
"s_nop 4\n" \
"buffer_load_dwordx4 a[128:131], v201, s[8:11], s43 offen sc1\n" \
"buffer_load_dwordx4 a[132:135], v201, s[8:11], s43 offen offset:1024 sc1\n" \
"v_readlane_b32 s43, v202, 23\n" \
"s_nop 4\n" \
"buffer_load_dwordx4 a[184:187], v201, s[8:11], s43 offen sc1\n" \
"buffer_load_dwordx4 a[188:191], v201, s[8:11], s43 offen offset:1024 sc1\n" \
"s_add_u32 s43, s30, 6\n" \
"s_mov_b32 s50, 0\n" \
"s_branch .LX0_w84c\n" \
".LX0_w84l:\n" \
"buffer_load_dword v240, v204, s[4:7], 0 offen sc1\n" \
"s_waitcnt vmcnt(0)\n" \
".LX0_w84c:\n" \
"v_subrev_u32 v241, s43, v240\n" \
"v_cmp_gt_u32 vcc, 0x100, v241\n" \
"s_cmp_eq_u64 vcc, -1\n" \
"s_cbranch_scc1 .LX0_w84d\n" \
"s_sleep 2\n" \
"s_add_u32 s50, s50, 1\n" \
"s_cmp_lt_u32 s50, 0x20000\n" \
"s_cbranch_scc1 .LX0_w84l\n" \
"s_branch .LX0_end\n" \
".LX0_w84d:\n" \
"v_readlane_b32 s43, v202, 6\n" \
"s_nop 4\n" \
"buffer_load_dwordx4 a[48:51], v201, s[8:11], s43 offen sc1\n" \
"buffer_load_dwordx4 a[52:55], v201, s[8:11], s43 offen offset:1024 sc1\n" \
"v_readlane_b32 s43, v202, 13\n" \
"s_nop 4\n" \
"buffer_load_dwordx4 a[104:107], v201, s[8:11], s43 offen sc1\n" \
"buffer_load_dwordx4 a[108:111], v201, s[8:11], s43 offen offset:1024 sc1\n" \
"v_readlane_b32 s43, v202, 20\n" \
"s_nop 4\n" \
"buffer_load_dwordx4 a[160:163], v201, s[8:11], s43 offen sc1\n" \
"buffer_load_dwordx4 a[164:167], v201, s[8:11], s43 offen offset:1024 sc1\n" \
"v_readlane_b32 s43, v202, 27\n" \
"s_nop 4\n" \
"buffer_load_dwordx4 a[216:219], v201, s[8:11], s43 offen sc1\n" \
"buffer_load_dwordx4 a[220:223], v201, s[8:11], s43 offen offset:1024 sc1\n" \
"s_add_u32 s43, s30, 7\n" \
"s_mov_b32 s50, 0\n" \
"s_branch .LX0_w85c\n" \
".LX0_w85l:\n" \
"buffer_load_dword v240, v204, s[4:7], 0 offen sc1\n" \
"s_waitcnt vmcnt(0)\n" \
".LX0_w85c:\n" \
"v_subrev_u32 v241, s43, v240\n" \
"v_cmp_gt_u32 vcc, 0x100, v241\n" \
"s_cmp_eq_u64 vcc, -1\n" \
"s_cbranch_scc1 .LX0_w85d\n" \
"s_sleep 2\n" \
"s_add_u32 s50, s50, 1\n" \
"s_cmp_lt_u32 s50, 0x20000\n" \
"s_cbranch_scc1 .LX0_w85l\n" \
"s_branch .LX0_end\n" \
".LX0_w85d:\n" \
"v_readlane_b32 s43, v202, 3\n" \
"s_nop 4\n" \
"buffer_load_dwordx4 a[24:27], v201, s[8:11], s43 offen sc1\n" \
"buffer_load_dwordx4 a[28:31], v201, s[8:11], s43 offen offset:1024 sc1\n" \
"v_readlane_b32 s43, v202, 10\n" \
"s_nop 4\n" \
"buffer_load_dwordx4 a[80:83], v201, s[8:11], s43 offen sc1\n" \
"buffer_load_dwordx4 a[84:87], v201, s[8:11], s43 offen offset:1024 sc1\n" \
"v_readlane_b32 s43, v202, 17\n" \
"s_nop 4\n" \
"buffer_load_dwordx4 a[136:139], v201, s[8:11], s43 offen sc1\n" \
"buffer_load_dwordx4 a[140:143], v201, s[8:11], s43 offen offset:1024 sc1\n" \
"v_readlane_b32 s43, v202, 24\n" \
"s_nop 4\n" \
"buffer_load_dwordx4 a[192:195], v201, s[8:11], s43 offen sc1\n" \
"buffer_load_dwordx4 a[196:199], v201, s[8:11], s43 offen offset:1024 sc1\n" \
"s_add_u32 s43, s30, 1\n" \
"s_mov_b32 s50, 0\n" \
".LX0_w86l:\n" \
"buffer_load_dword v240, v205, s[4:7], 0 offen sc1\n" \
"s_waitcnt vmcnt(0)\n" \
".LX0_w86c:\n" \
"v_subrev_u32 v241, s43, v240\n" \
"v_cmp_gt_u32 vcc, 0x100, v241\n" \
"s_cmp_eq_u64 vcc, -1\n" \
"s_cbranch_scc1 .LX0_w86d\n" \
"s_sleep 2\n" \
"s_add_u32 s50, s50, 1\n" \
"s_cmp_lt_u32 s50, 0x20000\n" \
"s_cbranch_scc1 .LX0_w86l\n" \
"s_branch .LX0_end\n" \
".LX0_w86d:\n" \
"s_add_u32 s43, s30, 1\n" \
"s_mov_b32 s50, 0\n" \
".LX0_w87l:\n" \
"buffer_load_dword v240, v206, s[4:7], 0 offen sc1\n" \
"s_waitcnt vmcnt(0)\n" \
".LX0_w87c:\n" \
"v_subrev_u32 v241, s43, v240\n" \
"v_cmp_gt_u32 vcc, 0x100, v241\n" \
"s_cmp_eq_u64 vcc, -1\n" \
"s_cbranch_scc1 .LX0_w87d\n" \
"s_sleep 2\n" \
"s_add_u32 s50, s50, 1\n" \
"s_cmp_lt_u32 s50, 0x20000\n" \
"s_cbranch_scc1 .LX0_w87l\n" \
"s_branch .LX0_end\n" \
".LX0_w87d:\n" \
"s_add_u32 s43, s30, 8\n" \
"s_mov_b32 s50, 0\n" \
".LX0_w88l:\n" \
"buffer_load_dword v240, v215, s[4:7], 0 offen sc1\n" \
"s_waitcnt vmcnt(0)\n" \
".LX0_w88c:\n" \
"v_subrev_u32 v241, s43, v240\n" \
"v_cmp_gt_u32 vcc, 0x100, v241\n" \
"s_cmp_eq_u64 vcc, -1\n" \
"s_cbranch_scc1 .LX0_w88d\n" \
"s_sleep 2\n" \
"s_add_u32 s50, s50, 1\n" \
"s_cmp_lt_u32 s50, 0x20000\n" \
"s_cbranch_scc1 .LX0_w88l\n" \
"s_branch .LX0_end\n" \
".LX0_w88d:\n" \
"s_add_u32 s50, s40, 0\n" \
"s_mul_i32 s50, s50, 458752\n" \
"s_add_u32 s50, s50, s34\n" \
"buffer_load_dwordx4 v[28:31], v201, s[12:15], s50 offen sc1\n" \
"buffer_load_dwordx4 v[32:35], v201, s[12:15], s50 offen offset:1024 sc1\n" \
"buffer_load_dwordx4 v[36:39], v201, s[12:15], s50 offen offset:2048 sc1\n" \
"buffer_load_dwordx4 v[40:43], v201, s[12:15], s50 offen offset:3072 sc1\n" \
"ds_read_b96 v[248:250], v252\n" \
"s_waitcnt lgkmcnt(0)\n" \
"v_readfirstlane_b32 s44, v248\n" \
"v_readfirstlane_b32 s45, v249\n" \
"v_readfirstlane_b32 s46, v250\n" \
"s_mul_i32 s42, s25, s46\n" \
"s_lshr_b32 s42, s42, 12\n" \
"s_mov_b32 s26, s44\n" \
"s_mov_b32 s27, s45\n" \
".p2align 3\n" \
"s_memtime s[54:55]\n" \
"s_memrealtime s[56:57]\n" \
"s_memtime s[58:59]\n" \
"s_memrealtime s[60:61]\n" \
"s_nop 0\n" \
"s_nop 0\n" \
"s_memtime s[62:63]\n" \
"s_memrealtime s[64:65]\n" \
"s_memtime s[66:67]\n" \
"s_memrealtime s[68:69]\n" \
"s_memtime s[70:71]\n" \
"s_memrealtime s[72:73]\n" \
"s_memtime s[74:75]\n" \
"s_memrealtime s[76:77]\n" \
"s_nop 0\n" \
"s_nop 0\n" \
"s_memtime s[78:79]\n" \
"s_memrealtime s[80:81]\n" \
"s_memtime s[82:83]\n" \
"s_memrealtime s[84:85]\n" \
"s_memtime s[86:87]\n" \
"s_memrealtime s[88:89]\n" \
"s_memtime s[90:91]\n" \
"s_memrealtime s[92:93]\n" \
"s_nop 0\n" \
"s_nop 0\n" \
"s_memtime s[94:95]\n" \
"s_memrealtime s[96:97]\n" \
"s_memtime s[98:99]\n" \
"s_mov_b32 s52, 0\n" \
"s_mov_b32 s53, 4096\n" \
"s_waitcnt lgkmcnt(0)\n" \
"s_cmp_lg_u32 s96, s56\n" \
"s_cselect_b32 s52, s90, s52\n" \
"s_cselect_b32 s53, s98, s53\n" \
"s_cmp_lg_u32 s92, s56\n" \
"s_cselect_b32 s52, s86, s52\n" \
"s_cselect_b32 s53, s94, s53\n" \
"s_cmp_lg_u32 s88, s56\n" \
"s_cselect_b32 s52, s82, s52\n" \
"s_cselect_b32 s53, s90, s53\n" \
"s_cmp_lg_u32 s84, s56\n" \
"s_cselect_b32 s52, s78, s52\n" \
"s_cselect_b32 s53, s86, s53\n" \
"s_cmp_lg_u32 s80, s56\n" \
"s_cselect_b32 s52, s74, s52\n" \
"s_cselect_b32 s53, s82, s53\n" \
"s_cmp_lg_u32 s76, s56\n" \
"s_cselect_b32 s52, s70, s52\n" \
"s_cselect_b32 s53, s78, s53\n" \
"s_cmp_lg_u32 s72, s56\n" \
"s_cselect_b32 s52, s66, s52\n" \
"s_cselect_b32 s53, s74, s53\n" \
"s_cmp_lg_u32 s68, s56\n" \
"s_cselect_b32 s52, s62, s52\n" \
"s_cselect_b32 s53, s70, s53\n" \
"s_cmp_lg_u32 s64, s56\n" \
"s_cselect_b32 s52, s58, s52\n" \
"s_cselect_b32 s53, s66, s53\n" \
"s_cmp_lg_u32 s60, s56\n" \
"s_cselect_b32 s52, s54, s52\n" \
"s_cselect_b32 s53, s62, s53\n" \
"s_sub_u32 s53, s53, s52\n" \
"s_lshr_b32 s55, s53, 1\n" \
"s_add_u32 s52, s52, s55\n" \
"s_add_u32 s55, s56, 4\n" \
"s_lshl_b32 s55, s55, 4\n" \
"s_cmp_le_u32 s53, 40\n" \
"s_cselect_b32 s44, s55, s44\n" \
"s_cselect_b32 s45, s52, s45\n" \
"v_mov_b32 v248, s44\n" \
"v_mov_b32 v249, s45\n" \
"v_mov_b32 v250, s46\n" \
"s_mov_b64 exec, 1\n" \
"ds_write_b96 v252, v[248:250]\n" \
"s_mov_b64 exec, -1\n" \
"s_sub_u32 s26, s44, s26\n" \
"s_sub_u32 s27, s45, s27\n" \
"v_cvt_f32_u32 v248, s26\n" \
"v_cvt_f32_u32 v249, s27\n" \
"v_rcp_f32 v248, v248\n" \
"v_mul_f32 v249, 0x457ae148, v249\n" \
"v_mul_f32 v248, v248, v249\n" \
"v_cvt_u32_f32 v248, v248\n" \
"s_nop 0\n" \
"v_readfirstlane_b32 s27, v248\n" \
"s_max_u32 s27, s27, 0x800\n" \
"s_min_u32 s27, s27, 0x2000\n" \
"s_cmp_gt_u32 s26, 1600\n" \
"s_cselect_b32 s46, s27, s46\n" \
".p2align 3\n" \
"s_memtime s[54:55]\n" \
"s_memrealtime s[56:57]\n" \
"s_memtime s[58:59]\n" \
"s_memrealtime s[60:61]\n" \
"s_nop 0\n" \
"s_nop 0\n" \
"s_memtime s[62:63]\n" \
"s_memrealtime s[64:65]\n" \
"s_memtime s[66:67]\n" \
"s_memrealtime s[68:69]\n" \
"s_memtime s[70:71]\n" \
"s_memrealtime s[72:73]\n" \
"s_memtime s[74:75]\n" \
"s_memrealtime s[76:77]\n" \
"s_nop 0\n" \
"s_nop 0\n" \
"s_memtime s[78:79]\n" \
"s_memrealtime s[80:81]\n" \
"s_memtime s[82:83]\n" \
"s_memrealtime s[84:85]\n" \
"s_memtime s[86:87]\n" \
"s_memrealtime s[88:89]\n" \
"s_memtime s[90:91]\n" \
"s_memrealtime s[92:93]\n" \
"s_nop 0\n" \
"s_nop 0\n" \
"s_memtime s[94:95]\n" \
"s_memrealtime s[96:97]\n" \
"s_memtime s[98:99]\n" \
"s_mov_b32 s52, 0\n" \
"s_mov_b32 s53, 4096\n" \
"s_waitcnt lgkmcnt(0)\n" \
"s_cmp_lg_u32 s96, s56\n" \
"s_cselect_b32 s52, s90, s52\n" \
"s_cselect_b32 s53, s98, s53\n" \
"s_cmp_lg_u32 s92, s56\n" \
"s_cselect_b32 s52, s86, s52\n" \
"s_cselect_b32 s53, s94, s53\n" \
"s_cmp_lg_u32 s88, s56\n" \
"s_cselect_b32 s52, s82, s52\n" \
"s_cselect_b32 s53, s90, s53\n" \
"s_cmp_lg_u32 s84, s56\n" \
"s_cselect_b32 s52, s78, s52\n" \
"s_cselect_b32 s53, s86, s53\n" \
"s_cmp_lg_u32 s80, s56\n" \
"s_cselect_b32 s52, s74, s52\n" \
"s_cselect_b32 s53, s82, s53\n" \
"s_cmp_lg_u32 s76, s56\n" \
"s_cselect_b32 s52, s70, s52\n" \
"s_cselect_b32 s53, s78, s53\n" \
"s_cmp_lg_u32 s72, s56\n" \
"s_cselect_b32 s52, s66, s52\n" \
"s_cselect_b32 s53, s74, s53\n" \
"s_cmp_lg_u32 s68, s56\n" \
"s_cselect_b32 s52, s62, s52\n" \
"s_cselect_b32 s53, s70, s53\n" \
"s_cmp_lg_u32 s64, s56\n" \
"s_cselect_b32 s52, s58, s52\n" \
"s_cselect_b32 s53, s66, s53\n" \
"s_cmp_lg_u32 s60, s56\n" \
"s_cselect_b32 s52, s54, s52\n" \
"s_cselect_b32 s53, s62, s53\n" \
"s_sub_u32 s53, s53, s52\n" \
"s_lshr_b32 s55, s53, 1\n" \
"s_add_u32 s52, s52, s55\n" \
"s_add_u32 s55, s56, 4\n" \
"s_lshl_b32 s55, s55, 4\n" \
"s_cmp_le_u32 s53, 40\n" \
"s_cselect_b32 s44, s55, s44\n" \
"s_cselect_b32 s45, s52, s45\n" \
"v_mov_b32 v248, s44\n" \
"v_mov_b32 v249, s45\n" \
"v_mov_b32 v250, s46\n" \
"s_mov_b64 exec, 1\n" \
"ds_write_b96 v252, v[248:250]\n" \
"s_mov_b64 exec, -1\n" \
"s_waitcnt lgkmcnt(0)\n" \
"s_add_u32 s43, s30, 9\n" \
"s_mov_b32 s50, 0\n" \
".LX0_w89l:\n" \
"buffer_load_dword v240, v215, s[4:7], 0 offen sc1\n" \
"s_waitcnt vmcnt(0)\n" \
".LX0_w89c:\n" \
"v_subrev_u32 v241, s43, v240\n" \
"v_cmp_gt_u32 vcc, 0x100, v241\n" \
"s_cmp_eq_u64 vcc, -1\n" \
"s_cbranch_scc1 .LX0_w89d\n" \
"s_sleep 2\n" \
"s_add_u32 s50, s50, 1\n" \
"s_cmp_lt_u32 s50, 0x20000\n" \
"s_cbranch_scc1 .LX0_w89l\n" \
"s_branch .LX0_end\n" \
".LX0_w89d:\n" \
".LX0_pre:\n" \
"s_barrier\n" \
".LX0_loop:\n" \
"s_add_u32 s43, s40, 2\n" \
"s_and_b32 s43, s43, 3\n" \
"s_bitcmp1_b32 s43, 0\n" \
"s_cselect_b32 s49, s32, s31\n" \
"s_lshl_b32 s43, s43, 19\n" \
"s_add_u32 s49, s49, s43\n" \
"ds_read_b128 v[0:3], v216 offset:0\n" \
"ds_read_b128 v[4:7], v216 offset:1024\n" \
"ds_read_b128 v[8:11], v216 offset:2048\n" \
"ds_read_b128 v[12:15], v216 offset:3072\n" \
"ds_read_b128 v[16:19], v216 offset:4096\n" \
"ds_read_b128 v[20:23], v216 offset:5120\n" \
"ds_read_b128 v[24:27], v216 offset:6144\n" \
"v_mov_b32 v60, 0\n" \
"v_mov_b32 v61, 0\n" \
"v_mov_b32 v62, 0\n" \
"v_mov_b32 v63, 0\n" \
"v_mov_b32 v64, 0\n" \
"v_mov_b32 v65, 0\n" \
"v_mov_b32 v66, 0\n" \
"v_mov_b32 v67, 0\n" \
"v_mov_b32 v68, 0\n" \
"v_mov_b32 v69, 0\n" \
"v_mov_b32 v70, 0\n" \
"v_mov_b32 v71, 0\n" \
"v_mov_b32 v72, 0\n" \
"v_mov_b32 v73, 0\n" \
"v_mov_b32 v74, 0\n" \
"v_mov_b32 v75, 0\n" \
"s_nop 1\n" \
"s_waitcnt lgkmcnt(6)\n" \
"v_smfmac_f32_16x16x64_bf16 v[60:63], v[0:3], a[0:7], v200\n" \
"v_smfmac_f32_16x16x64_bf16 v[64:67], v[0:3], a[56:63], v200\n" \
"v_smfmac_f32_16x16x64_bf16 v[68:71], v[0:3], a[112:119], v200\n" \
"v_smfmac_f32_16x16x64_bf16 v[72:75], v[0:3], a[168:175], v200\n" \
"s_waitcnt lgkmcnt(5)\n" \
"v_smfmac_f32_16x16x64_bf16 v[60:63], v[4:7], a[8:15], v200\n" \
"v_smfmac_f32_16x16x64_bf16 v[64:67], v[4:7], a[64:71], v200\n" \
"v_smfmac_f32_16x16x64_bf16 v[68:71], v[4:7], a[120:127], v200\n" \
"v_smfmac_f32_16x16x64_bf16 v[72:75], v[4:7], a[176:183], v200\n" \
"s_waitcnt lgkmcnt(4)\n" \
"v_smfmac_f32_16x16x64_bf16 v[60:63], v[8:11], a[16:23], v200\n" \
"v_smfmac_f32_16x16x64_bf16 v[64:67], v[8:11], a[72:79], v200\n" \
"v_smfmac_f32_16x16x64_bf16 v[68:71], v[8:11], a[128:135], v200\n" \
"v_smfmac_f32_16x16x64_bf16 v[72:75], v[8:11], a[184:191], v200\n" \
"s_waitcnt lgkmcnt(3)\n" \
"v_smfmac_f32_16x16x64_bf16 v[60:63], v[12:15], a[24:31], v200\n" \
"v_smfmac_f32_16x16x64_bf16 v[64:67], v[12:15], a[80:87], v200\n" \
"v_smfmac_f32_16x16x64_bf16 v[68:71], v[12:15], a[136:143], v200\n" \
"v_smfmac_f32_16x16x64_bf16 v[72:75], v[12:15], a[192:199], v200\n" \
"s_waitcnt lgkmcnt(2)\n" \
"v_smfmac_f32_16x16x64_bf16 v[60:63], v[16:19], a[32:39], v200\n" \
"v_smfmac_f32_16x16x64_bf16 v[64:67], v[16:19], a[88:95], v200\n" \
"v_smfmac_f32_16x16x64_bf16 v[68:71], v[16:19], a[144:151], v200\n" \
"v_smfmac_f32_16x16x64_bf16 v[72:75], v[16:19], a[200:207], v200\n" \
"s_waitcnt lgkmcnt(1)\n" \
"v_smfmac_f32_16x16x64_bf16 v[60:63], v[20:23], a[40:47], v200\n" \
"v_smfmac_f32_16x16x64_bf16 v[64:67], v[20:23], a[96:103], v200\n" \
"v_smfmac_f32_16x16x64_bf16 v[68:71], v[20:23], a[152:159], v200\n" \
"v_smfmac_f32_16x16x64_bf16 v[72:75], v[20:23], a[208:215], v200\n" \
"s_waitcnt lgkmcnt(0)\n" \
"v_smfmac_f32_16x16x64_bf16 v[60:63], v[24:27], a[48:55], v200\n" \
"v_smfmac_f32_16x16x64_bf16 v[64:67], v[24:27], a[104:111], v200\n" \
"v_smfmac_f32_16x16x64_bf16 v[68:71], v[24:27], a[160:167], v200\n" \
"v_smfmac_f32_16x16x64_bf16 v[72:75], v[24:27], a[216:223], v200\n" \
"s_barrier\n" \
"s_add_u32 s50, s40, 1\n" \
"s_mul_i32 s50, s50, 458752\n" \
"s_add_u32 s50, s50, s34\n" \
"buffer_load_dwordx4 v[44:47], v201, s[12:15], s50 offen sc1\n" \
"buffer_load_dwordx4 v[48:51], v201, s[12:15], s50 offen offset:1024 sc1\n" \
"buffer_load_dwordx4 v[52:55], v201, s[12:15], s50 offen offset:2048 sc1\n" \
"buffer_load_dwordx4 v[56:59], v201, s[12:15], s50 offen offset:3072 sc1\n" \
"s_nop 7\n" \
"v_permlane32_swap_b32 v60, v64\n" \
"v_permlane32_swap_b32 v61, v65\n" \
"v_permlane32_swap_b32 v62, v66\n" \
"v_permlane32_swap_b32 v63, v67\n" \
"v_add_f32 v60, v60, v64\n" \
"v_add_f32 v61, v61, v65\n" \
"v_add_f32 v62, v62, v66\n" \
"v_add_f32 v63, v63, v67\n" \
"v_cvt_pk_bf16_f32 v184, v60, v61\n" \
"v_cvt_pk_bf16_f32 v185, v62, v63\n" \
"ds_write_b64 v217, v[184:185] offset:0\n" \
"v_permlane32_swap_b32 v68, v72\n" \
"v_permlane32_swap_b32 v69, v73\n" \
"v_permlane32_swap_b32 v70, v74\n" \
"v_permlane32_swap_b32 v71, v75\n" \
"v_add_f32 v68, v68, v72\n" \
"v_add_f32 v69, v69, v73\n" \
"v_add_f32 v70, v70, v74\n" \
"v_add_f32 v71, v71, v75\n" \
"v_cvt_pk_bf16_f32 v186, v68, v69\n" \
"v_cvt_pk_bf16_f32 v187, v70, v71\n" \
"ds_write_b64 v217, v[186:187] offset:512\n" \
"ds_read_b64_tr_b16 v[0:1], v218 offset:0\n" \
"ds_read_b64_tr_b16 v[2:3], v218 offset:64\n" \
"s_add_u32 s43, s40, 0\n" \
"s_mul_i32 s43, s43, 114688\n" \
"s_waitcnt lgkmcnt(0)\n" \
"buffer_store_dwordx4 v[0:3], v219, s[16:19], s43 offen sc1\n" \
"s_waitcnt vmcnt(5)\n" \
"s_barrier\n" \
"ds_read_b64_tr_b16 v[92:93], v213\n" \
"ds_read_b64_tr_b16 v[94:95], v213 offset:64\n" \
"ds_read_b32 v84, v252 offset:16\n" \
"v_mov_b32 v245, v245\n" \
"s_nop 2\n" \
"v_mov_b32 v245, v245\n" \
"s_nop 2\n" \
"v_mov_b32 v245, v245\n" \
"s_waitcnt lgkmcnt(1)\n" \
"v_mfma_f32_16x16x32_bf16 v[68:71], v[92:95], v[28:31], 0\n" \
"s_waitcnt lgkmcnt(0)\n" \
"v_xor_b32 v85, s29, v84\n" \
"v_mfma_f32_16x16x32_bf16 v[76:79], v[92:95], v[36:39], 0\n" \
"v_add_u32 v84, v84, v210\n" \
"v_add_u32 v85, v85, v210\n" \
"v_mfma_f32_16x16x32_bf16 v[72:75], v[92:95], v[32:35], 0\n" \
"s_nop 1\n" \
"v_mfma_f32_16x16x32_bf16 v[80:83], v[92:95], v[40:43], 0\n" \
"v_cvt_pk_bf16_f32 v96, v68, v69\n" \
"v_cvt_pk_bf16_f32 v97, v70, v71\n" \
"v_cvt_pk_bf16_f32 v98, v76, v77\n" \
"v_cvt_pk_bf16_f32 v99, v78, v79\n" \
"s_mov_b64 exec, s[2:3]\n" \
"v_cvt_pk_bf16_f32 v96, v72, v73\n" \
"v_cvt_pk_bf16_f32 v97, v74, v75\n" \
"s_nop 0\n" \
"v_cvt_pk_bf16_f32 v98, v80, v81\n" \
"v_cvt_pk_bf16_f32 v99, v82, v83\n" \
"s_mov_b64 exec, -1\n" \
"s_nop 1\n" \
"v_permlane16_swap_b32 v96, v98\n" \
"v_permlane16_swap_b32 v97, v99\n" \
"buffer_store_dwordx4 v[96:99], v84, s[4:7], 0 offen sc1\n" \
"s_barrier\n" \
"buffer_store_dwordx4 v[96:99], v85, s[4:7], 0 offen sc1\n" \
"ds_read_b96 v[248:250], v252\n" \
"s_waitcnt lgkmcnt(0)\n" \
"v_readfirstlane_b32 s44, v248\n" \
"v_readfirstlane_b32 s45, v249\n" \
"v_readfirstlane_b32 s46, v250\n" \
"s_mul_i32 s42, s25, s46\n" \
"s_lshr_b32 s42, s42, 12\n" \
".p2align 3\n" \
"s_memtime s[54:55]\n" \
"s_memrealtime s[56:57]\n" \
"s_memtime s[58:59]\n" \
"s_memrealtime s[60:61]\n" \
"s_nop 0\n" \
"s_nop 0\n" \
"s_memtime s[62:63]\n" \
"s_memrealtime s[64:65]\n" \
"s_memtime s[66:67]\n" \
"s_memrealtime s[68:69]\n" \
"s_memtime s[70:71]\n" \
"s_memrealtime s[72:73]\n" \
"s_memtime s[74:75]\n" \
"s_memrealtime s[76:77]\n" \
"s_nop 0\n" \
"s_nop 0\n" \
"s_memtime s[78:79]\n" \
"s_memrealtime s[80:81]\n" \
"s_memtime s[82:83]\n" \
"s_memrealtime s[84:85]\n" \
"s_memtime s[86:87]\n" \
"s_memrealtime s[88:89]\n" \
"s_memtime s[90:91]\n" \
"s_memrealtime s[92:93]\n" \
"s_nop 0\n" \
"s_nop 0\n" \
"s_memtime s[94:95]\n" \
"s_memrealtime s[96:97]\n" \
"s_memtime s[98:99]\n" \
"s_mov_b32 s52, 0\n" \
"s_mov_b32 s53, 4096\n" \
"s_waitcnt lgkmcnt(0)\n" \
"s_cmp_lg_u32 s96, s56\n" \
"s_cselect_b32 s52, s90, s52\n" \
"s_cselect_b32 s53, s98, s53\n" \
"s_cmp_lg_u32 s92, s56\n" \
"s_cselect_b32 s52, s86, s52\n" \
"s_cselect_b32 s53, s94, s53\n" \
"s_cmp_lg_u32 s88, s56\n" \
"s_cselect_b32 s52, s82, s52\n" \
"s_cselect_b32 s53, s90, s53\n" \
"s_cmp_lg_u32 s84, s56\n" \
"s_cselect_b32 s52, s78, s52\n" \
"s_cselect_b32 s53, s86, s53\n" \
"s_cmp_lg_u32 s80, s56\n" \
"s_cselect_b32 s52, s74, s52\n" \
"s_cselect_b32 s53, s82, s53\n" \
"s_cmp_lg_u32 s76, s56\n" \
"s_cselect_b32 s52, s70, s52\n" \
"s_cselect_b32 s53, s78, s53\n" \
"s_cmp_lg_u32 s72, s56\n" \
"s_cselect_b32 s52, s66, s52\n" \
"s_cselect_b32 s53, s74, s53\n" \
"s_cmp_lg_u32 s68, s56\n" \
"s_cselect_b32 s52, s62, s52\n" \
"s_cselect_b32 s53, s70, s53\n" \
"s_cmp_lg_u32 s64, s56\n" \
"s_cselect_b32 s52, s58, s52\n" \
"s_cselect_b32 s53, s66, s53\n" \
"s_cmp_lg_u32 s60, s56\n" \
"s_cselect_b32 s52, s54, s52\n" \
"s_cselect_b32 s53, s62, s53\n" \
"s_sub_u32 s53, s53, s52\n" \
"s_lshr_b32 s55, s53, 1\n" \
"s_add_u32 s52, s52, s55\n" \
"s_add_u32 s55, s56, 4\n" \
"s_lshl_b32 s55, s55, 4\n" \
"s_sub_u32 s59, s55, s44\n" \
"s_cmp_lt_u32 s59, 2600\n" \
"s_cselect_b32 s63, 1, 2\n" \
"s_mul_i32 s59, s59, s46\n" \
"s_ashr_i32 s59, s59, 12\n" \
"s_add_u32 s59, s59, s45\n" \
"s_sub_u32 s59, s52, s59\n" \
"s_max_i32 s59, s59, -96\n" \
"s_min_i32 s59, s59, 96\n" \
"s_ashr_i32 s63, s59, s63\n" \
"s_add_u32 s63, s63, s46\n" \
"s_max_i32 s63, s63, 0x800\n" \
"s_min_i32 s63, s63, 0x2000\n" \
"s_cmp_le_u32 s53, 40\n" \
"s_cselect_b32 s44, s55, s44\n" \
"s_cselect_b32 s45, s52, s45\n" \
"s_cselect_b32 s46, s63, s46\n" \
"v_mov_b32 v248, s44\n" \
"v_mov_b32 v249, s45\n" \
"v_mov_b32 v250, s46\n" \
"s_mov_b64 exec, 1\n" \
"ds_write_b96 v252, v[248:250]\n" \
"s_mov_b64 exec, -1\n" \
"buffer_store_dwordx4 v[100:103], v214, s[4:7], s49 offen sc1\n" \
"s_xor_b32 s43, s49, s29\n" \
"buffer_store_dwordx4 v[100:103], v214, s[4:7], s43 offen sc1\n" \
"s_waitcnt vmcnt(4)\n" \
"v_mov_b32 v28, v44\n" \
"v_mov_b32 v29, v45\n" \
"v_mov_b32 v30, v46\n" \
"v_mov_b32 v31, v47\n" \
"v_mov_b32 v32, v48\n" \
"v_mov_b32 v33, v49\n" \
"v_mov_b32 v34, v50\n" \
"v_mov_b32 v35, v51\n" \
"v_mov_b32 v36, v52\n" \
"v_mov_b32 v37, v53\n" \
"v_mov_b32 v38, v54\n" \
"v_mov_b32 v39, v55\n" \
"v_mov_b32 v40, v56\n" \
"v_mov_b32 v41, v57\n" \
"v_mov_b32 v42, v58\n" \
"v_mov_b32 v43, v59\n" \
"s_add_u32 s40, s40, 1\n" \
"s_waitcnt lgkmcnt(0)\n" \
"s_barrier\n" \
"s_cmp_eq_u32 s40, 0\n" \
"s_cbranch_scc1 .LX0_wet\n" \
"s_cmp_lt_u32 s40, 63\n" \
"s_cbranch_scc1 .LX0_loop\n" \
"ds_read_b128 v[0:3], v216 offset:0\n" \
"ds_read_b128 v[4:7], v216 offset:1024\n" \
"ds_read_b128 v[8:11], v216 offset:2048\n" \
"ds_read_b128 v[12:15], v216 offset:3072\n" \
"ds_read_b128 v[16:19], v216 offset:4096\n" \
"ds_read_b128 v[20:23], v216 offset:5120\n" \
"ds_read_b128 v[24:27], v216 offset:6144\n" \
"v_mov_b32 v60, 0\n" \
"v_mov_b32 v61, 0\n" \
"v_mov_b32 v62, 0\n" \
"v_mov_b32 v63, 0\n" \
"v_mov_b32 v64, 0\n" \
"v_mov_b32 v65, 0\n" \
"v_mov_b32 v66, 0\n" \
"v_mov_b32 v67, 0\n" \
"v_mov_b32 v68, 0\n" \
"v_mov_b32 v69, 0\n" \
"v_mov_b32 v70, 0\n" \
"v_mov_b32 v71, 0\n" \
"v_mov_b32 v72, 0\n" \
"v_mov_b32 v73, 0\n" \
"v_mov_b32 v74, 0\n" \
"v_mov_b32 v75, 0\n" \
"s_nop 1\n" \
"s_waitcnt lgkmcnt(6)\n" \
"v_smfmac_f32_16x16x64_bf16 v[60:63], v[0:3], a[0:7], v200\n" \
"v_smfmac_f32_16x16x64_bf16 v[64:67], v[0:3], a[56:63], v200\n" \
"v_smfmac_f32_16x16x64_bf16 v[68:71], v[0:3], a[112:119], v200\n" \
"v_smfmac_f32_16x16x64_bf16 v[72:75], v[0:3], a[168:175], v200\n" \
"s_waitcnt lgkmcnt(5)\n" \
"v_smfmac_f32_16x16x64_bf16 v[60:63], v[4:7], a[8:15], v200\n" \
"v_smfmac_f32_16x16x64_bf16 v[64:67], v[4:7], a[64:71], v200\n" \
"v_smfmac_f32_16x16x64_bf16 v[68:71], v[4:7], a[120:127], v200\n" \
"v_smfmac_f32_16x16x64_bf16 v[72:75], v[4:7], a[176:183], v200\n" \
"s_waitcnt lgkmcnt(4)\n" \
"v_smfmac_f32_16x16x64_bf16 v[60:63], v[8:11], a[16:23], v200\n" \
"v_smfmac_f32_16x16x64_bf16 v[64:67], v[8:11], a[72:79], v200\n" \
"v_smfmac_f32_16x16x64_bf16 v[68:71], v[8:11], a[128:135], v200\n" \
"v_smfmac_f32_16x16x64_bf16 v[72:75], v[8:11], a[184:191], v200\n" \
"s_waitcnt lgkmcnt(3)\n" \
"v_smfmac_f32_16x16x64_bf16 v[60:63], v[12:15], a[24:31], v200\n" \
"v_smfmac_f32_16x16x64_bf16 v[64:67], v[12:15], a[80:87], v200\n" \
"v_smfmac_f32_16x16x64_bf16 v[68:71], v[12:15], a[136:143], v200\n" \
"v_smfmac_f32_16x16x64_bf16 v[72:75], v[12:15], a[192:199], v200\n" \
"s_waitcnt lgkmcnt(2)\n" \
"v_smfmac_f32_16x16x64_bf16 v[60:63], v[16:19], a[32:39], v200\n" \
"v_smfmac_f32_16x16x64_bf16 v[64:67], v[16:19], a[88:95], v200\n" \
"v_smfmac_f32_16x16x64_bf16 v[68:71], v[16:19], a[144:151], v200\n" \
"v_smfmac_f32_16x16x64_bf16 v[72:75], v[16:19], a[200:207], v200\n" \
"s_waitcnt lgkmcnt(1)\n" \
"v_smfmac_f32_16x16x64_bf16 v[60:63], v[20:23], a[40:47], v200\n" \
"v_smfmac_f32_16x16x64_bf16 v[64:67], v[20:23], a[96:103], v200\n" \
"v_smfmac_f32_16x16x64_bf16 v[68:71], v[20:23], a[152:159], v200\n" \
"v_smfmac_f32_16x16x64_bf16 v[72:75], v[20:23], a[208:215], v200\n" \
"s_waitcnt lgkmcnt(0)\n" \
"v_smfmac_f32_16x16x64_bf16 v[60:63], v[24:27], a[48:55], v200\n" \
"v_smfmac_f32_16x16x64_bf16 v[64:67], v[24:27], a[104:111], v200\n" \
"v_smfmac_f32_16x16x64_bf16 v[68:71], v[24:27], a[160:167], v200\n" \
"v_smfmac_f32_16x16x64_bf16 v[72:75], v[24:27], a[216:223], v200\n" \
"s_nop 7\n" \
"v_permlane32_swap_b32 v60, v64\n" \
"v_permlane32_swap_b32 v61, v65\n" \
"v_permlane32_swap_b32 v62, v66\n" \
"v_permlane32_swap_b32 v63, v67\n" \
"v_add_f32 v60, v60, v64\n" \
"v_add_f32 v61, v61, v65\n" \
"v_add_f32 v62, v62, v66\n" \
"v_add_f32 v63, v63, v67\n" \
"v_cvt_pk_bf16_f32 v184, v60, v61\n" \
"v_cvt_pk_bf16_f32 v185, v62, v63\n" \
"ds_write_b64 v217, v[184:185] offset:0\n" \
"v_permlane32_swap_b32 v68, v72\n" \
"v_permlane32_swap_b32 v69, v73\n" \
"v_permlane32_swap_b32 v70, v74\n" \
"v_permlane32_swap_b32 v71, v75\n" \
"v_add_f32 v68, v68, v72\n" \
"v_add_f32 v69, v69, v73\n" \
"v_add_f32 v70, v70, v74\n" \
"v_add_f32 v71, v71, v75\n" \
"v_cvt_pk_bf16_f32 v186, v68, v69\n" \
"v_cvt_pk_bf16_f32 v187, v70, v71\n" \
"ds_write_b64 v217, v[186:187] offset:512\n" \
"ds_read_b64_tr_b16 v[0:1], v218 offset:0\n" \
"ds_read_b64_tr_b16 v[2:3], v218 offset:64\n" \
"s_add_u32 s43, s40, 0\n" \
"s_mul_i32 s43, s43, 114688\n" \
"s_waitcnt lgkmcnt(0)\n" \
"buffer_store_dwordx4 v[0:3], v219, s[16:19], s43 offen sc1\n" \
"s_waitcnt vmcnt(0)\n" \
"s_barrier\n" \
"ds_read_b128 v[0:3], v216 offset:7168\n" \
"ds_read_b128 v[4:7], v216 offset:8192\n" \
"ds_read_b128 v[8:11], v216 offset:9216\n" \
"ds_read_b128 v[12:15], v216 offset:10240\n" \
"ds_read_b128 v[16:19], v216 offset:11264\n" \
"ds_read_b128 v[20:23], v216 offset:12288\n" \
"ds_read_b128 v[24:27], v216 offset:13312\n" \
"v_mov_b32 v60, 0\n" \
"v_mov_b32 v61, 0\n" \
"v_mov_b32 v62, 0\n" \
"v_mov_b32 v63, 0\n" \
"v_mov_b32 v64, 0\n" \
"v_mov_b32 v65, 0\n" \
"v_mov_b32 v66, 0\n" \
"v_mov_b32 v67, 0\n" \
"v_mov_b32 v68, 0\n" \
"v_mov_b32 v69, 0\n" \
"v_mov_b32 v70, 0\n" \
"v_mov_b32 v71, 0\n" \
"v_mov_b32 v72, 0\n" \
"v_mov_b32 v73, 0\n" \
"v_mov_b32 v74, 0\n" \
"v_mov_b32 v75, 0\n" \
"s_nop 1\n" \
"s_waitcnt lgkmcnt(6)\n" \
"v_smfmac_f32_16x16x64_bf16 v[60:63], v[0:3], a[0:7], v200\n" \
"v_smfmac_f32_16x16x64_bf16 v[64:67], v[0:3], a[56:63], v200\n" \
"v_smfmac_f32_16x16x64_bf16 v[68:71], v[0:3], a[112:119], v200\n" \
"v_smfmac_f32_16x16x64_bf16 v[72:75], v[0:3], a[168:175], v200\n" \
"s_waitcnt lgkmcnt(5)\n" \
"v_smfmac_f32_16x16x64_bf16 v[60:63], v[4:7], a[8:15], v200\n" \
"v_smfmac_f32_16x16x64_bf16 v[64:67], v[4:7], a[64:71], v200\n" \
"v_smfmac_f32_16x16x64_bf16 v[68:71], v[4:7], a[120:127], v200\n" \
"v_smfmac_f32_16x16x64_bf16 v[72:75], v[4:7], a[176:183], v200\n" \
"s_waitcnt lgkmcnt(4)\n" \
"v_smfmac_f32_16x16x64_bf16 v[60:63], v[8:11], a[16:23], v200\n" \
"v_smfmac_f32_16x16x64_bf16 v[64:67], v[8:11], a[72:79], v200\n" \
"v_smfmac_f32_16x16x64_bf16 v[68:71], v[8:11], a[128:135], v200\n" \
"v_smfmac_f32_16x16x64_bf16 v[72:75], v[8:11], a[184:191], v200\n" \
"s_waitcnt lgkmcnt(3)\n" \
"v_smfmac_f32_16x16x64_bf16 v[60:63], v[12:15], a[24:31], v200\n" \
"v_smfmac_f32_16x16x64_bf16 v[64:67], v[12:15], a[80:87], v200\n" \
"v_smfmac_f32_16x16x64_bf16 v[68:71], v[12:15], a[136:143], v200\n" \
"v_smfmac_f32_16x16x64_bf16 v[72:75], v[12:15], a[192:199], v200\n" \
"s_waitcnt lgkmcnt(2)\n" \
"v_smfmac_f32_16x16x64_bf16 v[60:63], v[16:19], a[32:39], v200\n" \
"v_smfmac_f32_16x16x64_bf16 v[64:67], v[16:19], a[88:95], v200\n" \
"v_smfmac_f32_16x16x64_bf16 v[68:71], v[16:19], a[144:151], v200\n" \
"v_smfmac_f32_16x16x64_bf16 v[72:75], v[16:19], a[200:207], v200\n" \
"s_waitcnt lgkmcnt(1)\n" \
"v_smfmac_f32_16x16x64_bf16 v[60:63], v[20:23], a[40:47], v200\n" \
"v_smfmac_f32_16x16x64_bf16 v[64:67], v[20:23], a[96:103], v200\n" \
"v_smfmac_f32_16x16x64_bf16 v[68:71], v[20:23], a[152:159], v200\n" \
"v_smfmac_f32_16x16x64_bf16 v[72:75], v[20:23], a[208:215], v200\n" \
"s_waitcnt lgkmcnt(0)\n" \
"v_smfmac_f32_16x16x64_bf16 v[60:63], v[24:27], a[48:55], v200\n" \
"v_smfmac_f32_16x16x64_bf16 v[64:67], v[24:27], a[104:111], v200\n" \
"v_smfmac_f32_16x16x64_bf16 v[68:71], v[24:27], a[160:167], v200\n" \
"v_smfmac_f32_16x16x64_bf16 v[72:75], v[24:27], a[216:223], v200\n" \
"s_nop 7\n" \
"v_permlane32_swap_b32 v60, v64\n" \
"v_permlane32_swap_b32 v61, v65\n" \
"v_permlane32_swap_b32 v62, v66\n" \
"v_permlane32_swap_b32 v63, v67\n" \
"v_add_f32 v60, v60, v64\n" \
"v_add_f32 v61, v61, v65\n" \
"v_add_f32 v62, v62, v66\n" \
"v_add_f32 v63, v63, v67\n" \
"v_cvt_pk_bf16_f32 v184, v60, v61\n" \
"v_cvt_pk_bf16_f32 v185, v62, v63\n" \
"ds_write_b64 v217, v[184:185] offset:0\n" \
"v_permlane32_swap_b32 v68, v72\n" \
"v_permlane32_swap_b32 v69, v73\n" \
"v_permlane32_swap_b32 v70, v74\n" \
"v_permlane32_swap_b32 v71, v75\n" \
"v_add_f32 v68, v68, v72\n" \
"v_add_f32 v69, v69, v73\n" \
"v_add_f32 v70, v70, v74\n" \
"v_add_f32 v71, v71, v75\n" \
"v_cvt_pk_bf16_f32 v186, v68, v69\n" \
"v_cvt_pk_bf16_f32 v187, v70, v71\n" \
"ds_write_b64 v217, v[186:187] offset:512\n" \
"ds_read_b64_tr_b16 v[0:1], v218 offset:0\n" \
"ds_read_b64_tr_b16 v[2:3], v218 offset:64\n" \
"s_add_u32 s43, s40, 1\n" \
"s_mul_i32 s43, s43, 114688\n" \
"s_waitcnt lgkmcnt(0)\n" \
"buffer_store_dwordx4 v[0:3], v219, s[16:19], s43 offen sc1\n" \
"s_waitcnt vmcnt(0)\n" \
"s_barrier\n" \
".LX0_end:\n" \
"s_endpgm\n" \
".p2align 6\n" \
"fk_role_X1:\n" \
"ds_read_b32 v200, v255 offset:0\n" \
"ds_read_b32 v201, v255 offset:256\n" \
"ds_read_b32 v202, v255 offset:512\n" \
"ds_read_b32 v203, v255 offset:768\n" \
"ds_read_b32 v204, v255 offset:1024\n" \
"ds_read_b32 v205, v255 offset:1280\n" \
"ds_read_b32 v206, v255 offset:1536\n" \
"ds_read_b32 v207, v255 offset:1792\n" \
"ds_read_b32 v208, v255 offset:2048\n" \
"ds_read_b32 v209, v255 offset:2304\n" \
"ds_read_b32 v210, v255 offset:2560\n" \
"ds_read_b32 v211, v255 offset:2816\n" \
"ds_read_b32 v212, v255 offset:3072\n" \
"ds_read_b32 v213, v255 offset:3328\n" \
"ds_read_b32 v214, v255 offset:3584\n" \
"ds_read_b32 v215, v255 offset:3840\n" \
"ds_read_b32 v216, v255 offset:4096\n" \
"ds_read_b32 v217, v255 offset:4352\n" \
"ds_read_b32 v218, v255 offset:4608\n" \
"ds_read_b32 v219, v255 offset:4864\n" \
"ds_read_b32 v220, v255 offset:5120\n" \
"ds_read_b32 v221, v255 offset:5376\n" \
"ds_read_b32 v254, v255 offset:7936\n" \
"s_waitcnt lgkmcnt(0)\n" \
"v_readlane_b32 s4, v254, 0\n" \
"v_readlane_b32 s5, v254, 1\n" \
"v_readlane_b32 s6, v254, 2\n" \
"v_readlane_b32 s7, v254, 3\n" \
"v_readlane_b32 s8, v254, 4\n" \
"v_readlane_b32 s9, v254, 5\n" \
"v_readlane_b32 s10, v254, 6\n" \
"v_readlane_b32 s11, v254, 7\n" \
"v_readlane_b32 s12, v254, 8\n" \
"v_readlane_b32 s13, v254, 9\n" \
"v_readlane_b32 s14, v254, 10\n" \
"v_readlane_b32 s15, v254, 11\n" \
"v_readlane_b32 s16, v254, 12\n" \
"v_readlane_b32 s17, v254, 13\n" \
"v_readlane_b32 s18, v254, 14\n" \
"v_readlane_b32 s19, v254, 15\n" \
"v_readlane_b32 s20, v254, 16\n" \
"v_readlane_b32 s21, v254, 17\n" \
"v_readlane_b32 s22, v254, 18\n" \
"v_readlane_b32 s23, v254, 19\n" \
"v_readlane_b32 s24, v254, 20\n" \
"v_readlane_b32 s25, v254, 21\n" \
"v_readlane_b32 s26, v254, 22\n" \
"v_readlane_b32 s27, v254, 23\n" \
"v_readlane_b32 s28, v254, 24\n" \
"v_readlane_b32 s29, v254, 25\n" \
"v_readlane_b32 s30, v254, 26\n" \
"v_readlane_b32 s31, v254, 27\n" \
"v_readlane_b32 s32, v254, 28\n" \
"v_readlane_b32 s33, v254, 29\n" \
"v_readlane_b32 s34, v254, 30\n" \
"v_readlane_b32 s35, v254, 31\n" \
"v_readlane_b32 s36, v254, 32\n" \
"v_readlane_b32 s37, v254, 33\n" \
"v_readlane_b32 s38, v254, 34\n" \
"v_readlane_b32 s39, v254, 35\n" \
"s_mov_b32 s40, 0\n" \
"s_mov_b32 s51, 0\n" \
"s_nop 0\n" \
"v_mov_b32 v252, s38\n" \
"s_nop 4\n" \
"v_mov_b32 v100, -1\n" \
"v_mov_b32 v101, -1\n" \
"v_mov_b32 v102, -1\n" \
"v_mov_b32 v103, -1\n" \
"s_mov_b32 s35, -2\n" \
"s_mov_b32 s2, 0\n" \
"s_mov_b32 s3, -1\n" \
"v_writelane_b32 v251, s6, 0\n" \
"v_writelane_b32 v251, s10, 1\n" \
"v_writelane_b32 v251, s14, 2\n" \
"v_writelane_b32 v251, s18, 3\n" \
"s_mov_b32 s6, 0\n" \
"s_mov_b32 s10, 0\n" \
"s_mov_b32 s14, 0\n" \
"s_mov_b32 s18, 0\n" \
"s_mov_b32 s40, -1\n" \
"s_branch .LX1_pre\n" \
".LX1_wet:\n" \
"s_waitcnt vmcnt(0)\n" \
"v_readlane_b32 s6, v251, 0\n" \
"v_readlane_b32 s10, v251, 1\n" \
"v_readlane_b32 s14, v251, 2\n" \
"v_readlane_b32 s18, v251, 3\n" \
"s_nop 4\n" \
"ds_read_b32 v232, v255 offset:7424\n" \
"ds_read_b32 v233, v255 offset:7680\n" \
"v_lshrrev_b32 v234, 4, v201\n" \
"v_and_b32 v234, 3, v234\n" \
"v_subrev_u32 v230, 512, v205\n" \
"s_add_u32 s43, s30, 1\n" \
"s_mov_b32 s50, 0\n" \
".LX1_w90l:\n" \
"buffer_load_dword v240, v230, s[4:7], 0 offen sc1\n" \
"s_waitcnt vmcnt(0)\n" \
".LX1_w90c:\n" \
"v_subrev_u32 v241, s43, v240\n" \
"v_cmp_gt_u32 vcc, 0x100, v241\n" \
"s_cmp_eq_u64 vcc, -1\n" \
"s_cbranch_scc1 .LX1_w90d\n" \
"s_sleep 2\n" \
"s_add_u32 s50, s50, 1\n" \
"s_cmp_lt_u32 s50, 0x20000\n" \
"s_cbranch_scc1 .LX1_w90l\n" \
"s_branch .LX1_end\n" \
".LX1_w90d:\n" \
"v_lshl_or_b32 v241, v241, 2, v234\n" \
"s_nop 1\n" \
"v_min_u32_dpp v241, v241, v241 row_ror:4 row_mask:0xf bank_mask:0xf\n" \
"s_nop 1\n" \
"v_min_u32_dpp v241, v241, v241 row_ror:8 row_mask:0xf bank_mask:0xf\n" \
"s_nop 1\n" \
"v_min_u32_dpp v241, v241, v241 quad_perm:[1,0,3,2] row_mask:0xf bank_mask:0xf\n" \
"s_nop 1\n" \
"v_min_u32_dpp v241, v241, v241 quad_perm:[2,3,0,1] row_mask:0xf bank_mask:0xf\n" \
"s_nop 0\n" \
"v_readlane_b32 s31, v241, 0\n" \
"v_readlane_b32 s43, v241, 16\n" \
"s_min_u32 s31, s31, s43\n" \
"v_readlane_b32 s43, v241, 32\n" \
"s_min_u32 s31, s31, s43\n" \
"v_readlane_b32 s43, v241, 48\n" \
"s_min_u32 s31, s31, s43\n" \
"v_subrev_u32 v230, 256, v205\n" \
"s_add_u32 s43, s30, 1\n" \
"s_mov_b32 s50, 0\n" \
".LX1_w91l:\n" \
"buffer_load_dword v240, v230, s[4:7], 0 offen sc1\n" \
"s_waitcnt vmcnt(0)\n" \
".LX1_w91c:\n" \
"v_subrev_u32 v241, s43, v240\n" \
"v_cmp_gt_u32 vcc, 0x100, v241\n" \
"s_cmp_eq_u64 vcc, -1\n" \
"s_cbranch_scc1 .LX1_w91d\n" \
"s_sleep 2\n" \
"s_add_u32 s50, s50, 1\n" \
"s_cmp_lt_u32 s50, 0x20000\n" \
"s_cbranch_scc1 .LX1_w91l\n" \
"s_branch .LX1_end\n" \
".LX1_w91d:\n" \
"v_lshl_or_b32 v241, v241, 2, v234\n" \
"s_nop 1\n" \
"v_min_u32_dpp v241, v241, v241 row_ror:4 row_mask:0xf bank_mask:0xf\n" \
"s_nop 1\n" \
"v_min_u32_dpp v241, v241, v241 row_ror:8 row_mask:0xf bank_mask:0xf\n" \
"s_nop 1\n" \
"v_min_u32_dpp v241, v241, v241 quad_perm:[1,0,3,2] row_mask:0xf bank_mask:0xf\n" \
"s_nop 1\n" \
"v_min_u32_dpp v241, v241, v241 quad_perm:[2,3,0,1] row_mask:0xf bank_mask:0xf\n" \
"s_nop 0\n" \
"v_readlane_b32 s32, v241, 0\n" \
"v_readlane_b32 s43, v241, 16\n" \
"s_min_u32 s32, s32, s43\n" \
"v_readlane_b32 s43, v241, 32\n" \
"s_min_u32 s32, s32, s43\n" \
"v_readlane_b32 s43, v241, 48\n" \
"s_min_u32 s32, s32, s43\n" \
"s_xor_b32 s29, s31, s32\n" \
"s_and_b32 s29, s29, 3\n" \
"s_lshl_b32 s29, s29, 13\n" \
"s_bitset1_b32 s29, 21\n" \
"s_and_b32 s31, s31, 3\n" \
"s_lshl_b32 s31, s31, 13\n" \
"s_bitcmp1_b32 s39, 0\n" \
"s_cselect_b32 s43, s29, 0\n" \
"s_xor_b32 s31, s31, s43\n" \
"s_xor_b32 s32, s31, 0x4000\n" \
"s_mov_b32 s39, 0\n" \
"v_mov_b32 v224, -1\n" \
"v_mov_b32 v225, -1\n" \
"v_mov_b32 v226, -1\n" \
"v_mov_b32 v227, -1\n" \
"s_waitcnt lgkmcnt(0)\n" \
"s_add_u32 s50, s31, 0\n" \
"buffer_store_dwordx4 v[224:227], v232, s[4:7], s50 offen sc1\n" \
"s_xor_b32 s43, s50, s29\n" \
"buffer_store_dwordx4 v[224:227], v232, s[4:7], s43 offen sc1\n" \
"s_add_u32 s50, s32, 524288\n" \
"buffer_store_dwordx4 v[224:227], v232, s[4:7], s50 offen sc1\n" \
"s_xor_b32 s43, s50, s29\n" \
"buffer_store_dwordx4 v[224:227], v232, s[4:7], s43 offen sc1\n" \
"s_add_u32 s50, s31, 1048576\n" \
"buffer_store_dwordx4 v[224:227], v232, s[4:7], s50 offen sc1\n" \
"s_xor_b32 s43, s50, s29\n" \
"buffer_store_dwordx4 v[224:227], v232, s[4:7], s43 offen sc1\n" \
"s_add_u32 s50, s32, 1572864\n" \
"buffer_store_dwordx4 v[224:227], v232, s[4:7], s50 offen sc1\n" \
"s_xor_b32 s43, s50, s29\n" \
"buffer_store_dwordx4 v[224:227], v232, s[4:7], s43 offen sc1\n" \
"s_waitcnt vmcnt(0)\n" \
"s_barrier\n" \
"s_add_u32 s43, s30, s40\n" \
"s_add_u32 s43, s43, 1\n" \
"v_mov_b32 v241, s43\n" \
"buffer_store_dword v241, v233, s[4:7], 0 offen sc1\n" \
"s_add_u32 s43, s30, 1\n" \
"s_mov_b32 s50, 0\n" \
".LX1_w92l:\n" \
"buffer_load_dword v240, v204, s[4:7], 0 offen sc1\n" \
"s_waitcnt vmcnt(0)\n" \
".LX1_w92c:\n" \
"v_subrev_u32 v241, s43, v240\n" \
"v_cmp_gt_u32 vcc, 0x100, v241\n" \
"s_cmp_eq_u64 vcc, -1\n" \
"s_cbranch_scc1 .LX1_w92d\n" \
"s_sleep 2\n" \
"s_add_u32 s50, s50, 1\n" \
"s_cmp_lt_u32 s50, 0x20000\n" \
"s_cbranch_scc1 .LX1_w92l\n" \
"s_branch .LX1_end\n" \
".LX1_w92d:\n" \
"v_readlane_b32 s43, v202, 0\n" \
"s_nop 4\n" \
"buffer_load_dwordx4 a[0:3], v201, s[8:11], s43 offen sc1\n" \
"buffer_load_dwordx4 a[4:7], v201, s[8:11], s43 offen offset:1024 sc1\n" \
"v_readlane_b32 s43, v202, 7\n" \
"s_nop 4\n" \
"buffer_load_dwordx4 a[56:59], v201, s[8:11], s43 offen sc1\n" \
"buffer_load_dwordx4 a[60:63], v201, s[8:11], s43 offen offset:1024 sc1\n" \
"v_readlane_b32 s43, v202, 14\n" \
"s_nop 4\n" \
"buffer_load_dwordx4 a[112:115], v201, s[8:11], s43 offen sc1\n" \
"buffer_load_dwordx4 a[116:119], v201, s[8:11], s43 offen offset:1024 sc1\n" \
"v_readlane_b32 s43, v202, 21\n" \
"s_nop 4\n" \
"buffer_load_dwordx4 a[168:171], v201, s[8:11], s43 offen sc1\n" \
"buffer_load_dwordx4 a[172:175], v201, s[8:11], s43 offen offset:1024 sc1\n" \
"s_add_u32 s43, s30, 2\n" \
"s_mov_b32 s50, 0\n" \
"s_branch .LX1_w93c\n" \
".LX1_w93l:\n" \
"buffer_load_dword v240, v204, s[4:7], 0 offen sc1\n" \
"s_waitcnt vmcnt(0)\n" \
".LX1_w93c:\n" \
"v_subrev_u32 v241, s43, v240\n" \
"v_cmp_gt_u32 vcc, 0x100, v241\n" \
"s_cmp_eq_u64 vcc, -1\n" \
"s_cbranch_scc1 .LX1_w93d\n" \
"s_sleep 2\n" \
"s_add_u32 s50, s50, 1\n" \
"s_cmp_lt_u32 s50, 0x20000\n" \
"s_cbranch_scc1 .LX1_w93l\n" \
"s_branch .LX1_end\n" \
".LX1_w93d:\n" \
"v_readlane_b32 s43, v202, 4\n" \
"s_nop 4\n" \
"buffer_load_dwordx4 a[32:35], v201, s[8:11], s43 offen sc1\n" \
"buffer_load_dwordx4 a[36:39], v201, s[8:11], s43 offen offset:1024 sc1\n" \
"v_readlane_b32 s43, v202, 11\n" \
"s_nop 4\n" \
"buffer_load_dwordx4 a[88:91], v201, s[8:11], s43 offen sc1\n" \
"buffer_load_dwordx4 a[92:95], v201, s[8:11], s43 offen offset:1024 sc1\n" \
"v_readlane_b32 s43, v202, 18\n" \
"s_nop 4\n" \
"buffer_load_dwordx4 a[144:147], v201, s[8:11], s43 offen sc1\n" \
"buffer_load_dwordx4 a[148:151], v201, s[8:11], s43 offen offset:1024 sc1\n" \
"v_readlane_b32 s43, v202, 25\n" \
"s_nop 4\n" \
"buffer_load_dwordx4 a[200:203], v201, s[8:11], s43 offen sc1\n" \
"buffer_load_dwordx4 a[204:207], v201, s[8:11], s43 offen offset:1024 sc1\n" \
"s_add_u32 s43, s30, 3\n" \
"s_mov_b32 s50, 0\n" \
"s_branch .LX1_w94c\n" \
".LX1_w94l:\n" \
"buffer_load_dword v240, v204, s[4:7], 0 offen sc1\n" \
"s_waitcnt vmcnt(0)\n" \
".LX1_w94c:\n" \
"v_subrev_u32 v241, s43, v240\n" \
"v_cmp_gt_u32 vcc, 0x100, v241\n" \
"s_cmp_eq_u64 vcc, -1\n" \
"s_cbranch_scc1 .LX1_w94d\n" \
"s_sleep 2\n" \
"s_add_u32 s50, s50, 1\n" \
"s_cmp_lt_u32 s50, 0x20000\n" \
"s_cbranch_scc1 .LX1_w94l\n" \
"s_branch .LX1_end\n" \
".LX1_w94d:\n" \
"v_readlane_b32 s43, v202, 1\n" \
"s_nop 4\n" \
"buffer_load_dwordx4 a[8:11], v201, s[8:11], s43 offen sc1\n" \
"buffer_load_dwordx4 a[12:15], v201, s[8:11], s43 offen offset:1024 sc1\n" \
"v_readlane_b32 s43, v202, 8\n" \
"s_nop 4\n" \
"buffer_load_dwordx4 a[64:67], v201, s[8:11], s43 offen sc1\n" \
"buffer_load_dwordx4 a[68:71], v201, s[8:11], s43 offen offset:1024 sc1\n" \
"v_readlane_b32 s43, v202, 15\n" \
"s_nop 4\n" \
"buffer_load_dwordx4 a[120:123], v201, s[8:11], s43 offen sc1\n" \
"buffer_load_dwordx4 a[124:127], v201, s[8:11], s43 offen offset:1024 sc1\n" \
"v_readlane_b32 s43, v202, 22\n" \
"s_nop 4\n" \
"buffer_load_dwordx4 a[176:179], v201, s[8:11], s43 offen sc1\n" \
"buffer_load_dwordx4 a[180:183], v201, s[8:11], s43 offen offset:1024 sc1\n" \
"s_add_u32 s43, s30, 4\n" \
"s_mov_b32 s50, 0\n" \
"s_branch .LX1_w95c\n" \
".LX1_w95l:\n" \
"buffer_load_dword v240, v204, s[4:7], 0 offen sc1\n" \
"s_waitcnt vmcnt(0)\n" \
".LX1_w95c:\n" \
"v_subrev_u32 v241, s43, v240\n" \
"v_cmp_gt_u32 vcc, 0x100, v241\n" \
"s_cmp_eq_u64 vcc, -1\n" \
"s_cbranch_scc1 .LX1_w95d\n" \
"s_sleep 2\n" \
"s_add_u32 s50, s50, 1\n" \
"s_cmp_lt_u32 s50, 0x20000\n" \
"s_cbranch_scc1 .LX1_w95l\n" \
"s_branch .LX1_end\n" \
".LX1_w95d:\n" \
"v_readlane_b32 s43, v202, 5\n" \
"s_nop 4\n" \
"buffer_load_dwordx4 a[40:43], v201, s[8:11], s43 offen sc1\n" \
"buffer_load_dwordx4 a[44:47], v201, s[8:11], s43 offen offset:1024 sc1\n" \
"v_readlane_b32 s43, v202, 12\n" \
"s_nop 4\n" \
"buffer_load_dwordx4 a[96:99], v201, s[8:11], s43 offen sc1\n" \
"buffer_load_dwordx4 a[100:103], v201, s[8:11], s43 offen offset:1024 sc1\n" \
"v_readlane_b32 s43, v202, 19\n" \
"s_nop 4\n" \
"buffer_load_dwordx4 a[152:155], v201, s[8:11], s43 offen sc1\n" \
"buffer_load_dwordx4 a[156:159], v201, s[8:11], s43 offen offset:1024 sc1\n" \
"v_readlane_b32 s43, v202, 26\n" \
"s_nop 4\n" \
"buffer_load_dwordx4 a[208:211], v201, s[8:11], s43 offen sc1\n" \
"buffer_load_dwordx4 a[212:215], v201, s[8:11], s43 offen offset:1024 sc1\n" \
"s_add_u32 s43, s30, 5\n" \
"s_mov_b32 s50, 0\n" \
"s_branch .LX1_w96c\n" \
".LX1_w96l:\n" \
"buffer_load_dword v240, v204, s[4:7], 0 offen sc1\n" \
"s_waitcnt vmcnt(0)\n" \
".LX1_w96c:\n" \
"v_subrev_u32 v241, s43, v240\n" \
"v_cmp_gt_u32 vcc, 0x100, v241\n" \
"s_cmp_eq_u64 vcc, -1\n" \
"s_cbranch_scc1 .LX1_w96d\n" \
"s_sleep 2\n" \
"s_add_u32 s50, s50, 1\n" \
"s_cmp_lt_u32 s50, 0x20000\n" \
"s_cbranch_scc1 .LX1_w96l\n" \
"s_branch .LX1_end\n" \
".LX1_w96d:\n" \
"v_readlane_b32 s43, v202, 2\n" \
"s_nop 4\n" \
"buffer_load_dwordx4 a[16:19], v201, s[8:11], s43 offen sc1\n" \
"buffer_load_dwordx4 a[20:23], v201, s[8:11], s43 offen offset:1024 sc1\n" \
"v_readlane_b32 s43, v202, 9\n" \
"s_nop 4\n" \
"buffer_load_dwordx4 a[72:75], v201, s[8:11], s43 offen sc1\n" \
"buffer_load_dwordx4 a[76:79], v201, s[8:11], s43 offen offset:1024 sc1\n" \
"v_readlane_b32 s43, v202, 16\n" \
"s_nop 4\n" \
"buffer_load_dwordx4 a[128:131], v201, s[8:11], s43 offen sc1\n" \
"buffer_load_dwordx4 a[132:135], v201, s[8:11], s43 offen offset:1024 sc1\n" \
"v_readlane_b32 s43, v202, 23\n" \
"s_nop 4\n" \
"buffer_load_dwordx4 a[184:187], v201, s[8:11], s43 offen sc1\n" \
"buffer_load_dwordx4 a[188:191], v201, s[8:11], s43 offen offset:1024 sc1\n" \
"s_add_u32 s43, s30, 6\n" \
"s_mov_b32 s50, 0\n" \
"s_branch .LX1_w97c\n" \
".LX1_w97l:\n" \
"buffer_load_dword v240, v204, s[4:7], 0 offen sc1\n" \
"s_waitcnt vmcnt(0)\n" \
".LX1_w97c:\n" \
"v_subrev_u32 v241, s43, v240\n" \
"v_cmp_gt_u32 vcc, 0x100, v241\n" \
"s_cmp_eq_u64 vcc, -1\n" \
"s_cbranch_scc1 .LX1_w97d\n" \
"s_sleep 2\n" \
"s_add_u32 s50, s50, 1\n" \
"s_cmp_lt_u32 s50, 0x20000\n" \
"s_cbranch_scc1 .LX1_w97l\n" \
"s_branch .LX1_end\n" \
".LX1_w97d:\n" \
"v_readlane_b32 s43, v202, 6\n" \
"s_nop 4\n" \
"buffer_load_dwordx4 a[48:51], v201, s[8:11], s43 offen sc1\n" \
"buffer_load_dwordx4 a[52:55], v201, s[8:11], s43 offen offset:1024 sc1\n" \
"v_readlane_b32 s43, v202, 13\n" \
"s_nop 4\n" \
"buffer_load_dwordx4 a[104:107], v201, s[8:11], s43 offen sc1\n" \
"buffer_load_dwordx4 a[108:111], v201, s[8:11], s43 offen offset:1024 sc1\n" \
"v_readlane_b32 s43, v202, 20\n" \
"s_nop 4\n" \
"buffer_load_dwordx4 a[160:163], v201, s[8:11], s43 offen sc1\n" \
"buffer_load_dwordx4 a[164:167], v201, s[8:11], s43 offen offset:1024 sc1\n" \
"v_readlane_b32 s43, v202, 27\n" \
"s_nop 4\n" \
"buffer_load_dwordx4 a[216:219], v201, s[8:11], s43 offen sc1\n" \
"buffer_load_dwordx4 a[220:223], v201, s[8:11], s43 offen offset:1024 sc1\n" \
"s_add_u32 s43, s30, 7\n" \
"s_mov_b32 s50, 0\n" \
"s_branch .LX1_w98c\n" \
".LX1_w98l:\n" \
"buffer_load_dword v240, v204, s[4:7], 0 offen sc1\n" \
"s_waitcnt vmcnt(0)\n" \
".LX1_w98c:\n" \
"v_subrev_u32 v241, s43, v240\n" \
"v_cmp_gt_u32 vcc, 0x100, v241\n" \
"s_cmp_eq_u64 vcc, -1\n" \
"s_cbranch_scc1 .LX1_w98d\n" \
"s_sleep 2\n" \
"s_add_u32 s50, s50, 1\n" \
"s_cmp_lt_u32 s50, 0x20000\n" \
"s_cbranch_scc1 .LX1_w98l\n" \
"s_branch .LX1_end\n" \
".LX1_w98d:\n" \
"v_readlane_b32 s43, v202, 3\n" \
"s_nop 4\n" \
"buffer_load_dwordx4 a[24:27], v201, s[8:11], s43 offen sc1\n" \
"buffer_load_dwordx4 a[28:31], v201, s[8:11], s43 offen offset:1024 sc1\n" \
"v_readlane_b32 s43, v202, 10\n" \
"s_nop 4\n" \
"buffer_load_dwordx4 a[80:83], v201, s[8:11], s43 offen sc1\n" \
"buffer_load_dwordx4 a[84:87], v201, s[8:11], s43 offen offset:1024 sc1\n" \
"v_readlane_b32 s43, v202, 17\n" \
"s_nop 4\n" \
"buffer_load_dwordx4 a[136:139], v201, s[8:11], s43 offen sc1\n" \
"buffer_load_dwordx4 a[140:143], v201, s[8:11], s43 offen offset:1024 sc1\n" \
"v_readlane_b32 s43, v202, 24\n" \
"s_nop 4\n" \
"buffer_load_dwordx4 a[192:195], v201, s[8:11], s43 offen sc1\n" \
"buffer_load_dwordx4 a[196:199], v201, s[8:11], s43 offen offset:1024 sc1\n" \
"s_add_u32 s43, s30, 1\n" \
"s_mov_b32 s50, 0\n" \
".LX1_w99l:\n" \
"buffer_load_dword v240, v205, s[4:7], 0 offen sc1\n" \
"s_waitcnt vmcnt(0)\n" \
".LX1_w99c:\n" \
"v_subrev_u32 v241, s43, v240\n" \
"v_cmp_gt_u32 vcc, 0x100, v241\n" \
"s_cmp_eq_u64 vcc, -1\n" \
"s_cbranch_scc1 .LX1_w99d\n" \
"s_sleep 2\n" \
"s_add_u32 s50, s50, 1\n" \
"s_cmp_lt_u32 s50, 0x20000\n" \
"s_cbranch_scc1 .LX1_w99l\n" \
"s_branch .LX1_end\n" \
".LX1_w99d:\n" \
"s_add_u32 s43, s30, 1\n" \
"s_mov_b32 s50, 0\n" \
".LX1_w100l:\n" \
"buffer_load_dword v240, v206, s[4:7], 0 offen sc1\n" \
"s_waitcnt vmcnt(0)\n" \
".LX1_w100c:\n" \
"v_subrev_u32 v241, s43, v240\n" \
"v_cmp_gt_u32 vcc, 0x100, v241\n" \
"s_cmp_eq_u64 vcc, -1\n" \
"s_cbranch_scc1 .LX1_w100d\n" \
"s_sleep 2\n" \
"s_add_u32 s50, s50, 1\n" \
"s_cmp_lt_u32 s50, 0x20000\n" \
"s_cbranch_scc1 .LX1_w100l\n" \
"s_branch .LX1_end\n" \
".LX1_w100d:\n" \
"s_add_u32 s43, s30, 8\n" \
"s_mov_b32 s50, 0\n" \
".LX1_w101l:\n" \
"buffer_load_dword v240, v215, s[4:7], 0 offen sc1\n" \
"s_waitcnt vmcnt(0)\n" \
".LX1_w101c:\n" \
"v_subrev_u32 v241, s43, v240\n" \
"v_cmp_gt_u32 vcc, 0x100, v241\n" \
"s_cmp_eq_u64 vcc, -1\n" \
"s_cbranch_scc1 .LX1_w101d\n" \
"s_sleep 2\n" \
"s_add_u32 s50, s50, 1\n" \
"s_cmp_lt_u32 s50, 0x20000\n" \
"s_cbranch_scc1 .LX1_w101l\n" \
"s_branch .LX1_end\n" \
".LX1_w101d:\n" \
"s_add_u32 s50, s40, 0\n" \
"s_mul_i32 s50, s50, 458752\n" \
"s_add_u32 s50, s50, s34\n" \
"buffer_load_dwordx4 v[28:31], v201, s[12:15], s50 offen sc1\n" \
"buffer_load_dwordx4 v[32:35], v201, s[12:15], s50 offen offset:1024 sc1\n" \
"buffer_load_dwordx4 v[36:39], v201, s[12:15], s50 offen offset:2048 sc1\n" \
"buffer_load_dwordx4 v[40:43], v201, s[12:15], s50 offen offset:3072 sc1\n" \
"s_add_u32 s43, s30, 9\n" \
"s_mov_b32 s50, 0\n" \
".LX1_w102l:\n" \
"buffer_load_dword v240, v215, s[4:7], 0 offen sc1\n" \
"s_waitcnt vmcnt(0)\n" \
".LX1_w102c:\n" \
"v_subrev_u32 v241, s43, v240\n" \
"v_cmp_gt_u32 vcc, 0x100, v241\n" \
"s_cmp_eq_u64 vcc, -1\n" \
"s_cbranch_scc1 .LX1_w102d\n" \
"s_sleep 2\n" \
"s_add_u32 s50, s50, 1\n" \
"s_cmp_lt_u32 s50, 0x20000\n" \
"s_cbranch_scc1 .LX1_w102l\n" \
"s_branch .LX1_end\n" \
".LX1_w102d:\n" \
".LX1_pre:\n" \
"s_barrier\n" \
".LX1_loop:\n" \
"s_add_u32 s43, s40, 2\n" \
"s_and_b32 s43, s43, 3\n" \
"s_bitcmp1_b32 s43, 0\n" \
"s_cselect_b32 s49, s32, s31\n" \
"s_lshl_b32 s43, s43, 19\n" \
"s_add_u32 s49, s49, s43\n" \
"s_add_u32 s43, s30, s40\n" \
"s_add_u32 s43, s43, -1\n" \
"v_mov_b32 v241, s43\n" \
"buffer_store_dword v241, v220, s[4:7], 0 offen sc1\n" \
"ds_read_b128 v[0:3], v216 offset:0\n" \
"ds_read_b128 v[4:7], v216 offset:1024\n" \
"ds_read_b128 v[8:11], v216 offset:2048\n" \
"ds_read_b128 v[12:15], v216 offset:3072\n" \
"ds_read_b128 v[16:19], v216 offset:4096\n" \
"ds_read_b128 v[20:23], v216 offset:5120\n" \
"ds_read_b128 v[24:27], v216 offset:6144\n" \
"v_mov_b32 v60, 0\n" \
"v_mov_b32 v61, 0\n" \
"v_mov_b32 v62, 0\n" \
"v_mov_b32 v63, 0\n" \
"v_mov_b32 v64, 0\n" \
"v_mov_b32 v65, 0\n" \
"v_mov_b32 v66, 0\n" \
"v_mov_b32 v67, 0\n" \
"v_mov_b32 v68, 0\n" \
"v_mov_b32 v69, 0\n" \
"v_mov_b32 v70, 0\n" \
"v_mov_b32 v71, 0\n" \
"v_mov_b32 v72, 0\n" \
"v_mov_b32 v73, 0\n" \
"v_mov_b32 v74, 0\n" \
"v_mov_b32 v75, 0\n" \
"s_nop 1\n" \
"s_waitcnt lgkmcnt(6)\n" \
"v_smfmac_f32_16x16x64_bf16 v[60:63], v[0:3], a[0:7], v200\n" \
"v_smfmac_f32_16x16x64_bf16 v[64:67], v[0:3], a[56:63], v200\n" \
"v_smfmac_f32_16x16x64_bf16 v[68:71], v[0:3], a[112:119], v200\n" \
"v_smfmac_f32_16x16x64_bf16 v[72:75], v[0:3], a[168:175], v200\n" \
"s_waitcnt lgkmcnt(5)\n" \
"v_smfmac_f32_16x16x64_bf16 v[60:63], v[4:7], a[8:15], v200\n" \
"v_smfmac_f32_16x16x64_bf16 v[64:67], v[4:7], a[64:71], v200\n" \
"v_smfmac_f32_16x16x64_bf16 v[68:71], v[4:7], a[120:127], v200\n" \
"v_smfmac_f32_16x16x64_bf16 v[72:75], v[4:7], a[176:183], v200\n" \
"s_waitcnt lgkmcnt(4)\n" \
"v_smfmac_f32_16x16x64_bf16 v[60:63], v[8:11], a[16:23], v200\n" \
"v_smfmac_f32_16x16x64_bf16 v[64:67], v[8:11], a[72:79], v200\n" \
"v_smfmac_f32_16x16x64_bf16 v[68:71], v[8:11], a[128:135], v200\n" \
"v_smfmac_f32_16x16x64_bf16 v[72:75], v[8:11], a[184:191], v200\n" \
"s_waitcnt lgkmcnt(3)\n" \
"v_smfmac_f32_16x16x64_bf16 v[60:63], v[12:15], a[24:31], v200\n" \
"v_smfmac_f32_16x16x64_bf16 v[64:67], v[12:15], a[80:87], v200\n" \
"v_smfmac_f32_16x16x64_bf16 v[68:71], v[12:15], a[136:143], v200\n" \
"v_smfmac_f32_16x16x64_bf16 v[72:75], v[12:15], a[192:199], v200\n" \
"s_waitcnt lgkmcnt(2)\n" \
"v_smfmac_f32_16x16x64_bf16 v[60:63], v[16:19], a[32:39], v200\n" \
"v_smfmac_f32_16x16x64_bf16 v[64:67], v[16:19], a[88:95], v200\n" \
"v_smfmac_f32_16x16x64_bf16 v[68:71], v[16:19], a[144:151], v200\n" \
"v_smfmac_f32_16x16x64_bf16 v[72:75], v[16:19], a[200:207], v200\n" \
"s_waitcnt lgkmcnt(1)\n" \
"v_smfmac_f32_16x16x64_bf16 v[60:63], v[20:23], a[40:47], v200\n" \
"v_smfmac_f32_16x16x64_bf16 v[64:67], v[20:23], a[96:103], v200\n" \
"v_smfmac_f32_16x16x64_bf16 v[68:71], v[20:23], a[152:159], v200\n" \
"v_smfmac_f32_16x16x64_bf16 v[72:75], v[20:23], a[208:215], v200\n" \
"s_waitcnt lgkmcnt(0)\n" \
"v_smfmac_f32_16x16x64_bf16 v[60:63], v[24:27], a[48:55], v200\n" \
"v_smfmac_f32_16x16x64_bf16 v[64:67], v[24:27], a[104:111], v200\n" \
"v_smfmac_f32_16x16x64_bf16 v[68:71], v[24:27], a[160:167], v200\n" \
"v_smfmac_f32_16x16x64_bf16 v[72:75], v[24:27], a[216:223], v200\n" \
"s_barrier\n" \
"s_add_u32 s50, s40, 1\n" \
"s_mul_i32 s50, s50, 458752\n" \
"s_add_u32 s50, s50, s34\n" \
"buffer_load_dwordx4 v[44:47], v201, s[12:15], s50 offen sc1\n" \
"buffer_load_dwordx4 v[48:51], v201, s[12:15], s50 offen offset:1024 sc1\n" \
"buffer_load_dwordx4 v[52:55], v201, s[12:15], s50 offen offset:2048 sc1\n" \
"buffer_load_dwordx4 v[56:59], v201, s[12:15], s50 offen offset:3072 sc1\n" \
"s_nop 7\n" \
"v_permlane32_swap_b32 v60, v64\n" \
"v_permlane32_swap_b32 v61, v65\n" \
"v_permlane32_swap_b32 v62, v66\n" \
"v_permlane32_swap_b32 v63, v67\n" \
"v_add_f32 v60, v60, v64\n" \
"v_add_f32 v61, v61, v65\n" \
"v_add_f32 v62, v62, v66\n" \
"v_add_f32 v63, v63, v67\n" \
"v_cvt_pk_bf16_f32 v184, v60, v61\n" \
"v_cvt_pk_bf16_f32 v185, v62, v63\n" \
"ds_write_b64 v217, v[184:185] offset:0\n" \
"v_permlane32_swap_b32 v68, v72\n" \
"v_permlane32_swap_b32 v69, v73\n" \
"v_permlane32_swap_b32 v70, v74\n" \
"v_permlane32_swap_b32 v71, v75\n" \
"v_add_f32 v68, v68, v72\n" \
"v_add_f32 v69, v69, v73\n" \
"v_add_f32 v70, v70, v74\n" \
"v_add_f32 v71, v71, v75\n" \
"v_cvt_pk_bf16_f32 v186, v68, v69\n" \
"v_cvt_pk_bf16_f32 v187, v70, v71\n" \
"ds_write_b64 v217, v[186:187] offset:512\n" \
"ds_read_b64_tr_b16 v[0:1], v218 offset:0\n" \
"ds_read_b64_tr_b16 v[2:3], v218 offset:64\n" \
"s_add_u32 s43, s40, 0\n" \
"s_mul_i32 s43, s43, 114688\n" \
"s_waitcnt lgkmcnt(0)\n" \
"buffer_store_dwordx4 v[0:3], v219, s[16:19], s43 offen sc1\n" \
"s_waitcnt vmcnt(5)\n" \
"s_barrier\n" \
"ds_read_b64_tr_b16 v[92:93], v213\n" \
"ds_read_b64_tr_b16 v[94:95], v213 offset:64\n" \
"ds_read_b32 v84, v252 offset:16\n" \
"v_mov_b32 v245, v245\n" \
"s_nop 2\n" \
"v_mov_b32 v245, v245\n" \
"s_nop 2\n" \
"v_mov_b32 v245, v245\n" \
"s_waitcnt lgkmcnt(1)\n" \
"v_mfma_f32_16x16x32_bf16 v[68:71], v[92:95], v[28:31], 0\n" \
"s_waitcnt lgkmcnt(0)\n" \
"v_xor_b32 v85, s29, v84\n" \
"v_mfma_f32_16x16x32_bf16 v[76:79], v[92:95], v[36:39], 0\n" \
"v_add_u32 v84, v84, v210\n" \
"v_add_u32 v85, v85, v210\n" \
"v_mfma_f32_16x16x32_bf16 v[72:75], v[92:95], v[32:35], 0\n" \
"s_nop 1\n" \
"v_mfma_f32_16x16x32_bf16 v[80:83], v[92:95], v[40:43], 0\n" \
"v_cvt_pk_bf16_f32 v96, v68, v69\n" \
"v_cvt_pk_bf16_f32 v97, v70, v71\n" \
"v_cvt_pk_bf16_f32 v98, v76, v77\n" \
"v_cvt_pk_bf16_f32 v99, v78, v79\n" \
"s_mov_b64 exec, s[2:3]\n" \
"v_cvt_pk_bf16_f32 v96, v72, v73\n" \
"v_cvt_pk_bf16_f32 v97, v74, v75\n" \
"s_nop 0\n" \
"v_cvt_pk_bf16_f32 v98, v80, v81\n" \
"v_cvt_pk_bf16_f32 v99, v82, v83\n" \
"s_mov_b64 exec, -1\n" \
"s_nop 1\n" \
"v_permlane16_swap_b32 v96, v98\n" \
"v_permlane16_swap_b32 v97, v99\n" \
"buffer_store_dwordx4 v[96:99], v84, s[4:7], 0 offen sc1\n" \
"s_barrier\n" \
"buffer_store_dwordx4 v[96:99], v85, s[4:7], 0 offen sc1\n" \
"s_nop 15\n" \
"s_nop 15\n" \
"s_nop 15\n" \
"s_nop 15\n" \
"s_nop 15\n" \
"buffer_store_dwordx4 v[100:103], v214, s[4:7], s49 offen sc1\n" \
"s_xor_b32 s43, s49, s29\n" \
"buffer_store_dwordx4 v[100:103], v214, s[4:7], s43 offen sc1\n" \
"s_waitcnt vmcnt(4)\n" \
"v_mov_b32 v28, v44\n" \
"v_mov_b32 v29, v45\n" \
"v_mov_b32 v30, v46\n" \
"v_mov_b32 v31, v47\n" \
"v_mov_b32 v32, v48\n" \
"v_mov_b32 v33, v49\n" \
"v_mov_b32 v34, v50\n" \
"v_mov_b32 v35, v51\n" \
"v_mov_b32 v36, v52\n" \
"v_mov_b32 v37, v53\n" \
"v_mov_b32 v38, v54\n" \
"v_mov_b32 v39, v55\n" \
"v_mov_b32 v40, v56\n" \
"v_mov_b32 v41, v57\n" \
"v_mov_b32 v42, v58\n" \
"v_mov_b32 v43, v59\n" \
"s_add_u32 s40, s40, 1\n" \
"s_waitcnt lgkmcnt(0)\n" \
"s_barrier\n" \
"s_cmp_eq_u32 s40, 0\n" \
"s_cbranch_scc1 .LX1_wet\n" \
"s_cmp_lt_u32 s40, 63\n" \
"s_cbranch_scc1 .LX1_loop\n" \
"s_add_u32 s43, s30, s40\n" \
"s_add_u32 s43, s43, -1\n" \
"v_mov_b32 v241, s43\n" \
"buffer_store_dword v241, v220, s[4:7], 0 offen sc1\n" \
"ds_read_b128 v[0:3], v216 offset:0\n" \
"ds_read_b128 v[4:7], v216 offset:1024\n" \
"ds_read_b128 v[8:11], v216 offset:2048\n" \
"ds_read_b128 v[12:15], v216 offset:3072\n" \
"ds_read_b128 v[16:19], v216 offset:4096\n" \
"ds_read_b128 v[20:23], v216 offset:5120\n" \
"ds_read_b128 v[24:27], v216 offset:6144\n" \
"v_mov_b32 v60, 0\n" \
"v_mov_b32 v61, 0\n" \
"v_mov_b32 v62, 0\n" \
"v_mov_b32 v63, 0\n" \
"v_mov_b32 v64, 0\n" \
"v_mov_b32 v65, 0\n" \
"v_mov_b32 v66, 0\n" \
"v_mov_b32 v67, 0\n" \
"v_mov_b32 v68, 0\n" \
"v_mov_b32 v69, 0\n" \
"v_mov_b32 v70, 0\n" \
"v_mov_b32 v71, 0\n" \
"v_mov_b32 v72, 0\n" \
"v_mov_b32 v73, 0\n" \
"v_mov_b32 v74, 0\n" \
"v_mov_b32 v75, 0\n" \
"s_nop 1\n" \
"s_waitcnt lgkmcnt(6)\n" \
"v_smfmac_f32_16x16x64_bf16 v[60:63], v[0:3], a[0:7], v200\n" \
"v_smfmac_f32_16x16x64_bf16 v[64:67], v[0:3], a[56:63], v200\n" \
"v_smfmac_f32_16x16x64_bf16 v[68:71], v[0:3], a[112:119], v200\n" \
"v_smfmac_f32_16x16x64_bf16 v[72:75], v[0:3], a[168:175], v200\n" \
"s_waitcnt lgkmcnt(5)\n" \
"v_smfmac_f32_16x16x64_bf16 v[60:63], v[4:7], a[8:15], v200\n" \
"v_smfmac_f32_16x16x64_bf16 v[64:67], v[4:7], a[64:71], v200\n" \
"v_smfmac_f32_16x16x64_bf16 v[68:71], v[4:7], a[120:127], v200\n" \
"v_smfmac_f32_16x16x64_bf16 v[72:75], v[4:7], a[176:183], v200\n" \
"s_waitcnt lgkmcnt(4)\n" \
"v_smfmac_f32_16x16x64_bf16 v[60:63], v[8:11], a[16:23], v200\n" \
"v_smfmac_f32_16x16x64_bf16 v[64:67], v[8:11], a[72:79], v200\n" \
"v_smfmac_f32_16x16x64_bf16 v[68:71], v[8:11], a[128:135], v200\n" \
"v_smfmac_f32_16x16x64_bf16 v[72:75], v[8:11], a[184:191], v200\n" \
"s_waitcnt lgkmcnt(3)\n" \
"v_smfmac_f32_16x16x64_bf16 v[60:63], v[12:15], a[24:31], v200\n" \
"v_smfmac_f32_16x16x64_bf16 v[64:67], v[12:15], a[80:87], v200\n" \
"v_smfmac_f32_16x16x64_bf16 v[68:71], v[12:15], a[136:143], v200\n" \
"v_smfmac_f32_16x16x64_bf16 v[72:75], v[12:15], a[192:199], v200\n" \
"s_waitcnt lgkmcnt(2)\n" \
"v_smfmac_f32_16x16x64_bf16 v[60:63], v[16:19], a[32:39], v200\n" \
"v_smfmac_f32_16x16x64_bf16 v[64:67], v[16:19], a[88:95], v200\n" \
"v_smfmac_f32_16x16x64_bf16 v[68:71], v[16:19], a[144:151], v200\n" \
"v_smfmac_f32_16x16x64_bf16 v[72:75], v[16:19], a[200:207], v200\n" \
"s_waitcnt lgkmcnt(1)\n" \
"v_smfmac_f32_16x16x64_bf16 v[60:63], v[20:23], a[40:47], v200\n" \
"v_smfmac_f32_16x16x64_bf16 v[64:67], v[20:23], a[96:103], v200\n" \
"v_smfmac_f32_16x16x64_bf16 v[68:71], v[20:23], a[152:159], v200\n" \
"v_smfmac_f32_16x16x64_bf16 v[72:75], v[20:23], a[208:215], v200\n" \
"s_waitcnt lgkmcnt(0)\n" \
"v_smfmac_f32_16x16x64_bf16 v[60:63], v[24:27], a[48:55], v200\n" \
"v_smfmac_f32_16x16x64_bf16 v[64:67], v[24:27], a[104:111], v200\n" \
"v_smfmac_f32_16x16x64_bf16 v[68:71], v[24:27], a[160:167], v200\n" \
"v_smfmac_f32_16x16x64_bf16 v[72:75], v[24:27], a[216:223], v200\n" \
"s_nop 7\n" \
"v_permlane32_swap_b32 v60, v64\n" \
"v_permlane32_swap_b32 v61, v65\n" \
"v_permlane32_swap_b32 v62, v66\n" \
"v_permlane32_swap_b32 v63, v67\n" \
"v_add_f32 v60, v60, v64\n" \
"v_add_f32 v61, v61, v65\n" \
"v_add_f32 v62, v62, v66\n" \
"v_add_f32 v63, v63, v67\n" \
"v_cvt_pk_bf16_f32 v184, v60, v61\n" \
"v_cvt_pk_bf16_f32 v185, v62, v63\n" \
"ds_write_b64 v217, v[184:185] offset:0\n" \
"v_permlane32_swap_b32 v68, v72\n" \
"v_permlane32_swap_b32 v69, v73\n" \
"v_permlane32_swap_b32 v70, v74\n" \
"v_permlane32_swap_b32 v71, v75\n" \
"v_add_f32 v68, v68, v72\n" \
"v_add_f32 v69, v69, v73\n" \
"v_add_f32 v70, v70, v74\n" \
"v_add_f32 v71, v71, v75\n" \
"v_cvt_pk_bf16_f32 v186, v68, v69\n" \
"v_cvt_pk_bf16_f32 v187, v70, v71\n" \
"ds_write_b64 v217, v[186:187] offset:512\n" \
"ds_read_b64_tr_b16 v[0:1], v218 offset:0\n" \
"ds_read_b64_tr_b16 v[2:3], v218 offset:64\n" \
"s_add_u32 s43, s40, 0\n" \
"s_mul_i32 s43, s43, 114688\n" \
"s_waitcnt lgkmcnt(0)\n" \
"buffer_store_dwordx4 v[0:3], v219, s[16:19], s43 offen sc1\n" \
"s_waitcnt vmcnt(0)\n" \
"s_barrier\n" \
"s_add_u32 s43, s30, s40\n" \
"v_mov_b32 v241, s43\n" \
"buffer_store_dword v241, v220, s[4:7], 0 offen sc1\n" \
"ds_read_b128 v[0:3], v216 offset:7168\n" \
"ds_read_b128 v[4:7], v216 offset:8192\n" \
"ds_read_b128 v[8:11], v216 offset:9216\n" \
"ds_read_b128 v[12:15], v216 offset:10240\n" \
"ds_read_b128 v[16:19], v216 offset:11264\n" \
"ds_read_b128 v[20:23], v216 offset:12288\n" \
"ds_read_b128 v[24:27], v216 offset:13312\n" \
"v_mov_b32 v60, 0\n" \
"v_mov_b32 v61, 0\n" \
"v_mov_b32 v62, 0\n" \
"v_mov_b32 v63, 0\n" \
"v_mov_b32 v64, 0\n" \
"v_mov_b32 v65, 0\n" \
"v_mov_b32 v66, 0\n" \
"v_mov_b32 v67, 0\n" \
"v_mov_b32 v68, 0\n" \
"v_mov_b32 v69, 0\n" \
"v_mov_b32 v70, 0\n" \
"v_mov_b32 v71, 0\n" \
"v_mov_b32 v72, 0\n" \
"v_mov_b32 v73, 0\n" \
"v_mov_b32 v74, 0\n" \
"v_mov_b32 v75, 0\n" \
"s_nop 1\n" \
"s_waitcnt lgkmcnt(6)\n" \
"v_smfmac_f32_16x16x64_bf16 v[60:63], v[0:3], a[0:7], v200\n" \
"v_smfmac_f32_16x16x64_bf16 v[64:67], v[0:3], a[56:63], v200\n" \
"v_smfmac_f32_16x16x64_bf16 v[68:71], v[0:3], a[112:119], v200\n" \
"v_smfmac_f32_16x16x64_bf16 v[72:75], v[0:3], a[168:175], v200\n" \
"s_waitcnt lgkmcnt(5)\n" \
"v_smfmac_f32_16x16x64_bf16 v[60:63], v[4:7], a[8:15], v200\n" \
"v_smfmac_f32_16x16x64_bf16 v[64:67], v[4:7], a[64:71], v200\n" \
"v_smfmac_f32_16x16x64_bf16 v[68:71], v[4:7], a[120:127], v200\n" \
"v_smfmac_f32_16x16x64_bf16 v[72:75], v[4:7], a[176:183], v200\n" \
"s_waitcnt lgkmcnt(4)\n" \
"v_smfmac_f32_16x16x64_bf16 v[60:63], v[8:11], a[16:23], v200\n" \
"v_smfmac_f32_16x16x64_bf16 v[64:67], v[8:11], a[72:79], v200\n" \
"v_smfmac_f32_16x16x64_bf16 v[68:71], v[8:11], a[128:135], v200\n" \
"v_smfmac_f32_16x16x64_bf16 v[72:75], v[8:11], a[184:191], v200\n" \
"s_waitcnt lgkmcnt(3)\n" \
"v_smfmac_f32_16x16x64_bf16 v[60:63], v[12:15], a[24:31], v200\n" \
"v_smfmac_f32_16x16x64_bf16 v[64:67], v[12:15], a[80:87], v200\n" \
"v_smfmac_f32_16x16x64_bf16 v[68:71], v[12:15], a[136:143], v200\n" \
"v_smfmac_f32_16x16x64_bf16 v[72:75], v[12:15], a[192:199], v200\n" \
"s_waitcnt lgkmcnt(2)\n" \
"v_smfmac_f32_16x16x64_bf16 v[60:63], v[16:19], a[32:39], v200\n" \
"v_smfmac_f32_16x16x64_bf16 v[64:67], v[16:19], a[88:95], v200\n" \
"v_smfmac_f32_16x16x64_bf16 v[68:71], v[16:19], a[144:151], v200\n" \
"v_smfmac_f32_16x16x64_bf16 v[72:75], v[16:19], a[200:207], v200\n" \
"s_waitcnt lgkmcnt(1)\n" \
"v_smfmac_f32_16x16x64_bf16 v[60:63], v[20:23], a[40:47], v200\n" \
"v_smfmac_f32_16x16x64_bf16 v[64:67], v[20:23], a[96:103], v200\n" \
"v_smfmac_f32_16x16x64_bf16 v[68:71], v[20:23], a[152:159], v200\n" \
"v_smfmac_f32_16x16x64_bf16 v[72:75], v[20:23], a[208:215], v200\n" \
"s_waitcnt lgkmcnt(0)\n" \
"v_smfmac_f32_16x16x64_bf16 v[60:63], v[24:27], a[48:55], v200\n" \
"v_smfmac_f32_16x16x64_bf16 v[64:67], v[24:27], a[104:111], v200\n" \
"v_smfmac_f32_16x16x64_bf16 v[68:71], v[24:27], a[160:167], v200\n" \
"v_smfmac_f32_16x16x64_bf16 v[72:75], v[24:27], a[216:223], v200\n" \
"s_nop 7\n" \
"v_permlane32_swap_b32 v60, v64\n" \
"v_permlane32_swap_b32 v61, v65\n" \
"v_permlane32_swap_b32 v62, v66\n" \
"v_permlane32_swap_b32 v63, v67\n" \
"v_add_f32 v60, v60, v64\n" \
"v_add_f32 v61, v61, v65\n" \
"v_add_f32 v62, v62, v66\n" \
"v_add_f32 v63, v63, v67\n" \
"v_cvt_pk_bf16_f32 v184, v60, v61\n" \
"v_cvt_pk_bf16_f32 v185, v62, v63\n" \
"ds_write_b64 v217, v[184:185] offset:0\n" \
"v_permlane32_swap_b32 v68, v72\n" \
"v_permlane32_swap_b32 v69, v73\n" \
"v_permlane32_swap_b32 v70, v74\n" \
"v_permlane32_swap_b32 v71, v75\n" \
"v_add_f32 v68, v68, v72\n" \
"v_add_f32 v69, v69, v73\n" \
"v_add_f32 v70, v70, v74\n" \
"v_add_f32 v71, v71, v75\n" \
"v_cvt_pk_bf16_f32 v186, v68, v69\n" \
"v_cvt_pk_bf16_f32 v187, v70, v71\n" \
"ds_write_b64 v217, v[186:187] offset:512\n" \
"ds_read_b64_tr_b16 v[0:1], v218 offset:0\n" \
"ds_read_b64_tr_b16 v[2:3], v218 offset:64\n" \
"s_add_u32 s43, s40, 1\n" \
"s_mul_i32 s43, s43, 114688\n" \
"s_waitcnt lgkmcnt(0)\n" \
"buffer_store_dwordx4 v[0:3], v219, s[16:19], s43 offen sc1\n" \
"s_waitcnt vmcnt(0)\n" \
"s_barrier\n" \
"s_add_u32 s43, s30, s40\n" \
"s_add_u32 s43, s43, 1\n" \
"v_mov_b32 v241, s43\n" \
"buffer_store_dword v241, v220, s[4:7], 0 offen sc1\n" \
".LX1_end:\n" \
"s_endpgm\n" \
".p2align 6\n" \
"fk_role_X2:\n" \
"ds_read_b32 v200, v255 offset:0\n" \
"ds_read_b32 v201, v255 offset:256\n" \
"ds_read_b32 v202, v255 offset:512\n" \
"ds_read_b32 v203, v255 offset:768\n" \
"ds_read_b32 v204, v255 offset:1024\n" \
"ds_read_b32 v205, v255 offset:1280\n" \
"ds_read_b32 v206, v255 offset:1536\n" \
"ds_read_b32 v207, v255 offset:1792\n" \
"ds_read_b32 v208, v255 offset:2048\n" \
"ds_read_b32 v209, v255 offset:2304\n" \
"ds_read_b32 v210, v255 offset:2560\n" \
"ds_read_b32 v211, v255 offset:2816\n" \
"ds_read_b32 v212, v255 offset:3072\n" \
"ds_read_b32 v213, v255 offset:3328\n" \
"ds_read_b32 v214, v255 offset:3584\n" \
"ds_read_b32 v215, v255 offset:3840\n" \
"ds_read_b32 v216, v255 offset:4096\n" \
"ds_read_b32 v217, v255 offset:4352\n" \
"ds_read_b32 v218, v255 offset:4608\n" \
"ds_read_b32 v219, v255 offset:4864\n" \
"ds_read_b32 v220, v255 offset:5120\n" \
"ds_read_b32 v221, v255 offset:5376\n" \
"ds_read_b32 v254, v255 offset:7936\n" \
"s_waitcnt lgkmcnt(0)\n" \
"v_readlane_b32 s4, v254, 0\n" \
"v_readlane_b32 s5, v254, 1\n" \
"v_readlane_b32 s6, v254, 2\n" \
"v_readlane_b32 s7, v254, 3\n" \
"v_readlane_b32 s8, v254, 4\n" \
"v_readlane_b32 s9, v254, 5\n" \
"v_readlane_b32 s10, v254, 6\n" \
"v_readlane_b32 s11, v254, 7\n" \
"v_readlane_b32 s12, v254, 8\n" \
"v_readlane_b32 s13, v254, 9\n" \
"v_readlane_b32 s14, v254, 10\n" \
"v_readlane_b32 s15, v254, 11\n" \
"v_readlane_b32 s16, v254, 12\n" \
"v_readlane_b32 s17, v254, 13\n" \
"v_readlane_b32 s18, v254, 14\n" \
"v_readlane_b32 s19, v254, 15\n" \
"v_readlane_b32 s20, v254, 16\n" \
"v_readlane_b32 s21, v254, 17\n" \
"v_readlane_b32 s22, v254, 18\n" \
"v_readlane_b32 s23, v254, 19\n" \
"v_readlane_b32 s24, v254, 20\n" \
"v_readlane_b32 s25, v254, 21\n" \
"v_readlane_b32 s26, v254, 22\n" \
"v_readlane_b32 s27, v254, 23\n" \
"v_readlane_b32 s28, v254, 24\n" \
"v_readlane_b32 s29, v254, 25\n" \
"v_readlane_b32 s30, v254, 26\n" \
"v_readlane_b32 s31, v254, 27\n" \
"v_readlane_b32 s32, v254, 28\n" \
"v_readlane_b32 s33, v254, 29\n" \
"v_readlane_b32 s34, v254, 30\n" \
"v_readlane_b32 s35, v254, 31\n" \
"v_readlane_b32 s36, v254, 32\n" \
"v_readlane_b32 s37, v254, 33\n" \
"v_readlane_b32 s38, v254, 34\n" \
"v_readlane_b32 s39, v254, 35\n" \
"s_mov_b32 s40, 0\n" \
"s_mov_b32 s51, 0\n" \
"s_nop 0\n" \
"v_mov_b32 v252, s38\n" \
"s_nop 4\n" \
"v_mov_b32 v100, -1\n" \
"v_mov_b32 v101, -1\n" \
"v_mov_b32 v102, -1\n" \
"v_mov_b32 v103, -1\n" \
"s_mov_b32 s35, -2\n" \
"s_mov_b32 s2, 0\n" \
"s_mov_b32 s3, -1\n" \
"v_writelane_b32 v251, s6, 0\n" \
"v_writelane_b32 v251, s10, 1\n" \
"v_writelane_b32 v251, s14, 2\n" \
"v_writelane_b32 v251, s18, 3\n" \
"s_mov_b32 s6, 0\n" \
"s_mov_b32 s10, 0\n" \
"s_mov_b32 s14, 0\n" \
"s_mov_b32 s18, 0\n" \
"s_mov_b32 s40, -1\n" \
"s_branch .LX2_pre\n" \
".LX2_wet:\n" \
"s_waitcnt vmcnt(0)\n" \
"v_readlane_b32 s6, v251, 0\n" \
"v_readlane_b32 s10, v251, 1\n" \
"v_readlane_b32 s14, v251, 2\n" \
"v_readlane_b32 s18, v251, 3\n" \
"s_nop 4\n" \
"ds_read_b32 v232, v255 offset:7424\n" \
"ds_read_b32 v233, v255 offset:7680\n" \
"v_lshrrev_b32 v234, 4, v201\n" \
"v_and_b32 v234, 3, v234\n" \
"v_subrev_u32 v230, 512, v205\n" \
"s_add_u32 s43, s30, 1\n" \
"s_mov_b32 s50, 0\n" \
".LX2_w103l:\n" \
"buffer_load_dword v240, v230, s[4:7], 0 offen sc1\n" \
"s_waitcnt vmcnt(0)\n" \
".LX2_w103c:\n" \
"v_subrev_u32 v241, s43, v240\n" \
"v_cmp_gt_u32 vcc, 0x100, v241\n" \
"s_cmp_eq_u64 vcc, -1\n" \
"s_cbranch_scc1 .LX2_w103d\n" \
"s_sleep 2\n" \
"s_add_u32 s50, s50, 1\n" \
"s_cmp_lt_u32 s50, 0x20000\n" \
"s_cbranch_scc1 .LX2_w103l\n" \
"s_branch .LX2_end\n" \
".LX2_w103d:\n" \
"v_lshl_or_b32 v241, v241, 2, v234\n" \
"s_nop 1\n" \
"v_min_u32_dpp v241, v241, v241 row_ror:4 row_mask:0xf bank_mask:0xf\n" \
"s_nop 1\n" \
"v_min_u32_dpp v241, v241, v241 row_ror:8 row_mask:0xf bank_mask:0xf\n" \
"s_nop 1\n" \
"v_min_u32_dpp v241, v241, v241 quad_perm:[1,0,3,2] row_mask:0xf bank_mask:0xf\n" \
"s_nop 1\n" \
"v_min_u32_dpp v241, v241, v241 quad_perm:[2,3,0,1] row_mask:0xf bank_mask:0xf\n" \
"s_nop 0\n" \
"v_readlane_b32 s31, v241, 0\n" \
"v_readlane_b32 s43, v241, 16\n" \
"s_min_u32 s31, s31, s43\n" \
"v_readlane_b32 s43, v241, 32\n" \
"s_min_u32 s31, s31, s43\n" \
"v_readlane_b32 s43, v241, 48\n" \
"s_min_u32 s31, s31, s43\n" \
"v_subrev_u32 v230, 256, v205\n" \
"s_add_u32 s43, s30, 1\n" \
"s_mov_b32 s50, 0\n" \
".LX2_w104l:\n" \
"buffer_load_dword v240, v230, s[4:7], 0 offen sc1\n" \
"s_waitcnt vmcnt(0)\n" \
".LX2_w104c:\n" \
"v_subrev_u32 v241, s43, v240\n" \
"v_cmp_gt_u32 vcc, 0x100, v241\n" \
"s_cmp_eq_u64 vcc, -1\n" \
"s_cbranch_scc1 .LX2_w104d\n" \
"s_sleep 2\n" \
"s_add_u32 s50, s50, 1\n" \
"s_cmp_lt_u32 s50, 0x20000\n" \
"s_cbranch_scc1 .LX2_w104l\n" \
"s_branch .LX2_end\n" \
".LX2_w104d:\n" \
"v_lshl_or_b32 v241, v241, 2, v234\n" \
"s_nop 1\n" \
"v_min_u32_dpp v241, v241, v241 row_ror:4 row_mask:0xf bank_mask:0xf\n" \
"s_nop 1\n" \
"v_min_u32_dpp v241, v241, v241 row_ror:8 row_mask:0xf bank_mask:0xf\n" \
"s_nop 1\n" \
"v_min_u32_dpp v241, v241, v241 quad_perm:[1,0,3,2] row_mask:0xf bank_mask:0xf\n" \
"s_nop 1\n" \
"v_min_u32_dpp v241, v241, v241 quad_perm:[2,3,0,1] row_mask:0xf bank_mask:0xf\n" \
"s_nop 0\n" \
"v_readlane_b32 s32, v241, 0\n" \
"v_readlane_b32 s43, v241, 16\n" \
"s_min_u32 s32, s32, s43\n" \
"v_readlane_b32 s43, v241, 32\n" \
"s_min_u32 s32, s32, s43\n" \
"v_readlane_b32 s43, v241, 48\n" \
"s_min_u32 s32, s32, s43\n" \
"s_xor_b32 s29, s31, s32\n" \
"s_and_b32 s29, s29, 3\n" \
"s_lshl_b32 s29, s29, 13\n" \
"s_bitset1_b32 s29, 21\n" \
"s_and_b32 s31, s31, 3\n" \
"s_lshl_b32 s31, s31, 13\n" \
"s_bitcmp1_b32 s39, 0\n" \
"s_cselect_b32 s43, s29, 0\n" \
"s_xor_b32 s31, s31, s43\n" \
"s_xor_b32 s32, s31, 0x4000\n" \
"s_mov_b32 s39, 0\n" \
"v_mov_b32 v224, -1\n" \
"v_mov_b32 v225, -1\n" \
"v_mov_b32 v226, -1\n" \
"v_mov_b32 v227, -1\n" \
"s_waitcnt lgkmcnt(0)\n" \
"s_add_u32 s50, s31, 0\n" \
"buffer_store_dwordx4 v[224:227], v232, s[4:7], s50 offen sc1\n" \
"s_xor_b32 s43, s50, s29\n" \
"buffer_store_dwordx4 v[224:227], v232, s[4:7], s43 offen sc1\n" \
"s_add_u32 s50, s32, 524288\n" \
"buffer_store_dwordx4 v[224:227], v232, s[4:7], s50 offen sc1\n" \
"s_xor_b32 s43, s50, s29\n" \
"buffer_store_dwordx4 v[224:227], v232, s[4:7], s43 offen sc1\n" \
"s_add_u32 s50, s31, 1048576\n" \
"buffer_store_dwordx4 v[224:227], v232, s[4:7], s50 offen sc1\n" \
"s_xor_b32 s43, s50, s29\n" \
"buffer_store_dwordx4 v[224:227], v232, s[4:7], s43 offen sc1\n" \
"s_add_u32 s50, s32, 1572864\n" \
"buffer_store_dwordx4 v[224:227], v232, s[4:7], s50 offen sc1\n" \
"s_xor_b32 s43, s50, s29\n" \
"buffer_store_dwordx4 v[224:227], v232, s[4:7], s43 offen sc1\n" \
"s_waitcnt vmcnt(0)\n" \
"s_barrier\n" \
"s_add_u32 s43, s30, s40\n" \
"s_add_u32 s43, s43, 1\n" \
"v_mov_b32 v241, s43\n" \
"buffer_store_dword v241, v233, s[4:7], 0 offen sc1\n" \
"s_add_u32 s43, s30, 1\n" \
"s_mov_b32 s50, 0\n" \
".LX2_w105l:\n" \
"buffer_load_dword v240, v204, s[4:7], 0 offen sc1\n" \
"s_waitcnt vmcnt(0)\n" \
".LX2_w105c:\n" \
"v_subrev_u32 v241, s43, v240\n" \
"v_cmp_gt_u32 vcc, 0x100, v241\n" \
"s_cmp_eq_u64 vcc, -1\n" \
"s_cbranch_scc1 .LX2_w105d\n" \
"s_sleep 2\n" \
"s_add_u32 s50, s50, 1\n" \
"s_cmp_lt_u32 s50, 0x20000\n" \
"s_cbranch_scc1 .LX2_w105l\n" \
"s_branch .LX2_end\n" \
".LX2_w105d:\n" \
"v_readlane_b32 s43, v202, 0\n" \
"s_nop 4\n" \
"buffer_load_dwordx4 a[0:3], v201, s[8:11], s43 offen sc1\n" \
"buffer_load_dwordx4 a[4:7], v201, s[8:11], s43 offen offset:1024 sc1\n" \
"v_readlane_b32 s43, v202, 7\n" \
"s_nop 4\n" \
"buffer_load_dwordx4 a[56:59], v201, s[8:11], s43 offen sc1\n" \
"buffer_load_dwordx4 a[60:63], v201, s[8:11], s43 offen offset:1024 sc1\n" \
"v_readlane_b32 s43, v202, 14\n" \
"s_nop 4\n" \
"buffer_load_dwordx4 a[112:115], v201, s[8:11], s43 offen sc1\n" \
"buffer_load_dwordx4 a[116:119], v201, s[8:11], s43 offen offset:1024 sc1\n" \
"v_readlane_b32 s43, v202, 21\n" \
"s_nop 4\n" \
"buffer_load_dwordx4 a[168:171], v201, s[8:11], s43 offen sc1\n" \
"buffer_load_dwordx4 a[172:175], v201, s[8:11], s43 offen offset:1024 sc1\n" \
"v_readlane_b32 s43, v202, 28\n" \
"s_nop 4\n" \
"buffer_load_dwordx4 a[224:227], v201, s[8:11], s43 offen sc1\n" \
"buffer_load_dwordx4 a[228:231], v201, s[8:11], s43 offen offset:1024 sc1\n" \
"v_readlane_b32 s43, v202, 35\n" \
"s_nop 4\n" \
"buffer_load_dwordx4 v[128:131], v201, s[8:11], s43 offen sc1\n" \
"buffer_load_dwordx4 v[132:135], v201, s[8:11], s43 offen offset:1024 sc1\n" \
"s_add_u32 s43, s30, 2\n" \
"s_mov_b32 s50, 0\n" \
"s_branch .LX2_w106c\n" \
".LX2_w106l:\n" \
"buffer_load_dword v240, v204, s[4:7], 0 offen sc1\n" \
"s_waitcnt vmcnt(0)\n" \
".LX2_w106c:\n" \
"v_subrev_u32 v241, s43, v240\n" \
"v_cmp_gt_u32 vcc, 0x100, v241\n" \
"s_cmp_eq_u64 vcc, -1\n" \
"s_cbranch_scc1 .LX2_w106d\n" \
"s_sleep 2\n" \
"s_add_u32 s50, s50, 1\n" \
"s_cmp_lt_u32 s50, 0x20000\n" \
"s_cbranch_scc1 .LX2_w106l\n" \
"s_branch .LX2_end\n" \
".LX2_w106d:\n" \
"v_readlane_b32 s43, v202, 4\n" \
"s_nop 4\n" \
"buffer_load_dwordx4 a[32:35], v201, s[8:11], s43 offen sc1\n" \
"buffer_load_dwordx4 a[36:39], v201, s[8:11], s43 offen offset:1024 sc1\n" \
"v_readlane_b32 s43, v202, 11\n" \
"s_nop 4\n" \
"buffer_load_dwordx4 a[88:91], v201, s[8:11], s43 offen sc1\n" \
"buffer_load_dwordx4 a[92:95], v201, s[8:11], s43 offen offset:1024 sc1\n" \
"v_readlane_b32 s43, v202, 18\n" \
"s_nop 4\n" \
"buffer_load_dwordx4 a[144:147], v201, s[8:11], s43 offen sc1\n" \
"buffer_load_dwordx4 a[148:151], v201, s[8:11], s43 offen offset:1024 sc1\n" \
"v_readlane_b32 s43, v202, 25\n" \
"s_nop 4\n" \
"buffer_load_dwordx4 a[200:203], v201, s[8:11], s43 offen sc1\n" \
"buffer_load_dwordx4 a[204:207], v201, s[8:11], s43 offen offset:1024 sc1\n" \
"v_readlane_b32 s43, v202, 32\n" \
"s_nop 4\n" \
"buffer_load_dwordx4 v[104:107], v201, s[8:11], s43 offen sc1\n" \
"buffer_load_dwordx4 v[108:111], v201, s[8:11], s43 offen offset:1024 sc1\n" \
"v_readlane_b32 s43, v202, 39\n" \
"s_nop 4\n" \
"buffer_load_dwordx4 v[160:163], v201, s[8:11], s43 offen sc1\n" \
"buffer_load_dwordx4 v[164:167], v201, s[8:11], s43 offen offset:1024 sc1\n" \
"s_add_u32 s43, s30, 3\n" \
"s_mov_b32 s50, 0\n" \
"s_branch .LX2_w107c\n" \
".LX2_w107l:\n" \
"buffer_load_dword v240, v204, s[4:7], 0 offen sc1\n" \
"s_waitcnt vmcnt(0)\n" \
".LX2_w107c:\n" \
"v_subrev_u32 v241, s43, v240\n" \
"v_cmp_gt_u32 vcc, 0x100, v241\n" \
"s_cmp_eq_u64 vcc, -1\n" \
"s_cbranch_scc1 .LX2_w107d\n" \
"s_sleep 2\n" \
"s_add_u32 s50, s50, 1\n" \
"s_cmp_lt_u32 s50, 0x20000\n" \
"s_cbranch_scc1 .LX2_w107l\n" \
"s_branch .LX2_end\n" \
".LX2_w107d:\n" \
"v_readlane_b32 s43, v202, 1\n" \
"s_nop 4\n" \
"buffer_load_dwordx4 a[8:11], v201, s[8:11], s43 offen sc1\n" \
"buffer_load_dwordx4 a[12:15], v201, s[8:11], s43 offen offset:1024 sc1\n" \
"v_readlane_b32 s43, v202, 8\n" \
"s_nop 4\n" \
"buffer_load_dwordx4 a[64:67], v201, s[8:11], s43 offen sc1\n" \
"buffer_load_dwordx4 a[68:71], v201, s[8:11], s43 offen offset:1024 sc1\n" \
"v_readlane_b32 s43, v202, 15\n" \
"s_nop 4\n" \
"buffer_load_dwordx4 a[120:123], v201, s[8:11], s43 offen sc1\n" \
"buffer_load_dwordx4 a[124:127], v201, s[8:11], s43 offen offset:1024 sc1\n" \
"v_readlane_b32 s43, v202, 22\n" \
"s_nop 4\n" \
"buffer_load_dwordx4 a[176:179], v201, s[8:11], s43 offen sc1\n" \
"buffer_load_dwordx4 a[180:183], v201, s[8:11], s43 offen offset:1024 sc1\n" \
"v_readlane_b32 s43, v202, 29\n" \
"s_nop 4\n" \
"buffer_load_dwordx4 a[232:235], v201, s[8:11], s43 offen sc1\n" \
"buffer_load_dwordx4 a[236:239], v201, s[8:11], s43 offen offset:1024 sc1\n" \
"v_readlane_b32 s43, v202, 36\n" \
"s_nop 4\n" \
"buffer_load_dwordx4 v[136:139], v201, s[8:11], s43 offen sc1\n" \
"buffer_load_dwordx4 v[140:143], v201, s[8:11], s43 offen offset:1024 sc1\n" \
"s_add_u32 s43, s30, 4\n" \
"s_mov_b32 s50, 0\n" \
"s_branch .LX2_w108c\n" \
".LX2_w108l:\n" \
"buffer_load_dword v240, v204, s[4:7], 0 offen sc1\n" \
"s_waitcnt vmcnt(0)\n" \
".LX2_w108c:\n" \
"v_subrev_u32 v241, s43, v240\n" \
"v_cmp_gt_u32 vcc, 0x100, v241\n" \
"s_cmp_eq_u64 vcc, -1\n" \
"s_cbranch_scc1 .LX2_w108d\n" \
"s_sleep 2\n" \
"s_add_u32 s50, s50, 1\n" \
"s_cmp_lt_u32 s50, 0x20000\n" \
"s_cbranch_scc1 .LX2_w108l\n" \
"s_branch .LX2_end\n" \
".LX2_w108d:\n" \
"v_readlane_b32 s43, v202, 5\n" \
"s_nop 4\n" \
"buffer_load_dwordx4 a[40:43], v201, s[8:11], s43 offen sc1\n" \
"buffer_load_dwordx4 a[44:47], v201, s[8:11], s43 offen offset:1024 sc1\n" \
"v_readlane_b32 s43, v202, 12\n" \
"s_nop 4\n" \
"buffer_load_dwordx4 a[96:99], v201, s[8:11], s43 offen sc1\n" \
"buffer_load_dwordx4 a[100:103], v201, s[8:11], s43 offen offset:1024 sc1\n" \
"v_readlane_b32 s43, v202, 19\n" \
"s_nop 4\n" \
"buffer_load_dwordx4 a[152:155], v201, s[8:11], s43 offen sc1\n" \
"buffer_load_dwordx4 a[156:159], v201, s[8:11], s43 offen offset:1024 sc1\n" \
"v_readlane_b32 s43, v202, 26\n" \
"s_nop 4\n" \
"buffer_load_dwordx4 a[208:211], v201, s[8:11], s43 offen sc1\n" \
"buffer_load_dwordx4 a[212:215], v201, s[8:11], s43 offen offset:1024 sc1\n" \
"v_readlane_b32 s43, v202, 33\n" \
"s_nop 4\n" \
"buffer_load_dwordx4 v[112:115], v201, s[8:11], s43 offen sc1\n" \
"buffer_load_dwordx4 v[116:119], v201, s[8:11], s43 offen offset:1024 sc1\n" \
"v_readlane_b32 s43, v202, 40\n" \
"s_nop 4\n" \
"buffer_load_dwordx4 v[168:171], v201, s[8:11], s43 offen sc1\n" \
"buffer_load_dwordx4 v[172:175], v201, s[8:11], s43 offen offset:1024 sc1\n" \
"s_add_u32 s43, s30, 5\n" \
"s_mov_b32 s50, 0\n" \
"s_branch .LX2_w109c\n" \
".LX2_w109l:\n" \
"buffer_load_dword v240, v204, s[4:7], 0 offen sc1\n" \
"s_waitcnt vmcnt(0)\n" \
".LX2_w109c:\n" \
"v_subrev_u32 v241, s43, v240\n" \
"v_cmp_gt_u32 vcc, 0x100, v241\n" \
"s_cmp_eq_u64 vcc, -1\n" \
"s_cbranch_scc1 .LX2_w109d\n" \
"s_sleep 2\n" \
"s_add_u32 s50, s50, 1\n" \
"s_cmp_lt_u32 s50, 0x20000\n" \
"s_cbranch_scc1 .LX2_w109l\n" \
"s_branch .LX2_end\n" \
".LX2_w109d:\n" \
"v_readlane_b32 s43, v202, 2\n" \
"s_nop 4\n" \
"buffer_load_dwordx4 a[16:19], v201, s[8:11], s43 offen sc1\n" \
"buffer_load_dwordx4 a[20:23], v201, s[8:11], s43 offen offset:1024 sc1\n" \
"v_readlane_b32 s43, v202, 9\n" \
"s_nop 4\n" \
"buffer_load_dwordx4 a[72:75], v201, s[8:11], s43 offen sc1\n" \
"buffer_load_dwordx4 a[76:79], v201, s[8:11], s43 offen offset:1024 sc1\n" \
"v_readlane_b32 s43, v202, 16\n" \
"s_nop 4\n" \
"buffer_load_dwordx4 a[128:131], v201, s[8:11], s43 offen sc1\n" \
"buffer_load_dwordx4 a[132:135], v201, s[8:11], s43 offen offset:1024 sc1\n" \
"v_readlane_b32 s43, v202, 23\n" \
"s_nop 4\n" \
"buffer_load_dwordx4 a[184:187], v201, s[8:11], s43 offen sc1\n" \
"buffer_load_dwordx4 a[188:191], v201, s[8:11], s43 offen offset:1024 sc1\n" \
"v_readlane_b32 s43, v202, 30\n" \
"s_nop 4\n" \
"buffer_load_dwordx4 a[240:243], v201, s[8:11], s43 offen sc1\n" \
"buffer_load_dwordx4 a[244:247], v201, s[8:11], s43 offen offset:1024 sc1\n" \
"v_readlane_b32 s43, v202, 37\n" \
"s_nop 4\n" \
"buffer_load_dwordx4 v[144:147], v201, s[8:11], s43 offen sc1\n" \
"buffer_load_dwordx4 v[148:151], v201, s[8:11], s43 offen offset:1024 sc1\n" \
"s_add_u32 s43, s30, 6\n" \
"s_mov_b32 s50, 0\n" \
"s_branch .LX2_w110c\n" \
".LX2_w110l:\n" \
"buffer_load_dword v240, v204, s[4:7], 0 offen sc1\n" \
"s_waitcnt vmcnt(0)\n" \
".LX2_w110c:\n" \
"v_subrev_u32 v241, s43, v240\n" \
"v_cmp_gt_u32 vcc, 0x100, v241\n" \
"s_cmp_eq_u64 vcc, -1\n" \
"s_cbranch_scc1 .LX2_w110d\n" \
"s_sleep 2\n" \
"s_add_u32 s50, s50, 1\n" \
"s_cmp_lt_u32 s50, 0x20000\n" \
"s_cbranch_scc1 .LX2_w110l\n" \
"s_branch .LX2_end\n" \
".LX2_w110d:\n" \
"v_readlane_b32 s43, v202, 6\n" \
"s_nop 4\n" \
"buffer_load_dwordx4 a[48:51], v201, s[8:11], s43 offen sc1\n" \
"buffer_load_dwordx4 a[52:55], v201, s[8:11], s43 offen offset:1024 sc1\n" \
"v_readlane_b32 s43, v202, 13\n" \
"s_nop 4\n" \
"buffer_load_dwordx4 a[104:107], v201, s[8:11], s43 offen sc1\n" \
"buffer_load_dwordx4 a[108:111], v201, s[8:11], s43 offen offset:1024 sc1\n" \
"v_readlane_b32 s43, v202, 20\n" \
"s_nop 4\n" \
"buffer_load_dwordx4 a[160:163], v201, s[8:11], s43 offen sc1\n" \
"buffer_load_dwordx4 a[164:167], v201, s[8:11], s43 offen offset:1024 sc1\n" \
"v_readlane_b32 s43, v202, 27\n" \
"s_nop 4\n" \
"buffer_load_dwordx4 a[216:219], v201, s[8:11], s43 offen sc1\n" \
"buffer_load_dwordx4 a[220:223], v201, s[8:11], s43 offen offset:1024 sc1\n" \
"v_readlane_b32 s43, v202, 34\n" \
"s_nop 4\n" \
"buffer_load_dwordx4 v[120:123], v201, s[8:11], s43 offen sc1\n" \
"buffer_load_dwordx4 v[124:127], v201, s[8:11], s43 offen offset:1024 sc1\n" \
"v_readlane_b32 s43, v202, 41\n" \
"s_nop 4\n" \
"buffer_load_dwordx4 v[176:179], v201, s[8:11], s43 offen sc1\n" \
"buffer_load_dwordx4 v[180:183], v201, s[8:11], s43 offen offset:1024 sc1\n" \
"s_add_u32 s43, s30, 7\n" \
"s_mov_b32 s50, 0\n" \
"s_branch .LX2_w111c\n" \
".LX2_w111l:\n" \
"buffer_load_dword v240, v204, s[4:7], 0 offen sc1\n" \
"s_waitcnt vmcnt(0)\n" \
".LX2_w111c:\n" \
"v_subrev_u32 v241, s43, v240\n" \
"v_cmp_gt_u32 vcc, 0x100, v241\n" \
"s_cmp_eq_u64 vcc, -1\n" \
"s_cbranch_scc1 .LX2_w111d\n" \
"s_sleep 2\n" \
"s_add_u32 s50, s50, 1\n" \
"s_cmp_lt_u32 s50, 0x20000\n" \
"s_cbranch_scc1 .LX2_w111l\n" \
"s_branch .LX2_end\n" \
".LX2_w111d:\n" \
"v_readlane_b32 s43, v202, 3\n" \
"s_nop 4\n" \
"buffer_load_dwordx4 a[24:27], v201, s[8:11], s43 offen sc1\n" \
"buffer_load_dwordx4 a[28:31], v201, s[8:11], s43 offen offset:1024 sc1\n" \
"v_readlane_b32 s43, v202, 10\n" \
"s_nop 4\n" \
"buffer_load_dwordx4 a[80:83], v201, s[8:11], s43 offen sc1\n" \
"buffer_load_dwordx4 a[84:87], v201, s[8:11], s43 offen offset:1024 sc1\n" \
"v_readlane_b32 s43, v202, 17\n" \
"s_nop 4\n" \
"buffer_load_dwordx4 a[136:139], v201, s[8:11], s43 offen sc1\n" \
"buffer_load_dwordx4 a[140:143], v201, s[8:11], s43 offen offset:1024 sc1\n" \
"v_readlane_b32 s43, v202, 24\n" \
"s_nop 4\n" \
"buffer_load_dwordx4 a[192:195], v201, s[8:11], s43 offen sc1\n" \
"buffer_load_dwordx4 a[196:199], v201, s[8:11], s43 offen offset:1024 sc1\n" \
"v_readlane_b32 s43, v202, 31\n" \
"s_nop 4\n" \
"buffer_load_dwordx4 a[248:251], v201, s[8:11], s43 offen sc1\n" \
"buffer_load_dwordx4 a[252:255], v201, s[8:11], s43 offen offset:1024 sc1\n" \
"v_readlane_b32 s43, v202, 38\n" \
"s_nop 4\n" \
"buffer_load_dwordx4 v[152:155], v201, s[8:11], s43 offen sc1\n" \
"buffer_load_dwordx4 v[156:159], v201, s[8:11], s43 offen offset:1024 sc1\n" \
"s_add_u32 s43, s30, 1\n" \
"s_mov_b32 s50, 0\n" \
".LX2_w112l:\n" \
"buffer_load_dword v240, v205, s[4:7], 0 offen sc1\n" \
"s_waitcnt vmcnt(0)\n" \
".LX2_w112c:\n" \
"v_subrev_u32 v241, s43, v240\n" \
"v_cmp_gt_u32 vcc, 0x100, v241\n" \
"s_cmp_eq_u64 vcc, -1\n" \
"s_cbranch_scc1 .LX2_w112d\n" \
"s_sleep 2\n" \
"s_add_u32 s50, s50, 1\n" \
"s_cmp_lt_u32 s50, 0x20000\n" \
"s_cbranch_scc1 .LX2_w112l\n" \
"s_branch .LX2_end\n" \
".LX2_w112d:\n" \
"s_add_u32 s43, s30, 1\n" \
"s_mov_b32 s50, 0\n" \
".LX2_w113l:\n" \
"buffer_load_dword v240, v206, s[4:7], 0 offen sc1\n" \
"s_waitcnt vmcnt(0)\n" \
".LX2_w113c:\n" \
"v_subrev_u32 v241, s43, v240\n" \
"v_cmp_gt_u32 vcc, 0x100, v241\n" \
"s_cmp_eq_u64 vcc, -1\n" \
"s_cbranch_scc1 .LX2_w113d\n" \
"s_sleep 2\n" \
"s_add_u32 s50, s50, 1\n" \
"s_cmp_lt_u32 s50, 0x20000\n" \
"s_cbranch_scc1 .LX2_w113l\n" \
"s_branch .LX2_end\n" \
".LX2_w113d:\n" \
"s_add_u32 s43, s30, 8\n" \
"s_mov_b32 s50, 0\n" \
".LX2_w114l:\n" \
"buffer_load_dword v240, v215, s[4:7], 0 offen sc1\n" \
"s_waitcnt vmcnt(0)\n" \
".LX2_w114c:\n" \
"v_subrev_u32 v241, s43, v240\n" \
"v_cmp_gt_u32 vcc, 0x100, v241\n" \
"s_cmp_eq_u64 vcc, -1\n" \
"s_cbranch_scc1 .LX2_w114d\n" \
"s_sleep 2\n" \
"s_add_u32 s50, s50, 1\n" \
"s_cmp_lt_u32 s50, 0x20000\n" \
"s_cbranch_scc1 .LX2_w114l\n" \
"s_branch .LX2_end\n" \
".LX2_w114d:\n" \
"s_add_u32 s50, s40, 0\n" \
"s_mul_i32 s50, s50, 458752\n" \
"s_add_u32 s50, s50, s34\n" \
"buffer_load_dwordx4 v[28:31], v201, s[12:15], s50 offen sc1\n" \
"buffer_load_dwordx4 v[32:35], v201, s[12:15], s50 offen offset:1024 sc1\n" \
"buffer_load_dwordx4 v[36:39], v201, s[12:15], s50 offen offset:2048 sc1\n" \
"buffer_load_dwordx4 v[40:43], v201, s[12:15], s50 offen offset:3072 sc1\n" \
"s_add_u32 s43, s30, 9\n" \
"s_mov_b32 s50, 0\n" \
".LX2_w115l:\n" \
"buffer_load_dword v240, v215, s[4:7], 0 offen sc1\n" \
"s_waitcnt vmcnt(0)\n" \
".LX2_w115c:\n" \
"v_subrev_u32 v241, s43, v240\n" \
"v_cmp_gt_u32 vcc, 0x100, v241\n" \
"s_cmp_eq_u64 vcc, -1\n" \
"s_cbranch_scc1 .LX2_w115d\n" \
"s_sleep 2\n" \
"s_add_u32 s50, s50, 1\n" \
"s_cmp_lt_u32 s50, 0x20000\n" \
"s_cbranch_scc1 .LX2_w115l\n" \
"s_branch .LX2_end\n" \
".LX2_w115d:\n" \
".LX2_pre:\n" \
"s_barrier\n" \
".LX2_loop:\n" \
"s_add_u32 s43, s40, 2\n" \
"s_and_b32 s43, s43, 3\n" \
"s_bitcmp1_b32 s43, 0\n" \
"s_cselect_b32 s49, s32, s31\n" \
"s_lshl_b32 s43, s43, 19\n" \
"s_add_u32 s49, s49, s43\n" \
"ds_read_b128 v[0:3], v216 offset:0\n" \
"ds_read_b128 v[4:7], v216 offset:1024\n" \
"ds_read_b128 v[8:11], v216 offset:2048\n" \
"ds_read_b128 v[12:15], v216 offset:3072\n" \
"ds_read_b128 v[16:19], v216 offset:4096\n" \
"ds_read_b128 v[20:23], v216 offset:5120\n" \
"ds_read_b128 v[24:27], v216 offset:6144\n" \
"v_mov_b32 v60, 0\n" \
"v_mov_b32 v61, 0\n" \
"v_mov_b32 v62, 0\n" \
"v_mov_b32 v63, 0\n" \
"v_mov_b32 v64, 0\n" \
"v_mov_b32 v65, 0\n" \
"v_mov_b32 v66, 0\n" \
"v_mov_b32 v67, 0\n" \
"v_mov_b32 v68, 0\n" \
"v_mov_b32 v69, 0\n" \
"v_mov_b32 v70, 0\n" \
"v_mov_b32 v71, 0\n" \
"v_mov_b32 v72, 0\n" \
"v_mov_b32 v73, 0\n" \
"v_mov_b32 v74, 0\n" \
"v_mov_b32 v75, 0\n" \
"v_mov_b32 v76, 0\n" \
"v_mov_b32 v77, 0\n" \
"v_mov_b32 v78, 0\n" \
"v_mov_b32 v79, 0\n" \
"v_mov_b32 v80, 0\n" \
"v_mov_b32 v81, 0\n" \
"v_mov_b32 v82, 0\n" \
"v_mov_b32 v83, 0\n" \
"s_nop 1\n" \
"s_waitcnt lgkmcnt(6)\n" \
"v_smfmac_f32_16x16x64_bf16 v[60:63], v[0:3], a[0:7], v200\n" \
"v_smfmac_f32_16x16x64_bf16 v[64:67], v[0:3], a[56:63], v200\n" \
"v_smfmac_f32_16x16x64_bf16 v[68:71], v[0:3], a[112:119], v200\n" \
"v_smfmac_f32_16x16x64_bf16 v[72:75], v[0:3], a[168:175], v200\n" \
"v_smfmac_f32_16x16x64_bf16 v[76:79], v[0:3], a[224:231], v200\n" \
"v_smfmac_f32_16x16x64_bf16 v[80:83], v[0:3], v[128:135], v200\n" \
"s_waitcnt lgkmcnt(5)\n" \
"v_smfmac_f32_16x16x64_bf16 v[60:63], v[4:7], a[8:15], v200\n" \
"v_smfmac_f32_16x16x64_bf16 v[64:67], v[4:7], a[64:71], v200\n" \
"v_smfmac_f32_16x16x64_bf16 v[68:71], v[4:7], a[120:127], v200\n" \
"v_smfmac_f32_16x16x64_bf16 v[72:75], v[4:7], a[176:183], v200\n" \
"v_smfmac_f32_16x16x64_bf16 v[76:79], v[4:7], a[232:239], v200\n" \
"v_smfmac_f32_16x16x64_bf16 v[80:83], v[4:7], v[136:143], v200\n" \
"s_waitcnt lgkmcnt(4)\n" \
"v_smfmac_f32_16x16x64_bf16 v[60:63], v[8:11], a[16:23], v200\n" \
"v_smfmac_f32_16x16x64_bf16 v[64:67], v[8:11], a[72:79], v200\n" \
"v_smfmac_f32_16x16x64_bf16 v[68:71], v[8:11], a[128:135], v200\n" \
"v_smfmac_f32_16x16x64_bf16 v[72:75], v[8:11], a[184:191], v200\n" \
"v_smfmac_f32_16x16x64_bf16 v[76:79], v[8:11], a[240:247], v200\n" \
"v_smfmac_f32_16x16x64_bf16 v[80:83], v[8:11], v[144:151], v200\n" \
"s_waitcnt lgkmcnt(3)\n" \
"v_smfmac_f32_16x16x64_bf16 v[60:63], v[12:15], a[24:31], v200\n" \
"v_smfmac_f32_16x16x64_bf16 v[64:67], v[12:15], a[80:87], v200\n" \
"v_smfmac_f32_16x16x64_bf16 v[68:71], v[12:15], a[136:143], v200\n" \
"v_smfmac_f32_16x16x64_bf16 v[72:75], v[12:15], a[192:199], v200\n" \
"v_smfmac_f32_16x16x64_bf16 v[76:79], v[12:15], a[248:255], v200\n" \
"v_smfmac_f32_16x16x64_bf16 v[80:83], v[12:15], v[152:159], v200\n" \
"s_waitcnt lgkmcnt(2)\n" \
"v_smfmac_f32_16x16x64_bf16 v[60:63], v[16:19], a[32:39], v200\n" \
"v_smfmac_f32_16x16x64_bf16 v[64:67], v[16:19], a[88:95], v200\n" \
"v_smfmac_f32_16x16x64_bf16 v[68:71], v[16:19], a[144:151], v200\n" \
"v_smfmac_f32_16x16x64_bf16 v[72:75], v[16:19], a[200:207], v200\n" \
"s_barrier\n" \
"s_add_u32 s50, s40, 1\n" \
"s_mul_i32 s50, s50, 458752\n" \
"s_add_u32 s50, s50, s34\n" \
"buffer_load_dwordx4 v[44:47], v201, s[12:15], s50 offen sc1\n" \
"buffer_load_dwordx4 v[48:51], v201, s[12:15], s50 offen offset:1024 sc1\n" \
"buffer_load_dwordx4 v[52:55], v201, s[12:15], s50 offen offset:2048 sc1\n" \
"buffer_load_dwordx4 v[56:59], v201, s[12:15], s50 offen offset:3072 sc1\n" \
"buffer_load_dword v240, v215, s[4:7], 0 offen sc1\n" \
"v_smfmac_f32_16x16x64_bf16 v[76:79], v[16:19], v[104:111], v200\n" \
"v_smfmac_f32_16x16x64_bf16 v[80:83], v[16:19], v[160:167], v200\n" \
"s_waitcnt lgkmcnt(1)\n" \
"v_smfmac_f32_16x16x64_bf16 v[60:63], v[20:23], a[40:47], v200\n" \
"v_smfmac_f32_16x16x64_bf16 v[64:67], v[20:23], a[96:103], v200\n" \
"v_smfmac_f32_16x16x64_bf16 v[68:71], v[20:23], a[152:159], v200\n" \
"v_smfmac_f32_16x16x64_bf16 v[72:75], v[20:23], a[208:215], v200\n" \
"v_smfmac_f32_16x16x64_bf16 v[76:79], v[20:23], v[112:119], v200\n" \
"v_smfmac_f32_16x16x64_bf16 v[80:83], v[20:23], v[168:175], v200\n" \
"s_waitcnt lgkmcnt(0)\n" \
"v_smfmac_f32_16x16x64_bf16 v[60:63], v[24:27], a[48:55], v200\n" \
"v_smfmac_f32_16x16x64_bf16 v[64:67], v[24:27], a[104:111], v200\n" \
"v_smfmac_f32_16x16x64_bf16 v[68:71], v[24:27], a[160:167], v200\n" \
"v_smfmac_f32_16x16x64_bf16 v[72:75], v[24:27], a[216:223], v200\n" \
"v_smfmac_f32_16x16x64_bf16 v[76:79], v[24:27], v[120:127], v200\n" \
"v_smfmac_f32_16x16x64_bf16 v[80:83], v[24:27], v[176:183], v200\n" \
"s_nop 7\n" \
"v_permlane32_swap_b32 v60, v64\n" \
"v_permlane32_swap_b32 v61, v65\n" \
"v_permlane32_swap_b32 v62, v66\n" \
"v_permlane32_swap_b32 v63, v67\n" \
"v_add_f32 v60, v60, v64\n" \
"v_add_f32 v61, v61, v65\n" \
"v_add_f32 v62, v62, v66\n" \
"v_add_f32 v63, v63, v67\n" \
"v_cvt_pk_bf16_f32 v184, v60, v61\n" \
"v_cvt_pk_bf16_f32 v185, v62, v63\n" \
"ds_write_b64 v217, v[184:185] offset:0\n" \
"v_permlane32_swap_b32 v68, v72\n" \
"v_permlane32_swap_b32 v69, v73\n" \
"v_permlane32_swap_b32 v70, v74\n" \
"v_permlane32_swap_b32 v71, v75\n" \
"v_add_f32 v68, v68, v72\n" \
"v_add_f32 v69, v69, v73\n" \
"v_add_f32 v70, v70, v74\n" \
"v_add_f32 v71, v71, v75\n" \
"v_cvt_pk_bf16_f32 v186, v68, v69\n" \
"v_cvt_pk_bf16_f32 v187, v70, v71\n" \
"ds_write_b64 v217, v[186:187] offset:512\n" \
"v_permlane32_swap_b32 v76, v80\n" \
"v_permlane32_swap_b32 v77, v81\n" \
"v_permlane32_swap_b32 v78, v82\n" \
"v_permlane32_swap_b32 v79, v83\n" \
"v_add_f32 v76, v76, v80\n" \
"v_add_f32 v77, v77, v81\n" \
"v_add_f32 v78, v78, v82\n" \
"v_add_f32 v79, v79, v83\n" \
"v_cvt_pk_bf16_f32 v188, v76, v77\n" \
"v_cvt_pk_bf16_f32 v189, v78, v79\n" \
"ds_write_b64 v217, v[188:189] offset:1024\n" \
"ds_read_b64_tr_b16 v[0:1], v218 offset:0\n" \
"ds_read_b64_tr_b16 v[2:3], v218 offset:64\n" \
"ds_read_b64_tr_b16 v[4:5], v218 offset:1024\n" \
"ds_read_b64_tr_b16 v[6:7], v218 offset:1088\n" \
"s_add_u32 s43, s40, 0\n" \
"s_mul_i32 s43, s43, 114688\n" \
"s_waitcnt lgkmcnt(0)\n" \
"buffer_store_dwordx4 v[0:3], v219, s[16:19], s43 offen sc1\n" \
"buffer_store_dwordx4 v[4:7], v221, s[16:19], s43 offen offset:1024 sc1\n" \
"s_waitcnt vmcnt(7)\n" \
"s_barrier\n" \
"ds_read_b64_tr_b16 v[92:93], v213\n" \
"ds_read_b64_tr_b16 v[94:95], v213 offset:64\n" \
"ds_read_b32 v84, v252 offset:16\n" \
"v_mov_b32 v245, v245\n" \
"s_nop 2\n" \
"v_mov_b32 v245, v245\n" \
"s_nop 2\n" \
"v_mov_b32 v245, v245\n" \
"s_waitcnt lgkmcnt(1)\n" \
"v_mfma_f32_16x16x32_bf16 v[68:71], v[92:95], v[28:31], 0\n" \
"s_waitcnt lgkmcnt(0)\n" \
"v_xor_b32 v85, s29, v84\n" \
"v_mfma_f32_16x16x32_bf16 v[76:79], v[92:95], v[36:39], 0\n" \
"v_add_u32 v84, v84, v210\n" \
"v_add_u32 v85, v85, v210\n" \
"v_mfma_f32_16x16x32_bf16 v[72:75], v[92:95], v[32:35], 0\n" \
"s_nop 1\n" \
"v_mfma_f32_16x16x32_bf16 v[80:83], v[92:95], v[40:43], 0\n" \
"v_cvt_pk_bf16_f32 v96, v68, v69\n" \
"v_cvt_pk_bf16_f32 v97, v70, v71\n" \
"v_cvt_pk_bf16_f32 v98, v76, v77\n" \
"v_cvt_pk_bf16_f32 v99, v78, v79\n" \
"s_mov_b64 exec, s[2:3]\n" \
"v_cvt_pk_bf16_f32 v96, v72, v73\n" \
"v_cvt_pk_bf16_f32 v97, v74, v75\n" \
"s_nop 0\n" \
"v_cvt_pk_bf16_f32 v98, v80, v81\n" \
"v_cvt_pk_bf16_f32 v99, v82, v83\n" \
"s_mov_b64 exec, -1\n" \
"s_nop 1\n" \
"v_permlane16_swap_b32 v96, v98\n" \
"v_permlane16_swap_b32 v97, v99\n" \
"buffer_store_dwordx4 v[96:99], v84, s[4:7], 0 offen sc1\n" \
"s_barrier\n" \
"buffer_store_dwordx4 v[96:99], v85, s[4:7], 0 offen sc1\n" \
"s_nop 15\n" \
"s_nop 15\n" \
"s_nop 15\n" \
"s_nop 15\n" \
"s_nop 15\n" \
"buffer_store_dwordx4 v[100:103], v214, s[4:7], s49 offen sc1\n" \
"s_xor_b32 s43, s49, s29\n" \
"buffer_store_dwordx4 v[100:103], v214, s[4:7], s43 offen sc1\n" \
"s_waitcnt vmcnt(4)\n" \
"v_mov_b32 v28, v44\n" \
"v_mov_b32 v29, v45\n" \
"v_mov_b32 v30, v46\n" \
"v_mov_b32 v31, v47\n" \
"v_mov_b32 v32, v48\n" \
"v_mov_b32 v33, v49\n" \
"v_mov_b32 v34, v50\n" \
"v_mov_b32 v35, v51\n" \
"v_mov_b32 v36, v52\n" \
"v_mov_b32 v37, v53\n" \
"v_mov_b32 v38, v54\n" \
"v_mov_b32 v39, v55\n" \
"v_mov_b32 v40, v56\n" \
"v_mov_b32 v41, v57\n" \
"v_mov_b32 v42, v58\n" \
"v_mov_b32 v43, v59\n" \
"s_cmp_eq_u32 s40, -1\n" \
"s_cbranch_scc1 .LX2_nogu\n" \
"s_add_u32 s36, s40, 10\n" \
"s_min_u32 s36, s36, 71\n" \
"s_add_u32 s43, s30, s36\n" \
"s_mov_b32 s50, 0\n" \
"s_branch .LX2_w116c\n" \
".LX2_w116l:\n" \
"buffer_load_dword v240, v215, s[4:7], 0 offen sc1\n" \
"s_waitcnt vmcnt(0)\n" \
".LX2_w116c:\n" \
"v_subrev_u32 v241, s43, v240\n" \
"v_cmp_gt_u32 vcc, 0x100, v241\n" \
"s_cmp_eq_u64 vcc, -1\n" \
"s_cbranch_scc1 .LX2_w116d\n" \
"s_sleep 2\n" \
"s_add_u32 s50, s50, 1\n" \
"s_cmp_lt_u32 s50, 0x20000\n" \
"s_cbranch_scc1 .LX2_w116l\n" \
"s_branch .LX2_end\n" \
".LX2_w116d:\n" \
".LX2_nogu:\n" \
"s_add_u32 s40, s40, 1\n" \
"s_waitcnt lgkmcnt(0)\n" \
"s_barrier\n" \
"s_cmp_eq_u32 s40, 0\n" \
"s_cbranch_scc1 .LX2_wet\n" \
"s_cmp_lt_u32 s40, 63\n" \
"s_cbranch_scc1 .LX2_loop\n" \
"ds_read_b128 v[0:3], v216 offset:0\n" \
"ds_read_b128 v[4:7], v216 offset:1024\n" \
"ds_read_b128 v[8:11], v216 offset:2048\n" \
"ds_read_b128 v[12:15], v216 offset:3072\n" \
"ds_read_b128 v[16:19], v216 offset:4096\n" \
"ds_read_b128 v[20:23], v216 offset:5120\n" \
"ds_read_b128 v[24:27], v216 offset:6144\n" \
"v_mov_b32 v60, 0\n" \
"v_mov_b32 v61, 0\n" \
"v_mov_b32 v62, 0\n" \
"v_mov_b32 v63, 0\n" \
"v_mov_b32 v64, 0\n" \
"v_mov_b32 v65, 0\n" \
"v_mov_b32 v66, 0\n" \
"v_mov_b32 v67, 0\n" \
"v_mov_b32 v68, 0\n" \
"v_mov_b32 v69, 0\n" \
"v_mov_b32 v70, 0\n" \
"v_mov_b32 v71, 0\n" \
"v_mov_b32 v72, 0\n" \
"v_mov_b32 v73, 0\n" \
"v_mov_b32 v74, 0\n" \
"v_mov_b32 v75, 0\n" \
"v_mov_b32 v76, 0\n" \
"v_mov_b32 v77, 0\n" \
"v_mov_b32 v78, 0\n" \
"v_mov_b32 v79, 0\n" \
"v_mov_b32 v80, 0\n" \
"v_mov_b32 v81, 0\n" \
"v_mov_b32 v82, 0\n" \
"v_mov_b32 v83, 0\n" \
"s_nop 1\n" \
"s_waitcnt lgkmcnt(6)\n" \
"v_smfmac_f32_16x16x64_bf16 v[60:63], v[0:3], a[0:7], v200\n" \
"v_smfmac_f32_16x16x64_bf16 v[64:67], v[0:3], a[56:63], v200\n" \
"v_smfmac_f32_16x16x64_bf16 v[68:71], v[0:3], a[112:119], v200\n" \
"v_smfmac_f32_16x16x64_bf16 v[72:75], v[0:3], a[168:175], v200\n" \
"v_smfmac_f32_16x16x64_bf16 v[76:79], v[0:3], a[224:231], v200\n" \
"v_smfmac_f32_16x16x64_bf16 v[80:83], v[0:3], v[128:135], v200\n" \
"s_waitcnt lgkmcnt(5)\n" \
"v_smfmac_f32_16x16x64_bf16 v[60:63], v[4:7], a[8:15], v200\n" \
"v_smfmac_f32_16x16x64_bf16 v[64:67], v[4:7], a[64:71], v200\n" \
"v_smfmac_f32_16x16x64_bf16 v[68:71], v[4:7], a[120:127], v200\n" \
"v_smfmac_f32_16x16x64_bf16 v[72:75], v[4:7], a[176:183], v200\n" \
"v_smfmac_f32_16x16x64_bf16 v[76:79], v[4:7], a[232:239], v200\n" \
"v_smfmac_f32_16x16x64_bf16 v[80:83], v[4:7], v[136:143], v200\n" \
"s_waitcnt lgkmcnt(4)\n" \
"v_smfmac_f32_16x16x64_bf16 v[60:63], v[8:11], a[16:23], v200\n" \
"v_smfmac_f32_16x16x64_bf16 v[64:67], v[8:11], a[72:79], v200\n" \
"v_smfmac_f32_16x16x64_bf16 v[68:71], v[8:11], a[128:135], v200\n" \
"v_smfmac_f32_16x16x64_bf16 v[72:75], v[8:11], a[184:191], v200\n" \
"v_smfmac_f32_16x16x64_bf16 v[76:79], v[8:11], a[240:247], v200\n" \
"v_smfmac_f32_16x16x64_bf16 v[80:83], v[8:11], v[144:151], v200\n" \
"s_waitcnt lgkmcnt(3)\n" \
"v_smfmac_f32_16x16x64_bf16 v[60:63], v[12:15], a[24:31], v200\n" \
"v_smfmac_f32_16x16x64_bf16 v[64:67], v[12:15], a[80:87], v200\n" \
"v_smfmac_f32_16x16x64_bf16 v[68:71], v[12:15], a[136:143], v200\n" \
"v_smfmac_f32_16x16x64_bf16 v[72:75], v[12:15], a[192:199], v200\n" \
"v_smfmac_f32_16x16x64_bf16 v[76:79], v[12:15], a[248:255], v200\n" \
"v_smfmac_f32_16x16x64_bf16 v[80:83], v[12:15], v[152:159], v200\n" \
"s_waitcnt lgkmcnt(2)\n" \
"v_smfmac_f32_16x16x64_bf16 v[60:63], v[16:19], a[32:39], v200\n" \
"v_smfmac_f32_16x16x64_bf16 v[64:67], v[16:19], a[88:95], v200\n" \
"v_smfmac_f32_16x16x64_bf16 v[68:71], v[16:19], a[144:151], v200\n" \
"v_smfmac_f32_16x16x64_bf16 v[72:75], v[16:19], a[200:207], v200\n" \
"v_smfmac_f32_16x16x64_bf16 v[76:79], v[16:19], v[104:111], v200\n" \
"v_smfmac_f32_16x16x64_bf16 v[80:83], v[16:19], v[160:167], v200\n" \
"s_waitcnt lgkmcnt(1)\n" \
"v_smfmac_f32_16x16x64_bf16 v[60:63], v[20:23], a[40:47], v200\n" \
"v_smfmac_f32_16x16x64_bf16 v[64:67], v[20:23], a[96:103], v200\n" \
"v_smfmac_f32_16x16x64_bf16 v[68:71], v[20:23], a[152:159], v200\n" \
"v_smfmac_f32_16x16x64_bf16 v[72:75], v[20:23], a[208:215], v200\n" \
"v_smfmac_f32_16x16x64_bf16 v[76:79], v[20:23], v[112:119], v200\n" \
"v_smfmac_f32_16x16x64_bf16 v[80:83], v[20:23], v[168:175], v200\n" \
"s_waitcnt lgkmcnt(0)\n" \
"v_smfmac_f32_16x16x64_bf16 v[60:63], v[24:27], a[48:55], v200\n" \
"v_smfmac_f32_16x16x64_bf16 v[64:67], v[24:27], a[104:111], v200\n" \
"v_smfmac_f32_16x16x64_bf16 v[68:71], v[24:27], a[160:167], v200\n" \
"v_smfmac_f32_16x16x64_bf16 v[72:75], v[24:27], a[216:223], v200\n" \
"v_smfmac_f32_16x16x64_bf16 v[76:79], v[24:27], v[120:127], v200\n" \
"v_smfmac_f32_16x16x64_bf16 v[80:83], v[24:27], v[176:183], v200\n" \
"s_nop 7\n" \
"v_permlane32_swap_b32 v60, v64\n" \
"v_permlane32_swap_b32 v61, v65\n" \
"v_permlane32_swap_b32 v62, v66\n" \
"v_permlane32_swap_b32 v63, v67\n" \
"v_add_f32 v60, v60, v64\n" \
"v_add_f32 v61, v61, v65\n" \
"v_add_f32 v62, v62, v66\n" \
"v_add_f32 v63, v63, v67\n" \
"v_cvt_pk_bf16_f32 v184, v60, v61\n" \
"v_cvt_pk_bf16_f32 v185, v62, v63\n" \
"ds_write_b64 v217, v[184:185] offset:0\n" \
"v_permlane32_swap_b32 v68, v72\n" \
"v_permlane32_swap_b32 v69, v73\n" \
"v_permlane32_swap_b32 v70, v74\n" \
"v_permlane32_swap_b32 v71, v75\n" \
"v_add_f32 v68, v68, v72\n" \
"v_add_f32 v69, v69, v73\n" \
"v_add_f32 v70, v70, v74\n" \
"v_add_f32 v71, v71, v75\n" \
"v_cvt_pk_bf16_f32 v186, v68, v69\n" \
"v_cvt_pk_bf16_f32 v187, v70, v71\n" \
"ds_write_b64 v217, v[186:187] offset:512\n" \
"v_permlane32_swap_b32 v76, v80\n" \
"v_permlane32_swap_b32 v77, v81\n" \
"v_permlane32_swap_b32 v78, v82\n" \
"v_permlane32_swap_b32 v79, v83\n" \
"v_add_f32 v76, v76, v80\n" \
"v_add_f32 v77, v77, v81\n" \
"v_add_f32 v78, v78, v82\n" \
"v_add_f32 v79, v79, v83\n" \
"v_cvt_pk_bf16_f32 v188, v76, v77\n" \
"v_cvt_pk_bf16_f32 v189, v78, v79\n" \
"ds_write_b64 v217, v[188:189] offset:1024\n" \
"ds_read_b64_tr_b16 v[0:1], v218 offset:0\n" \
"ds_read_b64_tr_b16 v[2:3], v218 offset:64\n" \
"ds_read_b64_tr_b16 v[4:5], v218 offset:1024\n" \
"ds_read_b64_tr_b16 v[6:7], v218 offset:1088\n" \
"s_add_u32 s43, s40, 0\n" \
"s_mul_i32 s43, s43, 114688\n" \
"s_waitcnt lgkmcnt(0)\n" \
"buffer_store_dwordx4 v[0:3], v219, s[16:19], s43 offen sc1\n" \
"buffer_store_dwordx4 v[4:7], v221, s[16:19], s43 offen offset:1024 sc1\n" \
"s_waitcnt vmcnt(0)\n" \
"s_barrier\n" \
"ds_read_b128 v[0:3], v216 offset:7168\n" \
"ds_read_b128 v[4:7], v216 offset:8192\n" \
"ds_read_b128 v[8:11], v216 offset:9216\n" \
"ds_read_b128 v[12:15], v216 offset:10240\n" \
"ds_read_b128 v[16:19], v216 offset:11264\n" \
"ds_read_b128 v[20:23], v216 offset:12288\n" \
"ds_read_b128 v[24:27], v216 offset:13312\n" \
"v_mov_b32 v60, 0\n" \
"v_mov_b32 v61, 0\n" \
"v_mov_b32 v62, 0\n" \
"v_mov_b32 v63, 0\n" \
"v_mov_b32 v64, 0\n" \
"v_mov_b32 v65, 0\n" \
"v_mov_b32 v66, 0\n" \
"v_mov_b32 v67, 0\n" \
"v_mov_b32 v68, 0\n" \
"v_mov_b32 v69, 0\n" \
"v_mov_b32 v70, 0\n" \
"v_mov_b32 v71, 0\n" \
"v_mov_b32 v72, 0\n" \
"v_mov_b32 v73, 0\n" \
"v_mov_b32 v74, 0\n" \
"v_mov_b32 v75, 0\n" \
"v_mov_b32 v76, 0\n" \
"v_mov_b32 v77, 0\n" \
"v_mov_b32 v78, 0\n" \
"v_mov_b32 v79, 0\n" \
"v_mov_b32 v80, 0\n" \
"v_mov_b32 v81, 0\n" \
"v_mov_b32 v82, 0\n" \
"v_mov_b32 v83, 0\n" \
"s_nop 1\n" \
"s_waitcnt lgkmcnt(6)\n" \
"v_smfmac_f32_16x16x64_bf16 v[60:63], v[0:3], a[0:7], v200\n" \
"v_smfmac_f32_16x16x64_bf16 v[64:67], v[0:3], a[56:63], v200\n" \
"v_smfmac_f32_16x16x64_bf16 v[68:71], v[0:3], a[112:119], v200\n" \
"v_smfmac_f32_16x16x64_bf16 v[72:75], v[0:3], a[168:175], v200\n" \
"v_smfmac_f32_16x16x64_bf16 v[76:79], v[0:3], a[224:231], v200\n" \
"v_smfmac_f32_16x16x64_bf16 v[80:83], v[0:3], v[128:135], v200\n" \
"s_waitcnt lgkmcnt(5)\n" \
"v_smfmac_f32_16x16x64_bf16 v[60:63], v[4:7], a[8:15], v200\n" \
"v_smfmac_f32_16x16x64_bf16 v[64:67], v[4:7], a[64:71], v200\n" \
"v_smfmac_f32_16x16x64_bf16 v[68:71], v[4:7], a[120:127], v200\n" \
"v_smfmac_f32_16x16x64_bf16 v[72:75], v[4:7], a[176:183], v200\n" \
"v_smfmac_f32_16x16x64_bf16 v[76:79], v[4:7], a[232:239], v200\n" \
"v_smfmac_f32_16x16x64_bf16 v[80:83], v[4:7], v[136:143], v200\n" \
"s_waitcnt lgkmcnt(4)\n" \
"v_smfmac_f32_16x16x64_bf16 v[60:63], v[8:11], a[16:23], v200\n" \
"v_smfmac_f32_16x16x64_bf16 v[64:67], v[8:11], a[72:79], v200\n" \
"v_smfmac_f32_16x16x64_bf16 v[68:71], v[8:11], a[128:135], v200\n" \
"v_smfmac_f32_16x16x64_bf16 v[72:75], v[8:11], a[184:191], v200\n" \
"v_smfmac_f32_16x16x64_bf16 v[76:79], v[8:11], a[240:247], v200\n" \
"v_smfmac_f32_16x16x64_bf16 v[80:83], v[8:11], v[144:151], v200\n" \
"s_waitcnt lgkmcnt(3)\n" \
"v_smfmac_f32_16x16x64_bf16 v[60:63], v[12:15], a[24:31], v200\n" \
"v_smfmac_f32_16x16x64_bf16 v[64:67], v[12:15], a[80:87], v200\n" \
"v_smfmac_f32_16x16x64_bf16 v[68:71], v[12:15], a[136:143], v200\n" \
"v_smfmac_f32_16x16x64_bf16 v[72:75], v[12:15], a[192:199], v200\n" \
"v_smfmac_f32_16x16x64_bf16 v[76:79], v[12:15], a[248:255], v200\n" \
"v_smfmac_f32_16x16x64_bf16 v[80:83], v[12:15], v[152:159], v200\n" \
"s_waitcnt lgkmcnt(2)\n" \
"v_smfmac_f32_16x16x64_bf16 v[60:63], v[16:19], a[32:39], v200\n" \
"v_smfmac_f32_16x16x64_bf16 v[64:67], v[16:19], a[88:95], v200\n" \
"v_smfmac_f32_16x16x64_bf16 v[68:71], v[16:19], a[144:151], v200\n" \
"v_smfmac_f32_16x16x64_bf16 v[72:75], v[16:19], a[200:207], v200\n" \
"v_smfmac_f32_16x16x64_bf16 v[76:79], v[16:19], v[104:111], v200\n" \
"v_smfmac_f32_16x16x64_bf16 v[80:83], v[16:19], v[160:167], v200\n" \
"s_waitcnt lgkmcnt(1)\n" \
"v_smfmac_f32_16x16x64_bf16 v[60:63], v[20:23], a[40:47], v200\n" \
"v_smfmac_f32_16x16x64_bf16 v[64:67], v[20:23], a[96:103], v200\n" \
"v_smfmac_f32_16x16x64_bf16 v[68:71], v[20:23], a[152:159], v200\n" \
"v_smfmac_f32_16x16x64_bf16 v[72:75], v[20:23], a[208:215], v200\n" \
"v_smfmac_f32_16x16x64_bf16 v[76:79], v[20:23], v[112:119], v200\n" \
"v_smfmac_f32_16x16x64_bf16 v[80:83], v[20:23], v[168:175], v200\n" \
"s_waitcnt lgkmcnt(0)\n" \
"v_smfmac_f32_16x16x64_bf16 v[60:63], v[24:27], a[48:55], v200\n" \
"v_smfmac_f32_16x16x64_bf16 v[64:67], v[24:27], a[104:111], v200\n" \
"v_smfmac_f32_16x16x64_bf16 v[68:71], v[24:27], a[160:167], v200\n" \
"v_smfmac_f32_16x16x64_bf16 v[72:75], v[24:27], a[216:223], v200\n" \
"v_smfmac_f32_16x16x64_bf16 v[76:79], v[24:27], v[120:127], v200\n" \
"v_smfmac_f32_16x16x64_bf16 v[80:83], v[24:27], v[176:183], v200\n" \
"s_nop 7\n" \
"v_permlane32_swap_b32 v60, v64\n" \
"v_permlane32_swap_b32 v61, v65\n" \
"v_permlane32_swap_b32 v62, v66\n" \
"v_permlane32_swap_b32 v63, v67\n" \
"v_add_f32 v60, v60, v64\n" \
"v_add_f32 v61, v61, v65\n" \
"v_add_f32 v62, v62, v66\n" \
"v_add_f32 v63, v63, v67\n" \
"v_cvt_pk_bf16_f32 v184, v60, v61\n" \
"v_cvt_pk_bf16_f32 v185, v62, v63\n" \
"ds_write_b64 v217, v[184:185] offset:0\n" \
"v_permlane32_swap_b32 v68, v72\n" \
"v_permlane32_swap_b32 v69, v73\n" \
"v_permlane32_swap_b32 v70, v74\n" \
"v_permlane32_swap_b32 v71, v75\n" \
"v_add_f32 v68, v68, v72\n" \
"v_add_f32 v69, v69, v73\n" \
"v_add_f32 v70, v70, v74\n" \
"v_add_f32 v71, v71, v75\n" \
"v_cvt_pk_bf16_f32 v186, v68, v69\n" \
"v_cvt_pk_bf16_f32 v187, v70, v71\n" \
"ds_write_b64 v217, v[186:187] offset:512\n" \
"v_permlane32_swap_b32 v76, v80\n" \
"v_permlane32_swap_b32 v77, v81\n" \
"v_permlane32_swap_b32 v78, v82\n" \
"v_permlane32_swap_b32 v79, v83\n" \
"v_add_f32 v76, v76, v80\n" \
"v_add_f32 v77, v77, v81\n" \
"v_add_f32 v78, v78, v82\n" \
"v_add_f32 v79, v79, v83\n" \
"v_cvt_pk_bf16_f32 v188, v76, v77\n" \
"v_cvt_pk_bf16_f32 v189, v78, v79\n" \
"ds_write_b64 v217, v[188:189] offset:1024\n" \
"ds_read_b64_tr_b16 v[0:1], v218 offset:0\n" \
"ds_read_b64_tr_b16 v[2:3], v218 offset:64\n" \
"ds_read_b64_tr_b16 v[4:5], v218 offset:1024\n" \
"ds_read_b64_tr_b16 v[6:7], v218 offset:1088\n" \
"s_add_u32 s43, s40, 1\n" \
"s_mul_i32 s43, s43, 114688\n" \
"s_waitcnt lgkmcnt(0)\n" \
"buffer_store_dwordx4 v[0:3], v219, s[16:19], s43 offen sc1\n" \
"buffer_store_dwordx4 v[4:7], v221, s[16:19], s43 offen offset:1024 sc1\n" \
"s_waitcnt vmcnt(0)\n" \
"s_barrier\n" \
".LX2_end:\n" \
"s_endpgm\n" \
".p2align 6\n" \
"fk_role_X3:\n" \
"ds_read_b32 v200, v255 offset:0\n" \
"ds_read_b32 v201, v255 offset:256\n" \
"ds_read_b32 v202, v255 offset:512\n" \
"ds_read_b32 v203, v255 offset:768\n" \
"ds_read_b32 v204, v255 offset:1024\n" \
"ds_read_b32 v205, v255 offset:1280\n" \
"ds_read_b32 v206, v255 offset:1536\n" \
"ds_read_b32 v207, v255 offset:1792\n" \
"ds_read_b32 v208, v255 offset:2048\n" \
"ds_read_b32 v209, v255 offset:2304\n" \
"ds_read_b32 v210, v255 offset:2560\n" \
"ds_read_b32 v211, v255 offset:2816\n" \
"ds_read_b32 v212, v255 offset:3072\n" \
"ds_read_b32 v213, v255 offset:3328\n" \
"ds_read_b32 v214, v255 offset:3584\n" \
"ds_read_b32 v215, v255 offset:3840\n" \
"ds_read_b32 v216, v255 offset:4096\n" \
"ds_read_b32 v217, v255 offset:4352\n" \
"ds_read_b32 v218, v255 offset:4608\n" \
"ds_read_b32 v219, v255 offset:4864\n" \
"ds_read_b32 v220, v255 offset:5120\n" \
"ds_read_b32 v221, v255 offset:5376\n" \
"ds_read_b32 v254, v255 offset:7936\n" \
"s_waitcnt lgkmcnt(0)\n" \
"v_readlane_b32 s4, v254, 0\n" \
"v_readlane_b32 s5, v254, 1\n" \
"v_readlane_b32 s6, v254, 2\n" \
"v_readlane_b32 s7, v254, 3\n" \
"v_readlane_b32 s8, v254, 4\n" \
"v_readlane_b32 s9, v254, 5\n" \
"v_readlane_b32 s10, v254, 6\n" \
"v_readlane_b32 s11, v254, 7\n" \
"v_readlane_b32 s12, v254, 8\n" \
"v_readlane_b32 s13, v254, 9\n" \
"v_readlane_b32 s14, v254, 10\n" \
"v_readlane_b32 s15, v254, 11\n" \
"v_readlane_b32 s16, v254, 12\n" \
"v_readlane_b32 s17, v254, 13\n" \
"v_readlane_b32 s18, v254, 14\n" \
"v_readlane_b32 s19, v254, 15\n" \
"v_readlane_b32 s20, v254, 16\n" \
"v_readlane_b32 s21, v254, 17\n" \
"v_readlane_b32 s22, v254, 18\n" \
"v_readlane_b32 s23, v254, 19\n" \
"v_readlane_b32 s24, v254, 20\n" \
"v_readlane_b32 s25, v254, 21\n" \
"v_readlane_b32 s26, v254, 22\n" \
"v_readlane_b32 s27, v254, 23\n" \
"v_readlane_b32 s28, v254, 24\n" \
"v_readlane_b32 s29, v254, 25\n" \
"v_readlane_b32 s30, v254, 26\n" \
"v_readlane_b32 s31, v254, 27\n" \
"v_readlane_b32 s32, v254, 28\n" \
"v_readlane_b32 s33, v254, 29\n" \
"v_readlane_b32 s34, v254, 30\n" \
"v_readlane_b32 s35, v254, 31\n" \
"v_readlane_b32 s36, v254, 32\n" \
"v_readlane_b32 s37, v254, 33\n" \
"v_readlane_b32 s38, v254, 34\n" \
"v_readlane_b32 s39, v254, 35\n" \
"s_mov_b32 s40, 0\n" \
"s_mov_b32 s51, 0\n" \
"s_nop 0\n" \
"v_mov_b32 v252, s38\n" \
"s_nop 4\n" \
"v_mov_b32 v100, -1\n" \
"v_mov_b32 v101, -1\n" \
"v_mov_b32 v102, -1\n" \
"v_mov_b32 v103, -1\n" \
"s_mov_b32 s35, -2\n" \
"s_mov_b32 s2, 0\n" \
"s_mov_b32 s3, -1\n" \
"v_writelane_b32 v251, s6, 0\n" \
"v_writelane_b32 v251, s10, 1\n" \
"v_writelane_b32 v251, s14, 2\n" \
"v_writelane_b32 v251, s18, 3\n" \
"s_mov_b32 s6, 0\n" \
"s_mov_b32 s10, 0\n" \
"s_mov_b32 s14, 0\n" \
"s_mov_b32 s18, 0\n" \
"s_mov_b32 s40, -1\n" \
"s_branch .LX3_pre\n" \
".LX3_wet:\n" \
"s_waitcnt vmcnt(0)\n" \
"v_readlane_b32 s6, v251, 0\n" \
"v_readlane_b32 s10, v251, 1\n" \
"v_readlane_b32 s14, v251, 2\n" \
"v_readlane_b32 s18, v251, 3\n" \
"s_nop 4\n" \
"ds_read_b32 v232, v255 offset:7424\n" \
"ds_read_b32 v233, v255 offset:7680\n" \
"v_lshrrev_b32 v234, 4, v201\n" \
"v_and_b32 v234, 3, v234\n" \
"v_subrev_u32 v230, 512, v205\n" \
"s_add_u32 s43, s30, 1\n" \
"s_mov_b32 s50, 0\n" \
".LX3_w117l:\n" \
"buffer_load_dword v240, v230, s[4:7], 0 offen sc1\n" \
"s_waitcnt vmcnt(0)\n" \
".LX3_w117c:\n" \
"v_subrev_u32 v241, s43, v240\n" \
"v_cmp_gt_u32 vcc, 0x100, v241\n" \
"s_cmp_eq_u64 vcc, -1\n" \
"s_cbranch_scc1 .LX3_w117d\n" \
"s_sleep 2\n" \
"s_add_u32 s50, s50, 1\n" \
"s_cmp_lt_u32 s50, 0x20000\n" \
"s_cbranch_scc1 .LX3_w117l\n" \
"s_branch .LX3_end\n" \
".LX3_w117d:\n" \
"v_lshl_or_b32 v241, v241, 2, v234\n" \
"s_nop 1\n" \
"v_min_u32_dpp v241, v241, v241 row_ror:4 row_mask:0xf bank_mask:0xf\n" \
"s_nop 1\n" \
"v_min_u32_dpp v241, v241, v241 row_ror:8 row_mask:0xf bank_mask:0xf\n" \
"s_nop 1\n" \
"v_min_u32_dpp v241, v241, v241 quad_perm:[1,0,3,2] row_mask:0xf bank_mask:0xf\n" \
"s_nop 1\n" \
"v_min_u32_dpp v241, v241, v241 quad_perm:[2,3,0,1] row_mask:0xf bank_mask:0xf\n" \
"s_nop 0\n" \
"v_readlane_b32 s31, v241, 0\n" \
"v_readlane_b32 s43, v241, 16\n" \
"s_min_u32 s31, s31, s43\n" \
"v_readlane_b32 s43, v241, 32\n" \
"s_min_u32 s31, s31, s43\n" \
"v_readlane_b32 s43, v241, 48\n" \
"s_min_u32 s31, s31, s43\n" \
"v_subrev_u32 v230, 256, v205\n" \
"s_add_u32 s43, s30, 1\n" \
"s_mov_b32 s50, 0\n" \
".LX3_w118l:\n" \
"buffer_load_dword v240, v230, s[4:7], 0 offen sc1\n" \
"s_waitcnt vmcnt(0)\n" \
".LX3_w118c:\n" \
"v_subrev_u32 v241, s43, v240\n" \
"v_cmp_gt_u32 vcc, 0x100, v241\n" \
"s_cmp_eq_u64 vcc, -1\n" \
"s_cbranch_scc1 .LX3_w118d\n" \
"s_sleep 2\n" \
"s_add_u32 s50, s50, 1\n" \
"s_cmp_lt_u32 s50, 0x20000\n" \
"s_cbranch_scc1 .LX3_w118l\n" \
"s_branch .LX3_end\n" \
".LX3_w118d:\n" \
"v_lshl_or_b32 v241, v241, 2, v234\n" \
"s_nop 1\n" \
"v_min_u32_dpp v241, v241, v241 row_ror:4 row_mask:0xf bank_mask:0xf\n" \
"s_nop 1\n" \
"v_min_u32_dpp v241, v241, v241 row_ror:8 row_mask:0xf bank_mask:0xf\n" \
"s_nop 1\n" \
"v_min_u32_dpp v241, v241, v241 quad_perm:[1,0,3,2] row_mask:0xf bank_mask:0xf\n" \
"s_nop 1\n" \
"v_min_u32_dpp v241, v241, v241 quad_perm:[2,3,0,1] row_mask:0xf bank_mask:0xf\n" \
"s_nop 0\n" \
"v_readlane_b32 s32, v241, 0\n" \
"v_readlane_b32 s43, v241, 16\n" \
"s_min_u32 s32, s32, s43\n" \
"v_readlane_b32 s43, v241, 32\n" \
"s_min_u32 s32, s32, s43\n" \
"v_readlane_b32 s43, v241, 48\n" \
"s_min_u32 s32, s32, s43\n" \
"s_xor_b32 s29, s31, s32\n" \
"s_and_b32 s29, s29, 3\n" \
"s_lshl_b32 s29, s29, 13\n" \
"s_bitset1_b32 s29, 21\n" \
"s_and_b32 s31, s31, 3\n" \
"s_lshl_b32 s31, s31, 13\n" \
"s_bitcmp1_b32 s39, 0\n" \
"s_cselect_b32 s43, s29, 0\n" \
"s_xor_b32 s31, s31, s43\n" \
"s_xor_b32 s32, s31, 0x4000\n" \
"s_mov_b32 s39, 0\n" \
"v_mov_b32 v224, -1\n" \
"v_mov_b32 v225, -1\n" \
"v_mov_b32 v226, -1\n" \
"v_mov_b32 v227, -1\n" \
"s_waitcnt lgkmcnt(0)\n" \
"s_add_u32 s50, s31, 0\n" \
"buffer_store_dwordx4 v[224:227], v232, s[4:7], s50 offen sc1\n" \
"s_xor_b32 s43, s50, s29\n" \
"buffer_store_dwordx4 v[224:227], v232, s[4:7], s43 offen sc1\n" \
"s_add_u32 s50, s32, 524288\n" \
"buffer_store_dwordx4 v[224:227], v232, s[4:7], s50 offen sc1\n" \
"s_xor_b32 s43, s50, s29\n" \
"buffer_store_dwordx4 v[224:227], v232, s[4:7], s43 offen sc1\n" \
"s_add_u32 s50, s31, 1048576\n" \
"buffer_store_dwordx4 v[224:227], v232, s[4:7], s50 offen sc1\n" \
"s_xor_b32 s43, s50, s29\n" \
"buffer_store_dwordx4 v[224:227], v232, s[4:7], s43 offen sc1\n" \
"s_add_u32 s50, s32, 1572864\n" \
"buffer_store_dwordx4 v[224:227], v232, s[4:7], s50 offen sc1\n" \
"s_xor_b32 s43, s50, s29\n" \
"buffer_store_dwordx4 v[224:227], v232, s[4:7], s43 offen sc1\n" \
"s_waitcnt vmcnt(0)\n" \
"s_barrier\n" \
"s_add_u32 s43, s30, s40\n" \
"s_add_u32 s43, s43, 1\n" \
"v_mov_b32 v241, s43\n" \
"buffer_store_dword v241, v233, s[4:7], 0 offen sc1\n" \
"s_add_u32 s43, s30, 1\n" \
"s_mov_b32 s50, 0\n" \
".LX3_w119l:\n" \
"buffer_load_dword v240, v205, s[4:7], 0 offen sc1\n" \
"s_waitcnt vmcnt(0)\n" \
".LX3_w119c:\n" \
"v_subrev_u32 v241, s43, v240\n" \
"v_cmp_gt_u32 vcc, 0x100, v241\n" \
"s_cmp_eq_u64 vcc, -1\n" \
"s_cbranch_scc1 .LX3_w119d\n" \
"s_sleep 2\n" \
"s_add_u32 s50, s50, 1\n" \
"s_cmp_lt_u32 s50, 0x20000\n" \
"s_cbranch_scc1 .LX3_w119l\n" \
"s_branch .LX3_end\n" \
".LX3_w119d:\n" \
"s_add_u32 s43, s30, 1\n" \
"s_mov_b32 s50, 0\n" \
".LX3_w120l:\n" \
"buffer_load_dword v240, v206, s[4:7], 0 offen sc1\n" \
"s_waitcnt vmcnt(0)\n" \
".LX3_w120c:\n" \
"v_subrev_u32 v241, s43, v240\n" \
"v_cmp_gt_u32 vcc, 0x100, v241\n" \
"s_cmp_eq_u64 vcc, -1\n" \
"s_cbranch_scc1 .LX3_w120d\n" \
"s_sleep 2\n" \
"s_add_u32 s50, s50, 1\n" \
"s_cmp_lt_u32 s50, 0x20000\n" \
"s_cbranch_scc1 .LX3_w120l\n" \
"s_branch .LX3_end\n" \
".LX3_w120d:\n" \
"s_add_u32 s43, s30, 8\n" \
"s_mov_b32 s50, 0\n" \
".LX3_w121l:\n" \
"buffer_load_dword v240, v215, s[4:7], 0 offen sc1\n" \
"s_waitcnt vmcnt(0)\n" \
".LX3_w121c:\n" \
"v_subrev_u32 v241, s43, v240\n" \
"v_cmp_gt_u32 vcc, 0x100, v241\n" \
"s_cmp_eq_u64 vcc, -1\n" \
"s_cbranch_scc1 .LX3_w121d\n" \
"s_sleep 2\n" \
"s_add_u32 s50, s50, 1\n" \
"s_cmp_lt_u32 s50, 0x20000\n" \
"s_cbranch_scc1 .LX3_w121l\n" \
"s_branch .LX3_end\n" \
".LX3_w121d:\n" \
"s_add_u32 s43, s40, 0\n" \
"s_mul_i32 s43, s43, 458752\n" \
"s_add_u32 s43, s43, s33\n" \
"buffer_load_dwordx4 a[0:3], v201, s[8:11], s43 offen sc1\n" \
"buffer_load_dwordx4 a[4:7], v201, s[8:11], s43 offen offset:1024 sc1\n" \
"buffer_load_dwordx4 a[8:11], v201, s[8:11], s43 offen offset:2048 sc1\n" \
"buffer_load_dwordx4 a[12:15], v201, s[8:11], s43 offen offset:3072 sc1\n" \
"s_add_u32 s43, s43, 4096\n" \
"buffer_load_dwordx4 a[16:19], v201, s[8:11], s43 offen sc1\n" \
"buffer_load_dwordx4 a[20:23], v201, s[8:11], s43 offen offset:1024 sc1\n" \
"buffer_load_dwordx4 a[24:27], v201, s[8:11], s43 offen offset:2048 sc1\n" \
"buffer_load_dwordx4 a[28:31], v201, s[8:11], s43 offen offset:3072 sc1\n" \
"s_add_u32 s43, s43, 4096\n" \
"buffer_load_dwordx4 a[32:35], v201, s[8:11], s43 offen sc1\n" \
"buffer_load_dwordx4 a[36:39], v201, s[8:11], s43 offen offset:1024 sc1\n" \
"buffer_load_dwordx4 a[40:43], v201, s[8:11], s43 offen offset:2048 sc1\n" \
"buffer_load_dwordx4 a[44:47], v201, s[8:11], s43 offen offset:3072 sc1\n" \
"s_add_u32 s43, s43, 4096\n" \
"buffer_load_dwordx4 a[48:51], v201, s[8:11], s43 offen sc1\n" \
"buffer_load_dwordx4 a[52:55], v201, s[8:11], s43 offen offset:1024 sc1\n" \
"buffer_load_dwordx4 a[56:59], v201, s[8:11], s43 offen offset:2048 sc1\n" \
"buffer_load_dwordx4 a[60:63], v201, s[8:11], s43 offen offset:3072 sc1\n" \
"s_add_u32 s43, s43, 4096\n" \
"buffer_load_dwordx4 a[64:67], v201, s[8:11], s43 offen sc1\n" \
"buffer_load_dwordx4 a[68:71], v201, s[8:11], s43 offen offset:1024 sc1\n" \
"buffer_load_dwordx4 a[72:75], v201, s[8:11], s43 offen offset:2048 sc1\n" \
"buffer_load_dwordx4 a[76:79], v201, s[8:11], s43 offen offset:3072 sc1\n" \
"s_add_u32 s43, s43, 4096\n" \
"buffer_load_dwordx4 a[80:83], v201, s[8:11], s43 offen sc1\n" \
"buffer_load_dwordx4 a[84:87], v201, s[8:11], s43 offen offset:1024 sc1\n" \
"buffer_load_dwordx4 a[88:91], v201, s[8:11], s43 offen offset:2048 sc1\n" \
"buffer_load_dwordx4 a[92:95], v201, s[8:11], s43 offen offset:3072 sc1\n" \
"s_add_u32 s43, s43, 4096\n" \
"buffer_load_dwordx4 a[96:99], v201, s[8:11], s43 offen sc1\n" \
"buffer_load_dwordx4 a[100:103], v201, s[8:11], s43 offen offset:1024 sc1\n" \
"buffer_load_dwordx4 a[104:107], v201, s[8:11], s43 offen offset:2048 sc1\n" \
"buffer_load_dwordx4 a[108:111], v201, s[8:11], s43 offen offset:3072 sc1\n" \
"s_add_u32 s50, s40, 0\n" \
"s_mul_i32 s50, s50, 458752\n" \
"s_add_u32 s50, s50, s34\n" \
"buffer_load_dwordx4 v[28:31], v201, s[12:15], s50 offen sc1\n" \
"buffer_load_dwordx4 v[32:35], v201, s[12:15], s50 offen offset:1024 sc1\n" \
"s_add_u32 s43, s30, 9\n" \
"s_mov_b32 s50, 0\n" \
".LX3_w122l:\n" \
"buffer_load_dword v240, v215, s[4:7], 0 offen sc1\n" \
"s_waitcnt vmcnt(0)\n" \
".LX3_w122c:\n" \
"v_subrev_u32 v241, s43, v240\n" \
"v_cmp_gt_u32 vcc, 0x100, v241\n" \
"s_cmp_eq_u64 vcc, -1\n" \
"s_cbranch_scc1 .LX3_w122d\n" \
"s_sleep 2\n" \
"s_add_u32 s50, s50, 1\n" \
"s_cmp_lt_u32 s50, 0x20000\n" \
"s_cbranch_scc1 .LX3_w122l\n" \
"s_branch .LX3_end\n" \
".LX3_w122d:\n" \
".LX3_pre:\n" \
"s_barrier\n" \
"ds_read_b96 v[248:250], v252\n" \
"s_waitcnt lgkmcnt(0)\n" \
"v_readfirstlane_b32 s44, v248\n" \
"v_readfirstlane_b32 s45, v249\n" \
"v_readfirstlane_b32 s46, v250\n" \
"s_mul_i32 s42, s25, s46\n" \
"s_lshr_b32 s42, s42, 12\n" \
".LX3_cu:\n" \
"s_sub_u32 s43, s24, s44\n" \
"s_cmp_lt_i32 s43, 0\n" \
"s_cbranch_scc0 .LX3_cud\n" \
"s_add_u32 s24, s24, s25\n" \
"s_xor_b32 s31, s31, s29\n" \
"s_xor_b32 s32, s32, s29\n" \
"s_xor_b32 s47, s47, s29\n" \
"s_add_u32 s39, s39, 1\n" \
"s_branch .LX3_cu\n" \
".LX3_cud:\n" \
".LX3_loop:\n" \
"s_add_u32 s43, s40, 2\n" \
"s_and_b32 s43, s43, 3\n" \
"s_bitcmp1_b32 s43, 0\n" \
"s_cselect_b32 s49, s32, s31\n" \
"s_lshl_b32 s43, s43, 19\n" \
"s_add_u32 s49, s49, s43\n" \
"ds_read_b96 v[248:250], v252\n" \
"s_waitcnt lgkmcnt(0)\n" \
"v_readfirstlane_b32 s44, v248\n" \
"v_readfirstlane_b32 s45, v249\n" \
"v_readfirstlane_b32 s46, v250\n" \
"s_mul_i32 s42, s25, s46\n" \
"s_lshr_b32 s42, s42, 12\n" \
"s_add_u32 s43, s40, 0\n" \
"s_and_b32 s43, s43, 3\n" \
"s_bitcmp1_b32 s43, 0\n" \
"s_cselect_b32 s47, s32, s31\n" \
"s_lshl_b32 s43, s43, 19\n" \
"s_add_u32 s47, s47, s43\n" \
"s_sub_u32 s43, s24, s44\n" \
"s_add_u32 s43, s43, s28\n" \
"s_max_i32 s43, s43, 0xfffd0000\n" \
"s_min_i32 s43, s43, 0x4000\n" \
"s_mul_i32 s43, s43, s46\n" \
"s_ashr_i32 s43, s43, 12\n" \
"s_add_u32 s41, s45, s43\n" \
"s_branch .LX3_a1\n" \
".LX3_retrya:\n" \
"s_add_u32 s51, s51, 1\n" \
"s_cmp_gt_u32 s51, 0x20000\n" \
"s_cbranch_scc1 .LX3_end\n" \
".LX3_a0:\n" \
"s_add_u32 s41, s41, s42\n" \
"s_add_u32 s24, s24, s25\n" \
"s_xor_b32 s31, s31, s29\n" \
"s_xor_b32 s32, s32, s29\n" \
"s_xor_b32 s47, s47, s29\n" \
"s_xor_b32 s48, s48, s29\n" \
"s_add_u32 s39, s39, 1\n" \
"buffer_store_dwordx4 v[100:103], v214, s[4:7], s47 offen sc1\n" \
"buffer_store_dwordx4 v[100:103], v217, s[4:7], s47 offen sc1\n" \
"buffer_store_dwordx4 v[100:103], v218, s[4:7], s47 offen sc1\n" \
"buffer_store_dwordx4 v[100:103], v219, s[4:7], s47 offen sc1\n" \
".p2align 6\n" \
".LX3_a1:\n" \
"s_memtime vcc\n" \
"s_waitcnt lgkmcnt(0)\n" \
"s_sub_u32 s43, s41, vcc_lo\n" \
"s_cmp_gt_i32 s43, 128\n" \
"s_cbranch_scc1 .LX3_a1\n" \
"s_cmp_lt_i32 s43, s37\n" \
"s_cbranch_scc1 .LX3_a0\n" \
"s_max_i32 s43, s43, 0\n" \
"s_and_b32 s43, s43, -4\n" \
"s_getpc_b64 vcc\n" \
".LX3_a3:\n" \
"s_sub_u32 s43, .LX3_a4-.LX3_a3, s43\n" \
"s_add_u32 vcc_lo, vcc_lo, s43\n" \
"s_addc_u32 vcc_hi, vcc_hi, 0\n" \
"s_setpc_b64 vcc\n" \
".rept 32\n" \
"s_nop 0\n" \
".endr\n" \
".LX3_a4:\n" \
"buffer_load_dwordx4 v[0:3], v208, s[4:7], s47 offen sc1\n" \
"buffer_load_dwordx4 v[4:7], v208, s[4:7], s47 offen offset:1024 sc1\n" \
"buffer_load_dwordx4 v[8:11], v208, s[4:7], s47 offen offset:2048 sc1\n" \
"buffer_load_dwordx4 v[12:15], v208, s[4:7], s47 offen offset:3072 sc1\n" \
"buffer_load_dwordx4 v[16:19], v209, s[4:7], s47 offen sc1\n" \
"buffer_load_dwordx4 v[20:23], v209, s[4:7], s47 offen offset:1024 sc1\n" \
"buffer_load_dwordx4 v[24:27], v209, s[4:7], s47 offen offset:2048 sc1\n" \
"s_cmp_eq_u32 s35, s40\n" \
"s_cbranch_scc1 .LX3_nb\n" \
"s_barrier\n" \
".LX3_nb:\n" \
"s_mov_b32 s35, s40\n" \
"v_mov_b32 v96, s47\n" \
"ds_write_b32 v252, v96 offset:16\n" \
"v_mov_b32 v60, 0\n" \
"v_mov_b32 v61, 0\n" \
"v_mov_b32 v62, 0\n" \
"v_mov_b32 v63, 0\n" \
"v_mov_b32 v64, 0\n" \
"v_mov_b32 v65, 0\n" \
"v_mov_b32 v66, 0\n" \
"v_mov_b32 v67, 0\n" \
"s_lshl_b32 s43, s40, 7\n" \
"buffer_load_dwordx4 v[236:239], v207, s[4:7], s43 offen sc1\n" \
"s_add_u32 s50, s40, 1\n" \
"s_mul_i32 s50, s50, 458752\n" \
"s_add_u32 s50, s50, s34\n" \
"buffer_load_dwordx4 v[44:47], v201, s[12:15], s50 offen sc1\n" \
"buffer_load_dwordx4 v[48:51], v201, s[12:15], s50 offen offset:1024 sc1\n" \
"s_waitcnt vmcnt(9)\n" \
"v_smfmac_f32_16x16x64_bf16 v[60:63], v[0:3], a[0:7], v200\n" \
"v_smfmac_f32_16x16x64_bf16 v[64:67], v[0:3], a[56:63], v200\n" \
"s_waitcnt vmcnt(8)\n" \
"v_smfmac_f32_16x16x64_bf16 v[60:63], v[4:7], a[8:15], v200\n" \
"v_smfmac_f32_16x16x64_bf16 v[64:67], v[4:7], a[64:71], v200\n" \
"s_waitcnt vmcnt(7)\n" \
"v_smfmac_f32_16x16x64_bf16 v[60:63], v[8:11], a[16:23], v200\n" \
"v_smfmac_f32_16x16x64_bf16 v[64:67], v[8:11], a[72:79], v200\n" \
"s_waitcnt vmcnt(6)\n" \
"v_smfmac_f32_16x16x64_bf16 v[60:63], v[12:15], a[24:31], v200\n" \
"v_smfmac_f32_16x16x64_bf16 v[64:67], v[12:15], a[80:87], v200\n" \
"s_waitcnt vmcnt(5)\n" \
"v_smfmac_f32_16x16x64_bf16 v[60:63], v[16:19], a[32:39], v200\n" \
"v_smfmac_f32_16x16x64_bf16 v[64:67], v[16:19], a[88:95], v200\n" \
"s_waitcnt vmcnt(4)\n" \
"v_smfmac_f32_16x16x64_bf16 v[60:63], v[20:23], a[40:47], v200\n" \
"v_smfmac_f32_16x16x64_bf16 v[64:67], v[20:23], a[96:103], v200\n" \
"s_waitcnt vmcnt(3)\n" \
"v_smfmac_f32_16x16x64_bf16 v[60:63], v[24:27], a[48:55], v200\n" \
"v_smfmac_f32_16x16x64_bf16 v[64:67], v[24:27], a[104:111], v200\n" \
"s_nop 2\n" \
"v_max3_u32 v84, v0, v0, v1\n" \
"v_max3_u32 v84, v84, v2, v3\n" \
"v_max3_u32 v84, v84, v4, v5\n" \
"v_max3_u32 v84, v84, v6, v7\n" \
"v_max3_u32 v84, v84, v8, v9\n" \
"v_permlane32_swap_b32 v60, v64\n" \
"v_permlane32_swap_b32 v61, v65\n" \
"v_permlane32_swap_b32 v62, v66\n" \
"v_permlane32_swap_b32 v63, v67\n" \
"v_pk_add_f32 v[60:61], v[60:61], v[64:65]\n" \
"v_pk_add_f32 v[62:63], v[62:63], v[66:67]\n" \
"v_max3_u32 v84, v84, v10, v11\n" \
"v_max_f32_dpp v88, v60, v60 row_ror:8 row_mask:0xf bank_mask:0x3\n" \
"v_max_f32_dpp v89, v61, v61 row_ror:8 row_mask:0xf bank_mask:0x3\n" \
"v_max_f32_dpp v88, v62, v62 row_ror:8 row_mask:0xf bank_mask:0xc\n" \
"v_max_f32_dpp v89, v63, v63 row_ror:8 row_mask:0xf bank_mask:0xc\n" \
"v_max3_u32 v84, v84, v12, v13\n" \
"v_max_f32_dpp v90, v88, v88 row_shl:4 row_mask:0xf bank_mask:0x5\n" \
"v_max_f32_dpp v90, v89, v89 row_shr:4 row_mask:0xf bank_mask:0xa\n" \
"v_max3_u32 v84, v84, v14, v15\n" \
"v_max3_u32 v84, v84, v16, v17\n" \
"v_max_f32_dpp v88, v90, v90 quad_perm:[1,0,3,2] row_mask:0xf bank_mask:0xf\n" \
"v_max3_u32 v84, v84, v18, v19\n" \
"v_max3_u32 v84, v84, v20, v21\n" \
"v_max_f32_dpp v86, v88, v88 quad_perm:[2,3,0,1] row_mask:0xf bank_mask:0xf\n" \
"v_max_f32_dpp v89, v88, v88 quad_perm:[2,3,0,1] row_mask:0xf bank_mask:0xf\n" \
"v_max3_u32 v84, v84, v22, v23\n" \
"v_max3_u32 v84, v84, v24, v25\n" \
"v_permlane32_swap_b32 v86, v89\n" \
"v_max_f32 v86, v86, v89\n" \
"v_max3_u32 v84, v84, v26, v27\n" \
"v_cmp_eq_u32 vcc, -1, v84\n" \
"v_subrev_f32_dpp v60, v86, v60 row_newbcast:0 row_mask:0xf bank_mask:0xf\n" \
"v_subrev_f32_dpp v61, v86, v61 row_newbcast:4 row_mask:0xf bank_mask:0xf\n" \
"v_subrev_f32_dpp v62, v86, v62 row_newbcast:8 row_mask:0xf bank_mask:0xf\n" \
"v_subrev_f32_dpp v63, v86, v63 row_newbcast:12 row_mask:0xf bank_mask:0xf\n" \
"v_exp_f32 v60, v60\n" \
"v_exp_f32 v61, v61\n" \
"v_exp_f32 v62, v62\n" \
"v_exp_f32 v63, v63\n" \
"v_cvt_pk_bf16_f32 v92, v60, v61\n" \
"v_cvt_pk_bf16_f32 v93, v62, v63\n" \
"ds_write_b64 v212, v[92:93]\n" \
"s_cbranch_vccnz .LX3_retrya\n" \
"v_add_f32_dpp v88, v60, v60 row_ror:8 row_mask:0xf bank_mask:0x3\n" \
"v_add_f32_dpp v89, v61, v61 row_ror:8 row_mask:0xf bank_mask:0x3\n" \
"v_add_f32_dpp v88, v62, v62 row_ror:8 row_mask:0xf bank_mask:0xc\n" \
"v_add_f32_dpp v89, v63, v63 row_ror:8 row_mask:0xf bank_mask:0xc\n" \
"v_mov_b32 v245, v245\n" \
"v_add_f32_dpp v90, v88, v88 row_shl:4 row_mask:0xf bank_mask:0x5\n" \
"v_add_f32_dpp v90, v89, v89 row_shr:4 row_mask:0xf bank_mask:0xa\n" \
"v_mov_b32 v245, v245\n" \
"v_mov_b32 v245, v245\n" \
"v_add_f32_dpp v88, v90, v90 quad_perm:[1,0,3,2] row_mask:0xf bank_mask:0xf\n" \
"v_mov_b32 v245, v245\n" \
"v_mov_b32 v245, v245\n" \
"v_add_f32_dpp v87, v88, v88 quad_perm:[2,3,0,1] row_mask:0xf bank_mask:0xf\n" \
"buffer_store_dwordx2 v[86:87], v211, s[4:7], s47 offen sc1\n" \
"s_nop 0\n" \
"v_mov_b32 v245, v245\n" \
"s_waitcnt lgkmcnt(0)\n" \
"s_barrier\n" \
"ds_read_b64_tr_b16 v[92:93], v213\n" \
"ds_read_b64_tr_b16 v[94:95], v213 offset:64\n" \
"ds_read_b32 v84, v252 offset:16\n" \
"v_mov_b32 v245, v245\n" \
"s_nop 2\n" \
"v_mov_b32 v245, v245\n" \
"s_nop 2\n" \
"v_mov_b32 v245, v245\n" \
"s_waitcnt lgkmcnt(1)\n" \
"v_mfma_f32_16x16x32_bf16 v[68:71], v[92:95], v[28:31], 0\n" \
"s_waitcnt lgkmcnt(0)\n" \
"v_xor_b32 v85, s29, v84\n" \
"v_mfma_f32_16x16x32_bf16 v[72:75], v[92:95], v[32:35], 0\n" \
"v_add_u32 v84, v84, v210\n" \
"v_add_u32 v85, v85, v210\n" \
"v_mov_b32 v245, v245\n" \
"v_mov_b32 v245, v245\n" \
"v_mov_b32 v245, v245\n" \
"v_cvt_pk_bf16_f32 v96, v68, v69\n" \
"v_cvt_pk_bf16_f32 v97, v70, v71\n" \
"s_mov_b64 exec, s[2:3]\n" \
"v_cvt_pk_bf16_f32 v96, v72, v73\n" \
"v_cvt_pk_bf16_f32 v97, v74, v75\n" \
"s_mov_b64 exec, -1\n" \
"v_mov_b32 v98, v96\n" \
"v_mov_b32 v99, v97\n" \
"s_nop 1\n" \
"v_permlane16_swap_b32 v96, v98\n" \
"v_permlane16_swap_b32 v97, v99\n" \
"buffer_store_dwordx4 v[96:99], v84, s[4:7], 0 offen sc1\n" \
"s_barrier\n" \
"buffer_store_dwordx4 v[96:99], v85, s[4:7], 0 offen sc1\n" \
"s_xor_b32 s43, s47, s29\n" \
"buffer_store_dwordx2 v[86:87], v211, s[4:7], s43 offen sc1\n" \
"ds_write_b128 v216, v[0:3] offset:0\n" \
"ds_write_b128 v216, v[4:7] offset:1024\n" \
"ds_write_b128 v216, v[8:11] offset:2048\n" \
"ds_write_b128 v216, v[12:15] offset:3072\n" \
"ds_write_b128 v216, v[16:19] offset:4096\n" \
"ds_write_b128 v216, v[20:23] offset:5120\n" \
"ds_write_b128 v216, v[24:27] offset:6144\n" \
"s_add_u32 s43, s40, 1\n" \
"s_mul_i32 s43, s43, 458752\n" \
"s_add_u32 s43, s43, s33\n" \
"buffer_load_dwordx4 a[0:3], v201, s[8:11], s43 offen sc1\n" \
"buffer_load_dwordx4 a[4:7], v201, s[8:11], s43 offen offset:1024 sc1\n" \
"buffer_load_dwordx4 a[8:11], v201, s[8:11], s43 offen offset:2048 sc1\n" \
"buffer_load_dwordx4 a[12:15], v201, s[8:11], s43 offen offset:3072 sc1\n" \
"s_add_u32 s43, s43, 4096\n" \
"buffer_load_dwordx4 a[16:19], v201, s[8:11], s43 offen sc1\n" \
"buffer_load_dwordx4 a[20:23], v201, s[8:11], s43 offen offset:1024 sc1\n" \
"buffer_load_dwordx4 a[24:27], v201, s[8:11], s43 offen offset:2048 sc1\n" \
"buffer_load_dwordx4 a[28:31], v201, s[8:11], s43 offen offset:3072 sc1\n" \
"s_add_u32 s43, s43, 4096\n" \
"buffer_load_dwordx4 a[32:35], v201, s[8:11], s43 offen sc1\n" \
"buffer_load_dwordx4 a[36:39], v201, s[8:11], s43 offen offset:1024 sc1\n" \
"buffer_load_dwordx4 a[40:43], v201, s[8:11], s43 offen offset:2048 sc1\n" \
"buffer_load_dwordx4 a[44:47], v201, s[8:11], s43 offen offset:3072 sc1\n" \
"s_add_u32 s43, s43, 4096\n" \
"buffer_load_dwordx4 a[48:51], v201, s[8:11], s43 offen sc1\n" \
"buffer_load_dwordx4 a[52:55], v201, s[8:11], s43 offen offset:1024 sc1\n" \
"buffer_load_dwordx4 a[56:59], v201, s[8:11], s43 offen offset:2048 sc1\n" \
"buffer_load_dwordx4 a[60:63], v201, s[8:11], s43 offen offset:3072 sc1\n" \
"s_add_u32 s43, s43, 4096\n" \
"buffer_load_dwordx4 a[64:67], v201, s[8:11], s43 offen sc1\n" \
"buffer_load_dwordx4 a[68:71], v201, s[8:11], s43 offen offset:1024 sc1\n" \
"buffer_load_dwordx4 a[72:75], v201, s[8:11], s43 offen offset:2048 sc1\n" \
"buffer_load_dwordx4 a[76:79], v201, s[8:11], s43 offen offset:3072 sc1\n" \
"s_add_u32 s43, s43, 4096\n" \
"buffer_load_dwordx4 a[80:83], v201, s[8:11], s43 offen sc1\n" \
"buffer_load_dwordx4 a[84:87], v201, s[8:11], s43 offen offset:1024 sc1\n" \
"buffer_load_dwordx4 a[88:91], v201, s[8:11], s43 offen offset:2048 sc1\n" \
"buffer_load_dwordx4 a[92:95], v201, s[8:11], s43 offen offset:3072 sc1\n" \
"s_add_u32 s43, s43, 4096\n" \
"buffer_load_dwordx4 a[96:99], v201, s[8:11], s43 offen sc1\n" \
"buffer_load_dwordx4 a[100:103], v201, s[8:11], s43 offen offset:1024 sc1\n" \
"buffer_load_dwordx4 a[104:107], v201, s[8:11], s43 offen offset:2048 sc1\n" \
"buffer_load_dwordx4 a[108:111], v201, s[8:11], s43 offen offset:3072 sc1\n" \
"buffer_store_dwordx4 v[100:103], v214, s[4:7], s49 offen sc1\n" \
"s_xor_b32 s43, s49, s29\n" \
"buffer_store_dwordx4 v[100:103], v214, s[4:7], s43 offen sc1\n" \
"s_waitcnt vmcnt(34)\n" \
"v_mov_b32 v28, v44\n" \
"v_mov_b32 v29, v45\n" \
"v_mov_b32 v30, v46\n" \
"v_mov_b32 v31, v47\n" \
"v_mov_b32 v32, v48\n" \
"v_mov_b32 v33, v49\n" \
"v_mov_b32 v34, v50\n" \
"v_mov_b32 v35, v51\n" \
"v_max_u32 v236, v236, v238\n" \
"v_max_u32 v237, v237, v239\n" \
"v_and_b32 v236, 0xfff, v236\n" \
"v_and_b32 v237, 0xfff, v237\n" \
"s_nop 0\n" \
"v_readfirstlane_b32 s26, v236\n" \
"v_readfirstlane_b32 s27, v237\n" \
"s_add_u32 s24, s24, s26\n" \
"s_add_u32 s24, s24, s27\n" \
"s_add_u32 s40, s40, 1\n" \
"s_waitcnt lgkmcnt(0)\n" \
"s_barrier\n" \
"s_cmp_eq_u32 s40, 0\n" \
"s_cbranch_scc1 .LX3_wet\n" \
"s_cmp_lt_u32 s40, 63\n" \
"s_cbranch_scc1 .LX3_loop\n" \
"ds_read_b96 v[248:250], v252\n" \
"s_waitcnt lgkmcnt(0)\n" \
"v_readfirstlane_b32 s44, v248\n" \
"v_readfirstlane_b32 s45, v249\n" \
"v_readfirstlane_b32 s46, v250\n" \
"s_mul_i32 s42, s25, s46\n" \
"s_lshr_b32 s42, s42, 12\n" \
"s_add_u32 s43, s40, 0\n" \
"s_and_b32 s43, s43, 3\n" \
"s_bitcmp1_b32 s43, 0\n" \
"s_cselect_b32 s47, s32, s31\n" \
"s_lshl_b32 s43, s43, 19\n" \
"s_add_u32 s47, s47, s43\n" \
"s_sub_u32 s43, s24, s44\n" \
"s_add_u32 s43, s43, s28\n" \
"s_max_i32 s43, s43, 0xfffd0000\n" \
"s_min_i32 s43, s43, 0x4000\n" \
"s_mul_i32 s43, s43, s46\n" \
"s_ashr_i32 s43, s43, 12\n" \
"s_add_u32 s41, s45, s43\n" \
"s_branch .LX3_z1\n" \
".LX3_retryz:\n" \
"s_add_u32 s51, s51, 1\n" \
"s_cmp_gt_u32 s51, 0x20000\n" \
"s_cbranch_scc1 .LX3_end\n" \
".LX3_z0:\n" \
"s_add_u32 s41, s41, s42\n" \
"s_add_u32 s24, s24, s25\n" \
"s_xor_b32 s31, s31, s29\n" \
"s_xor_b32 s32, s32, s29\n" \
"s_xor_b32 s47, s47, s29\n" \
"s_xor_b32 s48, s48, s29\n" \
"s_add_u32 s39, s39, 1\n" \
".p2align 6\n" \
".LX3_z1:\n" \
"s_memtime vcc\n" \
"s_waitcnt lgkmcnt(0)\n" \
"s_sub_u32 s43, s41, vcc_lo\n" \
"s_cmp_gt_i32 s43, 128\n" \
"s_cbranch_scc1 .LX3_z1\n" \
"s_cmp_lt_i32 s43, s37\n" \
"s_cbranch_scc1 .LX3_z0\n" \
"s_max_i32 s43, s43, 0\n" \
"s_and_b32 s43, s43, -4\n" \
"s_getpc_b64 vcc\n" \
".LX3_z3:\n" \
"s_sub_u32 s43, .LX3_z4-.LX3_z3, s43\n" \
"s_add_u32 vcc_lo, vcc_lo, s43\n" \
"s_addc_u32 vcc_hi, vcc_hi, 0\n" \
"s_setpc_b64 vcc\n" \
".rept 32\n" \
"s_nop 0\n" \
".endr\n" \
".LX3_z4:\n" \
"buffer_load_dwordx4 v[0:3], v208, s[4:7], s47 offen sc1\n" \
"buffer_load_dwordx4 v[4:7], v208, s[4:7], s47 offen offset:1024 sc1\n" \
"buffer_load_dwordx4 v[8:11], v208, s[4:7], s47 offen offset:2048 sc1\n" \
"buffer_load_dwordx4 v[12:15], v208, s[4:7], s47 offen offset:3072 sc1\n" \
"buffer_load_dwordx4 v[16:19], v209, s[4:7], s47 offen sc1\n" \
"buffer_load_dwordx4 v[20:23], v209, s[4:7], s47 offen offset:1024 sc1\n" \
"buffer_load_dwordx4 v[24:27], v209, s[4:7], s47 offen offset:2048 sc1\n" \
"s_waitcnt vmcnt(0)\n" \
"ds_write_b128 v216, v[0:3] offset:7168\n" \
"ds_write_b128 v216, v[4:7] offset:8192\n" \
"ds_write_b128 v216, v[8:11] offset:9216\n" \
"ds_write_b128 v216, v[12:15] offset:10240\n" \
"ds_write_b128 v216, v[16:19] offset:11264\n" \
"ds_write_b128 v216, v[20:23] offset:12288\n" \
"ds_write_b128 v216, v[24:27] offset:13312\n" \
"v_max3_u32 v84, v0, v0, v1\n" \
"v_max3_u32 v84, v84, v2, v3\n" \
"v_max3_u32 v84, v84, v4, v5\n" \
"v_max3_u32 v84, v84, v6, v7\n" \
"v_max3_u32 v84, v84, v8, v9\n" \
"v_max3_u32 v84, v84, v10, v11\n" \
"v_max3_u32 v84, v84, v12, v13\n" \
"v_max3_u32 v84, v84, v14, v15\n" \
"v_max3_u32 v84, v84, v16, v17\n" \
"v_max3_u32 v84, v84, v18, v19\n" \
"v_max3_u32 v84, v84, v20, v21\n" \
"v_max3_u32 v84, v84, v22, v23\n" \
"v_max3_u32 v84, v84, v24, v25\n" \
"v_max3_u32 v84, v84, v26, v27\n" \
"v_cmp_eq_u32 vcc, -1, v84\n" \
"s_cbranch_vccnz .LX3_retryz\n" \
"s_waitcnt lgkmcnt(0)\n" \
"s_barrier\n" \
"s_barrier\n" \
".LX3_end:\n" \
"s_endpgm\n"
typedef __attribute__((ext_vector_type(16))) float f16v;
DI u4 pack8(const f16v& x, int s, float sc) { return (u4){pk2(x[8 * s] * sc, x[8 * s + 1] * sc), pk2(x[8 * s + 2] * sc, x[8 * s + 3] * sc), pk2(x[8 * s + 4] * sc, x[8 * s + 5] * sc), pk2(x[8 * s + 6] * sc, x[8 * s + 7] * sc)}; }
DI u4 pack8(const f16v& x, int s) { return (u4){pk2(x[8 * s], x[8 * s + 1]), pk2(x[8 * s + 2], x[8 * s + 3]), pk2(x[8 * s + 4], x[8 * s + 5]), pk2(x[8 * s + 6], x[8 * s + 7])}; }
DI void dma_pair(u4 srd, int v0, int v1, int soff, unsigned m) {
    asm volatile("s_mov_b32 m0, %4\n\ts_nop 0\n\tbuffer_load_dwordx4 %0, %2, %3 offen lds\n\tbuffer_load_dwordx4 %1, %2, %3 offen offset:1024 lds" :: "v"(v0), "v"(v1), "s"(srd), "s"(soff), "s"(m) : "memory");
}
DI void dma_one(u4 srd, int v0, int soff, unsigned m) {
    asm volatile("s_mov_b32 m0, %3\n\ts_nop 0\n\tbuffer_load_dwordx4 %0, %1, %2 offen lds" :: "v"(v0), "s"(srd), "s"(soff), "s"(m) : "memory");
}
DI void mV_va(f16v& c, u4 x, u4 y) { asm volatile("v_mfma_f32_32x32x16_bf16 %0, %1, %2, %0" : "+v"(c) : "v"(x), "a"(y)); }
DI void mV_va0(f16v& c, u4 x, u4 y) { asm volatile("v_mfma_f32_32x32x16_bf16 %0, %1, %2, 0" : "=&v"(c) : "v"(x), "a"(y)); }
DI void mV_av(f16v& c, u4 x, u4 y) { asm volatile("v_mfma_f32_32x32x16_bf16 %0, %1, %2, %0" : "+v"(c) : "a"(x), "v"(y)); }
DI void mV_av0(f16v& c, u4 x, u4 y) { asm volatile("v_mfma_f32_32x32x16_bf16 %0, %1, %2, 0" : "=&v"(c) : "a"(x), "v"(y)); }
DI void mV_vv(f16v& c, u4 x, u4 y) { asm volatile("v_mfma_f32_32x32x16_bf16 %0, %1, %2, %0" : "+v"(c) : "v"(x), "v"(y)); }
DI void mV_vv0(f16v& c, u4 x, u4 y) { asm volatile("v_mfma_f32_32x32x16_bf16 %0, %1, %2, 0" : "=&v"(c) : "v"(x), "v"(y)); }
template <int N, int PD, class RD, class MM, class EX> DI void pipe(RD rd, MM mm, EX ex) {
    u4 f[PD];
#pragma unroll
    for (int j = 0; j < PD; ++j) f[j] = rd(j);
    SB();
#pragma unroll
    for (int j = 0; j < N; ++j) { mm(j, f[j % PD]); if (j + PD < N) f[j % PD] = rd(j + PD); ex(j); SB(); }
}
DI u4 sld128(u4 srd, int voff, int soff) { u4 v; asm volatile("s_nop 4\n\tbuffer_load_dwordx4 %0, %1, %2, %3 offen sc1" : "=v"(v) : "v"(voff), "s"(srd), "s"(soff) : "memory"); return v; }
DI u2 sld64(u4 srd, int voff, int soff) { u2 v; asm volatile("s_nop 4\n\tbuffer_load_dwordx2 %0, %1, %2, %3 offen sc1" : "=v"(v) : "v"(voff), "s"(srd), "s"(soff) : "memory"); return v; }
DI void sst128(u4 v, u4 srd, int voff, int soff) { asm volatile("s_nop 4\n\tbuffer_store_dwordx4 %0, %1, %2, %3 offen sc1\n\ts_nop 1" :: "v"(v), "v"(voff), "s"(srd), "s"(soff) : "memory"); }
DI void sst64(u2 v, u4 srd, int voff, int soff) { asm volatile("s_nop 4\n\tbuffer_store_dwordx2 %0, %1, %2, %3 offen sc1" :: "v"(v), "v"(voff), "s"(srd), "s"(soff) : "memory"); }
DI void sst32(unsigned v, u4 srd, int voff, int soff) { asm volatile("s_nop 4\n\tbuffer_store_dword %0, %1, %2, %3 offen sc1" :: "v"(v), "v"(voff), "s"(srd), "s"(soff) : "memory"); }
DI unsigned sld32(u4 srd, int voff, int soff) { unsigned v; asm volatile("s_nop 4\n\tbuffer_load_dword %0, %1, %2, %3 offen sc1" : "=v"(v) : "v"(voff), "s"(srd), "s"(soff) : "memory"); return v; }
template <int N> DI void waitvm() { asm volatile("s_waitcnt vmcnt(%0)" :: "i"(N) : "memory"); }
DI int swzr(int row) { return ((row & 1) << 2) | (row & 8) | ((row >> 1) & 3); }
#define SLICE 32768
#define CSCALE (LOG2E * 0.0625f)

struct SideCtx {
    char LAS* sm; unsigned smb; int lane, wave;
    int vr0, vr1, vc0, vc1;
};
DI int fresh_lane() { int l; asm volatile("v_mbcnt_lo_u32_b32 %0, -1, 0\n\tv_mbcnt_hi_u32_b32 %0, -1, %0" : "=v"(l)); __builtin_assume(l >= 0 && l < 64); return l; }
DI SideCtx make_ctx(int wave) {
    SideCtx c; c.sm = (char LAS*)smem_; c.smb = (unsigned)(size_t)c.sm; c.lane = fresh_lane(); c.wave = wave;
    asm volatile("" : "+s"(c.wave), "+s"(c.smb));
    const int l = c.lane;
    { int row = 4 * c.wave + (l >> 5); c.vr0 = row * 8192 + (((l & 31) ^ swzr(row)) * 16); row += 2; c.vr1 = row * 8192 + (((l & 31) ^ swzr(row)) * 16) - 1024; }
    { int row = l >> 3; c.vc0 = row * 896 + (((l & 7) ^ (((row >> 1) & 1) << 2)) * 16); c.vc1 = c.vc0 + 8 * 896 - 1024; }
    return c;
}
DI void dma_rows_q(const SideCtx& c, u4 srd, int h, int r0, int slot, int g) { dma_pair(srd, c.vr0, c.vr1, (r0 + 16 * g) * 8192 + h * 512, c.smb + slot * SLICE + (8 * g + 2 * c.wave) * 1024); }
DI void dma_cols_q(const SideCtx& c, u4 srd, int h, int c0, int slot, int g) { const int k = 8 * c.wave + 2 * g; dma_pair(srd, c.vc0, c.vc1, (h * 256 + 8 * k) * 896 + c0 * 2, c.smb + slot * SLICE + k * 1024); }
DI void dma_rows(const SideCtx& c, u4 srd, int h, int r0, int slot) {
#pragma unroll
    for (int g = 0; g < 4; ++g) dma_rows_q(c, srd, h, r0, slot, g);
}
DI void dma_cols(const SideCtx& c, u4 srd, int h, int c0, int slot) {
#pragma unroll
    for (int g = 0; g < 4; ++g) dma_cols_q(c, srd, h, c0, slot, g);
}
DI u4 rd_rows(const SideCtx& c, int slot, int ks, int tau) {
    const int l = c.lane, hi = l >> 5, ro = l & 31;
    const int rowc = 32 * (ro >> 4) + 4 * ((ro >> 2) & 1) + 8 * ((ro >> 1) & 1) + 2 * ((ro >> 3) & 1) + (ro & 1);
    const int ch = (2 * (ks & 7) + hi) ^ swzr(rowc);
    return *(const u4 LAS*)(c.sm + (rowc * 512 + ch * 16) + (slot * SLICE + tau * 8192 + (ks >> 3) * 256));
}
DI u4 tr_cols(const SideCtx& c, int slot, int ks, int nt) {
    const int l = c.lane, hi = l >> 5, blk = (l >> 4) & 1, r = (l & 15) >> 2, p = l & 3;
    u2 o[2];
#pragma unroll
    for (int t = 0; t < 2; ++t) {
        const int ch = (4 * nt + 2 * blk + (p >> 1)) ^ (((r >> 1) & 1) << 2);
        o[t] = trread(c.sm + ((8 * hi + r) * 128 + ch * 16 + 8 * (p & 1)) + (slot * SLICE + ks * 2048 + t * 512));
    }
    return (u4){o[0][0], o[0][1], o[1][0], o[1][1]};
}
#define PF_AHEAD 12
#define NSEG1 8
DI void set_flag(const SideCtx& c, u4 rhb, int off, unsigned val) {
    waitvm<0>(); bar();
    if (c.wave == 0) sst32(val, rhb, c.lane == 0 ? off : OOB, 0);
}
DI void wait_prog(u4 rhb, int voff_or_oob, unsigned want) {
    unsigned sp = 0;
#pragma nounroll
    for (;;) {
        unsigned v = sld32(rhb, voff_or_oob, 0); WAITV(0, "+v"(v));
        if (__all((v - want < 256u) | (voff_or_oob >= OOB))) break;
        asm volatile("s_sleep 4" ::: "memory");
        if (++sp > SPIN_MAX) __builtin_amdgcn_endpgm();
    }
}
#define MFW32(x, y) asm volatile("s_nop 11" : "+v"(x), "+v"(y))
DI int perm16(int r) { return 4 * ((r >> 2) & 1) + 8 * ((r >> 1) & 1) + 2 * ((r >> 3) & 1) + (r & 1); }
#define CHUNK(u) ((u) == 6 ? 3 : ((u) >> 1) + 4 * ((u) & 1))
DI void w_first(int wave, int type, int h, int sub, int part) {
    CArgs& a = get_args();
    const SideCtx c = make_ctx(wave);
    h = __builtin_amdgcn_readfirstlane(h); sub = __builtin_amdgcn_readfirstlane(sub); asm volatile("" : "+s"(h), "+s"(sub));
    const u4 rw1 = mksrd(type ? a.Wv : a.Wk, 448 * 8192), rw2 = mksrd(type ? a.Wo : a.Wq, 448 * 8192);
    const int c1 = sub == 3 ? 384 : sub * 128 + 64;
    if (part) { dma_rows(c, rw1, h, 64 * CHUNK(0), 0); dma_rows(c, rw1, h, 64 * CHUNK(1), 1); }
    else if (type) { dma_cols(c, rw2, h, sub * 128, 2); dma_cols(c, rw2, h, c1, 3); }
    else { dma_rows(c, rw2, h, sub * 128, 2); dma_rows(c, rw2, h, c1, 3); }
}
template <int N> struct IntC { static constexpr int value = N; };
template <int TYPE>
DI void wgu_task(unsigned tag, int vb, int wave, int h, int sub) {
    CArgs& a = get_args();
    const SideCtx c = make_ctx(wave);
    const int lane = c.lane, hi = lane >> 5, l31 = lane & 31, w = c.wave;
    h = __builtin_amdgcn_readfirstlane(h); sub = __builtin_amdgcn_readfirstlane(sub); asm volatile("" : "+s"(h), "+s"(sub));
    const u4 rw1 = mksrd(TYPE ? a.Wv : a.Wk, 448 * 8192), rx = mksrd(a.X, 2048 * 896), rhb = mksrd(a.HB, HB_SZ);
    const int o_fl = (w == 0 && lane == 0) ? O_FW + ((h * 2 + TYPE) * 4 + sub) * 128 : OOB;
    int xo[7];
#pragma unroll
    for (int k = 0; k < 7; ++k) { const int n = 64 * (7 * (w & 1) + k) + lane, row = (n * 1171) >> 16, pos = n - 56 * row; xo[k] = (row + 16 * (w >> 1)) * 896 + ((pos ^ ((row >> 1) & 7)) * 16); }
    auto xdma = [&](int s, int reg, int k) { dma_one(rx, xo[k], s * 28672, c.smb + reg + (7 * w + k) * 1024); };
    const int m = sub * 4 + w;
    u4 wk[28];
    f16v d[2][2];
    {
        u4 kf[16];
        const int rowp = 16 * (l31 >> 4) + perm16(l31 & 15);
#pragma unroll
        for (int ks = 0; ks < 16; ++ks)
            kf[ks] = TYPE ? tr_cols(c, 2 + (w >> 1), ks, w & 1) : *(const u4 LAS*)(c.sm + 2 * SLICE + w * 16384 + (rowp * 512 + ((2 * (ks & 7) + hi) ^ swzr(rowp)) * 16) + (ks >> 3) * 256);
        bar();
        unsigned pf[3];
#pragma unroll
        for (int i = 0; i < 3; ++i) asm volatile("buffer_load_dword %0, %1, %2, %3 offen" : "=v"(pf[i]) : "v"(lane < 7 ? (vb >> 3) * 896 + lane * 128 : OOB), "s"(rx), "s"((4 * i + w) * 28672) : "memory");
        dma_rows(c, rw1, h, 64 * CHUNK(2), 2); dma_rows(c, rw1, h, 64 * CHUNK(3), 3); dma_rows(c, rw1, h, 64 * CHUNK(4), 4);
        const u4 rout = mksrd(TYPE ? a.WvoF : a.WqkF, 28 * 16 * 14336);
        const int t16 = 2 * m + (l31 >> 4);
        const int o_st = m > 13 ? OOB : (TYPE ? t16 * 16 + h : h * 28 + t16) * 14336 + (hi * 16 + (TYPE ? l31 & 15 : perm16(l31 & 15))) * 16;
        u4 p[4], pn; unsigned ts[7];
        auto epi = [&](int v, int e, int st) __attribute__((always_inline)) {
            const f16v& x = d[v & 1][e & 1]; const int o = 8 * (e >> 1) + 4 * st; const float sc = TYPE ? 1.f : CSCALE;
            if (st < 2) { p[e][2 * st] = pk2(x[o] * sc, x[o + 1] * sc); p[e][2 * st + 1] = pk2(x[o + 2] * sc, x[o + 3] * sc); asm volatile("" : "+v"(p[e][2 * st]), "+v"(p[e][2 * st + 1])); }
            else if (st == 2) st128(p[e], rout, o_st, (CHUNK(v) * 2 + (e >> 1)) * 1024 + (e & 1) * 512);
            else if (st == 3) { const auto s0 = __builtin_amdgcn_permlane32_swap(p[e][0], p[e][1], false, false), s1 = __builtin_amdgcn_permlane32_swap(p[e][2], p[e][3], false, false);
                                pn = (u4){s0[0], s1[0], s0[1], s1[1]}; PIN(pn); }
            else { wk[4 * CHUNK(v) + e] = pn; asm volatile("" : "+a"(wk[4 * CHUNK(v) + e])); }
        };
#pragma unroll
        for (int u = 0; u < 7; ++u) {
            if (u == 0) waitvm<35>(); else if (u == 1) waitvm<27>(); else if (u == 2) waitvm<28>(); else if (u == 3) waitvm<20>(); else if (u == 4) waitvm<13>(); else waitvm<7>();
            if (u == 2) asm volatile("" :: "v"(pf[0]), "v"(pf[1]), "v"(pf[2]));
            bar(); ts[u] = TNOW();
            if (u >= 3) st32(tag + (u < 5 ? u - 2 : u - 1), rhb, o_fl, 0);
            pipe<32, 4>([&](int j) { return rd_rows(c, u % 5, j >> 1, j & 1); },
                        [&](int j, u4 f) { if (j < 2) mV_vv0(d[u & 1][j & 1], f, kf[j >> 1]); else mV_vv(d[u & 1][j & 1], f, kf[j >> 1]); },
                        [&](int j) { if (u > 0 && j >= 2 && j < 14) { if (j == 2) asm volatile("s_nop 3" : "+v"(d[(u - 1) & 1][0]), "+v"(d[(u - 1) & 1][1])); epi(u - 1, (j - 2) / 3, (j - 2) % 3); }
                                     if (j < 14 || j >= 30) return;
                                     const int g = (j - 14) >> 1;
                                     if (j & 1) { if (u > 0) epi(u - 1, g >> 1, 3 + (g & 1)); }
                                     else if (u == 1 || u == 2) dma_one(rw1, (g & 1) ? c.vr1 + 1024 : c.vr0, (64 * CHUNK(u + 4) + 16 * (g >> 1)) * 8192 + h * 512, c.smb + (u - 1) * SLICE + (8 * (g >> 1) + 2 * w + (g & 1)) * 1024);
                                     else if (u >= 4 && g < 7) xdma(u - 4, (u - 2) * SLICE, g); });
        }
        MFW32(d[0][0], d[0][1]);
#pragma unroll
        for (int e = 0; e < 12; ++e) epi(6, e / 3, e % 3);
#pragma unroll
        for (int e = 0; e < 8; ++e) epi(6, e >> 1, 3 + (e & 1));
    }
    const u4 rout = mksrd(TYPE ? a.UF : a.GF, 64 * 16 * 28 * 1024);
    auto o_out = [&](int mm) { return mm > 13 ? OOB : h * 28672 + (TYPE ? (2 * mm + (l31 >> 4)) * 1024 : mm * 1024 + (l31 >> 4) * 14336) + (hi * 16 + (l31 & 15)) * 16; };
    const int o_st = o_out(m);
    const char LAS* xb[4];
#pragma unroll
    for (int k = 0; k < 4; ++k) xb[k] = c.sm + l31 * 896 + ((2 * k + hi) ^ ((l31 >> 1) & 7)) * 16;
    const int o_pf = (w == 1 && lane < 56) ? (vb >> 3) * 896 + lane * 16 : OOB;
    unsigned ring = 0x10432;
    auto mm1 = [&](f16v& acc, const u4& wt, u4 f, bool first) __attribute__((always_inline)) {
        if (TYPE) { if (first) mV_va0(acc, f, wt); else mV_va(acc, f, wt); } else { if (first) mV_av0(acc, wt, f); else mV_av(acc, wt, f); } };
    u4 ph;
    auto half = [&](const f16v& x, int x8, int st, int vo, int so) __attribute__((always_inline)) {
        if (st == 0) { ph = pack8(x, x8); PIN(ph); } else st128(ph, rout, vo, so + x8 * 512); };
    auto seg = [&](auto first_, auto par_, int i) __attribute__((always_inline)) {
        constexpr int FI = decltype(first_)::value, PAR = decltype(par_)::value;
        if (FI == 0) waitvm<11>(); else if (FI == 1) waitvm<9>(); else waitvm<7>();
        bar();
        st32(tag + 6 + i, rhb, o_fl, 0);
        dma_one(rx, o_pf, (i + PF_AHEAD) * 28672, c.smb + 4 * SLICE + 28672 + w * 1024);
        const int r0 = (ring & 7) * SLICE, r3 = ((ring >> 12) & 7) * SLICE;
        ring = (ring >> 4) | ((ring & 7) << 16);
        pipe<28, 4>([&](int k) { return *(const u4 LAS*)(xb[k & 3] + r0 + (k >> 2) * 128); },
                    [&](int k, u4 f) { mm1(d[PAR][0], wk[k], f, k == 0); },
                    [&](int k) { if (FI != 0 && k >= 1 && k < 5) { if (k == 1) asm volatile("s_nop 5" : "+v"(d[PAR ^ 1][0])); half(d[PAR ^ 1][0], (k - 1) >> 1, (k - 1) & 1, o_st, (i - 1) * 458752); }
                                 if (k >= 6 && k % 3 == 0 && k < 27) xdma(i + 3, r3, k / 3 - 2); });
    };
    seg(IntC<0>{}, IntC<0>{}, 0); seg(IntC<1>{}, IntC<1>{}, 1);
#pragma nounroll
    for (int i = 2; i < NSEG1; i += 2) { seg(IntC<2>{}, IntC<0>{}, i); seg(IntC<2>{}, IntC<1>{}, i + 1); }
    asm volatile("s_nop 11" : "+v"(d[1][0]));
    sst128(pack8(d[1][0], 0), rout, o_st, (NSEG1 - 1) * 458752); sst128(pack8(d[1][0], 1), rout, o_st, (NSEG1 - 1) * 458752 + 512);
    if (sub & 1) { set_flag(c, rhb, O_FW + ((h * 2 + TYPE) * 4 + sub) * 128, tag + 7 + NSEG); return; }
    wait_prog(rhb, lane < 2 ? O_FW + ((h * 2 + TYPE) * 4 + 1 + 2 * lane) * 128 : OOB, tag + 7);
    bar();
    if (w == 0) sst32(tag + 7 + NSEG1, rhb, o_fl, 0);
    const int pfo = lane < 56 ? ((vb >> 3 & 7) + (vb >> 7) * 8) * 1792 + lane * 16 : OOB;
    if (w == 3) {
        int xp[7];
#pragma unroll
        for (int k = 0; k < 7; ++k) { const int n = 64 * k + lane, row = (n * 1171) >> 16, pos = n - 56 * row; xp[k] = row * 896 + ((pos ^ ((row >> 1) & 7)) * 16); xo[k] -= 16 * 896; }
        auto segl = [&](auto first_, auto par_, int i) __attribute__((always_inline)) {
            constexpr int FI = decltype(first_)::value, PAR = decltype(par_)::value;
            waitvm<30>();
            bar();
            const int r0 = (ring & 7) * SLICE, r3 = ((ring >> 12) & 7) * SLICE;
            ring = (ring >> 4) | ((ring & 7) << 16);
            pipe<28, 4>([&](int k) { return *(const u4 LAS*)(xb[k & 3] + r0 + (k >> 2) * 128); },
                        [&](int k, u4 f) { mm1(d[PAR][0], wk[k], f, k == 0); },
                        [&](int k) { if (k == 0) { if (FI) { asm volatile("s_nop 9" : "+v"(d[PAR ^ 1][0])); st128(pack8(d[PAR ^ 1][0], 0), rout, o_st, (i - 1) * 458752); st128(pack8(d[PAR ^ 1][0], 1), rout, o_st, (i - 1) * 458752 + 512); }
                                                   dma_pair(rx, pfo, pfo - 128, (i + PF_AHEAD) * 28672, c.smb + 4 * SLICE + 28672); }
                                     dma_one(rx, (k / 7) & 1 ? xo[k % 7] : xp[k % 7], (i + 3) * 28672 + (k / 14) * 14336, c.smb + r3 + k * 1024); });
        };
        segl(IntC<0>{}, IntC<0>{}, NSEG1); segl(IntC<1>{}, IntC<1>{}, NSEG1 + 1);
#pragma nounroll
        for (int i = NSEG1 + 2; i < NSEG; i += 2) { segl(IntC<1>{}, IntC<0>{}, i); segl(IntC<1>{}, IntC<1>{}, i + 1); }
        asm volatile("s_nop 11" : "+v"(d[1][0]));
    } else {
        const int m2 = sub == 0 ? 4 + w : w == 2 ? 7 : 12 + w, t16 = 2 * m2 + (l31 >> 4);
        const int o_s2 = o_out(m2);
        u4 w2[28];
        {
            const u4 rin = mksrd(TYPE ? a.WvoF : a.WqkF, 28 * 16 * 14336);
            const int o_in = (TYPE ? t16 * 16 + h : h * 28 + t16) * 14336 + (hi * 16 + (TYPE ? l31 & 15 : perm16(l31 & 15))) * 16;
#pragma unroll
            for (int k = 0; k < 28; ++k) w2[k] = sld128(rin, o_in, k * 512);
            waitvm<0>();
#pragma unroll
            for (int k = 0; k < 28; ++k) {
                PIN(w2[k]);
                const auto s0 = __builtin_amdgcn_permlane32_swap(w2[k][0], w2[k][1], false, false), s1 = __builtin_amdgcn_permlane32_swap(w2[k][2], w2[k][3], false, false);
                w2[k] = (u4){s0[0], s1[0], s0[1], s1[1]};
                asm volatile("" : "+a"(w2[k]));
            }
        }
        u4 fr[4];
#pragma unroll
        for (int k = 0; k < 4; ++k) fr[k] = *(const u4 LAS*)(xb[k] + (ring & 7) * SLICE);
        auto seg2 = [&](auto first_, auto par_, int i) __attribute__((always_inline)) {
            constexpr int FI = decltype(first_)::value, PAR = decltype(par_)::value;
            const int r0 = (ring & 7) * SLICE, r1 = ((ring >> 4) & 7) * SLICE;
            ring = (ring >> 4) | ((ring & 7) << 16);
#pragma unroll
            for (int k = 0; k < 28; ++k) {
                mm1(d[PAR][0], wk[k], fr[k & 3], k == 0);
                if (k == 0) { asm volatile("s_waitcnt vmcnt(0)\n\ts_barrier" ::: "memory"); st32(tag + (FI ? 6 + i : 7 + NSEG1), rhb, o_fl, 0); }
                if (FI && k >= 3 && k < 11) { if (k == 3) asm volatile("" : "+v"(d[PAR ^ 1][0]), "+v"(d[PAR ^ 1][1])); half(d[PAR ^ 1][k >= 7], ((k - 3) >> 1) & 1, (k - 3) & 1, k >= 7 ? o_s2 : o_st, (i - 1) * 458752); }
                SB();
                mm1(d[PAR][1], w2[k], fr[k & 3], k == 0);
                fr[k & 3] = *(const u4 LAS*)(xb[k & 3] + (k < 24 ? r0 + ((k + 4) >> 2) * 128 : r1));
                SB();
            }
        };
        seg2(IntC<0>{}, IntC<0>{}, NSEG1); seg2(IntC<1>{}, IntC<1>{}, NSEG1 + 1);
#pragma nounroll
        for (int i = NSEG1 + 2; i < NSEG; i += 2) { seg2(IntC<1>{}, IntC<0>{}, i); seg2(IntC<1>{}, IntC<1>{}, i + 1); }
        MFW32(d[1][0], d[1][1]);
        sst128(pack8(d[1][1], 0), rout, o_s2, 63 * 458752); sst128(pack8(d[1][1], 1), rout, o_s2, 63 * 458752 + 512);
    }
    sst128(pack8(d[1][0], 0), rout, o_st, 63 * 458752); sst128(pack8(d[1][0], 1), rout, o_st, 63 * 458752 + 512);
    set_flag(c, rhb, O_FW + ((h * 2 + TYPE) * 4 + sub) * 128, tag + 7 + NSEG);
}

DI void x_image(const SideCtx& c, u4 rx, int s) {
#pragma unroll
    for (int k = 0; k < 7; ++k) {
        const int n = 64 * (7 * (c.wave & 1) + k) + c.lane, row = (n * 1171) >> 16, pos = n - 56 * row;
        dma_one(rx, row * 896 + ((pos ^ ((row >> 1) & 7)) * 16), (s * 32 + 16 * (c.wave >> 1)) * 896, c.smb + 40960 + (7 * c.wave + k) * 1024);
    }
}
DI void e_task(unsigned tag, int wave, int s, int hg) {
    CArgs& a = get_args();
    const SideCtx c = make_ctx(wave);
    const int lane = c.lane, hi = lane >> 5, l31 = lane & 31;
    s = __builtin_amdgcn_readfirstlane(s); hg = __builtin_amdgcn_readfirstlane(hg); asm volatile("" : "+s"(s), "+s"(hg));
    const int sh = s * 16 + 4 * hg + c.wave;
    const u4 rhb = mksrd(a.HB, HB_SZ), rg = mksrd(a.GF, 64 * 16 * 28 * 1024);
    bar();
    x_image(c, mksrd(a.X, 2048 * 896), s);
    wait_prog(rhb, lane < 4 ? O_FW + ((4 * hg + c.wave) * 8 + lane) * 128 : OOB, tag + 8 + s);
    u4 g[28];
    const int o_g = (l31 >> 4) * 14336 + (hi * 16 + (l31 & 15)) * 16;
#pragma unroll
    for (int k = 0; k < 28; ++k) asm volatile("buffer_load_dwordx4 %0, %1, %2, %3 offen sc1" : "=a"(g[k]) : "v"(o_g), "s"(rg), "s"(sh * 28672 + k * 512) : "memory");
    bar();
    f16v sx;
    pipe<28, 4>([&](int k) { const char LAS* b = c.sm + 40960 + l31 * 896 + hi * 8 + (k >> 2) * 128;
                             const u2 c0 = *(const u2 LAS*)(b + ((2 * (k & 3)) ^ ((l31 >> 1) & 7)) * 16), c1 = *(const u2 LAS*)(b + ((2 * (k & 3) + 1) ^ ((l31 >> 1) & 7)) * 16);
                             return (u4){c0[0], c0[1], c1[0], c1[1]}; },
                [&](int k, u4 f) { if (k == 0) WAITV(21, "+a"(g[0]), "+a"(g[1]), "+a"(g[2]), "+a"(g[3]), "+a"(g[4]), "+a"(g[5]), "+a"(g[6]));
                                   if (k == 7) WAITV(14, "+a"(g[7]), "+a"(g[8]), "+a"(g[9]), "+a"(g[10]), "+a"(g[11]), "+a"(g[12]), "+a"(g[13]));
                                   if (k == 14) WAITV(7, "+a"(g[14]), "+a"(g[15]), "+a"(g[16]), "+a"(g[17]), "+a"(g[18]), "+a"(g[19]), "+a"(g[20]));
                                   if (k == 21) WAITV(0, "+a"(g[21]), "+a"(g[22]), "+a"(g[23]), "+a"(g[24]), "+a"(g[25]), "+a"(g[26]), "+a"(g[27]));
                                   u4 y = {f[0], f[2], f[1], f[3]}; asm volatile("s_nop 1" : "+v"(y));
                                   if (k == 0) mV_av0(sx, g[k], y); else mV_av(sx, g[k], y); },
                [&](int) {});
    asm volatile("s_nop 11" : "+v"(sx));
    float m = -3.0e38f;
#pragma unroll
    for (int j = 0; j < 16; ++j) { const int key = (j & 3) + 8 * (j >> 2) + 4 * hi; if (key > l31) sx[j] = -INFINITY; m = fmaxf(m, sx[j]); }
    m = x32max(m);
    float z = 0.f;
#pragma unroll
    for (int j = 0; j < 16; ++j) { sx[j] = __builtin_amdgcn_exp2f(sx[j] - m); z += sx[j]; }
    z = x32add(z);
    const u4 rex = mksrd(a.EX, 64 * 16 * 2048), rmz = mksrd(a.MZX, 64 * 16 * 256);
    sst128(pack8(sx, 0), rex, lane * 16, sh * 2048); sst128(pack8(sx, 1), rex, lane * 16, sh * 2048 + 1024);
    sst64((u2){__float_as_uint(m), __float_as_uint(z)}, rmz, lane < 32 ? lane * 8 : OOB, sh * 256);
    set_flag(c, rhb, O_FE + (s * 4 + hg) * 4, tag + 1);
}
#define P_XSLP 0
DI void slp(int n) {
#pragma nounroll
    for (int i = 0; i < n; i += 8) asm volatile("s_sleep 8");
}
DI void x_task(unsigned tag, int wave, int s, int cb) {
    CArgs& a = get_args();
    const SideCtx c = make_ctx(wave);
    const int lane = c.lane, hi = lane >> 5, l31 = lane & 31, w = c.wave;
    s = __builtin_amdgcn_readfirstlane(s); cb = __builtin_amdgcn_readfirstlane(cb); asm volatile("" : "+s"(s), "+s"(cb));
    const u4 rhb = mksrd(a.HB, HB_SZ);
    const u4 ru = mksrd(a.UF, 64 * 16 * 28 * 1024), rex = mksrd(a.EX, 64 * 16 * 2048), rmz = mksrd(a.MZX, 64 * 16 * 256);
    const u4 rum = mksrd(a.UM, 65 * 28 * 4096), rtf = mksrd(a.TF, 65 * 32 * 3584), rx = mksrd(a.X, 2048 * 896);
    bar();
    x_image(c, rx, s);
    wait_prog(rhb, lane < 16 ? O_FW + (((4 * w + (lane >> 2)) * 2 + 1) * 4 + (lane & 3)) * 128 : lane == 16 ? O_FE + (s * 4 + w) * 4 : OOB, lane < 16 ? tag + 8 + s : tag + 1);
    u4 uf[4][2][2], ex[4][2]; u2 mz[4];
    const int o_u = (l31 >> 4) * 1024 + hi * 256 + (l31 & 15) * 16;
#pragma unroll
    for (int k = 0; k < 4; ++k) {
        const int sh = s * 16 + 4 * w + k;
#pragma unroll
        for (int sg = 0; sg < 2; ++sg) {
            ex[k][sg] = sld128(rex, lane * 16, sh * 2048 + sg * 1024);
#pragma unroll
            for (int nt = 0; nt < 2; ++nt) uf[k][sg][nt] = sld128(ru, o_u, (sh * 28 + 4 * cb + 2 * nt) * 1024 + sg * 512);
        }
        mz[k] = sld64(rmz, l31 * 8, sh * 256);
        asm volatile("s_sleep %0" :: "i"(P_XSLP));
    }
    waitvm<0>();
    f16v rr[4][2];
#pragma unroll
    for (int k = 0; k < 4; ++k) {
        asm volatile("" : "+v"(mz[k]), "+v"(ex[k][0]), "+v"(ex[k][1]), "+v"(uf[k][0][0]), "+v"(uf[k][0][1]), "+v"(uf[k][1][0]), "+v"(uf[k][1][1]));
        mV_vv0(rr[k][0], uf[k][0][0], ex[k][0]); mV_vv0(rr[k][1], uf[k][0][1], ex[k][0]);
        mV_vv(rr[k][0], uf[k][1][0], ex[k][1]); mV_vv(rr[k][1], uf[k][1][1], ex[k][1]);
        MFW32(rr[k][0], rr[k][1]);
    }
    bar();
    wait_prog(rhb, lane < 8 ? O_FT + (8 * w + lane) * 128 : lane < 12 ? O_FT + (24 + 4 * cb + lane) * 128 : OOB, tag + s + 1);
    u4 tf[28]; u4 um[2][2];
    const int o_tf = (l31 >> 3) * 7168 + hi * 128 + (l31 & 7) * 16, o_um = (l31 >> 4) * 4096 + hi * 256 + (l31 & 15) * 16;
#pragma unroll
    for (int k = 0; k < 28; ++k) {
        asm volatile("buffer_load_dwordx4 %0, %1, %2, %3 offen sc1" : "=a"(tf[k]) : "v"(o_tf), "s"(rtf), "s"(((s + 1) * 32 + 8 * w) * 3584 + k * 256) : "memory");
        if (k % 7 == 6) slp(s < 58 ? P_XSLP : 0);
    }
#pragma unroll
    for (int k = 0; k < 4; ++k) um[k >> 1][k & 1] = ld128(rum, o_um, (s * 28 + 28 + 4 * cb + 2 * (k & 1)) * 4096 + w * 1024 + (k >> 1) * 512);
    f16v sm_;
    pipe<28, 4>([&](int k) { return *(const u4 LAS*)(c.sm + 40960 + l31 * 896 + (((2 * (k & 3) + hi) ^ ((l31 >> 1) & 7)) * 16) + (k >> 2) * 128); },
                [&](int k, u4 f) { if (k == 0) WAITV(25, "+a"(tf[0]), "+a"(tf[1]), "+a"(tf[2]), "+a"(tf[3]), "+a"(tf[4]), "+a"(tf[5]), "+a"(tf[6]));
                                   if (k == 7) WAITV(18, "+a"(tf[7]), "+a"(tf[8]), "+a"(tf[9]), "+a"(tf[10]), "+a"(tf[11]), "+a"(tf[12]), "+a"(tf[13]));
                                   if (k == 14) WAITV(11, "+a"(tf[14]), "+a"(tf[15]), "+a"(tf[16]), "+a"(tf[17]), "+a"(tf[18]), "+a"(tf[19]), "+a"(tf[20]));
                                   if (k == 21) WAITV(4, "+a"(tf[21]), "+a"(tf[22]), "+a"(tf[23]), "+a"(tf[24]), "+a"(tf[25]), "+a"(tf[26]), "+a"(tf[27]));
                                   if (k == 0) mV_av0(sm_, tf[k], f); else mV_av(sm_, tf[k], f); },
                [&](int) {});
    waitvm<0>();
    asm volatile("s_nop 11" : "+v"(sm_));
#pragma unroll
    for (int k = 0; k < 4; ++k) PIN(um[k >> 1][k & 1]);
    u2 pm[4]; float scx[4];
#pragma unroll
    for (int k = 0; k < 4; ++k) {
        const f4 sc = {sm_[4 * k], sm_[4 * k + 1], sm_[4 * k + 2], sm_[4 * k + 3]};
        const float mx = __uint_as_float(mz[k][0]);
        float m = fmaxf(x32max(fmaxf(fmaxf(sc[0], sc[1]), fmaxf(sc[2], sc[3]))), mx);
        float e[4];
#pragma unroll
        for (int j = 0; j < 4; ++j) e[j] = __builtin_amdgcn_exp2f(sc[j] - m);
        const float cx = __builtin_amdgcn_exp2f(mx - m);
        const float z = x32add((e[0] + e[1]) + (e[2] + e[3])) + cx * __uint_as_float(mz[k][1]);
        const float inv = __builtin_amdgcn_rcpf(z);
        scx[k] = cx * inv;
        pm[k] = (u2){pk2(e[0] * inv, e[1] * inv), pk2(e[2] * inv, e[3] * inv)};
    }
    f16v d0, d1;
    {
        u4 p0 = {pm[0][0], pm[0][1], pm[2][0], pm[2][1]}, p1 = {pm[1][0], pm[1][1], pm[3][0], pm[3][1]};
        asm volatile("s_nop 1\n\tv_mfma_f32_32x32x16_bf16 %0, %2, %6, 0\n\tv_mfma_f32_32x32x16_bf16 %1, %3, %6, 0\n\tv_mfma_f32_32x32x16_bf16 %0, %4, %7, %0\n\tv_mfma_f32_32x32x16_bf16 %1, %5, %7, %1"
                     : "=&v"(d0), "=&v"(d1) : "v"(um[0][0]), "v"(um[0][1]), "v"(um[1][0]), "v"(um[1][1]), "v"(p0), "v"(p1));
    }
    f16v a0 = (rr[0][0] * scx[0] + rr[1][0] * scx[1]) + (rr[2][0] * scx[2] + rr[3][0] * scx[3]);
    f16v a1 = (rr[0][1] * scx[0] + rr[1][1] * scx[1]) + (rr[2][1] * scx[2] + rr[3][1] * scx[3]);
    asm volatile("s_nop 11" : "+v"(d0), "+v"(d1), "+v"(a0), "+v"(a1));
    a0 += d0; a1 += d1;
#pragma unroll
    for (int g = 0; g < 4; ++g) {
        *(f4 LAS*)(c.sm + w * 8704 + l31 * 272 + (8 * g + 4 * hi) * 4) = (f4){a0[4 * g], a0[4 * g + 1], a0[4 * g + 2], a0[4 * g + 3]};
        *(f4 LAS*)(c.sm + w * 8704 + l31 * 272 + 128 + (8 * g + 4 * hi) * 4) = (f4){a1[4 * g], a1[4 * g + 1], a1[4 * g + 2], a1[4 * g + 3]};
    }
    bar();
    {
        const int t = w * 8 + (lane >> 3), c8 = lane & 7;
        f4 s0 = {0.f, 0.f, 0.f, 0.f}, s1 = s0;
#pragma unroll
        for (int p = 0; p < 4; ++p) { s0 += *(const f4 LAS*)(c.sm + p * 8704 + t * 272 + c8 * 32); s1 += *(const f4 LAS*)(c.sm + p * 8704 + t * 272 + c8 * 32 + 16); }
        sst128((u4){pk2(s0[0], s0[1]), pk2(s0[2], s0[3]), pk2(s1[0], s1[1]), pk2(s1[2], s1[3])}, mksrd(a.out, 2048 * 1792), t * 1792 + c8 * 16, s * 32 * 1792 + 896 + cb * 128);
    }
}

#define P_ASLP 8
#define PA_UT 0
#define PA_TU 33792
#define PA_KH (PA_TU + 35456)
#define PA_KL (PA_KH + 7680)
#define PA_KT (PA_KL + 7680)
#define PA_RED (PA_KT + 12288)
DI float bf2f(u16 x) { return __uint_as_float((unsigned)x << 16); }
DI void parta_task(unsigned tag, int wave, int task) {
    CArgs& a = get_args();
    const SideCtx c = make_ctx(wave);
    char LAS* sm = c.sm;
    const int lane = c.lane, w = c.wave, tid = w * 64 + lane, hi = lane >> 5, l31 = lane & 31, l15 = lane & 15, kk = lane >> 4;
    task = __builtin_amdgcn_readfirstlane(task); asm volatile("" : "+s"(task));
    const int d0 = task * 4;
    const u4 rpa = mksrd(a.PA, 56 * 32768);
    bar();
#pragma unroll
    for (int i = 0; i < 8; ++i) {
        const int l = tid + 256 * i;
        const u2 v = *(const u2*)(a.Au + (size_t)l * 448 + d0);
#pragma unroll
        for (int ch = 0; ch < 4; ++ch) *(u16 LAS*)(sm + PA_UT + ch * 4224 + (2048 - l) * 2) = (u16)(v[ch >> 1] >> (16 * (ch & 1)));
    }
    { const int i = tid, ch = i >> 6, m = i & 63; *(u16 LAS*)(sm + PA_UT + ch * 4224 + (m == 0 ? 0 : (2048 + m) * 2)) = 0; }
    for (int i = tid; i < 2 * 31 * 5; i += 256) { const int x = i / 155, r = i % 155; *(u4 LAS*)(sm + (x ? PA_KL : PA_KH) + r * 16) = (u4){0u, 0u, 0u, 0u}; }
    int toff[8];
#pragma unroll
    for (int j = 0; j < 8; ++j) {
        const int p = (int)bfr((float)(tid * 8 + j));
        int col, ar = (p & 255) >> 4;
        if (p < 256) col = p & 15; else if (p < 512) col = 16 + ((p & 15) >> 1); else if (p < 1024) col = 24 + ((p - 512) >> 8) * 4 + ((p & 15) >> 2);
        else if (p < 2048) col = 32 + ((p - 1024) >> 8) * 2 + ((p & 15) >> 3); else col = 40;
        toff[j] = (col * 16 + ar) * 4;
    }
    float qpos[3];
    qpos[0] = (float)l15;
    qpos[1] = l15 < 8 ? (float)(256 + 2 * l15) : (float)(512 + ((l15 - 8) >> 2) * 256 + (l15 & 3) * 4);
    qpos[2] = l15 < 8 ? (float)(1024 + (l15 >> 1) * 256 + (l15 & 1) * 8) : 2048.f;
    const float ppos = (float)(16 * l15);
    {
        {
            const int d = d0 + w;
            const float dt = bfr(expf(bf2f(a.Alogdt[d])));
            char LAS* pt = sm + PA_RED + w * 9216;
#pragma unroll
            for (int i = 0; i < 4; ++i) {
                const int n = lane + 64 * i;
                const float ar = -bfr(expf(bf2f(a.AlogA[d * 256 + n])));
                *(f4 LAS*)(pt + n * 16) = (f4){ar * dt * LOG2E, bf2f(a.Aim[d * 256 + n]) * dt * 0.15915494309189535f, 2.f * bf2f(a.ACr[d * 256 + n]) * dt, 2.f * bf2f(a.ACi[d * 256 + n]) * dt};
            }
            f4 acc[3] = {{0.f, 0.f, 0.f, 0.f}, {0.f, 0.f, 0.f, 0.f}, {0.f, 0.f, 0.f, 0.f}};
#pragma unroll 2
            for (int st = 0; st < 64; ++st) {
                const f4 z = *(const f4 LAS*)(pt + (4 * st + kk) * 16);
                float er, ei;
                { const float ex = __builtin_amdgcn_exp2f(z[0] * ppos); const float rv = __builtin_amdgcn_fractf(z[1] * ppos);
                  er = ex * __builtin_amdgcn_cosf(rv); ei = ex * __builtin_amdgcn_sinf(rv); }
                const float are = z[2] * er - z[3] * ei, aim = -(z[2] * ei + z[3] * er);
#pragma unroll
                for (int t = 0; t < 3; ++t) {
                    const float ex = __builtin_amdgcn_exp2f(z[0] * qpos[t]); const float rv = __builtin_amdgcn_fractf(z[1] * qpos[t]);
                    const float br = ex * __builtin_amdgcn_cosf(rv), bi = ex * __builtin_amdgcn_sinf(rv);
                    acc[t] = __builtin_amdgcn_mfma_f32_16x16x4f32(are, br, acc[t], 0, 0, 0);
                    acc[t] = __builtin_amdgcn_mfma_f32_16x16x4f32(aim, bi, acc[t], 0, 0, 0);
                }
            }
#pragma unroll
            for (int t = 0; t < 3; ++t) *(f4 LAS*)(sm + PA_KT + w * 3072 + ((t * 16 + l15) * 16 + 4 * kk) * 4) = acc[t];
        }
        bar();
#pragma nounroll
        for (int cc = 0; cc < 4; ++cc) {
            const int ch = cc;
            {
                float kv[8];
#pragma unroll
                for (int j = 0; j < 8; ++j) kv[j] = *(const float LAS*)(sm + PA_KT + cc * 3072 + toff[j]);
                u4 h4, l4;
#pragma unroll
                for (int j = 0; j < 4; ++j) { h4[j] = pk2(kv[2 * j], kv[2 * j + 1]);
                    l4[j] = pk2(kv[2 * j] - __uint_as_float(h4[j] << 16), kv[2 * j + 1] - __uint_as_float(h4[j] & 0xffff0000u)); }
                const int o = (31 + (tid >> 2)) * 80 + (tid & 3) * 16;
                *(u4 LAS*)(sm + PA_KH + o) = h4; *(u4 LAS*)(sm + PA_KL + o) = l4;
            }
            for (int i = tid; i < 262; i += 256) {
                const u4 x0 = *(const u4 LAS*)(sm + PA_UT + ch * 4224 + i * 16), x1 = *(const u4 LAS*)(sm + PA_UT + ch * 4224 + i * 16 + 16);
                const unsigned r[8] = {x0[0], x0[1], x0[2], x0[3], x1[0], x1[1], x1[2], x1[3]};
#pragma unroll
                for (int cp = 0; cp < 8; ++cp) {
                    u4 o;
#pragma unroll
                    for (int j = 0; j < 4; ++j) o[j] = (cp & 1) ? __builtin_amdgcn_alignbit(r[(cp >> 1) + j + 1], r[(cp >> 1) + j], 16) : r[(cp >> 1) + j];
                    *(u4 LAS*)(sm + PA_TU + (cp * 276 + (cp ? 1 : 0) + i) * 16) = o;
                }
            }
            bar();
            {
                f16v y0, y1;
                const int m00 = 2048 - l31 + 8 * hi, cp = m00 & 7;
                const char LAS* ab = sm + PA_TU + (cp * 276 + (cp ? 1 : 0) + (m00 >> 3)) * 16 - 63 * 64;
                const char LAS* bb = sm + PA_KH + l31 * 80 + hi * 16;
                const char LAS* a1w = ab - w * 64; const char LAS* b1w = bb - w * 80;
                const char LAS* a2w = ab + w * 64; const char LAS* b2w = bb + w * 80;
                u4 fr[2][4];
                auto rdg = [&](int g, int sl) __attribute__((always_inline)) {
                    const int i = (g & 15) >> 1, ks = g & 1;
                    if (g < 16) { fr[sl][0] = *(const u4 LAS*)(b1w + (31 - 4 * i) * 80 + ks * 32); fr[sl][2] = *(const u4 LAS*)(a1w + (63 - 4 * i) * 64 + ks * 32);
                                  fr[sl][3] = *(const u4 LAS*)(a1w + (31 - 4 * i) * 64 + ks * 32); fr[sl][1] = *(const u4 LAS*)(b1w + 7680 + (31 - 4 * i) * 80 + ks * 32); }
                    else { fr[sl][0] = *(const u4 LAS*)(b2w + (32 + 4 * i) * 80 + ks * 32); fr[sl][3] = *(const u4 LAS*)(a2w + (32 + 4 * i) * 64 + ks * 32);
                           fr[sl][1] = *(const u4 LAS*)(b2w + 7680 + (32 + 4 * i) * 80 + ks * 32); }
                };
                rdg(0, 0); SB();
#pragma unroll
                for (int g = 0; g < 32; ++g) {
                    const int sl = g & 1;
                    if (g + 1 < 32) rdg(g + 1, sl ^ 1);
                    SB();
                    if (g == 0) { mV_vv0(y0, fr[sl][2], fr[sl][0]); mV_vv0(y1, fr[sl][3], fr[sl][0]); }
                    else { if (g < 16) mV_vv(y0, fr[sl][2], fr[sl][0]); mV_vv(y1, fr[sl][3], fr[sl][0]); }
                    if (g < 16) mV_vv(y0, fr[sl][2], fr[sl][1]);
                    mV_vv(y1, fr[sl][3], fr[sl][1]);
                    SB();
                }
                MFW32(y0, y1);
#pragma unroll
                for (int g = 0; g < 4; ++g) {
                    *(f4 LAS*)(sm + PA_RED + w * 9216 + l31 * 144 + (8 * g + 4 * hi) * 4) = (f4){y0[4 * g], y0[4 * g + 1], y0[4 * g + 2], y0[4 * g + 3]};
                    *(f4 LAS*)(sm + PA_RED + w * 9216 + (32 + l31) * 144 + (8 * g + 4 * hi) * 4) = (f4){y1[4 * g], y1[4 * g + 1], y1[4 * g + 2], y1[4 * g + 3]};
                }
            }
            bar();
            {
                f4 s0 = {0.f, 0.f, 0.f, 0.f}, s1 = s0;
#pragma unroll
                for (int p = 0; p < 4; ++p) { const char LAS* q_ = sm + PA_RED + p * 9216 + (tid >> 2) * 144 + (tid & 3) * 32; s0 += *(const f4 LAS*)q_; s1 += *(const f4 LAS*)(q_ + 16); }
                const float dsk = bf2f(a.ADsk[d0 + ch]);
                float yv[8];
#pragma unroll
                for (int j = 0; j < 8; ++j) yv[j] = bfr(j < 4 ? s0[j & 3] : s1[j & 3]) + bfr(bf2f(*(const u16 LAS*)(sm + PA_UT + ch * 4224 + (2048 - (tid * 8 + j)) * 2)) * dsk);
                sst128((u4){pk2(yv[0], yv[1]), pk2(yv[2], yv[3]), pk2(yv[4], yv[5]), pk2(yv[6], yv[7])}, rpa, tid * 16, (d0 + ch) * 4096);
            }
            bar();
        }
    }
    set_flag(c, mksrd(a.HB, HB_SZ), O_FPA + task * 4, tag + 1);
}
DI void pa_out(unsigned tag, int wave, int t) {
    CArgs& a = get_args();
    const SideCtx c = make_ctx(wave);
    const int lane = c.lane, tid = c.wave * 64 + lane;
    t = __builtin_amdgcn_readfirstlane(t); asm volatile("" : "+s"(t));
    const u4 rpa = mksrd(a.PA, 56 * 32768), ro = mksrd(a.out, 2048 * 1792);
    wait_prog(mksrd(a.HB, HB_SZ), lane < 16 ? O_FPA + (t & ~7) * 8 + lane * 4 : OOB, tag + 1);
    u4 v[8];
#pragma unroll
    for (int i = 0; i < 8; ++i) v[i] = sld128(rpa, (tid >> 5) * 4096 + (tid & 31) * 16, ((t >> 3) * 64 + i * 8) * 4096 + (t & 7) * 512);
    WAITV(0, "+v"(v[0]), "+v"(v[1]), "+v"(v[2]), "+v"(v[3]), "+v"(v[4]), "+v"(v[5]), "+v"(v[6]), "+v"(v[7]));
    bar();
#pragma unroll
    for (int i = 0; i < 8; ++i) *(u4 LAS*)(c.sm + (i * 8 + (tid >> 5)) * 528 + (tid & 31) * 16) = v[i];
    bar();
#pragma unroll
    for (int k = 0; k < 8; ++k) {
        const char LAS* p = c.sm + (tid & 7) * 8 * 528 + ((tid >> 3) + 32 * k) * 2;
        u4 o;
#pragma unroll
        for (int j = 0; j < 4; ++j) o[j] = (unsigned)*(const u16 LAS*)(p + (2 * j) * 528) | ((unsigned)*(const u16 LAS*)(p + (2 * j + 1) * 528) << 16);
        sst128(o, ro, (tid >> 3) * 1792 + (tid & 7) * 16, ((t & 7) * 256 + 32 * k) * 1792 + (t >> 3) * 128);
        asm volatile("s_sleep %0" :: "i"(P_ASLP));
    }
}
#define L_TAB 0
#define L_CLK 32768
#define L_MEM 33792
#define L_WRK 49152
DI void edge(unsigned& ce, unsigned& ne) {
    const unsigned n0 = rt_now(); u64 t, c; int n = 0;
#pragma nounroll
    do { asm volatile("s_memrealtime %0\n\ts_memtime %1\n\ts_waitcnt lgkmcnt(0)" : "=&s"(t), "=&s"(c) :: "memory"); } while ((unsigned)t == n0 && ++n < 64);
    ce = (unsigned)c - 30u; ne = (unsigned)t;
}
DI unsigned srdw(const void* p, unsigned bytes, int i) { const u64 x = (u64)p; return i == 0 ? (unsigned)x : i == 1 ? (unsigned)(x >> 32) & 0xffffu : i == 2 ? bytes : 0x00020000u; }
#define RUN(r) asm volatile("v_mov_b32 v255, %0\n\ts_getpc_b64 vcc\n.Lfk_from_" #r ":\n\ts_add_u32 vcc_lo, vcc_lo, fk_role_" #r "-.Lfk_from_" #r "\n\ts_addc_u32 vcc_hi, vcc_hi, 0\n\ts_setpc_b64 vcc" :: "v"(tab) : "memory", "vcc", "scc", "s99", "v255", "a255")
DI unsigned probe(u4 r, int id, int xh) {
    const int lane = fresh_lane(), o = xh * (2 << 20) + O_PROBE + id * 128; unsigned best = 1016;
#pragma nounroll
    for (int rep = 0; rep < 2; ++rep) {
        sst32(rep, r, lane < 4 ? o + lane * 8192 : OOB, 0); waitvm<0>();
#pragma nounroll
        for (int k = 0; k < 4; ++k) {
            const int kk = (k + id) & 3, ol = lane == 0 ? o + kk * 8192 : OOB;
            const unsigned t0 = now32(); unsigned v = sld32(r, ol, 0); WAITV(0, "+v"(v));
            const unsigned d = now32() - t0;
            if (lane == kk) best = min(best, d);
        }
    }
    return best;
}
DI void chain_role(unsigned tag, int vb, int W, int role, int id, int xh, unsigned best) {
    CArgs& a = get_args();
    char LAS* sm = (char LAS*)smem_;
    const unsigned smb = (unsigned)(size_t)sm;
    const int lane = fresh_lane(), q = lane >> 4, l15 = lane & 15, i8 = lane & 7, hb = (lane >> 3) & 1;
    unsigned LAS* tb = (unsigned LAS*)(sm + L_TAB + W * 8192) + lane;
    const u4 r = mksrd(a.HB, HB_SZ);
    const int wave = W;
    unsigned c0, n0, c1, n1; edge(c0, n0);
    if (role == 0) {
        if (W == 3) sst32(tag + 1 + (best >> 2), r, lane < 4 ? O_PAR + id * 16 + lane * 4 : OOB, 0);
    } else {
        unsigned v = sld32(mksrd(role == 1 ? a.UM : W == 0 ? a.GF : W == 1 ? a.UF : a.TF, role == 1 ? 65 * 28 * 4096 : W < 2 ? 64 * 16 * 28 * 1024 : 65 * 32 * 3584), lane << 19, 0); WAITV(0, "+v"(v));
    }
    const int cp = xh * 32768;
    const void *p1, *p2 = a.UF; unsigned x1 = 0, x2 = 0, po = 0; int poi, fw, wk;
    if (role == 0) {
        const int part = id & 1, h = id >> 1, rr = l15 >> 2, c = lane & 3;
        const int bq = q == 0 ? 0 : q == 1 ? 528 : q == 2 ? 1152 : 1680;
        tb[8 * 64] = O_MEM + cp + lane * 16 + part * 4096; tb[9 * 64] = O_MEM + cp + lane * 16 + (1 - part) * 4096; tb[10 * 64] = O_MEM + cp + lane * 16 + 3072;
        tb[11 * 64] = smb + L_WRK + bq + l15 * 8 + (W == 3 ? 3 * 2304 + part * 256 : 0); tb[14 * 64] = smb + L_WRK + bq + l15 * 8 + 3 * 2304 + (1 - part) * 256;
        tb[12 * 64] = smb + L_WRK + (c == 0 ? 0 : c == 2 ? 528 : c == 1 ? 1152 : 1680) + (16 * (q >> 1) + 4 * (q & 1) + 8 * (rr >> 1) + (rr & 1)) * 8;
        const int op = O_SMM + id * 256 + hb * 128 + (4 * (q & 1) + 2 * (q >> 1)) * 16 + i8 * 4;
        tb[13 * 64] = op + cp; tb[15 * 64] = op + (cp ^ 32768);
        int tile, chunk;
        if (W < 3) {
            const int i = lane < 28 ? lane : 0;
            const int p = i < 8 ? i : i < 19 ? (int)(0x43424130210ull >> (4 * (i - 8))) & 7 : 5 + (i - 19) / 3, k = i < 8 ? 0 : i < 19 ? (int)(0x32221212111ull >> (4 * (i - 8))) & 7 : 1 + (i - 19) % 3;
            const int n = 3 * (p & 3) + W;
            tile = 4 * (4 * (p < 4 ? part : 1 - part) + (n >> 2)) + (n & 3); chunk = k < 3 ? 4 * part + (W + k) % 3 : 3;
        } else {
            const int p = lane < 2 ? 2 + lane : (lane - 2) & 3, k = lane < 2 || lane >= 14 ? 3 : (lane - 2) >> 2;
            tile = 12 + (p < 2 ? 2 * part + p : 2 * (1 - part) + p - 2); chunk = k < 3 ? 4 * part + k : 3;
        }
        wk = ((h * 28 + tile) * 7 + chunk) * 2048; fw = h * 8 + (lane & 3);
        poi = (W == 0 && q < 2) ? O_SMM + q * 32768 + id * 256 + l15 * 16 : OOB;
        p1 = a.WqkF; x1 = part + 1;
    } else if (role == 1) {
        const int hc = 4 * W + ((q & 1) << 1) + (q >> 1), k = i8 - 4 * hb;
        const int ocp = W == 0 ? O_MEM : W == 3 ? O_MEM + 32768 : O_MEMX + (W - 1) * 32768;
        tb[8 * 64] = O_MEM + cp + lane * 16; tb[9 * 64] = O_MEM + cp + lane * 16 + 4096;
        tb[10 * 64] = O_SMM + cp + hc * 512 + l15 * 16; tb[11 * 64] = O_MZ + hc * 128 + l15 * 8;
        tb[12 * 64] = RXW(id >> 1) + (id & 1) * 4096 + hc * 256 + l15 * 16;
        tb[13 * 64] = ocp + id * 256 + lane * 4;
        tb[14 * 64] = k == 0 ? 0xffffu : k == 1 ? 0xffff0000u : 0u; tb[15 * 64] = k == 2 ? 0xffffu : k == 3 ? 0xffff0000u : 0u;
        tb[16 * 64] = smb + L_WRK + W * 512 + lane * 8;
        tb[17 * 64] = smb + L_WRK + ((lane & 32) + (lane & 2) * 8 + (lane & 1) * 8 + ((lane >> 2) & 7)) * 8;
        tb[18 * 64] = id * 4096 + W * 1024 + lane * 16;
        tb[19 * 64] = san(*(const unsigned*)(a.mem0 + ((lane >> 2) & 7) * 448 + id * 16 + (lane >> 5) * 8 + (lane & 3) * 2));
        tb[20 * 64] = smb + L_MEM + lane * 16;
        tb[21 * 64] = lane == 0 ? O_FT + (32 + id) * 128 : OOB;
        tb[23 * 64] = (q == 0 ? O_MEM : q == 3 ? O_MEM + 32768 : O_MEMX + (q - 1) * 32768) + id * 256 + l15 * 16;
        const bool lead = id == 14 * xh;
        tb[22 * 64] = lane != 0 ? OOB : W == 2 ? (lead ? O_REC + xh * 8 : OOB) : id == 0 ? O_EPOCH : OOB;
        if (id == 0)
            for (int k = 0; k < 3; ++k) sst128((u4){REC_A, REC_B, REC_A, REC_B}, r, O_REC + lane * 16, (W * 3 + k) * 1024);
        const int ord = ((W == 0 ? 0x6543210 : W == 1 ? 0x3106542 : W == 2 ? 0x4321065 : 0x6542103) >> (4 * (lane % 7))) & 7;
        wk = ((id * 16 + 4 * W + (lane / 7 & 3)) * 7 + ord) * 2048; fw = (4 * W + (lane & 3)) * 8 + 4 + (id >> 3);
        poi = q == 0 ? ocp + id * 256 + l15 * 16 : OOB;
        p1 = a.WvoF; p2 = a.UM; po = OFF_P;
    } else {
        const int h = id >> 1, half = id & 1, pt = 4 * W + q;
        tb[8 * 64] = O_MEMX + cp + lane * 16; tb[9 * 64] = O_MEMX + cp + lane * 16 + 4096;
        tb[10 * 64] = (W < 3 || (q & 1) == 0) ? RXW(half * 7 + 2 * W + (q & 1)) + (q >> 1) * 4096 + h * 256 + l15 * 16 : OOB;
        tb[11 * 64] = ((lane & 3) == 0 && half == 0) ? O_MZ + h * 128 + (lane >> 2) * 8 : OOB;
        tb[12 * 64] = smb + L_WRK + lane * 8;
        tb[13 * 64] = smb + L_WRK + ((q >> 1) * 32 + (lane & 1) * 16 + 4 * (q & 1) + (l15 >> 2)) * 8;
        poi = pt < 14 ? RXW(half * 7 + (pt >> 1)) + (pt & 1) * 4096 + h * 256 + l15 * 16 : (pt == 14 && half == 0 && l15 < 8) ? O_MZ + h * 128 + l15 * 16 : OOB;
        tb[14 * 64] = poi;
        tb[15 * 64] = O_FW + (h * 8 + i8) * 128;
        tb[16 * 64] = smb + L_MEM + lane * 16;
        tb[17 * 64] = smb + L_WRK + 1024 + W * 2048 + ((q >> 1) * 16 + l15) * 16 + (q & 1) * 8;
        tb[18 * 64] = smb + L_WRK + 1024 + W * 2048 + (8 * q + (l15 >> 2) + 32 * ((lane >> 1) & 1)) * 16 + (lane & 1) * 8;
        const int otf = id * 3584 + W * 1024 + (4 * hb + q) * 128 + i8 * 16;
        tb[19 * 64] = otf; tb[21 * 64] = hb ? OOB : otf;
        tb[20 * 64] = lane == 0 ? O_FT + id * 128 : OOB;
        if (W == 3)
            for (int k = 0; k < 3; ++k) tb[(17 + k) * 64] = RXW(half * 7 + 2 * k + (q >> 1)) + (q & 1) * 4096 + h * 256 + l15 * 16;
        wk = ((h * 28 + half * 14 + 4 * W) * 7 + (lane < 42 ? lane : 0)) * 2048; fw = h * 8 + (lane & 3);
        p1 = W == 3 ? a.GF : a.WqkF; x1 = h * 28 * 1024; x2 = (h * 28 + half * 14 + 4 * W) * 1024; po = OFF_X;
    }
    tb[0 * 64] = (lane & 8) ? 0xEEEE : 0x4444; tb[1 * 64] = lane * 16; tb[2 * 64] = wk; tb[3 * 64] = ((vb >> 3) * 16 + (vb & 3) * 4 + W) * 1280 + lane * 4;
    tb[4 * 64] = O_FW + fw * 128; tb[5 * 64] = O_RDY + lane * 4; tb[6 * 64] = O_RDY + 256 + lane * 4; tb[7 * 64] = O_REC;
    tb[29 * 64] = poi; tb[30 * 64] = (W == 0 && lane == 0) ? O_RDY + ((vb & 3) * 32 + (vb >> 3)) * 4 : OOB;
#pragma nounroll
    while (rt_now() - n0 < 100u) {}
    edge(c1, n1);
    const unsigned f = min(max((unsigned)((float)(c1 - c0) * 252.f / (float)(n1 - n0)), 0x800u), 0x2000u);
    const unsigned k0 = n1 * 16u / LAT_R;
    const unsigned sz1 = (role == 2 && W == 3) ? 64 * 16 * 28 * 1024 : 28 * 16 * 14336, sz2 = role == 1 ? 65 * 28 * 4096 : 64 * 16 * 28 * 1024;
    unsigned sv;
    if (lane < 4) sv = srdw(a.HB, HB_SZ, lane); else if (lane < 8) sv = srdw(p1, sz1, lane - 4); else if (lane < 12) sv = srdw(p2, sz2, lane - 8); else if (lane < 16) sv = srdw(a.TF, 65 * 32 * 3584, lane - 12);
    else if (lane < 20) sv = srdw(a.TS, 512 * 1280, lane - 16);
    else sv = lane == 20 ? k0 * LAT_R : lane == 21 ? LAT_R : lane == 24 ? po : lane == 26 ? tag : lane == 35 ? k0 & 1 : lane == 29 ? x1 : lane == 30 ? x2
            : lane == 33 ? (unsigned)TOL_C : lane == 34 ? smb + L_CLK : 0u;
    tb[31 * 64] = sv;
    if (W == 0 && lane < 2) *(u4 LAS*)(sm + L_CLK + lane * 16) = lane == 0 ? (u4){n1 * 16u, c1, f, 0u} : (u4){0u, 0u, 0u, 0u};
    bar();
    const unsigned tab = smb + L_TAB + W * 8192 + lane * 4;
    if (role == 0) { if (W == 0) RUN(S0); else if (W == 1) RUN(S1); else if (W == 2) RUN(S2); else RUN(S3); }
    else if (role == 1) { if (W == 0) RUN(P0); else if (W == 1) RUN(P1); else if (W == 2) RUN(P2); else RUN(P3); }
    else { if (W == 0) RUN(X0); else if (W == 1) RUN(X1); else if (W == 2) RUN(X2); else RUN(X3); }
}

#define NPA_FAR 10
#define PA_HOLD 0
#define PAO_AT 118
__global__ __launch_bounds__(256, 1) void fused_kernel(Args a_) {
    CArgs& a = get_args();
    const int wave = __builtin_amdgcn_readfirstlane(threadIdx.x >> 6);
    const u4 rhb = mksrd(a.HB, HB_SZ);
    unsigned ep = 0;
    if (wave == 0) ep = ld32(rhb, O_EPOCH, 0);
    else {
        u64 pc; unsigned len;
        asm volatile("s_getpc_b64 %0\nfk_code_lo:\n\ts_mov_b32 %1, fk_code_hi-fk_code_lo" : "=s"(pc), "=s"(len));
        const u4 rc = mksrd((const void*)pc, len & ~15u);
        const int vo = (((blockIdx.x >> 3) * 3 + wave - 1) * 2) * 1024 + (threadIdx.x & 63) * 16;
#pragma unroll
        for (int i = 0; i < 2; ++i) dma_one(rc, vo + i * 1024, 0, (unsigned)(size_t)(char LAS*)smem_ + 147456 + (wave * 3 + i) * 1024);
    }
    const int xcc = __builtin_amdgcn_s_getreg((31 << 11) | 20) & 7;
    int vb = xcc + (blockIdx.x & ~7);
    unsigned tag, best = 1016;
    if ((vb & 0x85) == 1 && wave == 3) best = probe(rhb, vb >> 3, (vb >> 1) & 1);
#pragma nounroll
    for (int pass = 0;; ++pass) {
        const bool far = vb & 4;
        if (far) w_first(wave, (vb >> 5) & 1, 4 * (vb & 3) + ((vb >> 3) & 3), vb >> 6, 0); else waitvm<0>();
        unsigned slot = (unsigned)xcc * 32u + (blockIdx.x >> 3), old = 0;
        if (wave == 0 && !pass) {
            WAITV(16, "+v"(ep));
            ep += 0x9E3779B4u; old = ep;
            asm volatile("s_nop 4\n\tbuffer_atomic_swap %0, %1, %2, 0 offen sc0 sc1" : "+v"(old) : "v"(threadIdx.x == 0 ? O_CNT + slot * 128u : (unsigned)OOB), "s"(rhb) : "memory");
        }
        if (far) { w_first(wave, (vb >> 5) & 1, 4 * (vb & 3) + ((vb >> 3) & 3), vb >> 6, 1); } else waitvm<0>();
        WAITV(16, "+v"(old));
        if (pass) { bar(); break; }
        if (threadIdx.x == 0) {
            for (int k = 0; k < 256 && old == ep; ++k) {
                slot = (slot + 1u) & 255u;
                old = __hip_atomic_exchange((unsigned*)(a.HB + O_CNT) + slot * 32u, ep, __ATOMIC_RELAXED, __HIP_MEMORY_SCOPE_AGENT);
            }
            *(u2 LAS*)(smem_ + 147456) = (u2){(slot >> 5) + 8u * (slot & 31u), ep};
        }
        bar();
        const int got = __builtin_amdgcn_readfirstlane(*(const unsigned LAS*)(smem_ + 147456));
        tag = __builtin_amdgcn_readfirstlane(*(const unsigned LAS*)(smem_ + 147460));
        if (got == vb) break;
        vb = got; best = 1016; waitvm<0>(); bar();
    }
    const int res = vb & 7, idx = vb >> 3;
    int j;
    const int xq = res & 3;
    unsigned* const tick = (unsigned*)(a.HB + O_TICK) + xq * 32;
    if (res < 4) {
        const int xh = res >> 1;
        if (idx < 16 || ((res & 1) && idx < 30)) { chain_role(tag, vb, wave, (res & 1) ? idx >> 4 : 2, idx < 16 ? xh * 16 + idx : xh * 14 + idx - 16, xh, best); return; }
        if (wave == 0) sst32(tag + 1, rhb, fresh_lane() == 0 ? O_RDY + (res * 32 + idx) * 4 : OOB, 0);
        slp(PA_HOLD);
        j = -2;
    } else {
        if (idx == 0 && wave == 0) sst32(0u, rhb, fresh_lane() == 0 ? O_TICK + xq * 128 : OOB, 0);
        if (idx & 4) wgu_task<1>(tag, vb, wave, 4 * xq + (idx & 3), idx >> 3); else wgu_task<0>(tag, vb, wave, 4 * xq + (idx & 3), idx >> 3);
        wait_prog(rhb, fresh_lane() == 0 ? O_FW + xq * 32 * 128 : OOB, tag + 1);
        j = 0;
    }
    int round = 0;
#pragma nounroll
    for (; round < 256; ++round) {
        if (j >= 0) {
            if (wave == 0 && fresh_lane() == 0) *(unsigned LAS*)(smem_ + 160 * 1024 - 16) = __hip_atomic_fetch_add(tick, 1u, __ATOMIC_RELAXED, __HIP_MEMORY_SCOPE_AGENT);
            bar();
            j = __builtin_amdgcn_readfirstlane(*(const unsigned LAS*)(smem_ + 160 * 1024 - 16));
            if ((unsigned)j >= NPA_FAR + 190u) break;
        }
        int k = j - NPA_FAR;
        if (k < 0) {
            parta_task(tag, wave, j < 0 ? 2 * ((res & 1) ? (res >> 1) * 2 + idx + 2 : (res >> 1) * 16 + idx - 16) + j + 2 : 72 + xq * NPA_FAR + j);
            if (j < 0 && ++j == 0) break;
            continue;
        }
        if (k >= PAO_AT && k < PAO_AT + 14) { pa_out(tag, wave, (k - PAO_AT) * 4 + xq); bar(); continue; }
        if (k >= PAO_AT) k -= 14;
        const int g = k < 162 ? (k + 14) / 11 : 16 + (k - 162) / 7, o = k < 162 ? (k + 14) % 11 : (k - 162) % 7;
        if (k < 8 || o >= 7) e_task(tag, wave, 4 * (k < 8 ? k >> 2 : g) + xq, k < 8 ? k & 3 : o - 7);
        else { x_task(tag, wave, 4 * (g - 2) + xq, o); bar(); }
    }
}
__device__ __attribute__((used, noinline)) void fused_kernel_roles() { asm volatile("s_endpgm\n" ASM_ROLES "fk_code_hi:"); }

#define WS_SKIP 0
extern "C" void kernel_launch(void* const* d_in, const int* in_sizes, int n_in, void* d_out, int out_size, void* d_ws, size_t ws_size, hipStream_t stream) {
    Args a;
    a.Au = (const u16*)d_in[0]; a.AlogA = (const u16*)d_in[1]; a.Aim = (const u16*)d_in[2]; a.ACr = (const u16*)d_in[3]; a.ACi = (const u16*)d_in[4];
    a.Alogdt = (const u16*)d_in[5]; a.ADsk = (const u16*)d_in[6]; a.X = (const u16*)d_in[7]; a.mem0 = (const u16*)d_in[8];
    a.Wq = (const u16*)d_in[9]; a.Wk = (const u16*)d_in[10]; a.Wv = (const u16*)d_in[11]; a.Wo = (const u16*)d_in[12];
    a.out = (u16*)d_out;
    char* base = (char*)(((size_t)d_ws + WS_SKIP + (2u << 20) - 1) & ~(size_t)((2u << 20) - 1));
    a.HB = base;
    size_t off = HB_SZ;
    auto alloc = [&](size_t n) { char* p = base + off; off += (n + 4095) & ~(size_t)4095; return p; };
    a.WvoF = (u16*)alloc(28 * 16 * 14336); a.WqkF = (u16*)alloc(28 * 16 * 14336); a.GF = (u16*)alloc((size_t)64 * 16 * 28 * 1024); a.UF = (u16*)alloc((size_t)64 * 16 * 28 * 1024);
    a.EX = (u16*)alloc(64 * 16 * 2048); a.UM = (u16*)alloc(65 * 28 * 4096); a.MZX = (float*)alloc(64 * 16 * 256); a.TF = (u16*)alloc(65 * 32 * 3584); a.PA = (u16*)alloc(56 * 32768);
    a.TS = (unsigned*)alloc(768 * 1280);
    (void)hipFuncSetAttribute((const void*)fused_kernel, hipFuncAttributeMaxDynamicSharedMemorySize, 160 * 1024);
    fused_kernel<<<256, 256, 160 * 1024, stream>>>(a);
}
```

```cpp
#include <hip/hip_runtime.h>
#include <stdint.h>
typedef unsigned short u16;
typedef unsigned long long u64;
typedef __attribute__((ext_vector_type(8))) __bf16 bf8;
typedef __attribute__((ext_vector_type(16))) __bf16 bf16v;
typedef __attribute__((ext_vector_type(2))) __bf16 bf2;
typedef __attribute__((ext_vector_type(2))) float f2;
typedef __attribute__((ext_vector_type(4))) float f4;
typedef __attribute__((ext_vector_type(4))) unsigned u4;
typedef __attribute__((ext_vector_type(2))) unsigned u2;
typedef __attribute__((ext_vector_type(8))) unsigned u8v;
typedef __attribute__((ext_vector_type(4))) short s4;
#define LAS __attribute__((address_space(3)))
#define DI __device__ __forceinline__
#define SB() __builtin_amdgcn_sched_barrier(0)

DI unsigned pk2(float a, float b) { f2 v = {a, b}; return __builtin_bit_cast(unsigned, __builtin_convertvector(v, bf2)); }
DI float bfr(float a) { return __uint_as_float(pk2(a, a) & 0xffff0000u); }
DI unsigned san(unsigned x) { return x < 0xFFFFFFFEu ? x : 0xFFFFFFFEu; }
DI void bar() { asm volatile("s_waitcnt lgkmcnt(0)\n\ts_barrier" ::: "memory"); SB(); }
DI float x32max(float a) { auto r = __builtin_amdgcn_permlane32_swap(__float_as_uint(a), __float_as_uint(a), false, false); return fmaxf(__uint_as_float(r[0]), __uint_as_float(r[1])); }
DI float x32add(float a) { auto r = __builtin_amdgcn_permlane32_swap(__float_as_uint(a), __float_as_uint(a), false, false); return __uint_as_float(r[0]) + __uint_as_float(r[1]); }
DI u2 trread(const char LAS* p) { return __builtin_bit_cast(u2, __builtin_amdgcn_ds_read_tr16_b64_v4i16((LAS s4*)p)); }
DI u4 mksrd(const void* p, unsigned bytes) { u64 x = (u64)p; return (u4){(unsigned)x, (unsigned)(x >> 32) & 0xffffu, bytes, 0x00020000u}; }
DI u4 ld128(u4 srd, int voff, int soff) { u4 v; asm volatile("buffer_load_dwordx4 %0, %1, %2, %3 offen sc1" : "=v"(v) : "v"(voff), "s"(srd), "s"(soff) : "memory"); return v; }
DI unsigned ld32(u4 srd, int voff, int soff) { unsigned v; asm volatile("buffer_load_dword %0, %1, %2, %3 offen sc1" : "=v"(v) : "v"(voff), "s"(srd), "s"(soff) : "memory"); return v; }
DI void st128(u4 v, u4 srd, int voff, int soff) { asm volatile("buffer_store_dwordx4 %0, %1, %2, %3 offen sc1\n\ts_nop 1" :: "v"(v), "v"(voff), "s"(srd), "s"(soff) : "memory"); }
DI void st32(unsigned v, u4 srd, int voff, int soff) { asm volatile("buffer_store_dword %0, %1, %2, %3 offen sc1" :: "v"(v), "v"(voff), "s"(srd), "s"(soff) : "memory"); }
#define PIN(x) asm volatile("" : "+v"(x))
#define WAITV(n, ...) asm volatile("s_waitcnt vmcnt(" #n ")" : __VA_ARGS__ :: "memory")

#define NSEG 64
#define O_MEM 0
#define O_SMM (2 * 32768)
#define O_MZ (4 * 32768)
#define O_MEMX 16384
#define RXW(n) ((n) < 7 ? (5 + (n)) * 32768 : 16384 + ((n) - 5) * 32768)
#define O_PROBE (15 * 32768)
#define O_FL (4 << 20)
#define O_PAR O_FL
#define O_SIDE (O_FL + 256)
#define O_RDY (O_FL + 512)
#define O_FPA (O_FL + 1024)
#define O_FE (O_FL + 2048)
#define O_FW (O_FL + 4096)
#define O_FT (O_FW + 16384)
#define O_REC (O_FT + 8192)
#define O_CNT (O_REC + 16384)
#define O_EPOCH (O_CNT + 32768)
#define O_TICK (O_EPOCH + 128)
#define HB_SZ (5 << 20)
#define GU_H ((2 << 20) + 15360)
#define GU_SZ (32 << 20)
#define TU_P 524288
#define SPIN_MAX (1u << 17)
#define LOG2E 1.4426950408889634f
#define FF 0xFFFFFFFFu
#define OOB 0x7f000000

struct Args {
    const u16 *Au, *AlogA, *Aim, *ACr, *ACi, *Alogdt, *ADsk, *X, *mem0, *Wq, *Wk, *Wv, *Wo;
    u16* out;
    char* HB;
    u16 *WvoF, *WqkF, *GF, *UF, *EX, *UM, *TF, *PA; float *MZX;
    unsigned* TS;
};
typedef const Args __attribute__((address_space(4))) CArgs;
DI CArgs& get_args() { CArgs* p = (CArgs*)__builtin_amdgcn_kernarg_segment_ptr(); asm volatile("" : "+s"(p)); return *p; }
extern __shared__ __attribute__((aligned(16))) char smem_[];
DI unsigned now32() { u64 t; asm volatile("s_memtime %0\n\ts_waitcnt lgkmcnt(0)" : "=s"(t) :: "memory"); return (unsigned)t; }
DI unsigned rt_now() { u64 t; asm volatile("s_memrealtime %0\n\ts_waitcnt lgkmcnt(0)" : "=s"(t) :: "memory"); return (unsigned)t; }
#define TNOW() 0
#define REC_A 1145
#define REC_B 930
#define LAT_R 1200u
#define OFF_P 60
#define OFF_X 20
#define TOL_C -100
#define ASM_ROLES \
".p2align 6\n" \
"fk_role_S0:\n" \
"ds_read_b32 v200, v255 offset:0\n" \
"ds_read_b32 v201, v255 offset:256\n" \
"ds_read_b32 v202, v255 offset:512\n" \
"ds_read_b32 v203, v255 offset:768\n" \
"ds_read_b32 v204, v255 offset:1024\n" \
"ds_read_b32 v205, v255 offset:1280\n" \
"ds_read_b32 v206, v255 offset:1536\n" \
"ds_read_b32 v207, v255 offset:1792\n" \
"ds_read_b32 v208, v255 offset:2048\n" \
"ds_read_b32 v209, v255 offset:2304\n" \
"ds_read_b32 v210, v255 offset:2560\n" \
"ds_read_b32 v211, v255 offset:2816\n" \
"ds_read_b32 v212, v255 offset:3072\n" \
"ds_read_b32 v213, v255 offset:3328\n" \
"ds_read_b32 v214, v255 offset:3584\n" \
"ds_read_b32 v215, v255 offset:3840\n" \
"ds_read_b32 v254, v255 offset:7936\n" \
"s_waitcnt lgkmcnt(0)\n" \
"v_readlane_b32 s4, v254, 0\n" \
"v_readlane_b32 s5, v254, 1\n" \
"v_readlane_b32 s6, v254, 2\n" \
"v_readlane_b32 s7, v254, 3\n" \
"v_readlane_b32 s8, v254, 4\n" \
"v_readlane_b32 s9, v254, 5\n" \
"v_readlane_b32 s10, v254, 6\n" \
"v_readlane_b32 s11, v254, 7\n" \
"v_readlane_b32 s12, v254, 8\n" \
"v_readlane_b32 s13, v254, 9\n" \
"v_readlane_b32 s14, v254, 10\n" \
"v_readlane_b32 s15, v254, 11\n" \
"v_readlane_b32 s16, v254, 12\n" \
"v_readlane_b32 s17, v254, 13\n" \
"v_readlane_b32 s18, v254, 14\n" \
"v_readlane_b32 s19, v254, 15\n" \
"v_readlane_b32 s20, v254, 16\n" \
"v_readlane_b32 s21, v254, 17\n" \
"v_readlane_b32 s22, v254, 18\n" \
"v_readlane_b32 s23, v254, 19\n" \
"v_readlane_b32 s24, v254, 20\n" \
"v_readlane_b32 s25, v254, 21\n" \
"v_readlane_b32 s26, v254, 22\n" \
"v_readlane_b32 s27, v254, 23\n" \
"v_readlane_b32 s28, v254, 24\n" \
"v_readlane_b32 s29, v254, 25\n" \
"v_readlane_b32 s30, v254, 26\n" \
"v_readlane_b32 s31, v254, 27\n" \
"v_readlane_b32 s32, v254, 28\n" \
"v_readlane_b32 s33, v254, 29\n" \
"v_readlane_b32 s34, v254, 30\n" \
"v_readlane_b32 s35, v254, 31\n" \
"v_readlane_b32 s36, v254, 32\n" \
"v_readlane_b32 s37, v254, 33\n" \
"v_readlane_b32 s38, v254, 34\n" \
"v_readlane_b32 s39, v254, 35\n" \
"s_mov_b32 s40, 0\n" \
"s_mov_b32 s51, 0\n" \
"s_nop 0\n" \
"v_mov_b32 v252, s38\n" \
"s_nop 4\n" \
"v_mov_b32 v188, -1\n" \
"s_mov_b32 s28, s33\n" \
"s_mov_b32 s33, -1\n" \
"v_writelane_b32 v251, s6, 0\n" \
"v_writelane_b32 v251, s10, 1\n" \
"v_writelane_b32 v251, s14, 2\n" \
"v_writelane_b32 v251, s18, 3\n" \
"s_mov_b32 s6, 0\n" \
"s_mov_b32 s10, 0\n" \
"s_mov_b32 s14, 0\n" \
"s_mov_b32 s18, 0\n" \
"s_mov_b32 s40, -1\n" \
"s_branch .LS0_pre\n" \
".LS0_wet:\n" \
"s_waitcnt vmcnt(0)\n" \
"v_readlane_b32 s6, v251, 0\n" \
"v_readlane_b32 s10, v251, 1\n" \
"v_readlane_b32 s14, v251, 2\n" \
"v_readlane_b32 s18, v251, 3\n" \
"s_nop 4\n" \
"s_mov_b32 s33, s28\n" \
"s_add_u32 s34, s33, 2\n" \
"s_add_u32 s36, s33, 4\n" \
"s_add_u32 s43, s30, s33\n" \
"s_mov_b32 s50, 0\n" \
".LS0_w1l:\n" \
"buffer_load_dword v240, v204, s[4:7], 0 offen sc1\n" \
"s_waitcnt vmcnt(0)\n" \
".LS0_w1c:\n" \
"v_subrev_u32 v241, s43, v240\n" \
"v_cmp_gt_u32 vcc, 0x100, v241\n" \
"s_cmp_eq_u64 vcc, -1\n" \
"s_cbranch_scc1 .LS0_w1d\n" \
"s_sleep 2\n" \
"s_add_u32 s50, s50, 1\n" \
"s_cmp_lt_u32 s50, 0x20000\n" \
"s_cbranch_scc1 .LS0_w1l\n" \
"s_branch .LS0_end\n" \
".LS0_w1d:\n" \
"v_readlane_b32 s43, v202, 0\n" \
"s_nop 4\n" \
"buffer_load_dwordx4 a[0:3], v201, s[8:11], s43 offen sc1\n" \
"buffer_load_dwordx4 a[4:7], v201, s[8:11], s43 offen offset:1024 sc1\n" \
"v_readlane_b32 s43, v202, 1\n" \
"s_nop 4\n" \
"buffer_load_dwordx4 a[8:11], v201, s[8:11], s43 offen sc1\n" \
"buffer_load_dwordx4 a[12:15], v201, s[8:11], s43 offen offset:1024 sc1\n" \
"v_readlane_b32 s43, v202, 2\n" \
"s_nop 4\n" \
"buffer_load_dwordx4 a[16:19], v201, s[8:11], s43 offen sc1\n" \
"buffer_load_dwordx4 a[20:23], v201, s[8:11], s43 offen offset:1024 sc1\n" \
"v_readlane_b32 s43, v202, 3\n" \
"s_nop 4\n" \
"buffer_load_dwordx4 a[24:27], v201, s[8:11], s43 offen sc1\n" \
"buffer_load_dwordx4 a[28:31], v201, s[8:11], s43 offen offset:1024 sc1\n" \
"v_readlane_b32 s43, v202, 4\n" \
"s_nop 4\n" \
"buffer_load_dwordx4 a[32:35], v201, s[8:11], s43 offen sc1\n" \
"buffer_load_dwordx4 a[36:39], v201, s[8:11], s43 offen offset:1024 sc1\n" \
"v_readlane_b32 s43, v202, 5\n" \
"s_nop 4\n" \
"buffer_load_dwordx4 a[40:43], v201, s[8:11], s43 offen sc1\n" \
"buffer_load_dwordx4 a[44:47], v201, s[8:11], s43 offen offset:1024 sc1\n" \
"v_readlane_b32 s43, v202, 6\n" \
"s_nop 4\n" \
"buffer_load_dwordx4 a[48:51], v201, s[8:11], s43 offen sc1\n" \
"buffer_load_dwordx4 a[52:55], v201, s[8:11], s43 offen offset:1024 sc1\n" \
"v_readlane_b32 s43, v202, 7\n" \
"s_nop 4\n" \
"buffer_load_dwordx4 a[56:59], v201, s[8:11], s43 offen sc1\n" \
"buffer_load_dwordx4 a[60:63], v201, s[8:11], s43 offen offset:1024 sc1\n" \
"s_add_u32 s43, s30, s34\n" \
"s_mov_b32 s50, 0\n" \
"s_branch .LS0_w2c\n" \
".LS0_w2l:\n" \
"buffer_load_dword v240, v204, s[4:7], 0 offen sc1\n" \
"s_waitcnt vmcnt(0)\n" \
".LS0_w2c:\n" \
"v_subrev_u32 v241, s43, v240\n" \
"v_cmp_gt_u32 vcc, 0x100, v241\n" \
"s_cmp_eq_u64 vcc, -1\n" \
"s_cbranch_scc1 .LS0_w2d\n" \
"s_sleep 2\n" \
"s_add_u32 s50, s50, 1\n" \
"s_cmp_lt_u32 s50, 0x20000\n" \
"s_cbranch_scc1 .LS0_w2l\n" \
"s_branch .LS0_end\n" \
".LS0_w2d:\n" \
"v_readlane_b32 s43, v202, 8\n" \
"s_nop 4\n" \
"buffer_load_dwordx4 a[64:67], v201, s[8:11], s43 offen sc1\n" \
"buffer_load_dwordx4 a[68:71], v201, s[8:11], s43 offen offset:1024 sc1\n" \
"v_readlane_b32 s43, v202, 9\n" \
"s_nop 4\n" \
"buffer_load_dwordx4 a[72:75], v201, s[8:11], s43 offen sc1\n" \
"buffer_load_dwordx4 a[76:79], v201, s[8:11], s43 offen offset:1024 sc1\n" \
"v_readlane_b32 s43, v202, 10\n" \
"s_nop 4\n" \
"buffer_load_dwordx4 a[80:83], v201, s[8:11], s43 offen sc1\n" \
"buffer_load_dwordx4 a[84:87], v201, s[8:11], s43 offen offset:1024 sc1\n" \
"v_readlane_b32 s43, v202, 12\n" \
"s_nop 4\n" \
"buffer_load_dwordx4 a[96:99], v201, s[8:11], s43 offen sc1\n" \
"buffer_load_dwordx4 a[100:103], v201, s[8:11], s43 offen offset:1024 sc1\n" \
"v_readlane_b32 s43, v202, 14\n" \
"s_nop 4\n" \
"buffer_load_dwordx4 a[112:115], v201, s[8:11], s43 offen sc1\n" \
"buffer_load_dwordx4 a[116:119], v201, s[8:11], s43 offen offset:1024 sc1\n" \
"v_readlane_b32 s43, v202, 19\n" \
"s_nop 4\n" \
"buffer_load_dwordx4 a[152:155], v201, s[8:11], s43 offen sc1\n" \
"buffer_load_dwordx4 a[156:159], v201, s[8:11], s43 offen offset:1024 sc1\n" \
"v_readlane_b32 s43, v202, 22\n" \
"s_nop 4\n" \
"buffer_load_dwordx4 a[176:179], v201, s[8:11], s43 offen sc1\n" \
"buffer_load_dwordx4 a[180:183], v201, s[8:11], s43 offen offset:1024 sc1\n" \
"v_readlane_b32 s43, v202, 25\n" \
"s_nop 4\n" \
"buffer_load_dwordx4 a[200:203], v201, s[8:11], s43 offen sc1\n" \
"buffer_load_dwordx4 a[204:207], v201, s[8:11], s43 offen offset:1024 sc1\n" \
"s_add_u32 s43, s30, s36\n" \
"s_mov_b32 s50, 0\n" \
"s_branch .LS0_w3c\n" \
".LS0_w3l:\n" \
"buffer_load_dword v240, v204, s[4:7], 0 offen sc1\n" \
"s_waitcnt vmcnt(0)\n" \
".LS0_w3c:\n" \
"v_subrev_u32 v241, s43, v240\n" \
"v_cmp_gt_u32 vcc, 0x100, v241\n" \
"s_cmp_eq_u64 vcc, -1\n" \
"s_cbranch_scc1 .LS0_w3d\n" \
"s_sleep 2\n" \
"s_add_u32 s50, s50, 1\n" \
"s_cmp_lt_u32 s50, 0x20000\n" \
"s_cbranch_scc1 .LS0_w3l\n" \
"s_branch .LS0_end\n" \
".LS0_w3d:\n" \
"v_readlane_b32 s43, v202, 11\n" \
"s_nop 4\n" \
"buffer_load_dwordx4 a[88:91], v201, s[8:11], s43 offen sc1\n" \
"buffer_load_dwordx4 a[92:95], v201, s[8:11], s43 offen offset:1024 sc1\n" \
"v_readlane_b32 s43, v202, 13\n" \
"s_nop 4\n" \
"buffer_load_dwordx4 a[104:107], v201, s[8:11], s43 offen sc1\n" \
"buffer_load_dwordx4 a[108:111], v201, s[8:11], s43 offen offset:1024 sc1\n" \
"v_readlane_b32 s43, v202, 15\n" \
"s_nop 4\n" \
"buffer_load_dwordx4 a[120:123], v201, s[8:11], s43 offen sc1\n" \
"buffer_load_dwordx4 a[124:127], v201, s[8:11], s43 offen offset:1024 sc1\n" \
"v_readlane_b32 s43, v202, 16\n" \
"s_nop 4\n" \
"buffer_load_dwordx4 a[128:131], v201, s[8:11], s43 offen sc1\n" \
"buffer_load_dwordx4 a[132:135], v201, s[8:11], s43 offen offset:1024 sc1\n" \
"v_readlane_b32 s43, v202, 17\n" \
"s_nop 4\n" \
"buffer_load_dwordx4 a[136:139], v201, s[8:11], s43 offen sc1\n" \
"buffer_load_dwordx4 a[140:143], v201, s[8:11], s43 offen offset:1024 sc1\n" \
"v_readlane_b32 s43, v202, 20\n" \
"s_nop 4\n" \
"buffer_load_dwordx4 a[160:163], v201, s[8:11], s43 offen sc1\n" \
"buffer_load_dwordx4 a[164:167], v201, s[8:11], s43 offen offset:1024 sc1\n" \
"v_readlane_b32 s43, v202, 23\n" \
"s_nop 4\n" \
"buffer_load_dwordx4 a[184:187], v201, s[8:11], s43 offen sc1\n" \
"buffer_load_dwordx4 a[188:191], v201, s[8:11], s43 offen offset:1024 sc1\n" \
"v_readlane_b32 s43, v202, 26\n" \
"s_nop 4\n" \
"buffer_load_dwordx4 a[208:211], v201, s[8:11], s43 offen sc1\n" \
"buffer_load_dwordx4 a[212:215], v201, s[8:11], s43 offen offset:1024 sc1\n" \
"s_add_u32 s43, s30, 7\n" \
"s_mov_b32 s50, 0\n" \
"s_branch .LS0_w4c\n" \
".LS0_w4l:\n" \
"buffer_load_dword v240, v204, s[4:7], 0 offen sc1\n" \
"s_waitcnt vmcnt(0)\n" \
".LS0_w4c:\n" \
"v_subrev_u32 v241, s43, v240\n" \
"v_cmp_gt_u32 vcc, 0x100, v241\n" \
"s_cmp_eq_u64 vcc, -1\n" \
"s_cbranch_scc1 .LS0_w4d\n" \
"s_sleep 2\n" \
"s_add_u32 s50, s50, 1\n" \
"s_cmp_lt_u32 s50, 0x20000\n" \
"s_cbranch_scc1 .LS0_w4l\n" \
"s_branch .LS0_end\n" \
".LS0_w4d:\n" \
"v_readlane_b32 s43, v202, 18\n" \
"s_nop 4\n" \
"buffer_load_dwordx4 a[144:147], v201, s[8:11], s43 offen sc1\n" \
"buffer_load_dwordx4 a[148:151], v201, s[8:11], s43 offen offset:1024 sc1\n" \
"v_readlane_b32 s43, v202, 21\n" \
"s_nop 4\n" \
"buffer_load_dwordx4 a[168:171], v201, s[8:11], s43 offen sc1\n" \
"buffer_load_dwordx4 a[172:175], v201, s[8:11], s43 offen offset:1024 sc1\n" \
"v_readlane_b32 s43, v202, 24\n" \
"s_nop 4\n" \
"buffer_load_dwordx4 a[192:195], v201, s[8:11], s43 offen sc1\n" \
"buffer_load_dwordx4 a[196:199], v201, s[8:11], s43 offen offset:1024 sc1\n" \
"v_readlane_b32 s43, v202, 27\n" \
"s_nop 4\n" \
"buffer_load_dwordx4 a[216:219], v201, s[8:11], s43 offen sc1\n" \
"buffer_load_dwordx4 a[220:223], v201, s[8:11], s43 offen offset:1024 sc1\n" \
"s_mov_b32 s33, -1\n" \
"s_add_u32 s43, s30, 1\n" \
"s_mov_b32 s50, 0\n" \
".LS0_w5l:\n" \
"buffer_load_dword v240, v205, s[4:7], 0 offen sc1\n" \
"s_waitcnt vmcnt(0)\n" \
".LS0_w5c:\n" \
"v_subrev_u32 v241, s43, v240\n" \
"v_cmp_gt_u32 vcc, 0x100, v241\n" \
"s_cmp_eq_u64 vcc, -1\n" \
"s_cbranch_scc1 .LS0_w5d\n" \
"s_sleep 2\n" \
"s_add_u32 s50, s50, 1\n" \
"s_cmp_lt_u32 s50, 0x20000\n" \
"s_cbranch_scc1 .LS0_w5l\n" \
"s_branch .LS0_end\n" \
".LS0_w5d:\n" \
"v_readfirstlane_b32 s31, v241\n" \
"s_lshr_b32 s32, s31, 2\n" \
"s_xor_b32 s29, s31, s32\n" \
"s_and_b32 s29, s29, 3\n" \
"s_lshl_b32 s29, s29, 13\n" \
"s_bitset1_b32 s29, 21\n" \
"s_and_b32 s31, s31, 3\n" \
"s_lshl_b32 s31, s31, 13\n" \
"s_bitcmp1_b32 s39, 0\n" \
"s_cselect_b32 s43, s29, 0\n" \
"s_xor_b32 s31, s31, s43\n" \
"s_xor_b32 s32, s31, 0x4000\n" \
"s_mov_b32 s39, 0\n" \
".LS0_pre:\n" \
"s_barrier\n" \
"ds_read_b96 v[248:250], v252\n" \
"s_waitcnt lgkmcnt(0)\n" \
"v_readfirstlane_b32 s44, v248\n" \
"v_readfirstlane_b32 s45, v249\n" \
"v_readfirstlane_b32 s46, v250\n" \
"s_mul_i32 s42, s25, s46\n" \
"s_lshr_b32 s42, s42, 12\n" \
".LS0_cu:\n" \
"s_sub_u32 s43, s24, s44\n" \
"s_cmp_lt_i32 s43, 0\n" \
"s_cbranch_scc0 .LS0_cud\n" \
"s_add_u32 s24, s24, s25\n" \
"s_xor_b32 s31, s31, s29\n" \
"s_xor_b32 s32, s32, s29\n" \
"s_xor_b32 s47, s47, s29\n" \
"s_add_u32 s39, s39, 1\n" \
"s_branch .LS0_cu\n" \
".LS0_cud:\n" \
".LS0_loop:\n" \
"s_add_u32 s43, s40, 0\n" \
"s_and_b32 s43, s43, 3\n" \
"s_bitcmp1_b32 s43, 0\n" \
"s_cselect_b32 s47, s32, s31\n" \
"s_lshl_b32 s43, s43, 19\n" \
"s_add_u32 s47, s47, s43\n" \
"s_sub_u32 s43, s24, s44\n" \
"s_max_i32 s43, s43, 0xfffd0000\n" \
"s_min_i32 s43, s43, 0x4000\n" \
"s_mul_i32 s43, s43, s46\n" \
"s_ashr_i32 s43, s43, 12\n" \
"s_add_u32 s41, s45, s43\n" \
"s_branch .LS0_a1\n" \
".LS0_retry:\n" \
"s_add_u32 s51, s51, 1\n" \
"s_cmp_gt_u32 s51, 0x20000\n" \
"s_cbranch_scc1 .LS0_end\n" \
".LS0_a0:\n" \
"s_add_u32 s41, s41, s42\n" \
"s_add_u32 s24, s24, s25\n" \
"s_xor_b32 s31, s31, s29\n" \
"s_xor_b32 s32, s32, s29\n" \
"s_xor_b32 s47, s47, s29\n" \
"s_xor_b32 s48, s48, s29\n" \
"s_add_u32 s39, s39, 1\n" \
".p2align 6\n" \
".LS0_a1:\n" \
"s_memtime vcc\n" \
"s_waitcnt lgkmcnt(0)\n" \
"s_sub_u32 s43, s41, vcc_lo\n" \
"s_cmp_gt_i32 s43, 128\n" \
"s_cbranch_scc1 .LS0_a1\n" \
"s_cmp_lt_i32 s43, s37\n" \
"s_cbranch_scc1 .LS0_a0\n" \
"s_max_i32 s43, s43, 0\n" \
"s_and_b32 s43, s43, -4\n" \
"s_getpc_b64 vcc\n" \
".LS0_a3:\n" \
"s_sub_u32 s43, .LS0_a4-.LS0_a3, s43\n" \
"s_add_u32 vcc_lo, vcc_lo, s43\n" \
"s_addc_u32 vcc_hi, vcc_hi, 0\n" \
"s_setpc_b64 vcc\n" \
".rept 32\n" \
"s_nop 0\n" \
".endr\n" \
".LS0_a4:\n" \
"buffer_load_dwordx4 v[0:3], v208, s[4:7], s47 offen sc1\n" \
"s_nop 9\n" \
"buffer_load_dwordx4 v[4:7], v208, s[4:7], s47 offen offset:1024 sc1\n" \
"s_nop 9\n" \
"buffer_load_dwordx4 v[8:11], v208, s[4:7], s47 offen offset:2048 sc1\n" \
"s_nop 9\n" \
"buffer_load_dwordx4 v[12:15], v210, s[4:7], s47 offen sc1\n" \
"s_add_u32 s35, s35, 1\n" \
"v_mov_b32 v28, 0\n" \
"v_mov_b32 v29, 0\n" \
"v_mov_b32 v30, 0\n" \
"v_mov_b32 v31, 0\n" \
"v_mov_b32 v32, 0\n" \
"v_mov_b32 v33, 0\n" \
"v_mov_b32 v34, 0\n" \
"v_mov_b32 v35, 0\n" \
"v_mov_b32 v36, 0\n" \
"v_mov_b32 v37, 0\n" \
"v_mov_b32 v38, 0\n" \
"v_mov_b32 v39, 0\n" \
"v_mov_b32 v40, 0\n" \
"v_mov_b32 v41, 0\n" \
"v_mov_b32 v42, 0\n" \
"v_mov_b32 v43, 0\n" \
"v_mov_b32 v44, 0\n" \
"v_mov_b32 v45, 0\n" \
"v_mov_b32 v46, 0\n" \
"v_mov_b32 v47, 0\n" \
"v_mov_b32 v48, 0\n" \
"v_mov_b32 v49, 0\n" \
"v_mov_b32 v50, 0\n" \
"v_mov_b32 v51, 0\n" \
"v_mov_b32 v52, 0\n" \
"v_mov_b32 v53, 0\n" \
"v_mov_b32 v54, 0\n" \
"v_mov_b32 v55, 0\n" \
"v_mov_b32 v56, 0\n" \
"v_mov_b32 v57, 0\n" \
"v_mov_b32 v58, 0\n" \
"v_mov_b32 v59, 0\n" \
"s_waitcnt vmcnt(3)\n" \
"v_smfmac_f32_16x16x64_bf16 v[28:31], v[0:3], a[0:7], v200\n" \
"v_smfmac_f32_16x16x64_bf16 v[32:35], v[0:3], a[8:15], v200\n" \
"v_smfmac_f32_16x16x64_bf16 v[36:39], v[0:3], a[16:23], v200\n" \
"v_smfmac_f32_16x16x64_bf16 v[40:43], v[0:3], a[24:31], v200\n" \
"v_smfmac_f32_16x16x64_bf16 v[44:47], v[0:3], a[32:39], v200\n" \
"v_smfmac_f32_16x16x64_bf16 v[48:51], v[0:3], a[40:47], v200\n" \
"v_smfmac_f32_16x16x64_bf16 v[52:55], v[0:3], a[48:55], v200\n" \
"v_smfmac_f32_16x16x64_bf16 v[56:59], v[0:3], a[56:63], v200\n" \
"s_waitcnt vmcnt(2)\n" \
"v_smfmac_f32_16x16x64_bf16 v[28:31], v[4:7], a[64:71], v200\n" \
"v_smfmac_f32_16x16x64_bf16 v[32:35], v[4:7], a[72:79], v200\n" \
"v_smfmac_f32_16x16x64_bf16 v[36:39], v[4:7], a[80:87], v200\n" \
"s_waitcnt vmcnt(1)\n" \
"v_smfmac_f32_16x16x64_bf16 v[28:31], v[8:11], a[88:95], v200\n" \
"s_nop 2\n" \
"v_smfmac_f32_16x16x64_bf16 v[40:43], v[4:7], a[96:103], v200\n" \
"s_nop 2\n" \
"v_smfmac_f32_16x16x64_bf16 v[32:35], v[8:11], a[104:111], v200\n" \
"s_nop 0\n" \
"v_cvt_pk_bf16_f32 v128, v28, v29\n" \
"v_cvt_pk_bf16_f32 v129, v30, v31\n" \
"v_smfmac_f32_16x16x64_bf16 v[44:47], v[4:7], a[112:119], v200\n" \
"ds_write_b64 v211, v[128:129] offset:0\n" \
"s_nop 1\n" \
"v_smfmac_f32_16x16x64_bf16 v[36:39], v[8:11], a[120:127], v200\n" \
"s_nop 0\n" \
"v_cvt_pk_bf16_f32 v130, v32, v33\n" \
"v_cvt_pk_bf16_f32 v131, v34, v35\n" \
"v_smfmac_f32_16x16x64_bf16 v[44:47], v[8:11], a[128:135], v200\n" \
"ds_write_b64 v211, v[130:131] offset:384\n" \
"s_nop 1\n" \
"v_smfmac_f32_16x16x64_bf16 v[40:43], v[8:11], a[136:143], v200\n" \
"s_waitcnt vmcnt(0)\n" \
"v_cvt_pk_bf16_f32 v132, v36, v37\n" \
"v_cvt_pk_bf16_f32 v133, v38, v39\n" \
"v_smfmac_f32_16x16x64_bf16 v[44:47], v[12:15], a[144:151], v200\n" \
"ds_write_b64 v211, v[132:133] offset:2560\n" \
"s_nop 1\n" \
"v_smfmac_f32_16x16x64_bf16 v[48:51], v[4:7], a[152:159], v200\n" \
"s_nop 0\n" \
"v_cvt_pk_bf16_f32 v134, v40, v41\n" \
"v_cvt_pk_bf16_f32 v135, v42, v43\n" \
"v_smfmac_f32_16x16x64_bf16 v[48:51], v[8:11], a[160:167], v200\n" \
"ds_write_b64 v211, v[134:135] offset:4736\n" \
"v_cvt_pk_bf16_f32 v136, v44, v45\n" \
"v_cvt_pk_bf16_f32 v137, v46, v47\n" \
"v_smfmac_f32_16x16x64_bf16 v[48:51], v[12:15], a[168:175], v200\n" \
"ds_write_b64 v211, v[136:137] offset:9216\n" \
"s_nop 1\n" \
"v_smfmac_f32_16x16x64_bf16 v[52:55], v[4:7], a[176:183], v200\n" \
"s_nop 2\n" \
"v_smfmac_f32_16x16x64_bf16 v[52:55], v[8:11], a[184:191], v200\n" \
"s_nop 0\n" \
"v_cvt_pk_bf16_f32 v138, v48, v49\n" \
"v_cvt_pk_bf16_f32 v139, v50, v51\n" \
"v_smfmac_f32_16x16x64_bf16 v[52:55], v[12:15], a[192:199], v200\n" \
"ds_write_b64 v211, v[138:139] offset:9600\n" \
"s_waitcnt lgkmcnt(2)\n" \
"s_barrier\n" \
"v_smfmac_f32_16x16x64_bf16 v[56:59], v[4:7], a[200:207], v200\n" \
"s_nop 2\n" \
"v_smfmac_f32_16x16x64_bf16 v[56:59], v[8:11], a[208:215], v200\n" \
"s_nop 2\n" \
"v_smfmac_f32_16x16x64_bf16 v[56:59], v[12:15], a[216:223], v200\n" \
"v_cvt_pk_bf16_f32 v140, v52, v53\n" \
"v_cvt_pk_bf16_f32 v141, v54, v55\n" \
"s_nop 1\n" \
"ds_write_b64 v211, v[140:141] offset:11776\n" \
"s_nop 2\n" \
"v_cvt_pk_bf16_f32 v142, v56, v57\n" \
"v_cvt_pk_bf16_f32 v143, v58, v59\n" \
"ds_write_b64 v211, v[142:143] offset:13952\n" \
"s_waitcnt lgkmcnt(2)\n" \
"s_barrier\n" \
"s_waitcnt lgkmcnt(1)\n" \
"s_barrier\n" \
"s_waitcnt lgkmcnt(0)\n" \
"s_barrier\n" \
"s_nop 3\n" \
"ds_read_b32 v64, v252 offset:20\n" \
"s_waitcnt lgkmcnt(0)\n" \
"v_readfirstlane_b32 s43, v64\n" \
"s_cmp_eq_u32 s43, s35\n" \
"s_cbranch_scc1 .LS0_retry\n" \
"s_nop 15\n" \
"v_max3_u32 v64, v0, v0, v1\n" \
"v_max3_u32 v64, v64, v2, v3\n" \
"v_max3_u32 v64, v64, v4, v5\n" \
"v_max3_u32 v64, v64, v6, v7\n" \
"v_max3_u32 v64, v64, v8, v9\n" \
"v_max3_u32 v64, v64, v10, v11\n" \
"v_max3_u32 v64, v64, v12, v13\n" \
"v_max3_u32 v64, v64, v14, v15\n" \
"v_cmp_eq_u32 vcc, -1, v64\n" \
"s_cbranch_vccnz .LS0_veto\n" \
".LS0_back:\n" \
"s_waitcnt lgkmcnt(0)\n" \
"s_barrier\n" \
"ds_read_b32 v64, v252 offset:16\n" \
"s_waitcnt lgkmcnt(0)\n" \
"v_readfirstlane_b32 s43, v64\n" \
"s_cmp_eq_u32 s43, s35\n" \
"s_cbranch_scc1 .LS0_retry\n" \
"s_waitcnt lgkmcnt(0)\n" \
"s_barrier\n" \
"ds_read_b64 v[246:247], v252 offset:24\n" \
"ds_read_b96 v[248:250], v252\n" \
"s_waitcnt lgkmcnt(0)\n" \
"v_readfirstlane_b32 s44, v248\n" \
"v_readfirstlane_b32 s45, v249\n" \
"v_readfirstlane_b32 s46, v250\n" \
"v_readfirstlane_b32 s26, v246\n" \
"v_readfirstlane_b32 s27, v247\n" \
"s_mul_i32 s42, s25, s46\n" \
"s_lshr_b32 s42, s42, 12\n" \
"s_add_u32 s40, s40, 1\n" \
"s_add_u32 s24, s24, s26\n" \
"s_add_u32 s24, s24, s27\n" \
"s_cmp_eq_u32 s40, 0\n" \
"s_cbranch_scc1 .LS0_wet\n" \
"s_cmp_lt_u32 s40, 63\n" \
"s_cbranch_scc1 .LS0_loop\n" \
".LS0_end:\n" \
"s_endpgm\n" \
".LS0_veto:\n" \
"v_mov_b32 v64, s35\n" \
"ds_write_b32 v252, v64 offset:16\n" \
"s_branch .LS0_back\n" \
".p2align 6\n" \
"fk_role_S1:\n" \
"ds_read_b32 v200, v255 offset:0\n" \
"ds_read_b32 v201, v255 offset:256\n" \
"ds_read_b32 v202, v255 offset:512\n" \
"ds_read_b32 v203, v255 offset:768\n" \
"ds_read_b32 v204, v255 offset:1024\n" \
"ds_read_b32 v205, v255 offset:1280\n" \
"ds_read_b32 v206, v255 offset:1536\n" \
"ds_read_b32 v207, v255 offset:1792\n" \
"ds_read_b32 v208, v255 offset:2048\n" \
"ds_read_b32 v209, v255 offset:2304\n" \
"ds_read_b32 v210, v255 offset:2560\n" \
"ds_read_b32 v211, v255 offset:2816\n" \
"ds_read_b32 v212, v255 offset:3072\n" \
"ds_read_b32 v213, v255 offset:3328\n" \
"ds_read_b32 v214, v255 offset:3584\n" \
"ds_read_b32 v215, v255 offset:3840\n" \
"ds_read_b32 v254, v255 offset:7936\n" \
"s_waitcnt lgkmcnt(0)\n" \
"v_readlane_b32 s4, v254, 0\n" \
"v_readlane_b32 s5, v254, 1\n" \
"v_readlane_b32 s6, v254, 2\n" \
"v_readlane_b32 s7, v254, 3\n" \
"v_readlane_b32 s8, v254, 4\n" \
"v_readlane_b32 s9, v254, 5\n" \
"v_readlane_b32 s10, v254, 6\n" \
"v_readlane_b32 s11, v254, 7\n" \
"v_readlane_b32 s12, v254, 8\n" \
"v_readlane_b32 s13, v254, 9\n" \
"v_readlane_b32 s14, v254, 10\n" \
"v_readlane_b32 s15, v254, 11\n" \
"v_readlane_b32 s16, v254, 12\n" \
"v_readlane_b32 s17, v254, 13\n" \
"v_readlane_b32 s18, v254, 14\n" \
"v_readlane_b32 s19, v254, 15\n" \
"v_readlane_b32 s20, v254, 16\n" \
"v_readlane_b32 s21, v254, 17\n" \
"v_readlane_b32 s22, v254, 18\n" \
"v_readlane_b32 s23, v254, 19\n" \
"v_readlane_b32 s24, v254, 20\n" \
"v_readlane_b32 s25, v254, 21\n" \
"v_readlane_b32 s26, v254, 22\n" \
"v_readlane_b32 s27, v254, 23\n" \
"v_readlane_b32 s28, v254, 24\n" \
"v_readlane_b32 s29, v254, 25\n" \
"v_readlane_b32 s30, v254, 26\n" \
"v_readlane_b32 s31, v254, 27\n" \
"v_readlane_b32 s32, v254, 28\n" \
"v_readlane_b32 s33, v254, 29\n" \
"v_readlane_b32 s34, v254, 30\n" \
"v_readlane_b32 s35, v254, 31\n" \
"v_readlane_b32 s36, v254, 32\n" \
"v_readlane_b32 s37, v254, 33\n" \
"v_readlane_b32 s38, v254, 34\n" \
"v_readlane_b32 s39, v254, 35\n" \
"s_mov_b32 s40, 0\n" \
"s_mov_b32 s51, 0\n" \
"s_nop 0\n" \
"v_mov_b32 v252, s38\n" \
"s_nop 4\n" \
"v_mov_b32 v188, -1\n" \
"s_mov_b32 s28, s33\n" \
"s_mov_b32 s33, -1\n" \
"v_writelane_b32 v251, s6, 0\n" \
"v_writelane_b32 v251, s10, 1\n" \
"v_writelane_b32 v251, s14, 2\n" \
"v_writelane_b32 v251, s18, 3\n" \
"s_mov_b32 s6, 0\n" \
"s_mov_b32 s10, 0\n" \
"s_mov_b32 s14, 0\n" \
"s_mov_b32 s18, 0\n" \
"s_mov_b32 s40, -1\n" \
"s_branch .LS1_pre\n" \
".LS1_wet:\n" \
"s_waitcnt vmcnt(0)\n" \
"v_readlane_b32 s6, v251, 0\n" \
"v_readlane_b32 s10, v251, 1\n" \
"v_readlane_b32 s14, v251, 2\n" \
"v_readlane_b32 s18, v251, 3\n" \
"s_nop 4\n" \
"s_mov_b32 s33, s28\n" \
"s_add_u32 s34, s33, 2\n" \
"s_add_u32 s36, s33, 4\n" \
"s_add_u32 s43, s30, s33\n" \
"s_mov_b32 s50, 0\n" \
".LS1_w6l:\n" \
"buffer_load_dword v240, v204, s[4:7], 0 offen sc1\n" \
"s_waitcnt vmcnt(0)\n" \
".LS1_w6c:\n" \
"v_subrev_u32 v241, s43, v240\n" \
"v_cmp_gt_u32 vcc, 0x100, v241\n" \
"s_cmp_eq_u64 vcc, -1\n" \
"s_cbranch_scc1 .LS1_w6d\n" \
"s_sleep 2\n" \
"s_add_u32 s50, s50, 1\n" \
"s_cmp_lt_u32 s50, 0x20000\n" \
"s_cbranch_scc1 .LS1_w6l\n" \
"s_branch .LS1_end\n" \
".LS1_w6d:\n" \
"v_readlane_b32 s43, v202, 11\n" \
"s_nop 4\n" \
"buffer_load_dwordx4 a[88:91], v201, s[8:11], s43 offen sc1\n" \
"buffer_load_dwordx4 a[92:95], v201, s[8:11], s43 offen offset:1024 sc1\n" \
"v_readlane_b32 s43, v202, 13\n" \
"s_nop 4\n" \
"buffer_load_dwordx4 a[104:107], v201, s[8:11], s43 offen sc1\n" \
"buffer_load_dwordx4 a[108:111], v201, s[8:11], s43 offen offset:1024 sc1\n" \
"v_readlane_b32 s43, v202, 15\n" \
"s_nop 4\n" \
"buffer_load_dwordx4 a[120:123], v201, s[8:11], s43 offen sc1\n" \
"buffer_load_dwordx4 a[124:127], v201, s[8:11], s43 offen offset:1024 sc1\n" \
"v_readlane_b32 s43, v202, 16\n" \
"s_nop 4\n" \
"buffer_load_dwordx4 a[128:131], v201, s[8:11], s43 offen sc1\n" \
"buffer_load_dwordx4 a[132:135], v201, s[8:11], s43 offen offset:1024 sc1\n" \
"v_readlane_b32 s43, v202, 17\n" \
"s_nop 4\n" \
"buffer_load_dwordx4 a[136:139], v201, s[8:11], s43 offen sc1\n" \
"buffer_load_dwordx4 a[140:143], v201, s[8:11], s43 offen offset:1024 sc1\n" \
"v_readlane_b32 s43, v202, 20\n" \
"s_nop 4\n" \
"buffer_load_dwordx4 a[160:163], v201, s[8:11], s43 offen sc1\n" \
"buffer_load_dwordx4 a[164:167], v201, s[8:11], s43 offen offset:1024 sc1\n" \
"v_readlane_b32 s43, v202, 23\n" \
"s_nop 4\n" \
"buffer_load_dwordx4 a[184:187], v201, s[8:11], s43 offen sc1\n" \
"buffer_load_dwordx4 a[188:191], v201, s[8:11], s43 offen offset:1024 sc1\n" \
"v_readlane_b32 s43, v202, 26\n" \
"s_nop 4\n" \
"buffer_load_dwordx4 a[208:211], v201, s[8:11], s43 offen sc1\n" \
"buffer_load_dwordx4 a[212:215], v201, s[8:11], s43 offen offset:1024 sc1\n" \
"s_add_u32 s43, s30, s34\n" \
"s_mov_b32 s50, 0\n" \
"s_branch .LS1_w7c\n" \
".LS1_w7l:\n" \
"buffer_load_dword v240, v204, s[4:7], 0 offen sc1\n" \
"s_waitcnt vmcnt(0)\n" \
".LS1_w7c:\n" \
"v_subrev_u32 v241, s43, v240\n" \
"v_cmp_gt_u32 vcc, 0x100, v241\n" \
"s_cmp_eq_u64 vcc, -1\n" \
"s_cbranch_scc1 .LS1_w7d\n" \
"s_sleep 2\n" \
"s_add_u32 s50, s50, 1\n" \
"s_cmp_lt_u32 s50, 0x20000\n" \
"s_cbranch_scc1 .LS1_w7l\n" \
"s_branch .LS1_end\n" \
".LS1_w7d:\n" \
"v_readlane_b32 s43, v202, 0\n" \
"s_nop 4\n" \
"buffer_load_dwordx4 a[0:3], v201, s[8:11], s43 offen sc1\n" \
"buffer_load_dwordx4 a[4:7], v201, s[8:11], s43 offen offset:1024 sc1\n" \
"v_readlane_b32 s43, v202, 1\n" \
"s_nop 4\n" \
"buffer_load_dwordx4 a[8:11], v201, s[8:11], s43 offen sc1\n" \
"buffer_load_dwordx4 a[12:15], v201, s[8:11], s43 offen offset:1024 sc1\n" \
"v_readlane_b32 s43, v202, 2\n" \
"s_nop 4\n" \
"buffer_load_dwordx4 a[16:19], v201, s[8:11], s43 offen sc1\n" \
"buffer_load_dwordx4 a[20:23], v201, s[8:11], s43 offen offset:1024 sc1\n" \
"v_readlane_b32 s43, v202, 3\n" \
"s_nop 4\n" \
"buffer_load_dwordx4 a[24:27], v201, s[8:11], s43 offen sc1\n" \
"buffer_load_dwordx4 a[28:31], v201, s[8:11], s43 offen offset:1024 sc1\n" \
"v_readlane_b32 s43, v202, 4\n" \
"s_nop 4\n" \
"buffer_load_dwordx4 a[32:35], v201, s[8:11], s43 offen sc1\n" \
"buffer_load_dwordx4 a[36:39], v201, s[8:11], s43 offen offset:1024 sc1\n" \
"v_readlane_b32 s43, v202, 5\n" \
"s_nop 4\n" \
"buffer_load_dwordx4 a[40:43], v201, s[8:11], s43 offen sc1\n" \
"buffer_load_dwordx4 a[44:47], v201, s[8:11], s43 offen offset:1024 sc1\n" \
"v_readlane_b32 s43, v202, 6\n" \
"s_nop 4\n" \
"buffer_load_dwordx4 a[48:51], v201, s[8:11], s43 offen sc1\n" \
"buffer_load_dwordx4 a[52:55], v201, s[8:11], s43 offen offset:1024 sc1\n" \
"v_readlane_b32 s43, v202, 7\n" \
"s_nop 4\n" \
"buffer_load_dwordx4 a[56:59], v201, s[8:11], s43 offen sc1\n" \
"buffer_load_dwordx4 a[60:63], v201, s[8:11], s43 offen offset:1024 sc1\n" \
"s_add_u32 s43, s30, s36\n" \
"s_mov_b32 s50, 0\n" \
"s_branch .LS1_w8c\n" \
".LS1_w8l:\n" \
"buffer_load_dword v240, v204, s[4:7], 0 offen sc1\n" \
"s_waitcnt vmcnt(0)\n" \
".LS1_w8c:\n" \
"v_subrev_u32 v241, s43, v240\n" \
"v_cmp_gt_u32 vcc, 0x100, v241\n" \
"s_cmp_eq_u64 vcc, -1\n" \
"s_cbranch_scc1 .LS1_w8d\n" \
"s_sleep 2\n" \
"s_add_u32 s50, s50, 1\n" \
"s_cmp_lt_u32 s50, 0x20000\n" \
"s_cbranch_scc1 .LS1_w8l\n" \
"s_branch .LS1_end\n" \
".LS1_w8d:\n" \
"v_readlane_b32 s43, v202, 8\n" \
"s_nop 4\n" \
"buffer_load_dwordx4 a[64:67], v201, s[8:11], s43 offen sc1\n" \
"buffer_load_dwordx4 a[68:71], v201, s[8:11], s43 offen offset:1024 sc1\n" \
"v_readlane_b32 s43, v202, 9\n" \
"s_nop 4\n" \
"buffer_load_dwordx4 a[72:75], v201, s[8:11], s43 offen sc1\n" \
"buffer_load_dwordx4 a[76:79], v201, s[8:11], s43 offen offset:1024 sc1\n" \
"v_readlane_b32 s43, v202, 10\n" \
"s_nop 4\n" \
"buffer_load_dwordx4 a[80:83], v201, s[8:11], s43 offen sc1\n" \
"buffer_load_dwordx4 a[84:87], v201, s[8:11], s43 offen offset:1024 sc1\n" \
"v_readlane_b32 s43, v202, 12\n" \
"s_nop 4\n" \
"buffer_load_dwordx4 a[96:99], v201, s[8:11], s43 offen sc1\n" \
"buffer_load_dwordx4 a[100:103], v201, s[8:11], s43 offen offset:1024 sc1\n" \
"v_readlane_b32 s43, v202, 14\n" \
"s_nop 4\n" \
"buffer_load_dwordx4 a[112:115], v201, s[8:11], s43 offen sc1\n" \
"buffer_load_dwordx4 a[116:119], v201, s[8:11], s43 offen offset:1024 sc1\n" \
"v_readlane_b32 s43, v202, 19\n" \
"s_nop 4\n" \
"buffer_load_dwordx4 a[152:155], v201, s[8:11], s43 offen sc1\n" \
"buffer_load_dwordx4 a[156:159], v201, s[8:11], s43 offen offset:1024 sc1\n" \
"v_readlane_b32 s43, v202, 22\n" \
"s_nop 4\n" \
"buffer_load_dwordx4 a[176:179], v201, s[8:11], s43 offen sc1\n" \
"buffer_load_dwordx4 a[180:183], v201, s[8:11], s43 offen offset:1024 sc1\n" \
"v_readlane_b32 s43, v202, 25\n" \
"s_nop 4\n" \
"buffer_load_dwordx4 a[200:203], v201, s[8:11], s43 offen sc1\n" \
"buffer_load_dwordx4 a[204:207], v201, s[8:11], s43 offen offset:1024 sc1\n" \
"s_add_u32 s43, s30, 7\n" \
"s_mov_b32 s50, 0\n" \
"s_branch .LS1_w9c\n" \
".LS1_w9l:\n" \
"buffer_load_dword v240, v204, s[4:7], 0 offen sc1\n" \
"s_waitcnt vmcnt(0)\n" \
".LS1_w9c:\n" \
"v_subrev_u32 v241, s43, v240\n" \
"v_cmp_gt_u32 vcc, 0x100, v241\n" \
"s_cmp_eq_u64 vcc, -1\n" \
"s_cbranch_scc1 .LS1_w9d\n" \
"s_sleep 2\n" \
"s_add_u32 s50, s50, 1\n" \
"s_cmp_lt_u32 s50, 0x20000\n" \
"s_cbranch_scc1 .LS1_w9l\n" \
"s_branch .LS1_end\n" \
".LS1_w9d:\n" \
"v_readlane_b32 s43, v202, 18\n" \
"s_nop 4\n" \
"buffer_load_dwordx4 a[144:147], v201, s[8:11], s43 offen sc1\n" \
"buffer_load_dwordx4 a[148:151], v201, s[8:11], s43 offen offset:1024 sc1\n" \
"v_readlane_b32 s43, v202, 21\n" \
"s_nop 4\n" \
"buffer_load_dwordx4 a[168:171], v201, s[8:11], s43 offen sc1\n" \
"buffer_load_dwordx4 a[172:175], v201, s[8:11], s43 offen offset:1024 sc1\n" \
"v_readlane_b32 s43, v202, 24\n" \
"s_nop 4\n" \
"buffer_load_dwordx4 a[192:195], v201, s[8:11], s43 offen sc1\n" \
"buffer_load_dwordx4 a[196:199], v201, s[8:11], s43 offen offset:1024 sc1\n" \
"v_readlane_b32 s43, v202, 27\n" \
"s_nop 4\n" \
"buffer_load_dwordx4 a[216:219], v201, s[8:11], s43 offen sc1\n" \
"buffer_load_dwordx4 a[220:223], v201, s[8:11], s43 offen offset:1024 sc1\n" \
"s_mov_b32 s33, -1\n" \
"s_add_u32 s43, s30, 1\n" \
"s_mov_b32 s50, 0\n" \
".LS1_w10l:\n" \
"buffer_load_dword v240, v205, s[4:7], 0 offen sc1\n" \
"s_waitcnt vmcnt(0)\n" \
".LS1_w10c:\n" \
"v_subrev_u32 v241, s43, v240\n" \
"v_cmp_gt_u32 vcc, 0x100, v241\n" \
"s_cmp_eq_u64 vcc, -1\n" \
"s_cbranch_scc1 .LS1_w10d\n" \
"s_sleep 2\n" \
"s_add_u32 s50, s50, 1\n" \
"s_cmp_lt_u32 s50, 0x20000\n" \
"s_cbranch_scc1 .LS1_w10l\n" \
"s_branch .LS1_end\n" \
".LS1_w10d:\n" \
"v_readfirstlane_b32 s31, v241\n" \
"s_lshr_b32 s32, s31, 2\n" \
"s_xor_b32 s29, s31, s32\n" \
"s_and_b32 s29, s29, 3\n" \
"s_lshl_b32 s29, s29, 13\n" \
"s_bitset1_b32 s29, 21\n" \
"s_and_b32 s31, s31, 3\n" \
"s_lshl_b32 s31, s31, 13\n" \
"s_bitcmp1_b32 s39, 0\n" \
"s_cselect_b32 s43, s29, 0\n" \
"s_xor_b32 s31, s31, s43\n" \
"s_xor_b32 s32, s31, 0x4000\n" \
"s_mov_b32 s39, 0\n" \
".LS1_pre:\n" \
"s_barrier\n" \
"ds_read_b96 v[248:250], v252\n" \
"s_waitcnt lgkmcnt(0)\n" \
"v_readfirstlane_b32 s44, v248\n" \
"v_readfirstlane_b32 s45, v249\n" \
"v_readfirstlane_b32 s46, v250\n" \
"s_mul_i32 s42, s25, s46\n" \
"s_lshr_b32 s42, s42, 12\n" \
".LS1_cu:\n" \
"s_sub_u32 s43, s24, s44\n" \
"s_cmp_lt_i32 s43, 0\n" \
"s_cbranch_scc0 .LS1_cud\n" \
"s_add_u32 s24, s24, s25\n" \
"s_xor_b32 s31, s31, s29\n" \
"s_xor_b32 s32, s32, s29\n" \
"s_xor_b32 s47, s47, s29\n" \
"s_add_u32 s39, s39, 1\n" \
"s_branch .LS1_cu\n" \
".LS1_cud:\n" \
".LS1_loop:\n" \
"s_add_u32 s43, s40, 0\n" \
"s_and_b32 s43, s43, 3\n" \
"s_bitcmp1_b32 s43, 0\n" \
"s_cselect_b32 s47, s32, s31\n" \
"s_lshl_b32 s43, s43, 19\n" \
"s_add_u32 s47, s47, s43\n" \
"s_sub_u32 s43, s24, s44\n" \
"s_max_i32 s43, s43, 0xfffd0000\n" \
"s_min_i32 s43, s43, 0x4000\n" \
"s_mul_i32 s43, s43, s46\n" \
"s_ashr_i32 s43, s43, 12\n" \
"s_add_u32 s41, s45, s43\n" \
"s_add_u32 s41, s41, 4\n" \
"s_branch .LS1_a1\n" \
".LS1_retry:\n" \
"s_add_u32 s51, s51, 1\n" \
"s_cmp_gt_u32 s51, 0x20000\n" \
"s_cbranch_scc1 .LS1_end\n" \
".LS1_a0:\n" \
"s_add_u32 s41, s41, s42\n" \
"s_add_u32 s24, s24, s25\n" \
"s_xor_b32 s31, s31, s29\n" \
"s_xor_b32 s32, s32, s29\n" \
"s_xor_b32 s47, s47, s29\n" \
"s_xor_b32 s48, s48, s29\n" \
"s_add_u32 s39, s39, 1\n" \
".p2align 6\n" \
".LS1_a1:\n" \
"s_memtime vcc\n" \
"s_waitcnt lgkmcnt(0)\n" \
"s_sub_u32 s43, s41, vcc_lo\n" \
"s_cmp_gt_i32 s43, 128\n" \
"s_cbranch_scc1 .LS1_a1\n" \
"s_cmp_lt_i32 s43, s37\n" \
"s_cbranch_scc1 .LS1_a0\n" \
"s_max_i32 s43, s43, 0\n" \
"s_and_b32 s43, s43, -4\n" \
"s_getpc_b64 vcc\n" \
".LS1_a3:\n" \
"s_sub_u32 s43, .LS1_a4-.LS1_a3, s43\n" \
"s_add_u32 vcc_lo, vcc_lo, s43\n" \
"s_addc_u32 vcc_hi, vcc_hi, 0\n" \
"s_setpc_b64 vcc\n" \
".rept 32\n" \
"s_nop 0\n" \
".endr\n" \
".LS1_a4:\n" \
"buffer_load_dwordx4 v[0:3], v208, s[4:7], s47 offen offset:1024 sc1\n" \
"s_nop 9\n" \
"buffer_load_dwordx4 v[4:7], v208, s[4:7], s47 offen offset:2048 sc1\n" \
"s_nop 9\n" \
"buffer_load_dwordx4 v[8:11], v208, s[4:7], s47 offen sc1\n" \
"s_nop 9\n" \
"buffer_load_dwordx4 v[12:15], v210, s[4:7], s47 offen sc1\n" \
"s_add_u32 s35, s35, 1\n" \
"v_mov_b32 v28, 0\n" \
"v_mov_b32 v29, 0\n" \
"v_mov_b32 v30, 0\n" \
"v_mov_b32 v31, 0\n" \
"v_mov_b32 v32, 0\n" \
"v_mov_b32 v33, 0\n" \
"v_mov_b32 v34, 0\n" \
"v_mov_b32 v35, 0\n" \
"v_mov_b32 v36, 0\n" \
"v_mov_b32 v37, 0\n" \
"v_mov_b32 v38, 0\n" \
"v_mov_b32 v39, 0\n" \
"v_mov_b32 v40, 0\n" \
"v_mov_b32 v41, 0\n" \
"v_mov_b32 v42, 0\n" \
"v_mov_b32 v43, 0\n" \
"v_mov_b32 v44, 0\n" \
"v_mov_b32 v45, 0\n" \
"v_mov_b32 v46, 0\n" \
"v_mov_b32 v47, 0\n" \
"v_mov_b32 v48, 0\n" \
"v_mov_b32 v49, 0\n" \
"v_mov_b32 v50, 0\n" \
"v_mov_b32 v51, 0\n" \
"v_mov_b32 v52, 0\n" \
"v_mov_b32 v53, 0\n" \
"v_mov_b32 v54, 0\n" \
"v_mov_b32 v55, 0\n" \
"v_mov_b32 v56, 0\n" \
"v_mov_b32 v57, 0\n" \
"v_mov_b32 v58, 0\n" \
"v_mov_b32 v59, 0\n" \
"s_waitcnt vmcnt(3)\n" \
"v_smfmac_f32_16x16x64_bf16 v[28:31], v[0:3], a[0:7], v200\n" \
"v_smfmac_f32_16x16x64_bf16 v[32:35], v[0:3], a[8:15], v200\n" \
"v_smfmac_f32_16x16x64_bf16 v[36:39], v[0:3], a[16:23], v200\n" \
"v_smfmac_f32_16x16x64_bf16 v[40:43], v[0:3], a[24:31], v200\n" \
"v_smfmac_f32_16x16x64_bf16 v[44:47], v[0:3], a[32:39], v200\n" \
"v_smfmac_f32_16x16x64_bf16 v[48:51], v[0:3], a[40:47], v200\n" \
"v_smfmac_f32_16x16x64_bf16 v[52:55], v[0:3], a[48:55], v200\n" \
"v_smfmac_f32_16x16x64_bf16 v[56:59], v[0:3], a[56:63], v200\n" \
"s_waitcnt vmcnt(2)\n" \
"v_smfmac_f32_16x16x64_bf16 v[28:31], v[4:7], a[64:71], v200\n" \
"v_smfmac_f32_16x16x64_bf16 v[32:35], v[4:7], a[72:79], v200\n" \
"v_smfmac_f32_16x16x64_bf16 v[36:39], v[4:7], a[80:87], v200\n" \
"s_waitcnt vmcnt(1)\n" \
"v_smfmac_f32_16x16x64_bf16 v[28:31], v[8:11], a[88:95], v200\n" \
"s_nop 2\n" \
"v_smfmac_f32_16x16x64_bf16 v[40:43], v[4:7], a[96:103], v200\n" \
"s_nop 2\n" \
"v_smfmac_f32_16x16x64_bf16 v[32:35], v[8:11], a[104:111], v200\n" \
"s_nop 0\n" \
"v_cvt_pk_bf16_f32 v128, v28, v29\n" \
"v_cvt_pk_bf16_f32 v129, v30, v31\n" \
"v_smfmac_f32_16x16x64_bf16 v[44:47], v[4:7], a[112:119], v200\n" \
"ds_write_b64 v211, v[128:129] offset:128\n" \
"s_nop 1\n" \
"v_smfmac_f32_16x16x64_bf16 v[36:39], v[8:11], a[120:127], v200\n" \
"s_nop 0\n" \
"v_cvt_pk_bf16_f32 v130, v32, v33\n" \
"v_cvt_pk_bf16_f32 v131, v34, v35\n" \
"v_smfmac_f32_16x16x64_bf16 v[44:47], v[8:11], a[128:135], v200\n" \
"ds_write_b64 v211, v[130:131] offset:2304\n" \
"s_nop 1\n" \
"v_smfmac_f32_16x16x64_bf16 v[40:43], v[8:11], a[136:143], v200\n" \
"s_waitcnt vmcnt(0)\n" \
"v_cvt_pk_bf16_f32 v132, v36, v37\n" \
"v_cvt_pk_bf16_f32 v133, v38, v39\n" \
"v_smfmac_f32_16x16x64_bf16 v[44:47], v[12:15], a[144:151], v200\n" \
"ds_write_b64 v211, v[132:133] offset:2688\n" \
"s_nop 1\n" \
"v_smfmac_f32_16x16x64_bf16 v[48:51], v[4:7], a[152:159], v200\n" \
"s_nop 0\n" \
"v_cvt_pk_bf16_f32 v134, v40, v41\n" \
"v_cvt_pk_bf16_f32 v135, v42, v43\n" \
"v_smfmac_f32_16x16x64_bf16 v[48:51], v[8:11], a[160:167], v200\n" \
"ds_write_b64 v211, v[134:135] offset:4864\n" \
"v_cvt_pk_bf16_f32 v136, v44, v45\n" \
"v_cvt_pk_bf16_f32 v137, v46, v47\n" \
"v_smfmac_f32_16x16x64_bf16 v[48:51], v[12:15], a[168:175], v200\n" \
"ds_write_b64 v211, v[136:137] offset:9344\n" \
"s_nop 1\n" \
"v_smfmac_f32_16x16x64_bf16 v[52:55], v[4:7], a[176:183], v200\n" \
"s_nop 2\n" \
"v_smfmac_f32_16x16x64_bf16 v[52:55], v[8:11], a[184:191], v200\n" \
"s_nop 0\n" \
"v_cvt_pk_bf16_f32 v138, v48, v49\n" \
"v_cvt_pk_bf16_f32 v139, v50, v51\n" \
"v_smfmac_f32_16x16x64_bf16 v[52:55], v[12:15], a[192:199], v200\n" \
"ds_write_b64 v211, v[138:139] offset:11520\n" \
"s_waitcnt lgkmcnt(2)\n" \
"s_barrier\n" \
"v_smfmac_f32_16x16x64_bf16 v[56:59], v[4:7], a[200:207], v200\n" \
"s_nop 2\n" \
"v_smfmac_f32_16x16x64_bf16 v[56:59], v[8:11], a[208:215], v200\n" \
"s_nop 2\n" \
"v_smfmac_f32_16x16x64_bf16 v[56:59], v[12:15], a[216:223], v200\n" \
"v_cvt_pk_bf16_f32 v140, v52, v53\n" \
"v_cvt_pk_bf16_f32 v141, v54, v55\n" \
"s_nop 1\n" \
"ds_write_b64 v211, v[140:141] offset:11904\n" \
"s_nop 2\n" \
"v_cvt_pk_bf16_f32 v142, v56, v57\n" \
"v_cvt_pk_bf16_f32 v143, v58, v59\n" \
"ds_write_b64 v211, v[142:143] offset:14080\n" \
"s_waitcnt lgkmcnt(2)\n" \
"s_barrier\n" \
"s_waitcnt lgkmcnt(1)\n" \
"s_barrier\n" \
"s_waitcnt lgkmcnt(0)\n" \
"s_barrier\n" \
"s_nop 3\n" \
"ds_read_b32 v64, v252 offset:20\n" \
"s_waitcnt lgkmcnt(0)\n" \
"v_readfirstlane_b32 s43, v64\n" \
"s_cmp_eq_u32 s43, s35\n" \
"s_cbranch_scc1 .LS1_retry\n" \
"s_nop 15\n" \
"v_max3_u32 v64, v0, v0, v1\n" \
"v_max3_u32 v64, v64, v2, v3\n" \
"v_max3_u32 v64, v64, v4, v5\n" \
"v_max3_u32 v64, v64, v6, v7\n" \
"v_max3_u32 v64, v64, v8, v9\n" \
"v_max3_u32 v64, v64, v10, v11\n" \
"v_max3_u32 v64, v64, v12, v13\n" \
"v_max3_u32 v64, v64, v14, v15\n" \
"v_cmp_eq_u32 vcc, -1, v64\n" \
"s_cbranch_vccnz .LS1_veto\n" \
".LS1_back:\n" \
"s_waitcnt lgkmcnt(0)\n" \
"s_barrier\n" \
"ds_read_b32 v64, v252 offset:16\n" \
"s_waitcnt lgkmcnt(0)\n" \
"v_readfirstlane_b32 s43, v64\n" \
"s_cmp_eq_u32 s43, s35\n" \
"s_cbranch_scc1 .LS1_retry\n" \
"s_waitcnt lgkmcnt(0)\n" \
"s_barrier\n" \
"ds_read_b64 v[246:247], v252 offset:24\n" \
"ds_read_b96 v[248:250], v252\n" \
"s_waitcnt lgkmcnt(0)\n" \
"v_readfirstlane_b32 s44, v248\n" \
"v_readfirstlane_b32 s45, v249\n" \
"v_readfirstlane_b32 s46, v250\n" \
"v_readfirstlane_b32 s26, v246\n" \
"v_readfirstlane_b32 s27, v247\n" \
"s_mul_i32 s42, s25, s46\n" \
"s_lshr_b32 s42, s42, 12\n" \
"s_add_u32 s40, s40, 1\n" \
"s_add_u32 s24, s24, s26\n" \
"s_add_u32 s24, s24, s27\n" \
"s_cmp_eq_u32 s40, 0\n" \
"s_cbranch_scc1 .LS1_wet\n" \
"s_cmp_lt_u32 s40, 63\n" \
"s_cbranch_scc1 .LS1_loop\n" \
".LS1_end:\n" \
"s_endpgm\n" \
".LS1_veto:\n" \
"v_mov_b32 v64, s35\n" \
"ds_write_b32 v252, v64 offset:16\n" \
"s_branch .LS1_back\n" \
".p2align 6\n" \
"fk_role_S2:\n" \
"ds_read_b32 v200, v255 offset:0\n" \
"ds_read_b32 v201, v255 offset:256\n" \
"ds_read_b32 v202, v255 offset:512\n" \
"ds_read_b32 v203, v255 offset:768\n" \
"ds_read_b32 v204, v255 offset:1024\n" \
"ds_read_b32 v205, v255 offset:1280\n" \
"ds_read_b32 v206, v255 offset:1536\n" \
"ds_read_b32 v207, v255 offset:1792\n" \
"ds_read_b32 v208, v255 offset:2048\n" \
"ds_read_b32 v209, v255 offset:2304\n" \
"ds_read_b32 v210, v255 offset:2560\n" \
"ds_read_b32 v211, v255 offset:2816\n" \
"ds_read_b32 v212, v255 offset:3072\n" \
"ds_read_b32 v213, v255 offset:3328\n" \
"ds_read_b32 v214, v255 offset:3584\n" \
"ds_read_b32 v215, v255 offset:3840\n" \
"ds_read_b32 v254, v255 offset:7936\n" \
"s_waitcnt lgkmcnt(0)\n" \
"v_readlane_b32 s4, v254, 0\n" \
"v_readlane_b32 s5, v254, 1\n" \
"v_readlane_b32 s6, v254, 2\n" \
"v_readlane_b32 s7, v254, 3\n" \
"v_readlane_b32 s8, v254, 4\n" \
"v_readlane_b32 s9, v254, 5\n" \
"v_readlane_b32 s10, v254, 6\n" \
"v_readlane_b32 s11, v254, 7\n" \
"v_readlane_b32 s12, v254, 8\n" \
"v_readlane_b32 s13, v254, 9\n" \
"v_readlane_b32 s14, v254, 10\n" \
"v_readlane_b32 s15, v254, 11\n" \
"v_readlane_b32 s16, v254, 12\n" \
"v_readlane_b32 s17, v254, 13\n" \
"v_readlane_b32 s18, v254, 14\n" \
"v_readlane_b32 s19, v254, 15\n" \
"v_readlane_b32 s20, v254, 16\n" \
"v_readlane_b32 s21, v254, 17\n" \
"v_readlane_b32 s22, v254, 18\n" \
"v_readlane_b32 s23, v254, 19\n" \
"v_readlane_b32 s24, v254, 20\n" \
"v_readlane_b32 s25, v254, 21\n" \
"v_readlane_b32 s26, v254, 22\n" \
"v_readlane_b32 s27, v254, 23\n" \
"v_readlane_b32 s28, v254, 24\n" \
"v_readlane_b32 s29, v254, 25\n" \
"v_readlane_b32 s30, v254, 26\n" \
"v_readlane_b32 s31, v254, 27\n" \
"v_readlane_b32 s32, v254, 28\n" \
"v_readlane_b32 s33, v254, 29\n" \
"v_readlane_b32 s34, v254, 30\n" \
"v_readlane_b32 s35, v254, 31\n" \
"v_readlane_b32 s36, v254, 32\n" \
"v_readlane_b32 s37, v254, 33\n" \
"v_readlane_b32 s38, v254, 34\n" \
"v_readlane_b32 s39, v254, 35\n" \
"s_mov_b32 s40, 0\n" \
"s_mov_b32 s51, 0\n" \
"s_nop 0\n" \
"v_mov_b32 v252, s38\n" \
"s_nop 4\n" \
"v_mov_b32 v188, -1\n" \
"s_mov_b32 s28, s33\n" \
"s_mov_b32 s33, -1\n" \
"v_writelane_b32 v251, s6, 0\n" \
"v_writelane_b32 v251, s10, 1\n" \
"v_writelane_b32 v251, s14, 2\n" \
"v_writelane_b32 v251, s18, 3\n" \
"s_mov_b32 s6, 0\n" \
"s_mov_b32 s10, 0\n" \
"s_mov_b32 s14, 0\n" \
"s_mov_b32 s18, 0\n" \
"s_mov_b32 s40, -1\n" \
"s_branch .LS2_pre\n" \
".LS2_wet:\n" \
"s_waitcnt vmcnt(0)\n" \
"v_readlane_b32 s6, v251, 0\n" \
"v_readlane_b32 s10, v251, 1\n" \
"v_readlane_b32 s14, v251, 2\n" \
"v_readlane_b32 s18, v251, 3\n" \
"s_nop 4\n" \
"s_mov_b32 s33, s28\n" \
"s_add_u32 s34, s33, 2\n" \
"s_add_u32 s36, s33, 4\n" \
"s_add_u32 s43, s30, s33\n" \
"s_mov_b32 s50, 0\n" \
".LS2_w11l:\n" \
"buffer_load_dword v240, v204, s[4:7], 0 offen sc1\n" \
"s_waitcnt vmcnt(0)\n" \
".LS2_w11c:\n" \
"v_subrev_u32 v241, s43, v240\n" \
"v_cmp_gt_u32 vcc, 0x100, v241\n" \
"s_cmp_eq_u64 vcc, -1\n" \
"s_cbranch_scc1 .LS2_w11d\n" \
"s_sleep 2\n" \
"s_add_u32 s50, s50, 1\n" \
"s_cmp_lt_u32 s50, 0x20000\n" \
"s_cbranch_scc1 .LS2_w11l\n" \
"s_branch .LS2_end\n" \
".LS2_w11d:\n" \
"v_readlane_b32 s43, v202, 8\n" \
"s_nop 4\n" \
"buffer_load_dwordx4 a[64:67], v201, s[8:11], s43 offen sc1\n" \
"buffer_load_dwordx4 a[68:71], v201, s[8:11], s43 offen offset:1024 sc1\n" \
"v_readlane_b32 s43, v202, 9\n" \
"s_nop 4\n" \
"buffer_load_dwordx4 a[72:75], v201, s[8:11], s43 offen sc1\n" \
"buffer_load_dwordx4 a[76:79], v201, s[8:11], s43 offen offset:1024 sc1\n" \
"v_readlane_b32 s43, v202, 10\n" \
"s_nop 4\n" \
"buffer_load_dwordx4 a[80:83], v201, s[8:11], s43 offen sc1\n" \
"buffer_load_dwordx4 a[84:87], v201, s[8:11], s43 offen offset:1024 sc1\n" \
"v_readlane_b32 s43, v202, 12\n" \
"s_nop 4\n" \
"buffer_load_dwordx4 a[96:99], v201, s[8:11], s43 offen sc1\n" \
"buffer_load_dwordx4 a[100:103], v201, s[8:11], s43 offen offset:1024 sc1\n" \
"v_readlane_b32 s43, v202, 14\n" \
"s_nop 4\n" \
"buffer_load_dwordx4 a[112:115], v201, s[8:11], s43 offen sc1\n" \
"buffer_load_dwordx4 a[116:119], v201, s[8:11], s43 offen offset:1024 sc1\n" \
"v_readlane_b32 s43, v202, 19\n" \
"s_nop 4\n" \
"buffer_load_dwordx4 a[152:155], v201, s[8:11], s43 offen sc1\n" \
"buffer_load_dwordx4 a[156:159], v201, s[8:11], s43 offen offset:1024 sc1\n" \
"v_readlane_b32 s43, v202, 22\n" \
"s_nop 4\n" \
"buffer_load_dwordx4 a[176:179], v201, s[8:11], s43 offen sc1\n" \
"buffer_load_dwordx4 a[180:183], v201, s[8:11], s43 offen offset:1024 sc1\n" \
"v_readlane_b32 s43, v202, 25\n" \
"s_nop 4\n" \
"buffer_load_dwordx4 a[200:203], v201, s[8:11], s43 offen sc1\n" \
"buffer_load_dwordx4 a[204:207], v201, s[8:11], s43 offen offset:1024 sc1\n" \
"s_add_u32 s43, s30, s34\n" \
"s_mov_b32 s50, 0\n" \
"s_branch .LS2_w12c\n" \
".LS2_w12l:\n" \
"buffer_load_dword v240, v204, s[4:7], 0 offen sc1\n" \
"s_waitcnt vmcnt(0)\n" \
".LS2_w12c:\n" \
"v_subrev_u32 v241, s43, v240\n" \
"v_cmp_gt_u32 vcc, 0x100, v241\n" \
"s_cmp_eq_u64 vcc, -1\n" \
"s_cbranch_scc1 .LS2_w12d\n" \
"s_sleep 2\n" \
"s_add_u32 s50, s50, 1\n" \
"s_cmp_lt_u32 s50, 0x20000\n" \
"s_cbranch_scc1 .LS2_w12l\n" \
"s_branch .LS2_end\n" \
".LS2_w12d:\n" \
"v_readlane_b32 s43, v202, 11\n" \
"s_nop 4\n" \
"buffer_load_dwordx4 a[88:91], v201, s[8:11], s43 offen sc1\n" \
"buffer_load_dwordx4 a[92:95], v201, s[8:11], s43 offen offset:1024 sc1\n" \
"v_readlane_b32 s43, v202, 13\n" \
"s_nop 4\n" \
"buffer_load_dwordx4 a[104:107], v201, s[8:11], s43 offen sc1\n" \
"buffer_load_dwordx4 a[108:111], v201, s[8:11], s43 offen offset:1024 sc1\n" \
"v_readlane_b32 s43, v202, 15\n" \
"s_nop 4\n" \
"buffer_load_dwordx4 a[120:123], v201, s[8:11], s43 offen sc1\n" \
"buffer_load_dwordx4 a[124:127], v201, s[8:11], s43 offen offset:1024 sc1\n" \
"v_readlane_b32 s43, v202, 16\n" \
"s_nop 4\n" \
"buffer_load_dwordx4 a[128:131], v201, s[8:11], s43 offen sc1\n" \
"buffer_load_dwordx4 a[132:135], v201, s[8:11], s43 offen offset:1024 sc1\n" \
"v_readlane_b32 s43, v202, 17\n" \
"s_nop 4\n" \
"buffer_load_dwordx4 a[136:139], v201, s[8:11], s43 offen sc1\n" \
"buffer_load_dwordx4 a[140:143], v201, s[8:11], s43 offen offset:1024 sc1\n" \
"v_readlane_b32 s43, v202, 20\n" \
"s_nop 4\n" \
"buffer_load_dwordx4 a[160:163], v201, s[8:11], s43 offen sc1\n" \
"buffer_load_dwordx4 a[164:167], v201, s[8:11], s43 offen offset:1024 sc1\n" \
"v_readlane_b32 s43, v202, 23\n" \
"s_nop 4\n" \
"buffer_load_dwordx4 a[184:187], v201, s[8:11], s43 offen sc1\n" \
"buffer_load_dwordx4 a[188:191], v201, s[8:11], s43 offen offset:1024 sc1\n" \
"v_readlane_b32 s43, v202, 26\n" \
"s_nop 4\n" \
"buffer_load_dwordx4 a[208:211], v201, s[8:11], s43 offen sc1\n" \
"buffer_load_dwordx4 a[212:215], v201, s[8:11], s43 offen offset:1024 sc1\n" \
"s_add_u32 s43, s30, s36\n" \
"s_mov_b32 s50, 0\n" \
"s_branch .LS2_w13c\n" \
".LS2_w13l:\n" \
"buffer_load_dword v240, v204, s[4:7], 0 offen sc1\n" \
"s_waitcnt vmcnt(0)\n" \
".LS2_w13c:\n" \
"v_subrev_u32 v241, s43, v240\n" \
"v_cmp_gt_u32 vcc, 0x100, v241\n" \
"s_cmp_eq_u64 vcc, -1\n" \
"s_cbranch_scc1 .LS2_w13d\n" \
"s_sleep 2\n" \
"s_add_u32 s50, s50, 1\n" \
"s_cmp_lt_u32 s50, 0x20000\n" \
"s_cbranch_scc1 .LS2_w13l\n" \
"s_branch .LS2_end\n" \
".LS2_w13d:\n" \
"v_readlane_b32 s43, v202, 0\n" \
"s_nop 4\n" \
"buffer_load_dwordx4 a[0:3], v201, s[8:11], s43 offen sc1\n" \
"buffer_load_dwordx4 a[4:7], v201, s[8:11], s43 offen offset:1024 sc1\n" \
"v_readlane_b32 s43, v202, 1\n" \
"s_nop 4\n" \
"buffer_load_dwordx4 a[8:11], v201, s[8:11], s43 offen sc1\n" \
"buffer_load_dwordx4 a[12:15], v201, s[8:11], s43 offen offset:1024 sc1\n" \
"v_readlane_b32 s43, v202, 2\n" \
"s_nop 4\n" \
"buffer_load_dwordx4 a[16:19], v201, s[8:11], s43 offen sc1\n" \
"buffer_load_dwordx4 a[20:23], v201, s[8:11], s43 offen offset:1024 sc1\n" \
"v_readlane_b32 s43, v202, 3\n" \
"s_nop 4\n" \
"buffer_load_dwordx4 a[24:27], v201, s[8:11], s43 offen sc1\n" \
"buffer_load_dwordx4 a[28:31], v201, s[8:11], s43 offen offset:1024 sc1\n" \
"v_readlane_b32 s43, v202, 4\n" \
"s_nop 4\n" \
"buffer_load_dwordx4 a[32:35], v201, s[8:11], s43 offen sc1\n" \
"buffer_load_dwordx4 a[36:39], v201, s[8:11], s43 offen offset:1024 sc1\n" \
"v_readlane_b32 s43, v202, 5\n" \
"s_nop 4\n" \
"buffer_load_dwordx4 a[40:43], v201, s[8:11], s43 offen sc1\n" \
"buffer_load_dwordx4 a[44:47], v201, s[8:11], s43 offen offset:1024 sc1\n" \
"v_readlane_b32 s43, v202, 6\n" \
"s_nop 4\n" \
"buffer_load_dwordx4 a[48:51], v201, s[8:11], s43 offen sc1\n" \
"buffer_load_dwordx4 a[52:55], v201, s[8:11], s43 offen offset:1024 sc1\n" \
"v_readlane_b32 s43, v202, 7\n" \
"s_nop 4\n" \
"buffer_load_dwordx4 a[56:59], v201, s[8:11], s43 offen sc1\n" \
"buffer_load_dwordx4 a[60:63], v201, s[8:11], s43 offen offset:1024 sc1\n" \
"s_add_u32 s43, s30, 7\n" \
"s_mov_b32 s50, 0\n" \
"s_branch .LS2_w14c\n" \
".LS2_w14l:\n" \
"buffer_load_dword v240, v204, s[4:7], 0 offen sc1\n" \
"s_waitcnt vmcnt(0)\n" \
".LS2_w14c:\n" \
"v_subrev_u32 v241, s43, v240\n" \
"v_cmp_gt_u32 vcc, 0x100, v241\n" \
"s_cmp_eq_u64 vcc, -1\n" \
"s_cbranch_scc1 .LS2_w14d\n" \
"s_sleep 2\n" \
"s_add_u32 s50, s50, 1\n" \
"s_cmp_lt_u32 s50, 0x20000\n" \
"s_cbranch_scc1 .LS2_w14l\n" \
"s_branch .LS2_end\n" \
".LS2_w14d:\n" \
"v_readlane_b32 s43, v202, 18\n" \
"s_nop 4\n" \
"buffer_load_dwordx4 a[144:147], v201, s[8:11], s43 offen sc1\n" \
"buffer_load_dwordx4 a[148:151], v201, s[8:11], s43 offen offset:1024 sc1\n" \
"v_readlane_b32 s43, v202, 21\n" \
"s_nop 4\n" \
"buffer_load_dwordx4 a[168:171], v201, s[8:11], s43 offen sc1\n" \
"buffer_load_dwordx4 a[172:175], v201, s[8:11], s43 offen offset:1024 sc1\n" \
"v_readlane_b32 s43, v202, 24\n" \
"s_nop 4\n" \
"buffer_load_dwordx4 a[192:195], v201, s[8:11], s43 offen sc1\n" \
"buffer_load_dwordx4 a[196:199], v201, s[8:11], s43 offen offset:1024 sc1\n" \
"v_readlane_b32 s43, v202, 27\n" \
"s_nop 4\n" \
"buffer_load_dwordx4 a[216:219], v201, s[8:11], s43 offen sc1\n" \
"buffer_load_dwordx4 a[220:223], v201, s[8:11], s43 offen offset:1024 sc1\n" \
"s_mov_b32 s33, -1\n" \
"ds_read_b96 v[248:250], v252\n" \
"s_waitcnt lgkmcnt(0)\n" \
"v_readfirstlane_b32 s44, v248\n" \
"v_readfirstlane_b32 s45, v249\n" \
"v_readfirstlane_b32 s46, v250\n" \
"s_mul_i32 s42, s25, s46\n" \
"s_lshr_b32 s42, s42, 12\n" \
"s_mov_b32 s26, s44\n" \
"s_mov_b32 s27, s45\n" \
".p2align 3\n" \
"s_memtime s[54:55]\n" \
"s_memrealtime s[56:57]\n" \
"s_memtime s[58:59]\n" \
"s_memrealtime s[60:61]\n" \
"s_nop 0\n" \
"s_nop 0\n" \
"s_memtime s[62:63]\n" \
"s_memrealtime s[64:65]\n" \
"s_memtime s[66:67]\n" \
"s_memrealtime s[68:69]\n" \
"s_memtime s[70:71]\n" \
"s_memrealtime s[72:73]\n" \
"s_memtime s[74:75]\n" \
"s_memrealtime s[76:77]\n" \
"s_nop 0\n" \
"s_nop 0\n" \
"s_memtime s[78:79]\n" \
"s_memrealtime s[80:81]\n" \
"s_memtime s[82:83]\n" \
"s_memrealtime s[84:85]\n" \
"s_memtime s[86:87]\n" \
"s_memrealtime s[88:89]\n" \
"s_memtime s[90:91]\n" \
"s_memrealtime s[92:93]\n" \
"s_nop 0\n" \
"s_nop 0\n" \
"s_memtime s[94:95]\n" \
"s_memrealtime s[96:97]\n" \
"s_memtime s[98:99]\n" \
"s_mov_b32 s52, 0\n" \
"s_mov_b32 s53, 4096\n" \
"s_waitcnt lgkmcnt(0)\n" \
"s_cmp_lg_u32 s96, s56\n" \
"s_cselect_b32 s52, s90, s52\n" \
"s_cselect_b32 s53, s98, s53\n" \
"s_cmp_lg_u32 s92, s56\n" \
"s_cselect_b32 s52, s86, s52\n" \
"s_cselect_b32 s53, s94, s53\n" \
"s_cmp_lg_u32 s88, s56\n" \
"s_cselect_b32 s52, s82, s52\n" \
"s_cselect_b32 s53, s90, s53\n" \
"s_cmp_lg_u32 s84, s56\n" \
"s_cselect_b32 s52, s78, s52\n" \
"s_cselect_b32 s53, s86, s53\n" \
"s_cmp_lg_u32 s80, s56\n" \
"s_cselect_b32 s52, s74, s52\n" \
"s_cselect_b32 s53, s82, s53\n" \
"s_cmp_lg_u32 s76, s56\n" \
"s_cselect_b32 s52, s70, s52\n" \
"s_cselect_b32 s53, s78, s53\n" \
"s_cmp_lg_u32 s72, s56\n" \
"s_cselect_b32 s52, s66, s52\n" \
"s_cselect_b32 s53, s74, s53\n" \
"s_cmp_lg_u32 s68, s56\n" \
"s_cselect_b32 s52, s62, s52\n" \
"s_cselect_b32 s53, s70, s53\n" \
"s_cmp_lg_u32 s64, s56\n" \
"s_cselect_b32 s52, s58, s52\n" \
"s_cselect_b32 s53, s66, s53\n" \
"s_cmp_lg_u32 s60, s56\n" \
"s_cselect_b32 s52, s54, s52\n" \
"s_cselect_b32 s53, s62, s53\n" \
"s_sub_u32 s53, s53, s52\n" \
"s_lshr_b32 s55, s53, 1\n" \
"s_add_u32 s52, s52, s55\n" \
"s_add_u32 s55, s56, 4\n" \
"s_lshl_b32 s55, s55, 4\n" \
"s_cmp_le_u32 s53, 40\n" \
"s_cselect_b32 s44, s55, s44\n" \
"s_cselect_b32 s45, s52, s45\n" \
"v_mov_b32 v248, s44\n" \
"v_mov_b32 v249, s45\n" \
"v_mov_b32 v250, s46\n" \
"s_mov_b64 exec, 1\n" \
"ds_write_b96 v252, v[248:250]\n" \
"s_mov_b64 exec, -1\n" \
"s_sub_u32 s26, s44, s26\n" \
"s_sub_u32 s27, s45, s27\n" \
"v_cvt_f32_u32 v248, s26\n" \
"v_cvt_f32_u32 v249, s27\n" \
"v_rcp_f32 v248, v248\n" \
"v_mul_f32 v249, 0x457ae148, v249\n" \
"v_mul_f32 v248, v248, v249\n" \
"v_cvt_u32_f32 v248, v248\n" \
"s_nop 0\n" \
"v_readfirstlane_b32 s27, v248\n" \
"s_max_u32 s27, s27, 0x800\n" \
"s_min_u32 s27, s27, 0x2000\n" \
"s_cmp_gt_u32 s26, 1600\n" \
"s_cselect_b32 s46, s27, s46\n" \
".p2align 3\n" \
"s_memtime s[54:55]\n" \
"s_memrealtime s[56:57]\n" \
"s_memtime s[58:59]\n" \
"s_memrealtime s[60:61]\n" \
"s_nop 0\n" \
"s_nop 0\n" \
"s_memtime s[62:63]\n" \
"s_memrealtime s[64:65]\n" \
"s_memtime s[66:67]\n" \
"s_memrealtime s[68:69]\n" \
"s_memtime s[70:71]\n" \
"s_memrealtime s[72:73]\n" \
"s_memtime s[74:75]\n" \
"s_memrealtime s[76:77]\n" \
"s_nop 0\n" \
"s_nop 0\n" \
"s_memtime s[78:79]\n" \
"s_memrealtime s[80:81]\n" \
"s_memtime s[82:83]\n" \
"s_memrealtime s[84:85]\n" \
"s_memtime s[86:87]\n" \
"s_memrealtime s[88:89]\n" \
"s_memtime s[90:91]\n" \
"s_memrealtime s[92:93]\n" \
"s_nop 0\n" \
"s_nop 0\n" \
"s_memtime s[94:95]\n" \
"s_memrealtime s[96:97]\n" \
"s_memtime s[98:99]\n" \
"s_mov_b32 s52, 0\n" \
"s_mov_b32 s53, 4096\n" \
"s_waitcnt lgkmcnt(0)\n" \
"s_cmp_lg_u32 s96, s56\n" \
"s_cselect_b32 s52, s90, s52\n" \
"s_cselect_b32 s53, s98, s53\n" \
"s_cmp_lg_u32 s92, s56\n" \
"s_cselect_b32 s52, s86, s52\n" \
"s_cselect_b32 s53, s94, s53\n" \
"s_cmp_lg_u32 s88, s56\n" \
"s_cselect_b32 s52, s82, s52\n" \
"s_cselect_b32 s53, s90, s53\n" \
"s_cmp_lg_u32 s84, s56\n" \
"s_cselect_b32 s52, s78, s52\n" \
"s_cselect_b32 s53, s86, s53\n" \
"s_cmp_lg_u32 s80, s56\n" \
"s_cselect_b32 s52, s74, s52\n" \
"s_cselect_b32 s53, s82, s53\n" \
"s_cmp_lg_u32 s76, s56\n" \
"s_cselect_b32 s52, s70, s52\n" \
"s_cselect_b32 s53, s78, s53\n" \
"s_cmp_lg_u32 s72, s56\n" \
"s_cselect_b32 s52, s66, s52\n" \
"s_cselect_b32 s53, s74, s53\n" \
"s_cmp_lg_u32 s68, s56\n" \
"s_cselect_b32 s52, s62, s52\n" \
"s_cselect_b32 s53, s70, s53\n" \
"s_cmp_lg_u32 s64, s56\n" \
"s_cselect_b32 s52, s58, s52\n" \
"s_cselect_b32 s53, s66, s53\n" \
"s_cmp_lg_u32 s60, s56\n" \
"s_cselect_b32 s52, s54, s52\n" \
"s_cselect_b32 s53, s62, s53\n" \
"s_sub_u32 s53, s53, s52\n" \
"s_lshr_b32 s55, s53, 1\n" \
"s_add_u32 s52, s52, s55\n" \
"s_add_u32 s55, s56, 4\n" \
"s_lshl_b32 s55, s55, 4\n" \
"s_cmp_le_u32 s53, 40\n" \
"s_cselect_b32 s44, s55, s44\n" \
"s_cselect_b32 s45, s52, s45\n" \
"v_mov_b32 v248, s44\n" \
"v_mov_b32 v249, s45\n" \
"v_mov_b32 v250, s46\n" \
"s_mov_b64 exec, 1\n" \
"ds_write_b96 v252, v[248:250]\n" \
"s_mov_b64 exec, -1\n" \
"s_waitcnt lgkmcnt(0)\n" \
"s_add_u32 s43, s30, 1\n" \
"s_mov_b32 s50, 0\n" \
".LS2_w15l:\n" \
"buffer_load_dword v240, v205, s[4:7], 0 offen sc1\n" \
"s_waitcnt vmcnt(0)\n" \
".LS2_w15c:\n" \
"v_subrev_u32 v241, s43, v240\n" \
"v_cmp_gt_u32 vcc, 0x100, v241\n" \
"s_cmp_eq_u64 vcc, -1\n" \
"s_cbranch_scc1 .LS2_w15d\n" \
"s_sleep 2\n" \
"s_add_u32 s50, s50, 1\n" \
"s_cmp_lt_u32 s50, 0x20000\n" \
"s_cbranch_scc1 .LS2_w15l\n" \
"s_branch .LS2_end\n" \
".LS2_w15d:\n" \
"v_readfirstlane_b32 s31, v241\n" \
"s_lshr_b32 s32, s31, 2\n" \
"s_xor_b32 s29, s31, s32\n" \
"s_and_b32 s29, s29, 3\n" \
"s_lshl_b32 s29, s29, 13\n" \
"s_bitset1_b32 s29, 21\n" \
"s_and_b32 s31, s31, 3\n" \
"s_lshl_b32 s31, s31, 13\n" \
"s_bitcmp1_b32 s39, 0\n" \
"s_cselect_b32 s43, s29, 0\n" \
"s_xor_b32 s31, s31, s43\n" \
"s_xor_b32 s32, s31, 0x4000\n" \
"s_mov_b32 s39, 0\n" \
".LS2_pre:\n" \
"s_barrier\n" \
"ds_read_b96 v[248:250], v252\n" \
"s_waitcnt lgkmcnt(0)\n" \
"v_readfirstlane_b32 s44, v248\n" \
"v_readfirstlane_b32 s45, v249\n" \
"v_readfirstlane_b32 s46, v250\n" \
"s_mul_i32 s42, s25, s46\n" \
"s_lshr_b32 s42, s42, 12\n" \
".LS2_cu:\n" \
"s_sub_u32 s43, s24, s44\n" \
"s_cmp_lt_i32 s43, 0\n" \
"s_cbranch_scc0 .LS2_cud\n" \
"s_add_u32 s24, s24, s25\n" \
"s_xor_b32 s31, s31, s29\n" \
"s_xor_b32 s32, s32, s29\n" \
"s_xor_b32 s47, s47, s29\n" \
"s_add_u32 s39, s39, 1\n" \
"s_branch .LS2_cu\n" \
".LS2_cud:\n" \
".LS2_loop:\n" \
"s_add_u32 s43, s40, 0\n" \
"s_and_b32 s43, s43, 3\n" \
"s_bitcmp1_b32 s43, 0\n" \
"s_cselect_b32 s47, s32, s31\n" \
"s_lshl_b32 s43, s43, 19\n" \
"s_add_u32 s47, s47, s43\n" \
"s_sub_u32 s43, s24, s44\n" \
"s_max_i32 s43, s43, 0xfffd0000\n" \
"s_min_i32 s43, s43, 0x4000\n" \
"s_mul_i32 s43, s43, s46\n" \
"s_ashr_i32 s43, s43, 12\n" \
"s_add_u32 s41, s45, s43\n" \
"s_add_u32 s41, s41, 8\n" \
"s_branch .LS2_a1\n" \
".LS2_retry:\n" \
"s_add_u32 s51, s51, 1\n" \
"s_cmp_gt_u32 s51, 0x20000\n" \
"s_cbranch_scc1 .LS2_end\n" \
".LS2_a0:\n" \
"s_add_u32 s41, s41, s42\n" \
"s_add_u32 s24, s24, s25\n" \
"s_xor_b32 s31, s31, s29\n" \
"s_xor_b32 s32, s32, s29\n" \
"s_xor_b32 s47, s47, s29\n" \
"s_xor_b32 s48, s48, s29\n" \
"s_add_u32 s39, s39, 1\n" \
".p2align 6\n" \
".LS2_a1:\n" \
"s_memtime vcc\n" \
"s_waitcnt lgkmcnt(0)\n" \
"s_sub_u32 s43, s41, vcc_lo\n" \
"s_cmp_gt_i32 s43, 128\n" \
"s_cbranch_scc1 .LS2_a1\n" \
"s_cmp_lt_i32 s43, s37\n" \
"s_cbranch_scc1 .LS2_a0\n" \
"s_max_i32 s43, s43, 0\n" \
"s_and_b32 s43, s43, -4\n" \
"s_getpc_b64 vcc\n" \
".LS2_a3:\n" \
"s_sub_u32 s43, .LS2_a4-.LS2_a3, s43\n" \
"s_add_u32 vcc_lo, vcc_lo, s43\n" \
"s_addc_u32 vcc_hi, vcc_hi, 0\n" \
"s_setpc_b64 vcc\n" \
".rept 32\n" \
"s_nop 0\n" \
".endr\n" \
".LS2_a4:\n" \
"buffer_load_dwordx4 v[0:3], v208, s[4:7], s47 offen offset:2048 sc1\n" \
"s_nop 9\n" \
"buffer_load_dwordx4 v[4:7], v208, s[4:7], s47 offen sc1\n" \
"s_nop 9\n" \
"buffer_load_dwordx4 v[8:11], v208, s[4:7], s47 offen offset:1024 sc1\n" \
"s_nop 9\n" \
"buffer_load_dwordx4 v[12:15], v210, s[4:7], s47 offen sc1\n" \
"s_add_u32 s35, s35, 1\n" \
"v_mov_b32 v28, 0\n" \
"v_mov_b32 v29, 0\n" \
"v_mov_b32 v30, 0\n" \
"v_mov_b32 v31, 0\n" \
"v_mov_b32 v32, 0\n" \
"v_mov_b32 v33, 0\n" \
"v_mov_b32 v34, 0\n" \
"v_mov_b32 v35, 0\n" \
"v_mov_b32 v36, 0\n" \
"v_mov_b32 v37, 0\n" \
"v_mov_b32 v38, 0\n" \
"v_mov_b32 v39, 0\n" \
"v_mov_b32 v40, 0\n" \
"v_mov_b32 v41, 0\n" \
"v_mov_b32 v42, 0\n" \
"v_mov_b32 v43, 0\n" \
"v_mov_b32 v44, 0\n" \
"v_mov_b32 v45, 0\n" \
"v_mov_b32 v46, 0\n" \
"v_mov_b32 v47, 0\n" \
"v_mov_b32 v48, 0\n" \
"v_mov_b32 v49, 0\n" \
"v_mov_b32 v50, 0\n" \
"v_mov_b32 v51, 0\n" \
"v_mov_b32 v52, 0\n" \
"v_mov_b32 v53, 0\n" \
"v_mov_b32 v54, 0\n" \
"v_mov_b32 v55, 0\n" \
"v_mov_b32 v56, 0\n" \
"v_mov_b32 v57, 0\n" \
"v_mov_b32 v58, 0\n" \
"v_mov_b32 v59, 0\n" \
"s_waitcnt vmcnt(3)\n" \
"v_smfmac_f32_16x16x64_bf16 v[28:31], v[0:3], a[0:7], v200\n" \
"v_smfmac_f32_16x16x64_bf16 v[32:35], v[0:3], a[8:15], v200\n" \
"v_smfmac_f32_16x16x64_bf16 v[36:39], v[0:3], a[16:23], v200\n" \
"v_smfmac_f32_16x16x64_bf16 v[40:43], v[0:3], a[24:31], v200\n" \
"v_smfmac_f32_16x16x64_bf16 v[44:47], v[0:3], a[32:39], v200\n" \
"v_smfmac_f32_16x16x64_bf16 v[48:51], v[0:3], a[40:47], v200\n" \
"v_smfmac_f32_16x16x64_bf16 v[52:55], v[0:3], a[48:55], v200\n" \
"v_smfmac_f32_16x16x64_bf16 v[56:59], v[0:3], a[56:63], v200\n" \
"s_waitcnt vmcnt(2)\n" \
"v_smfmac_f32_16x16x64_bf16 v[28:31], v[4:7], a[64:71], v200\n" \
"v_smfmac_f32_16x16x64_bf16 v[32:35], v[4:7], a[72:79], v200\n" \
"v_smfmac_f32_16x16x64_bf16 v[36:39], v[4:7], a[80:87], v200\n" \
"s_waitcnt vmcnt(1)\n" \
"v_smfmac_f32_16x16x64_bf16 v[28:31], v[8:11], a[88:95], v200\n" \
"s_nop 2\n" \
"v_smfmac_f32_16x16x64_bf16 v[40:43], v[4:7], a[96:103], v200\n" \
"s_nop 2\n" \
"v_smfmac_f32_16x16x64_bf16 v[32:35], v[8:11], a[104:111], v200\n" \
"s_nop 0\n" \
"v_cvt_pk_bf16_f32 v128, v28, v29\n" \
"v_cvt_pk_bf16_f32 v129, v30, v31\n" \
"v_smfmac_f32_16x16x64_bf16 v[44:47], v[4:7], a[112:119], v200\n" \
"ds_write_b64 v211, v[128:129] offset:256\n" \
"s_nop 1\n" \
"v_smfmac_f32_16x16x64_bf16 v[36:39], v[8:11], a[120:127], v200\n" \
"s_nop 0\n" \
"v_cvt_pk_bf16_f32 v130, v32, v33\n" \
"v_cvt_pk_bf16_f32 v131, v34, v35\n" \
"v_smfmac_f32_16x16x64_bf16 v[44:47], v[8:11], a[128:135], v200\n" \
"ds_write_b64 v211, v[130:131] offset:2432\n" \
"s_nop 1\n" \
"v_smfmac_f32_16x16x64_bf16 v[40:43], v[8:11], a[136:143], v200\n" \
"s_waitcnt vmcnt(0)\n" \
"v_cvt_pk_bf16_f32 v132, v36, v37\n" \
"v_cvt_pk_bf16_f32 v133, v38, v39\n" \
"v_smfmac_f32_16x16x64_bf16 v[44:47], v[12:15], a[144:151], v200\n" \
"ds_write_b64 v211, v[132:133] offset:4608\n" \
"s_nop 1\n" \
"v_smfmac_f32_16x16x64_bf16 v[48:51], v[4:7], a[152:159], v200\n" \
"s_nop 0\n" \
"v_cvt_pk_bf16_f32 v134, v40, v41\n" \
"v_cvt_pk_bf16_f32 v135, v42, v43\n" \
"v_smfmac_f32_16x16x64_bf16 v[48:51], v[8:11], a[160:167], v200\n" \
"ds_write_b64 v211, v[134:135] offset:4992\n" \
"v_cvt_pk_bf16_f32 v136, v44, v45\n" \
"v_cvt_pk_bf16_f32 v137, v46, v47\n" \
"v_smfmac_f32_16x16x64_bf16 v[48:51], v[12:15], a[168:175], v200\n" \
"ds_write_b64 v211, v[136:137] offset:9472\n" \
"s_nop 1\n" \
"v_smfmac_f32_16x16x64_bf16 v[52:55], v[4:7], a[176:183], v200\n" \
"s_nop 2\n" \
"v_smfmac_f32_16x16x64_bf16 v[52:55], v[8:11], a[184:191], v200\n" \
"s_nop 0\n" \
"v_cvt_pk_bf16_f32 v138, v48, v49\n" \
"v_cvt_pk_bf16_f32 v139, v50, v51\n" \
"v_smfmac_f32_16x16x64_bf16 v[52:55], v[12:15], a[192:199], v200\n" \
"ds_write_b64 v211, v[138:139] offset:11648\n" \
"s_waitcnt lgkmcnt(2)\n" \
"s_barrier\n" \
"v_smfmac_f32_16x16x64_bf16 v[56:59], v[4:7], a[200:207], v200\n" \
"s_nop 2\n" \
"v_smfmac_f32_16x16x64_bf16 v[56:59], v[8:11], a[208:215], v200\n" \
"s_nop 2\n" \
"v_smfmac_f32_16x16x64_bf16 v[56:59], v[12:15], a[216:223], v200\n" \
"v_cvt_pk_bf16_f32 v140, v52, v53\n" \
"v_cvt_pk_bf16_f32 v141, v54, v55\n" \
"s_nop 1\n" \
"ds_write_b64 v211, v[140:141] offset:13824\n" \
"s_nop 2\n" \
"v_cvt_pk_bf16_f32 v142, v56, v57\n" \
"v_cvt_pk_bf16_f32 v143, v58, v59\n" \
"ds_write_b64 v211, v[142:143] offset:14208\n" \
"s_waitcnt lgkmcnt(2)\n" \
"s_barrier\n" \
"s_waitcnt lgkmcnt(1)\n" \
"s_barrier\n" \
"s_waitcnt lgkmcnt(0)\n" \
"s_barrier\n" \
"s_nop 3\n" \
"ds_read_b32 v64, v252 offset:20\n" \
"s_waitcnt lgkmcnt(0)\n" \
"v_readfirstlane_b32 s43, v64\n" \
"s_cmp_eq_u32 s43, s35\n" \
"s_cbranch_scc1 .LS2_retry\n" \
"s_nop 15\n" \
"v_max3_u32 v64, v0, v0, v1\n" \
"v_max3_u32 v64, v64, v2, v3\n" \
"v_max3_u32 v64, v64, v4, v5\n" \
"v_max3_u32 v64, v64, v6, v7\n" \
"v_max3_u32 v64, v64, v8, v9\n" \
"v_max3_u32 v64, v64, v10, v11\n" \
"v_max3_u32 v64, v64, v12, v13\n" \
"v_max3_u32 v64, v64, v14, v15\n" \
"v_cmp_eq_u32 vcc, -1, v64\n" \
"s_cbranch_vccnz .LS2_veto\n" \
".LS2_back:\n" \
"s_waitcnt lgkmcnt(0)\n" \
"s_barrier\n" \
"ds_read_b32 v64, v252 offset:16\n" \
"s_waitcnt lgkmcnt(0)\n" \
"v_readfirstlane_b32 s43, v64\n" \
"s_cmp_eq_u32 s43, s35\n" \
"s_cbranch_scc1 .LS2_retry\n" \
".p2align 3\n" \
"s_memtime s[54:55]\n" \
"s_memrealtime s[56:57]\n" \
"s_memtime s[58:59]\n" \
"s_memrealtime s[60:61]\n" \
"s_nop 0\n" \
"s_nop 0\n" \
"s_memtime s[62:63]\n" \
"s_memrealtime s[64:65]\n" \
"s_memtime s[66:67]\n" \
"s_memrealtime s[68:69]\n" \
"s_memtime s[70:71]\n" \
"s_memrealtime s[72:73]\n" \
"s_memtime s[74:75]\n" \
"s_memrealtime s[76:77]\n" \
"s_nop 0\n" \
"s_nop 0\n" \
"s_memtime s[78:79]\n" \
"s_memrealtime s[80:81]\n" \
"s_memtime s[82:83]\n" \
"s_memrealtime s[84:85]\n" \
"s_memtime s[86:87]\n" \
"s_memrealtime s[88:89]\n" \
"s_memtime s[90:91]\n" \
"s_memrealtime s[92:93]\n" \
"s_nop 0\n" \
"s_nop 0\n" \
"s_memtime s[94:95]\n" \
"s_memrealtime s[96:97]\n" \
"s_memtime s[98:99]\n" \
"s_mov_b32 s52, 0\n" \
"s_mov_b32 s53, 4096\n" \
"s_waitcnt lgkmcnt(0)\n" \
"s_cmp_lg_u32 s96, s56\n" \
"s_cselect_b32 s52, s90, s52\n" \
"s_cselect_b32 s53, s98, s53\n" \
"s_cmp_lg_u32 s92, s56\n" \
"s_cselect_b32 s52, s86, s52\n" \
"s_cselect_b32 s53, s94, s53\n" \
"s_cmp_lg_u32 s88, s56\n" \
"s_cselect_b32 s52, s82, s52\n" \
"s_cselect_b32 s53, s90, s53\n" \
"s_cmp_lg_u32 s84, s56\n" \
"s_cselect_b32 s52, s78, s52\n" \
"s_cselect_b32 s53, s86, s53\n" \
"s_cmp_lg_u32 s80, s56\n" \
"s_cselect_b32 s52, s74, s52\n" \
"s_cselect_b32 s53, s82, s53\n" \
"s_cmp_lg_u32 s76, s56\n" \
"s_cselect_b32 s52, s70, s52\n" \
"s_cselect_b32 s53, s78, s53\n" \
"s_cmp_lg_u32 s72, s56\n" \
"s_cselect_b32 s52, s66, s52\n" \
"s_cselect_b32 s53, s74, s53\n" \
"s_cmp_lg_u32 s68, s56\n" \
"s_cselect_b32 s52, s62, s52\n" \
"s_cselect_b32 s53, s70, s53\n" \
"s_cmp_lg_u32 s64, s56\n" \
"s_cselect_b32 s52, s58, s52\n" \
"s_cselect_b32 s53, s66, s53\n" \
"s_cmp_lg_u32 s60, s56\n" \
"s_cselect_b32 s52, s54, s52\n" \
"s_cselect_b32 s53, s62, s53\n" \
"s_sub_u32 s53, s53, s52\n" \
"s_lshr_b32 s55, s53, 1\n" \
"s_add_u32 s52, s52, s55\n" \
"s_add_u32 s55, s56, 4\n" \
"s_lshl_b32 s55, s55, 4\n" \
"s_sub_u32 s59, s55, s44\n" \
"s_cmp_lt_u32 s59, 2600\n" \
"s_cselect_b32 s63, 1, 2\n" \
"s_mul_i32 s59, s59, s46\n" \
"s_ashr_i32 s59, s59, 12\n" \
"s_add_u32 s59, s59, s45\n" \
"s_sub_u32 s59, s52, s59\n" \
"s_max_i32 s59, s59, -96\n" \
"s_min_i32 s59, s59, 96\n" \
"s_ashr_i32 s63, s59, s63\n" \
"s_add_u32 s63, s63, s46\n" \
"s_max_i32 s63, s63, 0x800\n" \
"s_min_i32 s63, s63, 0x2000\n" \
"s_cmp_le_u32 s53, 40\n" \
"s_cselect_b32 s44, s55, s44\n" \
"s_cselect_b32 s45, s52, s45\n" \
"s_cselect_b32 s46, s63, s46\n" \
"v_mov_b32 v248, s44\n" \
"v_mov_b32 v249, s45\n" \
"v_mov_b32 v250, s46\n" \
"s_mov_b64 exec, 1\n" \
"ds_write_b96 v252, v[248:250]\n" \
"s_mov_b64 exec, -1\n" \
"s_waitcnt lgkmcnt(0)\n" \
"s_barrier\n" \
"ds_read_b64 v[246:247], v252 offset:24\n" \
"ds_read_b96 v[248:250], v252\n" \
"s_waitcnt lgkmcnt(0)\n" \
"v_readfirstlane_b32 s44, v248\n" \
"v_readfirstlane_b32 s45, v249\n" \
"v_readfirstlane_b32 s46, v250\n" \
"v_readfirstlane_b32 s26, v246\n" \
"v_readfirstlane_b32 s27, v247\n" \
"s_mul_i32 s42, s25, s46\n" \
"s_lshr_b32 s42, s42, 12\n" \
"s_add_u32 s40, s40, 1\n" \
"s_add_u32 s24, s24, s26\n" \
"s_add_u32 s24, s24, s27\n" \
"s_cmp_eq_u32 s40, 0\n" \
"s_cbranch_scc1 .LS2_wet\n" \
"s_cmp_lt_u32 s40, 63\n" \
"s_cbranch_scc1 .LS2_loop\n" \
".LS2_end:\n" \
"s_endpgm\n" \
".LS2_veto:\n" \
"v_mov_b32 v64, s35\n" \
"ds_write_b32 v252, v64 offset:16\n" \
"s_branch .LS2_back\n" \
".p2align 6\n" \
"fk_role_S3:\n" \
"ds_read_b32 v200, v255 offset:0\n" \
"ds_read_b32 v201, v255 offset:256\n" \
"ds_read_b32 v202, v255 offset:512\n" \
"ds_read_b32 v203, v255 offset:768\n" \
"ds_read_b32 v204, v255 offset:1024\n" \
"ds_read_b32 v205, v255 offset:1280\n" \
"ds_read_b32 v206, v255 offset:1536\n" \
"ds_read_b32 v207, v255 offset:1792\n" \
"ds_read_b32 v208, v255 offset:2048\n" \
"ds_read_b32 v209, v255 offset:2304\n" \
"ds_read_b32 v210, v255 offset:2560\n" \
"ds_read_b32 v211, v255 offset:2816\n" \
"ds_read_b32 v212, v255 offset:3072\n" \
"ds_read_b32 v213, v255 offset:3328\n" \
"ds_read_b32 v214, v255 offset:3584\n" \
"ds_read_b32 v215, v255 offset:3840\n" \
"ds_read_b32 v254, v255 offset:7936\n" \
"s_waitcnt lgkmcnt(0)\n" \
"v_readlane_b32 s4, v254, 0\n" \
"v_readlane_b32 s5, v254, 1\n" \
"v_readlane_b32 s6, v254, 2\n" \
"v_readlane_b32 s7, v254, 3\n" \
"v_readlane_b32 s8, v254, 4\n" \
"v_readlane_b32 s9, v254, 5\n" \
"v_readlane_b32 s10, v254, 6\n" \
"v_readlane_b32 s11, v254, 7\n" \
"v_readlane_b32 s12, v254, 8\n" \
"v_readlane_b32 s13, v254, 9\n" \
"v_readlane_b32 s14, v254, 10\n" \
"v_readlane_b32 s15, v254, 11\n" \
"v_readlane_b32 s16, v254, 12\n" \
"v_readlane_b32 s17, v254, 13\n" \
"v_readlane_b32 s18, v254, 14\n" \
"v_readlane_b32 s19, v254, 15\n" \
"v_readlane_b32 s20, v254, 16\n" \
"v_readlane_b32 s21, v254, 17\n" \
"v_readlane_b32 s22, v254, 18\n" \
"v_readlane_b32 s23, v254, 19\n" \
"v_readlane_b32 s24, v254, 20\n" \
"v_readlane_b32 s25, v254, 21\n" \
"v_readlane_b32 s26, v254, 22\n" \
"v_readlane_b32 s27, v254, 23\n" \
"v_readlane_b32 s28, v254, 24\n" \
"v_readlane_b32 s29, v254, 25\n" \
"v_readlane_b32 s30, v254, 26\n" \
"v_readlane_b32 s31, v254, 27\n" \
"v_readlane_b32 s32, v254, 28\n" \
"v_readlane_b32 s33, v254, 29\n" \
"v_readlane_b32 s34, v254, 30\n" \
"v_readlane_b32 s35, v254, 31\n" \
"v_readlane_b32 s36, v254, 32\n" \
"v_readlane_b32 s37, v254, 33\n" \
"v_readlane_b32 s38, v254, 34\n" \
"v_readlane_b32 s39, v254, 35\n" \
"s_mov_b32 s40, 0\n" \
"s_mov_b32 s51, 0\n" \
"s_nop 0\n" \
"v_mov_b32 v252, s38\n" \
"s_nop 4\n" \
"v_mov_b32 v188, -1\n" \
"s_mov_b32 s28, s33\n" \
"s_mov_b32 s33, -1\n" \
"v_writelane_b32 v251, s6, 0\n" \
"v_writelane_b32 v251, s10, 1\n" \
"v_writelane_b32 v251, s14, 2\n" \
"v_writelane_b32 v251, s18, 3\n" \
"s_mov_b32 s6, 0\n" \
"s_mov_b32 s10, 0\n" \
"s_mov_b32 s14, 0\n" \
"s_mov_b32 s18, 0\n" \
"s_mov_b32 s40, -1\n" \
"s_branch .LS3_pre\n" \
".LS3_wet:\n" \
"s_waitcnt vmcnt(0)\n" \
"v_readlane_b32 s6, v251, 0\n" \
"v_readlane_b32 s10, v251, 1\n" \
"v_readlane_b32 s14, v251, 2\n" \
"v_readlane_b32 s18, v251, 3\n" \
"s_nop 4\n" \
"s_mov_b32 s33, s28\n" \
"s_add_u32 s34, s33, 2\n" \
"s_add_u32 s36, s33, 4\n" \
"s_add_u32 s43, s30, s33\n" \
"s_mov_b32 s50, 0\n" \
".LS3_w16l:\n" \
"buffer_load_dword v240, v204, s[4:7], 0 offen sc1\n" \
"s_waitcnt vmcnt(0)\n" \
".LS3_w16c:\n" \
"v_subrev_u32 v241, s43, v240\n" \
"v_cmp_gt_u32 vcc, 0x100, v241\n" \
"s_cmp_eq_u64 vcc, -1\n" \
"s_cbranch_scc1 .LS3_w16d\n" \
"s_sleep 2\n" \
"s_add_u32 s50, s50, 1\n" \
"s_cmp_lt_u32 s50, 0x20000\n" \
"s_cbranch_scc1 .LS3_w16l\n" \
"s_branch .LS3_end\n" \
".LS3_w16d:\n" \
"v_readlane_b32 s43, v202, 2\n" \
"s_nop 4\n" \
"buffer_load_dwordx4 a[16:19], v201, s[8:11], s43 offen sc1\n" \
"buffer_load_dwordx4 a[20:23], v201, s[8:11], s43 offen offset:1024 sc1\n" \
"v_readlane_b32 s43, v202, 3\n" \
"s_nop 4\n" \
"buffer_load_dwordx4 a[24:27], v201, s[8:11], s43 offen sc1\n" \
"buffer_load_dwordx4 a[28:31], v201, s[8:11], s43 offen offset:1024 sc1\n" \
"v_readlane_b32 s43, v202, 4\n" \
"s_nop 4\n" \
"buffer_load_dwordx4 a[32:35], v201, s[8:11], s43 offen sc1\n" \
"buffer_load_dwordx4 a[36:39], v201, s[8:11], s43 offen offset:1024 sc1\n" \
"v_readlane_b32 s43, v202, 5\n" \
"s_nop 4\n" \
"buffer_load_dwordx4 a[40:43], v201, s[8:11], s43 offen sc1\n" \
"buffer_load_dwordx4 a[44:47], v201, s[8:11], s43 offen offset:1024 sc1\n" \
"s_add_u32 s43, s30, s34\n" \
"s_mov_b32 s50, 0\n" \
"s_branch .LS3_w17c\n" \
".LS3_w17l:\n" \
"buffer_load_dword v240, v204, s[4:7], 0 offen sc1\n" \
"s_waitcnt vmcnt(0)\n" \
".LS3_w17c:\n" \
"v_subrev_u32 v241, s43, v240\n" \
"v_cmp_gt_u32 vcc, 0x100, v241\n" \
"s_cmp_eq_u64 vcc, -1\n" \
"s_cbranch_scc1 .LS3_w17d\n" \
"s_sleep 2\n" \
"s_add_u32 s50, s50, 1\n" \
"s_cmp_lt_u32 s50, 0x20000\n" \
"s_cbranch_scc1 .LS3_w17l\n" \
"s_branch .LS3_end\n" \
".LS3_w17d:\n" \
"v_readlane_b32 s43, v202, 6\n" \
"s_nop 4\n" \
"buffer_load_dwordx4 a[48:51], v201, s[8:11], s43 offen sc1\n" \
"buffer_load_dwordx4 a[52:55], v201, s[8:11], s43 offen offset:1024 sc1\n" \
"v_readlane_b32 s43, v202, 7\n" \
"s_nop 4\n" \
"buffer_load_dwordx4 a[56:59], v201, s[8:11], s43 offen sc1\n" \
"buffer_load_dwordx4 a[60:63], v201, s[8:11], s43 offen offset:1024 sc1\n" \
"v_readlane_b32 s43, v202, 8\n" \
"s_nop 4\n" \
"buffer_load_dwordx4 a[64:67], v201, s[8:11], s43 offen sc1\n" \
"buffer_load_dwordx4 a[68:71], v201, s[8:11], s43 offen offset:1024 sc1\n" \
"v_readlane_b32 s43, v202, 9\n" \
"s_nop 4\n" \
"buffer_load_dwordx4 a[72:75], v201, s[8:11], s43 offen sc1\n" \
"buffer_load_dwordx4 a[76:79], v201, s[8:11], s43 offen offset:1024 sc1\n" \
"s_add_u32 s43, s30, s36\n" \
"s_mov_b32 s50, 0\n" \
"s_branch .LS3_w18c\n" \
".LS3_w18l:\n" \
"buffer_load_dword v240, v204, s[4:7], 0 offen sc1\n" \
"s_waitcnt vmcnt(0)\n" \
".LS3_w18c:\n" \
"v_subrev_u32 v241, s43, v240\n" \
"v_cmp_gt_u32 vcc, 0x100, v241\n" \
"s_cmp_eq_u64 vcc, -1\n" \
"s_cbranch_scc1 .LS3_w18d\n" \
"s_sleep 2\n" \
"s_add_u32 s50, s50, 1\n" \
"s_cmp_lt_u32 s50, 0x20000\n" \
"s_cbranch_scc1 .LS3_w18l\n" \
"s_branch .LS3_end\n" \
".LS3_w18d:\n" \
"v_readlane_b32 s43, v202, 10\n" \
"s_nop 4\n" \
"buffer_load_dwordx4 a[80:83], v201, s[8:11], s43 offen sc1\n" \
"buffer_load_dwordx4 a[84:87], v201, s[8:11], s43 offen offset:1024 sc1\n" \
"v_readlane_b32 s43, v202, 11\n" \
"s_nop 4\n" \
"buffer_load_dwordx4 a[88:91], v201, s[8:11], s43 offen sc1\n" \
"buffer_load_dwordx4 a[92:95], v201, s[8:11], s43 offen offset:1024 sc1\n" \
"v_readlane_b32 s43, v202, 12\n" \
"s_nop 4\n" \
"buffer_load_dwordx4 a[96:99], v201, s[8:11], s43 offen sc1\n" \
"buffer_load_dwordx4 a[100:103], v201, s[8:11], s43 offen offset:1024 sc1\n" \
"v_readlane_b32 s43, v202, 13\n" \
"s_nop 4\n" \
"buffer_load_dwordx4 a[104:107], v201, s[8:11], s43 offen sc1\n" \
"buffer_load_dwordx4 a[108:111], v201, s[8:11], s43 offen offset:1024 sc1\n" \
"s_add_u32 s43, s30, 7\n" \
"s_mov_b32 s50, 0\n" \
"s_branch .LS3_w19c\n" \
".LS3_w19l:\n" \
"buffer_load_dword v240, v204, s[4:7], 0 offen sc1\n" \
"s_waitcnt vmcnt(0)\n" \
".LS3_w19c:\n" \
"v_subrev_u32 v241, s43, v240\n" \
"v_cmp_gt_u32 vcc, 0x100, v241\n" \
"s_cmp_eq_u64 vcc, -1\n" \
"s_cbranch_scc1 .LS3_w19d\n" \
"s_sleep 2\n" \
"s_add_u32 s50, s50, 1\n" \
"s_cmp_lt_u32 s50, 0x20000\n" \
"s_cbranch_scc1 .LS3_w19l\n" \
"s_branch .LS3_end\n" \
".LS3_w19d:\n" \
"v_readlane_b32 s43, v202, 0\n" \
"s_nop 4\n" \
"buffer_load_dwordx4 a[0:3], v201, s[8:11], s43 offen sc1\n" \
"buffer_load_dwordx4 a[4:7], v201, s[8:11], s43 offen offset:1024 sc1\n" \
"v_readlane_b32 s43, v202, 1\n" \
"s_nop 4\n" \
"buffer_load_dwordx4 a[8:11], v201, s[8:11], s43 offen sc1\n" \
"buffer_load_dwordx4 a[12:15], v201, s[8:11], s43 offen offset:1024 sc1\n" \
"s_mov_b32 s33, -1\n" \
"s_add_u32 s43, s30, 1\n" \
"s_mov_b32 s50, 0\n" \
".LS3_w20l:\n" \
"buffer_load_dword v240, v205, s[4:7], 0 offen sc1\n" \
"s_waitcnt vmcnt(0)\n" \
".LS3_w20c:\n" \
"v_subrev_u32 v241, s43, v240\n" \
"v_cmp_gt_u32 vcc, 0x100, v241\n" \
"s_cmp_eq_u64 vcc, -1\n" \
"s_cbranch_scc1 .LS3_w20d\n" \
"s_sleep 2\n" \
"s_add_u32 s50, s50, 1\n" \
"s_cmp_lt_u32 s50, 0x20000\n" \
"s_cbranch_scc1 .LS3_w20l\n" \
"s_branch .LS3_end\n" \
".LS3_w20d:\n" \
"v_readfirstlane_b32 s31, v241\n" \
"s_lshr_b32 s32, s31, 2\n" \
"s_xor_b32 s29, s31, s32\n" \
"s_and_b32 s29, s29, 3\n" \
"s_lshl_b32 s29, s29, 13\n" \
"s_bitset1_b32 s29, 21\n" \
"s_and_b32 s31, s31, 3\n" \
"s_lshl_b32 s31, s31, 13\n" \
"s_bitcmp1_b32 s39, 0\n" \
"s_cselect_b32 s43, s29, 0\n" \
"s_xor_b32 s31, s31, s43\n" \
"s_xor_b32 s32, s31, 0x4000\n" \
"s_mov_b32 s39, 0\n" \
".LS3_pre:\n" \
"s_barrier\n" \
"ds_read_b96 v[248:250], v252\n" \
"s_waitcnt lgkmcnt(0)\n" \
"v_readfirstlane_b32 s44, v248\n" \
"v_readfirstlane_b32 s45, v249\n" \
"v_readfirstlane_b32 s46, v250\n" \
"s_mul_i32 s42, s25, s46\n" \
"s_lshr_b32 s42, s42, 12\n" \
".LS3_cu:\n" \
"s_sub_u32 s43, s24, s44\n" \
"s_cmp_lt_i32 s43, 0\n" \
"s_cbranch_scc0 .LS3_cud\n" \
"s_add_u32 s24, s24, s25\n" \
"s_xor_b32 s31, s31, s29\n" \
"s_xor_b32 s32, s32, s29\n" \
"s_xor_b32 s47, s47, s29\n" \
"s_add_u32 s39, s39, 1\n" \
"s_branch .LS3_cu\n" \
".LS3_cud:\n" \
".LS3_loop:\n" \
"s_add_u32 s43, s40, 0\n" \
"s_and_b32 s43, s43, 3\n" \
"s_bitcmp1_b32 s43, 0\n" \
"s_cselect_b32 s47, s32, s31\n" \
"s_lshl_b32 s43, s43, 19\n" \
"s_add_u32 s47, s47, s43\n" \
"s_add_u32 s43, s40, 2\n" \
"s_and_b32 s43, s43, 3\n" \
"s_bitcmp1_b32 s43, 0\n" \
"s_cselect_b32 s49, s32, s31\n" \
"s_lshl_b32 s43, s43, 19\n" \
"s_add_u32 s49, s49, s43\n" \
"s_sub_u32 s43, s24, s44\n" \
"s_max_i32 s43, s43, 0xfffd0000\n" \
"s_min_i32 s43, s43, 0x4000\n" \
"s_mul_i32 s43, s43, s46\n" \
"s_ashr_i32 s43, s43, 12\n" \
"s_add_u32 s41, s45, s43\n" \
"s_add_u32 s41, s41, 12\n" \
"s_branch .LS3_a1\n" \
".LS3_retry:\n" \
"s_add_u32 s51, s51, 1\n" \
"s_cmp_gt_u32 s51, 0x20000\n" \
"s_cbranch_scc1 .LS3_end\n" \
".LS3_a0:\n" \
"s_add_u32 s41, s41, s42\n" \
"s_add_u32 s24, s24, s25\n" \
"s_xor_b32 s31, s31, s29\n" \
"s_xor_b32 s32, s32, s29\n" \
"s_xor_b32 s47, s47, s29\n" \
"s_xor_b32 s48, s48, s29\n" \
"s_add_u32 s39, s39, 1\n" \
"buffer_store_dword v188, v213, s[4:7], s47 offen sc1\n" \
"buffer_store_dword v188, v215, s[4:7], s47 offen sc1\n" \
".p2align 6\n" \
".LS3_a1:\n" \
"s_memtime vcc\n" \
"s_waitcnt lgkmcnt(0)\n" \
"s_sub_u32 s43, s41, vcc_lo\n" \
"s_cmp_gt_i32 s43, 128\n" \
"s_cbranch_scc1 .LS3_a1\n" \
"s_cmp_lt_i32 s43, s37\n" \
"s_cbranch_scc1 .LS3_a0\n" \
"s_max_i32 s43, s43, 0\n" \
"s_and_b32 s43, s43, -4\n" \
"s_getpc_b64 vcc\n" \
".LS3_a3:\n" \
"s_sub_u32 s43, .LS3_a4-.LS3_a3, s43\n" \
"s_add_u32 vcc_lo, vcc_lo, s43\n" \
"s_addc_u32 vcc_hi, vcc_hi, 0\n" \
"s_setpc_b64 vcc\n" \
".rept 32\n" \
"s_nop 0\n" \
".endr\n" \
".LS3_a4:\n" \
"buffer_load_dwordx4 v[12:15], v210, s[4:7], s47 offen sc1\n" \
"s_nop 9\n" \
"buffer_load_dwordx4 v[0:3], v208, s[4:7], s47 offen sc1\n" \
"s_nop 9\n" \
"buffer_load_dwordx4 v[4:7], v208, s[4:7], s47 offen offset:1024 sc1\n" \
"s_nop 9\n" \
"buffer_load_dwordx4 v[8:11], v208, s[4:7], s47 offen offset:2048 sc1\n" \
"s_nop 9\n" \
"buffer_load_dwordx4 v[16:19], v209, s[4:7], s47 offen sc1\n" \
"s_nop 3\n" \
"buffer_load_dwordx4 v[20:23], v209, s[4:7], s47 offen offset:1024 sc1\n" \
"s_nop 3\n" \
"buffer_load_dwordx4 v[24:27], v209, s[4:7], s47 offen offset:2048 sc1\n" \
"s_lshl_b32 s43, s40, 7\n" \
"buffer_load_dwordx4 v[236:239], v207, s[4:7], s43 offen sc1\n" \
"s_add_u32 s35, s35, 1\n" \
"v_mov_b32 v28, 0\n" \
"v_mov_b32 v29, 0\n" \
"v_mov_b32 v30, 0\n" \
"v_mov_b32 v31, 0\n" \
"v_mov_b32 v32, 0\n" \
"v_mov_b32 v33, 0\n" \
"v_mov_b32 v34, 0\n" \
"v_mov_b32 v35, 0\n" \
"v_mov_b32 v36, 0\n" \
"v_mov_b32 v37, 0\n" \
"v_mov_b32 v38, 0\n" \
"v_mov_b32 v39, 0\n" \
"v_mov_b32 v40, 0\n" \
"v_mov_b32 v41, 0\n" \
"v_mov_b32 v42, 0\n" \
"v_mov_b32 v43, 0\n" \
"v_mov_b32 v68, 0\n" \
"v_mov_b32 v69, 0\n" \
"v_mov_b32 v70, 0\n" \
"v_mov_b32 v71, 0\n" \
"s_waitcnt vmcnt(7)\n" \
"v_smfmac_f32_16x16x64_bf16 v[36:39], v[12:15], a[0:7], v200\n" \
"v_smfmac_f32_16x16x64_bf16 v[40:43], v[12:15], a[8:15], v200\n" \
"v_max3_u32 v64, v12, v12, v13\n" \
"v_max3_u32 v64, v64, v14, v15\n" \
"s_waitcnt vmcnt(6)\n" \
"v_smfmac_f32_16x16x64_bf16 v[28:31], v[0:3], a[16:23], v200\n" \
"v_smfmac_f32_16x16x64_bf16 v[32:35], v[0:3], a[24:31], v200\n" \
"v_smfmac_f32_16x16x64_bf16 v[36:39], v[0:3], a[32:39], v200\n" \
"v_smfmac_f32_16x16x64_bf16 v[40:43], v[0:3], a[40:47], v200\n" \
"v_max3_u32 v64, v64, v0, v1\n" \
"v_max3_u32 v64, v64, v2, v3\n" \
"s_waitcnt vmcnt(5)\n" \
"v_smfmac_f32_16x16x64_bf16 v[28:31], v[4:7], a[48:55], v200\n" \
"v_smfmac_f32_16x16x64_bf16 v[32:35], v[4:7], a[56:63], v200\n" \
"v_smfmac_f32_16x16x64_bf16 v[36:39], v[4:7], a[64:71], v200\n" \
"v_smfmac_f32_16x16x64_bf16 v[40:43], v[4:7], a[72:79], v200\n" \
"v_max3_u32 v64, v64, v4, v5\n" \
"v_max3_u32 v64, v64, v6, v7\n" \
"s_waitcnt vmcnt(4)\n" \
"v_smfmac_f32_16x16x64_bf16 v[28:31], v[8:11], a[80:87], v200\n" \
"v_smfmac_f32_16x16x64_bf16 v[32:35], v[8:11], a[88:95], v200\n" \
"v_smfmac_f32_16x16x64_bf16 v[36:39], v[8:11], a[96:103], v200\n" \
"v_smfmac_f32_16x16x64_bf16 v[40:43], v[8:11], a[104:111], v200\n" \
"v_max3_u32 v64, v64, v8, v9\n" \
"v_max3_u32 v64, v64, v10, v11\n" \
"s_nop 2\n" \
"v_cvt_pk_bf16_f32 v128, v28, v29\n" \
"v_cvt_pk_bf16_f32 v129, v30, v31\n" \
"v_cvt_pk_bf16_f32 v130, v32, v33\n" \
"v_cvt_pk_bf16_f32 v131, v34, v35\n" \
"ds_write_b64 v211, v[128:129] offset:0\n" \
"v_cvt_pk_bf16_f32 v132, v36, v37\n" \
"v_cvt_pk_bf16_f32 v133, v38, v39\n" \
"ds_write_b64 v211, v[130:131] offset:128\n" \
"v_cvt_pk_bf16_f32 v134, v40, v41\n" \
"v_cvt_pk_bf16_f32 v135, v42, v43\n" \
"ds_write_b64 v214, v[132:133] offset:0\n" \
"ds_write_b64 v214, v[134:135] offset:128\n" \
"ds_read_b64_tr_b16 v[96:97], v212 offset:6912\n" \
"ds_read_b64_tr_b16 v[98:99], v212 offset:6928\n" \
"ds_read_b64_tr_b16 v[100:101], v212 offset:7168\n" \
"ds_read_b64_tr_b16 v[102:103], v212 offset:7184\n" \
"s_nop 0\n" \
"s_waitcnt vmcnt(1)\n" \
"v_max3_u32 v64, v64, v16, v17\n" \
"v_max3_u32 v64, v64, v18, v19\n" \
"v_max3_u32 v64, v64, v20, v21\n" \
"v_max3_u32 v64, v64, v22, v23\n" \
"v_max3_u32 v64, v64, v24, v25\n" \
"v_max3_u32 v64, v64, v26, v27\n" \
"v_cmp_eq_u32 vcc, -1, v64\n" \
"s_barrier\n" \
"ds_read_b64_tr_b16 v[72:73], v212 offset:0\n" \
"ds_read_b64_tr_b16 v[74:75], v212 offset:16\n" \
"ds_read_b64_tr_b16 v[76:77], v212 offset:256\n" \
"ds_read_b64_tr_b16 v[78:79], v212 offset:272\n" \
"ds_read_b64_tr_b16 v[80:81], v212 offset:2304\n" \
"ds_read_b64_tr_b16 v[82:83], v212 offset:2320\n" \
"ds_read_b64_tr_b16 v[84:85], v212 offset:2560\n" \
"ds_read_b64_tr_b16 v[86:87], v212 offset:2576\n" \
"s_waitcnt lgkmcnt(8)\n" \
"v_smfmac_f32_16x16x64_bf16 v[68:71], v[12:15], v[96:103], v200\n" \
"ds_read_b64_tr_b16 v[88:89], v212 offset:4608\n" \
"ds_read_b64_tr_b16 v[90:91], v212 offset:4624\n" \
"ds_read_b64_tr_b16 v[92:93], v212 offset:4864\n" \
"ds_read_b64_tr_b16 v[94:95], v212 offset:4880\n" \
"s_waitcnt vmcnt(1)\n" \
"s_waitcnt lgkmcnt(8)\n" \
"v_smfmac_f32_16x16x64_bf16 v[68:71], v[0:3], v[72:79], v200\n" \
"s_waitcnt lgkmcnt(4)\n" \
"v_smfmac_f32_16x16x64_bf16 v[68:71], v[4:7], v[80:87], v200\n" \
"s_waitcnt lgkmcnt(0)\n" \
"v_smfmac_f32_16x16x64_bf16 v[68:71], v[8:11], v[88:95], v200\n" \
"s_cbranch_vccnz .LS3_veto\n" \
"s_barrier\n" \
"ds_read_b64_tr_b16 v[104:105], v212 offset:9216\n" \
"ds_read_b64_tr_b16 v[106:107], v212 offset:9232\n" \
"ds_read_b64_tr_b16 v[108:109], v212 offset:9472\n" \
"ds_read_b64_tr_b16 v[110:111], v212 offset:9488\n" \
"s_nop 2\n" \
"v_cmp_u_f32 vcc, v68, v68\n" \
"s_barrier\n" \
"ds_read_b64_tr_b16 v[112:113], v212 offset:11520\n" \
"ds_read_b64_tr_b16 v[114:115], v212 offset:11536\n" \
"ds_read_b64_tr_b16 v[116:117], v212 offset:11776\n" \
"ds_read_b64_tr_b16 v[118:119], v212 offset:11792\n" \
"s_cbranch_vccnz .LS3_veto2\n" \
".LS3_veto2r:\n" \
"s_barrier\n" \
"ds_read_b64_tr_b16 v[120:121], v212 offset:13824\n" \
"ds_read_b64_tr_b16 v[122:123], v212 offset:13840\n" \
"ds_read_b64_tr_b16 v[124:125], v212 offset:14080\n" \
"ds_read_b64_tr_b16 v[126:127], v212 offset:14096\n" \
"s_waitcnt lgkmcnt(8)\n" \
"v_smfmac_f32_16x16x64_bf16 v[68:71], v[16:19], v[104:111], v200\n" \
"s_waitcnt lgkmcnt(4)\n" \
"v_smfmac_f32_16x16x64_bf16 v[68:71], v[20:23], v[112:119], v200\n" \
"s_waitcnt lgkmcnt(0)\n" \
"v_smfmac_f32_16x16x64_bf16 v[68:71], v[24:27], v[120:127], v200\n" \
"s_nop 7\n" \
"v_add_f32_dpp v128, v68, v68 row_shl:4 row_mask:0xf bank_mask:0x5\n" \
"v_add_f32_dpp v129, v70, v70 row_shl:4 row_mask:0xf bank_mask:0x5\n" \
"v_add_f32_dpp v128, v69, v69 row_shr:4 row_mask:0xf bank_mask:0xa\n" \
"v_add_f32_dpp v129, v71, v71 row_shr:4 row_mask:0xf bank_mask:0xa\n" \
"s_nop 1\n" \
"v_permlane32_swap_b32 v128, v129\n" \
"v_add_f32 v128, v128, v129\n" \
"buffer_store_dword v128, v213, s[4:7], s47 offen sc1\n" \
"buffer_store_dword v128, v215, s[4:7], s47 offen sc1\n" \
"s_xor_b32 s43, s47, s29\n" \
"buffer_store_dword v128, v213, s[4:7], s43 offen sc1\n" \
"buffer_store_dword v128, v215, s[4:7], s43 offen sc1\n" \
"s_waitcnt lgkmcnt(0)\n" \
"s_barrier\n" \
"ds_read_b32 v64, v252 offset:16\n" \
"s_waitcnt lgkmcnt(0)\n" \
"v_readfirstlane_b32 s43, v64\n" \
"s_cmp_eq_u32 s43, s35\n" \
"s_cbranch_scc1 .LS3_retry\n" \
"v_cmp_u_f32 vcc, v128, v128\n" \
"s_cbranch_vccnz .LS3_nan\n" \
".LS3_nanr:\n" \
"s_waitcnt vmcnt(4)\n" \
"v_max_u32 v236, v236, v238\n" \
"v_max_u32 v237, v237, v239\n" \
"v_and_b32 v236, 0xfff, v236\n" \
"v_and_b32 v237, 0xfff, v237\n" \
"s_mov_b64 exec, 1\n" \
"ds_write_b64 v252, v[236:237] offset:24\n" \
"s_mov_b64 exec, -1\n" \
"buffer_store_dword v188, v213, s[4:7], s49 offen sc1\n" \
"buffer_store_dword v188, v215, s[4:7], s49 offen sc1\n" \
"s_xor_b32 s43, s49, s29\n" \
"buffer_store_dword v188, v213, s[4:7], s43 offen sc1\n" \
"buffer_store_dword v188, v215, s[4:7], s43 offen sc1\n" \
"s_waitcnt lgkmcnt(0)\n" \
"s_barrier\n" \
"ds_read_b64 v[246:247], v252 offset:24\n" \
"ds_read_b96 v[248:250], v252\n" \
"s_waitcnt lgkmcnt(0)\n" \
"v_readfirstlane_b32 s44, v248\n" \
"v_readfirstlane_b32 s45, v249\n" \
"v_readfirstlane_b32 s46, v250\n" \
"v_readfirstlane_b32 s26, v246\n" \
"v_readfirstlane_b32 s27, v247\n" \
"s_mul_i32 s42, s25, s46\n" \
"s_lshr_b32 s42, s42, 12\n" \
"s_add_u32 s40, s40, 1\n" \
"s_add_u32 s24, s24, s26\n" \
"s_add_u32 s24, s24, s27\n" \
"s_cmp_eq_u32 s40, 0\n" \
"s_cbranch_scc1 .LS3_wet\n" \
"s_cmp_lt_u32 s40, 63\n" \
"s_cbranch_scc1 .LS3_loop\n" \
".LS3_end:\n" \
"s_endpgm\n" \
".LS3_nan:\n" \
"v_cmp_u_f32 vcc, v128, v128\n" \
"v_mov_b32 v129, 0x7fc00001\n" \
"s_nop 1\n" \
"v_cndmask_b32 v128, v128, v129, vcc\n" \
"buffer_store_dword v128, v213, s[4:7], s47 offen sc1\n" \
"buffer_store_dword v128, v215, s[4:7], s47 offen sc1\n" \
"s_xor_b32 s43, s47, s29\n" \
"buffer_store_dword v128, v213, s[4:7], s43 offen sc1\n" \
"buffer_store_dword v128, v215, s[4:7], s43 offen sc1\n" \
"s_branch .LS3_nanr\n" \
".LS3_veto:\n" \
"v_mov_b32 v64, s35\n" \
"ds_write_b32 v252, v64 offset:20\n" \
"s_barrier\n" \
"s_barrier\n" \
"s_waitcnt lgkmcnt(0)\n" \
"s_barrier\n" \
"s_branch .LS3_retry\n" \
".LS3_veto2:\n" \
"s_cmp_eq_u32 s33, s40\n" \
"s_cbranch_scc1 .LS3_veto2r\n" \
"s_mov_b32 s33, s40\n" \
"v_mov_b32 v64, s35\n" \
"ds_write_b32 v252, v64 offset:20\n" \
"s_waitcnt lgkmcnt(0)\n" \
"s_barrier\n" \
"s_branch .LS3_retry\n" \
".p2align 6\n" \
"fk_role_P0:\n" \
"ds_read_b32 v200, v255 offset:0\n" \
"ds_read_b32 v201, v255 offset:256\n" \
"ds_read_b32 v202, v255 offset:512\n" \
"ds_read_b32 v203, v255 offset:768\n" \
"ds_read_b32 v204, v255 offset:1024\n" \
"ds_read_b32 v205, v255 offset:1280\n" \
"ds_read_b32 v206, v255 offset:1536\n" \
"ds_read_b32 v207, v255 offset:1792\n" \
"ds_read_b32 v208, v255 offset:2048\n" \
"ds_read_b32 v209, v255 offset:2304\n" \
"ds_read_b32 v210, v255 offset:2560\n" \
"ds_read_b32 v211, v255 offset:2816\n" \
"ds_read_b32 v212, v255 offset:3072\n" \
"ds_read_b32 v213, v255 offset:3328\n" \
"ds_read_b32 v214, v255 offset:3584\n" \
"ds_read_b32 v215, v255 offset:3840\n" \
"ds_read_b32 v216, v255 offset:4096\n" \
"ds_read_b32 v217, v255 offset:4352\n" \
"ds_read_b32 v218, v255 offset:4608\n" \
"ds_read_b32 v219, v255 offset:4864\n" \
"ds_read_b32 v220, v255 offset:5120\n" \
"ds_read_b32 v221, v255 offset:5376\n" \
"ds_read_b32 v222, v255 offset:5632\n" \
"ds_read_b32 v223, v255 offset:5888\n" \
"ds_read_b32 v254, v255 offset:7936\n" \
"s_waitcnt lgkmcnt(0)\n" \
"v_readlane_b32 s4, v254, 0\n" \
"v_readlane_b32 s5, v254, 1\n" \
"v_readlane_b32 s6, v254, 2\n" \
"v_readlane_b32 s7, v254, 3\n" \
"v_readlane_b32 s8, v254, 4\n" \
"v_readlane_b32 s9, v254, 5\n" \
"v_readlane_b32 s10, v254, 6\n" \
"v_readlane_b32 s11, v254, 7\n" \
"v_readlane_b32 s12, v254, 8\n" \
"v_readlane_b32 s13, v254, 9\n" \
"v_readlane_b32 s14, v254, 10\n" \
"v_readlane_b32 s15, v254, 11\n" \
"v_readlane_b32 s16, v254, 12\n" \
"v_readlane_b32 s17, v254, 13\n" \
"v_readlane_b32 s18, v254, 14\n" \
"v_readlane_b32 s19, v254, 15\n" \
"v_readlane_b32 s20, v254, 16\n" \
"v_readlane_b32 s21, v254, 17\n" \
"v_readlane_b32 s22, v254, 18\n" \
"v_readlane_b32 s23, v254, 19\n" \
"v_readlane_b32 s24, v254, 20\n" \
"v_readlane_b32 s25, v254, 21\n" \
"v_readlane_b32 s26, v254, 22\n" \
"v_readlane_b32 s27, v254, 23\n" \
"v_readlane_b32 s28, v254, 24\n" \
"v_readlane_b32 s29, v254, 25\n" \
"v_readlane_b32 s30, v254, 26\n" \
"v_readlane_b32 s31, v254, 27\n" \
"v_readlane_b32 s32, v254, 28\n" \
"v_readlane_b32 s33, v254, 29\n" \
"v_readlane_b32 s34, v254, 30\n" \
"v_readlane_b32 s35, v254, 31\n" \
"v_readlane_b32 s36, v254, 32\n" \
"v_readlane_b32 s37, v254, 33\n" \
"v_readlane_b32 s38, v254, 34\n" \
"v_readlane_b32 s39, v254, 35\n" \
"s_mov_b32 s40, 0\n" \
"s_mov_b32 s51, 0\n" \
"s_nop 0\n" \
"v_mov_b32 v252, s38\n" \
"s_nop 4\n" \
"v_mov_b32 v112, -1\n" \
"v_mov_b32 v113, -1\n" \
"v_mov_b32 v114, -1\n" \
"v_mov_b32 v115, -1\n" \
"v_mov_b32 v109, 0x7fc00001\n" \
"v_writelane_b32 v251, s6, 0\n" \
"v_writelane_b32 v251, s10, 1\n" \
"v_writelane_b32 v251, s14, 2\n" \
"v_writelane_b32 v251, s18, 3\n" \
"s_mov_b32 s6, 0\n" \
"s_mov_b32 s10, 0\n" \
"s_mov_b32 s14, 0\n" \
"s_mov_b32 s18, 0\n" \
"s_mov_b32 s40, -1\n" \
"s_branch .LP0_pre\n" \
".LP0_wet:\n" \
"s_waitcnt vmcnt(0)\n" \
"v_readlane_b32 s6, v251, 0\n" \
"v_readlane_b32 s10, v251, 1\n" \
"v_readlane_b32 s14, v251, 2\n" \
"v_readlane_b32 s18, v251, 3\n" \
"s_nop 4\n" \
"s_mov_b32 s33, 40\n" \
"s_mov_b32 s34, 0\n" \
"s_add_u32 s43, s30, 1\n" \
"s_mov_b32 s50, 0\n" \
".LP0_w21l:\n" \
"buffer_load_dword v240, v204, s[4:7], 0 offen sc1\n" \
"s_waitcnt vmcnt(0)\n" \
".LP0_w21c:\n" \
"v_subrev_u32 v241, s43, v240\n" \
"v_cmp_gt_u32 vcc, 0x100, v241\n" \
"s_cmp_eq_u64 vcc, -1\n" \
"s_cbranch_scc1 .LP0_w21d\n" \
"s_sleep 2\n" \
"s_add_u32 s50, s50, 1\n" \
"s_cmp_lt_u32 s50, 0x20000\n" \
"s_cbranch_scc1 .LP0_w21l\n" \
"s_branch .LP0_end\n" \
".LP0_w21d:\n" \
"v_readlane_b32 s43, v202, 0\n" \
"s_nop 4\n" \
"buffer_load_dwordx4 a[0:3], v201, s[8:11], s43 offen sc1\n" \
"buffer_load_dwordx4 a[4:7], v201, s[8:11], s43 offen offset:1024 sc1\n" \
"v_readlane_b32 s43, v202, 7\n" \
"s_nop 4\n" \
"buffer_load_dwordx4 a[56:59], v201, s[8:11], s43 offen sc1\n" \
"buffer_load_dwordx4 a[60:63], v201, s[8:11], s43 offen offset:1024 sc1\n" \
"v_readlane_b32 s43, v202, 14\n" \
"s_nop 4\n" \
"buffer_load_dwordx4 a[112:115], v201, s[8:11], s43 offen sc1\n" \
"buffer_load_dwordx4 a[116:119], v201, s[8:11], s43 offen offset:1024 sc1\n" \
"v_readlane_b32 s43, v202, 21\n" \
"s_nop 4\n" \
"buffer_load_dwordx4 a[168:171], v201, s[8:11], s43 offen sc1\n" \
"buffer_load_dwordx4 a[172:175], v201, s[8:11], s43 offen offset:1024 sc1\n" \
"s_add_u32 s43, s30, 2\n" \
"s_mov_b32 s50, 0\n" \
"s_branch .LP0_w22c\n" \
".LP0_w22l:\n" \
"buffer_load_dword v240, v204, s[4:7], 0 offen sc1\n" \
"s_waitcnt vmcnt(0)\n" \
".LP0_w22c:\n" \
"v_subrev_u32 v241, s43, v240\n" \
"v_cmp_gt_u32 vcc, 0x100, v241\n" \
"s_cmp_eq_u64 vcc, -1\n" \
"s_cbranch_scc1 .LP0_w22d\n" \
"s_sleep 2\n" \
"s_add_u32 s50, s50, 1\n" \
"s_cmp_lt_u32 s50, 0x20000\n" \
"s_cbranch_scc1 .LP0_w22l\n" \
"s_branch .LP0_end\n" \
".LP0_w22d:\n" \
"v_readlane_b32 s43, v202, 4\n" \
"s_nop 4\n" \
"buffer_load_dwordx4 a[32:35], v201, s[8:11], s43 offen sc1\n" \
"buffer_load_dwordx4 a[36:39], v201, s[8:11], s43 offen offset:1024 sc1\n" \
"v_readlane_b32 s43, v202, 11\n" \
"s_nop 4\n" \
"buffer_load_dwordx4 a[88:91], v201, s[8:11], s43 offen sc1\n" \
"buffer_load_dwordx4 a[92:95], v201, s[8:11], s43 offen offset:1024 sc1\n" \
"v_readlane_b32 s43, v202, 18\n" \
"s_nop 4\n" \
"buffer_load_dwordx4 a[144:147], v201, s[8:11], s43 offen sc1\n" \
"buffer_load_dwordx4 a[148:151], v201, s[8:11], s43 offen offset:1024 sc1\n" \
"v_readlane_b32 s43, v202, 25\n" \
"s_nop 4\n" \
"buffer_load_dwordx4 a[200:203], v201, s[8:11], s43 offen sc1\n" \
"buffer_load_dwordx4 a[204:207], v201, s[8:11], s43 offen offset:1024 sc1\n" \
"s_add_u32 s43, s30, 3\n" \
"s_mov_b32 s50, 0\n" \
"s_branch .LP0_w23c\n" \
".LP0_w23l:\n" \
"buffer_load_dword v240, v204, s[4:7], 0 offen sc1\n" \
"s_waitcnt vmcnt(0)\n" \
".LP0_w23c:\n" \
"v_subrev_u32 v241, s43, v240\n" \
"v_cmp_gt_u32 vcc, 0x100, v241\n" \
"s_cmp_eq_u64 vcc, -1\n" \
"s_cbranch_scc1 .LP0_w23d\n" \
"s_sleep 2\n" \
"s_add_u32 s50, s50, 1\n" \
"s_cmp_lt_u32 s50, 0x20000\n" \
"s_cbranch_scc1 .LP0_w23l\n" \
"s_branch .LP0_end\n" \
".LP0_w23d:\n" \
"v_readlane_b32 s43, v202, 1\n" \
"s_nop 4\n" \
"buffer_load_dwordx4 a[8:11], v201, s[8:11], s43 offen sc1\n" \
"buffer_load_dwordx4 a[12:15], v201, s[8:11], s43 offen offset:1024 sc1\n" \
"v_readlane_b32 s43, v202, 8\n" \
"s_nop 4\n" \
"buffer_load_dwordx4 a[64:67], v201, s[8:11], s43 offen sc1\n" \
"buffer_load_dwordx4 a[68:71], v201, s[8:11], s43 offen offset:1024 sc1\n" \
"v_readlane_b32 s43, v202, 15\n" \
"s_nop 4\n" \
"buffer_load_dwordx4 a[120:123], v201, s[8:11], s43 offen sc1\n" \
"buffer_load_dwordx4 a[124:127], v201, s[8:11], s43 offen offset:1024 sc1\n" \
"v_readlane_b32 s43, v202, 22\n" \
"s_nop 4\n" \
"buffer_load_dwordx4 a[176:179], v201, s[8:11], s43 offen sc1\n" \
"buffer_load_dwordx4 a[180:183], v201, s[8:11], s43 offen offset:1024 sc1\n" \
"s_add_u32 s43, s30, 4\n" \
"s_mov_b32 s50, 0\n" \
"s_branch .LP0_w24c\n" \
".LP0_w24l:\n" \
"buffer_load_dword v240, v204, s[4:7], 0 offen sc1\n" \
"s_waitcnt vmcnt(0)\n" \
".LP0_w24c:\n" \
"v_subrev_u32 v241, s43, v240\n" \
"v_cmp_gt_u32 vcc, 0x100, v241\n" \
"s_cmp_eq_u64 vcc, -1\n" \
"s_cbranch_scc1 .LP0_w24d\n" \
"s_sleep 2\n" \
"s_add_u32 s50, s50, 1\n" \
"s_cmp_lt_u32 s50, 0x20000\n" \
"s_cbranch_scc1 .LP0_w24l\n" \
"s_branch .LP0_end\n" \
".LP0_w24d:\n" \
"v_readlane_b32 s43, v202, 5\n" \
"s_nop 4\n" \
"buffer_load_dwordx4 a[40:43], v201, s[8:11], s43 offen sc1\n" \
"buffer_load_dwordx4 a[44:47], v201, s[8:11], s43 offen offset:1024 sc1\n" \
"v_readlane_b32 s43, v202, 12\n" \
"s_nop 4\n" \
"buffer_load_dwordx4 a[96:99], v201, s[8:11], s43 offen sc1\n" \
"buffer_load_dwordx4 a[100:103], v201, s[8:11], s43 offen offset:1024 sc1\n" \
"v_readlane_b32 s43, v202, 19\n" \
"s_nop 4\n" \
"buffer_load_dwordx4 a[152:155], v201, s[8:11], s43 offen sc1\n" \
"buffer_load_dwordx4 a[156:159], v201, s[8:11], s43 offen offset:1024 sc1\n" \
"v_readlane_b32 s43, v202, 26\n" \
"s_nop 4\n" \
"buffer_load_dwordx4 a[208:211], v201, s[8:11], s43 offen sc1\n" \
"buffer_load_dwordx4 a[212:215], v201, s[8:11], s43 offen offset:1024 sc1\n" \
"s_add_u32 s43, s30, 5\n" \
"s_mov_b32 s50, 0\n" \
"s_branch .LP0_w25c\n" \
".LP0_w25l:\n" \
"buffer_load_dword v240, v204, s[4:7], 0 offen sc1\n" \
"s_waitcnt vmcnt(0)\n" \
".LP0_w25c:\n" \
"v_subrev_u32 v241, s43, v240\n" \
"v_cmp_gt_u32 vcc, 0x100, v241\n" \
"s_cmp_eq_u64 vcc, -1\n" \
"s_cbranch_scc1 .LP0_w25d\n" \
"s_sleep 2\n" \
"s_add_u32 s50, s50, 1\n" \
"s_cmp_lt_u32 s50, 0x20000\n" \
"s_cbranch_scc1 .LP0_w25l\n" \
"s_branch .LP0_end\n" \
".LP0_w25d:\n" \
"v_readlane_b32 s43, v202, 2\n" \
"s_nop 4\n" \
"buffer_load_dwordx4 a[16:19], v201, s[8:11], s43 offen sc1\n" \
"buffer_load_dwordx4 a[20:23], v201, s[8:11], s43 offen offset:1024 sc1\n" \
"v_readlane_b32 s43, v202, 9\n" \
"s_nop 4\n" \
"buffer_load_dwordx4 a[72:75], v201, s[8:11], s43 offen sc1\n" \
"buffer_load_dwordx4 a[76:79], v201, s[8:11], s43 offen offset:1024 sc1\n" \
"v_readlane_b32 s43, v202, 16\n" \
"s_nop 4\n" \
"buffer_load_dwordx4 a[128:131], v201, s[8:11], s43 offen sc1\n" \
"buffer_load_dwordx4 a[132:135], v201, s[8:11], s43 offen offset:1024 sc1\n" \
"v_readlane_b32 s43, v202, 23\n" \
"s_nop 4\n" \
"buffer_load_dwordx4 a[184:187], v201, s[8:11], s43 offen sc1\n" \
"buffer_load_dwordx4 a[188:191], v201, s[8:11], s43 offen offset:1024 sc1\n" \
"s_add_u32 s43, s30, 6\n" \
"s_mov_b32 s50, 0\n" \
"s_branch .LP0_w26c\n" \
".LP0_w26l:\n" \
"buffer_load_dword v240, v204, s[4:7], 0 offen sc1\n" \
"s_waitcnt vmcnt(0)\n" \
".LP0_w26c:\n" \
"v_subrev_u32 v241, s43, v240\n" \
"v_cmp_gt_u32 vcc, 0x100, v241\n" \
"s_cmp_eq_u64 vcc, -1\n" \
"s_cbranch_scc1 .LP0_w26d\n" \
"s_sleep 2\n" \
"s_add_u32 s50, s50, 1\n" \
"s_cmp_lt_u32 s50, 0x20000\n" \
"s_cbranch_scc1 .LP0_w26l\n" \
"s_branch .LP0_end\n" \
".LP0_w26d:\n" \
"v_readlane_b32 s43, v202, 6\n" \
"s_nop 4\n" \
"buffer_load_dwordx4 a[48:51], v201, s[8:11], s43 offen sc1\n" \
"buffer_load_dwordx4 a[52:55], v201, s[8:11], s43 offen offset:1024 sc1\n" \
"v_readlane_b32 s43, v202, 13\n" \
"s_nop 4\n" \
"buffer_load_dwordx4 a[104:107], v201, s[8:11], s43 offen sc1\n" \
"buffer_load_dwordx4 a[108:111], v201, s[8:11], s43 offen offset:1024 sc1\n" \
"v_readlane_b32 s43, v202, 20\n" \
"s_nop 4\n" \
"buffer_load_dwordx4 a[160:163], v201, s[8:11], s43 offen sc1\n" \
"buffer_load_dwordx4 a[164:167], v201, s[8:11], s43 offen offset:1024 sc1\n" \
"v_readlane_b32 s43, v202, 27\n" \
"s_nop 4\n" \
"buffer_load_dwordx4 a[216:219], v201, s[8:11], s43 offen sc1\n" \
"buffer_load_dwordx4 a[220:223], v201, s[8:11], s43 offen offset:1024 sc1\n" \
"s_add_u32 s43, s30, 7\n" \
"s_mov_b32 s50, 0\n" \
"s_branch .LP0_w27c\n" \
".LP0_w27l:\n" \
"buffer_load_dword v240, v204, s[4:7], 0 offen sc1\n" \
"s_waitcnt vmcnt(0)\n" \
".LP0_w27c:\n" \
"v_subrev_u32 v241, s43, v240\n" \
"v_cmp_gt_u32 vcc, 0x100, v241\n" \
"s_cmp_eq_u64 vcc, -1\n" \
"s_cbranch_scc1 .LP0_w27d\n" \
"s_sleep 2\n" \
"s_add_u32 s50, s50, 1\n" \
"s_cmp_lt_u32 s50, 0x20000\n" \
"s_cbranch_scc1 .LP0_w27l\n" \
"s_branch .LP0_end\n" \
".LP0_w27d:\n" \
"v_readlane_b32 s43, v202, 3\n" \
"s_nop 4\n" \
"buffer_load_dwordx4 a[24:27], v201, s[8:11], s43 offen sc1\n" \
"buffer_load_dwordx4 a[28:31], v201, s[8:11], s43 offen offset:1024 sc1\n" \
"v_readlane_b32 s43, v202, 10\n" \
"s_nop 4\n" \
"buffer_load_dwordx4 a[80:83], v201, s[8:11], s43 offen sc1\n" \
"buffer_load_dwordx4 a[84:87], v201, s[8:11], s43 offen offset:1024 sc1\n" \
"v_readlane_b32 s43, v202, 17\n" \
"s_nop 4\n" \
"buffer_load_dwordx4 a[136:139], v201, s[8:11], s43 offen sc1\n" \
"buffer_load_dwordx4 a[140:143], v201, s[8:11], s43 offen offset:1024 sc1\n" \
"v_readlane_b32 s43, v202, 24\n" \
"s_nop 4\n" \
"buffer_load_dwordx4 a[192:195], v201, s[8:11], s43 offen sc1\n" \
"buffer_load_dwordx4 a[196:199], v201, s[8:11], s43 offen offset:1024 sc1\n" \
"s_add_u32 s43, s30, 1\n" \
"s_mov_b32 s50, 0\n" \
".LP0_w28l:\n" \
"buffer_load_dword v240, v205, s[4:7], 0 offen sc1\n" \
"s_waitcnt vmcnt(0)\n" \
".LP0_w28c:\n" \
"v_subrev_u32 v241, s43, v240\n" \
"v_cmp_gt_u32 vcc, 0x100, v241\n" \
"s_cmp_eq_u64 vcc, -1\n" \
"s_cbranch_scc1 .LP0_w28d\n" \
"s_sleep 2\n" \
"s_add_u32 s50, s50, 1\n" \
"s_cmp_lt_u32 s50, 0x20000\n" \
"s_cbranch_scc1 .LP0_w28l\n" \
"s_branch .LP0_end\n" \
".LP0_w28d:\n" \
"v_readfirstlane_b32 s31, v241\n" \
"s_lshr_b32 s32, s31, 2\n" \
"s_xor_b32 s29, s31, s32\n" \
"s_and_b32 s29, s29, 3\n" \
"s_lshl_b32 s29, s29, 13\n" \
"s_bitset1_b32 s29, 21\n" \
"s_and_b32 s31, s31, 3\n" \
"s_lshl_b32 s31, s31, 13\n" \
"s_bitcmp1_b32 s39, 0\n" \
"s_cselect_b32 s43, s29, 0\n" \
"s_xor_b32 s31, s31, s43\n" \
"s_xor_b32 s32, s31, 0x4000\n" \
"s_mov_b32 s39, 0\n" \
"s_add_u32 s43, s40, 0\n" \
"s_and_b32 s43, s43, 3\n" \
"s_bitcmp1_b32 s43, 0\n" \
"s_cselect_b32 s47, s32, s31\n" \
"s_lshl_b32 s43, s43, 19\n" \
"s_add_u32 s47, s47, s43\n" \
".LP0_pre:\n" \
"s_barrier\n" \
"ds_read_b96 v[248:250], v252\n" \
"s_waitcnt lgkmcnt(0)\n" \
"v_readfirstlane_b32 s44, v248\n" \
"v_readfirstlane_b32 s45, v249\n" \
"v_readfirstlane_b32 s46, v250\n" \
"s_mul_i32 s42, s25, s46\n" \
"s_lshr_b32 s42, s42, 12\n" \
".LP0_cu:\n" \
"s_sub_u32 s43, s24, s44\n" \
"s_cmp_lt_i32 s43, 0\n" \
"s_cbranch_scc0 .LP0_cud\n" \
"s_add_u32 s24, s24, s25\n" \
"s_xor_b32 s31, s31, s29\n" \
"s_xor_b32 s32, s32, s29\n" \
"s_xor_b32 s47, s47, s29\n" \
"s_add_u32 s39, s39, 1\n" \
"s_branch .LP0_cu\n" \
".LP0_cud:\n" \
"s_mov_b32 s3, s46\n" \
"s_add_u32 s43, s40, 1\n" \
"s_and_b32 s43, s43, 3\n" \
"s_bitcmp1_b32 s43, 0\n" \
"s_cselect_b32 s48, s32, s31\n" \
"s_lshl_b32 s43, s43, 19\n" \
"s_add_u32 s48, s48, s43\n" \
"s_add_u32 s43, s40, 2\n" \
"s_and_b32 s43, s43, 3\n" \
"s_bitcmp1_b32 s43, 0\n" \
"s_cselect_b32 s49, s32, s31\n" \
"s_lshl_b32 s43, s43, 19\n" \
"s_add_u32 s49, s49, s43\n" \
"s_sub_u32 s43, s24, s44\n" \
"s_add_u32 s43, s43, s28\n" \
"s_max_i32 s43, s43, 0xfffd0000\n" \
"s_min_i32 s43, s43, 0x4000\n" \
"s_mul_i32 s43, s43, s46\n" \
"s_ashr_i32 s43, s43, 12\n" \
"s_add_u32 s41, s45, s43\n" \
"s_lshr_b32 s35, s42, 1\n" \
"s_sub_u32 s35, 0, s35\n" \
".LP0_loop:\n" \
"s_branch .LP0_m1\n" \
".LP0_retry1:\n" \
"s_add_u32 s51, s51, 1\n" \
"s_cmp_gt_u32 s51, 0x20000\n" \
"s_cbranch_scc1 .LP0_end\n" \
".LP0_m0:\n" \
"s_add_u32 s41, s41, s42\n" \
"s_add_u32 s24, s24, s25\n" \
"s_xor_b32 s31, s31, s29\n" \
"s_xor_b32 s32, s32, s29\n" \
"s_xor_b32 s47, s47, s29\n" \
"s_xor_b32 s48, s48, s29\n" \
"s_add_u32 s39, s39, 1\n" \
"s_bitcmp1_b32 s31, 21\n" \
"s_cselect_b32 s43, s29, 0\n" \
"v_mov_b32 v116, s43\n" \
"ds_write_b32 v252, v116 offset:16\n" \
".LP0_m1:\n" \
"s_memtime vcc\n" \
"s_waitcnt lgkmcnt(0)\n" \
"s_sub_u32 s43, s41, vcc_lo\n" \
"s_cmp_gt_i32 s43, 0\n" \
"s_cbranch_scc1 .LP0_m1\n" \
"s_cmp_lt_i32 s43, s35\n" \
"s_cbranch_scc1 .LP0_m0\n" \
"buffer_load_dwordx4 v[0:3], v208, s[4:7], s47 offen sc1\n" \
"buffer_load_dwordx4 v[4:7], v208, s[4:7], s47 offen offset:1024 sc1\n" \
"s_nop 15\n" \
"s_nop 13\n" \
"s_mul_i32 s50, s40, 4096\n" \
"buffer_store_dwordx4 v[68:71], v218, s[12:15], s50 offen sc1\n" \
"buffer_store_dword v112, v213, s[4:7], s49 offen sc1\n" \
"s_xor_b32 s43, s49, s29\n" \
"buffer_store_dword v112, v213, s[4:7], s43 offen sc1\n" \
"s_add_u32 s43, s30, s40\n" \
"s_add_u32 s43, s43, -1\n" \
"v_mov_b32 v241, s43\n" \
"buffer_store_dword v241, v221, s[4:7], 0 offen sc1\n" \
"s_bitcmp1_b32 s31, 21\n" \
"s_cselect_b32 s43, s29, 0\n" \
"v_mov_b32 v116, s43\n" \
"ds_write_b32 v252, v116 offset:16\n" \
"v_mov_b32 v28, 0\n" \
"v_mov_b32 v29, 0\n" \
"v_mov_b32 v30, 0\n" \
"v_mov_b32 v31, 0\n" \
"v_mov_b32 v32, 0\n" \
"v_mov_b32 v33, 0\n" \
"v_mov_b32 v34, 0\n" \
"v_mov_b32 v35, 0\n" \
"v_mov_b32 v36, 0\n" \
"v_mov_b32 v37, 0\n" \
"v_mov_b32 v38, 0\n" \
"v_mov_b32 v39, 0\n" \
"v_mov_b32 v40, 0\n" \
"v_mov_b32 v41, 0\n" \
"v_mov_b32 v42, 0\n" \
"v_mov_b32 v43, 0\n" \
"s_waitcnt vmcnt(5)\n" \
"ds_write_b128 v220, v[0:3] offset:0\n" \
"s_waitcnt vmcnt(4)\n" \
"ds_write_b128 v220, v[4:7] offset:1024\n" \
"v_max3_u32 v44, v0, v0, v1\n" \
"v_max3_u32 v44, v44, v2, v3\n" \
"v_max3_u32 v44, v44, v4, v5\n" \
"v_max3_u32 v44, v44, v6, v7\n" \
"v_cmp_eq_u32 vcc, -1, v44\n" \
"v_smfmac_f32_16x16x64_bf16 v[28:31], v[0:3], a[0:7], v200\n" \
"v_smfmac_f32_16x16x64_bf16 v[32:35], v[0:3], a[56:63], v200\n" \
"s_cbranch_vccnz .LP0_retry1\n" \
"s_waitcnt lgkmcnt(0)\n" \
"s_barrier\n" \
"ds_read_b128 v[8:11], v220 offset:2048\n" \
"ds_read_b128 v[12:15], v220 offset:3072\n" \
"ds_read_b128 v[16:19], v220 offset:4096\n" \
"ds_read_b128 v[20:23], v220 offset:5120\n" \
"ds_read_b128 v[24:27], v220 offset:6144\n" \
"ds_read_b64 v[246:247], v252 offset:24\n" \
"ds_read_b96 v[248:250], v252\n" \
"v_smfmac_f32_16x16x64_bf16 v[36:39], v[0:3], a[112:119], v200\n" \
"v_smfmac_f32_16x16x64_bf16 v[40:43], v[0:3], a[168:175], v200\n" \
"v_smfmac_f32_16x16x64_bf16 v[28:31], v[4:7], a[8:15], v200\n" \
"v_smfmac_f32_16x16x64_bf16 v[32:35], v[4:7], a[64:71], v200\n" \
"v_smfmac_f32_16x16x64_bf16 v[36:39], v[4:7], a[120:127], v200\n" \
"v_smfmac_f32_16x16x64_bf16 v[40:43], v[4:7], a[176:183], v200\n" \
"s_waitcnt lgkmcnt(6)\n" \
"v_smfmac_f32_16x16x64_bf16 v[28:31], v[8:11], a[16:23], v200\n" \
"v_smfmac_f32_16x16x64_bf16 v[32:35], v[8:11], a[72:79], v200\n" \
"v_smfmac_f32_16x16x64_bf16 v[36:39], v[8:11], a[128:135], v200\n" \
"v_smfmac_f32_16x16x64_bf16 v[40:43], v[8:11], a[184:191], v200\n" \
"s_waitcnt lgkmcnt(0)\n" \
"v_readfirstlane_b32 s44, v248\n" \
"v_readfirstlane_b32 s45, v249\n" \
"v_smfmac_f32_16x16x64_bf16 v[28:31], v[12:15], a[24:31], v200\n" \
"v_readfirstlane_b32 s46, v250\n" \
"v_readfirstlane_b32 s26, v246\n" \
"v_readfirstlane_b32 s27, v247\n" \
"v_smfmac_f32_16x16x64_bf16 v[32:35], v[12:15], a[80:87], v200\n" \
"s_mul_i32 s42, s25, s46\n" \
"s_lshr_b32 s42, s42, 12\n" \
"s_add_u32 s24, s24, s26\n" \
"v_smfmac_f32_16x16x64_bf16 v[36:39], v[12:15], a[136:143], v200\n" \
"s_sub_u32 s43, s24, s44\n" \
"s_max_i32 s43, s43, 0xfffd0000\n" \
"s_min_i32 s43, s43, 0x4000\n" \
"v_smfmac_f32_16x16x64_bf16 v[40:43], v[12:15], a[192:199], v200\n" \
"s_mul_i32 s43, s43, s46\n" \
"s_ashr_i32 s43, s43, 12\n" \
"s_add_u32 s41, s45, s43\n" \
"s_cmp_eq_u32 s40, 63\n" \
"s_cbranch_scc1 .LP0_last\n" \
"s_branch .LP0_z1\n" \
".LP0_z0:\n" \
"s_add_u32 s41, s41, s42\n" \
"s_add_u32 s24, s24, s25\n" \
"s_xor_b32 s31, s31, s29\n" \
"s_xor_b32 s32, s32, s29\n" \
"s_xor_b32 s47, s47, s29\n" \
"s_xor_b32 s48, s48, s29\n" \
"s_add_u32 s39, s39, 1\n" \
"buffer_store_dwordx4 v[112:115], v223, s[4:7], s48 offen sc1\n" \
"s_bitcmp1_b32 s31, 21\n" \
"s_cselect_b32 s43, s29, 0\n" \
"v_mov_b32 v116, s43\n" \
"ds_write_b32 v252, v116 offset:16\n" \
".LP0_z1:\n" \
"s_memtime vcc\n" \
"s_waitcnt lgkmcnt(0)\n" \
"s_sub_u32 s43, s41, vcc_lo\n" \
"s_cmp_gt_i32 s43, 200\n" \
"s_cbranch_scc1 .LP0_z1\n" \
"s_cmp_lt_i32 s43, s37\n" \
"s_cbranch_scc1 .LP0_z0\n" \
"buffer_load_dwordx2 v[64:65], v211, s[4:7], s47 offen sc1\n" \
".p2align 6\n" \
".LP0_b1:\n" \
"s_memtime vcc\n" \
"s_waitcnt lgkmcnt(0)\n" \
"s_sub_u32 s43, s41, vcc_lo\n" \
"s_cmp_gt_i32 s43, 128\n" \
"s_cbranch_scc1 .LP0_b1\n" \
"s_cmp_lt_i32 s43, s37\n" \
"s_cbranch_scc1 .LP0_z0\n" \
"s_max_i32 s43, s43, 0\n" \
"s_and_b32 s43, s43, -4\n" \
"s_getpc_b64 vcc\n" \
".LP0_b3:\n" \
"s_sub_u32 s43, .LP0_b4-.LP0_b3, s43\n" \
"s_add_u32 vcc_lo, vcc_lo, s43\n" \
"s_addc_u32 vcc_hi, vcc_hi, 0\n" \
"s_setpc_b64 vcc\n" \
".rept 32\n" \
"s_nop 0\n" \
".endr\n" \
".LP0_b4:\n" \
"buffer_load_dwordx4 v[56:59], v210, s[4:7], s47 offen sc1\n" \
"buffer_load_dwordx4 v[60:63], v210, s[4:7], s47 offen offset:256 sc1\n" \
"v_smfmac_f32_16x16x64_bf16 v[28:31], v[16:19], a[32:39], v200\n" \
"v_smfmac_f32_16x16x64_bf16 v[32:35], v[16:19], a[88:95], v200\n" \
"v_smfmac_f32_16x16x64_bf16 v[36:39], v[16:19], a[144:151], v200\n" \
"v_smfmac_f32_16x16x64_bf16 v[40:43], v[16:19], a[200:207], v200\n" \
"v_smfmac_f32_16x16x64_bf16 v[28:31], v[20:23], a[40:47], v200\n" \
"v_smfmac_f32_16x16x64_bf16 v[32:35], v[20:23], a[96:103], v200\n" \
"buffer_load_dwordx4 v[50:53], v212, s[4:7], s47 offen sc1\n" \
"v_smfmac_f32_16x16x64_bf16 v[36:39], v[20:23], a[152:159], v200\n" \
"v_smfmac_f32_16x16x64_bf16 v[40:43], v[20:23], a[208:215], v200\n" \
"v_smfmac_f32_16x16x64_bf16 v[28:31], v[24:27], a[48:55], v200\n" \
"v_smfmac_f32_16x16x64_bf16 v[32:35], v[24:27], a[104:111], v200\n" \
"v_smfmac_f32_16x16x64_bf16 v[36:39], v[24:27], a[160:167], v200\n" \
"v_smfmac_f32_16x16x64_bf16 v[40:43], v[24:27], a[216:223], v200\n" \
"s_nop 2\n" \
"v_mov_b32 v245, v245\n" \
"v_mov_b32 v245, v245\n" \
"v_mov_b32 v245, v245\n" \
"v_permlane32_swap_b32 v28, v32\n" \
"v_permlane32_swap_b32 v29, v33\n" \
"v_permlane32_swap_b32 v30, v34\n" \
"v_permlane32_swap_b32 v31, v35\n" \
"v_add_f32 v28, v28, v32\n" \
"v_add_f32 v29, v29, v33\n" \
"v_add_f32 v30, v30, v34\n" \
"v_add_f32 v31, v31, v35\n" \
"v_cvt_pk_bf16_f32 v68, v28, v29\n" \
"v_cvt_pk_bf16_f32 v69, v30, v31\n" \
"v_permlane32_swap_b32 v36, v40\n" \
"v_permlane32_swap_b32 v37, v41\n" \
"v_permlane32_swap_b32 v38, v42\n" \
"v_permlane32_swap_b32 v39, v43\n" \
"v_add_f32 v36, v36, v40\n" \
"v_add_f32 v37, v37, v41\n" \
"v_add_f32 v38, v38, v42\n" \
"v_add_f32 v39, v39, v43\n" \
"v_cvt_pk_bf16_f32 v70, v36, v37\n" \
"v_cvt_pk_bf16_f32 v71, v38, v39\n" \
"v_mov_b32 v48, v68\n" \
"v_mov_b32 v49, v69\n" \
"v_mov_b32 v54, v70\n" \
"v_mov_b32 v55, v71\n" \
"s_nop 1\n" \
"v_permlane16_swap_b32 v48, v54\n" \
"v_permlane16_swap_b32 v49, v55\n" \
".LP0_wait:\n" \
"s_waitcnt vmcnt(1)\n" \
"v_pk_add_f32 v[72:73], v[56:57], v[60:61]\n" \
"v_pk_add_f32 v[74:75], v[58:59], v[62:63]\n" \
"v_max3_f32 v77, v72, v73, v64\n" \
"v_max3_f32 v77, v77, v74, v75\n" \
"s_nop 1\n" \
"v_max_f32_dpp v76, v77, v77 row_ror:8 row_mask:0xf bank_mask:0xf\n" \
"v_pk_add_f32 v[72:73], v[72:73], v[76:77] op_sel_hi:[1,0] neg_lo:[0,1] neg_hi:[0,1]\n" \
"v_pk_add_f32 v[74:75], v[74:75], v[76:77] op_sel_hi:[1,0] neg_lo:[0,1] neg_hi:[0,1]\n" \
"v_sub_f32 v78, v64, v76\n" \
"v_exp_f32 v72, v72\n" \
"v_exp_f32 v73, v73\n" \
"v_exp_f32 v74, v74\n" \
"v_exp_f32 v75, v75\n" \
"v_exp_f32 v78, v78\n" \
"v_pk_add_f32 v[80:81], v[72:73], v[74:75]\n" \
"v_fma_f32 v80, v78, v65, v80\n" \
"v_add_f32 v81, v80, v81\n" \
"s_nop 1\n" \
"v_add_f32_dpp v80, v81, v81 row_ror:8 row_mask:0xf bank_mask:0xf\n" \
"v_rcp_f32 v80, v80\n" \
"s_nop 0\n" \
"v_mul_f32 v78, v78, v80\n" \
"v_pk_mul_f32 v[72:73], v[72:73], v[80:81] op_sel_hi:[1,0]\n" \
"v_pk_mul_f32 v[74:75], v[74:75], v[80:81] op_sel_hi:[1,0]\n" \
"v_cvt_pk_bf16_f32 v78, v78, v78\n" \
"v_cvt_pk_bf16_f32 v82, v72, v73\n" \
"v_cvt_pk_bf16_f32 v83, v74, v75\n" \
"v_and_b32 v84, v78, v214\n" \
"v_and_b32 v85, v78, v215\n" \
"v_cvt_pk_bf16_f32 v86, v72, v73\n" \
"v_cvt_pk_bf16_f32 v87, v74, v75\n" \
"s_waitcnt vmcnt(0)\n" \
"v_mfma_f32_16x16x32_bf16 v[88:91], v[48:51], v[82:85], 0\n" \
"v_mfma_f32_16x16x32_bf16 v[92:95], v[52:55], v[84:87], 0\n" \
"s_nop 2\n" \
"v_mov_b32 v245, v245\n" \
"s_nop 2\n" \
"v_mov_b32 v245, v245\n" \
"v_add_f32_dpp v96, v92, v88 row_ror:8 row_mask:0xf bank_mask:0x3\n" \
"v_add_f32_dpp v97, v93, v89 row_ror:8 row_mask:0xf bank_mask:0x3\n" \
"v_add_f32_dpp v96, v90, v94 row_ror:8 row_mask:0xf bank_mask:0xc\n" \
"v_add_f32_dpp v97, v91, v95 row_ror:8 row_mask:0xf bank_mask:0xc\n" \
"ds_write_b64 v216, v[96:97]\n" \
"v_cmp_u_f32 vcc, v96, v97\n" \
"s_bitcmp1_b32 s31, 21\n" \
"v_mov_b32 v245, v245\n" \
"s_cselect_b32 s18, s29, 0\n" \
"s_xor_b32 s18, s18, s48\n" \
"s_xor_b32 s19, s18, s29\n" \
"v_mov_b32 v245, v245\n" \
"s_nop 0\n" \
"s_nop 0\n" \
"s_nop 0\n" \
"v_mov_b32 v245, v245\n" \
"s_cbranch_vccnz .LP0_cls\n" \
".LP0_clsr:\n" \
"s_waitcnt lgkmcnt(0)\n" \
"s_barrier\n" \
"ds_read_b64 v[100:101], v217 offset:0\n" \
"v_mov_b32 v245, v245\n" \
"ds_read_b64 v[102:103], v217 offset:512\n" \
"ds_read_b32 v116, v252 offset:16\n" \
"ds_read_b64 v[104:105], v217 offset:1024\n" \
"ds_read_b64 v[106:107], v217 offset:1536\n" \
"s_add_u32 s40, s40, 1\n" \
"s_add_u32 s24, s24, s27\n" \
"v_mov_b32 v245, v245\n" \
"s_waitcnt lgkmcnt(2)\n" \
"v_xad_u32 v117, v116, s18, v213\n" \
"v_pk_add_f32 v[100:101], v[100:101], v[102:103]\n" \
"s_waitcnt lgkmcnt(0)\n" \
"v_pk_add_f32 v[104:105], v[104:105], v[106:107]\n" \
"v_pk_add_f32 v[100:101], v[100:101], v[104:105]\n" \
"v_cvt_pk_bf16_f32 v96, v100, v101\n" \
"buffer_store_dword v96, v117, s[4:7], 0 offen sc1\n" \
"s_mov_b32 s47, s48\n" \
"s_add_u32 s43, s40, 1\n" \
"s_and_b32 s43, s43, 3\n" \
"s_bitcmp1_b32 s43, 0\n" \
"s_cselect_b32 s48, s32, s31\n" \
"s_lshl_b32 s43, s43, 19\n" \
"s_add_u32 s48, s48, s43\n" \
"s_add_u32 s43, s40, 2\n" \
"s_and_b32 s43, s43, 3\n" \
"s_bitcmp1_b32 s43, 0\n" \
"s_cselect_b32 s49, s32, s31\n" \
"s_lshl_b32 s43, s43, 19\n" \
"s_add_u32 s49, s49, s43\n" \
"s_sub_u32 s43, s24, s44\n" \
"s_add_u32 s43, s43, s28\n" \
"s_max_i32 s43, s43, 0xfffd0000\n" \
"s_min_i32 s43, s43, 0x4000\n" \
"s_mul_i32 s43, s43, s46\n" \
"s_ashr_i32 s43, s43, 12\n" \
"s_add_u32 s41, s45, s43\n" \
"s_lshr_b32 s35, s42, 1\n" \
"s_sub_u32 s35, 0, s35\n" \
"v_xad_u32 v116, v116, s19, v213\n" \
"buffer_store_dword v96, v116, s[4:7], 0 offen sc1\n" \
"s_cmp_eq_u32 s40, 0\n" \
"s_cbranch_scc1 .LP0_wet\n" \
"s_branch .LP0_loop\n" \
"s_branch .LP0_y1\n" \
".LP0_retry2:\n" \
"s_add_u32 s51, s51, 1\n" \
"s_cmp_gt_u32 s51, 0x20000\n" \
"s_cbranch_scc1 .LP0_end\n" \
".LP0_y0:\n" \
"s_add_u32 s41, s41, s42\n" \
"s_add_u32 s24, s24, s25\n" \
"s_xor_b32 s31, s31, s29\n" \
"s_xor_b32 s32, s32, s29\n" \
"s_xor_b32 s47, s47, s29\n" \
"s_xor_b32 s48, s48, s29\n" \
"s_add_u32 s39, s39, 1\n" \
"buffer_store_dwordx4 v[112:115], v223, s[4:7], s48 offen sc1\n" \
"s_bitcmp1_b32 s31, 21\n" \
"s_cselect_b32 s43, s29, 0\n" \
"v_mov_b32 v116, s43\n" \
"ds_write_b32 v252, v116 offset:16\n" \
".LP0_y1:\n" \
"s_memtime vcc\n" \
"s_waitcnt lgkmcnt(0)\n" \
"s_sub_u32 s43, s41, vcc_lo\n" \
"s_cmp_gt_i32 s43, 200\n" \
"s_cbranch_scc1 .LP0_y1\n" \
"s_cmp_lt_i32 s43, s37\n" \
"s_cbranch_scc1 .LP0_y0\n" \
"buffer_load_dwordx2 v[64:65], v211, s[4:7], s47 offen sc1\n" \
".p2align 6\n" \
".LP0_c1:\n" \
"s_memtime vcc\n" \
"s_waitcnt lgkmcnt(0)\n" \
"s_sub_u32 s43, s41, vcc_lo\n" \
"s_cmp_gt_i32 s43, 128\n" \
"s_cbranch_scc1 .LP0_c1\n" \
"s_cmp_lt_i32 s43, s37\n" \
"s_cbranch_scc1 .LP0_y0\n" \
"s_max_i32 s43, s43, 0\n" \
"s_and_b32 s43, s43, -4\n" \
"s_getpc_b64 vcc\n" \
".LP0_c3:\n" \
"s_sub_u32 s43, .LP0_c4-.LP0_c3, s43\n" \
"s_add_u32 vcc_lo, vcc_lo, s43\n" \
"s_addc_u32 vcc_hi, vcc_hi, 0\n" \
"s_setpc_b64 vcc\n" \
".rept 32\n" \
"s_nop 0\n" \
".endr\n" \
".LP0_c4:\n" \
"buffer_load_dwordx4 v[56:59], v210, s[4:7], s47 offen sc1\n" \
"buffer_load_dwordx4 v[60:63], v210, s[4:7], s47 offen offset:256 sc1\n" \
"s_nop 2\n" \
"v_mov_b32 v245, v245\n" \
"s_nop 2\n" \
"v_mov_b32 v245, v245\n" \
"s_nop 2\n" \
"v_mov_b32 v245, v245\n" \
"s_nop 2\n" \
"v_mov_b32 v245, v245\n" \
"s_nop 2\n" \
"v_mov_b32 v245, v245\n" \
"s_nop 2\n" \
"v_mov_b32 v245, v245\n" \
"buffer_load_dwordx4 v[50:53], v212, s[4:7], s47 offen sc1\n" \
"s_nop 2\n" \
"v_mov_b32 v245, v245\n" \
"s_nop 2\n" \
"v_mov_b32 v245, v245\n" \
"s_nop 2\n" \
"v_mov_b32 v245, v245\n" \
"s_nop 2\n" \
"v_mov_b32 v245, v245\n" \
"s_nop 2\n" \
"v_mov_b32 v245, v245\n" \
"s_nop 2\n" \
"v_mov_b32 v245, v245\n" \
"s_nop 2\n" \
"v_mov_b32 v245, v245\n" \
"s_nop 2\n" \
"v_mov_b32 v245, v245\n" \
"s_nop 2\n" \
"v_mov_b32 v245, v245\n" \
"s_nop 2\n" \
"v_mov_b32 v245, v245\n" \
"s_nop 2\n" \
"v_mov_b32 v245, v245\n" \
"s_nop 2\n" \
"v_mov_b32 v245, v245\n" \
"s_nop 2\n" \
"v_mov_b32 v245, v245\n" \
"s_nop 2\n" \
"v_mov_b32 v245, v245\n" \
"s_nop 2\n" \
"v_mov_b32 v245, v245\n" \
"s_nop 2\n" \
"v_mov_b32 v245, v245\n" \
"s_nop 2\n" \
"v_mov_b32 v245, v245\n" \
"s_branch .LP0_wait\n" \
".LP0_cls:\n" \
"s_waitcnt vmcnt(0)\n" \
"v_max3_u32 v44, v64, v64, v65\n" \
"v_max3_u32 v44, v44, v50, v51\n" \
"v_max3_u32 v44, v44, v52, v53\n" \
"v_cmp_eq_u32 vcc, -1, v44\n" \
"s_cbranch_vccnz .LP0_retry2\n" \
"v_cmp_u_f32 vcc, v56, v56\n" \
"v_cmp_ne_u32 s[52:53], v56, v109\n" \
"s_and_b64 vcc, vcc, s[52:53]\n" \
"s_cbranch_vccnz .LP0_retry2\n" \
"v_cmp_u_f32 vcc, v57, v57\n" \
"v_cmp_ne_u32 s[52:53], v57, v109\n" \
"s_and_b64 vcc, vcc, s[52:53]\n" \
"s_cbranch_vccnz .LP0_retry2\n" \
"v_cmp_u_f32 vcc, v58, v58\n" \
"v_cmp_ne_u32 s[52:53], v58, v109\n" \
"s_and_b64 vcc, vcc, s[52:53]\n" \
"s_cbranch_vccnz .LP0_retry2\n" \
"v_cmp_u_f32 vcc, v59, v59\n" \
"v_cmp_ne_u32 s[52:53], v59, v109\n" \
"s_and_b64 vcc, vcc, s[52:53]\n" \
"s_cbranch_vccnz .LP0_retry2\n" \
"v_cmp_u_f32 vcc, v60, v60\n" \
"v_cmp_ne_u32 s[52:53], v60, v109\n" \
"s_and_b64 vcc, vcc, s[52:53]\n" \
"s_cbranch_vccnz .LP0_retry2\n" \
"v_cmp_u_f32 vcc, v61, v61\n" \
"v_cmp_ne_u32 s[52:53], v61, v109\n" \
"s_and_b64 vcc, vcc, s[52:53]\n" \
"s_cbranch_vccnz .LP0_retry2\n" \
"v_cmp_u_f32 vcc, v62, v62\n" \
"v_cmp_ne_u32 s[52:53], v62, v109\n" \
"s_and_b64 vcc, vcc, s[52:53]\n" \
"s_cbranch_vccnz .LP0_retry2\n" \
"v_cmp_u_f32 vcc, v63, v63\n" \
"v_cmp_ne_u32 s[52:53], v63, v109\n" \
"s_and_b64 vcc, vcc, s[52:53]\n" \
"s_cbranch_vccnz .LP0_retry2\n" \
"s_branch .LP0_clsr\n" \
".LP0_last:\n" \
"v_smfmac_f32_16x16x64_bf16 v[28:31], v[16:19], a[32:39], v200\n" \
"v_smfmac_f32_16x16x64_bf16 v[32:35], v[16:19], a[88:95], v200\n" \
"v_smfmac_f32_16x16x64_bf16 v[36:39], v[16:19], a[144:151], v200\n" \
"v_smfmac_f32_16x16x64_bf16 v[40:43], v[16:19], a[200:207], v200\n" \
"v_smfmac_f32_16x16x64_bf16 v[28:31], v[20:23], a[40:47], v200\n" \
"v_smfmac_f32_16x16x64_bf16 v[32:35], v[20:23], a[96:103], v200\n" \
"v_smfmac_f32_16x16x64_bf16 v[36:39], v[20:23], a[152:159], v200\n" \
"v_smfmac_f32_16x16x64_bf16 v[40:43], v[20:23], a[208:215], v200\n" \
"v_smfmac_f32_16x16x64_bf16 v[28:31], v[24:27], a[48:55], v200\n" \
"v_smfmac_f32_16x16x64_bf16 v[32:35], v[24:27], a[104:111], v200\n" \
"v_smfmac_f32_16x16x64_bf16 v[36:39], v[24:27], a[160:167], v200\n" \
"v_smfmac_f32_16x16x64_bf16 v[40:43], v[24:27], a[216:223], v200\n" \
"s_waitcnt vmcnt(0)\n" \
"s_nop 7\n" \
"v_permlane32_swap_b32 v28, v32\n" \
"v_permlane32_swap_b32 v29, v33\n" \
"v_permlane32_swap_b32 v30, v34\n" \
"v_permlane32_swap_b32 v31, v35\n" \
"v_add_f32 v28, v28, v32\n" \
"v_add_f32 v29, v29, v33\n" \
"v_add_f32 v30, v30, v34\n" \
"v_add_f32 v31, v31, v35\n" \
"v_cvt_pk_bf16_f32 v68, v28, v29\n" \
"v_cvt_pk_bf16_f32 v69, v30, v31\n" \
"v_permlane32_swap_b32 v36, v40\n" \
"v_permlane32_swap_b32 v37, v41\n" \
"v_permlane32_swap_b32 v38, v42\n" \
"v_permlane32_swap_b32 v39, v43\n" \
"v_add_f32 v36, v36, v40\n" \
"v_add_f32 v37, v37, v41\n" \
"v_add_f32 v38, v38, v42\n" \
"v_add_f32 v39, v39, v43\n" \
"v_cvt_pk_bf16_f32 v70, v36, v37\n" \
"v_cvt_pk_bf16_f32 v71, v38, v39\n" \
"s_mov_b32 s50, 262144\n" \
"buffer_store_dwordx4 v[68:71], v218, s[12:15], s50 offen sc1\n" \
"s_waitcnt vmcnt(0)\n" \
"s_barrier\n" \
"s_add_u32 s43, s30, s40\n" \
"s_add_u32 s43, s43, 1\n" \
"v_mov_b32 v241, s43\n" \
"buffer_store_dword v241, v221, s[4:7], 0 offen sc1\n" \
".LP0_end:\n" \
"s_endpgm\n" \
".p2align 6\n" \
"fk_role_P1:\n" \
"ds_read_b32 v200, v255 offset:0\n" \
"ds_read_b32 v201, v255 offset:256\n" \
"ds_read_b32 v202, v255 offset:512\n" \
"ds_read_b32 v203, v255 offset:768\n" \
"ds_read_b32 v204, v255 offset:1024\n" \
"ds_read_b32 v205, v255 offset:1280\n" \
"ds_read_b32 v206, v255 offset:1536\n" \
"ds_read_b32 v207, v255 offset:1792\n" \
"ds_read_b32 v208, v255 offset:2048\n" \
"ds_read_b32 v209, v255 offset:2304\n" \
"ds_read_b32 v210, v255 offset:2560\n" \
"ds_read_b32 v211, v255 offset:2816\n" \
"ds_read_b32 v212, v255 offset:3072\n" \
"ds_read_b32 v213, v255 offset:3328\n" \
"ds_read_b32 v214, v255 offset:3584\n" \
"ds_read_b32 v215, v255 offset:3840\n" \
"ds_read_b32 v216, v255 offset:4096\n" \
"ds_read_b32 v217, v255 offset:4352\n" \
"ds_read_b32 v218, v255 offset:4608\n" \
"ds_read_b32 v219, v255 offset:4864\n" \
"ds_read_b32 v220, v255 offset:5120\n" \
"ds_read_b32 v221, v255 offset:5376\n" \
"ds_read_b32 v222, v255 offset:5632\n" \
"ds_read_b32 v223, v255 offset:5888\n" \
"ds_read_b32 v254, v255 offset:7936\n" \
"s_waitcnt lgkmcnt(0)\n" \
"v_readlane_b32 s4, v254, 0\n" \
"v_readlane_b32 s5, v254, 1\n" \
"v_readlane_b32 s6, v254, 2\n" \
"v_readlane_b32 s7, v254, 3\n" \
"v_readlane_b32 s8, v254, 4\n" \
"v_readlane_b32 s9, v254, 5\n" \
"v_readlane_b32 s10, v254, 6\n" \
"v_readlane_b32 s11, v254, 7\n" \
"v_readlane_b32 s12, v254, 8\n" \
"v_readlane_b32 s13, v254, 9\n" \
"v_readlane_b32 s14, v254, 10\n" \
"v_readlane_b32 s15, v254, 11\n" \
"v_readlane_b32 s16, v254, 12\n" \
"v_readlane_b32 s17, v254, 13\n" \
"v_readlane_b32 s18, v254, 14\n" \
"v_readlane_b32 s19, v254, 15\n" \
"v_readlane_b32 s20, v254, 16\n" \
"v_readlane_b32 s21, v254, 17\n" \
"v_readlane_b32 s22, v254, 18\n" \
"v_readlane_b32 s23, v254, 19\n" \
"v_readlane_b32 s24, v254, 20\n" \
"v_readlane_b32 s25, v254, 21\n" \
"v_readlane_b32 s26, v254, 22\n" \
"v_readlane_b32 s27, v254, 23\n" \
"v_readlane_b32 s28, v254, 24\n" \
"v_readlane_b32 s29, v254, 25\n" \
"v_readlane_b32 s30, v254, 26\n" \
"v_readlane_b32 s31, v254, 27\n" \
"v_readlane_b32 s32, v254, 28\n" \
"v_readlane_b32 s33, v254, 29\n" \
"v_readlane_b32 s34, v254, 30\n" \
"v_readlane_b32 s35, v254, 31\n" \
"v_readlane_b32 s36, v254, 32\n" \
"v_readlane_b32 s37, v254, 33\n" \
"v_readlane_b32 s38, v254, 34\n" \
"v_readlane_b32 s39, v254, 35\n" \
"s_mov_b32 s40, 0\n" \
"s_mov_b32 s51, 0\n" \
"s_nop 0\n" \
"v_mov_b32 v252, s38\n" \
"s_nop 4\n" \
"v_mov_b32 v112, -1\n" \
"v_mov_b32 v113, -1\n" \
"v_mov_b32 v114, -1\n" \
"v_mov_b32 v115, -1\n" \
"v_mov_b32 v109, 0x7fc00001\n" \
"v_writelane_b32 v251, s6, 0\n" \
"v_writelane_b32 v251, s10, 1\n" \
"v_writelane_b32 v251, s14, 2\n" \
"v_writelane_b32 v251, s18, 3\n" \
"s_mov_b32 s6, 0\n" \
"s_mov_b32 s10, 0\n" \
"s_mov_b32 s14, 0\n" \
"s_mov_b32 s18, 0\n" \
"s_mov_b32 s40, -1\n" \
"s_branch .LP1_pre\n" \
".LP1_wet:\n" \
"s_waitcnt vmcnt(0)\n" \
"v_readlane_b32 s6, v251, 0\n" \
"v_readlane_b32 s10, v251, 1\n" \
"v_readlane_b32 s14, v251, 2\n" \
"v_readlane_b32 s18, v251, 3\n" \
"s_nop 4\n" \
"s_mov_b32 s33, 40\n" \
"s_mov_b32 s34, 0\n" \
"s_add_u32 s43, s30, 1\n" \
"s_mov_b32 s50, 0\n" \
".LP1_w29l:\n" \
"buffer_load_dword v240, v204, s[4:7], 0 offen sc1\n" \
"s_waitcnt vmcnt(0)\n" \
".LP1_w29c:\n" \
"v_subrev_u32 v241, s43, v240\n" \
"v_cmp_gt_u32 vcc, 0x100, v241\n" \
"s_cmp_eq_u64 vcc, -1\n" \
"s_cbranch_scc1 .LP1_w29d\n" \
"s_sleep 2\n" \
"s_add_u32 s50, s50, 1\n" \
"s_cmp_lt_u32 s50, 0x20000\n" \
"s_cbranch_scc1 .LP1_w29l\n" \
"s_branch .LP1_end\n" \
".LP1_w29d:\n" \
"v_readlane_b32 s43, v202, 4\n" \
"s_nop 4\n" \
"buffer_load_dwordx4 a[32:35], v201, s[8:11], s43 offen sc1\n" \
"buffer_load_dwordx4 a[36:39], v201, s[8:11], s43 offen offset:1024 sc1\n" \
"v_readlane_b32 s43, v202, 11\n" \
"s_nop 4\n" \
"buffer_load_dwordx4 a[88:91], v201, s[8:11], s43 offen sc1\n" \
"buffer_load_dwordx4 a[92:95], v201, s[8:11], s43 offen offset:1024 sc1\n" \
"v_readlane_b32 s43, v202, 18\n" \
"s_nop 4\n" \
"buffer_load_dwordx4 a[144:147], v201, s[8:11], s43 offen sc1\n" \
"buffer_load_dwordx4 a[148:151], v201, s[8:11], s43 offen offset:1024 sc1\n" \
"v_readlane_b32 s43, v202, 25\n" \
"s_nop 4\n" \
"buffer_load_dwordx4 a[200:203], v201, s[8:11], s43 offen sc1\n" \
"buffer_load_dwordx4 a[204:207], v201, s[8:11], s43 offen offset:1024 sc1\n" \
"s_add_u32 s43, s30, 2\n" \
"s_mov_b32 s50, 0\n" \
"s_branch .LP1_w30c\n" \
".LP1_w30l:\n" \
"buffer_load_dword v240, v204, s[4:7], 0 offen sc1\n" \
"s_waitcnt vmcnt(0)\n" \
".LP1_w30c:\n" \
"v_subrev_u32 v241, s43, v240\n" \
"v_cmp_gt_u32 vcc, 0x100, v241\n" \
"s_cmp_eq_u64 vcc, -1\n" \
"s_cbranch_scc1 .LP1_w30d\n" \
"s_sleep 2\n" \
"s_add_u32 s50, s50, 1\n" \
"s_cmp_lt_u32 s50, 0x20000\n" \
"s_cbranch_scc1 .LP1_w30l\n" \
"s_branch .LP1_end\n" \
".LP1_w30d:\n" \
"v_readlane_b32 s43, v202, 1\n" \
"s_nop 4\n" \
"buffer_load_dwordx4 a[8:11], v201, s[8:11], s43 offen sc1\n" \
"buffer_load_dwordx4 a[12:15], v201, s[8:11], s43 offen offset:1024 sc1\n" \
"v_readlane_b32 s43, v202, 8\n" \
"s_nop 4\n" \
"buffer_load_dwordx4 a[64:67], v201, s[8:11], s43 offen sc1\n" \
"buffer_load_dwordx4 a[68:71], v201, s[8:11], s43 offen offset:1024 sc1\n" \
"v_readlane_b32 s43, v202, 15\n" \
"s_nop 4\n" \
"buffer_load_dwordx4 a[120:123], v201, s[8:11], s43 offen sc1\n" \
"buffer_load_dwordx4 a[124:127], v201, s[8:11], s43 offen offset:1024 sc1\n" \
"v_readlane_b32 s43, v202, 22\n" \
"s_nop 4\n" \
"buffer_load_dwordx4 a[176:179], v201, s[8:11], s43 offen sc1\n" \
"buffer_load_dwordx4 a[180:183], v201, s[8:11], s43 offen offset:1024 sc1\n" \
"s_add_u32 s43, s30, 3\n" \
"s_mov_b32 s50, 0\n" \
"s_branch .LP1_w31c\n" \
".LP1_w31l:\n" \
"buffer_load_dword v240, v204, s[4:7], 0 offen sc1\n" \
"s_waitcnt vmcnt(0)\n" \
".LP1_w31c:\n" \
"v_subrev_u32 v241, s43, v240\n" \
"v_cmp_gt_u32 vcc, 0x100, v241\n" \
"s_cmp_eq_u64 vcc, -1\n" \
"s_cbranch_scc1 .LP1_w31d\n" \
"s_sleep 2\n" \
"s_add_u32 s50, s50, 1\n" \
"s_cmp_lt_u32 s50, 0x20000\n" \
"s_cbranch_scc1 .LP1_w31l\n" \
"s_branch .LP1_end\n" \
".LP1_w31d:\n" \
"v_readlane_b32 s43, v202, 5\n" \
"s_nop 4\n" \
"buffer_load_dwordx4 a[40:43], v201, s[8:11], s43 offen sc1\n" \
"buffer_load_dwordx4 a[44:47], v201, s[8:11], s43 offen offset:1024 sc1\n" \
"v_readlane_b32 s43, v202, 12\n" \
"s_nop 4\n" \
"buffer_load_dwordx4 a[96:99], v201, s[8:11], s43 offen sc1\n" \
"buffer_load_dwordx4 a[100:103], v201, s[8:11], s43 offen offset:1024 sc1\n" \
"v_readlane_b32 s43, v202, 19\n" \
"s_nop 4\n" \
"buffer_load_dwordx4 a[152:155], v201, s[8:11], s43 offen sc1\n" \
"buffer_load_dwordx4 a[156:159], v201, s[8:11], s43 offen offset:1024 sc1\n" \
"v_readlane_b32 s43, v202, 26\n" \
"s_nop 4\n" \
"buffer_load_dwordx4 a[208:211], v201, s[8:11], s43 offen sc1\n" \
"buffer_load_dwordx4 a[212:215], v201, s[8:11], s43 offen offset:1024 sc1\n" \
"s_add_u32 s43, s30, 4\n" \
"s_mov_b32 s50, 0\n" \
"s_branch .LP1_w32c\n" \
".LP1_w32l:\n" \
"buffer_load_dword v240, v204, s[4:7], 0 offen sc1\n" \
"s_waitcnt vmcnt(0)\n" \
".LP1_w32c:\n" \
"v_subrev_u32 v241, s43, v240\n" \
"v_cmp_gt_u32 vcc, 0x100, v241\n" \
"s_cmp_eq_u64 vcc, -1\n" \
"s_cbranch_scc1 .LP1_w32d\n" \
"s_sleep 2\n" \
"s_add_u32 s50, s50, 1\n" \
"s_cmp_lt_u32 s50, 0x20000\n" \
"s_cbranch_scc1 .LP1_w32l\n" \
"s_branch .LP1_end\n" \
".LP1_w32d:\n" \
"v_readlane_b32 s43, v202, 2\n" \
"s_nop 4\n" \
"buffer_load_dwordx4 a[16:19], v201, s[8:11], s43 offen sc1\n" \
"buffer_load_dwordx4 a[20:23], v201, s[8:11], s43 offen offset:1024 sc1\n" \
"v_readlane_b32 s43, v202, 9\n" \
"s_nop 4\n" \
"buffer_load_dwordx4 a[72:75], v201, s[8:11], s43 offen sc1\n" \
"buffer_load_dwordx4 a[76:79], v201, s[8:11], s43 offen offset:1024 sc1\n" \
"v_readlane_b32 s43, v202, 16\n" \
"s_nop 4\n" \
"buffer_load_dwordx4 a[128:131], v201, s[8:11], s43 offen sc1\n" \
"buffer_load_dwordx4 a[132:135], v201, s[8:11], s43 offen offset:1024 sc1\n" \
"v_readlane_b32 s43, v202, 23\n" \
"s_nop 4\n" \
"buffer_load_dwordx4 a[184:187], v201, s[8:11], s43 offen sc1\n" \
"buffer_load_dwordx4 a[188:191], v201, s[8:11], s43 offen offset:1024 sc1\n" \
"s_add_u32 s43, s30, 5\n" \
"s_mov_b32 s50, 0\n" \
"s_branch .LP1_w33c\n" \
".LP1_w33l:\n" \
"buffer_load_dword v240, v204, s[4:7], 0 offen sc1\n" \
"s_waitcnt vmcnt(0)\n" \
".LP1_w33c:\n" \
"v_subrev_u32 v241, s43, v240\n" \
"v_cmp_gt_u32 vcc, 0x100, v241\n" \
"s_cmp_eq_u64 vcc, -1\n" \
"s_cbranch_scc1 .LP1_w33d\n" \
"s_sleep 2\n" \
"s_add_u32 s50, s50, 1\n" \
"s_cmp_lt_u32 s50, 0x20000\n" \
"s_cbranch_scc1 .LP1_w33l\n" \
"s_branch .LP1_end\n" \
".LP1_w33d:\n" \
"v_readlane_b32 s43, v202, 0\n" \
"s_nop 4\n" \
"buffer_load_dwordx4 a[0:3], v201, s[8:11], s43 offen sc1\n" \
"buffer_load_dwordx4 a[4:7], v201, s[8:11], s43 offen offset:1024 sc1\n" \
"v_readlane_b32 s43, v202, 7\n" \
"s_nop 4\n" \
"buffer_load_dwordx4 a[56:59], v201, s[8:11], s43 offen sc1\n" \
"buffer_load_dwordx4 a[60:63], v201, s[8:11], s43 offen offset:1024 sc1\n" \
"v_readlane_b32 s43, v202, 14\n" \
"s_nop 4\n" \
"buffer_load_dwordx4 a[112:115], v201, s[8:11], s43 offen sc1\n" \
"buffer_load_dwordx4 a[116:119], v201, s[8:11], s43 offen offset:1024 sc1\n" \
"v_readlane_b32 s43, v202, 21\n" \
"s_nop 4\n" \
"buffer_load_dwordx4 a[168:171], v201, s[8:11], s43 offen sc1\n" \
"buffer_load_dwordx4 a[172:175], v201, s[8:11], s43 offen offset:1024 sc1\n" \
"s_add_u32 s43, s30, 6\n" \
"s_mov_b32 s50, 0\n" \
"s_branch .LP1_w34c\n" \
".LP1_w34l:\n" \
"buffer_load_dword v240, v204, s[4:7], 0 offen sc1\n" \
"s_waitcnt vmcnt(0)\n" \
".LP1_w34c:\n" \
"v_subrev_u32 v241, s43, v240\n" \
"v_cmp_gt_u32 vcc, 0x100, v241\n" \
"s_cmp_eq_u64 vcc, -1\n" \
"s_cbranch_scc1 .LP1_w34d\n" \
"s_sleep 2\n" \
"s_add_u32 s50, s50, 1\n" \
"s_cmp_lt_u32 s50, 0x20000\n" \
"s_cbranch_scc1 .LP1_w34l\n" \
"s_branch .LP1_end\n" \
".LP1_w34d:\n" \
"v_readlane_b32 s43, v202, 3\n" \
"s_nop 4\n" \
"buffer_load_dwordx4 a[24:27], v201, s[8:11], s43 offen sc1\n" \
"buffer_load_dwordx4 a[28:31], v201, s[8:11], s43 offen offset:1024 sc1\n" \
"v_readlane_b32 s43, v202, 10\n" \
"s_nop 4\n" \
"buffer_load_dwordx4 a[80:83], v201, s[8:11], s43 offen sc1\n" \
"buffer_load_dwordx4 a[84:87], v201, s[8:11], s43 offen offset:1024 sc1\n" \
"v_readlane_b32 s43, v202, 17\n" \
"s_nop 4\n" \
"buffer_load_dwordx4 a[136:139], v201, s[8:11], s43 offen sc1\n" \
"buffer_load_dwordx4 a[140:143], v201, s[8:11], s43 offen offset:1024 sc1\n" \
"v_readlane_b32 s43, v202, 24\n" \
"s_nop 4\n" \
"buffer_load_dwordx4 a[192:195], v201, s[8:11], s43 offen sc1\n" \
"buffer_load_dwordx4 a[196:199], v201, s[8:11], s43 offen offset:1024 sc1\n" \
"s_add_u32 s43, s30, 7\n" \
"s_mov_b32 s50, 0\n" \
"s_branch .LP1_w35c\n" \
".LP1_w35l:\n" \
"buffer_load_dword v240, v204, s[4:7], 0 offen sc1\n" \
"s_waitcnt vmcnt(0)\n" \
".LP1_w35c:\n" \
"v_subrev_u32 v241, s43, v240\n" \
"v_cmp_gt_u32 vcc, 0x100, v241\n" \
"s_cmp_eq_u64 vcc, -1\n" \
"s_cbranch_scc1 .LP1_w35d\n" \
"s_sleep 2\n" \
"s_add_u32 s50, s50, 1\n" \
"s_cmp_lt_u32 s50, 0x20000\n" \
"s_cbranch_scc1 .LP1_w35l\n" \
"s_branch .LP1_end\n" \
".LP1_w35d:\n" \
"v_readlane_b32 s43, v202, 6\n" \
"s_nop 4\n" \
"buffer_load_dwordx4 a[48:51], v201, s[8:11], s43 offen sc1\n" \
"buffer_load_dwordx4 a[52:55], v201, s[8:11], s43 offen offset:1024 sc1\n" \
"v_readlane_b32 s43, v202, 13\n" \
"s_nop 4\n" \
"buffer_load_dwordx4 a[104:107], v201, s[8:11], s43 offen sc1\n" \
"buffer_load_dwordx4 a[108:111], v201, s[8:11], s43 offen offset:1024 sc1\n" \
"v_readlane_b32 s43, v202, 20\n" \
"s_nop 4\n" \
"buffer_load_dwordx4 a[160:163], v201, s[8:11], s43 offen sc1\n" \
"buffer_load_dwordx4 a[164:167], v201, s[8:11], s43 offen offset:1024 sc1\n" \
"v_readlane_b32 s43, v202, 27\n" \
"s_nop 4\n" \
"buffer_load_dwordx4 a[216:219], v201, s[8:11], s43 offen sc1\n" \
"buffer_load_dwordx4 a[220:223], v201, s[8:11], s43 offen offset:1024 sc1\n" \
"s_add_u32 s43, s30, 1\n" \
"s_mov_b32 s50, 0\n" \
".LP1_w36l:\n" \
"buffer_load_dword v240, v205, s[4:7], 0 offen sc1\n" \
"s_waitcnt vmcnt(0)\n" \
".LP1_w36c:\n" \
"v_subrev_u32 v241, s43, v240\n" \
"v_cmp_gt_u32 vcc, 0x100, v241\n" \
"s_cmp_eq_u64 vcc, -1\n" \
"s_cbranch_scc1 .LP1_w36d\n" \
"s_sleep 2\n" \
"s_add_u32 s50, s50, 1\n" \
"s_cmp_lt_u32 s50, 0x20000\n" \
"s_cbranch_scc1 .LP1_w36l\n" \
"s_branch .LP1_end\n" \
".LP1_w36d:\n" \
"v_readfirstlane_b32 s31, v241\n" \
"s_lshr_b32 s32, s31, 2\n" \
"s_xor_b32 s29, s31, s32\n" \
"s_and_b32 s29, s29, 3\n" \
"s_lshl_b32 s29, s29, 13\n" \
"s_bitset1_b32 s29, 21\n" \
"s_and_b32 s31, s31, 3\n" \
"s_lshl_b32 s31, s31, 13\n" \
"s_bitcmp1_b32 s39, 0\n" \
"s_cselect_b32 s43, s29, 0\n" \
"s_xor_b32 s31, s31, s43\n" \
"s_xor_b32 s32, s31, 0x4000\n" \
"s_mov_b32 s39, 0\n" \
"s_add_u32 s43, s40, 0\n" \
"s_and_b32 s43, s43, 3\n" \
"s_bitcmp1_b32 s43, 0\n" \
"s_cselect_b32 s47, s32, s31\n" \
"s_lshl_b32 s43, s43, 19\n" \
"s_add_u32 s47, s47, s43\n" \
".LP1_pre:\n" \
"v_mov_b32 v236, 1145\n" \
"v_mov_b32 v237, 930\n" \
"v_mov_b32 v238, 1145\n" \
"v_mov_b32 v239, 930\n" \
"s_cmp_eq_u32 s40, 0\n" \
"s_cselect_b32 s43, -1, 0\n" \
"v_and_b32 v236, s43, v236\n" \
"v_and_b32 v237, s43, v237\n" \
"v_and_b32 v238, s43, v238\n" \
"v_and_b32 v239, s43, v239\n" \
"s_barrier\n" \
"ds_read_b96 v[248:250], v252\n" \
"s_waitcnt lgkmcnt(0)\n" \
"v_readfirstlane_b32 s44, v248\n" \
"v_readfirstlane_b32 s45, v249\n" \
"v_readfirstlane_b32 s46, v250\n" \
"s_mul_i32 s42, s25, s46\n" \
"s_lshr_b32 s42, s42, 12\n" \
".LP1_cu:\n" \
"s_sub_u32 s43, s24, s44\n" \
"s_cmp_lt_i32 s43, 0\n" \
"s_cbranch_scc0 .LP1_cud\n" \
"s_add_u32 s24, s24, s25\n" \
"s_xor_b32 s31, s31, s29\n" \
"s_xor_b32 s32, s32, s29\n" \
"s_xor_b32 s47, s47, s29\n" \
"s_add_u32 s39, s39, 1\n" \
"s_branch .LP1_cu\n" \
".LP1_cud:\n" \
"s_mov_b32 s3, s46\n" \
"s_add_u32 s43, s40, 1\n" \
"s_and_b32 s43, s43, 3\n" \
"s_bitcmp1_b32 s43, 0\n" \
"s_cselect_b32 s48, s32, s31\n" \
"s_lshl_b32 s43, s43, 19\n" \
"s_add_u32 s48, s48, s43\n" \
"s_add_u32 s43, s40, 2\n" \
"s_and_b32 s43, s43, 3\n" \
"s_bitcmp1_b32 s43, 0\n" \
"s_cselect_b32 s49, s32, s31\n" \
"s_lshl_b32 s43, s43, 19\n" \
"s_add_u32 s49, s49, s43\n" \
"s_sub_u32 s43, s24, s44\n" \
"s_add_u32 s43, s43, s28\n" \
"s_max_i32 s43, s43, 0xfffd0000\n" \
"s_min_i32 s43, s43, 0x4000\n" \
"s_mul_i32 s43, s43, s46\n" \
"s_ashr_i32 s43, s43, 12\n" \
"s_add_u32 s41, s45, s43\n" \
"s_lshr_b32 s35, s42, 1\n" \
"s_sub_u32 s35, 0, s35\n" \
"v_max_u32 v236, v236, v238\n" \
"v_max_u32 v237, v237, v239\n" \
"v_and_b32 v236, 0xfff, v236\n" \
"v_and_b32 v237, 0xfff, v237\n" \
"s_mov_b64 exec, 1\n" \
"ds_write_b64 v252, v[236:237] offset:24\n" \
"s_mov_b64 exec, -1\n" \
".LP1_loop:\n" \
"s_branch .LP1_m1\n" \
".LP1_retry1:\n" \
"s_add_u32 s51, s51, 1\n" \
"s_cmp_gt_u32 s51, 0x20000\n" \
"s_cbranch_scc1 .LP1_end\n" \
".LP1_m0:\n" \
"s_add_u32 s41, s41, s42\n" \
"s_add_u32 s24, s24, s25\n" \
"s_xor_b32 s31, s31, s29\n" \
"s_xor_b32 s32, s32, s29\n" \
"s_xor_b32 s47, s47, s29\n" \
"s_xor_b32 s48, s48, s29\n" \
"s_add_u32 s39, s39, 1\n" \
"s_bitcmp1_b32 s31, 21\n" \
"s_cselect_b32 s43, s29, 0\n" \
"v_mov_b32 v116, s43\n" \
"ds_write_b32 v252, v116 offset:16\n" \
".LP1_m1:\n" \
"s_memtime vcc\n" \
"s_waitcnt lgkmcnt(0)\n" \
"s_sub_u32 s43, s41, vcc_lo\n" \
"s_cmp_gt_i32 s43, 0\n" \
"s_cbranch_scc1 .LP1_m1\n" \
"s_cmp_lt_i32 s43, s35\n" \
"s_cbranch_scc1 .LP1_m0\n" \
"buffer_load_dwordx4 v[0:3], v208, s[4:7], s47 offen offset:2048 sc1\n" \
"buffer_load_dwordx4 v[4:7], v209, s[4:7], s47 offen sc1\n" \
"s_nop 15\n" \
"s_nop 13\n" \
"s_mul_i32 s50, s40, 4096\n" \
"buffer_store_dwordx4 v[68:71], v218, s[12:15], s50 offen sc1\n" \
"buffer_store_dword v112, v213, s[4:7], s49 offen sc1\n" \
"s_xor_b32 s43, s49, s29\n" \
"buffer_store_dword v112, v213, s[4:7], s43 offen sc1\n" \
"s_lshl_b32 s43, s40, 7\n" \
"buffer_load_dwordx4 v[236:239], v207, s[4:7], s43 offen offset:128 sc1\n" \
"v_mov_b32 v28, 0\n" \
"v_mov_b32 v29, 0\n" \
"v_mov_b32 v30, 0\n" \
"v_mov_b32 v31, 0\n" \
"v_mov_b32 v32, 0\n" \
"v_mov_b32 v33, 0\n" \
"v_mov_b32 v34, 0\n" \
"v_mov_b32 v35, 0\n" \
"v_mov_b32 v36, 0\n" \
"v_mov_b32 v37, 0\n" \
"v_mov_b32 v38, 0\n" \
"v_mov_b32 v39, 0\n" \
"v_mov_b32 v40, 0\n" \
"v_mov_b32 v41, 0\n" \
"v_mov_b32 v42, 0\n" \
"v_mov_b32 v43, 0\n" \
"s_waitcnt vmcnt(5)\n" \
"ds_write_b128 v220, v[0:3] offset:2048\n" \
"s_waitcnt vmcnt(4)\n" \
"ds_write_b128 v220, v[4:7] offset:4096\n" \
"v_max3_u32 v44, v0, v0, v1\n" \
"v_max3_u32 v44, v44, v2, v3\n" \
"v_max3_u32 v44, v44, v4, v5\n" \
"v_max3_u32 v44, v44, v6, v7\n" \
"v_cmp_eq_u32 vcc, -1, v44\n" \
"v_smfmac_f32_16x16x64_bf16 v[28:31], v[0:3], a[0:7], v200\n" \
"v_smfmac_f32_16x16x64_bf16 v[32:35], v[0:3], a[56:63], v200\n" \
"s_cbranch_vccnz .LP1_retry1\n" \
"s_waitcnt lgkmcnt(0)\n" \
"s_barrier\n" \
"ds_read_b128 v[8:11], v220 offset:5120\n" \
"ds_read_b128 v[12:15], v220 offset:6144\n" \
"ds_read_b128 v[16:19], v220 offset:0\n" \
"ds_read_b128 v[20:23], v220 offset:1024\n" \
"ds_read_b128 v[24:27], v220 offset:3072\n" \
"ds_read_b64 v[246:247], v252 offset:24\n" \
"ds_read_b96 v[248:250], v252\n" \
"v_smfmac_f32_16x16x64_bf16 v[36:39], v[0:3], a[112:119], v200\n" \
"v_smfmac_f32_16x16x64_bf16 v[40:43], v[0:3], a[168:175], v200\n" \
"v_smfmac_f32_16x16x64_bf16 v[28:31], v[4:7], a[8:15], v200\n" \
"v_smfmac_f32_16x16x64_bf16 v[32:35], v[4:7], a[64:71], v200\n" \
"v_smfmac_f32_16x16x64_bf16 v[36:39], v[4:7], a[120:127], v200\n" \
"v_smfmac_f32_16x16x64_bf16 v[40:43], v[4:7], a[176:183], v200\n" \
"s_waitcnt lgkmcnt(6)\n" \
"v_smfmac_f32_16x16x64_bf16 v[28:31], v[8:11], a[16:23], v200\n" \
"v_smfmac_f32_16x16x64_bf16 v[32:35], v[8:11], a[72:79], v200\n" \
"v_smfmac_f32_16x16x64_bf16 v[36:39], v[8:11], a[128:135], v200\n" \
"v_smfmac_f32_16x16x64_bf16 v[40:43], v[8:11], a[184:191], v200\n" \
"s_waitcnt lgkmcnt(0)\n" \
"v_readfirstlane_b32 s44, v248\n" \
"v_readfirstlane_b32 s45, v249\n" \
"v_smfmac_f32_16x16x64_bf16 v[28:31], v[12:15], a[24:31], v200\n" \
"v_readfirstlane_b32 s46, v250\n" \
"v_readfirstlane_b32 s26, v246\n" \
"v_readfirstlane_b32 s27, v247\n" \
"v_smfmac_f32_16x16x64_bf16 v[32:35], v[12:15], a[80:87], v200\n" \
"s_mul_i32 s42, s25, s46\n" \
"s_lshr_b32 s42, s42, 12\n" \
"s_add_u32 s24, s24, s26\n" \
"v_smfmac_f32_16x16x64_bf16 v[36:39], v[12:15], a[136:143], v200\n" \
"s_sub_u32 s43, s24, s44\n" \
"s_max_i32 s43, s43, 0xfffd0000\n" \
"s_min_i32 s43, s43, 0x4000\n" \
"v_smfmac_f32_16x16x64_bf16 v[40:43], v[12:15], a[192:199], v200\n" \
"s_mul_i32 s43, s43, s46\n" \
"s_ashr_i32 s43, s43, 12\n" \
"s_add_u32 s41, s45, s43\n" \
"s_cmp_eq_u32 s40, 63\n" \
"s_cbranch_scc1 .LP1_last\n" \
"s_branch .LP1_z1\n" \
".LP1_z0:\n" \
"s_add_u32 s41, s41, s42\n" \
"s_add_u32 s24, s24, s25\n" \
"s_xor_b32 s31, s31, s29\n" \
"s_xor_b32 s32, s32, s29\n" \
"s_xor_b32 s47, s47, s29\n" \
"s_xor_b32 s48, s48, s29\n" \
"s_add_u32 s39, s39, 1\n" \
"buffer_store_dwordx4 v[112:115], v223, s[4:7], s48 offen sc1\n" \
"s_bitcmp1_b32 s31, 21\n" \
"s_cselect_b32 s43, s29, 0\n" \
"v_mov_b32 v116, s43\n" \
"ds_write_b32 v252, v116 offset:16\n" \
".LP1_z1:\n" \
"s_memtime vcc\n" \
"s_waitcnt lgkmcnt(0)\n" \
"s_sub_u32 s43, s41, vcc_lo\n" \
"s_cmp_gt_i32 s43, 200\n" \
"s_cbranch_scc1 .LP1_z1\n" \
"s_cmp_lt_i32 s43, s37\n" \
"s_cbranch_scc1 .LP1_z0\n" \
"buffer_load_dwordx2 v[64:65], v211, s[4:7], s47 offen sc1\n" \
".p2align 6\n" \
".LP1_b1:\n" \
"s_memtime vcc\n" \
"s_waitcnt lgkmcnt(0)\n" \
"s_sub_u32 s43, s41, vcc_lo\n" \
"s_cmp_gt_i32 s43, 128\n" \
"s_cbranch_scc1 .LP1_b1\n" \
"s_cmp_lt_i32 s43, s37\n" \
"s_cbranch_scc1 .LP1_z0\n" \
"s_max_i32 s43, s43, 0\n" \
"s_and_b32 s43, s43, -4\n" \
"s_getpc_b64 vcc\n" \
".LP1_b3:\n" \
"s_sub_u32 s43, .LP1_b4-.LP1_b3, s43\n" \
"s_add_u32 vcc_lo, vcc_lo, s43\n" \
"s_addc_u32 vcc_hi, vcc_hi, 0\n" \
"s_setpc_b64 vcc\n" \
".rept 32\n" \
"s_nop 0\n" \
".endr\n" \
".LP1_b4:\n" \
"buffer_load_dwordx4 v[56:59], v210, s[4:7], s47 offen sc1\n" \
"buffer_load_dwordx4 v[60:63], v210, s[4:7], s47 offen offset:256 sc1\n" \
"v_smfmac_f32_16x16x64_bf16 v[28:31], v[16:19], a[32:39], v200\n" \
"v_smfmac_f32_16x16x64_bf16 v[32:35], v[16:19], a[88:95], v200\n" \
"v_smfmac_f32_16x16x64_bf16 v[36:39], v[16:19], a[144:151], v200\n" \
"v_smfmac_f32_16x16x64_bf16 v[40:43], v[16:19], a[200:207], v200\n" \
"v_smfmac_f32_16x16x64_bf16 v[28:31], v[20:23], a[40:47], v200\n" \
"v_smfmac_f32_16x16x64_bf16 v[32:35], v[20:23], a[96:103], v200\n" \
"buffer_load_dwordx4 v[50:53], v212, s[4:7], s47 offen sc1\n" \
"v_smfmac_f32_16x16x64_bf16 v[36:39], v[20:23], a[152:159], v200\n" \
"v_smfmac_f32_16x16x64_bf16 v[40:43], v[20:23], a[208:215], v200\n" \
"v_smfmac_f32_16x16x64_bf16 v[28:31], v[24:27], a[48:55], v200\n" \
"v_smfmac_f32_16x16x64_bf16 v[32:35], v[24:27], a[104:111], v200\n" \
"v_smfmac_f32_16x16x64_bf16 v[36:39], v[24:27], a[160:167], v200\n" \
"v_smfmac_f32_16x16x64_bf16 v[40:43], v[24:27], a[216:223], v200\n" \
"s_nop 2\n" \
"v_mov_b32 v245, v245\n" \
"v_mov_b32 v245, v245\n" \
"v_mov_b32 v245, v245\n" \
"v_permlane32_swap_b32 v28, v32\n" \
"v_permlane32_swap_b32 v29, v33\n" \
"v_permlane32_swap_b32 v30, v34\n" \
"v_permlane32_swap_b32 v31, v35\n" \
"v_add_f32 v28, v28, v32\n" \
"v_add_f32 v29, v29, v33\n" \
"v_add_f32 v30, v30, v34\n" \
"v_add_f32 v31, v31, v35\n" \
"v_cvt_pk_bf16_f32 v68, v28, v29\n" \
"v_cvt_pk_bf16_f32 v69, v30, v31\n" \
"v_permlane32_swap_b32 v36, v40\n" \
"v_permlane32_swap_b32 v37, v41\n" \
"v_permlane32_swap_b32 v38, v42\n" \
"v_permlane32_swap_b32 v39, v43\n" \
"v_add_f32 v36, v36, v40\n" \
"v_add_f32 v37, v37, v41\n" \
"v_add_f32 v38, v38, v42\n" \
"v_add_f32 v39, v39, v43\n" \
"v_cvt_pk_bf16_f32 v70, v36, v37\n" \
"v_cvt_pk_bf16_f32 v71, v38, v39\n" \
"v_mov_b32 v48, v68\n" \
"v_mov_b32 v49, v69\n" \
"v_mov_b32 v54, v70\n" \
"v_mov_b32 v55, v71\n" \
"s_nop 1\n" \
"v_permlane16_swap_b32 v48, v54\n" \
"v_permlane16_swap_b32 v49, v55\n" \
".LP1_wait:\n" \
"s_waitcnt vmcnt(1)\n" \
"v_pk_add_f32 v[72:73], v[56:57], v[60:61]\n" \
"v_pk_add_f32 v[74:75], v[58:59], v[62:63]\n" \
"v_max3_f32 v77, v72, v73, v64\n" \
"v_max3_f32 v77, v77, v74, v75\n" \
"s_nop 1\n" \
"v_max_f32_dpp v76, v77, v77 row_ror:8 row_mask:0xf bank_mask:0xf\n" \
"v_pk_add_f32 v[72:73], v[72:73], v[76:77] op_sel_hi:[1,0] neg_lo:[0,1] neg_hi:[0,1]\n" \
"v_pk_add_f32 v[74:75], v[74:75], v[76:77] op_sel_hi:[1,0] neg_lo:[0,1] neg_hi:[0,1]\n" \
"v_sub_f32 v78, v64, v76\n" \
"v_exp_f32 v72, v72\n" \
"v_exp_f32 v73, v73\n" \
"v_exp_f32 v74, v74\n" \
"v_exp_f32 v75, v75\n" \
"v_exp_f32 v78, v78\n" \
"v_pk_add_f32 v[80:81], v[72:73], v[74:75]\n" \
"v_fma_f32 v80, v78, v65, v80\n" \
"v_add_f32 v81, v80, v81\n" \
"s_nop 1\n" \
"v_add_f32_dpp v80, v81, v81 row_ror:8 row_mask:0xf bank_mask:0xf\n" \
"v_rcp_f32 v80, v80\n" \
"s_nop 0\n" \
"v_mul_f32 v78, v78, v80\n" \
"v_pk_mul_f32 v[72:73], v[72:73], v[80:81] op_sel_hi:[1,0]\n" \
"v_pk_mul_f32 v[74:75], v[74:75], v[80:81] op_sel_hi:[1,0]\n" \
"v_cvt_pk_bf16_f32 v78, v78, v78\n" \
"v_cvt_pk_bf16_f32 v82, v72, v73\n" \
"v_cvt_pk_bf16_f32 v83, v74, v75\n" \
"v_and_b32 v84, v78, v214\n" \
"v_and_b32 v85, v78, v215\n" \
"v_cvt_pk_bf16_f32 v86, v72, v73\n" \
"v_cvt_pk_bf16_f32 v87, v74, v75\n" \
"s_waitcnt vmcnt(0)\n" \
"v_mfma_f32_16x16x32_bf16 v[88:91], v[48:51], v[82:85], 0\n" \
"v_mfma_f32_16x16x32_bf16 v[92:95], v[52:55], v[84:87], 0\n" \
"s_nop 2\n" \
"v_mov_b32 v245, v245\n" \
"s_nop 2\n" \
"v_mov_b32 v245, v245\n" \
"v_add_f32_dpp v96, v92, v88 row_ror:8 row_mask:0xf bank_mask:0x3\n" \
"v_add_f32_dpp v97, v93, v89 row_ror:8 row_mask:0xf bank_mask:0x3\n" \
"v_add_f32_dpp v96, v90, v94 row_ror:8 row_mask:0xf bank_mask:0xc\n" \
"v_add_f32_dpp v97, v91, v95 row_ror:8 row_mask:0xf bank_mask:0xc\n" \
"ds_write_b64 v216, v[96:97]\n" \
"v_cmp_u_f32 vcc, v96, v97\n" \
"s_bitcmp1_b32 s31, 21\n" \
"v_mov_b32 v245, v245\n" \
"s_cselect_b32 s18, s29, 0\n" \
"s_xor_b32 s18, s18, s48\n" \
"s_xor_b32 s19, s18, s29\n" \
"v_mov_b32 v245, v245\n" \
"s_nop 0\n" \
"s_nop 0\n" \
"s_nop 0\n" \
"v_mov_b32 v245, v245\n" \
"s_cbranch_vccnz .LP1_cls\n" \
".LP1_clsr:\n" \
"s_waitcnt lgkmcnt(0)\n" \
"s_barrier\n" \
"ds_read_b64 v[100:101], v217 offset:0\n" \
"v_mov_b32 v245, v245\n" \
"ds_read_b64 v[102:103], v217 offset:512\n" \
"ds_read_b32 v116, v252 offset:16\n" \
"ds_read_b64 v[104:105], v217 offset:1024\n" \
"ds_read_b64 v[106:107], v217 offset:1536\n" \
"s_add_u32 s40, s40, 1\n" \
"s_add_u32 s24, s24, s27\n" \
"v_mov_b32 v245, v245\n" \
"s_waitcnt lgkmcnt(2)\n" \
"v_xad_u32 v117, v116, s18, v213\n" \
"v_pk_add_f32 v[100:101], v[100:101], v[102:103]\n" \
"s_waitcnt lgkmcnt(0)\n" \
"v_pk_add_f32 v[104:105], v[104:105], v[106:107]\n" \
"v_pk_add_f32 v[100:101], v[100:101], v[104:105]\n" \
"v_cvt_pk_bf16_f32 v96, v100, v101\n" \
"s_mov_b32 s47, s48\n" \
"s_add_u32 s43, s40, 1\n" \
"s_and_b32 s43, s43, 3\n" \
"s_bitcmp1_b32 s43, 0\n" \
"s_cselect_b32 s48, s32, s31\n" \
"s_lshl_b32 s43, s43, 19\n" \
"s_add_u32 s48, s48, s43\n" \
"s_add_u32 s43, s40, 2\n" \
"buffer_store_dword v96, v117, s[4:7], 0 offen sc1\n" \
"s_and_b32 s43, s43, 3\n" \
"s_bitcmp1_b32 s43, 0\n" \
"s_cselect_b32 s49, s32, s31\n" \
"s_lshl_b32 s43, s43, 19\n" \
"s_add_u32 s49, s49, s43\n" \
"s_sub_u32 s43, s24, s44\n" \
"s_add_u32 s43, s43, s28\n" \
"s_max_i32 s43, s43, 0xfffd0000\n" \
"s_min_i32 s43, s43, 0x4000\n" \
"s_mul_i32 s43, s43, s46\n" \
"s_ashr_i32 s43, s43, 12\n" \
"s_add_u32 s41, s45, s43\n" \
"s_lshr_b32 s35, s42, 1\n" \
"s_sub_u32 s35, 0, s35\n" \
"v_max_u32 v236, v236, v238\n" \
"v_max_u32 v237, v237, v239\n" \
"v_and_b32 v236, 0xfff, v236\n" \
"v_and_b32 v237, 0xfff, v237\n" \
"s_mov_b64 exec, 1\n" \
"ds_write_b64 v252, v[236:237] offset:24\n" \
"s_mov_b64 exec, -1\n" \
"v_xad_u32 v116, v116, s19, v213\n" \
"buffer_store_dword v96, v116, s[4:7], 0 offen sc1\n" \
"s_cmp_eq_u32 s40, 0\n" \
"s_cbranch_scc1 .LP1_wet\n" \
"s_branch .LP1_loop\n" \
"s_branch .LP1_y1\n" \
".LP1_retry2:\n" \
"s_add_u32 s51, s51, 1\n" \
"s_cmp_gt_u32 s51, 0x20000\n" \
"s_cbranch_scc1 .LP1_end\n" \
".LP1_y0:\n" \
"s_add_u32 s41, s41, s42\n" \
"s_add_u32 s24, s24, s25\n" \
"s_xor_b32 s31, s31, s29\n" \
"s_xor_b32 s32, s32, s29\n" \
"s_xor_b32 s47, s47, s29\n" \
"s_xor_b32 s48, s48, s29\n" \
"s_add_u32 s39, s39, 1\n" \
"buffer_store_dwordx4 v[112:115], v223, s[4:7], s48 offen sc1\n" \
"s_bitcmp1_b32 s31, 21\n" \
"s_cselect_b32 s43, s29, 0\n" \
"v_mov_b32 v116, s43\n" \
"ds_write_b32 v252, v116 offset:16\n" \
".LP1_y1:\n" \
"s_memtime vcc\n" \
"s_waitcnt lgkmcnt(0)\n" \
"s_sub_u32 s43, s41, vcc_lo\n" \
"s_cmp_gt_i32 s43, 200\n" \
"s_cbranch_scc1 .LP1_y1\n" \
"s_cmp_lt_i32 s43, s37\n" \
"s_cbranch_scc1 .LP1_y0\n" \
"buffer_load_dwordx2 v[64:65], v211, s[4:7], s47 offen sc1\n" \
".p2align 6\n" \
".LP1_c1:\n" \
"s_memtime vcc\n" \
"s_waitcnt lgkmcnt(0)\n" \
"s_sub_u32 s43, s41, vcc_lo\n" \
"s_cmp_gt_i32 s43, 128\n" \
"s_cbranch_scc1 .LP1_c1\n" \
"s_cmp_lt_i32 s43, s37\n" \
"s_cbranch_scc1 .LP1_y0\n" \
"s_max_i32 s43, s43, 0\n" \
"s_and_b32 s43, s43, -4\n" \
"s_getpc_b64 vcc\n" \
".LP1_c3:\n" \
"s_sub_u32 s43, .LP1_c4-.LP1_c3, s43\n" \
"s_add_u32 vcc_lo, vcc_lo, s43\n" \
"s_addc_u32 vcc_hi, vcc_hi, 0\n" \
"s_setpc_b64 vcc\n" \
".rept 32\n" \
"s_nop 0\n" \
".endr\n" \
".LP1_c4:\n" \
"buffer_load_dwordx4 v[56:59], v210, s[4:7], s47 offen sc1\n" \
"buffer_load_dwordx4 v[60:63], v210, s[4:7], s47 offen offset:256 sc1\n" \
"s_nop 2\n" \
"v_mov_b32 v245, v245\n" \
"s_nop 2\n" \
"v_mov_b32 v245, v245\n" \
"s_nop 2\n" \
"v_mov_b32 v245, v245\n" \
"s_nop 2\n" \
"v_mov_b32 v245, v245\n" \
"s_nop 2\n" \
"v_mov_b32 v245, v245\n" \
"s_nop 2\n" \
"v_mov_b32 v245, v245\n" \
"buffer_load_dwordx4 v[50:53], v212, s[4:7], s47 offen sc1\n" \
"s_nop 2\n" \
"v_mov_b32 v245, v245\n" \
"s_nop 2\n" \
"v_mov_b32 v245, v245\n" \
"s_nop 2\n" \
"v_mov_b32 v245, v245\n" \
"s_nop 2\n" \
"v_mov_b32 v245, v245\n" \
"s_nop 2\n" \
"v_mov_b32 v245, v245\n" \
"s_nop 2\n" \
"v_mov_b32 v245, v245\n" \
"s_nop 2\n" \
"v_mov_b32 v245, v245\n" \
"s_nop 2\n" \
"v_mov_b32 v245, v245\n" \
"s_nop 2\n" \
"v_mov_b32 v245, v245\n" \
"s_nop 2\n" \
"v_mov_b32 v245, v245\n" \
"s_nop 2\n" \
"v_mov_b32 v245, v245\n" \
"s_nop 2\n" \
"v_mov_b32 v245, v245\n" \
"s_nop 2\n" \
"v_mov_b32 v245, v245\n" \
"s_nop 2\n" \
"v_mov_b32 v245, v245\n" \
"s_nop 2\n" \
"v_mov_b32 v245, v245\n" \
"s_nop 2\n" \
"v_mov_b32 v245, v245\n" \
"s_nop 2\n" \
"v_mov_b32 v245, v245\n" \
"s_branch .LP1_wait\n" \
".LP1_cls:\n" \
"s_waitcnt vmcnt(0)\n" \
"v_max3_u32 v44, v64, v64, v65\n" \
"v_max3_u32 v44, v44, v50, v51\n" \
"v_max3_u32 v44, v44, v52, v53\n" \
"v_cmp_eq_u32 vcc, -1, v44\n" \
"s_cbranch_vccnz .LP1_retry2\n" \
"v_cmp_u_f32 vcc, v56, v56\n" \
"v_cmp_ne_u32 s[52:53], v56, v109\n" \
"s_and_b64 vcc, vcc, s[52:53]\n" \
"s_cbranch_vccnz .LP1_retry2\n" \
"v_cmp_u_f32 vcc, v57, v57\n" \
"v_cmp_ne_u32 s[52:53], v57, v109\n" \
"s_and_b64 vcc, vcc, s[52:53]\n" \
"s_cbranch_vccnz .LP1_retry2\n" \
"v_cmp_u_f32 vcc, v58, v58\n" \
"v_cmp_ne_u32 s[52:53], v58, v109\n" \
"s_and_b64 vcc, vcc, s[52:53]\n" \
"s_cbranch_vccnz .LP1_retry2\n" \
"v_cmp_u_f32 vcc, v59, v59\n" \
"v_cmp_ne_u32 s[52:53], v59, v109\n" \
"s_and_b64 vcc, vcc, s[52:53]\n" \
"s_cbranch_vccnz .LP1_retry2\n" \
"v_cmp_u_f32 vcc, v60, v60\n" \
"v_cmp_ne_u32 s[52:53], v60, v109\n" \
"s_and_b64 vcc, vcc, s[52:53]\n" \
"s_cbranch_vccnz .LP1_retry2\n" \
"v_cmp_u_f32 vcc, v61, v61\n" \
"v_cmp_ne_u32 s[52:53], v61, v109\n" \
"s_and_b64 vcc, vcc, s[52:53]\n" \
"s_cbranch_vccnz .LP1_retry2\n" \
"v_cmp_u_f32 vcc, v62, v62\n" \
"v_cmp_ne_u32 s[52:53], v62, v109\n" \
"s_and_b64 vcc, vcc, s[52:53]\n" \
"s_cbranch_vccnz .LP1_retry2\n" \
"v_cmp_u_f32 vcc, v63, v63\n" \
"v_cmp_ne_u32 s[52:53], v63, v109\n" \
"s_and_b64 vcc, vcc, s[52:53]\n" \
"s_cbranch_vccnz .LP1_retry2\n" \
"s_branch .LP1_clsr\n" \
".LP1_last:\n" \
"v_smfmac_f32_16x16x64_bf16 v[28:31], v[16:19], a[32:39], v200\n" \
"v_smfmac_f32_16x16x64_bf16 v[32:35], v[16:19], a[88:95], v200\n" \
"v_smfmac_f32_16x16x64_bf16 v[36:39], v[16:19], a[144:151], v200\n" \
"v_smfmac_f32_16x16x64_bf16 v[40:43], v[16:19], a[200:207], v200\n" \
"v_smfmac_f32_16x16x64_bf16 v[28:31], v[20:23], a[40:47], v200\n" \
"v_smfmac_f32_16x16x64_bf16 v[32:35], v[20:23], a[96:103], v200\n" \
"v_smfmac_f32_16x16x64_bf16 v[36:39], v[20:23], a[152:159], v200\n" \
"v_smfmac_f32_16x16x64_bf16 v[40:43], v[20:23], a[208:215], v200\n" \
"v_smfmac_f32_16x16x64_bf16 v[28:31], v[24:27], a[48:55], v200\n" \
"v_smfmac_f32_16x16x64_bf16 v[32:35], v[24:27], a[104:111], v200\n" \
"v_smfmac_f32_16x16x64_bf16 v[36:39], v[24:27], a[160:167], v200\n" \
"v_smfmac_f32_16x16x64_bf16 v[40:43], v[24:27], a[216:223], v200\n" \
"s_waitcnt vmcnt(0)\n" \
"s_nop 7\n" \
"v_permlane32_swap_b32 v28, v32\n" \
"v_permlane32_swap_b32 v29, v33\n" \
"v_permlane32_swap_b32 v30, v34\n" \
"v_permlane32_swap_b32 v31, v35\n" \
"v_add_f32 v28, v28, v32\n" \
"v_add_f32 v29, v29, v33\n" \
"v_add_f32 v30, v30, v34\n" \
"v_add_f32 v31, v31, v35\n" \
"v_cvt_pk_bf16_f32 v68, v28, v29\n" \
"v_cvt_pk_bf16_f32 v69, v30, v31\n" \
"v_permlane32_swap_b32 v36, v40\n" \
"v_permlane32_swap_b32 v37, v41\n" \
"v_permlane32_swap_b32 v38, v42\n" \
"v_permlane32_swap_b32 v39, v43\n" \
"v_add_f32 v36, v36, v40\n" \
"v_add_f32 v37, v37, v41\n" \
"v_add_f32 v38, v38, v42\n" \
"v_add_f32 v39, v39, v43\n" \
"v_cvt_pk_bf16_f32 v70, v36, v37\n" \
"v_cvt_pk_bf16_f32 v71, v38, v39\n" \
"s_mov_b32 s50, 262144\n" \
"buffer_store_dwordx4 v[68:71], v218, s[12:15], s50 offen sc1\n" \
"s_waitcnt vmcnt(0)\n" \
"s_barrier\n" \
"s_add_u32 s43, s30, 1\n" \
"s_mov_b32 s50, 0\n" \
".LP1_w37l:\n" \
"buffer_load_dword v240, v206, s[4:7], 0 offen sc1\n" \
"s_waitcnt vmcnt(0)\n" \
".LP1_w37c:\n" \
"v_subrev_u32 v241, s43, v240\n" \
"v_cmp_gt_u32 vcc, 0x100, v241\n" \
"s_cmp_eq_u64 vcc, -1\n" \
"s_cbranch_scc1 .LP1_w37d\n" \
"s_sleep 2\n" \
"s_add_u32 s50, s50, 1\n" \
"s_cmp_lt_u32 s50, 0x20000\n" \
"s_cbranch_scc1 .LP1_w37l\n" \
"s_branch .LP1_end\n" \
".LP1_w37d:\n" \
"v_mov_b32 v241, s30\n" \
"buffer_store_dword v241, v222, s[4:7], 0 offen sc1\n" \
".LP1_end:\n" \
"s_endpgm\n" \
".p2align 6\n" \
"fk_role_P2:\n" \
"ds_read_b32 v200, v255 offset:0\n" \
"ds_read_b32 v201, v255 offset:256\n" \
"ds_read_b32 v202, v255 offset:512\n" \
"ds_read_b32 v203, v255 offset:768\n" \
"ds_read_b32 v204, v255 offset:1024\n" \
"ds_read_b32 v205, v255 offset:1280\n" \
"ds_read_b32 v206, v255 offset:1536\n" \
"ds_read_b32 v207, v255 offset:1792\n" \
"ds_read_b32 v208, v255 offset:2048\n" \
"ds_read_b32 v209, v255 offset:2304\n" \
"ds_read_b32 v210, v255 offset:2560\n" \
"ds_read_b32 v211, v255 offset:2816\n" \
"ds_read_b32 v212, v255 offset:3072\n" \
"ds_read_b32 v213, v255 offset:3328\n" \
"ds_read_b32 v214, v255 offset:3584\n" \
"ds_read_b32 v215, v255 offset:3840\n" \
"ds_read_b32 v216, v255 offset:4096\n" \
"ds_read_b32 v217, v255 offset:4352\n" \
"ds_read_b32 v218, v255 offset:4608\n" \
"ds_read_b32 v219, v255 offset:4864\n" \
"ds_read_b32 v220, v255 offset:5120\n" \
"ds_read_b32 v221, v255 offset:5376\n" \
"ds_read_b32 v222, v255 offset:5632\n" \
"ds_read_b32 v223, v255 offset:5888\n" \
"ds_read_b32 v254, v255 offset:7936\n" \
"s_waitcnt lgkmcnt(0)\n" \
"v_readlane_b32 s4, v254, 0\n" \
"v_readlane_b32 s5, v254, 1\n" \
"v_readlane_b32 s6, v254, 2\n" \
"v_readlane_b32 s7, v254, 3\n" \
"v_readlane_b32 s8, v254, 4\n" \
"v_readlane_b32 s9, v254, 5\n" \
"v_readlane_b32 s10, v254, 6\n" \
"v_readlane_b32 s11, v254, 7\n" \
"v_readlane_b32 s12, v254, 8\n" \
"v_readlane_b32 s13, v254, 9\n" \
"v_readlane_b32 s14, v254, 10\n" \
"v_readlane_b32 s15, v254, 11\n" \
"v_readlane_b32 s16, v254, 12\n" \
"v_readlane_b32 s17, v254, 13\n" \
"v_readlane_b32 s18, v254, 14\n" \
"v_readlane_b32 s19, v254, 15\n" \
"v_readlane_b32 s20, v254, 16\n" \
"v_readlane_b32 s21, v254, 17\n" \
"v_readlane_b32 s22, v254, 18\n" \
"v_readlane_b32 s23, v254, 19\n" \
"v_readlane_b32 s24, v254, 20\n" \
"v_readlane_b32 s25, v254, 21\n" \
"v_readlane_b32 s26, v254, 22\n" \
"v_readlane_b32 s27, v254, 23\n" \
"v_readlane_b32 s28, v254, 24\n" \
"v_readlane_b32 s29, v254, 25\n" \
"v_readlane_b32 s30, v254, 26\n" \
"v_readlane_b32 s31, v254, 27\n" \
"v_readlane_b32 s32, v254, 28\n" \
"v_readlane_b32 s33, v254, 29\n" \
"v_readlane_b32 s34, v254, 30\n" \
"v_readlane_b32 s35, v254, 31\n" \
"v_readlane_b32 s36, v254, 32\n" \
"v_readlane_b32 s37, v254, 33\n" \
"v_readlane_b32 s38, v254, 34\n" \
"v_readlane_b32 s39, v254, 35\n" \
"s_mov_b32 s40, 0\n" \
"s_mov_b32 s51, 0\n" \
"s_nop 0\n" \
"v_mov_b32 v252, s38\n" \
"s_nop 4\n" \
"v_mov_b32 v112, -1\n" \
"v_mov_b32 v113, -1\n" \
"v_mov_b32 v114, -1\n" \
"v_mov_b32 v115, -1\n" \
"v_mov_b32 v109, 0x7fc00001\n" \
"v_writelane_b32 v251, s6, 0\n" \
"v_writelane_b32 v251, s10, 1\n" \
"v_writelane_b32 v251, s14, 2\n" \
"v_writelane_b32 v251, s18, 3\n" \
"s_mov_b32 s6, 0\n" \
"s_mov_b32 s10, 0\n" \
"s_mov_b32 s14, 0\n" \
"s_mov_b32 s18, 0\n" \
"s_mov_b32 s40, -1\n" \
"s_branch .LP2_pre\n" \
".LP2_wet:\n" \
"s_waitcnt vmcnt(0)\n" \
"v_readlane_b32 s6, v251, 0\n" \
"v_readlane_b32 s10, v251, 1\n" \
"v_readlane_b32 s14, v251, 2\n" \
"v_readlane_b32 s18, v251, 3\n" \
"s_nop 4\n" \
"s_mov_b32 s33, 40\n" \
"s_mov_b32 s34, 0\n" \
"s_add_u32 s43, s30, 1\n" \
"s_mov_b32 s50, 0\n" \
".LP2_w38l:\n" \
"buffer_load_dword v240, v204, s[4:7], 0 offen sc1\n" \
"s_waitcnt vmcnt(0)\n" \
".LP2_w38c:\n" \
"v_subrev_u32 v241, s43, v240\n" \
"v_cmp_gt_u32 vcc, 0x100, v241\n" \
"s_cmp_eq_u64 vcc, -1\n" \
"s_cbranch_scc1 .LP2_w38d\n" \
"s_sleep 2\n" \
"s_add_u32 s50, s50, 1\n" \
"s_cmp_lt_u32 s50, 0x20000\n" \
"s_cbranch_scc1 .LP2_w38l\n" \
"s_branch .LP2_end\n" \
".LP2_w38d:\n" \
"v_readlane_b32 s43, v202, 2\n" \
"s_nop 4\n" \
"buffer_load_dwordx4 a[16:19], v201, s[8:11], s43 offen sc1\n" \
"buffer_load_dwordx4 a[20:23], v201, s[8:11], s43 offen offset:1024 sc1\n" \
"v_readlane_b32 s43, v202, 9\n" \
"s_nop 4\n" \
"buffer_load_dwordx4 a[72:75], v201, s[8:11], s43 offen sc1\n" \
"buffer_load_dwordx4 a[76:79], v201, s[8:11], s43 offen offset:1024 sc1\n" \
"v_readlane_b32 s43, v202, 16\n" \
"s_nop 4\n" \
"buffer_load_dwordx4 a[128:131], v201, s[8:11], s43 offen sc1\n" \
"buffer_load_dwordx4 a[132:135], v201, s[8:11], s43 offen offset:1024 sc1\n" \
"v_readlane_b32 s43, v202, 23\n" \
"s_nop 4\n" \
"buffer_load_dwordx4 a[184:187], v201, s[8:11], s43 offen sc1\n" \
"buffer_load_dwordx4 a[188:191], v201, s[8:11], s43 offen offset:1024 sc1\n" \
"s_add_u32 s43, s30, 2\n" \
"s_mov_b32 s50, 0\n" \
"s_branch .LP2_w39c\n" \
".LP2_w39l:\n" \
"buffer_load_dword v240, v204, s[4:7], 0 offen sc1\n" \
"s_waitcnt vmcnt(0)\n" \
".LP2_w39c:\n" \
"v_subrev_u32 v241, s43, v240\n" \
"v_cmp_gt_u32 vcc, 0x100, v241\n" \
"s_cmp_eq_u64 vcc, -1\n" \
"s_cbranch_scc1 .LP2_w39d\n" \
"s_sleep 2\n" \
"s_add_u32 s50, s50, 1\n" \
"s_cmp_lt_u32 s50, 0x20000\n" \
"s_cbranch_scc1 .LP2_w39l\n" \
"s_branch .LP2_end\n" \
".LP2_w39d:\n" \
"v_readlane_b32 s43, v202, 6\n" \
"s_nop 4\n" \
"buffer_load_dwordx4 a[48:51], v201, s[8:11], s43 offen sc1\n" \
"buffer_load_dwordx4 a[52:55], v201, s[8:11], s43 offen offset:1024 sc1\n" \
"v_readlane_b32 s43, v202, 13\n" \
"s_nop 4\n" \
"buffer_load_dwordx4 a[104:107], v201, s[8:11], s43 offen sc1\n" \
"buffer_load_dwordx4 a[108:111], v201, s[8:11], s43 offen offset:1024 sc1\n" \
"v_readlane_b32 s43, v202, 20\n" \
"s_nop 4\n" \
"buffer_load_dwordx4 a[160:163], v201, s[8:11], s43 offen sc1\n" \
"buffer_load_dwordx4 a[164:167], v201, s[8:11], s43 offen offset:1024 sc1\n" \
"v_readlane_b32 s43, v202, 27\n" \
"s_nop 4\n" \
"buffer_load_dwordx4 a[216:219], v201, s[8:11], s43 offen sc1\n" \
"buffer_load_dwordx4 a[220:223], v201, s[8:11], s43 offen offset:1024 sc1\n" \
"s_add_u32 s43, s30, 3\n" \
"s_mov_b32 s50, 0\n" \
"s_branch .LP2_w40c\n" \
".LP2_w40l:\n" \
"buffer_load_dword v240, v204, s[4:7], 0 offen sc1\n" \
"s_waitcnt vmcnt(0)\n" \
".LP2_w40c:\n" \
"v_subrev_u32 v241, s43, v240\n" \
"v_cmp_gt_u32 vcc, 0x100, v241\n" \
"s_cmp_eq_u64 vcc, -1\n" \
"s_cbranch_scc1 .LP2_w40d\n" \
"s_sleep 2\n" \
"s_add_u32 s50, s50, 1\n" \
"s_cmp_lt_u32 s50, 0x20000\n" \
"s_cbranch_scc1 .LP2_w40l\n" \
"s_branch .LP2_end\n" \
".LP2_w40d:\n" \
"v_readlane_b32 s43, v202, 3\n" \
"s_nop 4\n" \
"buffer_load_dwordx4 a[24:27], v201, s[8:11], s43 offen sc1\n" \
"buffer_load_dwordx4 a[28:31], v201, s[8:11], s43 offen offset:1024 sc1\n" \
"v_readlane_b32 s43, v202, 10\n" \
"s_nop 4\n" \
"buffer_load_dwordx4 a[80:83], v201, s[8:11], s43 offen sc1\n" \
"buffer_load_dwordx4 a[84:87], v201, s[8:11], s43 offen offset:1024 sc1\n" \
"v_readlane_b32 s43, v202, 17\n" \
"s_nop 4\n" \
"buffer_load_dwordx4 a[136:139], v201, s[8:11], s43 offen sc1\n" \
"buffer_load_dwordx4 a[140:143], v201, s[8:11], s43 offen offset:1024 sc1\n" \
"v_readlane_b32 s43, v202, 24\n" \
"s_nop 4\n" \
"buffer_load_dwordx4 a[192:195], v201, s[8:11], s43 offen sc1\n" \
"buffer_load_dwordx4 a[196:199], v201, s[8:11], s43 offen offset:1024 sc1\n" \
"s_add_u32 s43, s30, 4\n" \
"s_mov_b32 s50, 0\n" \
"s_branch .LP2_w41c\n" \
".LP2_w41l:\n" \
"buffer_load_dword v240, v204, s[4:7], 0 offen sc1\n" \
"s_waitcnt vmcnt(0)\n" \
".LP2_w41c:\n" \
"v_subrev_u32 v241, s43, v240\n" \
"v_cmp_gt_u32 vcc, 0x100, v241\n" \
"s_cmp_eq_u64 vcc, -1\n" \
"s_cbranch_scc1 .LP2_w41d\n" \
"s_sleep 2\n" \
"s_add_u32 s50, s50, 1\n" \
"s_cmp_lt_u32 s50, 0x20000\n" \
"s_cbranch_scc1 .LP2_w41l\n" \
"s_branch .LP2_end\n" \
".LP2_w41d:\n" \
"v_readlane_b32 s43, v202, 0\n" \
"s_nop 4\n" \
"buffer_load_dwordx4 a[0:3], v201, s[8:11], s43 offen sc1\n" \
"buffer_load_dwordx4 a[4:7], v201, s[8:11], s43 offen offset:1024 sc1\n" \
"v_readlane_b32 s43, v202, 7\n" \
"s_nop 4\n" \
"buffer_load_dwordx4 a[56:59], v201, s[8:11], s43 offen sc1\n" \
"buffer_load_dwordx4 a[60:63], v201, s[8:11], s43 offen offset:1024 sc1\n" \
"v_readlane_b32 s43, v202, 14\n" \
"s_nop 4\n" \
"buffer_load_dwordx4 a[112:115], v201, s[8:11], s43 offen sc1\n" \
"buffer_load_dwordx4 a[116:119], v201, s[8:11], s43 offen offset:1024 sc1\n" \
"v_readlane_b32 s43, v202, 21\n" \
"s_nop 4\n" \
"buffer_load_dwordx4 a[168:171], v201, s[8:11], s43 offen sc1\n" \
"buffer_load_dwordx4 a[172:175], v201, s[8:11], s43 offen offset:1024 sc1\n" \
"s_add_u32 s43, s30, 5\n" \
"s_mov_b32 s50, 0\n" \
"s_branch .LP2_w42c\n" \
".LP2_w42l:\n" \
"buffer_load_dword v240, v204, s[4:7], 0 offen sc1\n" \
"s_waitcnt vmcnt(0)\n" \
".LP2_w42c:\n" \
"v_subrev_u32 v241, s43, v240\n" \
"v_cmp_gt_u32 vcc, 0x100, v241\n" \
"s_cmp_eq_u64 vcc, -1\n" \
"s_cbranch_scc1 .LP2_w42d\n" \
"s_sleep 2\n" \
"s_add_u32 s50, s50, 1\n" \
"s_cmp_lt_u32 s50, 0x20000\n" \
"s_cbranch_scc1 .LP2_w42l\n" \
"s_branch .LP2_end\n" \
".LP2_w42d:\n" \
"v_readlane_b32 s43, v202, 4\n" \
"s_nop 4\n" \
"buffer_load_dwordx4 a[32:35], v201, s[8:11], s43 offen sc1\n" \
"buffer_load_dwordx4 a[36:39], v201, s[8:11], s43 offen offset:1024 sc1\n" \
"v_readlane_b32 s43, v202, 11\n" \
"s_nop 4\n" \
"buffer_load_dwordx4 a[88:91], v201, s[8:11], s43 offen sc1\n" \
"buffer_load_dwordx4 a[92:95], v201, s[8:11], s43 offen offset:1024 sc1\n" \
"v_readlane_b32 s43, v202, 18\n" \
"s_nop 4\n" \
"buffer_load_dwordx4 a[144:147], v201, s[8:11], s43 offen sc1\n" \
"buffer_load_dwordx4 a[148:151], v201, s[8:11], s43 offen offset:1024 sc1\n" \
"v_readlane_b32 s43, v202, 25\n" \
"s_nop 4\n" \
"buffer_load_dwordx4 a[200:203], v201, s[8:11], s43 offen sc1\n" \
"buffer_load_dwordx4 a[204:207], v201, s[8:11], s43 offen offset:1024 sc1\n" \
"s_add_u32 s43, s30, 6\n" \
"s_mov_b32 s50, 0\n" \
"s_branch .LP2_w43c\n" \
".LP2_w43l:\n" \
"buffer_load_dword v240, v204, s[4:7], 0 offen sc1\n" \
"s_waitcnt vmcnt(0)\n" \
".LP2_w43c:\n" \
"v_subrev_u32 v241, s43, v240\n" \
"v_cmp_gt_u32 vcc, 0x100, v241\n" \
"s_cmp_eq_u64 vcc, -1\n" \
"s_cbranch_scc1 .LP2_w43d\n" \
"s_sleep 2\n" \
"s_add_u32 s50, s50, 1\n" \
"s_cmp_lt_u32 s50, 0x20000\n" \
"s_cbranch_scc1 .LP2_w43l\n" \
"s_branch .LP2_end\n" \
".LP2_w43d:\n" \
"v_readlane_b32 s43, v202, 1\n" \
"s_nop 4\n" \
"buffer_load_dwordx4 a[8:11], v201, s[8:11], s43 offen sc1\n" \
"buffer_load_dwordx4 a[12:15], v201, s[8:11], s43 offen offset:1024 sc1\n" \
"v_readlane_b32 s43, v202, 8\n" \
"s_nop 4\n" \
"buffer_load_dwordx4 a[64:67], v201, s[8:11], s43 offen sc1\n" \
"buffer_load_dwordx4 a[68:71], v201, s[8:11], s43 offen offset:1024 sc1\n" \
"v_readlane_b32 s43, v202, 15\n" \
"s_nop 4\n" \
"buffer_load_dwordx4 a[120:123], v201, s[8:11], s43 offen sc1\n" \
"buffer_load_dwordx4 a[124:127], v201, s[8:11], s43 offen offset:1024 sc1\n" \
"v_readlane_b32 s43, v202, 22\n" \
"s_nop 4\n" \
"buffer_load_dwordx4 a[176:179], v201, s[8:11], s43 offen sc1\n" \
"buffer_load_dwordx4 a[180:183], v201, s[8:11], s43 offen offset:1024 sc1\n" \
"s_add_u32 s43, s30, 7\n" \
"s_mov_b32 s50, 0\n" \
"s_branch .LP2_w44c\n" \
".LP2_w44l:\n" \
"buffer_load_dword v240, v204, s[4:7], 0 offen sc1\n" \
"s_waitcnt vmcnt(0)\n" \
".LP2_w44c:\n" \
"v_subrev_u32 v241, s43, v240\n" \
"v_cmp_gt_u32 vcc, 0x100, v241\n" \
"s_cmp_eq_u64 vcc, -1\n" \
"s_cbranch_scc1 .LP2_w44d\n" \
"s_sleep 2\n" \
"s_add_u32 s50, s50, 1\n" \
"s_cmp_lt_u32 s50, 0x20000\n" \
"s_cbranch_scc1 .LP2_w44l\n" \
"s_branch .LP2_end\n" \
".LP2_w44d:\n" \
"v_readlane_b32 s43, v202, 5\n" \
"s_nop 4\n" \
"buffer_load_dwordx4 a[40:43], v201, s[8:11], s43 offen sc1\n" \
"buffer_load_dwordx4 a[44:47], v201, s[8:11], s43 offen offset:1024 sc1\n" \
"v_readlane_b32 s43, v202, 12\n" \
"s_nop 4\n" \
"buffer_load_dwordx4 a[96:99], v201, s[8:11], s43 offen sc1\n" \
"buffer_load_dwordx4 a[100:103], v201, s[8:11], s43 offen offset:1024 sc1\n" \
"v_readlane_b32 s43, v202, 19\n" \
"s_nop 4\n" \
"buffer_load_dwordx4 a[152:155], v201, s[8:11], s43 offen sc1\n" \
"buffer_load_dwordx4 a[156:159], v201, s[8:11], s43 offen offset:1024 sc1\n" \
"v_readlane_b32 s43, v202, 26\n" \
"s_nop 4\n" \
"buffer_load_dwordx4 a[208:211], v201, s[8:11], s43 offen sc1\n" \
"buffer_load_dwordx4 a[212:215], v201, s[8:11], s43 offen offset:1024 sc1\n" \
"s_add_u32 s43, s30, 1\n" \
"s_mov_b32 s50, 0\n" \
".LP2_w45l:\n" \
"buffer_load_dword v240, v205, s[4:7], 0 offen sc1\n" \
"s_waitcnt vmcnt(0)\n" \
".LP2_w45c:\n" \
"v_subrev_u32 v241, s43, v240\n" \
"v_cmp_gt_u32 vcc, 0x100, v241\n" \
"s_cmp_eq_u64 vcc, -1\n" \
"s_cbranch_scc1 .LP2_w45d\n" \
"s_sleep 2\n" \
"s_add_u32 s50, s50, 1\n" \
"s_cmp_lt_u32 s50, 0x20000\n" \
"s_cbranch_scc1 .LP2_w45l\n" \
"s_branch .LP2_end\n" \
".LP2_w45d:\n" \
"v_readfirstlane_b32 s31, v241\n" \
"s_lshr_b32 s32, s31, 2\n" \
"s_xor_b32 s29, s31, s32\n" \
"s_and_b32 s29, s29, 3\n" \
"s_lshl_b32 s29, s29, 13\n" \
"s_bitset1_b32 s29, 21\n" \
"s_and_b32 s31, s31, 3\n" \
"s_lshl_b32 s31, s31, 13\n" \
"s_bitcmp1_b32 s39, 0\n" \
"s_cselect_b32 s43, s29, 0\n" \
"s_xor_b32 s31, s31, s43\n" \
"s_xor_b32 s32, s31, 0x4000\n" \
"s_mov_b32 s39, 0\n" \
"s_add_u32 s43, s40, 0\n" \
"s_and_b32 s43, s43, 3\n" \
"s_bitcmp1_b32 s43, 0\n" \
"s_cselect_b32 s47, s32, s31\n" \
"s_lshl_b32 s43, s43, 19\n" \
"s_add_u32 s47, s47, s43\n" \
".LP2_pre:\n" \
"s_barrier\n" \
"ds_read_b96 v[248:250], v252\n" \
"s_waitcnt lgkmcnt(0)\n" \
"v_readfirstlane_b32 s44, v248\n" \
"v_readfirstlane_b32 s45, v249\n" \
"v_readfirstlane_b32 s46, v250\n" \
"s_mul_i32 s42, s25, s46\n" \
"s_lshr_b32 s42, s42, 12\n" \
".LP2_cu:\n" \
"s_sub_u32 s43, s24, s44\n" \
"s_cmp_lt_i32 s43, 0\n" \
"s_cbranch_scc0 .LP2_cud\n" \
"s_add_u32 s24, s24, s25\n" \
"s_xor_b32 s31, s31, s29\n" \
"s_xor_b32 s32, s32, s29\n" \
"s_xor_b32 s47, s47, s29\n" \
"s_add_u32 s39, s39, 1\n" \
"s_branch .LP2_cu\n" \
".LP2_cud:\n" \
"s_mov_b32 s3, s46\n" \
"s_add_u32 s43, s40, 1\n" \
"s_and_b32 s43, s43, 3\n" \
"s_bitcmp1_b32 s43, 0\n" \
"s_cselect_b32 s48, s32, s31\n" \
"s_lshl_b32 s43, s43, 19\n" \
"s_add_u32 s48, s48, s43\n" \
"s_add_u32 s43, s40, 2\n" \
"s_and_b32 s43, s43, 3\n" \
"s_bitcmp1_b32 s43, 0\n" \
"s_cselect_b32 s49, s32, s31\n" \
"s_lshl_b32 s43, s43, 19\n" \
"s_add_u32 s49, s49, s43\n" \
"s_sub_u32 s43, s24, s44\n" \
"s_add_u32 s43, s43, s28\n" \
"s_max_i32 s43, s43, 0xfffd0000\n" \
"s_min_i32 s43, s43, 0x4000\n" \
"s_mul_i32 s43, s43, s46\n" \
"s_ashr_i32 s43, s43, 12\n" \
"s_add_u32 s41, s45, s43\n" \
"s_lshr_b32 s35, s42, 1\n" \
"s_sub_u32 s35, 0, s35\n" \
".LP2_loop:\n" \
"s_branch .LP2_m1\n" \
".LP2_retry1:\n" \
"s_add_u32 s51, s51, 1\n" \
"s_cmp_gt_u32 s51, 0x20000\n" \
"s_cbranch_scc1 .LP2_end\n" \
".LP2_m0:\n" \
"s_add_u32 s41, s41, s42\n" \
"s_add_u32 s24, s24, s25\n" \
"s_xor_b32 s31, s31, s29\n" \
"s_xor_b32 s32, s32, s29\n" \
"s_xor_b32 s47, s47, s29\n" \
"s_xor_b32 s48, s48, s29\n" \
"s_add_u32 s39, s39, 1\n" \
"s_bitcmp1_b32 s31, 21\n" \
"s_cselect_b32 s43, s29, 0\n" \
"v_mov_b32 v116, s43\n" \
"ds_write_b32 v252, v116 offset:16\n" \
".LP2_m1:\n" \
"s_memtime vcc\n" \
"s_waitcnt lgkmcnt(0)\n" \
"s_sub_u32 s43, s41, vcc_lo\n" \
"s_cmp_gt_i32 s43, 0\n" \
"s_cbranch_scc1 .LP2_m1\n" \
"s_cmp_lt_i32 s43, s35\n" \
"s_cbranch_scc1 .LP2_m0\n" \
"buffer_load_dwordx4 v[0:3], v209, s[4:7], s47 offen offset:1024 sc1\n" \
"buffer_load_dwordx4 v[4:7], v209, s[4:7], s47 offen offset:2048 sc1\n" \
"s_cmp_lg_u32 s39, s34\n" \
"s_cselect_b32 s43, 24, -3\n" \
"s_add_u32 s33, s33, s43\n" \
"s_max_i32 s33, s33, 0\n" \
"s_min_i32 s33, s33, 60\n" \
"s_mov_b32 s34, s39\n" \
"s_add_u32 s3, s3, 6\n" \
"s_min_u32 s3, s3, s46\n" \
"s_sub_u32 s43, 6042, s3\n" \
"s_mov_b32 s50, s33\n" \
"s_mul_i32 s36, s43, 185\n" \
"s_ashr_i32 s36, s36, 10\n" \
"s_add_u32 s36, s36, s50\n" \
"s_add_u32 s36, s36, 1045\n" \
"v_mov_b32 v110, s36\n" \
"s_mul_i32 s36, s43, 148\n" \
"s_ashr_i32 s36, s36, 10\n" \
"s_add_u32 s36, s36, s50\n" \
"s_add_u32 s36, s36, 830\n" \
"v_mov_b32 v111, s36\n" \
"s_nop 15\n" \
"s_nop 13\n" \
"s_mul_i32 s50, s40, 4096\n" \
"buffer_store_dwordx4 v[68:71], v218, s[12:15], s50 offen sc1\n" \
"buffer_store_dword v112, v213, s[4:7], s49 offen sc1\n" \
"s_xor_b32 s43, s49, s29\n" \
"buffer_store_dword v112, v213, s[4:7], s43 offen sc1\n" \
"s_lshl_b32 s43, s40, 7\n" \
"buffer_store_dwordx2 v[110:111], v222, s[4:7], s43 offen offset:384 sc1\n" \
"v_mov_b32 v28, 0\n" \
"v_mov_b32 v29, 0\n" \
"v_mov_b32 v30, 0\n" \
"v_mov_b32 v31, 0\n" \
"v_mov_b32 v32, 0\n" \
"v_mov_b32 v33, 0\n" \
"v_mov_b32 v34, 0\n" \
"v_mov_b32 v35, 0\n" \
"v_mov_b32 v36, 0\n" \
"v_mov_b32 v37, 0\n" \
"v_mov_b32 v38, 0\n" \
"v_mov_b32 v39, 0\n" \
"v_mov_b32 v40, 0\n" \
"v_mov_b32 v41, 0\n" \
"v_mov_b32 v42, 0\n" \
"v_mov_b32 v43, 0\n" \
"s_waitcnt vmcnt(5)\n" \
"ds_write_b128 v220, v[0:3] offset:5120\n" \
"s_waitcnt vmcnt(4)\n" \
"ds_write_b128 v220, v[4:7] offset:6144\n" \
"v_max3_u32 v44, v0, v0, v1\n" \
"v_max3_u32 v44, v44, v2, v3\n" \
"v_max3_u32 v44, v44, v4, v5\n" \
"v_max3_u32 v44, v44, v6, v7\n" \
"v_cmp_eq_u32 vcc, -1, v44\n" \
"v_smfmac_f32_16x16x64_bf16 v[28:31], v[0:3], a[0:7], v200\n" \
"v_smfmac_f32_16x16x64_bf16 v[32:35], v[0:3], a[56:63], v200\n" \
"s_cbranch_vccnz .LP2_retry1\n" \
"s_waitcnt lgkmcnt(0)\n" \
"s_barrier\n" \
"ds_read_b128 v[8:11], v220 offset:0\n" \
"ds_read_b128 v[12:15], v220 offset:1024\n" \
"ds_read_b128 v[16:19], v220 offset:2048\n" \
"ds_read_b128 v[20:23], v220 offset:3072\n" \
"ds_read_b128 v[24:27], v220 offset:4096\n" \
"ds_read_b64 v[246:247], v252 offset:24\n" \
"ds_read_b96 v[248:250], v252\n" \
"v_smfmac_f32_16x16x64_bf16 v[36:39], v[0:3], a[112:119], v200\n" \
"v_smfmac_f32_16x16x64_bf16 v[40:43], v[0:3], a[168:175], v200\n" \
"v_smfmac_f32_16x16x64_bf16 v[28:31], v[4:7], a[8:15], v200\n" \
"v_smfmac_f32_16x16x64_bf16 v[32:35], v[4:7], a[64:71], v200\n" \
"v_smfmac_f32_16x16x64_bf16 v[36:39], v[4:7], a[120:127], v200\n" \
"v_smfmac_f32_16x16x64_bf16 v[40:43], v[4:7], a[176:183], v200\n" \
"s_waitcnt lgkmcnt(6)\n" \
"v_smfmac_f32_16x16x64_bf16 v[28:31], v[8:11], a[16:23], v200\n" \
"v_smfmac_f32_16x16x64_bf16 v[32:35], v[8:11], a[72:79], v200\n" \
"v_smfmac_f32_16x16x64_bf16 v[36:39], v[8:11], a[128:135], v200\n" \
"v_smfmac_f32_16x16x64_bf16 v[40:43], v[8:11], a[184:191], v200\n" \
"s_waitcnt lgkmcnt(0)\n" \
"v_readfirstlane_b32 s44, v248\n" \
"v_readfirstlane_b32 s45, v249\n" \
"v_smfmac_f32_16x16x64_bf16 v[28:31], v[12:15], a[24:31], v200\n" \
"v_readfirstlane_b32 s46, v250\n" \
"v_readfirstlane_b32 s26, v246\n" \
"v_readfirstlane_b32 s27, v247\n" \
"v_smfmac_f32_16x16x64_bf16 v[32:35], v[12:15], a[80:87], v200\n" \
"s_mul_i32 s42, s25, s46\n" \
"s_lshr_b32 s42, s42, 12\n" \
"s_add_u32 s24, s24, s26\n" \
"v_smfmac_f32_16x16x64_bf16 v[36:39], v[12:15], a[136:143], v200\n" \
"s_sub_u32 s43, s24, s44\n" \
"s_max_i32 s43, s43, 0xfffd0000\n" \
"s_min_i32 s43, s43, 0x4000\n" \
"v_smfmac_f32_16x16x64_bf16 v[40:43], v[12:15], a[192:199], v200\n" \
"s_mul_i32 s43, s43, s46\n" \
"s_ashr_i32 s43, s43, 12\n" \
"s_add_u32 s41, s45, s43\n" \
"s_cmp_eq_u32 s40, 63\n" \
"s_cbranch_scc1 .LP2_last\n" \
"s_branch .LP2_z1\n" \
".LP2_z0:\n" \
"s_add_u32 s41, s41, s42\n" \
"s_add_u32 s24, s24, s25\n" \
"s_xor_b32 s31, s31, s29\n" \
"s_xor_b32 s32, s32, s29\n" \
"s_xor_b32 s47, s47, s29\n" \
"s_xor_b32 s48, s48, s29\n" \
"s_add_u32 s39, s39, 1\n" \
"buffer_store_dwordx4 v[112:115], v223, s[4:7], s48 offen sc1\n" \
"s_bitcmp1_b32 s31, 21\n" \
"s_cselect_b32 s43, s29, 0\n" \
"v_mov_b32 v116, s43\n" \
"ds_write_b32 v252, v116 offset:16\n" \
".LP2_z1:\n" \
"s_memtime vcc\n" \
"s_waitcnt lgkmcnt(0)\n" \
"s_sub_u32 s43, s41, vcc_lo\n" \
"s_cmp_gt_i32 s43, 200\n" \
"s_cbranch_scc1 .LP2_z1\n" \
"s_cmp_lt_i32 s43, s37\n" \
"s_cbranch_scc1 .LP2_z0\n" \
"buffer_load_dwordx2 v[64:65], v211, s[4:7], s47 offen sc1\n" \
".p2align 6\n" \
".LP2_b1:\n" \
"s_memtime vcc\n" \
"s_waitcnt lgkmcnt(0)\n" \
"s_sub_u32 s43, s41, vcc_lo\n" \
"s_cmp_gt_i32 s43, 128\n" \
"s_cbranch_scc1 .LP2_b1\n" \
"s_cmp_lt_i32 s43, s37\n" \
"s_cbranch_scc1 .LP2_z0\n" \
"s_max_i32 s43, s43, 0\n" \
"s_and_b32 s43, s43, -4\n" \
"s_getpc_b64 vcc\n" \
".LP2_b3:\n" \
"s_sub_u32 s43, .LP2_b4-.LP2_b3, s43\n" \
"s_add_u32 vcc_lo, vcc_lo, s43\n" \
"s_addc_u32 vcc_hi, vcc_hi, 0\n" \
"s_setpc_b64 vcc\n" \
".rept 32\n" \
"s_nop 0\n" \
".endr\n" \
".LP2_b4:\n" \
"buffer_load_dwordx4 v[56:59], v210, s[4:7], s47 offen sc1\n" \
"buffer_load_dwordx4 v[60:63], v210, s[4:7], s47 offen offset:256 sc1\n" \
"v_smfmac_f32_16x16x64_bf16 v[28:31], v[16:19], a[32:39], v200\n" \
"v_smfmac_f32_16x16x64_bf16 v[32:35], v[16:19], a[88:95], v200\n" \
"v_smfmac_f32_16x16x64_bf16 v[36:39], v[16:19], a[144:151], v200\n" \
"v_smfmac_f32_16x16x64_bf16 v[40:43], v[16:19], a[200:207], v200\n" \
"v_smfmac_f32_16x16x64_bf16 v[28:31], v[20:23], a[40:47], v200\n" \
"v_smfmac_f32_16x16x64_bf16 v[32:35], v[20:23], a[96:103], v200\n" \
"buffer_load_dwordx4 v[50:53], v212, s[4:7], s47 offen sc1\n" \
"v_smfmac_f32_16x16x64_bf16 v[36:39], v[20:23], a[152:159], v200\n" \
"v_smfmac_f32_16x16x64_bf16 v[40:43], v[20:23], a[208:215], v200\n" \
"v_smfmac_f32_16x16x64_bf16 v[28:31], v[24:27], a[48:55], v200\n" \
"v_smfmac_f32_16x16x64_bf16 v[32:35], v[24:27], a[104:111], v200\n" \
"v_smfmac_f32_16x16x64_bf16 v[36:39], v[24:27], a[160:167], v200\n" \
"v_smfmac_f32_16x16x64_bf16 v[40:43], v[24:27], a[216:223], v200\n" \
"s_nop 2\n" \
"v_mov_b32 v245, v245\n" \
"v_mov_b32 v245, v245\n" \
"v_mov_b32 v245, v245\n" \
"v_permlane32_swap_b32 v28, v32\n" \
"v_permlane32_swap_b32 v29, v33\n" \
"v_permlane32_swap_b32 v30, v34\n" \
"v_permlane32_swap_b32 v31, v35\n" \
"v_add_f32 v28, v28, v32\n" \
"v_add_f32 v29, v29, v33\n" \
"v_add_f32 v30, v30, v34\n" \
"v_add_f32 v31, v31, v35\n" \
"v_cvt_pk_bf16_f32 v68, v28, v29\n" \
"v_cvt_pk_bf16_f32 v69, v30, v31\n" \
"v_permlane32_swap_b32 v36, v40\n" \
"v_permlane32_swap_b32 v37, v41\n" \
"v_permlane32_swap_b32 v38, v42\n" \
"v_permlane32_swap_b32 v39, v43\n" \
"v_add_f32 v36, v36, v40\n" \
"v_add_f32 v37, v37, v41\n" \
"v_add_f32 v38, v38, v42\n" \
"v_add_f32 v39, v39, v43\n" \
"v_cvt_pk_bf16_f32 v70, v36, v37\n" \
"v_cvt_pk_bf16_f32 v71, v38, v39\n" \
"v_mov_b32 v48, v68\n" \
"v_mov_b32 v49, v69\n" \
"v_mov_b32 v54, v70\n" \
"v_mov_b32 v55, v71\n" \
"s_nop 1\n" \
"v_permlane16_swap_b32 v48, v54\n" \
"v_permlane16_swap_b32 v49, v55\n" \
".LP2_wait:\n" \
"s_waitcnt vmcnt(1)\n" \
"v_pk_add_f32 v[72:73], v[56:57], v[60:61]\n" \
"v_pk_add_f32 v[74:75], v[58:59], v[62:63]\n" \
"v_max3_f32 v77, v72, v73, v64\n" \
"v_max3_f32 v77, v77, v74, v75\n" \
"s_nop 1\n" \
"v_max_f32_dpp v76, v77, v77 row_ror:8 row_mask:0xf bank_mask:0xf\n" \
"v_pk_add_f32 v[72:73], v[72:73], v[76:77] op_sel_hi:[1,0] neg_lo:[0,1] neg_hi:[0,1]\n" \
"v_pk_add_f32 v[74:75], v[74:75], v[76:77] op_sel_hi:[1,0] neg_lo:[0,1] neg_hi:[0,1]\n" \
"v_sub_f32 v78, v64, v76\n" \
"v_exp_f32 v72, v72\n" \
"v_exp_f32 v73, v73\n" \
"v_exp_f32 v74, v74\n" \
"v_exp_f32 v75, v75\n" \
"v_exp_f32 v78, v78\n" \
"v_pk_add_f32 v[80:81], v[72:73], v[74:75]\n" \
"v_fma_f32 v80, v78, v65, v80\n" \
"v_add_f32 v81, v80, v81\n" \
"s_nop 1\n" \
"v_add_f32_dpp v80, v81, v81 row_ror:8 row_mask:0xf bank_mask:0xf\n" \
"v_rcp_f32 v80, v80\n" \
"s_nop 0\n" \
"v_mul_f32 v78, v78, v80\n" \
"v_pk_mul_f32 v[72:73], v[72:73], v[80:81] op_sel_hi:[1,0]\n" \
"v_pk_mul_f32 v[74:75], v[74:75], v[80:81] op_sel_hi:[1,0]\n" \
"v_cvt_pk_bf16_f32 v78, v78, v78\n" \
"v_cvt_pk_bf16_f32 v82, v72, v73\n" \
"v_cvt_pk_bf16_f32 v83, v74, v75\n" \
"v_and_b32 v84, v78, v214\n" \
"v_and_b32 v85, v78, v215\n" \
"v_cvt_pk_bf16_f32 v86, v72, v73\n" \
"v_cvt_pk_bf16_f32 v87, v74, v75\n" \
"s_waitcnt vmcnt(0)\n" \
"v_mfma_f32_16x16x32_bf16 v[88:91], v[48:51], v[82:85], 0\n" \
"v_mfma_f32_16x16x32_bf16 v[92:95], v[52:55], v[84:87], 0\n" \
"s_nop 2\n" \
"v_mov_b32 v245, v245\n" \
"s_nop 2\n" \
"v_mov_b32 v245, v245\n" \
"v_add_f32_dpp v96, v92, v88 row_ror:8 row_mask:0xf bank_mask:0x3\n" \
"v_add_f32_dpp v97, v93, v89 row_ror:8 row_mask:0xf bank_mask:0x3\n" \
"v_add_f32_dpp v96, v90, v94 row_ror:8 row_mask:0xf bank_mask:0xc\n" \
"v_add_f32_dpp v97, v91, v95 row_ror:8 row_mask:0xf bank_mask:0xc\n" \
"ds_write_b64 v216, v[96:97]\n" \
"v_cmp_u_f32 vcc, v96, v97\n" \
"s_bitcmp1_b32 s31, 21\n" \
"v_mov_b32 v245, v245\n" \
"s_cselect_b32 s18, s29, 0\n" \
"s_xor_b32 s18, s18, s48\n" \
"s_xor_b32 s19, s18, s29\n" \
"v_mov_b32 v245, v245\n" \
"s_nop 0\n" \
"s_nop 0\n" \
"s_nop 0\n" \
"v_mov_b32 v245, v245\n" \
"s_cbranch_vccnz .LP2_cls\n" \
".LP2_clsr:\n" \
"s_waitcnt lgkmcnt(0)\n" \
"s_barrier\n" \
"ds_read_b64 v[100:101], v217 offset:0\n" \
"v_mov_b32 v245, v245\n" \
"ds_read_b64 v[102:103], v217 offset:512\n" \
"ds_read_b32 v116, v252 offset:16\n" \
"ds_read_b64 v[104:105], v217 offset:1024\n" \
"ds_read_b64 v[106:107], v217 offset:1536\n" \
"s_add_u32 s40, s40, 1\n" \
"s_add_u32 s24, s24, s27\n" \
"v_mov_b32 v245, v245\n" \
"s_waitcnt lgkmcnt(2)\n" \
"v_xad_u32 v117, v116, s18, v213\n" \
"v_pk_add_f32 v[100:101], v[100:101], v[102:103]\n" \
"s_waitcnt lgkmcnt(0)\n" \
"v_pk_add_f32 v[104:105], v[104:105], v[106:107]\n" \
"v_pk_add_f32 v[100:101], v[100:101], v[104:105]\n" \
"v_cvt_pk_bf16_f32 v96, v100, v101\n" \
"s_mov_b32 s47, s48\n" \
"s_add_u32 s43, s40, 1\n" \
"s_and_b32 s43, s43, 3\n" \
"s_bitcmp1_b32 s43, 0\n" \
"s_cselect_b32 s48, s32, s31\n" \
"s_lshl_b32 s43, s43, 19\n" \
"s_add_u32 s48, s48, s43\n" \
"s_add_u32 s43, s40, 2\n" \
"buffer_store_dword v96, v117, s[4:7], 0 offen sc1\n" \
"s_and_b32 s43, s43, 3\n" \
"s_bitcmp1_b32 s43, 0\n" \
"s_cselect_b32 s49, s32, s31\n" \
"s_lshl_b32 s43, s43, 19\n" \
"s_add_u32 s49, s49, s43\n" \
"s_sub_u32 s43, s24, s44\n" \
"s_add_u32 s43, s43, s28\n" \
"s_max_i32 s43, s43, 0xfffd0000\n" \
"s_min_i32 s43, s43, 0x4000\n" \
"s_mul_i32 s43, s43, s46\n" \
"s_ashr_i32 s43, s43, 12\n" \
"s_add_u32 s41, s45, s43\n" \
"s_lshr_b32 s35, s42, 1\n" \
"s_sub_u32 s35, 0, s35\n" \
"s_nop 6\n" \
"v_xad_u32 v116, v116, s19, v213\n" \
"buffer_store_dword v96, v116, s[4:7], 0 offen sc1\n" \
"s_cmp_eq_u32 s40, 0\n" \
"s_cbranch_scc1 .LP2_wet\n" \
"s_branch .LP2_loop\n" \
"s_branch .LP2_y1\n" \
".LP2_retry2:\n" \
"s_add_u32 s51, s51, 1\n" \
"s_cmp_gt_u32 s51, 0x20000\n" \
"s_cbranch_scc1 .LP2_end\n" \
".LP2_y0:\n" \
"s_add_u32 s41, s41, s42\n" \
"s_add_u32 s24, s24, s25\n" \
"s_xor_b32 s31, s31, s29\n" \
"s_xor_b32 s32, s32, s29\n" \
"s_xor_b32 s47, s47, s29\n" \
"s_xor_b32 s48, s48, s29\n" \
"s_add_u32 s39, s39, 1\n" \
"buffer_store_dwordx4 v[112:115], v223, s[4:7], s48 offen sc1\n" \
"s_bitcmp1_b32 s31, 21\n" \
"s_cselect_b32 s43, s29, 0\n" \
"v_mov_b32 v116, s43\n" \
"ds_write_b32 v252, v116 offset:16\n" \
".LP2_y1:\n" \
"s_memtime vcc\n" \
"s_waitcnt lgkmcnt(0)\n" \
"s_sub_u32 s43, s41, vcc_lo\n" \
"s_cmp_gt_i32 s43, 200\n" \
"s_cbranch_scc1 .LP2_y1\n" \
"s_cmp_lt_i32 s43, s37\n" \
"s_cbranch_scc1 .LP2_y0\n" \
"buffer_load_dwordx2 v[64:65], v211, s[4:7], s47 offen sc1\n" \
".p2align 6\n" \
".LP2_c1:\n" \
"s_memtime vcc\n" \
"s_waitcnt lgkmcnt(0)\n" \
"s_sub_u32 s43, s41, vcc_lo\n" \
"s_cmp_gt_i32 s43, 128\n" \
"s_cbranch_scc1 .LP2_c1\n" \
"s_cmp_lt_i32 s43, s37\n" \
"s_cbranch_scc1 .LP2_y0\n" \
"s_max_i32 s43, s43, 0\n" \
"s_and_b32 s43, s43, -4\n" \
"s_getpc_b64 vcc\n" \
".LP2_c3:\n" \
"s_sub_u32 s43, .LP2_c4-.LP2_c3, s43\n" \
"s_add_u32 vcc_lo, vcc_lo, s43\n" \
"s_addc_u32 vcc_hi, vcc_hi, 0\n" \
"s_setpc_b64 vcc\n" \
".rept 32\n" \
"s_nop 0\n" \
".endr\n" \
".LP2_c4:\n" \
"buffer_load_dwordx4 v[56:59], v210, s[4:7], s47 offen sc1\n" \
"buffer_load_dwordx4 v[60:63], v210, s[4:7], s47 offen offset:256 sc1\n" \
"s_nop 2\n" \
"v_mov_b32 v245, v245\n" \
"s_nop 2\n" \
"v_mov_b32 v245, v245\n" \
"s_nop 2\n" \
"v_mov_b32 v245, v245\n" \
"s_nop 2\n" \
"v_mov_b32 v245, v245\n" \
"s_nop 2\n" \
"v_mov_b32 v245, v245\n" \
"s_nop 2\n" \
"v_mov_b32 v245, v245\n" \
"buffer_load_dwordx4 v[50:53], v212, s[4:7], s47 offen sc1\n" \
"s_nop 2\n" \
"v_mov_b32 v245, v245\n" \
"s_nop 2\n" \
"v_mov_b32 v245, v245\n" \
"s_nop 2\n" \
"v_mov_b32 v245, v245\n" \
"s_nop 2\n" \
"v_mov_b32 v245, v245\n" \
"s_nop 2\n" \
"v_mov_b32 v245, v245\n" \
"s_nop 2\n" \
"v_mov_b32 v245, v245\n" \
"s_nop 2\n" \
"v_mov_b32 v245, v245\n" \
"s_nop 2\n" \
"v_mov_b32 v245, v245\n" \
"s_nop 2\n" \
"v_mov_b32 v245, v245\n" \
"s_nop 2\n" \
"v_mov_b32 v245, v245\n" \
"s_nop 2\n" \
"v_mov_b32 v245, v245\n" \
"s_nop 2\n" \
"v_mov_b32 v245, v245\n" \
"s_nop 2\n" \
"v_mov_b32 v245, v245\n" \
"s_nop 2\n" \
"v_mov_b32 v245, v245\n" \
"s_nop 2\n" \
"v_mov_b32 v245, v245\n" \
"s_nop 2\n" \
"v_mov_b32 v245, v245\n" \
"s_nop 2\n" \
"v_mov_b32 v245, v245\n" \
"s_branch .LP2_wait\n" \
".LP2_cls:\n" \
"s_waitcnt vmcnt(0)\n" \
"v_max3_u32 v44, v64, v64, v65\n" \
"v_max3_u32 v44, v44, v50, v51\n" \
"v_max3_u32 v44, v44, v52, v53\n" \
"v_cmp_eq_u32 vcc, -1, v44\n" \
"s_cbranch_vccnz .LP2_retry2\n" \
"v_cmp_u_f32 vcc, v56, v56\n" \
"v_cmp_ne_u32 s[52:53], v56, v109\n" \
"s_and_b64 vcc, vcc, s[52:53]\n" \
"s_cbranch_vccnz .LP2_retry2\n" \
"v_cmp_u_f32 vcc, v57, v57\n" \
"v_cmp_ne_u32 s[52:53], v57, v109\n" \
"s_and_b64 vcc, vcc, s[52:53]\n" \
"s_cbranch_vccnz .LP2_retry2\n" \
"v_cmp_u_f32 vcc, v58, v58\n" \
"v_cmp_ne_u32 s[52:53], v58, v109\n" \
"s_and_b64 vcc, vcc, s[52:53]\n" \
"s_cbranch_vccnz .LP2_retry2\n" \
"v_cmp_u_f32 vcc, v59, v59\n" \
"v_cmp_ne_u32 s[52:53], v59, v109\n" \
"s_and_b64 vcc, vcc, s[52:53]\n" \
"s_cbranch_vccnz .LP2_retry2\n" \
"v_cmp_u_f32 vcc, v60, v60\n" \
"v_cmp_ne_u32 s[52:53], v60, v109\n" \
"s_and_b64 vcc, vcc, s[52:53]\n" \
"s_cbranch_vccnz .LP2_retry2\n" \
"v_cmp_u_f32 vcc, v61, v61\n" \
"v_cmp_ne_u32 s[52:53], v61, v109\n" \
"s_and_b64 vcc, vcc, s[52:53]\n" \
"s_cbranch_vccnz .LP2_retry2\n" \
"v_cmp_u_f32 vcc, v62, v62\n" \
"v_cmp_ne_u32 s[52:53], v62, v109\n" \
"s_and_b64 vcc, vcc, s[52:53]\n" \
"s_cbranch_vccnz .LP2_retry2\n" \
"v_cmp_u_f32 vcc, v63, v63\n" \
"v_cmp_ne_u32 s[52:53], v63, v109\n" \
"s_and_b64 vcc, vcc, s[52:53]\n" \
"s_cbranch_vccnz .LP2_retry2\n" \
"s_branch .LP2_clsr\n" \
".LP2_last:\n" \
"v_smfmac_f32_16x16x64_bf16 v[28:31], v[16:19], a[32:39], v200\n" \
"v_smfmac_f32_16x16x64_bf16 v[32:35], v[16:19], a[88:95], v200\n" \
"v_smfmac_f32_16x16x64_bf16 v[36:39], v[16:19], a[144:151], v200\n" \
"v_smfmac_f32_16x16x64_bf16 v[40:43], v[16:19], a[200:207], v200\n" \
"v_smfmac_f32_16x16x64_bf16 v[28:31], v[20:23], a[40:47], v200\n" \
"v_smfmac_f32_16x16x64_bf16 v[32:35], v[20:23], a[96:103], v200\n" \
"v_smfmac_f32_16x16x64_bf16 v[36:39], v[20:23], a[152:159], v200\n" \
"v_smfmac_f32_16x16x64_bf16 v[40:43], v[20:23], a[208:215], v200\n" \
"v_smfmac_f32_16x16x64_bf16 v[28:31], v[24:27], a[48:55], v200\n" \
"v_smfmac_f32_16x16x64_bf16 v[32:35], v[24:27], a[104:111], v200\n" \
"v_smfmac_f32_16x16x64_bf16 v[36:39], v[24:27], a[160:167], v200\n" \
"v_smfmac_f32_16x16x64_bf16 v[40:43], v[24:27], a[216:223], v200\n" \
"s_waitcnt vmcnt(0)\n" \
"s_nop 7\n" \
"v_permlane32_swap_b32 v28, v32\n" \
"v_permlane32_swap_b32 v29, v33\n" \
"v_permlane32_swap_b32 v30, v34\n" \
"v_permlane32_swap_b32 v31, v35\n" \
"v_add_f32 v28, v28, v32\n" \
"v_add_f32 v29, v29, v33\n" \
"v_add_f32 v30, v30, v34\n" \
"v_add_f32 v31, v31, v35\n" \
"v_cvt_pk_bf16_f32 v68, v28, v29\n" \
"v_cvt_pk_bf16_f32 v69, v30, v31\n" \
"v_permlane32_swap_b32 v36, v40\n" \
"v_permlane32_swap_b32 v37, v41\n" \
"v_permlane32_swap_b32 v38, v42\n" \
"v_permlane32_swap_b32 v39, v43\n" \
"v_add_f32 v36, v36, v40\n" \
"v_add_f32 v37, v37, v41\n" \
"v_add_f32 v38, v38, v42\n" \
"v_add_f32 v39, v39, v43\n" \
"v_cvt_pk_bf16_f32 v70, v36, v37\n" \
"v_cvt_pk_bf16_f32 v71, v38, v39\n" \
"s_mov_b32 s50, 262144\n" \
"buffer_store_dwordx4 v[68:71], v218, s[12:15], s50 offen sc1\n" \
"s_waitcnt vmcnt(0)\n" \
"s_barrier\n" \
".LP2_end:\n" \
"s_endpgm\n" \
".p2align 6\n" \
"fk_role_P3:\n" \
"ds_read_b32 v200, v255 offset:0\n" \
"ds_read_b32 v201, v255 offset:256\n" \
"ds_read_b32 v202, v255 offset:512\n" \
"ds_read_b32 v203, v255 offset:768\n" \
"ds_read_b32 v204, v255 offset:1024\n" \
"ds_read_b32 v205, v255 offset:1280\n" \
"ds_read_b32 v206, v255 offset:1536\n" \
"ds_read_b32 v207, v255 offset:1792\n" \
"ds_read_b32 v208, v255 offset:2048\n" \
"ds_read_b32 v209, v255 offset:2304\n" \
"ds_read_b32 v210, v255 offset:2560\n" \
"ds_read_b32 v211, v255 offset:2816\n" \
"ds_read_b32 v212, v255 offset:3072\n" \
"ds_read_b32 v213, v255 offset:3328\n" \
"ds_read_b32 v214, v255 offset:3584\n" \
"ds_read_b32 v215, v255 offset:3840\n" \
"ds_read_b32 v216, v255 offset:4096\n" \
"ds_read_b32 v217, v255 offset:4352\n" \
"ds_read_b32 v218, v255 offset:4608\n" \
"ds_read_b32 v219, v255 offset:4864\n" \
"ds_read_b32 v220, v255 offset:5120\n" \
"ds_read_b32 v221, v255 offset:5376\n" \
"ds_read_b32 v222, v255 offset:5632\n" \
"ds_read_b32 v223, v255 offset:5888\n" \
"ds_read_b32 v254, v255 offset:7936\n" \
"s_waitcnt lgkmcnt(0)\n" \
"v_readlane_b32 s4, v254, 0\n" \
"v_readlane_b32 s5, v254, 1\n" \
"v_readlane_b32 s6, v254, 2\n" \
"v_readlane_b32 s7, v254, 3\n" \
"v_readlane_b32 s8, v254, 4\n" \
"v_readlane_b32 s9, v254, 5\n" \
"v_readlane_b32 s10, v254, 6\n" \
"v_readlane_b32 s11, v254, 7\n" \
"v_readlane_b32 s12, v254, 8\n" \
"v_readlane_b32 s13, v254, 9\n" \
"v_readlane_b32 s14, v254, 10\n" \
"v_readlane_b32 s15, v254, 11\n" \
"v_readlane_b32 s16, v254, 12\n" \
"v_readlane_b32 s17, v254, 13\n" \
"v_readlane_b32 s18, v254, 14\n" \
"v_readlane_b32 s19, v254, 15\n" \
"v_readlane_b32 s20, v254, 16\n" \
"v_readlane_b32 s21, v254, 17\n" \
"v_readlane_b32 s22, v254, 18\n" \
"v_readlane_b32 s23, v254, 19\n" \
"v_readlane_b32 s24, v254, 20\n" \
"v_readlane_b32 s25, v254, 21\n" \
"v_readlane_b32 s26, v254, 22\n" \
"v_readlane_b32 s27, v254, 23\n" \
"v_readlane_b32 s28, v254, 24\n" \
"v_readlane_b32 s29, v254, 25\n" \
"v_readlane_b32 s30, v254, 26\n" \
"v_readlane_b32 s31, v254, 27\n" \
"v_readlane_b32 s32, v254, 28\n" \
"v_readlane_b32 s33, v254, 29\n" \
"v_readlane_b32 s34, v254, 30\n" \
"v_readlane_b32 s35, v254, 31\n" \
"v_readlane_b32 s36, v254, 32\n" \
"v_readlane_b32 s37, v254, 33\n" \
"v_readlane_b32 s38, v254, 34\n" \
"v_readlane_b32 s39, v254, 35\n" \
"s_mov_b32 s40, 0\n" \
"s_mov_b32 s51, 0\n" \
"s_nop 0\n" \
"v_mov_b32 v252, s38\n" \
"s_nop 4\n" \
"v_mov_b32 v112, -1\n" \
"v_mov_b32 v113, -1\n" \
"v_mov_b32 v114, -1\n" \
"v_mov_b32 v115, -1\n" \
"v_mov_b32 v109, 0x7fc00001\n" \
"v_writelane_b32 v251, s6, 0\n" \
"v_writelane_b32 v251, s10, 1\n" \
"v_writelane_b32 v251, s14, 2\n" \
"v_writelane_b32 v251, s18, 3\n" \
"s_mov_b32 s6, 0\n" \
"s_mov_b32 s10, 0\n" \
"s_mov_b32 s14, 0\n" \
"s_mov_b32 s18, 0\n" \
"s_mov_b32 s40, -1\n" \
"s_branch .LP3_pre\n" \
".LP3_wet:\n" \
"s_waitcnt vmcnt(0)\n" \
"v_readlane_b32 s6, v251, 0\n" \
"v_readlane_b32 s10, v251, 1\n" \
"v_readlane_b32 s14, v251, 2\n" \
"v_readlane_b32 s18, v251, 3\n" \
"s_nop 4\n" \
"s_mov_b32 s33, 40\n" \
"s_mov_b32 s34, 0\n" \
"s_add_u32 s43, s30, 1\n" \
"s_mov_b32 s50, 0\n" \
".LP3_w46l:\n" \
"buffer_load_dword v240, v204, s[4:7], 0 offen sc1\n" \
"s_waitcnt vmcnt(0)\n" \
".LP3_w46c:\n" \
"v_subrev_u32 v241, s43, v240\n" \
"v_cmp_gt_u32 vcc, 0x100, v241\n" \
"s_cmp_eq_u64 vcc, -1\n" \
"s_cbranch_scc1 .LP3_w46d\n" \
"s_sleep 2\n" \
"s_add_u32 s50, s50, 1\n" \
"s_cmp_lt_u32 s50, 0x20000\n" \
"s_cbranch_scc1 .LP3_w46l\n" \
"s_branch .LP3_end\n" \
".LP3_w46d:\n" \
"v_readlane_b32 s43, v202, 1\n" \
"s_nop 4\n" \
"buffer_load_dwordx4 a[8:11], v201, s[8:11], s43 offen sc1\n" \
"buffer_load_dwordx4 a[12:15], v201, s[8:11], s43 offen offset:1024 sc1\n" \
"v_readlane_b32 s43, v202, 8\n" \
"s_nop 4\n" \
"buffer_load_dwordx4 a[64:67], v201, s[8:11], s43 offen sc1\n" \
"buffer_load_dwordx4 a[68:71], v201, s[8:11], s43 offen offset:1024 sc1\n" \
"v_readlane_b32 s43, v202, 15\n" \
"s_nop 4\n" \
"buffer_load_dwordx4 a[120:123], v201, s[8:11], s43 offen sc1\n" \
"buffer_load_dwordx4 a[124:127], v201, s[8:11], s43 offen offset:1024 sc1\n" \
"v_readlane_b32 s43, v202, 22\n" \
"s_nop 4\n" \
"buffer_load_dwordx4 a[176:179], v201, s[8:11], s43 offen sc1\n" \
"buffer_load_dwordx4 a[180:183], v201, s[8:11], s43 offen offset:1024 sc1\n" \
"s_add_u32 s43, s30, 2\n" \
"s_mov_b32 s50, 0\n" \
"s_branch .LP3_w47c\n" \
".LP3_w47l:\n" \
"buffer_load_dword v240, v204, s[4:7], 0 offen sc1\n" \
"s_waitcnt vmcnt(0)\n" \
".LP3_w47c:\n" \
"v_subrev_u32 v241, s43, v240\n" \
"v_cmp_gt_u32 vcc, 0x100, v241\n" \
"s_cmp_eq_u64 vcc, -1\n" \
"s_cbranch_scc1 .LP3_w47d\n" \
"s_sleep 2\n" \
"s_add_u32 s50, s50, 1\n" \
"s_cmp_lt_u32 s50, 0x20000\n" \
"s_cbranch_scc1 .LP3_w47l\n" \
"s_branch .LP3_end\n" \
".LP3_w47d:\n" \
"v_readlane_b32 s43, v202, 4\n" \
"s_nop 4\n" \
"buffer_load_dwordx4 a[32:35], v201, s[8:11], s43 offen sc1\n" \
"buffer_load_dwordx4 a[36:39], v201, s[8:11], s43 offen offset:1024 sc1\n" \
"v_readlane_b32 s43, v202, 11\n" \
"s_nop 4\n" \
"buffer_load_dwordx4 a[88:91], v201, s[8:11], s43 offen sc1\n" \
"buffer_load_dwordx4 a[92:95], v201, s[8:11], s43 offen offset:1024 sc1\n" \
"v_readlane_b32 s43, v202, 18\n" \
"s_nop 4\n" \
"buffer_load_dwordx4 a[144:147], v201, s[8:11], s43 offen sc1\n" \
"buffer_load_dwordx4 a[148:151], v201, s[8:11], s43 offen offset:1024 sc1\n" \
"v_readlane_b32 s43, v202, 25\n" \
"s_nop 4\n" \
"buffer_load_dwordx4 a[200:203], v201, s[8:11], s43 offen sc1\n" \
"buffer_load_dwordx4 a[204:207], v201, s[8:11], s43 offen offset:1024 sc1\n" \
"s_add_u32 s43, s30, 3\n" \
"s_mov_b32 s50, 0\n" \
"s_branch .LP3_w48c\n" \
".LP3_w48l:\n" \
"buffer_load_dword v240, v204, s[4:7], 0 offen sc1\n" \
"s_waitcnt vmcnt(0)\n" \
".LP3_w48c:\n" \
"v_subrev_u32 v241, s43, v240\n" \
"v_cmp_gt_u32 vcc, 0x100, v241\n" \
"s_cmp_eq_u64 vcc, -1\n" \
"s_cbranch_scc1 .LP3_w48d\n" \
"s_sleep 2\n" \
"s_add_u32 s50, s50, 1\n" \
"s_cmp_lt_u32 s50, 0x20000\n" \
"s_cbranch_scc1 .LP3_w48l\n" \
"s_branch .LP3_end\n" \
".LP3_w48d:\n" \
"v_readlane_b32 s43, v202, 2\n" \
"s_nop 4\n" \
"buffer_load_dwordx4 a[16:19], v201, s[8:11], s43 offen sc1\n" \
"buffer_load_dwordx4 a[20:23], v201, s[8:11], s43 offen offset:1024 sc1\n" \
"v_readlane_b32 s43, v202, 9\n" \
"s_nop 4\n" \
"buffer_load_dwordx4 a[72:75], v201, s[8:11], s43 offen sc1\n" \
"buffer_load_dwordx4 a[76:79], v201, s[8:11], s43 offen offset:1024 sc1\n" \
"v_readlane_b32 s43, v202, 16\n" \
"s_nop 4\n" \
"buffer_load_dwordx4 a[128:131], v201, s[8:11], s43 offen sc1\n" \
"buffer_load_dwordx4 a[132:135], v201, s[8:11], s43 offen offset:1024 sc1\n" \
"v_readlane_b32 s43, v202, 23\n" \
"s_nop 4\n" \
"buffer_load_dwordx4 a[184:187], v201, s[8:11], s43 offen sc1\n" \
"buffer_load_dwordx4 a[188:191], v201, s[8:11], s43 offen offset:1024 sc1\n" \
"s_add_u32 s43, s30, 4\n" \
"s_mov_b32 s50, 0\n" \
"s_branch .LP3_w49c\n" \
".LP3_w49l:\n" \
"buffer_load_dword v240, v204, s[4:7], 0 offen sc1\n" \
"s_waitcnt vmcnt(0)\n" \
".LP3_w49c:\n" \
"v_subrev_u32 v241, s43, v240\n" \
"v_cmp_gt_u32 vcc, 0x100, v241\n" \
"s_cmp_eq_u64 vcc, -1\n" \
"s_cbranch_scc1 .LP3_w49d\n" \
"s_sleep 2\n" \
"s_add_u32 s50, s50, 1\n" \
"s_cmp_lt_u32 s50, 0x20000\n" \
"s_cbranch_scc1 .LP3_w49l\n" \
"s_branch .LP3_end\n" \
".LP3_w49d:\n" \
"v_readlane_b32 s43, v202, 5\n" \
"s_nop 4\n" \
"buffer_load_dwordx4 a[40:43], v201, s[8:11], s43 offen sc1\n" \
"buffer_load_dwordx4 a[44:47], v201, s[8:11], s43 offen offset:1024 sc1\n" \
"v_readlane_b32 s43, v202, 12\n" \
"s_nop 4\n" \
"buffer_load_dwordx4 a[96:99], v201, s[8:11], s43 offen sc1\n" \
"buffer_load_dwordx4 a[100:103], v201, s[8:11], s43 offen offset:1024 sc1\n" \
"v_readlane_b32 s43, v202, 19\n" \
"s_nop 4\n" \
"buffer_load_dwordx4 a[152:155], v201, s[8:11], s43 offen sc1\n" \
"buffer_load_dwordx4 a[156:159], v201, s[8:11], s43 offen offset:1024 sc1\n" \
"v_readlane_b32 s43, v202, 26\n" \
"s_nop 4\n" \
"buffer_load_dwordx4 a[208:211], v201, s[8:11], s43 offen sc1\n" \
"buffer_load_dwordx4 a[212:215], v201, s[8:11], s43 offen offset:1024 sc1\n" \
"s_add_u32 s43, s30, 5\n" \
"s_mov_b32 s50, 0\n" \
"s_branch .LP3_w50c\n" \
".LP3_w50l:\n" \
"buffer_load_dword v240, v204, s[4:7], 0 offen sc1\n" \
"s_waitcnt vmcnt(0)\n" \
".LP3_w50c:\n" \
"v_subrev_u32 v241, s43, v240\n" \
"v_cmp_gt_u32 vcc, 0x100, v241\n" \
"s_cmp_eq_u64 vcc, -1\n" \
"s_cbranch_scc1 .LP3_w50d\n" \
"s_sleep 2\n" \
"s_add_u32 s50, s50, 1\n" \
"s_cmp_lt_u32 s50, 0x20000\n" \
"s_cbranch_scc1 .LP3_w50l\n" \
"s_branch .LP3_end\n" \
".LP3_w50d:\n" \
"v_readlane_b32 s43, v202, 3\n" \
"s_nop 4\n" \
"buffer_load_dwordx4 a[24:27], v201, s[8:11], s43 offen sc1\n" \
"buffer_load_dwordx4 a[28:31], v201, s[8:11], s43 offen offset:1024 sc1\n" \
"v_readlane_b32 s43, v202, 10\n" \
"s_nop 4\n" \
"buffer_load_dwordx4 a[80:83], v201, s[8:11], s43 offen sc1\n" \
"buffer_load_dwordx4 a[84:87], v201, s[8:11], s43 offen offset:1024 sc1\n" \
"v_readlane_b32 s43, v202, 17\n" \
"s_nop 4\n" \
"buffer_load_dwordx4 a[136:139], v201, s[8:11], s43 offen sc1\n" \
"buffer_load_dwordx4 a[140:143], v201, s[8:11], s43 offen offset:1024 sc1\n" \
"v_readlane_b32 s43, v202, 24\n" \
"s_nop 4\n" \
"buffer_load_dwordx4 a[192:195], v201, s[8:11], s43 offen sc1\n" \
"buffer_load_dwordx4 a[196:199], v201, s[8:11], s43 offen offset:1024 sc1\n" \
"s_add_u32 s43, s30, 6\n" \
"s_mov_b32 s50, 0\n" \
"s_branch .LP3_w51c\n" \
".LP3_w51l:\n" \
"buffer_load_dword v240, v204, s[4:7], 0 offen sc1\n" \
"s_waitcnt vmcnt(0)\n" \
".LP3_w51c:\n" \
"v_subrev_u32 v241, s43, v240\n" \
"v_cmp_gt_u32 vcc, 0x100, v241\n" \
"s_cmp_eq_u64 vcc, -1\n" \
"s_cbranch_scc1 .LP3_w51d\n" \
"s_sleep 2\n" \
"s_add_u32 s50, s50, 1\n" \
"s_cmp_lt_u32 s50, 0x20000\n" \
"s_cbranch_scc1 .LP3_w51l\n" \
"s_branch .LP3_end\n" \
".LP3_w51d:\n" \
"v_readlane_b32 s43, v202, 6\n" \
"s_nop 4\n" \
"buffer_load_dwordx4 a[48:51], v201, s[8:11], s43 offen sc1\n" \
"buffer_load_dwordx4 a[52:55], v201, s[8:11], s43 offen offset:1024 sc1\n" \
"v_readlane_b32 s43, v202, 13\n" \
"s_nop 4\n" \
"buffer_load_dwordx4 a[104:107], v201, s[8:11], s43 offen sc1\n" \
"buffer_load_dwordx4 a[108:111], v201, s[8:11], s43 offen offset:1024 sc1\n" \
"v_readlane_b32 s43, v202, 20\n" \
"s_nop 4\n" \
"buffer_load_dwordx4 a[160:163], v201, s[8:11], s43 offen sc1\n" \
"buffer_load_dwordx4 a[164:167], v201, s[8:11], s43 offen offset:1024 sc1\n" \
"v_readlane_b32 s43, v202, 27\n" \
"s_nop 4\n" \
"buffer_load_dwordx4 a[216:219], v201, s[8:11], s43 offen sc1\n" \
"buffer_load_dwordx4 a[220:223], v201, s[8:11], s43 offen offset:1024 sc1\n" \
"s_add_u32 s43, s30, 7\n" \
"s_mov_b32 s50, 0\n" \
"s_branch .LP3_w52c\n" \
".LP3_w52l:\n" \
"buffer_load_dword v240, v204, s[4:7], 0 offen sc1\n" \
"s_waitcnt vmcnt(0)\n" \
".LP3_w52c:\n" \
"v_subrev_u32 v241, s43, v240\n" \
"v_cmp_gt_u32 vcc, 0x100, v241\n" \
"s_cmp_eq_u64 vcc, -1\n" \
"s_cbranch_scc1 .LP3_w52d\n" \
"s_sleep 2\n" \
"s_add_u32 s50, s50, 1\n" \
"s_cmp_lt_u32 s50, 0x20000\n" \
"s_cbranch_scc1 .LP3_w52l\n" \
"s_branch .LP3_end\n" \
".LP3_w52d:\n" \
"v_readlane_b32 s43, v202, 0\n" \
"s_nop 4\n" \
"buffer_load_dwordx4 a[0:3], v201, s[8:11], s43 offen sc1\n" \
"buffer_load_dwordx4 a[4:7], v201, s[8:11], s43 offen offset:1024 sc1\n" \
"v_readlane_b32 s43, v202, 7\n" \
"s_nop 4\n" \
"buffer_load_dwordx4 a[56:59], v201, s[8:11], s43 offen sc1\n" \
"buffer_load_dwordx4 a[60:63], v201, s[8:11], s43 offen offset:1024 sc1\n" \
"v_readlane_b32 s43, v202, 14\n" \
"s_nop 4\n" \
"buffer_load_dwordx4 a[112:115], v201, s[8:11], s43 offen sc1\n" \
"buffer_load_dwordx4 a[116:119], v201, s[8:11], s43 offen offset:1024 sc1\n" \
"v_readlane_b32 s43, v202, 21\n" \
"s_nop 4\n" \
"buffer_load_dwordx4 a[168:171], v201, s[8:11], s43 offen sc1\n" \
"buffer_load_dwordx4 a[172:175], v201, s[8:11], s43 offen offset:1024 sc1\n" \
"ds_read_b96 v[248:250], v252\n" \
"s_waitcnt lgkmcnt(0)\n" \
"v_readfirstlane_b32 s44, v248\n" \
"v_readfirstlane_b32 s45, v249\n" \
"v_readfirstlane_b32 s46, v250\n" \
"s_mul_i32 s42, s25, s46\n" \
"s_lshr_b32 s42, s42, 12\n" \
"s_mov_b32 s26, s44\n" \
"s_mov_b32 s27, s45\n" \
".p2align 3\n" \
"s_memtime s[54:55]\n" \
"s_memrealtime s[56:57]\n" \
"s_memtime s[58:59]\n" \
"s_memrealtime s[60:61]\n" \
"s_nop 0\n" \
"s_nop 0\n" \
"s_memtime s[62:63]\n" \
"s_memrealtime s[64:65]\n" \
"s_memtime s[66:67]\n" \
"s_memrealtime s[68:69]\n" \
"s_memtime s[70:71]\n" \
"s_memrealtime s[72:73]\n" \
"s_memtime s[74:75]\n" \
"s_memrealtime s[76:77]\n" \
"s_nop 0\n" \
"s_nop 0\n" \
"s_memtime s[78:79]\n" \
"s_memrealtime s[80:81]\n" \
"s_memtime s[82:83]\n" \
"s_memrealtime s[84:85]\n" \
"s_memtime s[86:87]\n" \
"s_memrealtime s[88:89]\n" \
"s_memtime s[90:91]\n" \
"s_memrealtime s[92:93]\n" \
"s_nop 0\n" \
"s_nop 0\n" \
"s_memtime s[94:95]\n" \
"s_memrealtime s[96:97]\n" \
"s_memtime s[98:99]\n" \
"s_mov_b32 s52, 0\n" \
"s_mov_b32 s53, 4096\n" \
"s_waitcnt lgkmcnt(0)\n" \
"s_cmp_lg_u32 s96, s56\n" \
"s_cselect_b32 s52, s90, s52\n" \
"s_cselect_b32 s53, s98, s53\n" \
"s_cmp_lg_u32 s92, s56\n" \
"s_cselect_b32 s52, s86, s52\n" \
"s_cselect_b32 s53, s94, s53\n" \
"s_cmp_lg_u32 s88, s56\n" \
"s_cselect_b32 s52, s82, s52\n" \
"s_cselect_b32 s53, s90, s53\n" \
"s_cmp_lg_u32 s84, s56\n" \
"s_cselect_b32 s52, s78, s52\n" \
"s_cselect_b32 s53, s86, s53\n" \
"s_cmp_lg_u32 s80, s56\n" \
"s_cselect_b32 s52, s74, s52\n" \
"s_cselect_b32 s53, s82, s53\n" \
"s_cmp_lg_u32 s76, s56\n" \
"s_cselect_b32 s52, s70, s52\n" \
"s_cselect_b32 s53, s78, s53\n" \
"s_cmp_lg_u32 s72, s56\n" \
"s_cselect_b32 s52, s66, s52\n" \
"s_cselect_b32 s53, s74, s53\n" \
"s_cmp_lg_u32 s68, s56\n" \
"s_cselect_b32 s52, s62, s52\n" \
"s_cselect_b32 s53, s70, s53\n" \
"s_cmp_lg_u32 s64, s56\n" \
"s_cselect_b32 s52, s58, s52\n" \
"s_cselect_b32 s53, s66, s53\n" \
"s_cmp_lg_u32 s60, s56\n" \
"s_cselect_b32 s52, s54, s52\n" \
"s_cselect_b32 s53, s62, s53\n" \
"s_sub_u32 s53, s53, s52\n" \
"s_lshr_b32 s55, s53, 1\n" \
"s_add_u32 s52, s52, s55\n" \
"s_add_u32 s55, s56, 4\n" \
"s_lshl_b32 s55, s55, 4\n" \
"s_cmp_le_u32 s53, 40\n" \
"s_cselect_b32 s44, s55, s44\n" \
"s_cselect_b32 s45, s52, s45\n" \
"v_mov_b32 v248, s44\n" \
"v_mov_b32 v249, s45\n" \
"v_mov_b32 v250, s46\n" \
"s_mov_b64 exec, 1\n" \
"ds_write_b96 v252, v[248:250]\n" \
"s_mov_b64 exec, -1\n" \
"s_sub_u32 s26, s44, s26\n" \
"s_sub_u32 s27, s45, s27\n" \
"v_cvt_f32_u32 v248, s26\n" \
"v_cvt_f32_u32 v249, s27\n" \
"v_rcp_f32 v248, v248\n" \
"v_mul_f32 v249, 0x457ae148, v249\n" \
"v_mul_f32 v248, v248, v249\n" \
"v_cvt_u32_f32 v248, v248\n" \
"s_nop 0\n" \
"v_readfirstlane_b32 s27, v248\n" \
"s_max_u32 s27, s27, 0x800\n" \
"s_min_u32 s27, s27, 0x2000\n" \
"s_cmp_gt_u32 s26, 1600\n" \
"s_cselect_b32 s46, s27, s46\n" \
".p2align 3\n" \
"s_memtime s[54:55]\n" \
"s_memrealtime s[56:57]\n" \
"s_memtime s[58:59]\n" \
"s_memrealtime s[60:61]\n" \
"s_nop 0\n" \
"s_nop 0\n" \
"s_memtime s[62:63]\n" \
"s_memrealtime s[64:65]\n" \
"s_memtime s[66:67]\n" \
"s_memrealtime s[68:69]\n" \
"s_memtime s[70:71]\n" \
"s_memrealtime s[72:73]\n" \
"s_memtime s[74:75]\n" \
"s_memrealtime s[76:77]\n" \
"s_nop 0\n" \
"s_nop 0\n" \
"s_memtime s[78:79]\n" \
"s_memrealtime s[80:81]\n" \
"s_memtime s[82:83]\n" \
"s_memrealtime s[84:85]\n" \
"s_memtime s[86:87]\n" \
"s_memrealtime s[88:89]\n" \
"s_memtime s[90:91]\n" \
"s_memrealtime s[92:93]\n" \
"s_nop 0\n" \
"s_nop 0\n" \
"s_memtime s[94:95]\n" \
"s_memrealtime s[96:97]\n" \
"s_memtime s[98:99]\n" \
"s_mov_b32 s52, 0\n" \
"s_mov_b32 s53, 4096\n" \
"s_waitcnt lgkmcnt(0)\n" \
"s_cmp_lg_u32 s96, s56\n" \
"s_cselect_b32 s52, s90, s52\n" \
"s_cselect_b32 s53, s98, s53\n" \
"s_cmp_lg_u32 s92, s56\n" \
"s_cselect_b32 s52, s86, s52\n" \
"s_cselect_b32 s53, s94, s53\n" \
"s_cmp_lg_u32 s88, s56\n" \
"s_cselect_b32 s52, s82, s52\n" \
"s_cselect_b32 s53, s90, s53\n" \
"s_cmp_lg_u32 s84, s56\n" \
"s_cselect_b32 s52, s78, s52\n" \
"s_cselect_b32 s53, s86, s53\n" \
"s_cmp_lg_u32 s80, s56\n" \
"s_cselect_b32 s52, s74, s52\n" \
"s_cselect_b32 s53, s82, s53\n" \
"s_cmp_lg_u32 s76, s56\n" \
"s_cselect_b32 s52, s70, s52\n" \
"s_cselect_b32 s53, s78, s53\n" \
"s_cmp_lg_u32 s72, s56\n" \
"s_cselect_b32 s52, s66, s52\n" \
"s_cselect_b32 s53, s74, s53\n" \
"s_cmp_lg_u32 s68, s56\n" \
"s_cselect_b32 s52, s62, s52\n" \
"s_cselect_b32 s53, s70, s53\n" \
"s_cmp_lg_u32 s64, s56\n" \
"s_cselect_b32 s52, s58, s52\n" \
"s_cselect_b32 s53, s66, s53\n" \
"s_cmp_lg_u32 s60, s56\n" \
"s_cselect_b32 s52, s54, s52\n" \
"s_cselect_b32 s53, s62, s53\n" \
"s_sub_u32 s53, s53, s52\n" \
"s_lshr_b32 s55, s53, 1\n" \
"s_add_u32 s52, s52, s55\n" \
"s_add_u32 s55, s56, 4\n" \
"s_lshl_b32 s55, s55, 4\n" \
"s_cmp_le_u32 s53, 40\n" \
"s_cselect_b32 s44, s55, s44\n" \
"s_cselect_b32 s45, s52, s45\n" \
"v_mov_b32 v248, s44\n" \
"v_mov_b32 v249, s45\n" \
"v_mov_b32 v250, s46\n" \
"s_mov_b64 exec, 1\n" \
"ds_write_b96 v252, v[248:250]\n" \
"s_mov_b64 exec, -1\n" \
"s_waitcnt lgkmcnt(0)\n" \
"s_add_u32 s43, s30, 1\n" \
"s_mov_b32 s50, 0\n" \
".LP3_w53l:\n" \
"buffer_load_dword v240, v205, s[4:7], 0 offen sc1\n" \
"s_waitcnt vmcnt(0)\n" \
".LP3_w53c:\n" \
"v_subrev_u32 v241, s43, v240\n" \
"v_cmp_gt_u32 vcc, 0x100, v241\n" \
"s_cmp_eq_u64 vcc, -1\n" \
"s_cbranch_scc1 .LP3_w53d\n" \
"s_sleep 2\n" \
"s_add_u32 s50, s50, 1\n" \
"s_cmp_lt_u32 s50, 0x20000\n" \
"s_cbranch_scc1 .LP3_w53l\n" \
"s_branch .LP3_end\n" \
".LP3_w53d:\n" \
"v_readfirstlane_b32 s31, v241\n" \
"s_lshr_b32 s32, s31, 2\n" \
"s_xor_b32 s29, s31, s32\n" \
"s_and_b32 s29, s29, 3\n" \
"s_lshl_b32 s29, s29, 13\n" \
"s_bitset1_b32 s29, 21\n" \
"s_and_b32 s31, s31, 3\n" \
"s_lshl_b32 s31, s31, 13\n" \
"s_bitcmp1_b32 s39, 0\n" \
"s_cselect_b32 s43, s29, 0\n" \
"s_xor_b32 s31, s31, s43\n" \
"s_xor_b32 s32, s31, 0x4000\n" \
"s_mov_b32 s39, 0\n" \
"s_add_u32 s43, s40, 0\n" \
"s_and_b32 s43, s43, 3\n" \
"s_bitcmp1_b32 s43, 0\n" \
"s_cselect_b32 s47, s32, s31\n" \
"s_lshl_b32 s43, s43, 19\n" \
"s_add_u32 s47, s47, s43\n" \
".LP3_pre:\n" \
"s_barrier\n" \
"ds_read_b96 v[248:250], v252\n" \
"s_waitcnt lgkmcnt(0)\n" \
"v_readfirstlane_b32 s44, v248\n" \
"v_readfirstlane_b32 s45, v249\n" \
"v_readfirstlane_b32 s46, v250\n" \
"s_mul_i32 s42, s25, s46\n" \
"s_lshr_b32 s42, s42, 12\n" \
".LP3_cu:\n" \
"s_sub_u32 s43, s24, s44\n" \
"s_cmp_lt_i32 s43, 0\n" \
"s_cbranch_scc0 .LP3_cud\n" \
"s_add_u32 s24, s24, s25\n" \
"s_xor_b32 s31, s31, s29\n" \
"s_xor_b32 s32, s32, s29\n" \
"s_xor_b32 s47, s47, s29\n" \
"s_add_u32 s39, s39, 1\n" \
"s_branch .LP3_cu\n" \
".LP3_cud:\n" \
"s_mov_b32 s3, s46\n" \
"s_add_u32 s43, s40, 1\n" \
"s_and_b32 s43, s43, 3\n" \
"s_bitcmp1_b32 s43, 0\n" \
"s_cselect_b32 s48, s32, s31\n" \
"s_lshl_b32 s43, s43, 19\n" \
"s_add_u32 s48, s48, s43\n" \
"s_add_u32 s43, s40, 2\n" \
"s_and_b32 s43, s43, 3\n" \
"s_bitcmp1_b32 s43, 0\n" \
"s_cselect_b32 s49, s32, s31\n" \
"s_lshl_b32 s43, s43, 19\n" \
"s_add_u32 s49, s49, s43\n" \
"s_sub_u32 s43, s24, s44\n" \
"s_add_u32 s43, s43, s28\n" \
"s_max_i32 s43, s43, 0xfffd0000\n" \
"s_min_i32 s43, s43, 0x4000\n" \
"s_mul_i32 s43, s43, s46\n" \
"s_ashr_i32 s43, s43, 12\n" \
"s_add_u32 s41, s45, s43\n" \
"s_lshr_b32 s35, s42, 1\n" \
"s_sub_u32 s35, 0, s35\n" \
".LP3_loop:\n" \
"s_branch .LP3_m1\n" \
".LP3_retry1:\n" \
"s_add_u32 s51, s51, 1\n" \
"s_cmp_gt_u32 s51, 0x20000\n" \
"s_cbranch_scc1 .LP3_end\n" \
".LP3_m0:\n" \
"s_add_u32 s41, s41, s42\n" \
"s_add_u32 s24, s24, s25\n" \
"s_xor_b32 s31, s31, s29\n" \
"s_xor_b32 s32, s32, s29\n" \
"s_xor_b32 s47, s47, s29\n" \
"s_xor_b32 s48, s48, s29\n" \
"s_add_u32 s39, s39, 1\n" \
"s_bitcmp1_b32 s31, 21\n" \
"s_cselect_b32 s43, s29, 0\n" \
"v_mov_b32 v116, s43\n" \
"ds_write_b32 v252, v116 offset:16\n" \
".LP3_m1:\n" \
"s_memtime vcc\n" \
"s_waitcnt lgkmcnt(0)\n" \
"s_sub_u32 s43, s41, vcc_lo\n" \
"s_cmp_gt_i32 s43, 0\n" \
"s_cbranch_scc1 .LP3_m1\n" \
"s_cmp_lt_i32 s43, s35\n" \
"s_cbranch_scc1 .LP3_m0\n" \
"buffer_load_dwordx4 v[0:3], v208, s[4:7], s47 offen offset:3072 sc1\n" \
".p2align 3\n" \
"s_memtime s[54:55]\n" \
"s_memrealtime s[56:57]\n" \
"s_memtime s[58:59]\n" \
"s_memrealtime s[60:61]\n" \
"s_nop 0\n" \
"s_nop 0\n" \
"s_memtime s[62:63]\n" \
"s_memrealtime s[64:65]\n" \
"s_memtime s[66:67]\n" \
"s_memrealtime s[68:69]\n" \
"s_memtime s[70:71]\n" \
"s_memrealtime s[72:73]\n" \
"s_memtime s[74:75]\n" \
"s_memrealtime s[76:77]\n" \
"s_nop 0\n" \
"s_nop 0\n" \
"s_memtime s[78:79]\n" \
"s_memrealtime s[80:81]\n" \
"s_memtime s[82:83]\n" \
"s_memrealtime s[84:85]\n" \
"s_memtime s[86:87]\n" \
"s_memrealtime s[88:89]\n" \
"s_memtime s[90:91]\n" \
"s_memrealtime s[92:93]\n" \
"s_nop 0\n" \
"s_nop 0\n" \
"s_memtime s[94:95]\n" \
"s_memrealtime s[96:97]\n" \
"s_memtime s[98:99]\n" \
"s_mov_b32 s52, 0\n" \
"s_mov_b32 s53, 4096\n" \
"s_waitcnt lgkmcnt(0)\n" \
"s_cmp_lg_u32 s96, s56\n" \
"s_cselect_b32 s52, s90, s52\n" \
"s_cselect_b32 s53, s98, s53\n" \
"s_cmp_lg_u32 s92, s56\n" \
"s_cselect_b32 s52, s86, s52\n" \
"s_cselect_b32 s53, s94, s53\n" \
"s_cmp_lg_u32 s88, s56\n" \
"s_cselect_b32 s52, s82, s52\n" \
"s_cselect_b32 s53, s90, s53\n" \
"s_cmp_lg_u32 s84, s56\n" \
"s_cselect_b32 s52, s78, s52\n" \
"s_cselect_b32 s53, s86, s53\n" \
"s_cmp_lg_u32 s80, s56\n" \
"s_cselect_b32 s52, s74, s52\n" \
"s_cselect_b32 s53, s82, s53\n" \
"s_cmp_lg_u32 s76, s56\n" \
"s_cselect_b32 s52, s70, s52\n" \
"s_cselect_b32 s53, s78, s53\n" \
"s_cmp_lg_u32 s72, s56\n" \
"s_cselect_b32 s52, s66, s52\n" \
"s_cselect_b32 s53, s74, s53\n" \
"s_cmp_lg_u32 s68, s56\n" \
"s_cselect_b32 s52, s62, s52\n" \
"s_cselect_b32 s53, s70, s53\n" \
"s_cmp_lg_u32 s64, s56\n" \
"s_cselect_b32 s52, s58, s52\n" \
"s_cselect_b32 s53, s66, s53\n" \
"s_cmp_lg_u32 s60, s56\n" \
"s_cselect_b32 s52, s54, s52\n" \
"s_cselect_b32 s53, s62, s53\n" \
"s_sub_u32 s53, s53, s52\n" \
"s_lshr_b32 s55, s53, 1\n" \
"s_add_u32 s52, s52, s55\n" \
"s_add_u32 s55, s56, 4\n" \
"s_lshl_b32 s55, s55, 4\n" \
"s_sub_u32 s59, s55, s44\n" \
"s_cmp_lt_u32 s59, 2600\n" \
"s_cselect_b32 s63, 1, 2\n" \
"s_mul_i32 s59, s59, s46\n" \
"s_ashr_i32 s59, s59, 12\n" \
"s_add_u32 s59, s59, s45\n" \
"s_sub_u32 s59, s52, s59\n" \
"s_max_i32 s59, s59, -96\n" \
"s_min_i32 s59, s59, 96\n" \
"s_ashr_i32 s63, s59, s63\n" \
"s_add_u32 s63, s63, s46\n" \
"s_max_i32 s63, s63, 0x800\n" \
"s_min_i32 s63, s63, 0x2000\n" \
"s_cmp_le_u32 s53, 40\n" \
"s_cselect_b32 s44, s55, s44\n" \
"s_cselect_b32 s45, s52, s45\n" \
"s_cselect_b32 s46, s63, s46\n" \
"v_mov_b32 v248, s44\n" \
"v_mov_b32 v249, s45\n" \
"v_mov_b32 v250, s46\n" \
"s_mov_b64 exec, 1\n" \
"ds_write_b96 v252, v[248:250]\n" \
"s_mov_b64 exec, -1\n" \
"v_mov_b32 v28, 0\n" \
"v_mov_b32 v29, 0\n" \
"v_mov_b32 v30, 0\n" \
"v_mov_b32 v31, 0\n" \
"v_mov_b32 v32, 0\n" \
"v_mov_b32 v33, 0\n" \
"v_mov_b32 v34, 0\n" \
"v_mov_b32 v35, 0\n" \
"v_mov_b32 v36, 0\n" \
"v_mov_b32 v37, 0\n" \
"v_mov_b32 v38, 0\n" \
"v_mov_b32 v39, 0\n" \
"v_mov_b32 v40, 0\n" \
"v_mov_b32 v41, 0\n" \
"v_mov_b32 v42, 0\n" \
"v_mov_b32 v43, 0\n" \
"s_waitcnt vmcnt(0)\n" \
"ds_write_b128 v220, v[0:3] offset:3072\n" \
"v_max3_u32 v44, v0, v0, v1\n" \
"v_max3_u32 v44, v44, v2, v3\n" \
"v_cmp_eq_u32 vcc, -1, v44\n" \
"v_smfmac_f32_16x16x64_bf16 v[28:31], v[0:3], a[0:7], v200\n" \
"v_smfmac_f32_16x16x64_bf16 v[32:35], v[0:3], a[56:63], v200\n" \
"s_cbranch_vccnz .LP3_retry1\n" \
"s_waitcnt lgkmcnt(0)\n" \
"s_barrier\n" \
"ds_read_b128 v[4:7], v220 offset:0\n" \
"ds_read_b128 v[8:11], v220 offset:1024\n" \
"ds_read_b128 v[12:15], v220 offset:2048\n" \
"ds_read_b128 v[16:19], v220 offset:4096\n" \
"ds_read_b128 v[20:23], v220 offset:5120\n" \
"ds_read_b128 v[24:27], v220 offset:6144\n" \
"ds_read_b64 v[246:247], v252 offset:24\n" \
"ds_read_b96 v[248:250], v252\n" \
"s_mul_i32 s50, s40, 4096\n" \
"buffer_store_dwordx4 v[68:71], v218, s[12:15], s50 offen sc1\n" \
"buffer_store_dword v112, v213, s[4:7], s49 offen sc1\n" \
"s_xor_b32 s43, s49, s29\n" \
"buffer_store_dword v112, v213, s[4:7], s43 offen sc1\n" \
"v_smfmac_f32_16x16x64_bf16 v[36:39], v[0:3], a[112:119], v200\n" \
"v_smfmac_f32_16x16x64_bf16 v[40:43], v[0:3], a[168:175], v200\n" \
"s_waitcnt lgkmcnt(7)\n" \
"v_smfmac_f32_16x16x64_bf16 v[28:31], v[4:7], a[8:15], v200\n" \
"v_smfmac_f32_16x16x64_bf16 v[32:35], v[4:7], a[64:71], v200\n" \
"v_smfmac_f32_16x16x64_bf16 v[36:39], v[4:7], a[120:127], v200\n" \
"v_smfmac_f32_16x16x64_bf16 v[40:43], v[4:7], a[176:183], v200\n" \
"s_waitcnt lgkmcnt(6)\n" \
"v_smfmac_f32_16x16x64_bf16 v[28:31], v[8:11], a[16:23], v200\n" \
"v_smfmac_f32_16x16x64_bf16 v[32:35], v[8:11], a[72:79], v200\n" \
"v_smfmac_f32_16x16x64_bf16 v[36:39], v[8:11], a[128:135], v200\n" \
"v_smfmac_f32_16x16x64_bf16 v[40:43], v[8:11], a[184:191], v200\n" \
"s_waitcnt lgkmcnt(0)\n" \
"v_readfirstlane_b32 s44, v248\n" \
"v_readfirstlane_b32 s45, v249\n" \
"v_smfmac_f32_16x16x64_bf16 v[28:31], v[12:15], a[24:31], v200\n" \
"v_readfirstlane_b32 s46, v250\n" \
"v_readfirstlane_b32 s26, v246\n" \
"v_readfirstlane_b32 s27, v247\n" \
"v_smfmac_f32_16x16x64_bf16 v[32:35], v[12:15], a[80:87], v200\n" \
"s_mul_i32 s42, s25, s46\n" \
"s_lshr_b32 s42, s42, 12\n" \
"s_add_u32 s24, s24, s26\n" \
"v_smfmac_f32_16x16x64_bf16 v[36:39], v[12:15], a[136:143], v200\n" \
"s_sub_u32 s43, s24, s44\n" \
"s_max_i32 s43, s43, 0xfffd0000\n" \
"s_min_i32 s43, s43, 0x4000\n" \
"v_smfmac_f32_16x16x64_bf16 v[40:43], v[12:15], a[192:199], v200\n" \
"s_mul_i32 s43, s43, s46\n" \
"s_ashr_i32 s43, s43, 12\n" \
"s_add_u32 s41, s45, s43\n" \
"s_cmp_eq_u32 s40, 63\n" \
"s_cbranch_scc1 .LP3_last\n" \
"s_branch .LP3_z1\n" \
".LP3_z0:\n" \
"s_add_u32 s41, s41, s42\n" \
"s_add_u32 s24, s24, s25\n" \
"s_xor_b32 s31, s31, s29\n" \
"s_xor_b32 s32, s32, s29\n" \
"s_xor_b32 s47, s47, s29\n" \
"s_xor_b32 s48, s48, s29\n" \
"s_add_u32 s39, s39, 1\n" \
"buffer_store_dwordx4 v[112:115], v223, s[4:7], s48 offen sc1\n" \
"s_bitcmp1_b32 s31, 21\n" \
"s_cselect_b32 s43, s29, 0\n" \
"v_mov_b32 v116, s43\n" \
"ds_write_b32 v252, v116 offset:16\n" \
".LP3_z1:\n" \
"s_memtime vcc\n" \
"s_waitcnt lgkmcnt(0)\n" \
"s_sub_u32 s43, s41, vcc_lo\n" \
"s_cmp_gt_i32 s43, 200\n" \
"s_cbranch_scc1 .LP3_z1\n" \
"s_cmp_lt_i32 s43, s37\n" \
"s_cbranch_scc1 .LP3_z0\n" \
"buffer_load_dwordx2 v[64:65], v211, s[4:7], s47 offen sc1\n" \
".p2align 6\n" \
".LP3_b1:\n" \
"s_memtime vcc\n" \
"s_waitcnt lgkmcnt(0)\n" \
"s_sub_u32 s43, s41, vcc_lo\n" \
"s_cmp_gt_i32 s43, 128\n" \
"s_cbranch_scc1 .LP3_b1\n" \
"s_cmp_lt_i32 s43, s37\n" \
"s_cbranch_scc1 .LP3_z0\n" \
"s_max_i32 s43, s43, 0\n" \
"s_and_b32 s43, s43, -4\n" \
"s_getpc_b64 vcc\n" \
".LP3_b3:\n" \
"s_sub_u32 s43, .LP3_b4-.LP3_b3, s43\n" \
"s_add_u32 vcc_lo, vcc_lo, s43\n" \
"s_addc_u32 vcc_hi, vcc_hi, 0\n" \
"s_setpc_b64 vcc\n" \
".rept 32\n" \
"s_nop 0\n" \
".endr\n" \
".LP3_b4:\n" \
"buffer_load_dwordx4 v[56:59], v210, s[4:7], s47 offen sc1\n" \
"buffer_load_dwordx4 v[60:63], v210, s[4:7], s47 offen offset:256 sc1\n" \
"v_smfmac_f32_16x16x64_bf16 v[28:31], v[16:19], a[32:39], v200\n" \
"v_smfmac_f32_16x16x64_bf16 v[32:35], v[16:19], a[88:95], v200\n" \
"v_smfmac_f32_16x16x64_bf16 v[36:39], v[16:19], a[144:151], v200\n" \
"v_smfmac_f32_16x16x64_bf16 v[40:43], v[16:19], a[200:207], v200\n" \
"v_smfmac_f32_16x16x64_bf16 v[28:31], v[20:23], a[40:47], v200\n" \
"v_smfmac_f32_16x16x64_bf16 v[32:35], v[20:23], a[96:103], v200\n" \
"buffer_load_dwordx4 v[50:53], v212, s[4:7], s47 offen sc1\n" \
"v_smfmac_f32_16x16x64_bf16 v[36:39], v[20:23], a[152:159], v200\n" \
"v_smfmac_f32_16x16x64_bf16 v[40:43], v[20:23], a[208:215], v200\n" \
"v_smfmac_f32_16x16x64_bf16 v[28:31], v[24:27], a[48:55], v200\n" \
"v_smfmac_f32_16x16x64_bf16 v[32:35], v[24:27], a[104:111], v200\n" \
"v_smfmac_f32_16x16x64_bf16 v[36:39], v[24:27], a[160:167], v200\n" \
"v_smfmac_f32_16x16x64_bf16 v[40:43], v[24:27], a[216:223], v200\n" \
"s_nop 2\n" \
"v_mov_b32 v245, v245\n" \
"v_mov_b32 v245, v245\n" \
"v_mov_b32 v245, v245\n" \
"v_permlane32_swap_b32 v28, v32\n" \
"v_permlane32_swap_b32 v29, v33\n" \
"v_permlane32_swap_b32 v30, v34\n" \
"v_permlane32_swap_b32 v31, v35\n" \
"v_add_f32 v28, v28, v32\n" \
"v_add_f32 v29, v29, v33\n" \
"v_add_f32 v30, v30, v34\n" \
"v_add_f32 v31, v31, v35\n" \
"v_cvt_pk_bf16_f32 v68, v28, v29\n" \
"v_cvt_pk_bf16_f32 v69, v30, v31\n" \
"v_permlane32_swap_b32 v36, v40\n" \
"v_permlane32_swap_b32 v37, v41\n" \
"v_permlane32_swap_b32 v38, v42\n" \
"v_permlane32_swap_b32 v39, v43\n" \
"v_add_f32 v36, v36, v40\n" \
"v_add_f32 v37, v37, v41\n" \
"v_add_f32 v38, v38, v42\n" \
"v_add_f32 v39, v39, v43\n" \
"v_cvt_pk_bf16_f32 v70, v36, v37\n" \
"v_cvt_pk_bf16_f32 v71, v38, v39\n" \
"v_mov_b32 v48, v68\n" \
"v_mov_b32 v49, v69\n" \
"v_mov_b32 v54, v70\n" \
"v_mov_b32 v55, v71\n" \
"s_nop 1\n" \
"v_permlane16_swap_b32 v48, v54\n" \
"v_permlane16_swap_b32 v49, v55\n" \
".LP3_wait:\n" \
"s_waitcnt vmcnt(1)\n" \
"v_pk_add_f32 v[72:73], v[56:57], v[60:61]\n" \
"v_pk_add_f32 v[74:75], v[58:59], v[62:63]\n" \
"v_max3_f32 v77, v72, v73, v64\n" \
"v_max3_f32 v77, v77, v74, v75\n" \
"s_nop 1\n" \
"v_max_f32_dpp v76, v77, v77 row_ror:8 row_mask:0xf bank_mask:0xf\n" \
"v_pk_add_f32 v[72:73], v[72:73], v[76:77] op_sel_hi:[1,0] neg_lo:[0,1] neg_hi:[0,1]\n" \
"v_pk_add_f32 v[74:75], v[74:75], v[76:77] op_sel_hi:[1,0] neg_lo:[0,1] neg_hi:[0,1]\n" \
"v_sub_f32 v78, v64, v76\n" \
"v_exp_f32 v72, v72\n" \
"v_exp_f32 v73, v73\n" \
"v_exp_f32 v74, v74\n" \
"v_exp_f32 v75, v75\n" \
"v_exp_f32 v78, v78\n" \
"v_pk_add_f32 v[80:81], v[72:73], v[74:75]\n" \
"v_fma_f32 v80, v78, v65, v80\n" \
"v_add_f32 v81, v80, v81\n" \
"s_nop 1\n" \
"v_add_f32_dpp v80, v81, v81 row_ror:8 row_mask:0xf bank_mask:0xf\n" \
"v_rcp_f32 v80, v80\n" \
"s_nop 0\n" \
"v_mul_f32 v78, v78, v80\n" \
"v_pk_mul_f32 v[72:73], v[72:73], v[80:81] op_sel_hi:[1,0]\n" \
"v_pk_mul_f32 v[74:75], v[74:75], v[80:81] op_sel_hi:[1,0]\n" \
"v_cvt_pk_bf16_f32 v78, v78, v78\n" \
"v_cvt_pk_bf16_f32 v82, v72, v73\n" \
"v_cvt_pk_bf16_f32 v83, v74, v75\n" \
"v_and_b32 v84, v78, v214\n" \
"v_and_b32 v85, v78, v215\n" \
"v_cvt_pk_bf16_f32 v86, v72, v73\n" \
"v_cvt_pk_bf16_f32 v87, v74, v75\n" \
"s_waitcnt vmcnt(0)\n" \
"v_mfma_f32_16x16x32_bf16 v[88:91], v[48:51], v[82:85], 0\n" \
"v_mfma_f32_16x16x32_bf16 v[92:95], v[52:55], v[84:87], 0\n" \
"s_nop 2\n" \
"v_mov_b32 v245, v245\n" \
"s_nop 2\n" \
"v_mov_b32 v245, v245\n" \
"v_add_f32_dpp v96, v92, v88 row_ror:8 row_mask:0xf bank_mask:0x3\n" \
"v_add_f32_dpp v97, v93, v89 row_ror:8 row_mask:0xf bank_mask:0x3\n" \
"v_add_f32_dpp v96, v90, v94 row_ror:8 row_mask:0xf bank_mask:0xc\n" \
"v_add_f32_dpp v97, v91, v95 row_ror:8 row_mask:0xf bank_mask:0xc\n" \
"ds_write_b64 v216, v[96:97]\n" \
"v_cmp_u_f32 vcc, v96, v97\n" \
"s_bitcmp1_b32 s31, 21\n" \
"v_mov_b32 v245, v245\n" \
"s_cselect_b32 s18, s29, 0\n" \
"s_xor_b32 s18, s18, s48\n" \
"s_xor_b32 s19, s18, s29\n" \
"v_mov_b32 v245, v245\n" \
"s_nop 0\n" \
"s_nop 0\n" \
"s_nop 0\n" \
"v_mov_b32 v245, v245\n" \
"s_cbranch_vccnz .LP3_cls\n" \
".LP3_clsr:\n" \
"s_waitcnt lgkmcnt(0)\n" \
"s_barrier\n" \
"ds_read_b64 v[100:101], v217 offset:0\n" \
"v_mov_b32 v245, v245\n" \
"ds_read_b64 v[102:103], v217 offset:512\n" \
"ds_read_b32 v116, v252 offset:16\n" \
"ds_read_b64 v[104:105], v217 offset:1024\n" \
"ds_read_b64 v[106:107], v217 offset:1536\n" \
"s_add_u32 s40, s40, 1\n" \
"s_add_u32 s24, s24, s27\n" \
"v_mov_b32 v245, v245\n" \
"s_waitcnt lgkmcnt(2)\n" \
"v_xad_u32 v117, v116, s18, v213\n" \
"v_pk_add_f32 v[100:101], v[100:101], v[102:103]\n" \
"s_waitcnt lgkmcnt(0)\n" \
"v_pk_add_f32 v[104:105], v[104:105], v[106:107]\n" \
"v_pk_add_f32 v[100:101], v[100:101], v[104:105]\n" \
"v_cvt_pk_bf16_f32 v96, v100, v101\n" \
"buffer_store_dword v96, v117, s[4:7], 0 offen sc1\n" \
"s_mov_b32 s47, s48\n" \
"s_add_u32 s43, s40, 1\n" \
"s_and_b32 s43, s43, 3\n" \
"s_bitcmp1_b32 s43, 0\n" \
"s_cselect_b32 s48, s32, s31\n" \
"s_lshl_b32 s43, s43, 19\n" \
"s_add_u32 s48, s48, s43\n" \
"s_add_u32 s43, s40, 2\n" \
"s_and_b32 s43, s43, 3\n" \
"s_bitcmp1_b32 s43, 0\n" \
"s_cselect_b32 s49, s32, s31\n" \
"s_lshl_b32 s43, s43, 19\n" \
"s_add_u32 s49, s49, s43\n" \
"s_sub_u32 s43, s24, s44\n" \
"s_add_u32 s43, s43, s28\n" \
"s_max_i32 s43, s43, 0xfffd0000\n" \
"s_min_i32 s43, s43, 0x4000\n" \
"s_mul_i32 s43, s43, s46\n" \
"s_ashr_i32 s43, s43, 12\n" \
"s_add_u32 s41, s45, s43\n" \
"s_lshr_b32 s35, s42, 1\n" \
"s_sub_u32 s35, 0, s35\n" \
"v_xad_u32 v116, v116, s19, v213\n" \
"buffer_store_dword v96, v116, s[4:7], 0 offen sc1\n" \
"s_cmp_eq_u32 s40, 0\n" \
"s_cbranch_scc1 .LP3_wet\n" \
"s_branch .LP3_loop\n" \
"s_branch .LP3_y1\n" \
".LP3_retry2:\n" \
"s_add_u32 s51, s51, 1\n" \
"s_cmp_gt_u32 s51, 0x20000\n" \
"s_cbranch_scc1 .LP3_end\n" \
".LP3_y0:\n" \
"s_add_u32 s41, s41, s42\n" \
"s_add_u32 s24, s24, s25\n" \
"s_xor_b32 s31, s31, s29\n" \
"s_xor_b32 s32, s32, s29\n" \
"s_xor_b32 s47, s47, s29\n" \
"s_xor_b32 s48, s48, s29\n" \
"s_add_u32 s39, s39, 1\n" \
"buffer_store_dwordx4 v[112:115], v223, s[4:7], s48 offen sc1\n" \
"s_bitcmp1_b32 s31, 21\n" \
"s_cselect_b32 s43, s29, 0\n" \
"v_mov_b32 v116, s43\n" \
"ds_write_b32 v252, v116 offset:16\n" \
".LP3_y1:\n" \
"s_memtime vcc\n" \
"s_waitcnt lgkmcnt(0)\n" \
"s_sub_u32 s43, s41, vcc_lo\n" \
"s_cmp_gt_i32 s43, 200\n" \
"s_cbranch_scc1 .LP3_y1\n" \
"s_cmp_lt_i32 s43, s37\n" \
"s_cbranch_scc1 .LP3_y0\n" \
"buffer_load_dwordx2 v[64:65], v211, s[4:7], s47 offen sc1\n" \
".p2align 6\n" \
".LP3_c1:\n" \
"s_memtime vcc\n" \
"s_waitcnt lgkmcnt(0)\n" \
"s_sub_u32 s43, s41, vcc_lo\n" \
"s_cmp_gt_i32 s43, 128\n" \
"s_cbranch_scc1 .LP3_c1\n" \
"s_cmp_lt_i32 s43, s37\n" \
"s_cbranch_scc1 .LP3_y0\n" \
"s_max_i32 s43, s43, 0\n" \
"s_and_b32 s43, s43, -4\n" \
"s_getpc_b64 vcc\n" \
".LP3_c3:\n" \
"s_sub_u32 s43, .LP3_c4-.LP3_c3, s43\n" \
"s_add_u32 vcc_lo, vcc_lo, s43\n" \
"s_addc_u32 vcc_hi, vcc_hi, 0\n" \
"s_setpc_b64 vcc\n" \
".rept 32\n" \
"s_nop 0\n" \
".endr\n" \
".LP3_c4:\n" \
"buffer_load_dwordx4 v[56:59], v210, s[4:7], s47 offen sc1\n" \
"buffer_load_dwordx4 v[60:63], v210, s[4:7], s47 offen offset:256 sc1\n" \
"s_nop 2\n" \
"v_mov_b32 v245, v245\n" \
"s_nop 2\n" \
"v_mov_b32 v245, v245\n" \
"s_nop 2\n" \
"v_mov_b32 v245, v245\n" \
"s_nop 2\n" \
"v_mov_b32 v245, v245\n" \
"s_nop 2\n" \
"v_mov_b32 v245, v245\n" \
"s_nop 2\n" \
"v_mov_b32 v245, v245\n" \
"buffer_load_dwordx4 v[50:53], v212, s[4:7], s47 offen sc1\n" \
"s_nop 2\n" \
"v_mov_b32 v245, v245\n" \
"s_nop 2\n" \
"v_mov_b32 v245, v245\n" \
"s_nop 2\n" \
"v_mov_b32 v245, v245\n" \
"s_nop 2\n" \
"v_mov_b32 v245, v245\n" \
"s_nop 2\n" \
"v_mov_b32 v245, v245\n" \
"s_nop 2\n" \
"v_mov_b32 v245, v245\n" \
"s_nop 2\n" \
"v_mov_b32 v245, v245\n" \
"s_nop 2\n" \
"v_mov_b32 v245, v245\n" \
"s_nop 2\n" \
"v_mov_b32 v245, v245\n" \
"s_nop 2\n" \
"v_mov_b32 v245, v245\n" \
"s_nop 2\n" \
"v_mov_b32 v245, v245\n" \
"s_nop 2\n" \
"v_mov_b32 v245, v245\n" \
"s_nop 2\n" \
"v_mov_b32 v245, v245\n" \
"s_nop 2\n" \
"v_mov_b32 v245, v245\n" \
"s_nop 2\n" \
"v_mov_b32 v245, v245\n" \
"s_nop 2\n" \
"v_mov_b32 v245, v245\n" \
"s_nop 2\n" \
"v_mov_b32 v245, v245\n" \
"s_branch .LP3_wait\n" \
".LP3_cls:\n" \
"s_waitcnt vmcnt(0)\n" \
"v_max3_u32 v44, v64, v64, v65\n" \
"v_max3_u32 v44, v44, v50, v51\n" \
"v_max3_u32 v44, v44, v52, v53\n" \
"v_cmp_eq_u32 vcc, -1, v44\n" \
"s_cbranch_vccnz .LP3_retry2\n" \
"v_cmp_u_f32 vcc, v56, v56\n" \
"v_cmp_ne_u32 s[52:53], v56, v109\n" \
"s_and_b64 vcc, vcc, s[52:53]\n" \
"s_cbranch_vccnz .LP3_retry2\n" \
"v_cmp_u_f32 vcc, v57, v57\n" \
"v_cmp_ne_u32 s[52:53], v57, v109\n" \
"s_and_b64 vcc, vcc, s[52:53]\n" \
"s_cbranch_vccnz .LP3_retry2\n" \
"v_cmp_u_f32 vcc, v58, v58\n" \
"v_cmp_ne_u32 s[52:53], v58, v109\n" \
"s_and_b64 vcc, vcc, s[52:53]\n" \
"s_cbranch_vccnz .LP3_retry2\n" \
"v_cmp_u_f32 vcc, v59, v59\n" \
"v_cmp_ne_u32 s[52:53], v59, v109\n" \
"s_and_b64 vcc, vcc, s[52:53]\n" \
"s_cbranch_vccnz .LP3_retry2\n" \
"v_cmp_u_f32 vcc, v60, v60\n" \
"v_cmp_ne_u32 s[52:53], v60, v109\n" \
"s_and_b64 vcc, vcc, s[52:53]\n" \
"s_cbranch_vccnz .LP3_retry2\n" \
"v_cmp_u_f32 vcc, v61, v61\n" \
"v_cmp_ne_u32 s[52:53], v61, v109\n" \
"s_and_b64 vcc, vcc, s[52:53]\n" \
"s_cbranch_vccnz .LP3_retry2\n" \
"v_cmp_u_f32 vcc, v62, v62\n" \
"v_cmp_ne_u32 s[52:53], v62, v109\n" \
"s_and_b64 vcc, vcc, s[52:53]\n" \
"s_cbranch_vccnz .LP3_retry2\n" \
"v_cmp_u_f32 vcc, v63, v63\n" \
"v_cmp_ne_u32 s[52:53], v63, v109\n" \
"s_and_b64 vcc, vcc, s[52:53]\n" \
"s_cbranch_vccnz .LP3_retry2\n" \
"s_branch .LP3_clsr\n" \
".LP3_last:\n" \
"v_smfmac_f32_16x16x64_bf16 v[28:31], v[16:19], a[32:39], v200\n" \
"v_smfmac_f32_16x16x64_bf16 v[32:35], v[16:19], a[88:95], v200\n" \
"v_smfmac_f32_16x16x64_bf16 v[36:39], v[16:19], a[144:151], v200\n" \
"v_smfmac_f32_16x16x64_bf16 v[40:43], v[16:19], a[200:207], v200\n" \
"v_smfmac_f32_16x16x64_bf16 v[28:31], v[20:23], a[40:47], v200\n" \
"v_smfmac_f32_16x16x64_bf16 v[32:35], v[20:23], a[96:103], v200\n" \
"v_smfmac_f32_16x16x64_bf16 v[36:39], v[20:23], a[152:159], v200\n" \
"v_smfmac_f32_16x16x64_bf16 v[40:43], v[20:23], a[208:215], v200\n" \
"v_smfmac_f32_16x16x64_bf16 v[28:31], v[24:27], a[48:55], v200\n" \
"v_smfmac_f32_16x16x64_bf16 v[32:35], v[24:27], a[104:111], v200\n" \
"v_smfmac_f32_16x16x64_bf16 v[36:39], v[24:27], a[160:167], v200\n" \
"v_smfmac_f32_16x16x64_bf16 v[40:43], v[24:27], a[216:223], v200\n" \
"s_waitcnt vmcnt(0)\n" \
"s_nop 7\n" \
"v_permlane32_swap_b32 v28, v32\n" \
"v_permlane32_swap_b32 v29, v33\n" \
"v_permlane32_swap_b32 v30, v34\n" \
"v_permlane32_swap_b32 v31, v35\n" \
"v_add_f32 v28, v28, v32\n" \
"v_add_f32 v29, v29, v33\n" \
"v_add_f32 v30, v30, v34\n" \
"v_add_f32 v31, v31, v35\n" \
"v_cvt_pk_bf16_f32 v68, v28, v29\n" \
"v_cvt_pk_bf16_f32 v69, v30, v31\n" \
"v_permlane32_swap_b32 v36, v40\n" \
"v_permlane32_swap_b32 v37, v41\n" \
"v_permlane32_swap_b32 v38, v42\n" \
"v_permlane32_swap_b32 v39, v43\n" \
"v_add_f32 v36, v36, v40\n" \
"v_add_f32 v37, v37, v41\n" \
"v_add_f32 v38, v38, v42\n" \
"v_add_f32 v39, v39, v43\n" \
"v_cvt_pk_bf16_f32 v70, v36, v37\n" \
"v_cvt_pk_bf16_f32 v71, v38, v39\n" \
"s_mov_b32 s50, 262144\n" \
"buffer_store_dwordx4 v[68:71], v218, s[12:15], s50 offen sc1\n" \
"s_waitcnt vmcnt(0)\n" \
"s_barrier\n" \
".LP3_end:\n" \
"s_endpgm\n" \
".p2align 6\n" \
"fk_role_X0:\n" \
"ds_read_b32 v200, v255 offset:0\n" \
"ds_read_b32 v201, v255 offset:256\n" \
"ds_read_b32 v202, v255 offset:512\n" \
"ds_read_b32 v203, v255 offset:768\n" \
"ds_read_b32 v204, v255 offset:1024\n" \
"ds_read_b32 v205, v255 offset:1280\n" \
"ds_read_b32 v206, v255 offset:1536\n" \
"ds_read_b32 v207, v255 offset:1792\n" \
"ds_read_b32 v208, v255 offset:2048\n" \
"ds_read_b32 v209, v255 offset:2304\n" \
"ds_read_b32 v210, v255 offset:2560\n" \
"ds_read_b32 v211, v255 offset:2816\n" \
"ds_read_b32 v212, v255 offset:3072\n" \
"ds_read_b32 v213, v255 offset:3328\n" \
"ds_read_b32 v214, v255 offset:3584\n" \
"ds_read_b32 v215, v255 offset:3840\n" \
"ds_read_b32 v216, v255 offset:4096\n" \
"ds_read_b32 v217, v255 offset:4352\n" \
"ds_read_b32 v218, v255 offset:4608\n" \
"ds_read_b32 v219, v255 offset:4864\n" \
"ds_read_b32 v220, v255 offset:5120\n" \
"ds_read_b32 v221, v255 offset:5376\n" \
"ds_read_b32 v254, v255 offset:7936\n" \
"s_waitcnt lgkmcnt(0)\n" \
"v_readlane_b32 s4, v254, 0\n" \
"v_readlane_b32 s5, v254, 1\n" \
"v_readlane_b32 s6, v254, 2\n" \
"v_readlane_b32 s7, v254, 3\n" \
"v_readlane_b32 s8, v254, 4\n" \
"v_readlane_b32 s9, v254, 5\n" \
"v_readlane_b32 s10, v254, 6\n" \
"v_readlane_b32 s11, v254, 7\n" \
"v_readlane_b32 s12, v254, 8\n" \
"v_readlane_b32 s13, v254, 9\n" \
"v_readlane_b32 s14, v254, 10\n" \
"v_readlane_b32 s15, v254, 11\n" \
"v_readlane_b32 s16, v254, 12\n" \
"v_readlane_b32 s17, v254, 13\n" \
"v_readlane_b32 s18, v254, 14\n" \
"v_readlane_b32 s19, v254, 15\n" \
"v_readlane_b32 s20, v254, 16\n" \
"v_readlane_b32 s21, v254, 17\n" \
"v_readlane_b32 s22, v254, 18\n" \
"v_readlane_b32 s23, v254, 19\n" \
"v_readlane_b32 s24, v254, 20\n" \
"v_readlane_b32 s25, v254, 21\n" \
"v_readlane_b32 s26, v254, 22\n" \
"v_readlane_b32 s27, v254, 23\n" \
"v_readlane_b32 s28, v254, 24\n" \
"v_readlane_b32 s29, v254, 25\n" \
"v_readlane_b32 s30, v254, 26\n" \
"v_readlane_b32 s31, v254, 27\n" \
"v_readlane_b32 s32, v254, 28\n" \
"v_readlane_b32 s33, v254, 29\n" \
"v_readlane_b32 s34, v254, 30\n" \
"v_readlane_b32 s35, v254, 31\n" \
"v_readlane_b32 s36, v254, 32\n" \
"v_readlane_b32 s37, v254, 33\n" \
"v_readlane_b32 s38, v254, 34\n" \
"v_readlane_b32 s39, v254, 35\n" \
"s_mov_b32 s40, 0\n" \
"s_mov_b32 s51, 0\n" \
"s_nop 0\n" \
"v_mov_b32 v252, s38\n" \
"s_nop 4\n" \
"v_mov_b32 v100, -1\n" \
"v_mov_b32 v101, -1\n" \
"v_mov_b32 v102, -1\n" \
"v_mov_b32 v103, -1\n" \
"s_mov_b32 s35, -2\n" \
"s_mov_b32 s2, 0\n" \
"s_mov_b32 s3, -1\n" \
"v_writelane_b32 v251, s6, 0\n" \
"v_writelane_b32 v251, s10, 1\n" \
"v_writelane_b32 v251, s14, 2\n" \
"v_writelane_b32 v251, s18, 3\n" \
"s_mov_b32 s6, 0\n" \
"s_mov_b32 s10, 0\n" \
"s_mov_b32 s14, 0\n" \
"s_mov_b32 s18, 0\n" \
"s_mov_b32 s40, -1\n" \
"s_branch .LX0_pre\n" \
".LX0_wet:\n" \
"s_waitcnt vmcnt(0)\n" \
"v_readlane_b32 s6, v251, 0\n" \
"v_readlane_b32 s10, v251, 1\n" \
"v_readlane_b32 s14, v251, 2\n" \
"v_readlane_b32 s18, v251, 3\n" \
"s_nop 4\n" \
"s_add_u32 s43, s30, 1\n" \
"s_mov_b32 s50, 0\n" \
".LX0_w54l:\n" \
"buffer_load_dword v240, v204, s[4:7], 0 offen sc1\n" \
"s_waitcnt vmcnt(0)\n" \
".LX0_w54c:\n" \
"v_subrev_u32 v241, s43, v240\n" \
"v_cmp_gt_u32 vcc, 0x100, v241\n" \
"s_cmp_eq_u64 vcc, -1\n" \
"s_cbranch_scc1 .LX0_w54d\n" \
"s_sleep 2\n" \
"s_add_u32 s50, s50, 1\n" \
"s_cmp_lt_u32 s50, 0x20000\n" \
"s_cbranch_scc1 .LX0_w54l\n" \
"s_branch .LX0_end\n" \
".LX0_w54d:\n" \
"v_readlane_b32 s43, v202, 0\n" \
"s_nop 4\n" \
"buffer_load_dwordx4 a[0:3], v201, s[8:11], s43 offen sc1\n" \
"buffer_load_dwordx4 a[4:7], v201, s[8:11], s43 offen offset:1024 sc1\n" \
"v_readlane_b32 s43, v202, 7\n" \
"s_nop 4\n" \
"buffer_load_dwordx4 a[56:59], v201, s[8:11], s43 offen sc1\n" \
"buffer_load_dwordx4 a[60:63], v201, s[8:11], s43 offen offset:1024 sc1\n" \
"v_readlane_b32 s43, v202, 14\n" \
"s_nop 4\n" \
"buffer_load_dwordx4 a[112:115], v201, s[8:11], s43 offen sc1\n" \
"buffer_load_dwordx4 a[116:119], v201, s[8:11], s43 offen offset:1024 sc1\n" \
"v_readlane_b32 s43, v202, 21\n" \
"s_nop 4\n" \
"buffer_load_dwordx4 a[168:171], v201, s[8:11], s43 offen sc1\n" \
"buffer_load_dwordx4 a[172:175], v201, s[8:11], s43 offen offset:1024 sc1\n" \
"s_add_u32 s43, s30, 2\n" \
"s_mov_b32 s50, 0\n" \
"s_branch .LX0_w55c\n" \
".LX0_w55l:\n" \
"buffer_load_dword v240, v204, s[4:7], 0 offen sc1\n" \
"s_waitcnt vmcnt(0)\n" \
".LX0_w55c:\n" \
"v_subrev_u32 v241, s43, v240\n" \
"v_cmp_gt_u32 vcc, 0x100, v241\n" \
"s_cmp_eq_u64 vcc, -1\n" \
"s_cbranch_scc1 .LX0_w55d\n" \
"s_sleep 2\n" \
"s_add_u32 s50, s50, 1\n" \
"s_cmp_lt_u32 s50, 0x20000\n" \
"s_cbranch_scc1 .LX0_w55l\n" \
"s_branch .LX0_end\n" \
".LX0_w55d:\n" \
"v_readlane_b32 s43, v202, 4\n" \
"s_nop 4\n" \
"buffer_load_dwordx4 a[32:35], v201, s[8:11], s43 offen sc1\n" \
"buffer_load_dwordx4 a[36:39], v201, s[8:11], s43 offen offset:1024 sc1\n" \
"v_readlane_b32 s43, v202, 11\n" \
"s_nop 4\n" \
"buffer_load_dwordx4 a[88:91], v201, s[8:11], s43 offen sc1\n" \
"buffer_load_dwordx4 a[92:95], v201, s[8:11], s43 offen offset:1024 sc1\n" \
"v_readlane_b32 s43, v202, 18\n" \
"s_nop 4\n" \
"buffer_load_dwordx4 a[144:147], v201, s[8:11], s43 offen sc1\n" \
"buffer_load_dwordx4 a[148:151], v201, s[8:11], s43 offen offset:1024 sc1\n" \
"v_readlane_b32 s43, v202, 25\n" \
"s_nop 4\n" \
"buffer_load_dwordx4 a[200:203], v201, s[8:11], s43 offen sc1\n" \
"buffer_load_dwordx4 a[204:207], v201, s[8:11], s43 offen offset:1024 sc1\n" \
"s_add_u32 s43, s30, 3\n" \
"s_mov_b32 s50, 0\n" \
"s_branch .LX0_w56c\n" \
".LX0_w56l:\n" \
"buffer_load_dword v240, v204, s[4:7], 0 offen sc1\n" \
"s_waitcnt vmcnt(0)\n" \
".LX0_w56c:\n" \
"v_subrev_u32 v241, s43, v240\n" \
"v_cmp_gt_u32 vcc, 0x100, v241\n" \
"s_cmp_eq_u64 vcc, -1\n" \
"s_cbranch_scc1 .LX0_w56d\n" \
"s_sleep 2\n" \
"s_add_u32 s50, s50, 1\n" \
"s_cmp_lt_u32 s50, 0x20000\n" \
"s_cbranch_scc1 .LX0_w56l\n" \
"s_branch .LX0_end\n" \
".LX0_w56d:\n" \
"v_readlane_b32 s43, v202, 1\n" \
"s_nop 4\n" \
"buffer_load_dwordx4 a[8:11], v201, s[8:11], s43 offen sc1\n" \
"buffer_load_dwordx4 a[12:15], v201, s[8:11], s43 offen offset:1024 sc1\n" \
"v_readlane_b32 s43, v202, 8\n" \
"s_nop 4\n" \
"buffer_load_dwordx4 a[64:67], v201, s[8:11], s43 offen sc1\n" \
"buffer_load_dwordx4 a[68:71], v201, s[8:11], s43 offen offset:1024 sc1\n" \
"v_readlane_b32 s43, v202, 15\n" \
"s_nop 4\n" \
"buffer_load_dwordx4 a[120:123], v201, s[8:11], s43 offen sc1\n" \
"buffer_load_dwordx4 a[124:127], v201, s[8:11], s43 offen offset:1024 sc1\n" \
"v_readlane_b32 s43, v202, 22\n" \
"s_nop 4\n" \
"buffer_load_dwordx4 a[176:179], v201, s[8:11], s43 offen sc1\n" \
"buffer_load_dwordx4 a[180:183], v201, s[8:11], s43 offen offset:1024 sc1\n" \
"s_add_u32 s43, s30, 4\n" \
"s_mov_b32 s50, 0\n" \
"s_branch .LX0_w57c\n" \
".LX0_w57l:\n" \
"buffer_load_dword v240, v204, s[4:7], 0 offen sc1\n" \
"s_waitcnt vmcnt(0)\n" \
".LX0_w57c:\n" \
"v_subrev_u32 v241, s43, v240\n" \
"v_cmp_gt_u32 vcc, 0x100, v241\n" \
"s_cmp_eq_u64 vcc, -1\n" \
"s_cbranch_scc1 .LX0_w57d\n" \
"s_sleep 2\n" \
"s_add_u32 s50, s50, 1\n" \
"s_cmp_lt_u32 s50, 0x20000\n" \
"s_cbranch_scc1 .LX0_w57l\n" \
"s_branch .LX0_end\n" \
".LX0_w57d:\n" \
"v_readlane_b32 s43, v202, 5\n" \
"s_nop 4\n" \
"buffer_load_dwordx4 a[40:43], v201, s[8:11], s43 offen sc1\n" \
"buffer_load_dwordx4 a[44:47], v201, s[8:11], s43 offen offset:1024 sc1\n" \
"v_readlane_b32 s43, v202, 12\n" \
"s_nop 4\n" \
"buffer_load_dwordx4 a[96:99], v201, s[8:11], s43 offen sc1\n" \
"buffer_load_dwordx4 a[100:103], v201, s[8:11], s43 offen offset:1024 sc1\n" \
"v_readlane_b32 s43, v202, 19\n" \
"s_nop 4\n" \
"buffer_load_dwordx4 a[152:155], v201, s[8:11], s43 offen sc1\n" \
"buffer_load_dwordx4 a[156:159], v201, s[8:11], s43 offen offset:1024 sc1\n" \
"v_readlane_b32 s43, v202, 26\n" \
"s_nop 4\n" \
"buffer_load_dwordx4 a[208:211], v201, s[8:11], s43 offen sc1\n" \
"buffer_load_dwordx4 a[212:215], v201, s[8:11], s43 offen offset:1024 sc1\n" \
"s_add_u32 s43, s30, 5\n" \
"s_mov_b32 s50, 0\n" \
"s_branch .LX0_w58c\n" \
".LX0_w58l:\n" \
"buffer_load_dword v240, v204, s[4:7], 0 offen sc1\n" \
"s_waitcnt vmcnt(0)\n" \
".LX0_w58c:\n" \
"v_subrev_u32 v241, s43, v240\n" \
"v_cmp_gt_u32 vcc, 0x100, v241\n" \
"s_cmp_eq_u64 vcc, -1\n" \
"s_cbranch_scc1 .LX0_w58d\n" \
"s_sleep 2\n" \
"s_add_u32 s50, s50, 1\n" \
"s_cmp_lt_u32 s50, 0x20000\n" \
"s_cbranch_scc1 .LX0_w58l\n" \
"s_branch .LX0_end\n" \
".LX0_w58d:\n" \
"v_readlane_b32 s43, v202, 2\n" \
"s_nop 4\n" \
"buffer_load_dwordx4 a[16:19], v201, s[8:11], s43 offen sc1\n" \
"buffer_load_dwordx4 a[20:23], v201, s[8:11], s43 offen offset:1024 sc1\n" \
"v_readlane_b32 s43, v202, 9\n" \
"s_nop 4\n" \
"buffer_load_dwordx4 a[72:75], v201, s[8:11], s43 offen sc1\n" \
"buffer_load_dwordx4 a[76:79], v201, s[8:11], s43 offen offset:1024 sc1\n" \
"v_readlane_b32 s43, v202, 16\n" \
"s_nop 4\n" \
"buffer_load_dwordx4 a[128:131], v201, s[8:11], s43 offen sc1\n" \
"buffer_load_dwordx4 a[132:135], v201, s[8:11], s43 offen offset:1024 sc1\n" \
"v_readlane_b32 s43, v202, 23\n" \
"s_nop 4\n" \
"buffer_load_dwordx4 a[184:187], v201, s[8:11], s43 offen sc1\n" \
"buffer_load_dwordx4 a[188:191], v201, s[8:11], s43 offen offset:1024 sc1\n" \
"s_add_u32 s43, s30, 6\n" \
"s_mov_b32 s50, 0\n" \
"s_branch .LX0_w59c\n" \
".LX0_w59l:\n" \
"buffer_load_dword v240, v204, s[4:7], 0 offen sc1\n" \
"s_waitcnt vmcnt(0)\n" \
".LX0_w59c:\n" \
"v_subrev_u32 v241, s43, v240\n" \
"v_cmp_gt_u32 vcc, 0x100, v241\n" \
"s_cmp_eq_u64 vcc, -1\n" \
"s_cbranch_scc1 .LX0_w59d\n" \
"s_sleep 2\n" \
"s_add_u32 s50, s50, 1\n" \
"s_cmp_lt_u32 s50, 0x20000\n" \
"s_cbranch_scc1 .LX0_w59l\n" \
"s_branch .LX0_end\n" \
".LX0_w59d:\n" \
"v_readlane_b32 s43, v202, 6\n" \
"s_nop 4\n" \
"buffer_load_dwordx4 a[48:51], v201, s[8:11], s43 offen sc1\n" \
"buffer_load_dwordx4 a[52:55], v201, s[8:11], s43 offen offset:1024 sc1\n" \
"v_readlane_b32 s43, v202, 13\n" \
"s_nop 4\n" \
"buffer_load_dwordx4 a[104:107], v201, s[8:11], s43 offen sc1\n" \
"buffer_load_dwordx4 a[108:111], v201, s[8:11], s43 offen offset:1024 sc1\n" \
"v_readlane_b32 s43, v202, 20\n" \
"s_nop 4\n" \
"buffer_load_dwordx4 a[160:163], v201, s[8:11], s43 offen sc1\n" \
"buffer_load_dwordx4 a[164:167], v201, s[8:11], s43 offen offset:1024 sc1\n" \
"v_readlane_b32 s43, v202, 27\n" \
"s_nop 4\n" \
"buffer_load_dwordx4 a[216:219], v201, s[8:11], s43 offen sc1\n" \
"buffer_load_dwordx4 a[220:223], v201, s[8:11], s43 offen offset:1024 sc1\n" \
"s_add_u32 s43, s30, 7\n" \
"s_mov_b32 s50, 0\n" \
"s_branch .LX0_w60c\n" \
".LX0_w60l:\n" \
"buffer_load_dword v240, v204, s[4:7], 0 offen sc1\n" \
"s_waitcnt vmcnt(0)\n" \
".LX0_w60c:\n" \
"v_subrev_u32 v241, s43, v240\n" \
"v_cmp_gt_u32 vcc, 0x100, v241\n" \
"s_cmp_eq_u64 vcc, -1\n" \
"s_cbranch_scc1 .LX0_w60d\n" \
"s_sleep 2\n" \
"s_add_u32 s50, s50, 1\n" \
"s_cmp_lt_u32 s50, 0x20000\n" \
"s_cbranch_scc1 .LX0_w60l\n" \
"s_branch .LX0_end\n" \
".LX0_w60d:\n" \
"v_readlane_b32 s43, v202, 3\n" \
"s_nop 4\n" \
"buffer_load_dwordx4 a[24:27], v201, s[8:11], s43 offen sc1\n" \
"buffer_load_dwordx4 a[28:31], v201, s[8:11], s43 offen offset:1024 sc1\n" \
"v_readlane_b32 s43, v202, 10\n" \
"s_nop 4\n" \
"buffer_load_dwordx4 a[80:83], v201, s[8:11], s43 offen sc1\n" \
"buffer_load_dwordx4 a[84:87], v201, s[8:11], s43 offen offset:1024 sc1\n" \
"v_readlane_b32 s43, v202, 17\n" \
"s_nop 4\n" \
"buffer_load_dwordx4 a[136:139], v201, s[8:11], s43 offen sc1\n" \
"buffer_load_dwordx4 a[140:143], v201, s[8:11], s43 offen offset:1024 sc1\n" \
"v_readlane_b32 s43, v202, 24\n" \
"s_nop 4\n" \
"buffer_load_dwordx4 a[192:195], v201, s[8:11], s43 offen sc1\n" \
"buffer_load_dwordx4 a[196:199], v201, s[8:11], s43 offen offset:1024 sc1\n" \
"s_add_u32 s43, s30, 1\n" \
"s_mov_b32 s50, 0\n" \
".LX0_w61l:\n" \
"buffer_load_dword v240, v205, s[4:7], 0 offen sc1\n" \
"s_waitcnt vmcnt(0)\n" \
".LX0_w61c:\n" \
"v_subrev_u32 v241, s43, v240\n" \
"v_cmp_gt_u32 vcc, 0x100, v241\n" \
"s_cmp_eq_u64 vcc, -1\n" \
"s_cbranch_scc1 .LX0_w61d\n" \
"s_sleep 2\n" \
"s_add_u32 s50, s50, 1\n" \
"s_cmp_lt_u32 s50, 0x20000\n" \
"s_cbranch_scc1 .LX0_w61l\n" \
"s_branch .LX0_end\n" \
".LX0_w61d:\n" \
"v_readfirstlane_b32 s31, v241\n" \
"s_lshr_b32 s32, s31, 2\n" \
"s_xor_b32 s29, s31, s32\n" \
"s_and_b32 s29, s29, 3\n" \
"s_lshl_b32 s29, s29, 13\n" \
"s_bitset1_b32 s29, 21\n" \
"s_and_b32 s31, s31, 3\n" \
"s_lshl_b32 s31, s31, 13\n" \
"s_bitcmp1_b32 s39, 0\n" \
"s_cselect_b32 s43, s29, 0\n" \
"s_xor_b32 s31, s31, s43\n" \
"s_xor_b32 s32, s31, 0x4000\n" \
"s_mov_b32 s39, 0\n" \
"s_add_u32 s43, s30, 8\n" \
"s_mov_b32 s50, 0\n" \
".LX0_w62l:\n" \
"buffer_load_dword v240, v215, s[4:7], 0 offen sc1\n" \
"s_waitcnt vmcnt(0)\n" \
".LX0_w62c:\n" \
"v_subrev_u32 v241, s43, v240\n" \
"v_cmp_gt_u32 vcc, 0x100, v241\n" \
"s_cmp_eq_u64 vcc, -1\n" \
"s_cbranch_scc1 .LX0_w62d\n" \
"s_sleep 2\n" \
"s_add_u32 s50, s50, 1\n" \
"s_cmp_lt_u32 s50, 0x20000\n" \
"s_cbranch_scc1 .LX0_w62l\n" \
"s_branch .LX0_end\n" \
".LX0_w62d:\n" \
"s_add_u32 s50, s40, 0\n" \
"s_mul_i32 s50, s50, 28672\n" \
"s_add_u32 s50, s50, s34\n" \
"buffer_load_dwordx4 v[28:31], v201, s[12:15], s50 offen sc1\n" \
"buffer_load_dwordx4 v[32:35], v201, s[12:15], s50 offen offset:1024 sc1\n" \
"buffer_load_dwordx4 v[36:39], v201, s[12:15], s50 offen offset:2048 sc1\n" \
"buffer_load_dwordx4 v[40:43], v201, s[12:15], s50 offen offset:3072 sc1\n" \
"ds_read_b96 v[248:250], v252\n" \
"s_waitcnt lgkmcnt(0)\n" \
"v_readfirstlane_b32 s44, v248\n" \
"v_readfirstlane_b32 s45, v249\n" \
"v_readfirstlane_b32 s46, v250\n" \
"s_mul_i32 s42, s25, s46\n" \
"s_lshr_b32 s42, s42, 12\n" \
"s_mov_b32 s26, s44\n" \
"s_mov_b32 s27, s45\n" \
".p2align 3\n" \
"s_memtime s[54:55]\n" \
"s_memrealtime s[56:57]\n" \
"s_memtime s[58:59]\n" \
"s_memrealtime s[60:61]\n" \
"s_nop 0\n" \
"s_nop 0\n" \
"s_memtime s[62:63]\n" \
"s_memrealtime s[64:65]\n" \
"s_memtime s[66:67]\n" \
"s_memrealtime s[68:69]\n" \
"s_memtime s[70:71]\n" \
"s_memrealtime s[72:73]\n" \
"s_memtime s[74:75]\n" \
"s_memrealtime s[76:77]\n" \
"s_nop 0\n" \
"s_nop 0\n" \
"s_memtime s[78:79]\n" \
"s_memrealtime s[80:81]\n" \
"s_memtime s[82:83]\n" \
"s_memrealtime s[84:85]\n" \
"s_memtime s[86:87]\n" \
"s_memrealtime s[88:89]\n" \
"s_memtime s[90:91]\n" \
"s_memrealtime s[92:93]\n" \
"s_nop 0\n" \
"s_nop 0\n" \
"s_memtime s[94:95]\n" \
"s_memrealtime s[96:97]\n" \
"s_memtime s[98:99]\n" \
"s_mov_b32 s52, 0\n" \
"s_mov_b32 s53, 4096\n" \
"s_waitcnt lgkmcnt(0)\n" \
"s_cmp_lg_u32 s96, s56\n" \
"s_cselect_b32 s52, s90, s52\n" \
"s_cselect_b32 s53, s98, s53\n" \
"s_cmp_lg_u32 s92, s56\n" \
"s_cselect_b32 s52, s86, s52\n" \
"s_cselect_b32 s53, s94, s53\n" \
"s_cmp_lg_u32 s88, s56\n" \
"s_cselect_b32 s52, s82, s52\n" \
"s_cselect_b32 s53, s90, s53\n" \
"s_cmp_lg_u32 s84, s56\n" \
"s_cselect_b32 s52, s78, s52\n" \
"s_cselect_b32 s53, s86, s53\n" \
"s_cmp_lg_u32 s80, s56\n" \
"s_cselect_b32 s52, s74, s52\n" \
"s_cselect_b32 s53, s82, s53\n" \
"s_cmp_lg_u32 s76, s56\n" \
"s_cselect_b32 s52, s70, s52\n" \
"s_cselect_b32 s53, s78, s53\n" \
"s_cmp_lg_u32 s72, s56\n" \
"s_cselect_b32 s52, s66, s52\n" \
"s_cselect_b32 s53, s74, s53\n" \
"s_cmp_lg_u32 s68, s56\n" \
"s_cselect_b32 s52, s62, s52\n" \
"s_cselect_b32 s53, s70, s53\n" \
"s_cmp_lg_u32 s64, s56\n" \
"s_cselect_b32 s52, s58, s52\n" \
"s_cselect_b32 s53, s66, s53\n" \
"s_cmp_lg_u32 s60, s56\n" \
"s_cselect_b32 s52, s54, s52\n" \
"s_cselect_b32 s53, s62, s53\n" \
"s_sub_u32 s53, s53, s52\n" \
"s_lshr_b32 s55, s53, 1\n" \
"s_add_u32 s52, s52, s55\n" \
"s_add_u32 s55, s56, 4\n" \
"s_lshl_b32 s55, s55, 4\n" \
"s_cmp_le_u32 s53, 40\n" \
"s_cselect_b32 s44, s55, s44\n" \
"s_cselect_b32 s45, s52, s45\n" \
"v_mov_b32 v248, s44\n" \
"v_mov_b32 v249, s45\n" \
"v_mov_b32 v250, s46\n" \
"s_mov_b64 exec, 1\n" \
"ds_write_b96 v252, v[248:250]\n" \
"s_mov_b64 exec, -1\n" \
"s_sub_u32 s26, s44, s26\n" \
"s_sub_u32 s27, s45, s27\n" \
"v_cvt_f32_u32 v248, s26\n" \
"v_cvt_f32_u32 v249, s27\n" \
"v_rcp_f32 v248, v248\n" \
"v_mul_f32 v249, 0x457ae148, v249\n" \
"v_mul_f32 v248, v248, v249\n" \
"v_cvt_u32_f32 v248, v248\n" \
"s_nop 0\n" \
"v_readfirstlane_b32 s27, v248\n" \
"s_max_u32 s27, s27, 0x800\n" \
"s_min_u32 s27, s27, 0x2000\n" \
"s_cmp_gt_u32 s26, 1600\n" \
"s_cselect_b32 s46, s27, s46\n" \
".p2align 3\n" \
"s_memtime s[54:55]\n" \
"s_memrealtime s[56:57]\n" \
"s_memtime s[58:59]\n" \
"s_memrealtime s[60:61]\n" \
"s_nop 0\n" \
"s_nop 0\n" \
"s_memtime s[62:63]\n" \
"s_memrealtime s[64:65]\n" \
"s_memtime s[66:67]\n" \
"s_memrealtime s[68:69]\n" \
"s_memtime s[70:71]\n" \
"s_memrealtime s[72:73]\n" \
"s_memtime s[74:75]\n" \
"s_memrealtime s[76:77]\n" \
"s_nop 0\n" \
"s_nop 0\n" \
"s_memtime s[78:79]\n" \
"s_memrealtime s[80:81]\n" \
"s_memtime s[82:83]\n" \
"s_memrealtime s[84:85]\n" \
"s_memtime s[86:87]\n" \
"s_memrealtime s[88:89]\n" \
"s_memtime s[90:91]\n" \
"s_memrealtime s[92:93]\n" \
"s_nop 0\n" \
"s_nop 0\n" \
"s_memtime s[94:95]\n" \
"s_memrealtime s[96:97]\n" \
"s_memtime s[98:99]\n" \
"s_mov_b32 s52, 0\n" \
"s_mov_b32 s53, 4096\n" \
"s_waitcnt lgkmcnt(0)\n" \
"s_cmp_lg_u32 s96, s56\n" \
"s_cselect_b32 s52, s90, s52\n" \
"s_cselect_b32 s53, s98, s53\n" \
"s_cmp_lg_u32 s92, s56\n" \
"s_cselect_b32 s52, s86, s52\n" \
"s_cselect_b32 s53, s94, s53\n" \
"s_cmp_lg_u32 s88, s56\n" \
"s_cselect_b32 s52, s82, s52\n" \
"s_cselect_b32 s53, s90, s53\n" \
"s_cmp_lg_u32 s84, s56\n" \
"s_cselect_b32 s52, s78, s52\n" \
"s_cselect_b32 s53, s86, s53\n" \
"s_cmp_lg_u32 s80, s56\n" \
"s_cselect_b32 s52, s74, s52\n" \
"s_cselect_b32 s53, s82, s53\n" \
"s_cmp_lg_u32 s76, s56\n" \
"s_cselect_b32 s52, s70, s52\n" \
"s_cselect_b32 s53, s78, s53\n" \
"s_cmp_lg_u32 s72, s56\n" \
"s_cselect_b32 s52, s66, s52\n" \
"s_cselect_b32 s53, s74, s53\n" \
"s_cmp_lg_u32 s68, s56\n" \
"s_cselect_b32 s52, s62, s52\n" \
"s_cselect_b32 s53, s70, s53\n" \
"s_cmp_lg_u32 s64, s56\n" \
"s_cselect_b32 s52, s58, s52\n" \
"s_cselect_b32 s53, s66, s53\n" \
"s_cmp_lg_u32 s60, s56\n" \
"s_cselect_b32 s52, s54, s52\n" \
"s_cselect_b32 s53, s62, s53\n" \
"s_sub_u32 s53, s53, s52\n" \
"s_lshr_b32 s55, s53, 1\n" \
"s_add_u32 s52, s52, s55\n" \
"s_add_u32 s55, s56, 4\n" \
"s_lshl_b32 s55, s55, 4\n" \
"s_cmp_le_u32 s53, 40\n" \
"s_cselect_b32 s44, s55, s44\n" \
"s_cselect_b32 s45, s52, s45\n" \
"v_mov_b32 v248, s44\n" \
"v_mov_b32 v249, s45\n" \
"v_mov_b32 v250, s46\n" \
"s_mov_b64 exec, 1\n" \
"ds_write_b96 v252, v[248:250]\n" \
"s_mov_b64 exec, -1\n" \
"s_waitcnt lgkmcnt(0)\n" \
"s_waitcnt vmcnt(0)\n" \
"s_barrier\n" \
"s_add_u32 s43, s30, 9\n" \
"s_mov_b32 s50, 0\n" \
".LX0_w63l:\n" \
"buffer_load_dword v240, v215, s[4:7], 0 offen sc1\n" \
"s_waitcnt vmcnt(0)\n" \
".LX0_w63c:\n" \
"v_subrev_u32 v241, s43, v240\n" \
"v_cmp_gt_u32 vcc, 0x100, v241\n" \
"s_cmp_eq_u64 vcc, -1\n" \
"s_cbranch_scc1 .LX0_w63d\n" \
"s_sleep 2\n" \
"s_add_u32 s50, s50, 1\n" \
"s_cmp_lt_u32 s50, 0x20000\n" \
"s_cbranch_scc1 .LX0_w63l\n" \
"s_branch .LX0_end\n" \
".LX0_w63d:\n" \
".LX0_pre:\n" \
".LX0_loop:\n" \
"s_add_u32 s43, s40, 2\n" \
"s_and_b32 s43, s43, 3\n" \
"s_bitcmp1_b32 s43, 0\n" \
"s_cselect_b32 s49, s32, s31\n" \
"s_lshl_b32 s43, s43, 19\n" \
"s_add_u32 s49, s49, s43\n" \
"ds_read_b128 v[0:3], v216 offset:0\n" \
"ds_read_b128 v[4:7], v216 offset:1024\n" \
"ds_read_b128 v[8:11], v216 offset:2048\n" \
"ds_read_b128 v[12:15], v216 offset:3072\n" \
"ds_read_b128 v[16:19], v216 offset:4096\n" \
"ds_read_b128 v[20:23], v216 offset:5120\n" \
"ds_read_b128 v[24:27], v216 offset:6144\n" \
"v_mov_b32 v60, 0\n" \
"v_mov_b32 v61, 0\n" \
"v_mov_b32 v62, 0\n" \
"v_mov_b32 v63, 0\n" \
"v_mov_b32 v64, 0\n" \
"v_mov_b32 v65, 0\n" \
"v_mov_b32 v66, 0\n" \
"v_mov_b32 v67, 0\n" \
"v_mov_b32 v68, 0\n" \
"v_mov_b32 v69, 0\n" \
"v_mov_b32 v70, 0\n" \
"v_mov_b32 v71, 0\n" \
"v_mov_b32 v72, 0\n" \
"v_mov_b32 v73, 0\n" \
"v_mov_b32 v74, 0\n" \
"v_mov_b32 v75, 0\n" \
"s_nop 1\n" \
"s_waitcnt lgkmcnt(6)\n" \
"v_smfmac_f32_16x16x64_bf16 v[60:63], v[0:3], a[0:7], v200\n" \
"v_smfmac_f32_16x16x64_bf16 v[64:67], v[0:3], a[56:63], v200\n" \
"v_smfmac_f32_16x16x64_bf16 v[68:71], v[0:3], a[112:119], v200\n" \
"v_smfmac_f32_16x16x64_bf16 v[72:75], v[0:3], a[168:175], v200\n" \
"s_waitcnt lgkmcnt(5)\n" \
"v_smfmac_f32_16x16x64_bf16 v[60:63], v[4:7], a[8:15], v200\n" \
"v_smfmac_f32_16x16x64_bf16 v[64:67], v[4:7], a[64:71], v200\n" \
"v_smfmac_f32_16x16x64_bf16 v[68:71], v[4:7], a[120:127], v200\n" \
"v_smfmac_f32_16x16x64_bf16 v[72:75], v[4:7], a[176:183], v200\n" \
"s_waitcnt lgkmcnt(4)\n" \
"v_smfmac_f32_16x16x64_bf16 v[60:63], v[8:11], a[16:23], v200\n" \
"v_smfmac_f32_16x16x64_bf16 v[64:67], v[8:11], a[72:79], v200\n" \
"v_smfmac_f32_16x16x64_bf16 v[68:71], v[8:11], a[128:135], v200\n" \
"v_smfmac_f32_16x16x64_bf16 v[72:75], v[8:11], a[184:191], v200\n" \
"s_waitcnt lgkmcnt(3)\n" \
"v_smfmac_f32_16x16x64_bf16 v[60:63], v[12:15], a[24:31], v200\n" \
"v_smfmac_f32_16x16x64_bf16 v[64:67], v[12:15], a[80:87], v200\n" \
"v_smfmac_f32_16x16x64_bf16 v[68:71], v[12:15], a[136:143], v200\n" \
"v_smfmac_f32_16x16x64_bf16 v[72:75], v[12:15], a[192:199], v200\n" \
"s_waitcnt lgkmcnt(2)\n" \
"v_smfmac_f32_16x16x64_bf16 v[60:63], v[16:19], a[32:39], v200\n" \
"v_smfmac_f32_16x16x64_bf16 v[64:67], v[16:19], a[88:95], v200\n" \
"v_smfmac_f32_16x16x64_bf16 v[68:71], v[16:19], a[144:151], v200\n" \
"v_smfmac_f32_16x16x64_bf16 v[72:75], v[16:19], a[200:207], v200\n" \
"s_waitcnt lgkmcnt(1)\n" \
"v_smfmac_f32_16x16x64_bf16 v[60:63], v[20:23], a[40:47], v200\n" \
"v_smfmac_f32_16x16x64_bf16 v[64:67], v[20:23], a[96:103], v200\n" \
"v_smfmac_f32_16x16x64_bf16 v[68:71], v[20:23], a[152:159], v200\n" \
"v_smfmac_f32_16x16x64_bf16 v[72:75], v[20:23], a[208:215], v200\n" \
"s_waitcnt lgkmcnt(0)\n" \
"v_smfmac_f32_16x16x64_bf16 v[60:63], v[24:27], a[48:55], v200\n" \
"v_smfmac_f32_16x16x64_bf16 v[64:67], v[24:27], a[104:111], v200\n" \
"v_smfmac_f32_16x16x64_bf16 v[68:71], v[24:27], a[160:167], v200\n" \
"v_smfmac_f32_16x16x64_bf16 v[72:75], v[24:27], a[216:223], v200\n" \
"s_barrier\n" \
"s_add_u32 s50, s40, 1\n" \
"s_mul_i32 s50, s50, 28672\n" \
"s_add_u32 s50, s50, s34\n" \
"buffer_load_dwordx4 v[44:47], v201, s[12:15], s50 offen sc1\n" \
"buffer_load_dwordx4 v[48:51], v201, s[12:15], s50 offen offset:1024 sc1\n" \
"buffer_load_dwordx4 v[52:55], v201, s[12:15], s50 offen offset:2048 sc1\n" \
"buffer_load_dwordx4 v[56:59], v201, s[12:15], s50 offen offset:3072 sc1\n" \
"s_nop 7\n" \
"v_permlane32_swap_b32 v60, v64\n" \
"v_permlane32_swap_b32 v61, v65\n" \
"v_permlane32_swap_b32 v62, v66\n" \
"v_permlane32_swap_b32 v63, v67\n" \
"v_add_f32 v60, v60, v64\n" \
"v_add_f32 v61, v61, v65\n" \
"v_add_f32 v62, v62, v66\n" \
"v_add_f32 v63, v63, v67\n" \
"v_cvt_pk_bf16_f32 v184, v60, v61\n" \
"v_cvt_pk_bf16_f32 v185, v62, v63\n" \
"ds_write_b64 v217, v[184:185] offset:0\n" \
"v_permlane32_swap_b32 v68, v72\n" \
"v_permlane32_swap_b32 v69, v73\n" \
"v_permlane32_swap_b32 v70, v74\n" \
"v_permlane32_swap_b32 v71, v75\n" \
"v_add_f32 v68, v68, v72\n" \
"v_add_f32 v69, v69, v73\n" \
"v_add_f32 v70, v70, v74\n" \
"v_add_f32 v71, v71, v75\n" \
"v_cvt_pk_bf16_f32 v186, v68, v69\n" \
"v_cvt_pk_bf16_f32 v187, v70, v71\n" \
"ds_write_b64 v217, v[186:187] offset:512\n" \
"ds_read_b64_tr_b16 v[0:1], v218 offset:0\n" \
"ds_read_b64_tr_b16 v[2:3], v218 offset:64\n" \
"s_add_u32 s43, s40, 0\n" \
"s_mul_i32 s43, s43, 7168\n" \
"s_waitcnt lgkmcnt(0)\n" \
"buffer_store_dwordx4 v[0:3], v219, s[16:19], s43 offen sc1\n" \
"s_waitcnt vmcnt(5)\n" \
"s_barrier\n" \
"ds_read_b64_tr_b16 v[92:93], v213\n" \
"ds_read_b64_tr_b16 v[94:95], v213 offset:64\n" \
"ds_read_b32 v84, v252 offset:16\n" \
"v_mov_b32 v245, v245\n" \
"s_nop 2\n" \
"v_mov_b32 v245, v245\n" \
"s_nop 2\n" \
"v_mov_b32 v245, v245\n" \
"s_waitcnt lgkmcnt(1)\n" \
"v_mfma_f32_16x16x32_bf16 v[68:71], v[92:95], v[28:31], 0\n" \
"s_waitcnt lgkmcnt(0)\n" \
"v_xor_b32 v85, s29, v84\n" \
"v_mfma_f32_16x16x32_bf16 v[76:79], v[92:95], v[36:39], 0\n" \
"v_add_u32 v84, v84, v210\n" \
"v_add_u32 v85, v85, v210\n" \
"v_mfma_f32_16x16x32_bf16 v[72:75], v[92:95], v[32:35], 0\n" \
"s_nop 1\n" \
"v_mfma_f32_16x16x32_bf16 v[80:83], v[92:95], v[40:43], 0\n" \
"v_cvt_pk_bf16_f32 v96, v68, v69\n" \
"v_cvt_pk_bf16_f32 v97, v70, v71\n" \
"v_cvt_pk_bf16_f32 v98, v76, v77\n" \
"v_cvt_pk_bf16_f32 v99, v78, v79\n" \
"s_mov_b64 exec, s[2:3]\n" \
"v_cvt_pk_bf16_f32 v96, v72, v73\n" \
"v_cvt_pk_bf16_f32 v97, v74, v75\n" \
"s_nop 0\n" \
"v_cvt_pk_bf16_f32 v98, v80, v81\n" \
"v_cvt_pk_bf16_f32 v99, v82, v83\n" \
"s_mov_b64 exec, -1\n" \
"s_nop 1\n" \
"v_permlane16_swap_b32 v96, v98\n" \
"v_permlane16_swap_b32 v97, v99\n" \
"buffer_store_dwordx4 v[96:99], v84, s[4:7], 0 offen sc1\n" \
"s_barrier\n" \
"buffer_store_dwordx4 v[96:99], v85, s[4:7], 0 offen sc1\n" \
"ds_read_b96 v[248:250], v252\n" \
"s_waitcnt lgkmcnt(0)\n" \
"v_readfirstlane_b32 s44, v248\n" \
"v_readfirstlane_b32 s45, v249\n" \
"v_readfirstlane_b32 s46, v250\n" \
"s_mul_i32 s42, s25, s46\n" \
"s_lshr_b32 s42, s42, 12\n" \
".p2align 3\n" \
"s_memtime s[54:55]\n" \
"s_memrealtime s[56:57]\n" \
"s_memtime s[58:59]\n" \
"s_memrealtime s[60:61]\n" \
"s_nop 0\n" \
"s_nop 0\n" \
"s_memtime s[62:63]\n" \
"s_memrealtime s[64:65]\n" \
"s_memtime s[66:67]\n" \
"s_memrealtime s[68:69]\n" \
"s_memtime s[70:71]\n" \
"s_memrealtime s[72:73]\n" \
"s_memtime s[74:75]\n" \
"s_memrealtime s[76:77]\n" \
"s_nop 0\n" \
"s_nop 0\n" \
"s_memtime s[78:79]\n" \
"s_memrealtime s[80:81]\n" \
"s_memtime s[82:83]\n" \
"s_memrealtime s[84:85]\n" \
"s_memtime s[86:87]\n" \
"s_memrealtime s[88:89]\n" \
"s_memtime s[90:91]\n" \
"s_memrealtime s[92:93]\n" \
"s_nop 0\n" \
"s_nop 0\n" \
"s_memtime s[94:95]\n" \
"s_memrealtime s[96:97]\n" \
"s_memtime s[98:99]\n" \
"s_mov_b32 s52, 0\n" \
"s_mov_b32 s53, 4096\n" \
"s_waitcnt lgkmcnt(0)\n" \
"s_cmp_lg_u32 s96, s56\n" \
"s_cselect_b32 s52, s90, s52\n" \
"s_cselect_b32 s53, s98, s53\n" \
"s_cmp_lg_u32 s92, s56\n" \
"s_cselect_b32 s52, s86, s52\n" \
"s_cselect_b32 s53, s94, s53\n" \
"s_cmp_lg_u32 s88, s56\n" \
"s_cselect_b32 s52, s82, s52\n" \
"s_cselect_b32 s53, s90, s53\n" \
"s_cmp_lg_u32 s84, s56\n" \
"s_cselect_b32 s52, s78, s52\n" \
"s_cselect_b32 s53, s86, s53\n" \
"s_cmp_lg_u32 s80, s56\n" \
"s_cselect_b32 s52, s74, s52\n" \
"s_cselect_b32 s53, s82, s53\n" \
"s_cmp_lg_u32 s76, s56\n" \
"s_cselect_b32 s52, s70, s52\n" \
"s_cselect_b32 s53, s78, s53\n" \
"s_cmp_lg_u32 s72, s56\n" \
"s_cselect_b32 s52, s66, s52\n" \
"s_cselect_b32 s53, s74, s53\n" \
"s_cmp_lg_u32 s68, s56\n" \
"s_cselect_b32 s52, s62, s52\n" \
"s_cselect_b32 s53, s70, s53\n" \
"s_cmp_lg_u32 s64, s56\n" \
"s_cselect_b32 s52, s58, s52\n" \
"s_cselect_b32 s53, s66, s53\n" \
"s_cmp_lg_u32 s60, s56\n" \
"s_cselect_b32 s52, s54, s52\n" \
"s_cselect_b32 s53, s62, s53\n" \
"s_sub_u32 s53, s53, s52\n" \
"s_lshr_b32 s55, s53, 1\n" \
"s_add_u32 s52, s52, s55\n" \
"s_add_u32 s55, s56, 4\n" \
"s_lshl_b32 s55, s55, 4\n" \
"s_sub_u32 s59, s55, s44\n" \
"s_cmp_lt_u32 s59, 2600\n" \
"s_cselect_b32 s63, 1, 2\n" \
"s_mul_i32 s59, s59, s46\n" \
"s_ashr_i32 s59, s59, 12\n" \
"s_add_u32 s59, s59, s45\n" \
"s_sub_u32 s59, s52, s59\n" \
"s_max_i32 s59, s59, -96\n" \
"s_min_i32 s59, s59, 96\n" \
"s_ashr_i32 s63, s59, s63\n" \
"s_add_u32 s63, s63, s46\n" \
"s_max_i32 s63, s63, 0x800\n" \
"s_min_i32 s63, s63, 0x2000\n" \
"s_cmp_le_u32 s53, 40\n" \
"s_cselect_b32 s44, s55, s44\n" \
"s_cselect_b32 s45, s52, s45\n" \
"s_cselect_b32 s46, s63, s46\n" \
"v_mov_b32 v248, s44\n" \
"v_mov_b32 v249, s45\n" \
"v_mov_b32 v250, s46\n" \
"s_mov_b64 exec, 1\n" \
"ds_write_b96 v252, v[248:250]\n" \
"s_mov_b64 exec, -1\n" \
"buffer_store_dwordx4 v[100:103], v214, s[4:7], s49 offen sc1\n" \
"s_xor_b32 s43, s49, s29\n" \
"buffer_store_dwordx4 v[100:103], v214, s[4:7], s43 offen sc1\n" \
"s_waitcnt vmcnt(4)\n" \
"v_mov_b32 v28, v44\n" \
"v_mov_b32 v29, v45\n" \
"v_mov_b32 v30, v46\n" \
"v_mov_b32 v31, v47\n" \
"v_mov_b32 v32, v48\n" \
"v_mov_b32 v33, v49\n" \
"v_mov_b32 v34, v50\n" \
"v_mov_b32 v35, v51\n" \
"v_mov_b32 v36, v52\n" \
"v_mov_b32 v37, v53\n" \
"v_mov_b32 v38, v54\n" \
"v_mov_b32 v39, v55\n" \
"v_mov_b32 v40, v56\n" \
"v_mov_b32 v41, v57\n" \
"v_mov_b32 v42, v58\n" \
"v_mov_b32 v43, v59\n" \
"s_add_u32 s40, s40, 1\n" \
"s_waitcnt lgkmcnt(0)\n" \
"s_barrier\n" \
"s_cmp_eq_u32 s40, 0\n" \
"s_cbranch_scc1 .LX0_wet\n" \
"s_cmp_lt_u32 s40, 63\n" \
"s_cbranch_scc1 .LX0_loop\n" \
"ds_read_b128 v[0:3], v216 offset:0\n" \
"ds_read_b128 v[4:7], v216 offset:1024\n" \
"ds_read_b128 v[8:11], v216 offset:2048\n" \
"ds_read_b128 v[12:15], v216 offset:3072\n" \
"ds_read_b128 v[16:19], v216 offset:4096\n" \
"ds_read_b128 v[20:23], v216 offset:5120\n" \
"ds_read_b128 v[24:27], v216 offset:6144\n" \
"v_mov_b32 v60, 0\n" \
"v_mov_b32 v61, 0\n" \
"v_mov_b32 v62, 0\n" \
"v_mov_b32 v63, 0\n" \
"v_mov_b32 v64, 0\n" \
"v_mov_b32 v65, 0\n" \
"v_mov_b32 v66, 0\n" \
"v_mov_b32 v67, 0\n" \
"v_mov_b32 v68, 0\n" \
"v_mov_b32 v69, 0\n" \
"v_mov_b32 v70, 0\n" \
"v_mov_b32 v71, 0\n" \
"v_mov_b32 v72, 0\n" \
"v_mov_b32 v73, 0\n" \
"v_mov_b32 v74, 0\n" \
"v_mov_b32 v75, 0\n" \
"s_nop 1\n" \
"s_waitcnt lgkmcnt(6)\n" \
"v_smfmac_f32_16x16x64_bf16 v[60:63], v[0:3], a[0:7], v200\n" \
"v_smfmac_f32_16x16x64_bf16 v[64:67], v[0:3], a[56:63], v200\n" \
"v_smfmac_f32_16x16x64_bf16 v[68:71], v[0:3], a[112:119], v200\n" \
"v_smfmac_f32_16x16x64_bf16 v[72:75], v[0:3], a[168:175], v200\n" \
"s_waitcnt lgkmcnt(5)\n" \
"v_smfmac_f32_16x16x64_bf16 v[60:63], v[4:7], a[8:15], v200\n" \
"v_smfmac_f32_16x16x64_bf16 v[64:67], v[4:7], a[64:71], v200\n" \
"v_smfmac_f32_16x16x64_bf16 v[68:71], v[4:7], a[120:127], v200\n" \
"v_smfmac_f32_16x16x64_bf16 v[72:75], v[4:7], a[176:183], v200\n" \
"s_waitcnt lgkmcnt(4)\n" \
"v_smfmac_f32_16x16x64_bf16 v[60:63], v[8:11], a[16:23], v200\n" \
"v_smfmac_f32_16x16x64_bf16 v[64:67], v[8:11], a[72:79], v200\n" \
"v_smfmac_f32_16x16x64_bf16 v[68:71], v[8:11], a[128:135], v200\n" \
"v_smfmac_f32_16x16x64_bf16 v[72:75], v[8:11], a[184:191], v200\n" \
"s_waitcnt lgkmcnt(3)\n" \
"v_smfmac_f32_16x16x64_bf16 v[60:63], v[12:15], a[24:31], v200\n" \
"v_smfmac_f32_16x16x64_bf16 v[64:67], v[12:15], a[80:87], v200\n" \
"v_smfmac_f32_16x16x64_bf16 v[68:71], v[12:15], a[136:143], v200\n" \
"v_smfmac_f32_16x16x64_bf16 v[72:75], v[12:15], a[192:199], v200\n" \
"s_waitcnt lgkmcnt(2)\n" \
"v_smfmac_f32_16x16x64_bf16 v[60:63], v[16:19], a[32:39], v200\n" \
"v_smfmac_f32_16x16x64_bf16 v[64:67], v[16:19], a[88:95], v200\n" \
"v_smfmac_f32_16x16x64_bf16 v[68:71], v[16:19], a[144:151], v200\n" \
"v_smfmac_f32_16x16x64_bf16 v[72:75], v[16:19], a[200:207], v200\n" \
"s_waitcnt lgkmcnt(1)\n" \
"v_smfmac_f32_16x16x64_bf16 v[60:63], v[20:23], a[40:47], v200\n" \
"v_smfmac_f32_16x16x64_bf16 v[64:67], v[20:23], a[96:103], v200\n" \
"v_smfmac_f32_16x16x64_bf16 v[68:71], v[20:23], a[152:159], v200\n" \
"v_smfmac_f32_16x16x64_bf16 v[72:75], v[20:23], a[208:215], v200\n" \
"s_waitcnt lgkmcnt(0)\n" \
"v_smfmac_f32_16x16x64_bf16 v[60:63], v[24:27], a[48:55], v200\n" \
"v_smfmac_f32_16x16x64_bf16 v[64:67], v[24:27], a[104:111], v200\n" \
"v_smfmac_f32_16x16x64_bf16 v[68:71], v[24:27], a[160:167], v200\n" \
"v_smfmac_f32_16x16x64_bf16 v[72:75], v[24:27], a[216:223], v200\n" \
"s_nop 7\n" \
"v_permlane32_swap_b32 v60, v64\n" \
"v_permlane32_swap_b32 v61, v65\n" \
"v_permlane32_swap_b32 v62, v66\n" \
"v_permlane32_swap_b32 v63, v67\n" \
"v_add_f32 v60, v60, v64\n" \
"v_add_f32 v61, v61, v65\n" \
"v_add_f32 v62, v62, v66\n" \
"v_add_f32 v63, v63, v67\n" \
"v_cvt_pk_bf16_f32 v184, v60, v61\n" \
"v_cvt_pk_bf16_f32 v185, v62, v63\n" \
"ds_write_b64 v217, v[184:185] offset:0\n" \
"v_permlane32_swap_b32 v68, v72\n" \
"v_permlane32_swap_b32 v69, v73\n" \
"v_permlane32_swap_b32 v70, v74\n" \
"v_permlane32_swap_b32 v71, v75\n" \
"v_add_f32 v68, v68, v72\n" \
"v_add_f32 v69, v69, v73\n" \
"v_add_f32 v70, v70, v74\n" \
"v_add_f32 v71, v71, v75\n" \
"v_cvt_pk_bf16_f32 v186, v68, v69\n" \
"v_cvt_pk_bf16_f32 v187, v70, v71\n" \
"ds_write_b64 v217, v[186:187] offset:512\n" \
"ds_read_b64_tr_b16 v[0:1], v218 offset:0\n" \
"ds_read_b64_tr_b16 v[2:3], v218 offset:64\n" \
"s_add_u32 s43, s40, 0\n" \
"s_mul_i32 s43, s43, 7168\n" \
"s_waitcnt lgkmcnt(0)\n" \
"buffer_store_dwordx4 v[0:3], v219, s[16:19], s43 offen sc1\n" \
"s_waitcnt vmcnt(0)\n" \
"s_barrier\n" \
"ds_read_b128 v[0:3], v216 offset:7168\n" \
"ds_read_b128 v[4:7], v216 offset:8192\n" \
"ds_read_b128 v[8:11], v216 offset:9216\n" \
"ds_read_b128 v[12:15], v216 offset:10240\n" \
"ds_read_b128 v[16:19], v216 offset:11264\n" \
"ds_read_b128 v[20:23], v216 offset:12288\n" \
"ds_read_b128 v[24:27], v216 offset:13312\n" \
"v_mov_b32 v60, 0\n" \
"v_mov_b32 v61, 0\n" \
"v_mov_b32 v62, 0\n" \
"v_mov_b32 v63, 0\n" \
"v_mov_b32 v64, 0\n" \
"v_mov_b32 v65, 0\n" \
"v_mov_b32 v66, 0\n" \
"v_mov_b32 v67, 0\n" \
"v_mov_b32 v68, 0\n" \
"v_mov_b32 v69, 0\n" \
"v_mov_b32 v70, 0\n" \
"v_mov_b32 v71, 0\n" \
"v_mov_b32 v72, 0\n" \
"v_mov_b32 v73, 0\n" \
"v_mov_b32 v74, 0\n" \
"v_mov_b32 v75, 0\n" \
"s_nop 1\n" \
"s_waitcnt lgkmcnt(6)\n" \
"v_smfmac_f32_16x16x64_bf16 v[60:63], v[0:3], a[0:7], v200\n" \
"v_smfmac_f32_16x16x64_bf16 v[64:67], v[0:3], a[56:63], v200\n" \
"v_smfmac_f32_16x16x64_bf16 v[68:71], v[0:3], a[112:119], v200\n" \
"v_smfmac_f32_16x16x64_bf16 v[72:75], v[0:3], a[168:175], v200\n" \
"s_waitcnt lgkmcnt(5)\n" \
"v_smfmac_f32_16x16x64_bf16 v[60:63], v[4:7], a[8:15], v200\n" \
"v_smfmac_f32_16x16x64_bf16 v[64:67], v[4:7], a[64:71], v200\n" \
"v_smfmac_f32_16x16x64_bf16 v[68:71], v[4:7], a[120:127], v200\n" \
"v_smfmac_f32_16x16x64_bf16 v[72:75], v[4:7], a[176:183], v200\n" \
"s_waitcnt lgkmcnt(4)\n" \
"v_smfmac_f32_16x16x64_bf16 v[60:63], v[8:11], a[16:23], v200\n" \
"v_smfmac_f32_16x16x64_bf16 v[64:67], v[8:11], a[72:79], v200\n" \
"v_smfmac_f32_16x16x64_bf16 v[68:71], v[8:11], a[128:135], v200\n" \
"v_smfmac_f32_16x16x64_bf16 v[72:75], v[8:11], a[184:191], v200\n" \
"s_waitcnt lgkmcnt(3)\n" \
"v_smfmac_f32_16x16x64_bf16 v[60:63], v[12:15], a[24:31], v200\n" \
"v_smfmac_f32_16x16x64_bf16 v[64:67], v[12:15], a[80:87], v200\n" \
"v_smfmac_f32_16x16x64_bf16 v[68:71], v[12:15], a[136:143], v200\n" \
"v_smfmac_f32_16x16x64_bf16 v[72:75], v[12:15], a[192:199], v200\n" \
"s_waitcnt lgkmcnt(2)\n" \
"v_smfmac_f32_16x16x64_bf16 v[60:63], v[16:19], a[32:39], v200\n" \
"v_smfmac_f32_16x16x64_bf16 v[64:67], v[16:19], a[88:95], v200\n" \
"v_smfmac_f32_16x16x64_bf16 v[68:71], v[16:19], a[144:151], v200\n" \
"v_smfmac_f32_16x16x64_bf16 v[72:75], v[16:19], a[200:207], v200\n" \
"s_waitcnt lgkmcnt(1)\n" \
"v_smfmac_f32_16x16x64_bf16 v[60:63], v[20:23], a[40:47], v200\n" \
"v_smfmac_f32_16x16x64_bf16 v[64:67], v[20:23], a[96:103], v200\n" \
"v_smfmac_f32_16x16x64_bf16 v[68:71], v[20:23], a[152:159], v200\n" \
"v_smfmac_f32_16x16x64_bf16 v[72:75], v[20:23], a[208:215], v200\n" \
"s_waitcnt lgkmcnt(0)\n" \
"v_smfmac_f32_16x16x64_bf16 v[60:63], v[24:27], a[48:55], v200\n" \
"v_smfmac_f32_16x16x64_bf16 v[64:67], v[24:27], a[104:111], v200\n" \
"v_smfmac_f32_16x16x64_bf16 v[68:71], v[24:27], a[160:167], v200\n" \
"v_smfmac_f32_16x16x64_bf16 v[72:75], v[24:27], a[216:223], v200\n" \
"s_nop 7\n" \
"v_permlane32_swap_b32 v60, v64\n" \
"v_permlane32_swap_b32 v61, v65\n" \
"v_permlane32_swap_b32 v62, v66\n" \
"v_permlane32_swap_b32 v63, v67\n" \
"v_add_f32 v60, v60, v64\n" \
"v_add_f32 v61, v61, v65\n" \
"v_add_f32 v62, v62, v66\n" \
"v_add_f32 v63, v63, v67\n" \
"v_cvt_pk_bf16_f32 v184, v60, v61\n" \
"v_cvt_pk_bf16_f32 v185, v62, v63\n" \
"ds_write_b64 v217, v[184:185] offset:0\n" \
"v_permlane32_swap_b32 v68, v72\n" \
"v_permlane32_swap_b32 v69, v73\n" \
"v_permlane32_swap_b32 v70, v74\n" \
"v_permlane32_swap_b32 v71, v75\n" \
"v_add_f32 v68, v68, v72\n" \
"v_add_f32 v69, v69, v73\n" \
"v_add_f32 v70, v70, v74\n" \
"v_add_f32 v71, v71, v75\n" \
"v_cvt_pk_bf16_f32 v186, v68, v69\n" \
"v_cvt_pk_bf16_f32 v187, v70, v71\n" \
"ds_write_b64 v217, v[186:187] offset:512\n" \
"ds_read_b64_tr_b16 v[0:1], v218 offset:0\n" \
"ds_read_b64_tr_b16 v[2:3], v218 offset:64\n" \
"s_add_u32 s43, s40, 1\n" \
"s_mul_i32 s43, s43, 7168\n" \
"s_waitcnt lgkmcnt(0)\n" \
"buffer_store_dwordx4 v[0:3], v219, s[16:19], s43 offen sc1\n" \
"s_waitcnt vmcnt(0)\n" \
"s_barrier\n" \
".LX0_end:\n" \
"s_endpgm\n" \
".p2align 6\n" \
"fk_role_X1:\n" \
"ds_read_b32 v200, v255 offset:0\n" \
"ds_read_b32 v201, v255 offset:256\n" \
"ds_read_b32 v202, v255 offset:512\n" \
"ds_read_b32 v203, v255 offset:768\n" \
"ds_read_b32 v204, v255 offset:1024\n" \
"ds_read_b32 v205, v255 offset:1280\n" \
"ds_read_b32 v206, v255 offset:1536\n" \
"ds_read_b32 v207, v255 offset:1792\n" \
"ds_read_b32 v208, v255 offset:2048\n" \
"ds_read_b32 v209, v255 offset:2304\n" \
"ds_read_b32 v210, v255 offset:2560\n" \
"ds_read_b32 v211, v255 offset:2816\n" \
"ds_read_b32 v212, v255 offset:3072\n" \
"ds_read_b32 v213, v255 offset:3328\n" \
"ds_read_b32 v214, v255 offset:3584\n" \
"ds_read_b32 v215, v255 offset:3840\n" \
"ds_read_b32 v216, v255 offset:4096\n" \
"ds_read_b32 v217, v255 offset:4352\n" \
"ds_read_b32 v218, v255 offset:4608\n" \
"ds_read_b32 v219, v255 offset:4864\n" \
"ds_read_b32 v220, v255 offset:5120\n" \
"ds_read_b32 v221, v255 offset:5376\n" \
"ds_read_b32 v254, v255 offset:7936\n" \
"s_waitcnt lgkmcnt(0)\n" \
"v_readlane_b32 s4, v254, 0\n" \
"v_readlane_b32 s5, v254, 1\n" \
"v_readlane_b32 s6, v254, 2\n" \
"v_readlane_b32 s7, v254, 3\n" \
"v_readlane_b32 s8, v254, 4\n" \
"v_readlane_b32 s9, v254, 5\n" \
"v_readlane_b32 s10, v254, 6\n" \
"v_readlane_b32 s11, v254, 7\n" \
"v_readlane_b32 s12, v254, 8\n" \
"v_readlane_b32 s13, v254, 9\n" \
"v_readlane_b32 s14, v254, 10\n" \
"v_readlane_b32 s15, v254, 11\n" \
"v_readlane_b32 s16, v254, 12\n" \
"v_readlane_b32 s17, v254, 13\n" \
"v_readlane_b32 s18, v254, 14\n" \
"v_readlane_b32 s19, v254, 15\n" \
"v_readlane_b32 s20, v254, 16\n" \
"v_readlane_b32 s21, v254, 17\n" \
"v_readlane_b32 s22, v254, 18\n" \
"v_readlane_b32 s23, v254, 19\n" \
"v_readlane_b32 s24, v254, 20\n" \
"v_readlane_b32 s25, v254, 21\n" \
"v_readlane_b32 s26, v254, 22\n" \
"v_readlane_b32 s27, v254, 23\n" \
"v_readlane_b32 s28, v254, 24\n" \
"v_readlane_b32 s29, v254, 25\n" \
"v_readlane_b32 s30, v254, 26\n" \
"v_readlane_b32 s31, v254, 27\n" \
"v_readlane_b32 s32, v254, 28\n" \
"v_readlane_b32 s33, v254, 29\n" \
"v_readlane_b32 s34, v254, 30\n" \
"v_readlane_b32 s35, v254, 31\n" \
"v_readlane_b32 s36, v254, 32\n" \
"v_readlane_b32 s37, v254, 33\n" \
"v_readlane_b32 s38, v254, 34\n" \
"v_readlane_b32 s39, v254, 35\n" \
"s_mov_b32 s40, 0\n" \
"s_mov_b32 s51, 0\n" \
"s_nop 0\n" \
"v_mov_b32 v252, s38\n" \
"s_nop 4\n" \
"v_mov_b32 v100, -1\n" \
"v_mov_b32 v101, -1\n" \
"v_mov_b32 v102, -1\n" \
"v_mov_b32 v103, -1\n" \
"s_mov_b32 s35, -2\n" \
"s_mov_b32 s2, 0\n" \
"s_mov_b32 s3, -1\n" \
"v_writelane_b32 v251, s6, 0\n" \
"v_writelane_b32 v251, s10, 1\n" \
"v_writelane_b32 v251, s14, 2\n" \
"v_writelane_b32 v251, s18, 3\n" \
"s_mov_b32 s6, 0\n" \
"s_mov_b32 s10, 0\n" \
"s_mov_b32 s14, 0\n" \
"s_mov_b32 s18, 0\n" \
"s_mov_b32 s40, -1\n" \
"s_branch .LX1_pre\n" \
".LX1_wet:\n" \
"s_waitcnt vmcnt(0)\n" \
"v_readlane_b32 s6, v251, 0\n" \
"v_readlane_b32 s10, v251, 1\n" \
"v_readlane_b32 s14, v251, 2\n" \
"v_readlane_b32 s18, v251, 3\n" \
"s_nop 4\n" \
"s_add_u32 s43, s30, 1\n" \
"s_mov_b32 s50, 0\n" \
".LX1_w64l:\n" \
"buffer_load_dword v240, v204, s[4:7], 0 offen sc1\n" \
"s_waitcnt vmcnt(0)\n" \
".LX1_w64c:\n" \
"v_subrev_u32 v241, s43, v240\n" \
"v_cmp_gt_u32 vcc, 0x100, v241\n" \
"s_cmp_eq_u64 vcc, -1\n" \
"s_cbranch_scc1 .LX1_w64d\n" \
"s_sleep 2\n" \
"s_add_u32 s50, s50, 1\n" \
"s_cmp_lt_u32 s50, 0x20000\n" \
"s_cbranch_scc1 .LX1_w64l\n" \
"s_branch .LX1_end\n" \
".LX1_w64d:\n" \
"v_readlane_b32 s43, v202, 0\n" \
"s_nop 4\n" \
"buffer_load_dwordx4 a[0:3], v201, s[8:11], s43 offen sc1\n" \
"buffer_load_dwordx4 a[4:7], v201, s[8:11], s43 offen offset:1024 sc1\n" \
"v_readlane_b32 s43, v202, 7\n" \
"s_nop 4\n" \
"buffer_load_dwordx4 a[56:59], v201, s[8:11], s43 offen sc1\n" \
"buffer_load_dwordx4 a[60:63], v201, s[8:11], s43 offen offset:1024 sc1\n" \
"v_readlane_b32 s43, v202, 14\n" \
"s_nop 4\n" \
"buffer_load_dwordx4 a[112:115], v201, s[8:11], s43 offen sc1\n" \
"buffer_load_dwordx4 a[116:119], v201, s[8:11], s43 offen offset:1024 sc1\n" \
"v_readlane_b32 s43, v202, 21\n" \
"s_nop 4\n" \
"buffer_load_dwordx4 a[168:171], v201, s[8:11], s43 offen sc1\n" \
"buffer_load_dwordx4 a[172:175], v201, s[8:11], s43 offen offset:1024 sc1\n" \
"s_add_u32 s43, s30, 2\n" \
"s_mov_b32 s50, 0\n" \
"s_branch .LX1_w65c\n" \
".LX1_w65l:\n" \
"buffer_load_dword v240, v204, s[4:7], 0 offen sc1\n" \
"s_waitcnt vmcnt(0)\n" \
".LX1_w65c:\n" \
"v_subrev_u32 v241, s43, v240\n" \
"v_cmp_gt_u32 vcc, 0x100, v241\n" \
"s_cmp_eq_u64 vcc, -1\n" \
"s_cbranch_scc1 .LX1_w65d\n" \
"s_sleep 2\n" \
"s_add_u32 s50, s50, 1\n" \
"s_cmp_lt_u32 s50, 0x20000\n" \
"s_cbranch_scc1 .LX1_w65l\n" \
"s_branch .LX1_end\n" \
".LX1_w65d:\n" \
"v_readlane_b32 s43, v202, 4\n" \
"s_nop 4\n" \
"buffer_load_dwordx4 a[32:35], v201, s[8:11], s43 offen sc1\n" \
"buffer_load_dwordx4 a[36:39], v201, s[8:11], s43 offen offset:1024 sc1\n" \
"v_readlane_b32 s43, v202, 11\n" \
"s_nop 4\n" \
"buffer_load_dwordx4 a[88:91], v201, s[8:11], s43 offen sc1\n" \
"buffer_load_dwordx4 a[92:95], v201, s[8:11], s43 offen offset:1024 sc1\n" \
"v_readlane_b32 s43, v202, 18\n" \
"s_nop 4\n" \
"buffer_load_dwordx4 a[144:147], v201, s[8:11], s43 offen sc1\n" \
"buffer_load_dwordx4 a[148:151], v201, s[8:11], s43 offen offset:1024 sc1\n" \
"v_readlane_b32 s43, v202, 25\n" \
"s_nop 4\n" \
"buffer_load_dwordx4 a[200:203], v201, s[8:11], s43 offen sc1\n" \
"buffer_load_dwordx4 a[204:207], v201, s[8:11], s43 offen offset:1024 sc1\n" \
"s_add_u32 s43, s30, 3\n" \
"s_mov_b32 s50, 0\n" \
"s_branch .LX1_w66c\n" \
".LX1_w66l:\n" \
"buffer_load_dword v240, v204, s[4:7], 0 offen sc1\n" \
"s_waitcnt vmcnt(0)\n" \
".LX1_w66c:\n" \
"v_subrev_u32 v241, s43, v240\n" \
"v_cmp_gt_u32 vcc, 0x100, v241\n" \
"s_cmp_eq_u64 vcc, -1\n" \
"s_cbranch_scc1 .LX1_w66d\n" \
"s_sleep 2\n" \
"s_add_u32 s50, s50, 1\n" \
"s_cmp_lt_u32 s50, 0x20000\n" \
"s_cbranch_scc1 .LX1_w66l\n" \
"s_branch .LX1_end\n" \
".LX1_w66d:\n" \
"v_readlane_b32 s43, v202, 1\n" \
"s_nop 4\n" \
"buffer_load_dwordx4 a[8:11], v201, s[8:11], s43 offen sc1\n" \
"buffer_load_dwordx4 a[12:15], v201, s[8:11], s43 offen offset:1024 sc1\n" \
"v_readlane_b32 s43, v202, 8\n" \
"s_nop 4\n" \
"buffer_load_dwordx4 a[64:67], v201, s[8:11], s43 offen sc1\n" \
"buffer_load_dwordx4 a[68:71], v201, s[8:11], s43 offen offset:1024 sc1\n" \
"v_readlane_b32 s43, v202, 15\n" \
"s_nop 4\n" \
"buffer_load_dwordx4 a[120:123], v201, s[8:11], s43 offen sc1\n" \
"buffer_load_dwordx4 a[124:127], v201, s[8:11], s43 offen offset:1024 sc1\n" \
"v_readlane_b32 s43, v202, 22\n" \
"s_nop 4\n" \
"buffer_load_dwordx4 a[176:179], v201, s[8:11], s43 offen sc1\n" \
"buffer_load_dwordx4 a[180:183], v201, s[8:11], s43 offen offset:1024 sc1\n" \
"s_add_u32 s43, s30, 4\n" \
"s_mov_b32 s50, 0\n" \
"s_branch .LX1_w67c\n" \
".LX1_w67l:\n" \
"buffer_load_dword v240, v204, s[4:7], 0 offen sc1\n" \
"s_waitcnt vmcnt(0)\n" \
".LX1_w67c:\n" \
"v_subrev_u32 v241, s43, v240\n" \
"v_cmp_gt_u32 vcc, 0x100, v241\n" \
"s_cmp_eq_u64 vcc, -1\n" \
"s_cbranch_scc1 .LX1_w67d\n" \
"s_sleep 2\n" \
"s_add_u32 s50, s50, 1\n" \
"s_cmp_lt_u32 s50, 0x20000\n" \
"s_cbranch_scc1 .LX1_w67l\n" \
"s_branch .LX1_end\n" \
".LX1_w67d:\n" \
"v_readlane_b32 s43, v202, 5\n" \
"s_nop 4\n" \
"buffer_load_dwordx4 a[40:43], v201, s[8:11], s43 offen sc1\n" \
"buffer_load_dwordx4 a[44:47], v201, s[8:11], s43 offen offset:1024 sc1\n" \
"v_readlane_b32 s43, v202, 12\n" \
"s_nop 4\n" \
"buffer_load_dwordx4 a[96:99], v201, s[8:11], s43 offen sc1\n" \
"buffer_load_dwordx4 a[100:103], v201, s[8:11], s43 offen offset:1024 sc1\n" \
"v_readlane_b32 s43, v202, 19\n" \
"s_nop 4\n" \
"buffer_load_dwordx4 a[152:155], v201, s[8:11], s43 offen sc1\n" \
"buffer_load_dwordx4 a[156:159], v201, s[8:11], s43 offen offset:1024 sc1\n" \
"v_readlane_b32 s43, v202, 26\n" \
"s_nop 4\n" \
"buffer_load_dwordx4 a[208:211], v201, s[8:11], s43 offen sc1\n" \
"buffer_load_dwordx4 a[212:215], v201, s[8:11], s43 offen offset:1024 sc1\n" \
"s_add_u32 s43, s30, 5\n" \
"s_mov_b32 s50, 0\n" \
"s_branch .LX1_w68c\n" \
".LX1_w68l:\n" \
"buffer_load_dword v240, v204, s[4:7], 0 offen sc1\n" \
"s_waitcnt vmcnt(0)\n" \
".LX1_w68c:\n" \
"v_subrev_u32 v241, s43, v240\n" \
"v_cmp_gt_u32 vcc, 0x100, v241\n" \
"s_cmp_eq_u64 vcc, -1\n" \
"s_cbranch_scc1 .LX1_w68d\n" \
"s_sleep 2\n" \
"s_add_u32 s50, s50, 1\n" \
"s_cmp_lt_u32 s50, 0x20000\n" \
"s_cbranch_scc1 .LX1_w68l\n" \
"s_branch .LX1_end\n" \
".LX1_w68d:\n" \
"v_readlane_b32 s43, v202, 2\n" \
"s_nop 4\n" \
"buffer_load_dwordx4 a[16:19], v201, s[8:11], s43 offen sc1\n" \
"buffer_load_dwordx4 a[20:23], v201, s[8:11], s43 offen offset:1024 sc1\n" \
"v_readlane_b32 s43, v202, 9\n" \
"s_nop 4\n" \
"buffer_load_dwordx4 a[72:75], v201, s[8:11], s43 offen sc1\n" \
"buffer_load_dwordx4 a[76:79], v201, s[8:11], s43 offen offset:1024 sc1\n" \
"v_readlane_b32 s43, v202, 16\n" \
"s_nop 4\n" \
"buffer_load_dwordx4 a[128:131], v201, s[8:11], s43 offen sc1\n" \
"buffer_load_dwordx4 a[132:135], v201, s[8:11], s43 offen offset:1024 sc1\n" \
"v_readlane_b32 s43, v202, 23\n" \
"s_nop 4\n" \
"buffer_load_dwordx4 a[184:187], v201, s[8:11], s43 offen sc1\n" \
"buffer_load_dwordx4 a[188:191], v201, s[8:11], s43 offen offset:1024 sc1\n" \
"s_add_u32 s43, s30, 6\n" \
"s_mov_b32 s50, 0\n" \
"s_branch .LX1_w69c\n" \
".LX1_w69l:\n" \
"buffer_load_dword v240, v204, s[4:7], 0 offen sc1\n" \
"s_waitcnt vmcnt(0)\n" \
".LX1_w69c:\n" \
"v_subrev_u32 v241, s43, v240\n" \
"v_cmp_gt_u32 vcc, 0x100, v241\n" \
"s_cmp_eq_u64 vcc, -1\n" \
"s_cbranch_scc1 .LX1_w69d\n" \
"s_sleep 2\n" \
"s_add_u32 s50, s50, 1\n" \
"s_cmp_lt_u32 s50, 0x20000\n" \
"s_cbranch_scc1 .LX1_w69l\n" \
"s_branch .LX1_end\n" \
".LX1_w69d:\n" \
"v_readlane_b32 s43, v202, 6\n" \
"s_nop 4\n" \
"buffer_load_dwordx4 a[48:51], v201, s[8:11], s43 offen sc1\n" \
"buffer_load_dwordx4 a[52:55], v201, s[8:11], s43 offen offset:1024 sc1\n" \
"v_readlane_b32 s43, v202, 13\n" \
"s_nop 4\n" \
"buffer_load_dwordx4 a[104:107], v201, s[8:11], s43 offen sc1\n" \
"buffer_load_dwordx4 a[108:111], v201, s[8:11], s43 offen offset:1024 sc1\n" \
"v_readlane_b32 s43, v202, 20\n" \
"s_nop 4\n" \
"buffer_load_dwordx4 a[160:163], v201, s[8:11], s43 offen sc1\n" \
"buffer_load_dwordx4 a[164:167], v201, s[8:11], s43 offen offset:1024 sc1\n" \
"v_readlane_b32 s43, v202, 27\n" \
"s_nop 4\n" \
"buffer_load_dwordx4 a[216:219], v201, s[8:11], s43 offen sc1\n" \
"buffer_load_dwordx4 a[220:223], v201, s[8:11], s43 offen offset:1024 sc1\n" \
"s_add_u32 s43, s30, 7\n" \
"s_mov_b32 s50, 0\n" \
"s_branch .LX1_w70c\n" \
".LX1_w70l:\n" \
"buffer_load_dword v240, v204, s[4:7], 0 offen sc1\n" \
"s_waitcnt vmcnt(0)\n" \
".LX1_w70c:\n" \
"v_subrev_u32 v241, s43, v240\n" \
"v_cmp_gt_u32 vcc, 0x100, v241\n" \
"s_cmp_eq_u64 vcc, -1\n" \
"s_cbranch_scc1 .LX1_w70d\n" \
"s_sleep 2\n" \
"s_add_u32 s50, s50, 1\n" \
"s_cmp_lt_u32 s50, 0x20000\n" \
"s_cbranch_scc1 .LX1_w70l\n" \
"s_branch .LX1_end\n" \
".LX1_w70d:\n" \
"v_readlane_b32 s43, v202, 3\n" \
"s_nop 4\n" \
"buffer_load_dwordx4 a[24:27], v201, s[8:11], s43 offen sc1\n" \
"buffer_load_dwordx4 a[28:31], v201, s[8:11], s43 offen offset:1024 sc1\n" \
"v_readlane_b32 s43, v202, 10\n" \
"s_nop 4\n" \
"buffer_load_dwordx4 a[80:83], v201, s[8:11], s43 offen sc1\n" \
"buffer_load_dwordx4 a[84:87], v201, s[8:11], s43 offen offset:1024 sc1\n" \
"v_readlane_b32 s43, v202, 17\n" \
"s_nop 4\n" \
"buffer_load_dwordx4 a[136:139], v201, s[8:11], s43 offen sc1\n" \
"buffer_load_dwordx4 a[140:143], v201, s[8:11], s43 offen offset:1024 sc1\n" \
"v_readlane_b32 s43, v202, 24\n" \
"s_nop 4\n" \
"buffer_load_dwordx4 a[192:195], v201, s[8:11], s43 offen sc1\n" \
"buffer_load_dwordx4 a[196:199], v201, s[8:11], s43 offen offset:1024 sc1\n" \
"s_add_u32 s43, s30, 1\n" \
"s_mov_b32 s50, 0\n" \
".LX1_w71l:\n" \
"buffer_load_dword v240, v205, s[4:7], 0 offen sc1\n" \
"s_waitcnt vmcnt(0)\n" \
".LX1_w71c:\n" \
"v_subrev_u32 v241, s43, v240\n" \
"v_cmp_gt_u32 vcc, 0x100, v241\n" \
"s_cmp_eq_u64 vcc, -1\n" \
"s_cbranch_scc1 .LX1_w71d\n" \
"s_sleep 2\n" \
"s_add_u32 s50, s50, 1\n" \
"s_cmp_lt_u32 s50, 0x20000\n" \
"s_cbranch_scc1 .LX1_w71l\n" \
"s_branch .LX1_end\n" \
".LX1_w71d:\n" \
"v_readfirstlane_b32 s31, v241\n" \
"s_lshr_b32 s32, s31, 2\n" \
"s_xor_b32 s29, s31, s32\n" \
"s_and_b32 s29, s29, 3\n" \
"s_lshl_b32 s29, s29, 13\n" \
"s_bitset1_b32 s29, 21\n" \
"s_and_b32 s31, s31, 3\n" \
"s_lshl_b32 s31, s31, 13\n" \
"s_bitcmp1_b32 s39, 0\n" \
"s_cselect_b32 s43, s29, 0\n" \
"s_xor_b32 s31, s31, s43\n" \
"s_xor_b32 s32, s31, 0x4000\n" \
"s_mov_b32 s39, 0\n" \
"s_add_u32 s43, s30, 8\n" \
"s_mov_b32 s50, 0\n" \
".LX1_w72l:\n" \
"buffer_load_dword v240, v215, s[4:7], 0 offen sc1\n" \
"s_waitcnt vmcnt(0)\n" \
".LX1_w72c:\n" \
"v_subrev_u32 v241, s43, v240\n" \
"v_cmp_gt_u32 vcc, 0x100, v241\n" \
"s_cmp_eq_u64 vcc, -1\n" \
"s_cbranch_scc1 .LX1_w72d\n" \
"s_sleep 2\n" \
"s_add_u32 s50, s50, 1\n" \
"s_cmp_lt_u32 s50, 0x20000\n" \
"s_cbranch_scc1 .LX1_w72l\n" \
"s_branch .LX1_end\n" \
".LX1_w72d:\n" \
"s_add_u32 s50, s40, 0\n" \
"s_mul_i32 s50, s50, 28672\n" \
"s_add_u32 s50, s50, s34\n" \
"buffer_load_dwordx4 v[28:31], v201, s[12:15], s50 offen sc1\n" \
"buffer_load_dwordx4 v[32:35], v201, s[12:15], s50 offen offset:1024 sc1\n" \
"buffer_load_dwordx4 v[36:39], v201, s[12:15], s50 offen offset:2048 sc1\n" \
"buffer_load_dwordx4 v[40:43], v201, s[12:15], s50 offen offset:3072 sc1\n" \
"s_waitcnt vmcnt(0)\n" \
"s_barrier\n" \
"s_add_u32 s43, s30, 9\n" \
"s_mov_b32 s50, 0\n" \
".LX1_w73l:\n" \
"buffer_load_dword v240, v215, s[4:7], 0 offen sc1\n" \
"s_waitcnt vmcnt(0)\n" \
".LX1_w73c:\n" \
"v_subrev_u32 v241, s43, v240\n" \
"v_cmp_gt_u32 vcc, 0x100, v241\n" \
"s_cmp_eq_u64 vcc, -1\n" \
"s_cbranch_scc1 .LX1_w73d\n" \
"s_sleep 2\n" \
"s_add_u32 s50, s50, 1\n" \
"s_cmp_lt_u32 s50, 0x20000\n" \
"s_cbranch_scc1 .LX1_w73l\n" \
"s_branch .LX1_end\n" \
".LX1_w73d:\n" \
".LX1_pre:\n" \
".LX1_loop:\n" \
"s_add_u32 s43, s40, 2\n" \
"s_and_b32 s43, s43, 3\n" \
"s_bitcmp1_b32 s43, 0\n" \
"s_cselect_b32 s49, s32, s31\n" \
"s_lshl_b32 s43, s43, 19\n" \
"s_add_u32 s49, s49, s43\n" \
"s_add_u32 s43, s30, s40\n" \
"s_add_u32 s43, s43, -1\n" \
"v_mov_b32 v241, s43\n" \
"buffer_store_dword v241, v220, s[4:7], 0 offen sc1\n" \
"ds_read_b128 v[0:3], v216 offset:0\n" \
"ds_read_b128 v[4:7], v216 offset:1024\n" \
"ds_read_b128 v[8:11], v216 offset:2048\n" \
"ds_read_b128 v[12:15], v216 offset:3072\n" \
"ds_read_b128 v[16:19], v216 offset:4096\n" \
"ds_read_b128 v[20:23], v216 offset:5120\n" \
"ds_read_b128 v[24:27], v216 offset:6144\n" \
"v_mov_b32 v60, 0\n" \
"v_mov_b32 v61, 0\n" \
"v_mov_b32 v62, 0\n" \
"v_mov_b32 v63, 0\n" \
"v_mov_b32 v64, 0\n" \
"v_mov_b32 v65, 0\n" \
"v_mov_b32 v66, 0\n" \
"v_mov_b32 v67, 0\n" \
"v_mov_b32 v68, 0\n" \
"v_mov_b32 v69, 0\n" \
"v_mov_b32 v70, 0\n" \
"v_mov_b32 v71, 0\n" \
"v_mov_b32 v72, 0\n" \
"v_mov_b32 v73, 0\n" \
"v_mov_b32 v74, 0\n" \
"v_mov_b32 v75, 0\n" \
"s_nop 1\n" \
"s_waitcnt lgkmcnt(6)\n" \
"v_smfmac_f32_16x16x64_bf16 v[60:63], v[0:3], a[0:7], v200\n" \
"v_smfmac_f32_16x16x64_bf16 v[64:67], v[0:3], a[56:63], v200\n" \
"v_smfmac_f32_16x16x64_bf16 v[68:71], v[0:3], a[112:119], v200\n" \
"v_smfmac_f32_16x16x64_bf16 v[72:75], v[0:3], a[168:175], v200\n" \
"s_waitcnt lgkmcnt(5)\n" \
"v_smfmac_f32_16x16x64_bf16 v[60:63], v[4:7], a[8:15], v200\n" \
"v_smfmac_f32_16x16x64_bf16 v[64:67], v[4:7], a[64:71], v200\n" \
"v_smfmac_f32_16x16x64_bf16 v[68:71], v[4:7], a[120:127], v200\n" \
"v_smfmac_f32_16x16x64_bf16 v[72:75], v[4:7], a[176:183], v200\n" \
"s_waitcnt lgkmcnt(4)\n" \
"v_smfmac_f32_16x16x64_bf16 v[60:63], v[8:11], a[16:23], v200\n" \
"v_smfmac_f32_16x16x64_bf16 v[64:67], v[8:11], a[72:79], v200\n" \
"v_smfmac_f32_16x16x64_bf16 v[68:71], v[8:11], a[128:135], v200\n" \
"v_smfmac_f32_16x16x64_bf16 v[72:75], v[8:11], a[184:191], v200\n" \
"s_waitcnt lgkmcnt(3)\n" \
"v_smfmac_f32_16x16x64_bf16 v[60:63], v[12:15], a[24:31], v200\n" \
"v_smfmac_f32_16x16x64_bf16 v[64:67], v[12:15], a[80:87], v200\n" \
"v_smfmac_f32_16x16x64_bf16 v[68:71], v[12:15], a[136:143], v200\n" \
"v_smfmac_f32_16x16x64_bf16 v[72:75], v[12:15], a[192:199], v200\n" \
"s_waitcnt lgkmcnt(2)\n" \
"v_smfmac_f32_16x16x64_bf16 v[60:63], v[16:19], a[32:39], v200\n" \
"v_smfmac_f32_16x16x64_bf16 v[64:67], v[16:19], a[88:95], v200\n" \
"v_smfmac_f32_16x16x64_bf16 v[68:71], v[16:19], a[144:151], v200\n" \
"v_smfmac_f32_16x16x64_bf16 v[72:75], v[16:19], a[200:207], v200\n" \
"s_waitcnt lgkmcnt(1)\n" \
"v_smfmac_f32_16x16x64_bf16 v[60:63], v[20:23], a[40:47], v200\n" \
"v_smfmac_f32_16x16x64_bf16 v[64:67], v[20:23], a[96:103], v200\n" \
"v_smfmac_f32_16x16x64_bf16 v[68:71], v[20:23], a[152:159], v200\n" \
"v_smfmac_f32_16x16x64_bf16 v[72:75], v[20:23], a[208:215], v200\n" \
"s_waitcnt lgkmcnt(0)\n" \
"v_smfmac_f32_16x16x64_bf16 v[60:63], v[24:27], a[48:55], v200\n" \
"v_smfmac_f32_16x16x64_bf16 v[64:67], v[24:27], a[104:111], v200\n" \
"v_smfmac_f32_16x16x64_bf16 v[68:71], v[24:27], a[160:167], v200\n" \
"v_smfmac_f32_16x16x64_bf16 v[72:75], v[24:27], a[216:223], v200\n" \
"s_barrier\n" \
"s_add_u32 s50, s40, 1\n" \
"s_mul_i32 s50, s50, 28672\n" \
"s_add_u32 s50, s50, s34\n" \
"buffer_load_dwordx4 v[44:47], v201, s[12:15], s50 offen sc1\n" \
"buffer_load_dwordx4 v[48:51], v201, s[12:15], s50 offen offset:1024 sc1\n" \
"buffer_load_dwordx4 v[52:55], v201, s[12:15], s50 offen offset:2048 sc1\n" \
"buffer_load_dwordx4 v[56:59], v201, s[12:15], s50 offen offset:3072 sc1\n" \
"s_nop 7\n" \
"v_permlane32_swap_b32 v60, v64\n" \
"v_permlane32_swap_b32 v61, v65\n" \
"v_permlane32_swap_b32 v62, v66\n" \
"v_permlane32_swap_b32 v63, v67\n" \
"v_add_f32 v60, v60, v64\n" \
"v_add_f32 v61, v61, v65\n" \
"v_add_f32 v62, v62, v66\n" \
"v_add_f32 v63, v63, v67\n" \
"v_cvt_pk_bf16_f32 v184, v60, v61\n" \
"v_cvt_pk_bf16_f32 v185, v62, v63\n" \
"ds_write_b64 v217, v[184:185] offset:0\n" \
"v_permlane32_swap_b32 v68, v72\n" \
"v_permlane32_swap_b32 v69, v73\n" \
"v_permlane32_swap_b32 v70, v74\n" \
"v_permlane32_swap_b32 v71, v75\n" \
"v_add_f32 v68, v68, v72\n" \
"v_add_f32 v69, v69, v73\n" \
"v_add_f32 v70, v70, v74\n" \
"v_add_f32 v71, v71, v75\n" \
"v_cvt_pk_bf16_f32 v186, v68, v69\n" \
"v_cvt_pk_bf16_f32 v187, v70, v71\n" \
"ds_write_b64 v217, v[186:187] offset:512\n" \
"ds_read_b64_tr_b16 v[0:1], v218 offset:0\n" \
"ds_read_b64_tr_b16 v[2:3], v218 offset:64\n" \
"s_add_u32 s43, s40, 0\n" \
"s_mul_i32 s43, s43, 7168\n" \
"s_waitcnt lgkmcnt(0)\n" \
"buffer_store_dwordx4 v[0:3], v219, s[16:19], s43 offen sc1\n" \
"s_waitcnt vmcnt(5)\n" \
"s_barrier\n" \
"ds_read_b64_tr_b16 v[92:93], v213\n" \
"ds_read_b64_tr_b16 v[94:95], v213 offset:64\n" \
"ds_read_b32 v84, v252 offset:16\n" \
"v_mov_b32 v245, v245\n" \
"s_nop 2\n" \
"v_mov_b32 v245, v245\n" \
"s_nop 2\n" \
"v_mov_b32 v245, v245\n" \
"s_waitcnt lgkmcnt(1)\n" \
"v_mfma_f32_16x16x32_bf16 v[68:71], v[92:95], v[28:31], 0\n" \
"s_waitcnt lgkmcnt(0)\n" \
"v_xor_b32 v85, s29, v84\n" \
"v_mfma_f32_16x16x32_bf16 v[76:79], v[92:95], v[36:39], 0\n" \
"v_add_u32 v84, v84, v210\n" \
"v_add_u32 v85, v85, v210\n" \
"v_mfma_f32_16x16x32_bf16 v[72:75], v[92:95], v[32:35], 0\n" \
"s_nop 1\n" \
"v_mfma_f32_16x16x32_bf16 v[80:83], v[92:95], v[40:43], 0\n" \
"v_cvt_pk_bf16_f32 v96, v68, v69\n" \
"v_cvt_pk_bf16_f32 v97, v70, v71\n" \
"v_cvt_pk_bf16_f32 v98, v76, v77\n" \
"v_cvt_pk_bf16_f32 v99, v78, v79\n" \
"s_mov_b64 exec, s[2:3]\n" \
"v_cvt_pk_bf16_f32 v96, v72, v73\n" \
"v_cvt_pk_bf16_f32 v97, v74, v75\n" \
"s_nop 0\n" \
"v_cvt_pk_bf16_f32 v98, v80, v81\n" \
"v_cvt_pk_bf16_f32 v99, v82, v83\n" \
"s_mov_b64 exec, -1\n" \
"s_nop 1\n" \
"v_permlane16_swap_b32 v96, v98\n" \
"v_permlane16_swap_b32 v97, v99\n" \
"buffer_store_dwordx4 v[96:99], v84, s[4:7], 0 offen sc1\n" \
"s_barrier\n" \
"buffer_store_dwordx4 v[96:99], v85, s[4:7], 0 offen sc1\n" \
"s_nop 15\n" \
"s_nop 15\n" \
"s_nop 15\n" \
"s_nop 15\n" \
"s_nop 15\n" \
"buffer_store_dwordx4 v[100:103], v214, s[4:7], s49 offen sc1\n" \
"s_xor_b32 s43, s49, s29\n" \
"buffer_store_dwordx4 v[100:103], v214, s[4:7], s43 offen sc1\n" \
"s_waitcnt vmcnt(4)\n" \
"v_mov_b32 v28, v44\n" \
"v_mov_b32 v29, v45\n" \
"v_mov_b32 v30, v46\n" \
"v_mov_b32 v31, v47\n" \
"v_mov_b32 v32, v48\n" \
"v_mov_b32 v33, v49\n" \
"v_mov_b32 v34, v50\n" \
"v_mov_b32 v35, v51\n" \
"v_mov_b32 v36, v52\n" \
"v_mov_b32 v37, v53\n" \
"v_mov_b32 v38, v54\n" \
"v_mov_b32 v39, v55\n" \
"v_mov_b32 v40, v56\n" \
"v_mov_b32 v41, v57\n" \
"v_mov_b32 v42, v58\n" \
"v_mov_b32 v43, v59\n" \
"s_add_u32 s40, s40, 1\n" \
"s_waitcnt lgkmcnt(0)\n" \
"s_barrier\n" \
"s_cmp_eq_u32 s40, 0\n" \
"s_cbranch_scc1 .LX1_wet\n" \
"s_cmp_lt_u32 s40, 63\n" \
"s_cbranch_scc1 .LX1_loop\n" \
"s_add_u32 s43, s30, s40\n" \
"s_add_u32 s43, s43, -1\n" \
"v_mov_b32 v241, s43\n" \
"buffer_store_dword v241, v220, s[4:7], 0 offen sc1\n" \
"ds_read_b128 v[0:3], v216 offset:0\n" \
"ds_read_b128 v[4:7], v216 offset:1024\n" \
"ds_read_b128 v[8:11], v216 offset:2048\n" \
"ds_read_b128 v[12:15], v216 offset:3072\n" \
"ds_read_b128 v[16:19], v216 offset:4096\n" \
"ds_read_b128 v[20:23], v216 offset:5120\n" \
"ds_read_b128 v[24:27], v216 offset:6144\n" \
"v_mov_b32 v60, 0\n" \
"v_mov_b32 v61, 0\n" \
"v_mov_b32 v62, 0\n" \
"v_mov_b32 v63, 0\n" \
"v_mov_b32 v64, 0\n" \
"v_mov_b32 v65, 0\n" \
"v_mov_b32 v66, 0\n" \
"v_mov_b32 v67, 0\n" \
"v_mov_b32 v68, 0\n" \
"v_mov_b32 v69, 0\n" \
"v_mov_b32 v70, 0\n" \
"v_mov_b32 v71, 0\n" \
"v_mov_b32 v72, 0\n" \
"v_mov_b32 v73, 0\n" \
"v_mov_b32 v74, 0\n" \
"v_mov_b32 v75, 0\n" \
"s_nop 1\n" \
"s_waitcnt lgkmcnt(6)\n" \
"v_smfmac_f32_16x16x64_bf16 v[60:63], v[0:3], a[0:7], v200\n" \
"v_smfmac_f32_16x16x64_bf16 v[64:67], v[0:3], a[56:63], v200\n" \
"v_smfmac_f32_16x16x64_bf16 v[68:71], v[0:3], a[112:119], v200\n" \
"v_smfmac_f32_16x16x64_bf16 v[72:75], v[0:3], a[168:175], v200\n" \
"s_waitcnt lgkmcnt(5)\n" \
"v_smfmac_f32_16x16x64_bf16 v[60:63], v[4:7], a[8:15], v200\n" \
"v_smfmac_f32_16x16x64_bf16 v[64:67], v[4:7], a[64:71], v200\n" \
"v_smfmac_f32_16x16x64_bf16 v[68:71], v[4:7], a[120:127], v200\n" \
"v_smfmac_f32_16x16x64_bf16 v[72:75], v[4:7], a[176:183], v200\n" \
"s_waitcnt lgkmcnt(4)\n" \
"v_smfmac_f32_16x16x64_bf16 v[60:63], v[8:11], a[16:23], v200\n" \
"v_smfmac_f32_16x16x64_bf16 v[64:67], v[8:11], a[72:79], v200\n" \
"v_smfmac_f32_16x16x64_bf16 v[68:71], v[8:11], a[128:135], v200\n" \
"v_smfmac_f32_16x16x64_bf16 v[72:75], v[8:11], a[184:191], v200\n" \
"s_waitcnt lgkmcnt(3)\n" \
"v_smfmac_f32_16x16x64_bf16 v[60:63], v[12:15], a[24:31], v200\n" \
"v_smfmac_f32_16x16x64_bf16 v[64:67], v[12:15], a[80:87], v200\n" \
"v_smfmac_f32_16x16x64_bf16 v[68:71], v[12:15], a[136:143], v200\n" \
"v_smfmac_f32_16x16x64_bf16 v[72:75], v[12:15], a[192:199], v200\n" \
"s_waitcnt lgkmcnt(2)\n" \
"v_smfmac_f32_16x16x64_bf16 v[60:63], v[16:19], a[32:39], v200\n" \
"v_smfmac_f32_16x16x64_bf16 v[64:67], v[16:19], a[88:95], v200\n" \
"v_smfmac_f32_16x16x64_bf16 v[68:71], v[16:19], a[144:151], v200\n" \
"v_smfmac_f32_16x16x64_bf16 v[72:75], v[16:19], a[200:207], v200\n" \
"s_waitcnt lgkmcnt(1)\n" \
"v_smfmac_f32_16x16x64_bf16 v[60:63], v[20:23], a[40:47], v200\n" \
"v_smfmac_f32_16x16x64_bf16 v[64:67], v[20:23], a[96:103], v200\n" \
"v_smfmac_f32_16x16x64_bf16 v[68:71], v[20:23], a[152:159], v200\n" \
"v_smfmac_f32_16x16x64_bf16 v[72:75], v[20:23], a[208:215], v200\n" \
"s_waitcnt lgkmcnt(0)\n" \
"v_smfmac_f32_16x16x64_bf16 v[60:63], v[24:27], a[48:55], v200\n" \
"v_smfmac_f32_16x16x64_bf16 v[64:67], v[24:27], a[104:111], v200\n" \
"v_smfmac_f32_16x16x64_bf16 v[68:71], v[24:27], a[160:167], v200\n" \
"v_smfmac_f32_16x16x64_bf16 v[72:75], v[24:27], a[216:223], v200\n" \
"s_nop 7\n" \
"v_permlane32_swap_b32 v60, v64\n" \
"v_permlane32_swap_b32 v61, v65\n" \
"v_permlane32_swap_b32 v62, v66\n" \
"v_permlane32_swap_b32 v63, v67\n" \
"v_add_f32 v60, v60, v64\n" \
"v_add_f32 v61, v61, v65\n" \
"v_add_f32 v62, v62, v66\n" \
"v_add_f32 v63, v63, v67\n" \
"v_cvt_pk_bf16_f32 v184, v60, v61\n" \
"v_cvt_pk_bf16_f32 v185, v62, v63\n" \
"ds_write_b64 v217, v[184:185] offset:0\n" \
"v_permlane32_swap_b32 v68, v72\n" \
"v_permlane32_swap_b32 v69, v73\n" \
"v_permlane32_swap_b32 v70, v74\n" \
"v_permlane32_swap_b32 v71, v75\n" \
"v_add_f32 v68, v68, v72\n" \
"v_add_f32 v69, v69, v73\n" \
"v_add_f32 v70, v70, v74\n" \
"v_add_f32 v71, v71, v75\n" \
"v_cvt_pk_bf16_f32 v186, v68, v69\n" \
"v_cvt_pk_bf16_f32 v187, v70, v71\n" \
"ds_write_b64 v217, v[186:187] offset:512\n" \
"ds_read_b64_tr_b16 v[0:1], v218 offset:0\n" \
"ds_read_b64_tr_b16 v[2:3], v218 offset:64\n" \
"s_add_u32 s43, s40, 0\n" \
"s_mul_i32 s43, s43, 7168\n" \
"s_waitcnt lgkmcnt(0)\n" \
"buffer_store_dwordx4 v[0:3], v219, s[16:19], s43 offen sc1\n" \
"s_waitcnt vmcnt(0)\n" \
"s_barrier\n" \
"s_add_u32 s43, s30, s40\n" \
"v_mov_b32 v241, s43\n" \
"buffer_store_dword v241, v220, s[4:7], 0 offen sc1\n" \
"ds_read_b128 v[0:3], v216 offset:7168\n" \
"ds_read_b128 v[4:7], v216 offset:8192\n" \
"ds_read_b128 v[8:11], v216 offset:9216\n" \
"ds_read_b128 v[12:15], v216 offset:10240\n" \
"ds_read_b128 v[16:19], v216 offset:11264\n" \
"ds_read_b128 v[20:23], v216 offset:12288\n" \
"ds_read_b128 v[24:27], v216 offset:13312\n" \
"v_mov_b32 v60, 0\n" \
"v_mov_b32 v61, 0\n" \
"v_mov_b32 v62, 0\n" \
"v_mov_b32 v63, 0\n" \
"v_mov_b32 v64, 0\n" \
"v_mov_b32 v65, 0\n" \
"v_mov_b32 v66, 0\n" \
"v_mov_b32 v67, 0\n" \
"v_mov_b32 v68, 0\n" \
"v_mov_b32 v69, 0\n" \
"v_mov_b32 v70, 0\n" \
"v_mov_b32 v71, 0\n" \
"v_mov_b32 v72, 0\n" \
"v_mov_b32 v73, 0\n" \
"v_mov_b32 v74, 0\n" \
"v_mov_b32 v75, 0\n" \
"s_nop 1\n" \
"s_waitcnt lgkmcnt(6)\n" \
"v_smfmac_f32_16x16x64_bf16 v[60:63], v[0:3], a[0:7], v200\n" \
"v_smfmac_f32_16x16x64_bf16 v[64:67], v[0:3], a[56:63], v200\n" \
"v_smfmac_f32_16x16x64_bf16 v[68:71], v[0:3], a[112:119], v200\n" \
"v_smfmac_f32_16x16x64_bf16 v[72:75], v[0:3], a[168:175], v200\n" \
"s_waitcnt lgkmcnt(5)\n" \
"v_smfmac_f32_16x16x64_bf16 v[60:63], v[4:7], a[8:15], v200\n" \
"v_smfmac_f32_16x16x64_bf16 v[64:67], v[4:7], a[64:71], v200\n" \
"v_smfmac_f32_16x16x64_bf16 v[68:71], v[4:7], a[120:127], v200\n" \
"v_smfmac_f32_16x16x64_bf16 v[72:75], v[4:7], a[176:183], v200\n" \
"s_waitcnt lgkmcnt(4)\n" \
"v_smfmac_f32_16x16x64_bf16 v[60:63], v[8:11], a[16:23], v200\n" \
"v_smfmac_f32_16x16x64_bf16 v[64:67], v[8:11], a[72:79], v200\n" \
"v_smfmac_f32_16x16x64_bf16 v[68:71], v[8:11], a[128:135], v200\n" \
"v_smfmac_f32_16x16x64_bf16 v[72:75], v[8:11], a[184:191], v200\n" \
"s_waitcnt lgkmcnt(3)\n" \
"v_smfmac_f32_16x16x64_bf16 v[60:63], v[12:15], a[24:31], v200\n" \
"v_smfmac_f32_16x16x64_bf16 v[64:67], v[12:15], a[80:87], v200\n" \
"v_smfmac_f32_16x16x64_bf16 v[68:71], v[12:15], a[136:143], v200\n" \
"v_smfmac_f32_16x16x64_bf16 v[72:75], v[12:15], a[192:199], v200\n" \
"s_waitcnt lgkmcnt(2)\n" \
"v_smfmac_f32_16x16x64_bf16 v[60:63], v[16:19], a[32:39], v200\n" \
"v_smfmac_f32_16x16x64_bf16 v[64:67], v[16:19], a[88:95], v200\n" \
"v_smfmac_f32_16x16x64_bf16 v[68:71], v[16:19], a[144:151], v200\n" \
"v_smfmac_f32_16x16x64_bf16 v[72:75], v[16:19], a[200:207], v200\n" \
"s_waitcnt lgkmcnt(1)\n" \
"v_smfmac_f32_16x16x64_bf16 v[60:63], v[20:23], a[40:47], v200\n" \
"v_smfmac_f32_16x16x64_bf16 v[64:67], v[20:23], a[96:103], v200\n" \
"v_smfmac_f32_16x16x64_bf16 v[68:71], v[20:23], a[152:159], v200\n" \
"v_smfmac_f32_16x16x64_bf16 v[72:75], v[20:23], a[208:215], v200\n" \
"s_waitcnt lgkmcnt(0)\n" \
"v_smfmac_f32_16x16x64_bf16 v[60:63], v[24:27], a[48:55], v200\n" \
"v_smfmac_f32_16x16x64_bf16 v[64:67], v[24:27], a[104:111], v200\n" \
"v_smfmac_f32_16x16x64_bf16 v[68:71], v[24:27], a[160:167], v200\n" \
"v_smfmac_f32_16x16x64_bf16 v[72:75], v[24:27], a[216:223], v200\n" \
"s_nop 7\n" \
"v_permlane32_swap_b32 v60, v64\n" \
"v_permlane32_swap_b32 v61, v65\n" \
"v_permlane32_swap_b32 v62, v66\n" \
"v_permlane32_swap_b32 v63, v67\n" \
"v_add_f32 v60, v60, v64\n" \
"v_add_f32 v61, v61, v65\n" \
"v_add_f32 v62, v62, v66\n" \
"v_add_f32 v63, v63, v67\n" \
"v_cvt_pk_bf16_f32 v184, v60, v61\n" \
"v_cvt_pk_bf16_f32 v185, v62, v63\n" \
"ds_write_b64 v217, v[184:185] offset:0\n" \
"v_permlane32_swap_b32 v68, v72\n" \
"v_permlane32_swap_b32 v69, v73\n" \
"v_permlane32_swap_b32 v70, v74\n" \
"v_permlane32_swap_b32 v71, v75\n" \
"v_add_f32 v68, v68, v72\n" \
"v_add_f32 v69, v69, v73\n" \
"v_add_f32 v70, v70, v74\n" \
"v_add_f32 v71, v71, v75\n" \
"v_cvt_pk_bf16_f32 v186, v68, v69\n" \
"v_cvt_pk_bf16_f32 v187, v70, v71\n" \
"ds_write_b64 v217, v[186:187] offset:512\n" \
"ds_read_b64_tr_b16 v[0:1], v218 offset:0\n" \
"ds_read_b64_tr_b16 v[2:3], v218 offset:64\n" \
"s_add_u32 s43, s40, 1\n" \
"s_mul_i32 s43, s43, 7168\n" \
"s_waitcnt lgkmcnt(0)\n" \
"buffer_store_dwordx4 v[0:3], v219, s[16:19], s43 offen sc1\n" \
"s_waitcnt vmcnt(0)\n" \
"s_barrier\n" \
"s_add_u32 s43, s30, s40\n" \
"s_add_u32 s43, s43, 1\n" \
"v_mov_b32 v241, s43\n" \
"buffer_store_dword v241, v220, s[4:7], 0 offen sc1\n" \
".LX1_end:\n" \
"s_endpgm\n" \
".p2align 6\n" \
"fk_role_X2:\n" \
"ds_read_b32 v200, v255 offset:0\n" \
"ds_read_b32 v201, v255 offset:256\n" \
"ds_read_b32 v202, v255 offset:512\n" \
"ds_read_b32 v203, v255 offset:768\n" \
"ds_read_b32 v204, v255 offset:1024\n" \
"ds_read_b32 v205, v255 offset:1280\n" \
"ds_read_b32 v206, v255 offset:1536\n" \
"ds_read_b32 v207, v255 offset:1792\n" \
"ds_read_b32 v208, v255 offset:2048\n" \
"ds_read_b32 v209, v255 offset:2304\n" \
"ds_read_b32 v210, v255 offset:2560\n" \
"ds_read_b32 v211, v255 offset:2816\n" \
"ds_read_b32 v212, v255 offset:3072\n" \
"ds_read_b32 v213, v255 offset:3328\n" \
"ds_read_b32 v214, v255 offset:3584\n" \
"ds_read_b32 v215, v255 offset:3840\n" \
"ds_read_b32 v216, v255 offset:4096\n" \
"ds_read_b32 v217, v255 offset:4352\n" \
"ds_read_b32 v218, v255 offset:4608\n" \
"ds_read_b32 v219, v255 offset:4864\n" \
"ds_read_b32 v220, v255 offset:5120\n" \
"ds_read_b32 v221, v255 offset:5376\n" \
"ds_read_b32 v254, v255 offset:7936\n" \
"s_waitcnt lgkmcnt(0)\n" \
"v_readlane_b32 s4, v254, 0\n" \
"v_readlane_b32 s5, v254, 1\n" \
"v_readlane_b32 s6, v254, 2\n" \
"v_readlane_b32 s7, v254, 3\n" \
"v_readlane_b32 s8, v254, 4\n" \
"v_readlane_b32 s9, v254, 5\n" \
"v_readlane_b32 s10, v254, 6\n" \
"v_readlane_b32 s11, v254, 7\n" \
"v_readlane_b32 s12, v254, 8\n" \
"v_readlane_b32 s13, v254, 9\n" \
"v_readlane_b32 s14, v254, 10\n" \
"v_readlane_b32 s15, v254, 11\n" \
"v_readlane_b32 s16, v254, 12\n" \
"v_readlane_b32 s17, v254, 13\n" \
"v_readlane_b32 s18, v254, 14\n" \
"v_readlane_b32 s19, v254, 15\n" \
"v_readlane_b32 s20, v254, 16\n" \
"v_readlane_b32 s21, v254, 17\n" \
"v_readlane_b32 s22, v254, 18\n" \
"v_readlane_b32 s23, v254, 19\n" \
"v_readlane_b32 s24, v254, 20\n" \
"v_readlane_b32 s25, v254, 21\n" \
"v_readlane_b32 s26, v254, 22\n" \
"v_readlane_b32 s27, v254, 23\n" \
"v_readlane_b32 s28, v254, 24\n" \
"v_readlane_b32 s29, v254, 25\n" \
"v_readlane_b32 s30, v254, 26\n" \
"v_readlane_b32 s31, v254, 27\n" \
"v_readlane_b32 s32, v254, 28\n" \
"v_readlane_b32 s33, v254, 29\n" \
"v_readlane_b32 s34, v254, 30\n" \
"v_readlane_b32 s35, v254, 31\n" \
"v_readlane_b32 s36, v254, 32\n" \
"v_readlane_b32 s37, v254, 33\n" \
"v_readlane_b32 s38, v254, 34\n" \
"v_readlane_b32 s39, v254, 35\n" \
"s_mov_b32 s40, 0\n" \
"s_mov_b32 s51, 0\n" \
"s_nop 0\n" \
"v_mov_b32 v252, s38\n" \
"s_nop 4\n" \
"v_mov_b32 v100, -1\n" \
"v_mov_b32 v101, -1\n" \
"v_mov_b32 v102, -1\n" \
"v_mov_b32 v103, -1\n" \
"s_mov_b32 s35, -2\n" \
"s_mov_b32 s2, 0\n" \
"s_mov_b32 s3, -1\n" \
"v_writelane_b32 v251, s6, 0\n" \
"v_writelane_b32 v251, s10, 1\n" \
"v_writelane_b32 v251, s14, 2\n" \
"v_writelane_b32 v251, s18, 3\n" \
"s_mov_b32 s6, 0\n" \
"s_mov_b32 s10, 0\n" \
"s_mov_b32 s14, 0\n" \
"s_mov_b32 s18, 0\n" \
"s_mov_b32 s40, -1\n" \
"s_branch .LX2_pre\n" \
".LX2_wet:\n" \
"s_waitcnt vmcnt(0)\n" \
"v_readlane_b32 s6, v251, 0\n" \
"v_readlane_b32 s10, v251, 1\n" \
"v_readlane_b32 s14, v251, 2\n" \
"v_readlane_b32 s18, v251, 3\n" \
"s_nop 4\n" \
"s_add_u32 s43, s30, 1\n" \
"s_mov_b32 s50, 0\n" \
".LX2_w74l:\n" \
"buffer_load_dword v240, v204, s[4:7], 0 offen sc1\n" \
"s_waitcnt vmcnt(0)\n" \
".LX2_w74c:\n" \
"v_subrev_u32 v241, s43, v240\n" \
"v_cmp_gt_u32 vcc, 0x100, v241\n" \
"s_cmp_eq_u64 vcc, -1\n" \
"s_cbranch_scc1 .LX2_w74d\n" \
"s_sleep 2\n" \
"s_add_u32 s50, s50, 1\n" \
"s_cmp_lt_u32 s50, 0x20000\n" \
"s_cbranch_scc1 .LX2_w74l\n" \
"s_branch .LX2_end\n" \
".LX2_w74d:\n" \
"v_readlane_b32 s43, v202, 0\n" \
"s_nop 4\n" \
"buffer_load_dwordx4 a[0:3], v201, s[8:11], s43 offen sc1\n" \
"buffer_load_dwordx4 a[4:7], v201, s[8:11], s43 offen offset:1024 sc1\n" \
"v_readlane_b32 s43, v202, 7\n" \
"s_nop 4\n" \
"buffer_load_dwordx4 a[56:59], v201, s[8:11], s43 offen sc1\n" \
"buffer_load_dwordx4 a[60:63], v201, s[8:11], s43 offen offset:1024 sc1\n" \
"v_readlane_b32 s43, v202, 14\n" \
"s_nop 4\n" \
"buffer_load_dwordx4 a[112:115], v201, s[8:11], s43 offen sc1\n" \
"buffer_load_dwordx4 a[116:119], v201, s[8:11], s43 offen offset:1024 sc1\n" \
"v_readlane_b32 s43, v202, 21\n" \
"s_nop 4\n" \
"buffer_load_dwordx4 a[168:171], v201, s[8:11], s43 offen sc1\n" \
"buffer_load_dwordx4 a[172:175], v201, s[8:11], s43 offen offset:1024 sc1\n" \
"v_readlane_b32 s43, v202, 28\n" \
"s_nop 4\n" \
"buffer_load_dwordx4 a[224:227], v201, s[8:11], s43 offen sc1\n" \
"buffer_load_dwordx4 a[228:231], v201, s[8:11], s43 offen offset:1024 sc1\n" \
"v_readlane_b32 s43, v202, 35\n" \
"s_nop 4\n" \
"buffer_load_dwordx4 v[128:131], v201, s[8:11], s43 offen sc1\n" \
"buffer_load_dwordx4 v[132:135], v201, s[8:11], s43 offen offset:1024 sc1\n" \
"s_add_u32 s43, s30, 2\n" \
"s_mov_b32 s50, 0\n" \
"s_branch .LX2_w75c\n" \
".LX2_w75l:\n" \
"buffer_load_dword v240, v204, s[4:7], 0 offen sc1\n" \
"s_waitcnt vmcnt(0)\n" \
".LX2_w75c:\n" \
"v_subrev_u32 v241, s43, v240\n" \
"v_cmp_gt_u32 vcc, 0x100, v241\n" \
"s_cmp_eq_u64 vcc, -1\n" \
"s_cbranch_scc1 .LX2_w75d\n" \
"s_sleep 2\n" \
"s_add_u32 s50, s50, 1\n" \
"s_cmp_lt_u32 s50, 0x20000\n" \
"s_cbranch_scc1 .LX2_w75l\n" \
"s_branch .LX2_end\n" \
".LX2_w75d:\n" \
"v_readlane_b32 s43, v202, 4\n" \
"s_nop 4\n" \
"buffer_load_dwordx4 a[32:35], v201, s[8:11], s43 offen sc1\n" \
"buffer_load_dwordx4 a[36:39], v201, s[8:11], s43 offen offset:1024 sc1\n" \
"v_readlane_b32 s43, v202, 11\n" \
"s_nop 4\n" \
"buffer_load_dwordx4 a[88:91], v201, s[8:11], s43 offen sc1\n" \
"buffer_load_dwordx4 a[92:95], v201, s[8:11], s43 offen offset:1024 sc1\n" \
"v_readlane_b32 s43, v202, 18\n" \
"s_nop 4\n" \
"buffer_load_dwordx4 a[144:147], v201, s[8:11], s43 offen sc1\n" \
"buffer_load_dwordx4 a[148:151], v201, s[8:11], s43 offen offset:1024 sc1\n" \
"v_readlane_b32 s43, v202, 25\n" \
"s_nop 4\n" \
"buffer_load_dwordx4 a[200:203], v201, s[8:11], s43 offen sc1\n" \
"buffer_load_dwordx4 a[204:207], v201, s[8:11], s43 offen offset:1024 sc1\n" \
"v_readlane_b32 s43, v202, 32\n" \
"s_nop 4\n" \
"buffer_load_dwordx4 v[104:107], v201, s[8:11], s43 offen sc1\n" \
"buffer_load_dwordx4 v[108:111], v201, s[8:11], s43 offen offset:1024 sc1\n" \
"v_readlane_b32 s43, v202, 39\n" \
"s_nop 4\n" \
"buffer_load_dwordx4 v[160:163], v201, s[8:11], s43 offen sc1\n" \
"buffer_load_dwordx4 v[164:167], v201, s[8:11], s43 offen offset:1024 sc1\n" \
"s_add_u32 s43, s30, 3\n" \
"s_mov_b32 s50, 0\n" \
"s_branch .LX2_w76c\n" \
".LX2_w76l:\n" \
"buffer_load_dword v240, v204, s[4:7], 0 offen sc1\n" \
"s_waitcnt vmcnt(0)\n" \
".LX2_w76c:\n" \
"v_subrev_u32 v241, s43, v240\n" \
"v_cmp_gt_u32 vcc, 0x100, v241\n" \
"s_cmp_eq_u64 vcc, -1\n" \
"s_cbranch_scc1 .LX2_w76d\n" \
"s_sleep 2\n" \
"s_add_u32 s50, s50, 1\n" \
"s_cmp_lt_u32 s50, 0x20000\n" \
"s_cbranch_scc1 .LX2_w76l\n" \
"s_branch .LX2_end\n" \
".LX2_w76d:\n" \
"v_readlane_b32 s43, v202, 1\n" \
"s_nop 4\n" \
"buffer_load_dwordx4 a[8:11], v201, s[8:11], s43 offen sc1\n" \
"buffer_load_dwordx4 a[12:15], v201, s[8:11], s43 offen offset:1024 sc1\n" \
"v_readlane_b32 s43, v202, 8\n" \
"s_nop 4\n" \
"buffer_load_dwordx4 a[64:67], v201, s[8:11], s43 offen sc1\n" \
"buffer_load_dwordx4 a[68:71], v201, s[8:11], s43 offen offset:1024 sc1\n" \
"v_readlane_b32 s43, v202, 15\n" \
"s_nop 4\n" \
"buffer_load_dwordx4 a[120:123], v201, s[8:11], s43 offen sc1\n" \
"buffer_load_dwordx4 a[124:127], v201, s[8:11], s43 offen offset:1024 sc1\n" \
"v_readlane_b32 s43, v202, 22\n" \
"s_nop 4\n" \
"buffer_load_dwordx4 a[176:179], v201, s[8:11], s43 offen sc1\n" \
"buffer_load_dwordx4 a[180:183], v201, s[8:11], s43 offen offset:1024 sc1\n" \
"v_readlane_b32 s43, v202, 29\n" \
"s_nop 4\n" \
"buffer_load_dwordx4 a[232:235], v201, s[8:11], s43 offen sc1\n" \
"buffer_load_dwordx4 a[236:239], v201, s[8:11], s43 offen offset:1024 sc1\n" \
"v_readlane_b32 s43, v202, 36\n" \
"s_nop 4\n" \
"buffer_load_dwordx4 v[136:139], v201, s[8:11], s43 offen sc1\n" \
"buffer_load_dwordx4 v[140:143], v201, s[8:11], s43 offen offset:1024 sc1\n" \
"s_add_u32 s43, s30, 4\n" \
"s_mov_b32 s50, 0\n" \
"s_branch .LX2_w77c\n" \
".LX2_w77l:\n" \
"buffer_load_dword v240, v204, s[4:7], 0 offen sc1\n" \
"s_waitcnt vmcnt(0)\n" \
".LX2_w77c:\n" \
"v_subrev_u32 v241, s43, v240\n" \
"v_cmp_gt_u32 vcc, 0x100, v241\n" \
"s_cmp_eq_u64 vcc, -1\n" \
"s_cbranch_scc1 .LX2_w77d\n" \
"s_sleep 2\n" \
"s_add_u32 s50, s50, 1\n" \
"s_cmp_lt_u32 s50, 0x20000\n" \
"s_cbranch_scc1 .LX2_w77l\n" \
"s_branch .LX2_end\n" \
".LX2_w77d:\n" \
"v_readlane_b32 s43, v202, 5\n" \
"s_nop 4\n" \
"buffer_load_dwordx4 a[40:43], v201, s[8:11], s43 offen sc1\n" \
"buffer_load_dwordx4 a[44:47], v201, s[8:11], s43 offen offset:1024 sc1\n" \
"v_readlane_b32 s43, v202, 12\n" \
"s_nop 4\n" \
"buffer_load_dwordx4 a[96:99], v201, s[8:11], s43 offen sc1\n" \
"buffer_load_dwordx4 a[100:103], v201, s[8:11], s43 offen offset:1024 sc1\n" \
"v_readlane_b32 s43, v202, 19\n" \
"s_nop 4\n" \
"buffer_load_dwordx4 a[152:155], v201, s[8:11], s43 offen sc1\n" \
"buffer_load_dwordx4 a[156:159], v201, s[8:11], s43 offen offset:1024 sc1\n" \
"v_readlane_b32 s43, v202, 26\n" \
"s_nop 4\n" \
"buffer_load_dwordx4 a[208:211], v201, s[8:11], s43 offen sc1\n" \
"buffer_load_dwordx4 a[212:215], v201, s[8:11], s43 offen offset:1024 sc1\n" \
"v_readlane_b32 s43, v202, 33\n" \
"s_nop 4\n" \
"buffer_load_dwordx4 v[112:115], v201, s[8:11], s43 offen sc1\n" \
"buffer_load_dwordx4 v[116:119], v201, s[8:11], s43 offen offset:1024 sc1\n" \
"v_readlane_b32 s43, v202, 40\n" \
"s_nop 4\n" \
"buffer_load_dwordx4 v[168:171], v201, s[8:11], s43 offen sc1\n" \
"buffer_load_dwordx4 v[172:175], v201, s[8:11], s43 offen offset:1024 sc1\n" \
"s_add_u32 s43, s30, 5\n" \
"s_mov_b32 s50, 0\n" \
"s_branch .LX2_w78c\n" \
".LX2_w78l:\n" \
"buffer_load_dword v240, v204, s[4:7], 0 offen sc1\n" \
"s_waitcnt vmcnt(0)\n" \
".LX2_w78c:\n" \
"v_subrev_u32 v241, s43, v240\n" \
"v_cmp_gt_u32 vcc, 0x100, v241\n" \
"s_cmp_eq_u64 vcc, -1\n" \
"s_cbranch_scc1 .LX2_w78d\n" \
"s_sleep 2\n" \
"s_add_u32 s50, s50, 1\n" \
"s_cmp_lt_u32 s50, 0x20000\n" \
"s_cbranch_scc1 .LX2_w78l\n" \
"s_branch .LX2_end\n" \
".LX2_w78d:\n" \
"v_readlane_b32 s43, v202, 2\n" \
"s_nop 4\n" \
"buffer_load_dwordx4 a[16:19], v201, s[8:11], s43 offen sc1\n" \
"buffer_load_dwordx4 a[20:23], v201, s[8:11], s43 offen offset:1024 sc1\n" \
"v_readlane_b32 s43, v202, 9\n" \
"s_nop 4\n" \
"buffer_load_dwordx4 a[72:75], v201, s[8:11], s43 offen sc1\n" \
"buffer_load_dwordx4 a[76:79], v201, s[8:11], s43 offen offset:1024 sc1\n" \
"v_readlane_b32 s43, v202, 16\n" \
"s_nop 4\n" \
"buffer_load_dwordx4 a[128:131], v201, s[8:11], s43 offen sc1\n" \
"buffer_load_dwordx4 a[132:135], v201, s[8:11], s43 offen offset:1024 sc1\n" \
"v_readlane_b32 s43, v202, 23\n" \
"s_nop 4\n" \
"buffer_load_dwordx4 a[184:187], v201, s[8:11], s43 offen sc1\n" \
"buffer_load_dwordx4 a[188:191], v201, s[8:11], s43 offen offset:1024 sc1\n" \
"v_readlane_b32 s43, v202, 30\n" \
"s_nop 4\n" \
"buffer_load_dwordx4 a[240:243], v201, s[8:11], s43 offen sc1\n" \
"buffer_load_dwordx4 a[244:247], v201, s[8:11], s43 offen offset:1024 sc1\n" \
"v_readlane_b32 s43, v202, 37\n" \
"s_nop 4\n" \
"buffer_load_dwordx4 v[144:147], v201, s[8:11], s43 offen sc1\n" \
"buffer_load_dwordx4 v[148:151], v201, s[8:11], s43 offen offset:1024 sc1\n" \
"s_add_u32 s43, s30, 6\n" \
"s_mov_b32 s50, 0\n" \
"s_branch .LX2_w79c\n" \
".LX2_w79l:\n" \
"buffer_load_dword v240, v204, s[4:7], 0 offen sc1\n" \
"s_waitcnt vmcnt(0)\n" \
".LX2_w79c:\n" \
"v_subrev_u32 v241, s43, v240\n" \
"v_cmp_gt_u32 vcc, 0x100, v241\n" \
"s_cmp_eq_u64 vcc, -1\n" \
"s_cbranch_scc1 .LX2_w79d\n" \
"s_sleep 2\n" \
"s_add_u32 s50, s50, 1\n" \
"s_cmp_lt_u32 s50, 0x20000\n" \
"s_cbranch_scc1 .LX2_w79l\n" \
"s_branch .LX2_end\n" \
".LX2_w79d:\n" \
"v_readlane_b32 s43, v202, 6\n" \
"s_nop 4\n" \
"buffer_load_dwordx4 a[48:51], v201, s[8:11], s43 offen sc1\n" \
"buffer_load_dwordx4 a[52:55], v201, s[8:11], s43 offen offset:1024 sc1\n" \
"v_readlane_b32 s43, v202, 13\n" \
"s_nop 4\n" \
"buffer_load_dwordx4 a[104:107], v201, s[8:11], s43 offen sc1\n" \
"buffer_load_dwordx4 a[108:111], v201, s[8:11], s43 offen offset:1024 sc1\n" \
"v_readlane_b32 s43, v202, 20\n" \
"s_nop 4\n" \
"buffer_load_dwordx4 a[160:163], v201, s[8:11], s43 offen sc1\n" \
"buffer_load_dwordx4 a[164:167], v201, s[8:11], s43 offen offset:1024 sc1\n" \
"v_readlane_b32 s43, v202, 27\n" \
"s_nop 4\n" \
"buffer_load_dwordx4 a[216:219], v201, s[8:11], s43 offen sc1\n" \
"buffer_load_dwordx4 a[220:223], v201, s[8:11], s43 offen offset:1024 sc1\n" \
"v_readlane_b32 s43, v202, 34\n" \
"s_nop 4\n" \
"buffer_load_dwordx4 v[120:123], v201, s[8:11], s43 offen sc1\n" \
"buffer_load_dwordx4 v[124:127], v201, s[8:11], s43 offen offset:1024 sc1\n" \
"v_readlane_b32 s43, v202, 41\n" \
"s_nop 4\n" \
"buffer_load_dwordx4 v[176:179], v201, s[8:11], s43 offen sc1\n" \
"buffer_load_dwordx4 v[180:183], v201, s[8:11], s43 offen offset:1024 sc1\n" \
"s_add_u32 s43, s30, 7\n" \
"s_mov_b32 s50, 0\n" \
"s_branch .LX2_w80c\n" \
".LX2_w80l:\n" \
"buffer_load_dword v240, v204, s[4:7], 0 offen sc1\n" \
"s_waitcnt vmcnt(0)\n" \
".LX2_w80c:\n" \
"v_subrev_u32 v241, s43, v240\n" \
"v_cmp_gt_u32 vcc, 0x100, v241\n" \
"s_cmp_eq_u64 vcc, -1\n" \
"s_cbranch_scc1 .LX2_w80d\n" \
"s_sleep 2\n" \
"s_add_u32 s50, s50, 1\n" \
"s_cmp_lt_u32 s50, 0x20000\n" \
"s_cbranch_scc1 .LX2_w80l\n" \
"s_branch .LX2_end\n" \
".LX2_w80d:\n" \
"v_readlane_b32 s43, v202, 3\n" \
"s_nop 4\n" \
"buffer_load_dwordx4 a[24:27], v201, s[8:11], s43 offen sc1\n" \
"buffer_load_dwordx4 a[28:31], v201, s[8:11], s43 offen offset:1024 sc1\n" \
"v_readlane_b32 s43, v202, 10\n" \
"s_nop 4\n" \
"buffer_load_dwordx4 a[80:83], v201, s[8:11], s43 offen sc1\n" \
"buffer_load_dwordx4 a[84:87], v201, s[8:11], s43 offen offset:1024 sc1\n" \
"v_readlane_b32 s43, v202, 17\n" \
"s_nop 4\n" \
"buffer_load_dwordx4 a[136:139], v201, s[8:11], s43 offen sc1\n" \
"buffer_load_dwordx4 a[140:143], v201, s[8:11], s43 offen offset:1024 sc1\n" \
"v_readlane_b32 s43, v202, 24\n" \
"s_nop 4\n" \
"buffer_load_dwordx4 a[192:195], v201, s[8:11], s43 offen sc1\n" \
"buffer_load_dwordx4 a[196:199], v201, s[8:11], s43 offen offset:1024 sc1\n" \
"v_readlane_b32 s43, v202, 31\n" \
"s_nop 4\n" \
"buffer_load_dwordx4 a[248:251], v201, s[8:11], s43 offen sc1\n" \
"buffer_load_dwordx4 a[252:255], v201, s[8:11], s43 offen offset:1024 sc1\n" \
"v_readlane_b32 s43, v202, 38\n" \
"s_nop 4\n" \
"buffer_load_dwordx4 v[152:155], v201, s[8:11], s43 offen sc1\n" \
"buffer_load_dwordx4 v[156:159], v201, s[8:11], s43 offen offset:1024 sc1\n" \
"s_add_u32 s43, s30, 1\n" \
"s_mov_b32 s50, 0\n" \
".LX2_w81l:\n" \
"buffer_load_dword v240, v205, s[4:7], 0 offen sc1\n" \
"s_waitcnt vmcnt(0)\n" \
".LX2_w81c:\n" \
"v_subrev_u32 v241, s43, v240\n" \
"v_cmp_gt_u32 vcc, 0x100, v241\n" \
"s_cmp_eq_u64 vcc, -1\n" \
"s_cbranch_scc1 .LX2_w81d\n" \
"s_sleep 2\n" \
"s_add_u32 s50, s50, 1\n" \
"s_cmp_lt_u32 s50, 0x20000\n" \
"s_cbranch_scc1 .LX2_w81l\n" \
"s_branch .LX2_end\n" \
".LX2_w81d:\n" \
"v_readfirstlane_b32 s31, v241\n" \
"s_lshr_b32 s32, s31, 2\n" \
"s_xor_b32 s29, s31, s32\n" \
"s_and_b32 s29, s29, 3\n" \
"s_lshl_b32 s29, s29, 13\n" \
"s_bitset1_b32 s29, 21\n" \
"s_and_b32 s31, s31, 3\n" \
"s_lshl_b32 s31, s31, 13\n" \
"s_bitcmp1_b32 s39, 0\n" \
"s_cselect_b32 s43, s29, 0\n" \
"s_xor_b32 s31, s31, s43\n" \
"s_xor_b32 s32, s31, 0x4000\n" \
"s_mov_b32 s39, 0\n" \
"s_add_u32 s43, s30, 8\n" \
"s_mov_b32 s50, 0\n" \
".LX2_w82l:\n" \
"buffer_load_dword v240, v215, s[4:7], 0 offen sc1\n" \
"s_waitcnt vmcnt(0)\n" \
".LX2_w82c:\n" \
"v_subrev_u32 v241, s43, v240\n" \
"v_cmp_gt_u32 vcc, 0x100, v241\n" \
"s_cmp_eq_u64 vcc, -1\n" \
"s_cbranch_scc1 .LX2_w82d\n" \
"s_sleep 2\n" \
"s_add_u32 s50, s50, 1\n" \
"s_cmp_lt_u32 s50, 0x20000\n" \
"s_cbranch_scc1 .LX2_w82l\n" \
"s_branch .LX2_end\n" \
".LX2_w82d:\n" \
"s_add_u32 s50, s40, 0\n" \
"s_mul_i32 s50, s50, 28672\n" \
"s_add_u32 s50, s50, s34\n" \
"buffer_load_dwordx4 v[28:31], v201, s[12:15], s50 offen sc1\n" \
"buffer_load_dwordx4 v[32:35], v201, s[12:15], s50 offen offset:1024 sc1\n" \
"buffer_load_dwordx4 v[36:39], v201, s[12:15], s50 offen offset:2048 sc1\n" \
"buffer_load_dwordx4 v[40:43], v201, s[12:15], s50 offen offset:3072 sc1\n" \
"s_waitcnt vmcnt(0)\n" \
"s_barrier\n" \
"s_add_u32 s43, s30, 9\n" \
"s_mov_b32 s50, 0\n" \
".LX2_w83l:\n" \
"buffer_load_dword v240, v215, s[4:7], 0 offen sc1\n" \
"s_waitcnt vmcnt(0)\n" \
".LX2_w83c:\n" \
"v_subrev_u32 v241, s43, v240\n" \
"v_cmp_gt_u32 vcc, 0x100, v241\n" \
"s_cmp_eq_u64 vcc, -1\n" \
"s_cbranch_scc1 .LX2_w83d\n" \
"s_sleep 2\n" \
"s_add_u32 s50, s50, 1\n" \
"s_cmp_lt_u32 s50, 0x20000\n" \
"s_cbranch_scc1 .LX2_w83l\n" \
"s_branch .LX2_end\n" \
".LX2_w83d:\n" \
".LX2_pre:\n" \
".LX2_loop:\n" \
"s_add_u32 s43, s40, 2\n" \
"s_and_b32 s43, s43, 3\n" \
"s_bitcmp1_b32 s43, 0\n" \
"s_cselect_b32 s49, s32, s31\n" \
"s_lshl_b32 s43, s43, 19\n" \
"s_add_u32 s49, s49, s43\n" \
"ds_read_b128 v[0:3], v216 offset:0\n" \
"ds_read_b128 v[4:7], v216 offset:1024\n" \
"ds_read_b128 v[8:11], v216 offset:2048\n" \
"ds_read_b128 v[12:15], v216 offset:3072\n" \
"ds_read_b128 v[16:19], v216 offset:4096\n" \
"ds_read_b128 v[20:23], v216 offset:5120\n" \
"ds_read_b128 v[24:27], v216 offset:6144\n" \
"v_mov_b32 v60, 0\n" \
"v_mov_b32 v61, 0\n" \
"v_mov_b32 v62, 0\n" \
"v_mov_b32 v63, 0\n" \
"v_mov_b32 v64, 0\n" \
"v_mov_b32 v65, 0\n" \
"v_mov_b32 v66, 0\n" \
"v_mov_b32 v67, 0\n" \
"v_mov_b32 v68, 0\n" \
"v_mov_b32 v69, 0\n" \
"v_mov_b32 v70, 0\n" \
"v_mov_b32 v71, 0\n" \
"v_mov_b32 v72, 0\n" \
"v_mov_b32 v73, 0\n" \
"v_mov_b32 v74, 0\n" \
"v_mov_b32 v75, 0\n" \
"v_mov_b32 v76, 0\n" \
"v_mov_b32 v77, 0\n" \
"v_mov_b32 v78, 0\n" \
"v_mov_b32 v79, 0\n" \
"v_mov_b32 v80, 0\n" \
"v_mov_b32 v81, 0\n" \
"v_mov_b32 v82, 0\n" \
"v_mov_b32 v83, 0\n" \
"s_nop 1\n" \
"s_waitcnt lgkmcnt(6)\n" \
"v_smfmac_f32_16x16x64_bf16 v[60:63], v[0:3], a[0:7], v200\n" \
"v_smfmac_f32_16x16x64_bf16 v[64:67], v[0:3], a[56:63], v200\n" \
"v_smfmac_f32_16x16x64_bf16 v[68:71], v[0:3], a[112:119], v200\n" \
"v_smfmac_f32_16x16x64_bf16 v[72:75], v[0:3], a[168:175], v200\n" \
"v_smfmac_f32_16x16x64_bf16 v[76:79], v[0:3], a[224:231], v200\n" \
"v_smfmac_f32_16x16x64_bf16 v[80:83], v[0:3], v[128:135], v200\n" \
"s_waitcnt lgkmcnt(5)\n" \
"v_smfmac_f32_16x16x64_bf16 v[60:63], v[4:7], a[8:15], v200\n" \
"v_smfmac_f32_16x16x64_bf16 v[64:67], v[4:7], a[64:71], v200\n" \
"v_smfmac_f32_16x16x64_bf16 v[68:71], v[4:7], a[120:127], v200\n" \
"v_smfmac_f32_16x16x64_bf16 v[72:75], v[4:7], a[176:183], v200\n" \
"v_smfmac_f32_16x16x64_bf16 v[76:79], v[4:7], a[232:239], v200\n" \
"v_smfmac_f32_16x16x64_bf16 v[80:83], v[4:7], v[136:143], v200\n" \
"s_waitcnt lgkmcnt(4)\n" \
"v_smfmac_f32_16x16x64_bf16 v[60:63], v[8:11], a[16:23], v200\n" \
"v_smfmac_f32_16x16x64_bf16 v[64:67], v[8:11], a[72:79], v200\n" \
"v_smfmac_f32_16x16x64_bf16 v[68:71], v[8:11], a[128:135], v200\n" \
"v_smfmac_f32_16x16x64_bf16 v[72:75], v[8:11], a[184:191], v200\n" \
"v_smfmac_f32_16x16x64_bf16 v[76:79], v[8:11], a[240:247], v200\n" \
"v_smfmac_f32_16x16x64_bf16 v[80:83], v[8:11], v[144:151], v200\n" \
"s_waitcnt lgkmcnt(3)\n" \
"v_smfmac_f32_16x16x64_bf16 v[60:63], v[12:15], a[24:31], v200\n" \
"v_smfmac_f32_16x16x64_bf16 v[64:67], v[12:15], a[80:87], v200\n" \
"v_smfmac_f32_16x16x64_bf16 v[68:71], v[12:15], a[136:143], v200\n" \
"v_smfmac_f32_16x16x64_bf16 v[72:75], v[12:15], a[192:199], v200\n" \
"v_smfmac_f32_16x16x64_bf16 v[76:79], v[12:15], a[248:255], v200\n" \
"v_smfmac_f32_16x16x64_bf16 v[80:83], v[12:15], v[152:159], v200\n" \
"s_waitcnt lgkmcnt(2)\n" \
"v_smfmac_f32_16x16x64_bf16 v[60:63], v[16:19], a[32:39], v200\n" \
"v_smfmac_f32_16x16x64_bf16 v[64:67], v[16:19], a[88:95], v200\n" \
"v_smfmac_f32_16x16x64_bf16 v[68:71], v[16:19], a[144:151], v200\n" \
"v_smfmac_f32_16x16x64_bf16 v[72:75], v[16:19], a[200:207], v200\n" \
"s_barrier\n" \
"s_add_u32 s50, s40, 1\n" \
"s_mul_i32 s50, s50, 28672\n" \
"s_add_u32 s50, s50, s34\n" \
"buffer_load_dwordx4 v[44:47], v201, s[12:15], s50 offen sc1\n" \
"buffer_load_dwordx4 v[48:51], v201, s[12:15], s50 offen offset:1024 sc1\n" \
"buffer_load_dwordx4 v[52:55], v201, s[12:15], s50 offen offset:2048 sc1\n" \
"buffer_load_dwordx4 v[56:59], v201, s[12:15], s50 offen offset:3072 sc1\n" \
"buffer_load_dword v240, v215, s[4:7], 0 offen sc1\n" \
"v_smfmac_f32_16x16x64_bf16 v[76:79], v[16:19], v[104:111], v200\n" \
"v_smfmac_f32_16x16x64_bf16 v[80:83], v[16:19], v[160:167], v200\n" \
"s_waitcnt lgkmcnt(1)\n" \
"v_smfmac_f32_16x16x64_bf16 v[60:63], v[20:23], a[40:47], v200\n" \
"v_smfmac_f32_16x16x64_bf16 v[64:67], v[20:23], a[96:103], v200\n" \
"v_smfmac_f32_16x16x64_bf16 v[68:71], v[20:23], a[152:159], v200\n" \
"v_smfmac_f32_16x16x64_bf16 v[72:75], v[20:23], a[208:215], v200\n" \
"v_smfmac_f32_16x16x64_bf16 v[76:79], v[20:23], v[112:119], v200\n" \
"v_smfmac_f32_16x16x64_bf16 v[80:83], v[20:23], v[168:175], v200\n" \
"s_waitcnt lgkmcnt(0)\n" \
"v_smfmac_f32_16x16x64_bf16 v[60:63], v[24:27], a[48:55], v200\n" \
"v_smfmac_f32_16x16x64_bf16 v[64:67], v[24:27], a[104:111], v200\n" \
"v_smfmac_f32_16x16x64_bf16 v[68:71], v[24:27], a[160:167], v200\n" \
"v_smfmac_f32_16x16x64_bf16 v[72:75], v[24:27], a[216:223], v200\n" \
"v_smfmac_f32_16x16x64_bf16 v[76:79], v[24:27], v[120:127], v200\n" \
"v_smfmac_f32_16x16x64_bf16 v[80:83], v[24:27], v[176:183], v200\n" \
"s_nop 7\n" \
"v_permlane32_swap_b32 v60, v64\n" \
"v_permlane32_swap_b32 v61, v65\n" \
"v_permlane32_swap_b32 v62, v66\n" \
"v_permlane32_swap_b32 v63, v67\n" \
"v_add_f32 v60, v60, v64\n" \
"v_add_f32 v61, v61, v65\n" \
"v_add_f32 v62, v62, v66\n" \
"v_add_f32 v63, v63, v67\n" \
"v_cvt_pk_bf16_f32 v184, v60, v61\n" \
"v_cvt_pk_bf16_f32 v185, v62, v63\n" \
"ds_write_b64 v217, v[184:185] offset:0\n" \
"v_permlane32_swap_b32 v68, v72\n" \
"v_permlane32_swap_b32 v69, v73\n" \
"v_permlane32_swap_b32 v70, v74\n" \
"v_permlane32_swap_b32 v71, v75\n" \
"v_add_f32 v68, v68, v72\n" \
"v_add_f32 v69, v69, v73\n" \
"v_add_f32 v70, v70, v74\n" \
"v_add_f32 v71, v71, v75\n" \
"v_cvt_pk_bf16_f32 v186, v68, v69\n" \
"v_cvt_pk_bf16_f32 v187, v70, v71\n" \
"ds_write_b64 v217, v[186:187] offset:512\n" \
"v_permlane32_swap_b32 v76, v80\n" \
"v_permlane32_swap_b32 v77, v81\n" \
"v_permlane32_swap_b32 v78, v82\n" \
"v_permlane32_swap_b32 v79, v83\n" \
"v_add_f32 v76, v76, v80\n" \
"v_add_f32 v77, v77, v81\n" \
"v_add_f32 v78, v78, v82\n" \
"v_add_f32 v79, v79, v83\n" \
"v_cvt_pk_bf16_f32 v188, v76, v77\n" \
"v_cvt_pk_bf16_f32 v189, v78, v79\n" \
"ds_write_b64 v217, v[188:189] offset:1024\n" \
"ds_read_b64_tr_b16 v[0:1], v218 offset:0\n" \
"ds_read_b64_tr_b16 v[2:3], v218 offset:64\n" \
"ds_read_b64_tr_b16 v[4:5], v218 offset:1024\n" \
"ds_read_b64_tr_b16 v[6:7], v218 offset:1088\n" \
"s_add_u32 s43, s40, 0\n" \
"s_mul_i32 s43, s43, 7168\n" \
"s_waitcnt lgkmcnt(0)\n" \
"buffer_store_dwordx4 v[0:3], v219, s[16:19], s43 offen sc1\n" \
"buffer_store_dwordx4 v[4:7], v221, s[16:19], s43 offen offset:1024 sc1\n" \
"s_waitcnt vmcnt(7)\n" \
"s_barrier\n" \
"ds_read_b64_tr_b16 v[92:93], v213\n" \
"ds_read_b64_tr_b16 v[94:95], v213 offset:64\n" \
"ds_read_b32 v84, v252 offset:16\n" \
"v_mov_b32 v245, v245\n" \
"s_nop 2\n" \
"v_mov_b32 v245, v245\n" \
"s_nop 2\n" \
"v_mov_b32 v245, v245\n" \
"s_waitcnt lgkmcnt(1)\n" \
"v_mfma_f32_16x16x32_bf16 v[68:71], v[92:95], v[28:31], 0\n" \
"s_waitcnt lgkmcnt(0)\n" \
"v_xor_b32 v85, s29, v84\n" \
"v_mfma_f32_16x16x32_bf16 v[76:79], v[92:95], v[36:39], 0\n" \
"v_add_u32 v84, v84, v210\n" \
"v_add_u32 v85, v85, v210\n" \
"v_mfma_f32_16x16x32_bf16 v[72:75], v[92:95], v[32:35], 0\n" \
"s_nop 1\n" \
"v_mfma_f32_16x16x32_bf16 v[80:83], v[92:95], v[40:43], 0\n" \
"v_cvt_pk_bf16_f32 v96, v68, v69\n" \
"v_cvt_pk_bf16_f32 v97, v70, v71\n" \
"v_cvt_pk_bf16_f32 v98, v76, v77\n" \
"v_cvt_pk_bf16_f32 v99, v78, v79\n" \
"s_mov_b64 exec, s[2:3]\n" \
"v_cvt_pk_bf16_f32 v96, v72, v73\n" \
"v_cvt_pk_bf16_f32 v97, v74, v75\n" \
"s_nop 0\n" \
"v_cvt_pk_bf16_f32 v98, v80, v81\n" \
"v_cvt_pk_bf16_f32 v99, v82, v83\n" \
"s_mov_b64 exec, -1\n" \
"s_nop 1\n" \
"v_permlane16_swap_b32 v96, v98\n" \
"v_permlane16_swap_b32 v97, v99\n" \
"buffer_store_dwordx4 v[96:99], v84, s[4:7], 0 offen sc1\n" \
"s_barrier\n" \
"buffer_store_dwordx4 v[96:99], v85, s[4:7], 0 offen sc1\n" \
"s_nop 15\n" \
"s_nop 15\n" \
"s_nop 15\n" \
"s_nop 15\n" \
"s_nop 15\n" \
"buffer_store_dwordx4 v[100:103], v214, s[4:7], s49 offen sc1\n" \
"s_xor_b32 s43, s49, s29\n" \
"buffer_store_dwordx4 v[100:103], v214, s[4:7], s43 offen sc1\n" \
"s_waitcnt vmcnt(4)\n" \
"v_mov_b32 v28, v44\n" \
"v_mov_b32 v29, v45\n" \
"v_mov_b32 v30, v46\n" \
"v_mov_b32 v31, v47\n" \
"v_mov_b32 v32, v48\n" \
"v_mov_b32 v33, v49\n" \
"v_mov_b32 v34, v50\n" \
"v_mov_b32 v35, v51\n" \
"v_mov_b32 v36, v52\n" \
"v_mov_b32 v37, v53\n" \
"v_mov_b32 v38, v54\n" \
"v_mov_b32 v39, v55\n" \
"v_mov_b32 v40, v56\n" \
"v_mov_b32 v41, v57\n" \
"v_mov_b32 v42, v58\n" \
"v_mov_b32 v43, v59\n" \
"s_cmp_eq_u32 s40, -1\n" \
"s_cbranch_scc1 .LX2_nogu\n" \
"s_add_u32 s36, s40, 10\n" \
"s_min_u32 s36, s36, 71\n" \
"s_add_u32 s43, s30, s36\n" \
"s_mov_b32 s50, 0\n" \
"s_branch .LX2_w84c\n" \
".LX2_w84l:\n" \
"buffer_load_dword v240, v215, s[4:7], 0 offen sc1\n" \
"s_waitcnt vmcnt(0)\n" \
".LX2_w84c:\n" \
"v_subrev_u32 v241, s43, v240\n" \
"v_cmp_gt_u32 vcc, 0x100, v241\n" \
"s_cmp_eq_u64 vcc, -1\n" \
"s_cbranch_scc1 .LX2_w84d\n" \
"s_sleep 2\n" \
"s_add_u32 s50, s50, 1\n" \
"s_cmp_lt_u32 s50, 0x20000\n" \
"s_cbranch_scc1 .LX2_w84l\n" \
"s_branch .LX2_end\n" \
".LX2_w84d:\n" \
".LX2_nogu:\n" \
"s_add_u32 s40, s40, 1\n" \
"s_waitcnt lgkmcnt(0)\n" \
"s_barrier\n" \
"s_cmp_eq_u32 s40, 0\n" \
"s_cbranch_scc1 .LX2_wet\n" \
"s_cmp_lt_u32 s40, 63\n" \
"s_cbranch_scc1 .LX2_loop\n" \
"ds_read_b128 v[0:3], v216 offset:0\n" \
"ds_read_b128 v[4:7], v216 offset:1024\n" \
"ds_read_b128 v[8:11], v216 offset:2048\n" \
"ds_read_b128 v[12:15], v216 offset:3072\n" \
"ds_read_b128 v[16:19], v216 offset:4096\n" \
"ds_read_b128 v[20:23], v216 offset:5120\n" \
"ds_read_b128 v[24:27], v216 offset:6144\n" \
"v_mov_b32 v60, 0\n" \
"v_mov_b32 v61, 0\n" \
"v_mov_b32 v62, 0\n" \
"v_mov_b32 v63, 0\n" \
"v_mov_b32 v64, 0\n" \
"v_mov_b32 v65, 0\n" \
"v_mov_b32 v66, 0\n" \
"v_mov_b32 v67, 0\n" \
"v_mov_b32 v68, 0\n" \
"v_mov_b32 v69, 0\n" \
"v_mov_b32 v70, 0\n" \
"v_mov_b32 v71, 0\n" \
"v_mov_b32 v72, 0\n" \
"v_mov_b32 v73, 0\n" \
"v_mov_b32 v74, 0\n" \
"v_mov_b32 v75, 0\n" \
"v_mov_b32 v76, 0\n" \
"v_mov_b32 v77, 0\n" \
"v_mov_b32 v78, 0\n" \
"v_mov_b32 v79, 0\n" \
"v_mov_b32 v80, 0\n" \
"v_mov_b32 v81, 0\n" \
"v_mov_b32 v82, 0\n" \
"v_mov_b32 v83, 0\n" \
"s_nop 1\n" \
"s_waitcnt lgkmcnt(6)\n" \
"v_smfmac_f32_16x16x64_bf16 v[60:63], v[0:3], a[0:7], v200\n" \
"v_smfmac_f32_16x16x64_bf16 v[64:67], v[0:3], a[56:63], v200\n" \
"v_smfmac_f32_16x16x64_bf16 v[68:71], v[0:3], a[112:119], v200\n" \
"v_smfmac_f32_16x16x64_bf16 v[72:75], v[0:3], a[168:175], v200\n" \
"v_smfmac_f32_16x16x64_bf16 v[76:79], v[0:3], a[224:231], v200\n" \
"v_smfmac_f32_16x16x64_bf16 v[80:83], v[0:3], v[128:135], v200\n" \
"s_waitcnt lgkmcnt(5)\n" \
"v_smfmac_f32_16x16x64_bf16 v[60:63], v[4:7], a[8:15], v200\n" \
"v_smfmac_f32_16x16x64_bf16 v[64:67], v[4:7], a[64:71], v200\n" \
"v_smfmac_f32_16x16x64_bf16 v[68:71], v[4:7], a[120:127], v200\n" \
"v_smfmac_f32_16x16x64_bf16 v[72:75], v[4:7], a[176:183], v200\n" \
"v_smfmac_f32_16x16x64_bf16 v[76:79], v[4:7], a[232:239], v200\n" \
"v_smfmac_f32_16x16x64_bf16 v[80:83], v[4:7], v[136:143], v200\n" \
"s_waitcnt lgkmcnt(4)\n" \
"v_smfmac_f32_16x16x64_bf16 v[60:63], v[8:11], a[16:23], v200\n" \
"v_smfmac_f32_16x16x64_bf16 v[64:67], v[8:11], a[72:79], v200\n" \
"v_smfmac_f32_16x16x64_bf16 v[68:71], v[8:11], a[128:135], v200\n" \
"v_smfmac_f32_16x16x64_bf16 v[72:75], v[8:11], a[184:191], v200\n" \
"v_smfmac_f32_16x16x64_bf16 v[76:79], v[8:11], a[240:247], v200\n" \
"v_smfmac_f32_16x16x64_bf16 v[80:83], v[8:11], v[144:151], v200\n" \
"s_waitcnt lgkmcnt(3)\n" \
"v_smfmac_f32_16x16x64_bf16 v[60:63], v[12:15], a[24:31], v200\n" \
"v_smfmac_f32_16x16x64_bf16 v[64:67], v[12:15], a[80:87], v200\n" \
"v_smfmac_f32_16x16x64_bf16 v[68:71], v[12:15], a[136:143], v200\n" \
"v_smfmac_f32_16x16x64_bf16 v[72:75], v[12:15], a[192:199], v200\n" \
"v_smfmac_f32_16x16x64_bf16 v[76:79], v[12:15], a[248:255], v200\n" \
"v_smfmac_f32_16x16x64_bf16 v[80:83], v[12:15], v[152:159], v200\n" \
"s_waitcnt lgkmcnt(2)\n" \
"v_smfmac_f32_16x16x64_bf16 v[60:63], v[16:19], a[32:39], v200\n" \
"v_smfmac_f32_16x16x64_bf16 v[64:67], v[16:19], a[88:95], v200\n" \
"v_smfmac_f32_16x16x64_bf16 v[68:71], v[16:19], a[144:151], v200\n" \
"v_smfmac_f32_16x16x64_bf16 v[72:75], v[16:19], a[200:207], v200\n" \
"v_smfmac_f32_16x16x64_bf16 v[76:79], v[16:19], v[104:111], v200\n" \
"v_smfmac_f32_16x16x64_bf16 v[80:83], v[16:19], v[160:167], v200\n" \
"s_waitcnt lgkmcnt(1)\n" \
"v_smfmac_f32_16x16x64_bf16 v[60:63], v[20:23], a[40:47], v200\n" \
"v_smfmac_f32_16x16x64_bf16 v[64:67], v[20:23], a[96:103], v200\n" \
"v_smfmac_f32_16x16x64_bf16 v[68:71], v[20:23], a[152:159], v200\n" \
"v_smfmac_f32_16x16x64_bf16 v[72:75], v[20:23], a[208:215], v200\n" \
"v_smfmac_f32_16x16x64_bf16 v[76:79], v[20:23], v[112:119], v200\n" \
"v_smfmac_f32_16x16x64_bf16 v[80:83], v[20:23], v[168:175], v200\n" \
"s_waitcnt lgkmcnt(0)\n" \
"v_smfmac_f32_16x16x64_bf16 v[60:63], v[24:27], a[48:55], v200\n" \
"v_smfmac_f32_16x16x64_bf16 v[64:67], v[24:27], a[104:111], v200\n" \
"v_smfmac_f32_16x16x64_bf16 v[68:71], v[24:27], a[160:167], v200\n" \
"v_smfmac_f32_16x16x64_bf16 v[72:75], v[24:27], a[216:223], v200\n" \
"v_smfmac_f32_16x16x64_bf16 v[76:79], v[24:27], v[120:127], v200\n" \
"v_smfmac_f32_16x16x64_bf16 v[80:83], v[24:27], v[176:183], v200\n" \
"s_nop 7\n" \
"v_permlane32_swap_b32 v60, v64\n" \
"v_permlane32_swap_b32 v61, v65\n" \
"v_permlane32_swap_b32 v62, v66\n" \
"v_permlane32_swap_b32 v63, v67\n" \
"v_add_f32 v60, v60, v64\n" \
"v_add_f32 v61, v61, v65\n" \
"v_add_f32 v62, v62, v66\n" \
"v_add_f32 v63, v63, v67\n" \
"v_cvt_pk_bf16_f32 v184, v60, v61\n" \
"v_cvt_pk_bf16_f32 v185, v62, v63\n" \
"ds_write_b64 v217, v[184:185] offset:0\n" \
"v_permlane32_swap_b32 v68, v72\n" \
"v_permlane32_swap_b32 v69, v73\n" \
"v_permlane32_swap_b32 v70, v74\n" \
"v_permlane32_swap_b32 v71, v75\n" \
"v_add_f32 v68, v68, v72\n" \
"v_add_f32 v69, v69, v73\n" \
"v_add_f32 v70, v70, v74\n" \
"v_add_f32 v71, v71, v75\n" \
"v_cvt_pk_bf16_f32 v186, v68, v69\n" \
"v_cvt_pk_bf16_f32 v187, v70, v71\n" \
"ds_write_b64 v217, v[186:187] offset:512\n" \
"v_permlane32_swap_b32 v76, v80\n" \
"v_permlane32_swap_b32 v77, v81\n" \
"v_permlane32_swap_b32 v78, v82\n" \
"v_permlane32_swap_b32 v79, v83\n" \
"v_add_f32 v76, v76, v80\n" \
"v_add_f32 v77, v77, v81\n" \
"v_add_f32 v78, v78, v82\n" \
"v_add_f32 v79, v79, v83\n" \
"v_cvt_pk_bf16_f32 v188, v76, v77\n" \
"v_cvt_pk_bf16_f32 v189, v78, v79\n" \
"ds_write_b64 v217, v[188:189] offset:1024\n" \
"ds_read_b64_tr_b16 v[0:1], v218 offset:0\n" \
"ds_read_b64_tr_b16 v[2:3], v218 offset:64\n" \
"ds_read_b64_tr_b16 v[4:5], v218 offset:1024\n" \
"ds_read_b64_tr_b16 v[6:7], v218 offset:1088\n" \
"s_add_u32 s43, s40, 0\n" \
"s_mul_i32 s43, s43, 7168\n" \
"s_waitcnt lgkmcnt(0)\n" \
"buffer_store_dwordx4 v[0:3], v219, s[16:19], s43 offen sc1\n" \
"buffer_store_dwordx4 v[4:7], v221, s[16:19], s43 offen offset:1024 sc1\n" \
"s_waitcnt vmcnt(0)\n" \
"s_barrier\n" \
"ds_read_b128 v[0:3], v216 offset:7168\n" \
"ds_read_b128 v[4:7], v216 offset:8192\n" \
"ds_read_b128 v[8:11], v216 offset:9216\n" \
"ds_read_b128 v[12:15], v216 offset:10240\n" \
"ds_read_b128 v[16:19], v216 offset:11264\n" \
"ds_read_b128 v[20:23], v216 offset:12288\n" \
"ds_read_b128 v[24:27], v216 offset:13312\n" \
"v_mov_b32 v60, 0\n" \
"v_mov_b32 v61, 0\n" \
"v_mov_b32 v62, 0\n" \
"v_mov_b32 v63, 0\n" \
"v_mov_b32 v64, 0\n" \
"v_mov_b32 v65, 0\n" \
"v_mov_b32 v66, 0\n" \
"v_mov_b32 v67, 0\n" \
"v_mov_b32 v68, 0\n" \
"v_mov_b32 v69, 0\n" \
"v_mov_b32 v70, 0\n" \
"v_mov_b32 v71, 0\n" \
"v_mov_b32 v72, 0\n" \
"v_mov_b32 v73, 0\n" \
"v_mov_b32 v74, 0\n" \
"v_mov_b32 v75, 0\n" \
"v_mov_b32 v76, 0\n" \
"v_mov_b32 v77, 0\n" \
"v_mov_b32 v78, 0\n" \
"v_mov_b32 v79, 0\n" \
"v_mov_b32 v80, 0\n" \
"v_mov_b32 v81, 0\n" \
"v_mov_b32 v82, 0\n" \
"v_mov_b32 v83, 0\n" \
"s_nop 1\n" \
"s_waitcnt lgkmcnt(6)\n" \
"v_smfmac_f32_16x16x64_bf16 v[60:63], v[0:3], a[0:7], v200\n" \
"v_smfmac_f32_16x16x64_bf16 v[64:67], v[0:3], a[56:63], v200\n" \
"v_smfmac_f32_16x16x64_bf16 v[68:71], v[0:3], a[112:119], v200\n" \
"v_smfmac_f32_16x16x64_bf16 v[72:75], v[0:3], a[168:175], v200\n" \
"v_smfmac_f32_16x16x64_bf16 v[76:79], v[0:3], a[224:231], v200\n" \
"v_smfmac_f32_16x16x64_bf16 v[80:83], v[0:3], v[128:135], v200\n" \
"s_waitcnt lgkmcnt(5)\n" \
"v_smfmac_f32_16x16x64_bf16 v[60:63], v[4:7], a[8:15], v200\n" \
"v_smfmac_f32_16x16x64_bf16 v[64:67], v[4:7], a[64:71], v200\n" \
"v_smfmac_f32_16x16x64_bf16 v[68:71], v[4:7], a[120:127], v200\n" \
"v_smfmac_f32_16x16x64_bf16 v[72:75], v[4:7], a[176:183], v200\n" \
"v_smfmac_f32_16x16x64_bf16 v[76:79], v[4:7], a[232:239], v200\n" \
"v_smfmac_f32_16x16x64_bf16 v[80:83], v[4:7], v[136:143], v200\n" \
"s_waitcnt lgkmcnt(4)\n" \
"v_smfmac_f32_16x16x64_bf16 v[60:63], v[8:11], a[16:23], v200\n" \
"v_smfmac_f32_16x16x64_bf16 v[64:67], v[8:11], a[72:79], v200\n" \
"v_smfmac_f32_16x16x64_bf16 v[68:71], v[8:11], a[128:135], v200\n" \
"v_smfmac_f32_16x16x64_bf16 v[72:75], v[8:11], a[184:191], v200\n" \
"v_smfmac_f32_16x16x64_bf16 v[76:79], v[8:11], a[240:247], v200\n" \
"v_smfmac_f32_16x16x64_bf16 v[80:83], v[8:11], v[144:151], v200\n" \
"s_waitcnt lgkmcnt(3)\n" \
"v_smfmac_f32_16x16x64_bf16 v[60:63], v[12:15], a[24:31], v200\n" \
"v_smfmac_f32_16x16x64_bf16 v[64:67], v[12:15], a[80:87], v200\n" \
"v_smfmac_f32_16x16x64_bf16 v[68:71], v[12:15], a[136:143], v200\n" \
"v_smfmac_f32_16x16x64_bf16 v[72:75], v[12:15], a[192:199], v200\n" \
"v_smfmac_f32_16x16x64_bf16 v[76:79], v[12:15], a[248:255], v200\n" \
"v_smfmac_f32_16x16x64_bf16 v[80:83], v[12:15], v[152:159], v200\n" \
"s_waitcnt lgkmcnt(2)\n" \
"v_smfmac_f32_16x16x64_bf16 v[60:63], v[16:19], a[32:39], v200\n" \
"v_smfmac_f32_16x16x64_bf16 v[64:67], v[16:19], a[88:95], v200\n" \
"v_smfmac_f32_16x16x64_bf16 v[68:71], v[16:19], a[144:151], v200\n" \
"v_smfmac_f32_16x16x64_bf16 v[72:75], v[16:19], a[200:207], v200\n" \
"v_smfmac_f32_16x16x64_bf16 v[76:79], v[16:19], v[104:111], v200\n" \
"v_smfmac_f32_16x16x64_bf16 v[80:83], v[16:19], v[160:167], v200\n" \
"s_waitcnt lgkmcnt(1)\n" \
"v_smfmac_f32_16x16x64_bf16 v[60:63], v[20:23], a[40:47], v200\n" \
"v_smfmac_f32_16x16x64_bf16 v[64:67], v[20:23], a[96:103], v200\n" \
"v_smfmac_f32_16x16x64_bf16 v[68:71], v[20:23], a[152:159], v200\n" \
"v_smfmac_f32_16x16x64_bf16 v[72:75], v[20:23], a[208:215], v200\n" \
"v_smfmac_f32_16x16x64_bf16 v[76:79], v[20:23], v[112:119], v200\n" \
"v_smfmac_f32_16x16x64_bf16 v[80:83], v[20:23], v[168:175], v200\n" \
"s_waitcnt lgkmcnt(0)\n" \
"v_smfmac_f32_16x16x64_bf16 v[60:63], v[24:27], a[48:55], v200\n" \
"v_smfmac_f32_16x16x64_bf16 v[64:67], v[24:27], a[104:111], v200\n" \
"v_smfmac_f32_16x16x64_bf16 v[68:71], v[24:27], a[160:167], v200\n" \
"v_smfmac_f32_16x16x64_bf16 v[72:75], v[24:27], a[216:223], v200\n" \
"v_smfmac_f32_16x16x64_bf16 v[76:79], v[24:27], v[120:127], v200\n" \
"v_smfmac_f32_16x16x64_bf16 v[80:83], v[24:27], v[176:183], v200\n" \
"s_nop 7\n" \
"v_permlane32_swap_b32 v60, v64\n" \
"v_permlane32_swap_b32 v61, v65\n" \
"v_permlane32_swap_b32 v62, v66\n" \
"v_permlane32_swap_b32 v63, v67\n" \
"v_add_f32 v60, v60, v64\n" \
"v_add_f32 v61, v61, v65\n" \
"v_add_f32 v62, v62, v66\n" \
"v_add_f32 v63, v63, v67\n" \
"v_cvt_pk_bf16_f32 v184, v60, v61\n" \
"v_cvt_pk_bf16_f32 v185, v62, v63\n" \
"ds_write_b64 v217, v[184:185] offset:0\n" \
"v_permlane32_swap_b32 v68, v72\n" \
"v_permlane32_swap_b32 v69, v73\n" \
"v_permlane32_swap_b32 v70, v74\n" \
"v_permlane32_swap_b32 v71, v75\n" \
"v_add_f32 v68, v68, v72\n" \
"v_add_f32 v69, v69, v73\n" \
"v_add_f32 v70, v70, v74\n" \
"v_add_f32 v71, v71, v75\n" \
"v_cvt_pk_bf16_f32 v186, v68, v69\n" \
"v_cvt_pk_bf16_f32 v187, v70, v71\n" \
"ds_write_b64 v217, v[186:187] offset:512\n" \
"v_permlane32_swap_b32 v76, v80\n" \
"v_permlane32_swap_b32 v77, v81\n" \
"v_permlane32_swap_b32 v78, v82\n" \
"v_permlane32_swap_b32 v79, v83\n" \
"v_add_f32 v76, v76, v80\n" \
"v_add_f32 v77, v77, v81\n" \
"v_add_f32 v78, v78, v82\n" \
"v_add_f32 v79, v79, v83\n" \
"v_cvt_pk_bf16_f32 v188, v76, v77\n" \
"v_cvt_pk_bf16_f32 v189, v78, v79\n" \
"ds_write_b64 v217, v[188:189] offset:1024\n" \
"ds_read_b64_tr_b16 v[0:1], v218 offset:0\n" \
"ds_read_b64_tr_b16 v[2:3], v218 offset:64\n" \
"ds_read_b64_tr_b16 v[4:5], v218 offset:1024\n" \
"ds_read_b64_tr_b16 v[6:7], v218 offset:1088\n" \
"s_add_u32 s43, s40, 1\n" \
"s_mul_i32 s43, s43, 7168\n" \
"s_waitcnt lgkmcnt(0)\n" \
"buffer_store_dwordx4 v[0:3], v219, s[16:19], s43 offen sc1\n" \
"buffer_store_dwordx4 v[4:7], v221, s[16:19], s43 offen offset:1024 sc1\n" \
"s_waitcnt vmcnt(0)\n" \
"s_barrier\n" \
".LX2_end:\n" \
"s_endpgm\n" \
".p2align 6\n" \
"fk_role_X3:\n" \
"ds_read_b32 v200, v255 offset:0\n" \
"ds_read_b32 v201, v255 offset:256\n" \
"ds_read_b32 v202, v255 offset:512\n" \
"ds_read_b32 v203, v255 offset:768\n" \
"ds_read_b32 v204, v255 offset:1024\n" \
"ds_read_b32 v205, v255 offset:1280\n" \
"ds_read_b32 v206, v255 offset:1536\n" \
"ds_read_b32 v207, v255 offset:1792\n" \
"ds_read_b32 v208, v255 offset:2048\n" \
"ds_read_b32 v209, v255 offset:2304\n" \
"ds_read_b32 v210, v255 offset:2560\n" \
"ds_read_b32 v211, v255 offset:2816\n" \
"ds_read_b32 v212, v255 offset:3072\n" \
"ds_read_b32 v213, v255 offset:3328\n" \
"ds_read_b32 v214, v255 offset:3584\n" \
"ds_read_b32 v215, v255 offset:3840\n" \
"ds_read_b32 v216, v255 offset:4096\n" \
"ds_read_b32 v217, v255 offset:4352\n" \
"ds_read_b32 v218, v255 offset:4608\n" \
"ds_read_b32 v219, v255 offset:4864\n" \
"ds_read_b32 v220, v255 offset:5120\n" \
"ds_read_b32 v221, v255 offset:5376\n" \
"ds_read_b32 v254, v255 offset:7936\n" \
"s_waitcnt lgkmcnt(0)\n" \
"v_readlane_b32 s4, v254, 0\n" \
"v_readlane_b32 s5, v254, 1\n" \
"v_readlane_b32 s6, v254, 2\n" \
"v_readlane_b32 s7, v254, 3\n" \
"v_readlane_b32 s8, v254, 4\n" \
"v_readlane_b32 s9, v254, 5\n" \
"v_readlane_b32 s10, v254, 6\n" \
"v_readlane_b32 s11, v254, 7\n" \
"v_readlane_b32 s12, v254, 8\n" \
"v_readlane_b32 s13, v254, 9\n" \
"v_readlane_b32 s14, v254, 10\n" \
"v_readlane_b32 s15, v254, 11\n" \
"v_readlane_b32 s16, v254, 12\n" \
"v_readlane_b32 s17, v254, 13\n" \
"v_readlane_b32 s18, v254, 14\n" \
"v_readlane_b32 s19, v254, 15\n" \
"v_readlane_b32 s20, v254, 16\n" \
"v_readlane_b32 s21, v254, 17\n" \
"v_readlane_b32 s22, v254, 18\n" \
"v_readlane_b32 s23, v254, 19\n" \
"v_readlane_b32 s24, v254, 20\n" \
"v_readlane_b32 s25, v254, 21\n" \
"v_readlane_b32 s26, v254, 22\n" \
"v_readlane_b32 s27, v254, 23\n" \
"v_readlane_b32 s28, v254, 24\n" \
"v_readlane_b32 s29, v254, 25\n" \
"v_readlane_b32 s30, v254, 26\n" \
"v_readlane_b32 s31, v254, 27\n" \
"v_readlane_b32 s32, v254, 28\n" \
"v_readlane_b32 s33, v254, 29\n" \
"v_readlane_b32 s34, v254, 30\n" \
"v_readlane_b32 s35, v254, 31\n" \
"v_readlane_b32 s36, v254, 32\n" \
"v_readlane_b32 s37, v254, 33\n" \
"v_readlane_b32 s38, v254, 34\n" \
"v_readlane_b32 s39, v254, 35\n" \
"s_mov_b32 s40, 0\n" \
"s_mov_b32 s51, 0\n" \
"s_nop 0\n" \
"v_mov_b32 v252, s38\n" \
"s_nop 4\n" \
"v_mov_b32 v100, -1\n" \
"v_mov_b32 v101, -1\n" \
"v_mov_b32 v102, -1\n" \
"v_mov_b32 v103, -1\n" \
"s_mov_b32 s35, -2\n" \
"s_mov_b32 s2, 0\n" \
"s_mov_b32 s3, -1\n" \
"v_writelane_b32 v251, s6, 0\n" \
"v_writelane_b32 v251, s10, 1\n" \
"v_writelane_b32 v251, s14, 2\n" \
"v_writelane_b32 v251, s18, 3\n" \
"s_mov_b32 s6, 0\n" \
"s_mov_b32 s10, 0\n" \
"s_mov_b32 s14, 0\n" \
"s_mov_b32 s18, 0\n" \
"s_mov_b32 s40, -1\n" \
"s_branch .LX3_pre\n" \
".LX3_wet:\n" \
"s_waitcnt vmcnt(0)\n" \
"v_readlane_b32 s6, v251, 0\n" \
"v_readlane_b32 s10, v251, 1\n" \
"v_readlane_b32 s14, v251, 2\n" \
"v_readlane_b32 s18, v251, 3\n" \
"s_nop 4\n" \
"s_add_u32 s43, s30, 1\n" \
"s_mov_b32 s50, 0\n" \
".LX3_w85l:\n" \
"buffer_load_dword v240, v205, s[4:7], 0 offen sc1\n" \
"s_waitcnt vmcnt(0)\n" \
".LX3_w85c:\n" \
"v_subrev_u32 v241, s43, v240\n" \
"v_cmp_gt_u32 vcc, 0x100, v241\n" \
"s_cmp_eq_u64 vcc, -1\n" \
"s_cbranch_scc1 .LX3_w85d\n" \
"s_sleep 2\n" \
"s_add_u32 s50, s50, 1\n" \
"s_cmp_lt_u32 s50, 0x20000\n" \
"s_cbranch_scc1 .LX3_w85l\n" \
"s_branch .LX3_end\n" \
".LX3_w85d:\n" \
"v_readfirstlane_b32 s31, v241\n" \
"s_lshr_b32 s32, s31, 2\n" \
"s_xor_b32 s29, s31, s32\n" \
"s_and_b32 s29, s29, 3\n" \
"s_lshl_b32 s29, s29, 13\n" \
"s_bitset1_b32 s29, 21\n" \
"s_and_b32 s31, s31, 3\n" \
"s_lshl_b32 s31, s31, 13\n" \
"s_bitcmp1_b32 s39, 0\n" \
"s_cselect_b32 s43, s29, 0\n" \
"s_xor_b32 s31, s31, s43\n" \
"s_xor_b32 s32, s31, 0x4000\n" \
"s_mov_b32 s39, 0\n" \
"s_add_u32 s43, s30, 8\n" \
"s_mov_b32 s50, 0\n" \
".LX3_w86l:\n" \
"buffer_load_dword v240, v215, s[4:7], 0 offen sc1\n" \
"s_waitcnt vmcnt(0)\n" \
".LX3_w86c:\n" \
"v_subrev_u32 v241, s43, v240\n" \
"v_cmp_gt_u32 vcc, 0x100, v241\n" \
"s_cmp_eq_u64 vcc, -1\n" \
"s_cbranch_scc1 .LX3_w86d\n" \
"s_sleep 2\n" \
"s_add_u32 s50, s50, 1\n" \
"s_cmp_lt_u32 s50, 0x20000\n" \
"s_cbranch_scc1 .LX3_w86l\n" \
"s_branch .LX3_end\n" \
".LX3_w86d:\n" \
"s_add_u32 s43, s40, 0\n" \
"s_mul_i32 s43, s43, 28672\n" \
"s_add_u32 s43, s43, s33\n" \
"buffer_load_dwordx4 a[0:3], v201, s[8:11], s43 offen sc1\n" \
"buffer_load_dwordx4 a[4:7], v201, s[8:11], s43 offen offset:1024 sc1\n" \
"buffer_load_dwordx4 a[8:11], v201, s[8:11], s43 offen offset:2048 sc1\n" \
"buffer_load_dwordx4 a[12:15], v201, s[8:11], s43 offen offset:3072 sc1\n" \
"s_add_u32 s43, s43, 4096\n" \
"buffer_load_dwordx4 a[16:19], v201, s[8:11], s43 offen sc1\n" \
"buffer_load_dwordx4 a[20:23], v201, s[8:11], s43 offen offset:1024 sc1\n" \
"buffer_load_dwordx4 a[24:27], v201, s[8:11], s43 offen offset:2048 sc1\n" \
"buffer_load_dwordx4 a[28:31], v201, s[8:11], s43 offen offset:3072 sc1\n" \
"s_add_u32 s43, s43, 4096\n" \
"buffer_load_dwordx4 a[32:35], v201, s[8:11], s43 offen sc1\n" \
"buffer_load_dwordx4 a[36:39], v201, s[8:11], s43 offen offset:1024 sc1\n" \
"buffer_load_dwordx4 a[40:43], v201, s[8:11], s43 offen offset:2048 sc1\n" \
"buffer_load_dwordx4 a[44:47], v201, s[8:11], s43 offen offset:3072 sc1\n" \
"s_add_u32 s43, s43, 4096\n" \
"buffer_load_dwordx4 a[48:51], v201, s[8:11], s43 offen sc1\n" \
"buffer_load_dwordx4 a[52:55], v201, s[8:11], s43 offen offset:1024 sc1\n" \
"buffer_load_dwordx4 a[56:59], v201, s[8:11], s43 offen offset:2048 sc1\n" \
"buffer_load_dwordx4 a[60:63], v201, s[8:11], s43 offen offset:3072 sc1\n" \
"s_add_u32 s43, s43, 4096\n" \
"buffer_load_dwordx4 a[64:67], v201, s[8:11], s43 offen sc1\n" \
"buffer_load_dwordx4 a[68:71], v201, s[8:11], s43 offen offset:1024 sc1\n" \
"buffer_load_dwordx4 a[72:75], v201, s[8:11], s43 offen offset:2048 sc1\n" \
"buffer_load_dwordx4 a[76:79], v201, s[8:11], s43 offen offset:3072 sc1\n" \
"s_add_u32 s43, s43, 4096\n" \
"buffer_load_dwordx4 a[80:83], v201, s[8:11], s43 offen sc1\n" \
"buffer_load_dwordx4 a[84:87], v201, s[8:11], s43 offen offset:1024 sc1\n" \
"buffer_load_dwordx4 a[88:91], v201, s[8:11], s43 offen offset:2048 sc1\n" \
"buffer_load_dwordx4 a[92:95], v201, s[8:11], s43 offen offset:3072 sc1\n" \
"s_add_u32 s43, s43, 4096\n" \
"buffer_load_dwordx4 a[96:99], v201, s[8:11], s43 offen sc1\n" \
"buffer_load_dwordx4 a[100:103], v201, s[8:11], s43 offen offset:1024 sc1\n" \
"buffer_load_dwordx4 a[104:107], v201, s[8:11], s43 offen offset:2048 sc1\n" \
"buffer_load_dwordx4 a[108:111], v201, s[8:11], s43 offen offset:3072 sc1\n" \
"s_add_u32 s50, s40, 0\n" \
"s_mul_i32 s50, s50, 28672\n" \
"s_add_u32 s50, s50, s34\n" \
"buffer_load_dwordx4 v[28:31], v201, s[12:15], s50 offen sc1\n" \
"buffer_load_dwordx4 v[32:35], v201, s[12:15], s50 offen offset:1024 sc1\n" \
"s_waitcnt vmcnt(0)\n" \
"s_barrier\n" \
".LX3_pre:\n" \
"ds_read_b96 v[248:250], v252\n" \
"s_waitcnt lgkmcnt(0)\n" \
"v_readfirstlane_b32 s44, v248\n" \
"v_readfirstlane_b32 s45, v249\n" \
"v_readfirstlane_b32 s46, v250\n" \
"s_mul_i32 s42, s25, s46\n" \
"s_lshr_b32 s42, s42, 12\n" \
".LX3_cu:\n" \
"s_sub_u32 s43, s24, s44\n" \
"s_cmp_lt_i32 s43, 0\n" \
"s_cbranch_scc0 .LX3_cud\n" \
"s_add_u32 s24, s24, s25\n" \
"s_xor_b32 s31, s31, s29\n" \
"s_xor_b32 s32, s32, s29\n" \
"s_xor_b32 s47, s47, s29\n" \
"s_add_u32 s39, s39, 1\n" \
"s_branch .LX3_cu\n" \
".LX3_cud:\n" \
".LX3_loop:\n" \
"s_add_u32 s43, s40, 2\n" \
"s_and_b32 s43, s43, 3\n" \
"s_bitcmp1_b32 s43, 0\n" \
"s_cselect_b32 s49, s32, s31\n" \
"s_lshl_b32 s43, s43, 19\n" \
"s_add_u32 s49, s49, s43\n" \
"ds_read_b96 v[248:250], v252\n" \
"s_waitcnt lgkmcnt(0)\n" \
"v_readfirstlane_b32 s44, v248\n" \
"v_readfirstlane_b32 s45, v249\n" \
"v_readfirstlane_b32 s46, v250\n" \
"s_mul_i32 s42, s25, s46\n" \
"s_lshr_b32 s42, s42, 12\n" \
"s_add_u32 s43, s40, 0\n" \
"s_and_b32 s43, s43, 3\n" \
"s_bitcmp1_b32 s43, 0\n" \
"s_cselect_b32 s47, s32, s31\n" \
"s_lshl_b32 s43, s43, 19\n" \
"s_add_u32 s47, s47, s43\n" \
"s_sub_u32 s43, s24, s44\n" \
"s_add_u32 s43, s43, s28\n" \
"s_max_i32 s43, s43, 0xfffd0000\n" \
"s_min_i32 s43, s43, 0x4000\n" \
"s_mul_i32 s43, s43, s46\n" \
"s_ashr_i32 s43, s43, 12\n" \
"s_add_u32 s41, s45, s43\n" \
"s_branch .LX3_a1\n" \
".LX3_retrya:\n" \
"s_add_u32 s51, s51, 1\n" \
"s_cmp_gt_u32 s51, 0x20000\n" \
"s_cbranch_scc1 .LX3_end\n" \
".LX3_a0:\n" \
"s_add_u32 s41, s41, s42\n" \
"s_add_u32 s24, s24, s25\n" \
"s_xor_b32 s31, s31, s29\n" \
"s_xor_b32 s32, s32, s29\n" \
"s_xor_b32 s47, s47, s29\n" \
"s_xor_b32 s48, s48, s29\n" \
"s_add_u32 s39, s39, 1\n" \
"buffer_store_dwordx4 v[100:103], v214, s[4:7], s47 offen sc1\n" \
"buffer_store_dwordx4 v[100:103], v217, s[4:7], s47 offen sc1\n" \
"buffer_store_dwordx4 v[100:103], v218, s[4:7], s47 offen sc1\n" \
"buffer_store_dwordx4 v[100:103], v219, s[4:7], s47 offen sc1\n" \
".p2align 6\n" \
".LX3_a1:\n" \
"s_memtime vcc\n" \
"s_waitcnt lgkmcnt(0)\n" \
"s_sub_u32 s43, s41, vcc_lo\n" \
"s_cmp_gt_i32 s43, 128\n" \
"s_cbranch_scc1 .LX3_a1\n" \
"s_cmp_lt_i32 s43, s37\n" \
"s_cbranch_scc1 .LX3_a0\n" \
"s_max_i32 s43, s43, 0\n" \
"s_and_b32 s43, s43, -4\n" \
"s_getpc_b64 vcc\n" \
".LX3_a3:\n" \
"s_sub_u32 s43, .LX3_a4-.LX3_a3, s43\n" \
"s_add_u32 vcc_lo, vcc_lo, s43\n" \
"s_addc_u32 vcc_hi, vcc_hi, 0\n" \
"s_setpc_b64 vcc\n" \
".rept 32\n" \
"s_nop 0\n" \
".endr\n" \
".LX3_a4:\n" \
"buffer_load_dwordx4 v[0:3], v208, s[4:7], s47 offen sc1\n" \
"buffer_load_dwordx4 v[4:7], v208, s[4:7], s47 offen offset:1024 sc1\n" \
"buffer_load_dwordx4 v[8:11], v208, s[4:7], s47 offen offset:2048 sc1\n" \
"buffer_load_dwordx4 v[12:15], v208, s[4:7], s47 offen offset:3072 sc1\n" \
"buffer_load_dwordx4 v[16:19], v209, s[4:7], s47 offen sc1\n" \
"buffer_load_dwordx4 v[20:23], v209, s[4:7], s47 offen offset:1024 sc1\n" \
"buffer_load_dwordx4 v[24:27], v209, s[4:7], s47 offen offset:2048 sc1\n" \
"s_cmp_eq_u32 s35, s40\n" \
"s_cbranch_scc1 .LX3_nb\n" \
"s_barrier\n" \
".LX3_nb:\n" \
"s_mov_b32 s35, s40\n" \
"v_mov_b32 v96, s47\n" \
"ds_write_b32 v252, v96 offset:16\n" \
"v_mov_b32 v60, 0\n" \
"v_mov_b32 v61, 0\n" \
"v_mov_b32 v62, 0\n" \
"v_mov_b32 v63, 0\n" \
"v_mov_b32 v64, 0\n" \
"v_mov_b32 v65, 0\n" \
"v_mov_b32 v66, 0\n" \
"v_mov_b32 v67, 0\n" \
"s_lshl_b32 s43, s40, 7\n" \
"buffer_load_dwordx4 v[236:239], v207, s[4:7], s43 offen sc1\n" \
"s_add_u32 s50, s40, 1\n" \
"s_mul_i32 s50, s50, 28672\n" \
"s_add_u32 s50, s50, s34\n" \
"buffer_load_dwordx4 v[44:47], v201, s[12:15], s50 offen sc1\n" \
"buffer_load_dwordx4 v[48:51], v201, s[12:15], s50 offen offset:1024 sc1\n" \
"s_waitcnt vmcnt(9)\n" \
"v_smfmac_f32_16x16x64_bf16 v[60:63], v[0:3], a[0:7], v200\n" \
"v_smfmac_f32_16x16x64_bf16 v[64:67], v[0:3], a[56:63], v200\n" \
"s_waitcnt vmcnt(8)\n" \
"v_smfmac_f32_16x16x64_bf16 v[60:63], v[4:7], a[8:15], v200\n" \
"v_smfmac_f32_16x16x64_bf16 v[64:67], v[4:7], a[64:71], v200\n" \
"s_waitcnt vmcnt(7)\n" \
"v_smfmac_f32_16x16x64_bf16 v[60:63], v[8:11], a[16:23], v200\n" \
"v_smfmac_f32_16x16x64_bf16 v[64:67], v[8:11], a[72:79], v200\n" \
"s_waitcnt vmcnt(6)\n" \
"v_smfmac_f32_16x16x64_bf16 v[60:63], v[12:15], a[24:31], v200\n" \
"v_smfmac_f32_16x16x64_bf16 v[64:67], v[12:15], a[80:87], v200\n" \
"s_waitcnt vmcnt(5)\n" \
"v_smfmac_f32_16x16x64_bf16 v[60:63], v[16:19], a[32:39], v200\n" \
"v_smfmac_f32_16x16x64_bf16 v[64:67], v[16:19], a[88:95], v200\n" \
"s_waitcnt vmcnt(4)\n" \
"v_smfmac_f32_16x16x64_bf16 v[60:63], v[20:23], a[40:47], v200\n" \
"v_smfmac_f32_16x16x64_bf16 v[64:67], v[20:23], a[96:103], v200\n" \
"s_waitcnt vmcnt(3)\n" \
"v_smfmac_f32_16x16x64_bf16 v[60:63], v[24:27], a[48:55], v200\n" \
"v_smfmac_f32_16x16x64_bf16 v[64:67], v[24:27], a[104:111], v200\n" \
"s_nop 2\n" \
"v_max3_u32 v84, v0, v0, v1\n" \
"v_max3_u32 v84, v84, v2, v3\n" \
"v_max3_u32 v84, v84, v4, v5\n" \
"v_max3_u32 v84, v84, v6, v7\n" \
"v_max3_u32 v84, v84, v8, v9\n" \
"v_permlane32_swap_b32 v60, v64\n" \
"v_permlane32_swap_b32 v61, v65\n" \
"v_permlane32_swap_b32 v62, v66\n" \
"v_permlane32_swap_b32 v63, v67\n" \
"v_pk_add_f32 v[60:61], v[60:61], v[64:65]\n" \
"v_pk_add_f32 v[62:63], v[62:63], v[66:67]\n" \
"v_max3_u32 v84, v84, v10, v11\n" \
"v_max_f32_dpp v88, v60, v60 row_ror:8 row_mask:0xf bank_mask:0x3\n" \
"v_max_f32_dpp v89, v61, v61 row_ror:8 row_mask:0xf bank_mask:0x3\n" \
"v_max_f32_dpp v88, v62, v62 row_ror:8 row_mask:0xf bank_mask:0xc\n" \
"v_max_f32_dpp v89, v63, v63 row_ror:8 row_mask:0xf bank_mask:0xc\n" \
"v_max3_u32 v84, v84, v12, v13\n" \
"v_max_f32_dpp v90, v88, v88 row_shl:4 row_mask:0xf bank_mask:0x5\n" \
"v_max_f32_dpp v90, v89, v89 row_shr:4 row_mask:0xf bank_mask:0xa\n" \
"v_max3_u32 v84, v84, v14, v15\n" \
"v_max3_u32 v84, v84, v16, v17\n" \
"v_max_f32_dpp v88, v90, v90 quad_perm:[1,0,3,2] row_mask:0xf bank_mask:0xf\n" \
"v_max3_u32 v84, v84, v18, v19\n" \
"v_max3_u32 v84, v84, v20, v21\n" \
"v_max_f32_dpp v86, v88, v88 quad_perm:[2,3,0,1] row_mask:0xf bank_mask:0xf\n" \
"v_max_f32_dpp v89, v88, v88 quad_perm:[2,3,0,1] row_mask:0xf bank_mask:0xf\n" \
"v_max3_u32 v84, v84, v22, v23\n" \
"v_max3_u32 v84, v84, v24, v25\n" \
"v_permlane32_swap_b32 v86, v89\n" \
"v_max_f32 v86, v86, v89\n" \
"v_max3_u32 v84, v84, v26, v27\n" \
"v_cmp_eq_u32 vcc, -1, v84\n" \
"v_subrev_f32_dpp v60, v86, v60 row_newbcast:0 row_mask:0xf bank_mask:0xf\n" \
"v_subrev_f32_dpp v61, v86, v61 row_newbcast:4 row_mask:0xf bank_mask:0xf\n" \
"v_subrev_f32_dpp v62, v86, v62 row_newbcast:8 row_mask:0xf bank_mask:0xf\n" \
"v_subrev_f32_dpp v63, v86, v63 row_newbcast:12 row_mask:0xf bank_mask:0xf\n" \
"v_exp_f32 v60, v60\n" \
"v_exp_f32 v61, v61\n" \
"v_exp_f32 v62, v62\n" \
"v_exp_f32 v63, v63\n" \
"v_cvt_pk_bf16_f32 v92, v60, v61\n" \
"v_cvt_pk_bf16_f32 v93, v62, v63\n" \
"ds_write_b64 v212, v[92:93]\n" \
"s_cbranch_vccnz .LX3_retrya\n" \
"v_add_f32_dpp v88, v60, v60 row_ror:8 row_mask:0xf bank_mask:0x3\n" \
"v_add_f32_dpp v89, v61, v61 row_ror:8 row_mask:0xf bank_mask:0x3\n" \
"v_add_f32_dpp v88, v62, v62 row_ror:8 row_mask:0xf bank_mask:0xc\n" \
"v_add_f32_dpp v89, v63, v63 row_ror:8 row_mask:0xf bank_mask:0xc\n" \
"v_mov_b32 v245, v245\n" \
"v_add_f32_dpp v90, v88, v88 row_shl:4 row_mask:0xf bank_mask:0x5\n" \
"v_add_f32_dpp v90, v89, v89 row_shr:4 row_mask:0xf bank_mask:0xa\n" \
"v_mov_b32 v245, v245\n" \
"v_mov_b32 v245, v245\n" \
"v_add_f32_dpp v88, v90, v90 quad_perm:[1,0,3,2] row_mask:0xf bank_mask:0xf\n" \
"v_mov_b32 v245, v245\n" \
"v_mov_b32 v245, v245\n" \
"v_add_f32_dpp v87, v88, v88 quad_perm:[2,3,0,1] row_mask:0xf bank_mask:0xf\n" \
"buffer_store_dwordx2 v[86:87], v211, s[4:7], s47 offen sc1\n" \
"s_nop 0\n" \
"v_mov_b32 v245, v245\n" \
"s_waitcnt lgkmcnt(0)\n" \
"s_barrier\n" \
"ds_read_b64_tr_b16 v[92:93], v213\n" \
"ds_read_b64_tr_b16 v[94:95], v213 offset:64\n" \
"ds_read_b32 v84, v252 offset:16\n" \
"v_mov_b32 v245, v245\n" \
"s_nop 2\n" \
"v_mov_b32 v245, v245\n" \
"s_nop 2\n" \
"v_mov_b32 v245, v245\n" \
"s_waitcnt lgkmcnt(1)\n" \
"v_mfma_f32_16x16x32_bf16 v[68:71], v[92:95], v[28:31], 0\n" \
"s_waitcnt lgkmcnt(0)\n" \
"v_xor_b32 v85, s29, v84\n" \
"v_mfma_f32_16x16x32_bf16 v[72:75], v[92:95], v[32:35], 0\n" \
"v_add_u32 v84, v84, v210\n" \
"v_add_u32 v85, v85, v210\n" \
"v_mov_b32 v245, v245\n" \
"v_mov_b32 v245, v245\n" \
"v_mov_b32 v245, v245\n" \
"v_cvt_pk_bf16_f32 v96, v68, v69\n" \
"v_cvt_pk_bf16_f32 v97, v70, v71\n" \
"s_mov_b64 exec, s[2:3]\n" \
"v_cvt_pk_bf16_f32 v96, v72, v73\n" \
"v_cvt_pk_bf16_f32 v97, v74, v75\n" \
"s_mov_b64 exec, -1\n" \
"v_mov_b32 v98, v96\n" \
"v_mov_b32 v99, v97\n" \
"s_nop 1\n" \
"v_permlane16_swap_b32 v96, v98\n" \
"v_permlane16_swap_b32 v97, v99\n" \
"buffer_store_dwordx4 v[96:99], v84, s[4:7], 0 offen sc1\n" \
"s_barrier\n" \
"buffer_store_dwordx4 v[96:99], v85, s[4:7], 0 offen sc1\n" \
"s_xor_b32 s43, s47, s29\n" \
"buffer_store_dwordx2 v[86:87], v211, s[4:7], s43 offen sc1\n" \
"ds_write_b128 v216, v[0:3] offset:0\n" \
"ds_write_b128 v216, v[4:7] offset:1024\n" \
"ds_write_b128 v216, v[8:11] offset:2048\n" \
"ds_write_b128 v216, v[12:15] offset:3072\n" \
"ds_write_b128 v216, v[16:19] offset:4096\n" \
"ds_write_b128 v216, v[20:23] offset:5120\n" \
"ds_write_b128 v216, v[24:27] offset:6144\n" \
"s_add_u32 s43, s40, 1\n" \
"s_mul_i32 s43, s43, 28672\n" \
"s_add_u32 s43, s43, s33\n" \
"buffer_load_dwordx4 a[0:3], v201, s[8:11], s43 offen sc1\n" \
"buffer_load_dwordx4 a[4:7], v201, s[8:11], s43 offen offset:1024 sc1\n" \
"buffer_load_dwordx4 a[8:11], v201, s[8:11], s43 offen offset:2048 sc1\n" \
"buffer_load_dwordx4 a[12:15], v201, s[8:11], s43 offen offset:3072 sc1\n" \
"s_add_u32 s43, s43, 4096\n" \
"buffer_load_dwordx4 a[16:19], v201, s[8:11], s43 offen sc1\n" \
"buffer_load_dwordx4 a[20:23], v201, s[8:11], s43 offen offset:1024 sc1\n" \
"buffer_load_dwordx4 a[24:27], v201, s[8:11], s43 offen offset:2048 sc1\n" \
"buffer_load_dwordx4 a[28:31], v201, s[8:11], s43 offen offset:3072 sc1\n" \
"s_add_u32 s43, s43, 4096\n" \
"buffer_load_dwordx4 a[32:35], v201, s[8:11], s43 offen sc1\n" \
"buffer_load_dwordx4 a[36:39], v201, s[8:11], s43 offen offset:1024 sc1\n" \
"buffer_load_dwordx4 a[40:43], v201, s[8:11], s43 offen offset:2048 sc1\n" \
"buffer_load_dwordx4 a[44:47], v201, s[8:11], s43 offen offset:3072 sc1\n" \
"s_add_u32 s43, s43, 4096\n" \
"buffer_load_dwordx4 a[48:51], v201, s[8:11], s43 offen sc1\n" \
"buffer_load_dwordx4 a[52:55], v201, s[8:11], s43 offen offset:1024 sc1\n" \
"buffer_load_dwordx4 a[56:59], v201, s[8:11], s43 offen offset:2048 sc1\n" \
"buffer_load_dwordx4 a[60:63], v201, s[8:11], s43 offen offset:3072 sc1\n" \
"s_add_u32 s43, s43, 4096\n" \
"buffer_load_dwordx4 a[64:67], v201, s[8:11], s43 offen sc1\n" \
"buffer_load_dwordx4 a[68:71], v201, s[8:11], s43 offen offset:1024 sc1\n" \
"buffer_load_dwordx4 a[72:75], v201, s[8:11], s43 offen offset:2048 sc1\n" \
"buffer_load_dwordx4 a[76:79], v201, s[8:11], s43 offen offset:3072 sc1\n" \
"s_add_u32 s43, s43, 4096\n" \
"buffer_load_dwordx4 a[80:83], v201, s[8:11], s43 offen sc1\n" \
"buffer_load_dwordx4 a[84:87], v201, s[8:11], s43 offen offset:1024 sc1\n" \
"buffer_load_dwordx4 a[88:91], v201, s[8:11], s43 offen offset:2048 sc1\n" \
"buffer_load_dwordx4 a[92:95], v201, s[8:11], s43 offen offset:3072 sc1\n" \
"s_add_u32 s43, s43, 4096\n" \
"buffer_load_dwordx4 a[96:99], v201, s[8:11], s43 offen sc1\n" \
"buffer_load_dwordx4 a[100:103], v201, s[8:11], s43 offen offset:1024 sc1\n" \
"buffer_load_dwordx4 a[104:107], v201, s[8:11], s43 offen offset:2048 sc1\n" \
"buffer_load_dwordx4 a[108:111], v201, s[8:11], s43 offen offset:3072 sc1\n" \
"buffer_store_dwordx4 v[100:103], v214, s[4:7], s49 offen sc1\n" \
"s_xor_b32 s43, s49, s29\n" \
"buffer_store_dwordx4 v[100:103], v214, s[4:7], s43 offen sc1\n" \
"s_waitcnt vmcnt(34)\n" \
"v_mov_b32 v28, v44\n" \
"v_mov_b32 v29, v45\n" \
"v_mov_b32 v30, v46\n" \
"v_mov_b32 v31, v47\n" \
"v_mov_b32 v32, v48\n" \
"v_mov_b32 v33, v49\n" \
"v_mov_b32 v34, v50\n" \
"v_mov_b32 v35, v51\n" \
"v_max_u32 v236, v236, v238\n" \
"v_max_u32 v237, v237, v239\n" \
"v_and_b32 v236, 0xfff, v236\n" \
"v_and_b32 v237, 0xfff, v237\n" \
"s_nop 0\n" \
"v_readfirstlane_b32 s26, v236\n" \
"v_readfirstlane_b32 s27, v237\n" \
"s_add_u32 s24, s24, s26\n" \
"s_add_u32 s24, s24, s27\n" \
"s_add_u32 s40, s40, 1\n" \
"s_waitcnt lgkmcnt(0)\n" \
"s_barrier\n" \
"s_cmp_eq_u32 s40, 0\n" \
"s_cbranch_scc1 .LX3_wet\n" \
"s_cmp_lt_u32 s40, 63\n" \
"s_cbranch_scc1 .LX3_loop\n" \
"ds_read_b96 v[248:250], v252\n" \
"s_waitcnt lgkmcnt(0)\n" \
"v_readfirstlane_b32 s44, v248\n" \
"v_readfirstlane_b32 s45, v249\n" \
"v_readfirstlane_b32 s46, v250\n" \
"s_mul_i32 s42, s25, s46\n" \
"s_lshr_b32 s42, s42, 12\n" \
"s_add_u32 s43, s40, 0\n" \
"s_and_b32 s43, s43, 3\n" \
"s_bitcmp1_b32 s43, 0\n" \
"s_cselect_b32 s47, s32, s31\n" \
"s_lshl_b32 s43, s43, 19\n" \
"s_add_u32 s47, s47, s43\n" \
"s_sub_u32 s43, s24, s44\n" \
"s_add_u32 s43, s43, s28\n" \
"s_max_i32 s43, s43, 0xfffd0000\n" \
"s_min_i32 s43, s43, 0x4000\n" \
"s_mul_i32 s43, s43, s46\n" \
"s_ashr_i32 s43, s43, 12\n" \
"s_add_u32 s41, s45, s43\n" \
"s_branch .LX3_z1\n" \
".LX3_retryz:\n" \
"s_add_u32 s51, s51, 1\n" \
"s_cmp_gt_u32 s51, 0x20000\n" \
"s_cbranch_scc1 .LX3_end\n" \
".LX3_z0:\n" \
"s_add_u32 s41, s41, s42\n" \
"s_add_u32 s24, s24, s25\n" \
"s_xor_b32 s31, s31, s29\n" \
"s_xor_b32 s32, s32, s29\n" \
"s_xor_b32 s47, s47, s29\n" \
"s_xor_b32 s48, s48, s29\n" \
"s_add_u32 s39, s39, 1\n" \
".p2align 6\n" \
".LX3_z1:\n" \
"s_memtime vcc\n" \
"s_waitcnt lgkmcnt(0)\n" \
"s_sub_u32 s43, s41, vcc_lo\n" \
"s_cmp_gt_i32 s43, 128\n" \
"s_cbranch_scc1 .LX3_z1\n" \
"s_cmp_lt_i32 s43, s37\n" \
"s_cbranch_scc1 .LX3_z0\n" \
"s_max_i32 s43, s43, 0\n" \
"s_and_b32 s43, s43, -4\n" \
"s_getpc_b64 vcc\n" \
".LX3_z3:\n" \
"s_sub_u32 s43, .LX3_z4-.LX3_z3, s43\n" \
"s_add_u32 vcc_lo, vcc_lo, s43\n" \
"s_addc_u32 vcc_hi, vcc_hi, 0\n" \
"s_setpc_b64 vcc\n" \
".rept 32\n" \
"s_nop 0\n" \
".endr\n" \
".LX3_z4:\n" \
"buffer_load_dwordx4 v[0:3], v208, s[4:7], s47 offen sc1\n" \
"buffer_load_dwordx4 v[4:7], v208, s[4:7], s47 offen offset:1024 sc1\n" \
"buffer_load_dwordx4 v[8:11], v208, s[4:7], s47 offen offset:2048 sc1\n" \
"buffer_load_dwordx4 v[12:15], v208, s[4:7], s47 offen offset:3072 sc1\n" \
"buffer_load_dwordx4 v[16:19], v209, s[4:7], s47 offen sc1\n" \
"buffer_load_dwordx4 v[20:23], v209, s[4:7], s47 offen offset:1024 sc1\n" \
"buffer_load_dwordx4 v[24:27], v209, s[4:7], s47 offen offset:2048 sc1\n" \
"s_waitcnt vmcnt(0)\n" \
"ds_write_b128 v216, v[0:3] offset:7168\n" \
"ds_write_b128 v216, v[4:7] offset:8192\n" \
"ds_write_b128 v216, v[8:11] offset:9216\n" \
"ds_write_b128 v216, v[12:15] offset:10240\n" \
"ds_write_b128 v216, v[16:19] offset:11264\n" \
"ds_write_b128 v216, v[20:23] offset:12288\n" \
"ds_write_b128 v216, v[24:27] offset:13312\n" \
"v_max3_u32 v84, v0, v0, v1\n" \
"v_max3_u32 v84, v84, v2, v3\n" \
"v_max3_u32 v84, v84, v4, v5\n" \
"v_max3_u32 v84, v84, v6, v7\n" \
"v_max3_u32 v84, v84, v8, v9\n" \
"v_max3_u32 v84, v84, v10, v11\n" \
"v_max3_u32 v84, v84, v12, v13\n" \
"v_max3_u32 v84, v84, v14, v15\n" \
"v_max3_u32 v84, v84, v16, v17\n" \
"v_max3_u32 v84, v84, v18, v19\n" \
"v_max3_u32 v84, v84, v20, v21\n" \
"v_max3_u32 v84, v84, v22, v23\n" \
"v_max3_u32 v84, v84, v24, v25\n" \
"v_max3_u32 v84, v84, v26, v27\n" \
"v_cmp_eq_u32 vcc, -1, v84\n" \
"s_cbranch_vccnz .LX3_retryz\n" \
"s_waitcnt lgkmcnt(0)\n" \
"s_barrier\n" \
"s_barrier\n" \
".LX3_end:\n" \
"s_endpgm\n"
typedef __attribute__((ext_vector_type(16))) float f16v;
DI u4 pack8(const f16v& x, int s, float sc) { return (u4){pk2(x[8 * s] * sc, x[8 * s + 1] * sc), pk2(x[8 * s + 2] * sc, x[8 * s + 3] * sc), pk2(x[8 * s + 4] * sc, x[8 * s + 5] * sc), pk2(x[8 * s + 6] * sc, x[8 * s + 7] * sc)}; }
DI u4 pack8(const f16v& x, int s) { return (u4){pk2(x[8 * s], x[8 * s + 1]), pk2(x[8 * s + 2], x[8 * s + 3]), pk2(x[8 * s + 4], x[8 * s + 5]), pk2(x[8 * s + 6], x[8 * s + 7])}; }
DI void dma_pair(u4 srd, int v0, int v1, int soff, unsigned m) {
    asm volatile("s_mov_b32 m0, %4\n\ts_nop 0\n\tbuffer_load_dwordx4 %0, %2, %3 offen lds\n\tbuffer_load_dwordx4 %1, %2, %3 offen offset:1024 lds" :: "v"(v0), "v"(v1), "s"(srd), "s"(soff), "s"(m) : "memory");
}
DI void dma_one(u4 srd, int v0, int soff, unsigned m) {
    asm volatile("s_mov_b32 m0, %3\n\ts_nop 0\n\tbuffer_load_dwordx4 %0, %1, %2 offen lds" :: "v"(v0), "s"(srd), "s"(soff), "s"(m) : "memory");
}
DI void mV_va(f16v& c, u4 x, u4 y) { asm volatile("v_mfma_f32_32x32x16_bf16 %0, %1, %2, %0" : "+v"(c) : "v"(x), "a"(y)); }
DI void mV_va0(f16v& c, u4 x, u4 y) { asm volatile("v_mfma_f32_32x32x16_bf16 %0, %1, %2, 0" : "=&v"(c) : "v"(x), "a"(y)); }
DI void mV_av(f16v& c, u4 x, u4 y) { asm volatile("v_mfma_f32_32x32x16_bf16 %0, %1, %2, %0" : "+v"(c) : "a"(x), "v"(y)); }
DI void mV_av0(f16v& c, u4 x, u4 y) { asm volatile("v_mfma_f32_32x32x16_bf16 %0, %1, %2, 0" : "=&v"(c) : "a"(x), "v"(y)); }
DI void mV_vv(f16v& c, u4 x, u4 y) { asm volatile("v_mfma_f32_32x32x16_bf16 %0, %1, %2, %0" : "+v"(c) : "v"(x), "v"(y)); }
DI void mV_vv0(f16v& c, u4 x, u4 y) { asm volatile("v_mfma_f32_32x32x16_bf16 %0, %1, %2, 0" : "=&v"(c) : "v"(x), "v"(y)); }
template <int N, int PD, class RD, class MM, class EX> DI void pipe(RD rd, MM mm, EX ex) {
    u4 f[PD];
#pragma unroll
    for (int j = 0; j < PD; ++j) f[j] = rd(j);
    SB();
#pragma unroll
    for (int j = 0; j < N; ++j) { mm(j, f[j % PD]); if (j + PD < N) f[j % PD] = rd(j + PD); ex(j); SB(); }
}
DI u4 sld128(u4 srd, int voff, int soff) { u4 v; asm volatile("s_nop 4\n\tbuffer_load_dwordx4 %0, %1, %2, %3 offen sc1" : "=v"(v) : "v"(voff), "s"(srd), "s"(soff) : "memory"); return v; }
DI u2 sld64(u4 srd, int voff, int soff) { u2 v; asm volatile("s_nop 4\n\tbuffer_load_dwordx2 %0, %1, %2, %3 offen sc1" : "=v"(v) : "v"(voff), "s"(srd), "s"(soff) : "memory"); return v; }
DI void sst128(u4 v, u4 srd, int voff, int soff) { asm volatile("s_nop 4\n\tbuffer_store_dwordx4 %0, %1, %2, %3 offen sc1\n\ts_nop 1" :: "v"(v), "v"(voff), "s"(srd), "s"(soff) : "memory"); }
DI void sst64(u2 v, u4 srd, int voff, int soff) { asm volatile("s_nop 4\n\tbuffer_store_dwordx2 %0, %1, %2, %3 offen sc1" :: "v"(v), "v"(voff), "s"(srd), "s"(soff) : "memory"); }
DI void sst32(unsigned v, u4 srd, int voff, int soff) { asm volatile("s_nop 4\n\tbuffer_store_dword %0, %1, %2, %3 offen sc1" :: "v"(v), "v"(voff), "s"(srd), "s"(soff) : "memory"); }
DI unsigned sld32(u4 srd, int voff, int soff) { unsigned v; asm volatile("s_nop 4\n\tbuffer_load_dword %0, %1, %2, %3 offen sc1" : "=v"(v) : "v"(voff), "s"(srd), "s"(soff) : "memory"); return v; }
template <int N> DI void waitvm() { asm volatile("s_waitcnt vmcnt(%0)" :: "i"(N) : "memory"); }
DI int swzr(int row) { return ((row & 1) << 2) | (row & 8) | ((row >> 1) & 3); }
#define SLICE 32768
#define CSCALE (LOG2E * 0.0625f)

struct SideCtx {
    char LAS* sm; unsigned smb; int lane, wave;
    int vr0, vr1, vc0, vc1;
};
DI int fresh_lane() { int l; asm volatile("v_mbcnt_lo_u32_b32 %0, -1, 0\n\tv_mbcnt_hi_u32_b32 %0, -1, %0" : "=v"(l)); __builtin_assume(l >= 0 && l < 64); return l; }
DI SideCtx make_ctx(int wave) {
    SideCtx c; c.sm = (char LAS*)smem_; c.smb = (unsigned)(size_t)c.sm; c.lane = fresh_lane(); c.wave = wave;
    asm volatile("" : "+s"(c.wave), "+s"(c.smb));
    const int l = c.lane;
    { int row = 4 * c.wave + (l >> 5); c.vr0 = row * 8192 + (((l & 31) ^ swzr(row)) * 16); row += 2; c.vr1 = row * 8192 + (((l & 31) ^ swzr(row)) * 16) - 1024; }
    { int row = l >> 3; c.vc0 = row * 896 + (((l & 7) ^ (((row >> 1) & 1) << 2)) * 16); c.vc1 = c.vc0 + 8 * 896 - 1024; }
    return c;
}
DI void dma_rows_q(const SideCtx& c, u4 srd, int h, int r0, int slot, int g) { dma_pair(srd, c.vr0, c.vr1, (r0 + 16 * g) * 8192 + h * 512, c.smb + slot * SLICE + (8 * g + 2 * c.wave) * 1024); }
DI void dma_cols_q(const SideCtx& c, u4 srd, int h, int c0, int slot, int g) { const int k = 8 * c.wave + 2 * g; dma_pair(srd, c.vc0, c.vc1, (h * 256 + 8 * k) * 896 + c0 * 2, c.smb + slot * SLICE + k * 1024); }
DI void dma_rows(const SideCtx& c, u4 srd, int h, int r0, int slot) {
#pragma unroll
    for (int g = 0; g < 4; ++g) dma_rows_q(c, srd, h, r0, slot, g);
}
DI void dma_cols(const SideCtx& c, u4 srd, int h, int c0, int slot) {
#pragma unroll
    for (int g = 0; g < 4; ++g) dma_cols_q(c, srd, h, c0, slot, g);
}
DI u4 rd_rows(const SideCtx& c, int slot, int ks, int tau) {
    const int l = c.lane, hi = l >> 5, ro = l & 31;
    const int rowc = 32 * (ro >> 4) + 4 * ((ro >> 2) & 1) + 8 * ((ro >> 1) & 1) + 2 * ((ro >> 3) & 1) + (ro & 1);
    const int ch = (2 * (ks & 7) + hi) ^ swzr(rowc);
    return *(const u4 LAS*)(c.sm + (rowc * 512 + ch * 16) + (slot * SLICE + tau * 8192 + (ks >> 3) * 256));
}
DI u4 tr_cols(const SideCtx& c, int slot, int ks, int nt) {
    const int l = c.lane, hi = l >> 5, blk = (l >> 4) & 1, r = (l & 15) >> 2, p = l & 3;
    u2 o[2];
#pragma unroll
    for (int t = 0; t < 2; ++t) {
        const int ch = (4 * nt + 2 * blk + (p >> 1)) ^ (((r >> 1) & 1) << 2);
        o[t] = trread(c.sm + ((8 * hi + r) * 128 + ch * 16 + 8 * (p & 1)) + (slot * SLICE + ks * 2048 + t * 512));
    }
    return (u4){o[0][0], o[0][1], o[1][0], o[1][1]};
}
#define MID_K 20
#define PF_AHEAD 12
#define NSEG1 10
DI void set_flag(const SideCtx& c, u4 rhb, int off, unsigned val) {
    waitvm<0>(); bar();
    if (c.wave == 0) sst32(val, rhb, c.lane == 0 ? off : OOB, 0);
}
DI void wait_prog(u4 rhb, int voff_or_oob, unsigned want) {
    unsigned sp = 0;
#pragma nounroll
    for (;;) {
        unsigned v = sld32(rhb, voff_or_oob, 0); WAITV(0, "+v"(v));
        if (__all((v - want < 256u) | (voff_or_oob >= OOB))) break;
        asm volatile("s_sleep 4" ::: "memory");
        if (++sp > SPIN_MAX) __builtin_amdgcn_endpgm();
    }
}
#define MFW32(x, y) asm volatile("s_nop 11" : "+v"(x), "+v"(y))
DI int perm16(int r) { return 4 * ((r >> 2) & 1) + 8 * ((r >> 1) & 1) + 2 * ((r >> 3) & 1) + (r & 1); }
#define CHUNK(u) ((u) == 6 ? 3 : ((u) >> 1) + 4 * ((u) & 1))
DI void w_first(int wave, int type, int h, int sub, int part) {
    CArgs& a = get_args();
    const SideCtx c = make_ctx(wave);
    h = __builtin_amdgcn_readfirstlane(h); sub = __builtin_amdgcn_readfirstlane(sub); asm volatile("" : "+s"(h), "+s"(sub));
    const u4 rw1 = mksrd(type ? a.Wv : a.Wk, 448 * 8192), rw2 = mksrd(type ? a.Wo : a.Wq, 448 * 8192);
    const int c1 = sub == 3 ? 384 : sub * 128 + 64;
    if (part) { dma_rows(c, rw1, h, 64 * CHUNK(0), 0); dma_rows(c, rw1, h, 64 * CHUNK(1), 1); }
    else if (type) { dma_cols(c, rw2, h, sub * 128, 2); dma_cols(c, rw2, h, c1, 3); }
    else { dma_rows(c, rw2, h, sub * 128, 2); dma_rows(c, rw2, h, c1, 3); }
}
template <int N> struct IntC { static constexpr int value = N; };
template <int TYPE>
DI void wgu_task(unsigned tag, int vb, int wave, int h, int sub) {
    CArgs& a = get_args();
    const SideCtx c = make_ctx(wave);
    const int lane = c.lane, hi = lane >> 5, l31 = lane & 31, w = c.wave;
    h = __builtin_amdgcn_readfirstlane(h); sub = __builtin_amdgcn_readfirstlane(sub); asm volatile("" : "+s"(h), "+s"(sub));
    const u4 rw1 = mksrd(TYPE ? a.Wv : a.Wk, 448 * 8192), rx = mksrd(a.X, 2048 * 896), rhb = mksrd(a.HB, HB_SZ);
    const int o_fl = (w == 0 && lane == 0) ? O_FW + ((h * 2 + TYPE) * 4 + sub) * 128 : OOB;
    int xo[7];
#pragma unroll
    for (int k = 0; k < 7; ++k) { const int n = 64 * (7 * (w & 1) + k) + lane, row = (n * 1171) >> 16, pos = n - 56 * row; xo[k] = (row + 16 * (w >> 1)) * 896 + ((pos ^ ((row >> 1) & 7)) * 16); }
    auto xdma = [&](int s, int reg, int k) { dma_one(rx, xo[k], s * 28672, c.smb + reg + (7 * w + k) * 1024); };
    const int m = sub * 4 + w;
    u4 wk[28];
    f16v d[2][2];
    {
        u4 kf[16];
        const int rowp = 16 * (l31 >> 4) + perm16(l31 & 15);
#pragma unroll
        for (int ks = 0; ks < 16; ++ks)
            kf[ks] = TYPE ? tr_cols(c, 2 + (w >> 1), ks, w & 1) : *(const u4 LAS*)(c.sm + 2 * SLICE + w * 16384 + (rowp * 512 + ((2 * (ks & 7) + hi) ^ swzr(rowp)) * 16) + (ks >> 3) * 256);
        bar();
        unsigned pf[3];
#pragma unroll
        for (int i = 0; i < 3; ++i) asm volatile("buffer_load_dword %0, %1, %2, %3 offen" : "=v"(pf[i]) : "v"(lane < 7 ? (vb >> 3) * 896 + lane * 128 : OOB), "s"(rx), "s"((4 * i + w) * 28672) : "memory");
        dma_rows(c, rw1, h, 64 * CHUNK(2), 2); dma_rows(c, rw1, h, 64 * CHUNK(3), 3); dma_rows(c, rw1, h, 64 * CHUNK(4), 4);
        const u4 rout = mksrd(TYPE ? a.WvoF : a.WqkF, 28 * 16 * 14336);
        const int t16 = 2 * m + (l31 >> 4);
        const int o_st = m > 13 ? OOB : (TYPE ? t16 * 16 + h : h * 28 + t16) * 14336 + (hi * 16 + (l31 & 15)) * 16;
        u4 p[4], pn; unsigned ts[7];
        auto epi = [&](int v, int e, int st) __attribute__((always_inline)) {
            const f16v& x = d[v & 1][e & 1]; const int o = 8 * (e >> 1) + 4 * st; const float sc = TYPE ? 1.f : CSCALE;
            if (st < 2) { p[e][2 * st] = pk2(x[o] * sc, x[o + 1] * sc); p[e][2 * st + 1] = pk2(x[o + 2] * sc, x[o + 3] * sc); asm volatile("" : "+v"(p[e][2 * st]), "+v"(p[e][2 * st + 1])); }
            else if (st == 2) st128(p[e], rout, o_st, (CHUNK(v) * 2 + (e >> 1)) * 1024 + (e & 1) * 512);
            else if (st == 3) { const auto s0 = __builtin_amdgcn_permlane32_swap(p[e][0], p[e][1], false, false), s1 = __builtin_amdgcn_permlane32_swap(p[e][2], p[e][3], false, false);
                                pn = (u4){s0[0], s1[0], s0[1], s1[1]}; PIN(pn); }
            else { wk[4 * CHUNK(v) + e] = pn; asm volatile("" : "+a"(wk[4 * CHUNK(v) + e])); }
        };
#pragma unroll
        for (int u = 0; u < 7; ++u) {
            if (u == 0) waitvm<35>(); else if (u == 1) waitvm<27>(); else if (u == 2) waitvm<28>(); else if (u == 3) waitvm<20>(); else if (u == 4) waitvm<13>(); else waitvm<7>();
            if (u == 2) asm volatile("" :: "v"(pf[0]), "v"(pf[1]), "v"(pf[2]));
            bar(); ts[u] = TNOW();
            if (u >= 3) st32(tag + (u < 5 ? u - 2 : u - 1), rhb, o_fl, 0);
            pipe<32, 4>([&](int j) { return rd_rows(c, u % 5, j >> 1, j & 1); },
                        [&](int j, u4 f) { if (j < 2) mV_vv0(d[u & 1][j & 1], f, kf[j >> 1]); else mV_vv(d[u & 1][j & 1], f, kf[j >> 1]); },
                        [&](int j) { if (u > 0 && j >= 2 && j < 14) { if (j == 2) asm volatile("s_nop 3" : "+v"(d[(u - 1) & 1][0]), "+v"(d[(u - 1) & 1][1])); epi(u - 1, (j - 2) / 3, (j - 2) % 3); }
                                     if (j < 14 || j >= 30) return;
                                     const int g = (j - 14) >> 1;
                                     if (j & 1) { if (u > 0) epi(u - 1, g >> 1, 3 + (g & 1)); }
                                     else if (u == 1 || u == 2) dma_one(rw1, (g & 1) ? c.vr1 + 1024 : c.vr0, (64 * CHUNK(u + 4) + 16 * (g >> 1)) * 8192 + h * 512, c.smb + (u - 1) * SLICE + (8 * (g >> 1) + 2 * w + (g & 1)) * 1024);
                                     else if (u >= 4 && g < 7) xdma(u - 4, (u - 2) * SLICE, g); });
        }
        MFW32(d[0][0], d[0][1]);
#pragma unroll
        for (int e = 0; e < 12; ++e) epi(6, e / 3, e % 3);
#pragma unroll
        for (int e = 0; e < 8; ++e) epi(6, e >> 1, 3 + (e & 1));
    }
    const u4 rout = mksrd(TYPE ? a.UF : a.GF, GU_SZ);
    auto o_out = [&](int mm) { return mm > 13 ? OOB : h * GU_H + (TYPE ? (2 * mm + (l31 >> 4)) * 1024 : mm * 1024 + (l31 >> 4) * 14336) + (hi * 16 + (l31 & 15)) * 16; };
    const int o_st = o_out(m);
    const char LAS* xb[4];
#pragma unroll
    for (int k = 0; k < 4; ++k) xb[k] = c.sm + l31 * 896 + ((2 * k + hi) ^ ((l31 >> 1) & 7)) * 16;
    const int o_pf = (w == 1 && lane < 56) ? (vb >> 3) * 896 + lane * 16 : OOB;
    unsigned ring = 0x10432;
    auto mm1 = [&](f16v& acc, const u4& wt, u4 f, bool first) __attribute__((always_inline)) {
        if (TYPE) { if (first) mV_va0(acc, f, wt); else mV_va(acc, f, wt); } else { if (first) mV_av0(acc, wt, f); else mV_av(acc, wt, f); } };
    u4 ph;
    auto half = [&](const f16v& x, int x8, int st, int vo, int so) __attribute__((always_inline)) {
        if (st == 0) { ph = pack8(x, x8); PIN(ph); } else st128(ph, rout, vo, so + x8 * 512); };
    auto seg = [&](auto first_, auto par_, int i) __attribute__((always_inline)) {
        constexpr int FI = decltype(first_)::value, PAR = decltype(par_)::value;
        if (FI == 0) waitvm<11>(); else waitvm<8>();
        bar();
        if (FI == 0) st32(tag + 6, rhb, o_fl, 0);
        dma_one(rx, o_pf, (i + PF_AHEAD) * 28672, c.smb + 4 * SLICE + 28672 + w * 1024);
        const int r0 = (ring & 7) * SLICE, r3 = ((ring >> 12) & 7) * SLICE;
        ring = (ring >> 4) | ((ring & 7) << 16);
        pipe<28, 4>([&](int k) { return *(const u4 LAS*)(xb[k & 3] + r0 + (k >> 2) * 128); },
                    [&](int k, u4 f) { mm1(d[PAR][0], wk[k], f, k == 0); },
                    [&](int k) { if (FI != 0 && k >= 1 && k < 5) { if (k == 1) asm volatile("s_nop 5" : "+v"(d[PAR ^ 1][0])); half(d[PAR ^ 1][0], (k - 1) >> 1, (k - 1) & 1, o_st, (i - 1) * 28672); }
                                 if (k == (FI ? MID_K : MID_K - 6)) { asm volatile("s_waitcnt vmcnt(%0)\n\ts_barrier" :: "i"(FI ? MID_K / 3 - 1 : MID_K / 3 - 1) : "memory"); st32(tag + 7 + i, rhb, o_fl, 0); }
                                 if (k >= 6 && k % 3 == 0 && k < 27) xdma(i + 3, r3, k / 3 - 2); });
    };
    seg(IntC<0>{}, IntC<0>{}, 0); seg(IntC<1>{}, IntC<1>{}, 1);
#pragma nounroll
    for (int i = 2; i < NSEG1; i += 2) { seg(IntC<2>{}, IntC<0>{}, i); seg(IntC<2>{}, IntC<1>{}, i + 1); }
    asm volatile("s_nop 11" : "+v"(d[1][0]));
    sst128(pack8(d[1][0], 0), rout, o_st, (NSEG1 - 1) * 28672); sst128(pack8(d[1][0], 1), rout, o_st, (NSEG1 - 1) * 28672 + 512);
    if (sub & 1) { set_flag(c, rhb, O_FW + ((h * 2 + TYPE) * 4 + sub) * 128, tag + 7 + NSEG); return; }
    wait_prog(rhb, lane < 2 ? O_FW + ((h * 2 + TYPE) * 4 + 1 + 2 * lane) * 128 : OOB, tag + 7);
    bar();
    if (w == 0) sst32(tag + 7 + NSEG1, rhb, o_fl, 0);
    const int pfo = lane < 56 ? ((h & 3) * 4 + TYPE * 2 + (sub >> 1)) * 1792 + lane * 16 : OOB;
    if (w == 3) {
        int xp[7];
#pragma unroll
        for (int k = 0; k < 7; ++k) { const int n = 64 * k + lane, row = (n * 1171) >> 16, pos = n - 56 * row; xp[k] = row * 896 + ((pos ^ ((row >> 1) & 7)) * 16) - (k & 1) * 1024; xo[k] -= 16 * 896 + (~k & 1) * 1024; }
        auto segl = [&](auto first_, auto par_, int i) __attribute__((always_inline)) {
            constexpr int FI = decltype(first_)::value, PAR = decltype(par_)::value;
            if (FI) { waitvm<62>(); bar(); }
            const int r0 = (ring & 7) * SLICE, r3 = ((ring >> 12) & 7) * SLICE;
            ring = (ring >> 4) | ((ring & 7) << 16);
            pipe<28, 4>([&](int k) { return *(const u4 LAS*)(xb[k & 3] + r0 + (k >> 2) * 128); },
                        [&](int k, u4 f) { mm1(d[PAR][0], wk[k], f, k == 0); },
                        [&](int k) { if (k == 0) { if (FI) { asm volatile("s_nop 9" : "+v"(d[PAR ^ 1][0])); st128(pack8(d[PAR ^ 1][0], 0), rout, o_st, (i - 1) * 28672); st128(pack8(d[PAR ^ 1][0], 1), rout, o_st, (i - 1) * 28672 + 512); }
                                                   dma_pair(rx, pfo, pfo - 128, (i + PF_AHEAD) * 28672, c.smb + 4 * SLICE + 28672); }
                                     if (k & 1) { const int k0 = (k - 1) % 14, k1 = k % 14; dma_pair(rx, k0 < 7 ? xp[k0] : xo[k0 - 7], k1 < 7 ? xp[k1] : xo[k1 - 7], (i + 3) * 28672 + (k / 14) * 14336, c.smb + r3 + (k - 1) * 1024); } });
        };
        segl(IntC<0>{}, IntC<0>{}, NSEG1); segl(IntC<1>{}, IntC<1>{}, NSEG1 + 1);
#pragma nounroll
        for (int i = NSEG1 + 2; i < NSEG; i += 2) { segl(IntC<1>{}, IntC<0>{}, i); segl(IntC<1>{}, IntC<1>{}, i + 1); }
        waitvm<30>(); bar();
        asm volatile("s_nop 11" : "+v"(d[1][0]));
    } else {
        const int m2 = sub == 0 ? 4 + w : w == 2 ? 7 : 12 + w, t16 = 2 * m2 + (l31 >> 4);
        const int o_s2 = o_out(m2);
        u4 w2[28];
        {
            const u4 rin = mksrd(TYPE ? a.WvoF : a.WqkF, 28 * 16 * 14336);
            const int o_in = (TYPE ? t16 * 16 + h : h * 28 + t16) * 14336 + (hi * 16 + (l31 & 15)) * 16;
#pragma unroll
            for (int k = 0; k < 28; ++k) w2[k] = sld128(rin, o_in, k * 512);
            waitvm<0>();
#pragma unroll
            for (int k = 0; k < 28; ++k) {
                PIN(w2[k]);
                const auto s0 = __builtin_amdgcn_permlane32_swap(w2[k][0], w2[k][1], false, false), s1 = __builtin_amdgcn_permlane32_swap(w2[k][2], w2[k][3], false, false);
                w2[k] = (u4){s0[0], s1[0], s0[1], s1[1]};
                asm volatile("" : "+a"(w2[k]));
            }
        }
        u4 fr[4];
#pragma unroll
        for (int k = 0; k < 4; ++k) fr[k] = *(const u4 LAS*)(xb[k] + (ring & 7) * SLICE);
        auto seg2 = [&](auto first_, auto par_, int i) __attribute__((always_inline)) {
            constexpr int FI = decltype(first_)::value, PAR = decltype(par_)::value;
            const int r0 = (ring & 7) * SLICE, r1 = ((ring >> 4) & 7) * SLICE;
            ring = (ring >> 4) | ((ring & 7) << 16);
#pragma unroll
            for (int k = 0; k < 28; ++k) {
                mm1(d[PAR][0], wk[k], fr[k & 3], k == 0);
                if (k == 21) { asm volatile("s_waitcnt vmcnt(5)\n\ts_barrier" ::: "memory"); st32(tag + (FI ? 6 + i : 7 + NSEG1), rhb, o_fl, 0); }
                if (FI && k >= 3 && k < 11) { if (k == 3) asm volatile("" : "+v"(d[PAR ^ 1][0]), "+v"(d[PAR ^ 1][1])); half(d[PAR ^ 1][k >= 7], ((k - 3) >> 1) & 1, (k - 3) & 1, k >= 7 ? o_s2 : o_st, (i - 1) * 28672); }
                SB();
                mm1(d[PAR][1], w2[k], fr[k & 3], k == 0);
                fr[k & 3] = *(const u4 LAS*)(xb[k & 3] + (k < 24 ? r0 + ((k + 4) >> 2) * 128 : r1));
                SB();
            }
        };
        seg2(IntC<0>{}, IntC<0>{}, NSEG1); seg2(IntC<1>{}, IntC<1>{}, NSEG1 + 1);
#pragma nounroll
        for (int i = NSEG1 + 2; i < NSEG; i += 2) { seg2(IntC<1>{}, IntC<0>{}, i); seg2(IntC<1>{}, IntC<1>{}, i + 1); }
        MFW32(d[1][0], d[1][1]);
        sst128(pack8(d[1][1], 0), rout, o_s2, 63 * 28672); sst128(pack8(d[1][1], 1), rout, o_s2, 63 * 28672 + 512);
    }
    sst128(pack8(d[1][0], 0), rout, o_st, 63 * 28672); sst128(pack8(d[1][0], 1), rout, o_st, 63 * 28672 + 512);
    set_flag(c, rhb, O_FW + ((h * 2 + TYPE) * 4 + sub) * 128, tag + 7 + NSEG);
}

DI void x_image(const SideCtx& c, u4 rx, int s) {
#pragma unroll
    for (int k = 0; k < 7; ++k) {
        const int n = 64 * (7 * (c.wave & 1) + k) + c.lane, row = (n * 1171) >> 16, pos = n - 56 * row;
        dma_one(rx, row * 896 + ((pos ^ ((row >> 1) & 7)) * 16), (s * 32 + 16 * (c.wave >> 1)) * 896, c.smb + 40960 + (7 * c.wave + k) * 1024);
    }
}
DI void e_task(unsigned tag, int wave, int s, int hg) {
    CArgs& a = get_args();
    const SideCtx c = make_ctx(wave);
    const int lane = c.lane, hi = lane >> 5, l31 = lane & 31;
    s = __builtin_amdgcn_readfirstlane(s); hg = __builtin_amdgcn_readfirstlane(hg); asm volatile("" : "+s"(s), "+s"(hg));
    const int sh = s * 16 + 4 * hg + c.wave;
    const u4 rhb = mksrd(a.HB, HB_SZ), rg = mksrd(a.GF, GU_SZ);
    bar();
    x_image(c, mksrd(a.X, 2048 * 896), s);
    wait_prog(rhb, lane < 4 ? O_FW + ((4 * hg + c.wave) * 8 + lane) * 128 : OOB, tag + 8 + s);
    u4 g[28];
    const int o_g = (l31 >> 4) * 14336 + (hi * 16 + (l31 & 15)) * 16;
#pragma unroll
    for (int k = 0; k < 28; ++k) asm volatile("s_nop 0\n\tbuffer_load_dwordx4 %0, %1, %2, %3 offen sc1" : "=a"(g[k]) : "v"(o_g), "s"(rg), "s"((4 * hg + c.wave) * GU_H + s * 28672 + k * 512) : "memory");
    bar();
    f16v sx;
    pipe<28, 4>([&](int k) { const char LAS* b = c.sm + 40960 + l31 * 896 + hi * 8 + (k >> 2) * 128;
                             const u2 c0 = *(const u2 LAS*)(b + ((2 * (k & 3)) ^ ((l31 >> 1) & 7)) * 16), c1 = *(const u2 LAS*)(b + ((2 * (k & 3) + 1) ^ ((l31 >> 1) & 7)) * 16);
                             return (u4){c0[0], c0[1], c1[0], c1[1]}; },
                [&](int k, u4 f) { if (k == 0) WAITV(21, "+a"(g[0]), "+a"(g[1]), "+a"(g[2]), "+a"(g[3]), "+a"(g[4]), "+a"(g[5]), "+a"(g[6]));
                                   if (k == 7) WAITV(14, "+a"(g[7]), "+a"(g[8]), "+a"(g[9]), "+a"(g[10]), "+a"(g[11]), "+a"(g[12]), "+a"(g[13]));
                                   if (k == 14) WAITV(7, "+a"(g[14]), "+a"(g[15]), "+a"(g[16]), "+a"(g[17]), "+a"(g[18]), "+a"(g[19]), "+a"(g[20]));
                                   if (k == 21) WAITV(0, "+a"(g[21]), "+a"(g[22]), "+a"(g[23]), "+a"(g[24]), "+a"(g[25]), "+a"(g[26]), "+a"(g[27]));
                                   u4 y = {f[0], f[2], f[1], f[3]}; asm volatile("s_nop 1" : "+v"(y));
                                   if (k == 0) mV_av0(sx, g[k], y); else mV_av(sx, g[k], y); },
                [&](int) {});
    asm volatile("s_nop 11" : "+v"(sx));
    float m = -3.0e38f;
#pragma unroll
    for (int j = 0; j < 16; ++j) { const int key = (j & 3) + 8 * (j >> 2) + 4 * hi; if (key > l31) sx[j] = -INFINITY; m = fmaxf(m, sx[j]); }
    m = x32max(m);
    float z = 0.f;
#pragma unroll
    for (int j = 0; j < 16; ++j) { sx[j] = __builtin_amdgcn_exp2f(sx[j] - m); z += sx[j]; }
    z = x32add(z);
    const u4 rex = mksrd(a.EX, 64 * 16 * 2048), rmz = mksrd(a.MZX, 64 * 16 * 256);
    sst128(pack8(sx, 0), rex, lane * 16, sh * 2048); sst128(pack8(sx, 1), rex, lane * 16, sh * 2048 + 1024);
    sst64((u2){__float_as_uint(m), __float_as_uint(z)}, rmz, lane < 32 ? lane * 8 : OOB, sh * 256);
    set_flag(c, rhb, O_FE + (s * 4 + hg) * 4, tag + 1);
}
#define P_XSLP 0
DI void slp(int n) {
#pragma nounroll
    for (int i = 0; i < n; i += 8) asm volatile("s_sleep 8");
}
DI void x_task(unsigned tag, int wave, int s, int cb) {
    CArgs& a = get_args();
    const SideCtx c = make_ctx(wave);
    const int lane = c.lane, hi = lane >> 5, l31 = lane & 31, w = c.wave;
    s = __builtin_amdgcn_readfirstlane(s); cb = __builtin_amdgcn_readfirstlane(cb); asm volatile("" : "+s"(s), "+s"(cb));
    const u4 rhb = mksrd(a.HB, HB_SZ);
    const u4 ru = mksrd(a.UF, GU_SZ), rex = mksrd(a.EX, 64 * 16 * 2048), rmz = mksrd(a.MZX, 64 * 16 * 256);
    const u4 rum = mksrd(a.UM, 28 * TU_P), rtf = mksrd(a.TF, 16 * TU_P), rx = mksrd(a.X, 2048 * 896);
    bar();
    x_image(c, rx, s);
    wait_prog(rhb, lane < 16 ? O_FW + (((4 * w + (lane >> 2)) * 2 + 1) * 4 + (lane & 3)) * 128 : lane == 16 ? O_FE + (s * 4 + w) * 4 : OOB, lane < 16 ? tag + 8 + s : tag + 1);
    u4 uf[4][2][2], ex[4][2]; u2 mz[4];
    const int o_u = (l31 >> 4) * 1024 + hi * 256 + (l31 & 15) * 16;
#pragma unroll
    for (int k = 0; k < 4; ++k) {
        const int sh = s * 16 + 4 * w + k;
#pragma unroll
        for (int sg = 0; sg < 2; ++sg) {
            ex[k][sg] = sld128(rex, lane * 16, sh * 2048 + sg * 1024);
#pragma unroll
            for (int nt = 0; nt < 2; ++nt) uf[k][sg][nt] = sld128(ru, o_u, (4 * w + k) * GU_H + (s * 28 + 4 * cb + 2 * nt) * 1024 + sg * 512);
        }
        mz[k] = sld64(rmz, l31 * 8, sh * 256);
        asm volatile("s_sleep %0" :: "i"(P_XSLP));
    }
    waitvm<0>();
    f16v rr[4][2];
#pragma unroll
    for (int k = 0; k < 4; ++k) {
        asm volatile("" : "+v"(mz[k]), "+v"(ex[k][0]), "+v"(ex[k][1]), "+v"(uf[k][0][0]), "+v"(uf[k][0][1]), "+v"(uf[k][1][0]), "+v"(uf[k][1][1]));
        mV_vv0(rr[k][0], uf[k][0][0], ex[k][0]); mV_vv0(rr[k][1], uf[k][0][1], ex[k][0]);
        mV_vv(rr[k][0], uf[k][1][0], ex[k][1]); mV_vv(rr[k][1], uf[k][1][1], ex[k][1]);
        MFW32(rr[k][0], rr[k][1]);
    }
    bar();
    wait_prog(rhb, lane < 8 ? O_FT + (8 * w + lane) * 128 : lane < 12 ? O_FT + (24 + 4 * cb + lane) * 128 : OOB, tag + s + 1);
    u4 tf[28]; u4 um[2][2];
    const int o_tf = (l31 >> 3) * TU_P + hi * 128 + (l31 & 7) * 16, o_um = (l31 >> 4) * TU_P + hi * 256 + (l31 & 15) * 16;
#pragma unroll
    for (int k = 0; k < 28; ++k) {
        asm volatile("s_nop 0\n\tbuffer_load_dwordx4 %0, %1, %2, %3 offen sc1" : "=a"(tf[k]) : "v"(o_tf), "s"(rtf), "s"(4 * w * TU_P + (s + 1) * 7168 + k * 256) : "memory");
        if (k % 7 == 6) slp(s < 58 ? P_XSLP : 0);
    }
#pragma unroll
    for (int k = 0; k < 4; ++k) um[k >> 1][k & 1] = ld128(rum, o_um, (4 * cb + 2 * (k & 1)) * TU_P + (s + 1) * 4096 + w * 1024 + (k >> 1) * 512);
    f16v sm_;
    pipe<28, 4>([&](int k) { return *(const u4 LAS*)(c.sm + 40960 + l31 * 896 + (((2 * (k & 3) + hi) ^ ((l31 >> 1) & 7)) * 16) + (k >> 2) * 128); },
                [&](int k, u4 f) { if (k == 0) WAITV(25, "+a"(tf[0]), "+a"(tf[1]), "+a"(tf[2]), "+a"(tf[3]), "+a"(tf[4]), "+a"(tf[5]), "+a"(tf[6]));
                                   if (k == 7) WAITV(18, "+a"(tf[7]), "+a"(tf[8]), "+a"(tf[9]), "+a"(tf[10]), "+a"(tf[11]), "+a"(tf[12]), "+a"(tf[13]));
                                   if (k == 14) WAITV(11, "+a"(tf[14]), "+a"(tf[15]), "+a"(tf[16]), "+a"(tf[17]), "+a"(tf[18]), "+a"(tf[19]), "+a"(tf[20]));
                                   if (k == 21) WAITV(4, "+a"(tf[21]), "+a"(tf[22]), "+a"(tf[23]), "+a"(tf[24]), "+a"(tf[25]), "+a"(tf[26]), "+a"(tf[27]));
                                   if (k == 0) mV_av0(sm_, tf[k], f); else mV_av(sm_, tf[k], f); },
                [&](int) {});
    waitvm<0>();
    asm volatile("s_nop 11" : "+v"(sm_));
#pragma unroll
    for (int k = 0; k < 4; ++k) PIN(um[k >> 1][k & 1]);
    u2 pm[4]; float scx[4];
#pragma unroll
    for (int k = 0; k < 4; ++k) {
        const f4 sc = {sm_[4 * k], sm_[4 * k + 1], sm_[4 * k + 2], sm_[4 * k + 3]};
        const float mx = __uint_as_float(mz[k][0]);
        float m = fmaxf(x32max(fmaxf(fmaxf(sc[0], sc[1]), fmaxf(sc[2], sc[3]))), mx);
        float e[4];
#pragma unroll
        for (int j = 0; j < 4; ++j) e[j] = __builtin_amdgcn_exp2f(sc[j] - m);
        const float cx = __builtin_amdgcn_exp2f(mx - m);
        const float z = x32add((e[0] + e[1]) + (e[2] + e[3])) + cx * __uint_as_float(mz[k][1]);
        const float inv = __builtin_amdgcn_rcpf(z);
        scx[k] = cx * inv;
        pm[k] = (u2){pk2(e[0] * inv, e[1] * inv), pk2(e[2] * inv, e[3] * inv)};
    }
    f16v d0, d1;
    {
        u4 p0 = {pm[0][0], pm[0][1], pm[2][0], pm[2][1]}, p1 = {pm[1][0], pm[1][1], pm[3][0], pm[3][1]};
        asm volatile("s_nop 1\n\tv_mfma_f32_32x32x16_bf16 %0, %2, %6, 0\n\tv_mfma_f32_32x32x16_bf16 %1, %3, %6, 0\n\tv_mfma_f32_32x32x16_bf16 %0, %4, %7, %0\n\tv_mfma_f32_32x32x16_bf16 %1, %5, %7, %1"
                     : "=&v"(d0), "=&v"(d1) : "v"(um[0][0]), "v"(um[0][1]), "v"(um[1][0]), "v"(um[1][1]), "v"(p0), "v"(p1));
    }
    f16v a0 = (rr[0][0] * scx[0] + rr[1][0] * scx[1]) + (rr[2][0] * scx[2] + rr[3][0] * scx[3]);
    f16v a1 = (rr[0][1] * scx[0] + rr[1][1] * scx[1]) + (rr[2][1] * scx[2] + rr[3][1] * scx[3]);
    asm volatile("s_nop 11" : "+v"(d0), "+v"(d1), "+v"(a0), "+v"(a1));
    a0 += d0; a1 += d1;
#pragma unroll
    for (int g = 0; g < 4; ++g) {
        *(f4 LAS*)(c.sm + w * 8704 + l31 * 272 + (8 * g + 4 * hi) * 4) = (f4){a0[4 * g], a0[4 * g + 1], a0[4 * g + 2], a0[4 * g + 3]};
        *(f4 LAS*)(c.sm + w * 8704 + l31 * 272 + 128 + (8 * g + 4 * hi) * 4) = (f4){a1[4 * g], a1[4 * g + 1], a1[4 * g + 2], a1[4 * g + 3]};
    }
    bar();
    {
        const int t = w * 8 + (lane >> 3), c8 = lane & 7;
        f4 s0 = {0.f, 0.f, 0.f, 0.f}, s1 = s0;
#pragma unroll
        for (int p = 0; p < 4; ++p) { s0 += *(const f4 LAS*)(c.sm + p * 8704 + t * 272 + c8 * 32); s1 += *(const f4 LAS*)(c.sm + p * 8704 + t * 272 + c8 * 32 + 16); }
        sst128((u4){pk2(s0[0], s0[1]), pk2(s0[2], s0[3]), pk2(s1[0], s1[1]), pk2(s1[2], s1[3])}, mksrd(a.out, 2048 * 1792), t * 1792 + c8 * 16, s * 32 * 1792 + 896 + cb * 128);
    }
}

#define P_ASLP 8
#define PA_UT 0
#define PA_TU 50688
#define PA_KH (PA_TU + 35456)
#define PA_KL (PA_KH + 7680)
#define PA_KT (PA_KL + 7680)
#define PA_RED (PA_KT + 12288)
DI float bf2f(u16 x) { return __uint_as_float((unsigned)x << 16); }
DI void parta_task(unsigned tag, int vb, int wave, int task, int nt) {
    CArgs& a = get_args();
    const SideCtx c = make_ctx(wave);
    char LAS* sm = c.sm;
    const int lane = c.lane, w = c.wave, tid = w * 64 + lane, hi = lane >> 5, l31 = lane & 31, l15 = lane & 15, kk = lane >> 4;
    task = __builtin_amdgcn_readfirstlane(task); asm volatile("" : "+s"(task));
    int d0 = task * 4;
    char LAS* ut = sm + PA_UT;
    const u4 rpa = mksrd(a.PA, 56 * 32768);
    bar();
    u2 pr[3][4], dsks[3]; unsigned ldts[3];
#pragma unroll
    for (int g = 0; g < 3; ++g) {
        const int d = d0 + (g < nt ? 4 * g : 0);
        ldts[g] = a.Alogdt[d + w]; dsks[g] = *(const u2*)(a.ADsk + d);
        const int n = (d + w) * 256 + 4 * lane;
        pr[g][0] = *(const u2*)(a.AlogA + n); pr[g][1] = *(const u2*)(a.Aim + n); pr[g][2] = *(const u2*)(a.ACr + n); pr[g][3] = *(const u2*)(a.ACi + n);
    }
    SB();
    for (int i = tid; i < 768; i += 256) { const int ch = i >> 6, m = i & 63; *(u16 LAS*)(ut + ch * 4224 + (m == 0 ? 0 : (2048 + m) * 2)) = 0; }
    for (int i = tid; i < 2 * 31 * 5; i += 256) { const int x = i / 155, r = i % 155; *(u4 LAS*)(sm + (x ? PA_KL : PA_KH) + r * 16) = (u4){0u, 0u, 0u, 0u}; }
    if (tid < 48) *(u4 LAS*)(sm + PA_TU + ((tid / 6) * 276 + (tid >= 6) + 256 + tid % 6) * 16) = (u4){0u, 0u, 0u, 0u};
    int toff[8];
#pragma unroll
    for (int j = 0; j < 8; ++j) {
        const int p = (int)bfr((float)(tid * 8 + j));
        int col, ar = (p & 255) >> 4;
        if (p < 256) col = p & 15; else if (p < 512) col = 16 + ((p & 15) >> 1); else if (p < 1024) col = 24 + ((p - 512) >> 8) * 4 + ((p & 15) >> 2);
        else if (p < 2048) col = 32 + ((p - 1024) >> 8) * 2 + ((p & 15) >> 3); else col = 40;
        toff[j] = (col * 16 + ar) * 4;
    }
    float qpos[3];
    qpos[0] = (float)l15;
    qpos[1] = l15 < 8 ? (float)(256 + 2 * l15) : (float)(512 + ((l15 - 8) >> 2) * 256 + (l15 & 3) * 4);
    qpos[2] = l15 < 8 ? (float)(1024 + (l15 >> 1) * 256 + (l15 & 1) * 8) : 2048.f;
    const float ppos = (float)(16 * l15);
#pragma nounroll
    for (int rnd = 0; rnd < nt; ++rnd) {
        const int nld = rnd ? 0 : nt; const u2 dsk4 = dsks[0];
        {
            const float dt = bfr(expf(bf2f(ldts[0])));
            char LAS* pt = sm + PA_RED + w * 9216;
#pragma unroll
            for (int i = 0; i < 4; ++i) {
                auto el = [&](int k) { const unsigned x = pr[0][k][i >> 1]; return __uint_as_float((i & 1) ? x & 0xffff0000u : x << 16); };
                const float ar = -bfr(expf(el(0)));
                *(f4 LAS*)(pt + (lane + 64 * i) * 16) = (f4){ar * dt * LOG2E, el(1) * dt * 0.15915494309189535f, 2.f * el(2) * dt, 2.f * el(3) * dt};
            }
            f4 acc[3] = {{0.f, 0.f, 0.f, 0.f}, {0.f, 0.f, 0.f, 0.f}, {0.f, 0.f, 0.f, 0.f}};
            auto cexp = [](f2 z, float p) __attribute__((always_inline)) {
                const f2 x = z * p; const float ex = __builtin_amdgcn_exp2f(x[0]), rv = __builtin_amdgcn_fractf(x[1]);
                return (f2){__builtin_amdgcn_cosf(rv), __builtin_amdgcn_sinf(rv)} * ex; };
            auto split = [](f2 x, unsigned& h, unsigned& l) __attribute__((always_inline)) {
                h = pk2(x[0], x[1]); const f2 r = x - (f2){__uint_as_float(h << 16), __uint_as_float(h & 0xffff0000u)}; l = pk2(r[0], r[1]); };
            auto mf16 = [](u4 x, u4 y, f4 c) __attribute__((always_inline)) { return __builtin_amdgcn_mfma_f32_16x16x32_bf16(__builtin_bit_cast(bf8, x), __builtin_bit_cast(bf8, y), c, 0, 0, 0); };
            f4 zc[4];
#pragma unroll
            for (int j = 0; j < 4; ++j) zc[j] = *(const f4 LAS*)(pt + (4 * j + kk) * 16);
            const u16* up = a.Au + (size_t)tid * 448 + d0;
#pragma nounroll
            for (int s2 = 0; s2 < 8; ++s2) {
                u2 uv[3];
                if (nld) {
#pragma unroll
                    for (int g = 0; g < 3; ++g) uv[g] = *(const u2*)(up + (size_t)s2 * 256 * 448 + (g < nld ? 4 * g : 0));
                }
#pragma unroll
                for (int hf = 0; hf < 2; ++hf) {
                    unsigned ah[4], al[4], bh[4], bl[4]; f2 zz[4];
#pragma unroll
                    for (int j = 0; j < 4; ++j) {
                        const f4 z = zc[j]; zz[j] = (f2){z[0], z[1]};
                        const f2 e = cexp(zz[j], ppos);
                        split((f2){z[2] * e[0] - z[3] * e[1], -(z[2] * e[1] + z[3] * e[0])}, ah[j], al[j]);
                        zc[j] = *(const f4 LAS*)(pt + (32 * s2 + 16 * hf + 16 + 4 * j + kk) * 16);
                    }
                    const u4 AH = {ah[0], ah[1], ah[2], ah[3]}, AL = {al[0], al[1], al[2], al[3]};
#pragma unroll
                    for (int t = 0; t < 3; ++t) {
#pragma unroll
                        for (int j = 0; j < 4; ++j) split(cexp(zz[j], qpos[t]), bh[j], bl[j]);
                        const u4 BH = {bh[0], bh[1], bh[2], bh[3]}, BL = {bl[0], bl[1], bl[2], bl[3]};
                        acc[t] = mf16(AH, BH, mf16(AH, BL, mf16(AL, BH, acc[t])));
                        asm volatile("" : "+v"(acc[t]));
                    }
                }
                if (nld) {
#pragma unroll
                    for (int ch = 0; ch < 12; ++ch) *(u16 LAS*)(ut + ch * 4224 + (2048 - tid - 256 * s2) * 2) = (u16)(uv[ch >> 2][(ch >> 1) & 1] >> (16 * (ch & 1)));
                }
            }
#pragma unroll
            for (int t = 0; t < 3; ++t) *(f4 LAS*)(sm + PA_KT + w * 3072 + ((t * 16 + l15) * 16 + 4 * kk) * 4) = acc[t];
        }
        bar();
        auto emit = [&](int ch) __attribute__((always_inline)) {
            f4 s0 = {0.f, 0.f, 0.f, 0.f}, s1 = s0;
#pragma unroll
            for (int p = 0; p < 4; ++p) { const char LAS* q_ = sm + PA_RED + p * 9216 + (tid >> 2) * 144 + (tid & 3) * 32; s0 += *(const f4 LAS*)q_; s1 += *(const f4 LAS*)(q_ + 16); }
            const u4 x0 = *(const u4 LAS*)(ut + ch * 4224 + (255 - tid) * 16); const unsigned x1 = *(const unsigned LAS*)(ut + ch * 4224 + (256 - tid) * 16);
            const float dsk = __uint_as_float((ch & 2 ? dsk4[1] : dsk4[0]) >> (16 * (ch & 1)) << 16);
            float yv[8];
#pragma unroll
            for (int j = 0; j < 8; ++j) { const unsigned uw = j == 0 ? x1 : x0[(8 - j) >> 1]; yv[j] = bfr(j < 4 ? s0[j & 3] : s1[j & 3]) + bfr(__uint_as_float((j & 1) ? uw & 0xffff0000u : uw << 16) * dsk); }
            sst128((u4){pk2(yv[0], yv[1]), pk2(yv[2], yv[3]), pk2(yv[4], yv[5]), pk2(yv[6], yv[7])}, rpa, tid * 16, (d0 + ch) * 4096);
        };
#pragma nounroll
        for (int cc = 0; cc < 4; ++cc) {
            const int ch = cc;
            {
                float kv[8];
#pragma unroll
                for (int j = 0; j < 8; ++j) kv[j] = *(const float LAS*)(sm + PA_KT + cc * 3072 + toff[j]);
                u4 h4, l4;
#pragma unroll
                for (int j = 0; j < 4; ++j) { h4[j] = pk2(kv[2 * j], kv[2 * j + 1]);
                    l4[j] = pk2(kv[2 * j] - __uint_as_float(h4[j] << 16), kv[2 * j + 1] - __uint_as_float(h4[j] & 0xffff0000u)); }
                const int o = (31 + (tid >> 2)) * 80 + (tid & 3) * 16;
                *(u4 LAS*)(sm + PA_KH + o) = h4; *(u4 LAS*)(sm + PA_KL + o) = l4;
            }
            {
                const int i = tid;
                const u4 x0 = *(const u4 LAS*)(ut + ch * 4224 + i * 16), x1 = *(const u4 LAS*)(ut + ch * 4224 + i * 16 + 16);
                const unsigned r[8] = {x0[0], x0[1], x0[2], x0[3], x1[0], x1[1], x1[2], x1[3]};
#pragma unroll
                for (int cp = 0; cp < 8; ++cp) {
                    u4 o;
#pragma unroll
                    for (int j = 0; j < 4; ++j) o[j] = (cp & 1) ? __builtin_amdgcn_alignbit(r[(cp >> 1) + j + 1], r[(cp >> 1) + j], 16) : r[(cp >> 1) + j];
                    *(u4 LAS*)(sm + PA_TU + (cp * 276 + (cp ? 1 : 0) + i) * 16) = o;
                }
                if (tid == 255) *(u4 LAS*)(sm + PA_TU + 256 * 16) = x1;
            }
            if (cc) emit(cc - 1);
            bar();
            {
                f16v y0, y1;
                const int m00 = 2048 - l31 + 8 * hi, cp = m00 & 7;
                const char LAS* ab = sm + PA_TU + (cp * 276 + (cp ? 1 : 0) + (m00 >> 3)) * 16 - 63 * 64;
                const char LAS* bb = sm + PA_KH + l31 * 80 + hi * 16;
                const char LAS* a1w = ab - w * 64; const char LAS* b1w = bb - w * 80;
                const char LAS* a2w = ab + w * 64; const char LAS* b2w = bb + w * 80;
                u4 fr[2][4];
                auto rdg = [&](int g, int sl) __attribute__((always_inline)) {
                    const int i = (g & 15) >> 1, ks = g & 1;
                    if (g < 16) { fr[sl][0] = *(const u4 LAS*)(b1w + (31 - 4 * i) * 80 + ks * 32); fr[sl][2] = *(const u4 LAS*)(a1w + (63 - 4 * i) * 64 + ks * 32);
                                  fr[sl][3] = *(const u4 LAS*)(a1w + (31 - 4 * i) * 64 + ks * 32); fr[sl][1] = *(const u4 LAS*)(b1w + 7680 + (31 - 4 * i) * 80 + ks * 32); }
                    else { fr[sl][0] = *(const u4 LAS*)(b2w + (32 + 4 * i) * 80 + ks * 32); fr[sl][3] = *(const u4 LAS*)(a2w + (32 + 4 * i) * 64 + ks * 32);
                           fr[sl][1] = *(const u4 LAS*)(b2w + 7680 + (32 + 4 * i) * 80 + ks * 32); }
                };
                rdg(0, 0); SB();
#pragma unroll
                for (int g = 0; g < 32; ++g) {
                    const int sl = g & 1;
                    if (g + 1 < 32) rdg(g + 1, sl ^ 1);
                    SB();
                    if (g == 0) { mV_vv0(y0, fr[sl][2], fr[sl][0]); mV_vv0(y1, fr[sl][3], fr[sl][0]); }
                    else { if (g < 16) mV_vv(y0, fr[sl][2], fr[sl][0]); mV_vv(y1, fr[sl][3], fr[sl][0]); }
                    if (g < 16) mV_vv(y0, fr[sl][2], fr[sl][1]);
                    mV_vv(y1, fr[sl][3], fr[sl][1]);
                    SB();
                }
                MFW32(y0, y1);
#pragma unroll
                for (int g = 0; g < 4; ++g) {
                    *(f4 LAS*)(sm + PA_RED + w * 9216 + l31 * 144 + (8 * g + 4 * hi) * 4) = (f4){y0[4 * g], y0[4 * g + 1], y0[4 * g + 2], y0[4 * g + 3]};
                    *(f4 LAS*)(sm + PA_RED + w * 9216 + (32 + l31) * 144 + (8 * g + 4 * hi) * 4) = (f4){y1[4 * g], y1[4 * g + 1], y1[4 * g + 2], y1[4 * g + 3]};
                }
            }
            bar();
        }
        emit(3);
        bar();
        d0 += 4; ut += 16896;
#pragma unroll
        for (int g = 0; g < 2; ++g) {
            ldts[g] = ldts[g + 1]; dsks[g] = dsks[g + 1];
#pragma unroll
            for (int k = 0; k < 4; ++k) pr[g][k] = pr[g + 1][k];
        }
    }
    waitvm<0>(); bar();
    if (w == 0) sst32(tag + 1, mksrd(a.HB, HB_SZ), lane < nt ? O_FPA + (task + lane) * 4 : OOB, 0);
}
DI void pa_out(unsigned tag, int wave, int t) {
    CArgs& a = get_args();
    const SideCtx c = make_ctx(wave);
    const int lane = c.lane, tid = c.wave * 64 + lane;
    t = __builtin_amdgcn_readfirstlane(t); asm volatile("" : "+s"(t));
    const u4 rpa = mksrd(a.PA, 56 * 32768), ro = mksrd(a.out, 2048 * 1792);
    wait_prog(mksrd(a.HB, HB_SZ), lane < 16 ? O_FPA + (t & ~7) * 8 + lane * 4 : OOB, tag + 1);
    u4 v[8];
#pragma unroll
    for (int i = 0; i < 8; ++i) v[i] = sld128(rpa, (tid >> 5) * 4096 + (tid & 31) * 16, ((t >> 3) * 64 + i * 8) * 4096 + (t & 7) * 512);
    WAITV(0, "+v"(v[0]), "+v"(v[1]), "+v"(v[2]), "+v"(v[3]), "+v"(v[4]), "+v"(v[5]), "+v"(v[6]), "+v"(v[7]));
    bar();
#pragma unroll
    for (int i = 0; i < 8; ++i) *(u4 LAS*)(c.sm + (i * 8 + (tid >> 5)) * 528 + (tid & 31) * 16) = v[i];
    bar();
#pragma unroll
    for (int k = 0; k < 8; ++k) {
        const char LAS* p = c.sm + (tid & 7) * 8 * 528 + ((tid >> 3) + 32 * k) * 2;
        u4 o;
#pragma unroll
        for (int j = 0; j < 4; ++j) o[j] = (unsigned)*(const u16 LAS*)(p + (2 * j) * 528) | ((unsigned)*(const u16 LAS*)(p + (2 * j + 1) * 528) << 16);
        sst128(o, ro, (tid >> 3) * 1792 + (tid & 7) * 16, ((t & 7) * 256 + 32 * k) * 1792 + (t >> 3) * 128);
        asm volatile("s_sleep %0" :: "i"(P_ASLP));
    }
}
#define PROBE_N 2
#define L_TAB 0
#define L_CLK 32768
#define L_MEM 33792
#define L_WRK 49152
DI void edge(unsigned& ce, unsigned& ne) {
    const unsigned n0 = rt_now(); u64 t, c; int n = 0;
#pragma nounroll
    do { asm volatile("s_memrealtime %0\n\ts_memtime %1\n\ts_waitcnt lgkmcnt(0)" : "=&s"(t), "=&s"(c) :: "memory"); } while ((unsigned)t == n0 && ++n < 64);
    ce = (unsigned)c - 30u; ne = (unsigned)t;
}
DI unsigned srdw(const void* p, unsigned bytes, int i) { const u64 x = (u64)p; return i == 0 ? (unsigned)x : i == 1 ? (unsigned)(x >> 32) & 0xffffu : i == 2 ? bytes : 0x00020000u; }
#define RUN(r) asm volatile("v_mov_b32 v255, %0\n\ts_getpc_b64 vcc\n.Lfk_from_" #r ":\n\ts_add_u32 vcc_lo, vcc_lo, fk_role_" #r "-.Lfk_from_" #r "\n\ts_addc_u32 vcc_hi, vcc_hi, 0\n\ts_setpc_b64 vcc" :: "v"(tab) : "memory", "vcc", "scc", "s99", "v255", "a255")
DI unsigned probe(u4 r, int id, int xh) {
    const int lane = fresh_lane(), o = xh * (2 << 20) + O_PROBE + id * 128; unsigned best = 1016;
#pragma nounroll
    for (int rep = 0; rep < PROBE_N; ++rep) {
        sst32(rep, r, lane < 4 ? o + lane * 8192 : OOB, 0); waitvm<0>();
#pragma nounroll
        for (int k = 0; k < 4; ++k) {
            const int kk = (k + id) & 3, ol = lane == 0 ? o + kk * 8192 : OOB;
            const unsigned t0 = now32(); unsigned v = sld32(r, ol, 0); WAITV(0, "+v"(v));
            const unsigned d = now32() - t0;
            if (lane == kk) best = min(best, d);
        }
    }
    return best;
}
DI void ring_init(unsigned tag, int W, int b, unsigned best) {
    CArgs& a = get_args();
    const int lane = fresh_lane();
    const u4 r = mksrd(a.HB, HB_SZ);
    sst32(tag + 1 + (best >> 2), r, lane < 4 ? O_PAR + (b * 4 + W) * 16 + lane * 4 : OOB, 0);
    u4 m0[2];
#pragma unroll
    for (int k = 0; k < 2; ++k) {
        m0[k] = *(const u4*)(a.mem0 + (lane & 7) * 448 + min(2 * W + k, 6) * 64 + (lane >> 3) * 8);
        m0[k] = (b & 1) ? (u4){FF, FF, FF, FF} : (u4){san(m0[k][0]), san(m0[k][1]), san(m0[k][2]), san(m0[k][3])};
    }
    if (b == 0)
        for (int k = 0; k < 3; ++k) sst128((u4){REC_A, REC_B, REC_A, REC_B}, r, O_REC + lane * 16, (W * 3 + k) * 1024);
    wait_prog(r, O_PAR + lane * 4, tag + 1);
    unsigned la = sld32(r, O_PAR + lane * 4, 0); WAITV(0, "+v"(la));
    la = ((la - tag) << 2) | (lane & 3);
#pragma unroll
    for (int sft = 16; sft >= 1; sft >>= 1) la = min(la, (unsigned)__shfl_xor(la, sft));
    const unsigned pa = __builtin_amdgcn_readlane(la, 0) & 3, pb = __builtin_amdgcn_readlane(la, 32) & 3;
    const int base = (b >> 1) * (2 << 20) + (b & 1) * 524288 + ((((b & 2) ? pb : pa) ^ ((b & 1) * 2)) << 13) + W * 2048;
#pragma nounroll
    for (int n = 0; n < 21; ++n) {
        const int o = base + (n < 12 ? n * 32768 : 16384 + (n - 12) * 32768); const bool mem = n < 2 || n == 12 || n == 13;
#pragma unroll
        for (int k = 0; k < 2; ++k) { sst128(mem ? m0[k] : (u4){FF, FF, FF, FF}, r, lane * 16, o + k * 1024); sst128((u4){FF, FF, FF, FF}, r, lane * 16, o + k * 1024 + (1 << 20)); }
    }
    waitvm<0>(); bar();
    if (W == 0) sst32(tag + 1 + pa + 4 * pb, r, lane == 0 ? O_RDY + b * 128 : OOB, 0);
}
DI void chain_role(unsigned tag, int vb, int W, int role, int id, int xh) {
    CArgs& a = get_args();
    char LAS* sm = (char LAS*)smem_;
    const unsigned smb = (unsigned)(size_t)sm;
    const int lane = fresh_lane(), q = lane >> 4, l15 = lane & 15, i8 = lane & 7, hb = (lane >> 3) & 1;
    unsigned LAS* tb = (unsigned LAS*)(sm + L_TAB + W * 8192) + lane;
    const int wave = W;
    unsigned c1, n1;
    if (role && W == 0) dma_one(mksrd(role == 1 ? a.UM : a.TF, 28 * TU_P), (role == 1 ? id : id >> 1) * TU_P, 0, smb + 140 * 1024);
    const int cp = xh * 32768;
    const void *p1, *p2 = a.UF; unsigned x1 = 0, x2 = 0, po = 0; int fw, wk;
    if (role == 0) {
        const int part = id & 1, h = id >> 1, rr = l15 >> 2, c = lane & 3;
        const int bq = q == 0 ? 0 : q == 1 ? 528 : q == 2 ? 1152 : 1680;
        tb[8 * 64] = O_MEM + cp + lane * 16 + part * 4096; tb[9 * 64] = O_MEM + cp + lane * 16 + (1 - part) * 4096; tb[10 * 64] = O_MEM + cp + lane * 16 + 3072;
        tb[11 * 64] = smb + L_WRK + bq + perm16(l15) * 8 + (W == 3 ? 3 * 2304 + part * 256 : 0); tb[14 * 64] = smb + L_WRK + bq + perm16(l15) * 8 + 3 * 2304 + (1 - part) * 256;
        tb[12 * 64] = smb + L_WRK + (c == 0 ? 0 : c == 2 ? 528 : c == 1 ? 1152 : 1680) + (16 * (q >> 1) + 4 * (q & 1) + 8 * (rr >> 1) + (rr & 1)) * 8;
        const int op = O_SMM + id * 256 + hb * 128 + (4 * (q & 1) + 2 * (q >> 1)) * 16 + i8 * 4;
        tb[13 * 64] = op + cp; tb[15 * 64] = op + (cp ^ 32768);
        int tile, chunk;
        if (W < 3) {
            const int i = lane < 28 ? lane : 0;
            const int p = i < 8 ? i : i < 19 ? (int)(0x43424130210ull >> (4 * (i - 8))) & 7 : 5 + (i - 19) / 3, k = i < 8 ? 0 : i < 19 ? (int)(0x32221212111ull >> (4 * (i - 8))) & 7 : 1 + (i - 19) % 3;
            const int n = 3 * (p & 3) + W;
            tile = 4 * (4 * (p < 4 ? part : 1 - part) + (n >> 2)) + (n & 3); chunk = k < 3 ? 4 * part + (W + k) % 3 : 3;
        } else {
            const int p = lane < 2 ? 2 + lane : (lane - 2) & 3, k = lane < 2 || lane >= 14 ? 3 : (lane - 2) >> 2;
            tile = 12 + (p < 2 ? 2 * part + p : 2 * (1 - part) + p - 2); chunk = k < 3 ? 4 * part + k : 3;
        }
        wk = ((h * 28 + tile) * 7 + chunk) * 2048; fw = h * 8 + (lane & 3);
        p1 = a.WqkF; x1 = part + 1;
    } else if (role == 1) {
        const int hc = 4 * W + ((q & 1) << 1) + (q >> 1), k = i8 - 4 * hb;
        const int ocp = W == 0 ? O_MEM : W == 3 ? O_MEM + 32768 : O_MEMX + (W - 1) * 32768;
        tb[8 * 64] = O_MEM + cp + lane * 16; tb[9 * 64] = O_MEM + cp + lane * 16 + 4096;
        tb[10 * 64] = O_SMM + cp + hc * 512 + l15 * 16; tb[11 * 64] = O_MZ + hc * 128 + l15 * 8;
        tb[12 * 64] = RXW(id >> 1) + (id & 1) * 4096 + hc * 256 + l15 * 16;
        tb[13 * 64] = ocp + id * 256 + lane * 4;
        tb[14 * 64] = k == 0 ? 0xffffu : k == 1 ? 0xffff0000u : 0u; tb[15 * 64] = k == 2 ? 0xffffu : k == 3 ? 0xffff0000u : 0u;
        tb[16 * 64] = smb + L_WRK + W * 512 + lane * 8;
        tb[17 * 64] = smb + L_WRK + ((lane & 32) + (lane & 2) * 8 + (lane & 1) * 8 + ((lane >> 2) & 7)) * 8;
        tb[18 * 64] = id * TU_P + W * 1024 + lane * 16;
        tb[20 * 64] = smb + L_MEM + lane * 16;
        tb[21 * 64] = lane == 0 ? O_FT + (32 + id) * 128 : OOB;
        tb[23 * 64] = (q == 0 ? O_MEM : q == 3 ? O_MEM + 32768 : O_MEMX + (q - 1) * 32768) + id * 256 + l15 * 16;
        const bool lead = id == 14 * xh;
        tb[22 * 64] = lane != 0 ? OOB : W == 2 ? (lead ? O_REC + xh * 8 : OOB) : id == 0 ? O_EPOCH : OOB;
        const int ord = ((W == 0 ? 0x6543210 : W == 1 ? 0x3106542 : W == 2 ? 0x4321065 : 0x6542103) >> (4 * (lane % 7))) & 7;
        wk = ((id * 16 + 4 * W + (lane / 7 & 3)) * 7 + ord) * 2048; fw = (4 * W + (lane & 3)) * 8 + 4 + (id >> 3);
        p1 = a.WvoF; p2 = a.UM; po = OFF_P;
    } else {
        const int h = id >> 1, half = id & 1, pt = 4 * W + q;
        tb[8 * 64] = O_MEMX + cp + lane * 16; tb[9 * 64] = O_MEMX + cp + lane * 16 + 4096;
        tb[10 * 64] = (W < 3 || (q & 1) == 0) ? RXW(half * 7 + 2 * W + (q & 1)) + (q >> 1) * 4096 + h * 256 + l15 * 16 : OOB;
        tb[11 * 64] = ((lane & 3) == 0 && half == 0) ? O_MZ + h * 128 + (lane >> 2) * 8 : OOB;
        tb[12 * 64] = smb + L_WRK + lane * 8;
        tb[13 * 64] = smb + L_WRK + ((q >> 1) * 32 + (lane & 1) * 16 + 4 * (q & 1) + (l15 >> 2)) * 8;
        tb[14 * 64] = pt < 14 ? RXW(half * 7 + (pt >> 1)) + (pt & 1) * 4096 + h * 256 + l15 * 16 : (pt == 14 && half == 0 && l15 < 8) ? O_MZ + h * 128 + l15 * 16 : OOB;
        tb[15 * 64] = O_FW + (h * 8 + i8) * 128;
        tb[16 * 64] = smb + L_MEM + lane * 16;
        tb[17 * 64] = smb + L_WRK + 1024 + W * 2048 + ((q >> 1) * 16 + perm16(l15)) * 16 + (q & 1) * 8;
        tb[18 * 64] = smb + L_WRK + 1024 + W * 2048 + (8 * q + (l15 >> 2) + 32 * ((lane >> 1) & 1)) * 16 + (lane & 1) * 8;
        const int otf = h * TU_P + half * 3584 + W * 1024 + (4 * hb + q) * 128 + i8 * 16;
        tb[19 * 64] = otf; tb[21 * 64] = hb ? OOB : otf;
        tb[20 * 64] = lane == 0 ? O_FT + id * 128 : OOB;
        if (W == 3)
            for (int k = 0; k < 3; ++k) tb[(17 + k) * 64] = RXW(half * 7 + 2 * k + (q >> 1)) + (q & 1) * 4096 + h * 256 + l15 * 16;
        wk = ((h * 28 + half * 14 + 4 * W) * 7 + (lane < 42 ? lane : 0)) * 2048; fw = h * 8 + (lane & 3);
        p1 = W == 3 ? a.GF : a.WqkF; x1 = h * GU_H; x2 = h * GU_H + (half * 14 + 4 * W) * 1024; po = OFF_X;
    }
    tb[0 * 64] = (lane & 8) ? 0xEEEE : 0x4444; tb[1 * 64] = lane * 16; tb[2 * 64] = wk; tb[3 * 64] = ((vb >> 3) * 16 + (vb & 3) * 4 + W) * 1280 + lane * 4;
    tb[4 * 64] = O_FW + fw * 128; tb[5 * 64] = O_RDY + (lane & 3) * 128; tb[6 * 64] = O_SIDE + (lane & 31) * 4; tb[7 * 64] = O_REC;
    edge(c1, n1);
    const unsigned f = 5900;
    const unsigned k0 = n1 * 16u / LAT_R;
    const unsigned sz1 = (role == 2 && W == 3) ? GU_SZ : 28 * 16 * 14336, sz2 = role == 1 ? 28 * TU_P : GU_SZ;
    unsigned sv;
    if (lane < 4) sv = srdw(a.HB, HB_SZ, lane); else if (lane < 8) sv = srdw(p1, sz1, lane - 4); else if (lane < 12) sv = srdw(p2, sz2, lane - 8); else if (lane < 16) sv = srdw(a.TF, 16 * TU_P, lane - 12);
    else if (lane < 20) sv = srdw(a.TS, 512 * 1280, lane - 16);
    else sv = lane == 20 ? k0 * LAT_R : lane == 21 ? LAT_R : lane == 24 ? po : lane == 26 ? tag : lane == 35 ? k0 & 1 : lane == 29 ? x1 : lane == 30 ? x2
            : lane == 33 ? (unsigned)TOL_C : lane == 34 ? smb + L_CLK : 0u;
    tb[31 * 64] = sv;
    if (W == 0 && lane < 2) *(u4 LAS*)(sm + L_CLK + lane * 16) = lane == 0 ? (u4){n1 * 16u, c1, f, 0u} : (u4){0u, 0u, 0u, 0u};
    bar();
    const unsigned tab = smb + L_TAB + W * 8192 + lane * 4;
    if (role == 0) { if (W == 0) RUN(S0); else if (W == 1) RUN(S1); else if (W == 2) RUN(S2); else RUN(S3); }
    else if (role == 1) { if (W == 0) RUN(P0); else if (W == 1) RUN(P1); else if (W == 2) RUN(P2); else RUN(P3); }
    else { if (W == 0) RUN(X0); else if (W == 1) RUN(X1); else if (W == 2) RUN(X2); else RUN(X3); }
}

#define NPA_FAR 4
#define NPA_NEAR 3
#define XCC_XOR 4
#define HD(i) ((i) & 3)
#define SUB(i) ((i) >> 3)
#define PF_ROWS 864
#define PA_HOLD 0
#define PAO_AT 118
__global__ __launch_bounds__(256, 1) void fused_kernel(Args a_) {
    CArgs& a = get_args();
    const int wave = __builtin_amdgcn_readfirstlane(threadIdx.x >> 6);
    const u4 rhb = mksrd(a.HB, HB_SZ);
    unsigned ep = 0;
    if (wave == 0) ep = ld32(rhb, O_EPOCH, 0);
    else {
        u64 pc; unsigned len;
        asm volatile("s_getpc_b64 %0\nfk_code_lo:\n\ts_mov_b32 %1, fk_code_hi-fk_code_lo" : "=s"(pc), "=s"(len));
        const u4 rc = mksrd((const void*)pc, len & ~15u);
        const int vo = (((blockIdx.x >> 3) * 3 + wave - 1) * 2) * 1024 + (threadIdx.x & 63) * 16;
#pragma unroll
        for (int i = 0; i < 2; ++i) dma_one(rc, vo + i * 1024, 0, (unsigned)(size_t)(char LAS*)smem_ + 147456 + (wave * 3 + i) * 1024);
    }
    const int xcc = (__builtin_amdgcn_s_getreg((31 << 11) | 20) & 7) ^ XCC_XOR;
    int vb = xcc + (blockIdx.x & ~7);
    unsigned tag, best = 1016;
    if ((vb & 0xf5) == 0xf1) best = probe(rhb, ((vb >> 3) & 1) * 4 + wave, (vb >> 1) & 1);
    if ((vb & 0x85) == 0x80 && wave) {
        const int n = (((vb >> 1) & 1) * 16 + (vb >> 3) - 16) * 3 + wave - 1;
#pragma nounroll
        for (int r = n; r < PF_ROWS; r += 96) {
            const int u = 2 + (r >> 7);
            const u4 rs = r < 640 ? mksrd((r & 64) ? a.Wv : a.Wk, 448 * 8192) : mksrd(a.X, 2048 * 896);
            asm volatile("s_mov_b32 m0, %3\n\ts_nop 4\n\tbuffer_load_dword %0, %1, %2 offen lds" :: "v"((threadIdx.x & 63) * 128), "s"(rs), "s"((r < 640 ? 64 * CHUNK(u) + (r & 63) : r - 640) * 8192), "s"((unsigned)(size_t)(char LAS*)smem_ + 140 * 1024) : "memory");
        }
    }
#pragma nounroll
    for (int pass = 0;; ++pass) {
        const bool far = vb & 4;
        if (far) w_first(wave, (vb >> 5) & 1, 4 * (vb & 3) + HD(vb >> 3), SUB(vb >> 3), 0); else waitvm<0>();
        unsigned slot = (unsigned)xcc * 32u + (blockIdx.x >> 3), old = 0;
        if (wave == 0 && !pass) {
            WAITV(16, "+v"(ep));
            ep += 0x9E3779B4u; old = ep;
            asm volatile("s_nop 4\n\tbuffer_atomic_swap %0, %1, %2, 0 offen sc0 sc1" : "+v"(old) : "v"(threadIdx.x == 0 ? O_CNT + slot * 128u : (unsigned)OOB), "s"(rhb) : "memory");
        }
        if (far) { w_first(wave, (vb >> 5) & 1, 4 * (vb & 3) + HD(vb >> 3), SUB(vb >> 3), 1); } else waitvm<0>();
        WAITV(16, "+v"(old));
        if (pass) { bar(); break; }
        if (threadIdx.x == 0) {
            for (int k = 0; k < 256 && old == ep; ++k) {
                slot = (slot + 1u) & 255u;
                old = __hip_atomic_exchange((unsigned*)(a.HB + O_CNT) + slot * 32u, ep, __ATOMIC_RELAXED, __HIP_MEMORY_SCOPE_AGENT);
            }
            *(u2 LAS*)(smem_ + 147456) = (u2){(slot >> 5) + 8u * (slot & 31u), ep};
        }
        bar();
        const int got = __builtin_amdgcn_readfirstlane(*(const unsigned LAS*)(smem_ + 147456));
        tag = __builtin_amdgcn_readfirstlane(*(const unsigned LAS*)(smem_ + 147460));
        if (got == vb) break;
        vb = got; best = 1016; waitvm<0>(); bar();
    }
    const int res = vb & 7, idx = vb >> 3;
    int j;
    const int xq = res & 3;
    unsigned* const tick = (unsigned*)(a.HB + O_TICK) + xq * 32;
    if (res < 4) {
        const int xh = res >> 1;
        if (idx < 16 || ((res & 1) && idx < 30)) { chain_role(tag, vb, wave, (res & 1) ? idx >> 4 : 2, idx < 16 ? xh * 16 + idx : xh * 14 + idx - 16, xh); return; }
        if (res & 1) { ring_init(tag, wave, xh * 2 + (idx & 1), best); return; }
        if (wave == 0) sst32(tag + 1, rhb, fresh_lane() == 0 ? O_SIDE + (xh * 16 + idx - 16) * 4 : OOB, 0);
        slp(PA_HOLD * (idx & 15));
        j = -1;
    } else {
        if (idx == 0 && wave == 0) sst32(0u, rhb, fresh_lane() == 0 ? O_TICK + xq * 128 : OOB, 0);
        if (idx & 4) wgu_task<1>(tag, vb, wave, 4 * xq + HD(idx), SUB(idx)); else wgu_task<0>(tag, vb, wave, 4 * xq + HD(idx), SUB(idx));
        wait_prog(rhb, fresh_lane() == 0 ? O_FW + xq * 32 * 128 : OOB, tag + 1);
        j = 0;
    }
    int round = 0;
#pragma nounroll
    for (; round < 256; ++round) {
        if (j >= 0) {
            if (wave == 0 && fresh_lane() == 0) *(unsigned LAS*)(smem_ + 160 * 1024 - 16) = __hip_atomic_fetch_add(tick, 1u, __ATOMIC_RELAXED, __HIP_MEMORY_SCOPE_AGENT);
            bar();
            j = __builtin_amdgcn_readfirstlane(*(const unsigned LAS*)(smem_ + 160 * 1024 - 16));
            if ((unsigned)j >= NPA_FAR + 190u) break;
        }
        int k = j - NPA_FAR;
        if (k < 0) {
            parta_task(tag, vb, wave, j < 0 ? ((res >> 1) * 16 + idx - 16) * NPA_NEAR : 112 - (4 - xq) * NPA_FAR + j, j < 0 ? NPA_NEAR : 1);
            if (j < 0) break;
            continue;
        }
        if (k >= PAO_AT && k < PAO_AT + 14) { pa_out(tag, wave, (k - PAO_AT) * 4 + xq); bar(); continue; }
        if (k >= PAO_AT) k -= 14;
        const int g = k < 162 ? (k + 14) / 11 : 16 + (k - 162) / 7, o = k < 162 ? (k + 14) % 11 : (k - 162) % 7;
        if (k < 8 || o >= 7) e_task(tag, wave, 4 * (k < 8 ? k >> 2 : g) + xq, k < 8 ? k & 3 : o - 7);
        else { x_task(tag, wave, 4 * (g - 2) + xq, o); bar(); }
    }
}
__device__ __attribute__((used, noinline)) void fused_kernel_roles() { asm volatile("s_endpgm\n" ASM_ROLES "fk_code_hi:"); }

#define WS_SKIP 0
extern "C" void kernel_launch(void* const* d_in, const int* in_sizes, int n_in, void* d_out, int out_size, void* d_ws, size_t ws_size, hipStream_t stream) {
    Args a;
    a.Au = (const u16*)d_in[0]; a.AlogA = (const u16*)d_in[1]; a.Aim = (const u16*)d_in[2]; a.ACr = (const u16*)d_in[3]; a.ACi = (const u16*)d_in[4];
    a.Alogdt = (const u16*)d_in[5]; a.ADsk = (const u16*)d_in[6]; a.X = (const u16*)d_in[7]; a.mem0 = (const u16*)d_in[8];
    a.Wq = (const u16*)d_in[9]; a.Wk = (const u16*)d_in[10]; a.Wv = (const u16*)d_in[11]; a.Wo = (const u16*)d_in[12];
    a.out = (u16*)d_out;
    char* base = (char*)(((size_t)d_ws + WS_SKIP + (2u << 20) - 1) & ~(size_t)((2u << 20) - 1));
    a.HB = base;
    size_t off = 6u << 20;
    auto alloc = [&](size_t n) { char* p = base + off; off += (n + 4095) & ~(size_t)4095; return p; };
    a.GF = (u16*)alloc(GU_SZ); a.UF = (u16*)alloc(GU_SZ); a.TF = (u16*)alloc(16 * TU_P); a.UM = (u16*)alloc(28 * TU_P);
    a.WvoF = (u16*)alloc(28 * 16 * 14336); a.WqkF = (u16*)alloc(28 * 16 * 14336); a.EX = (u16*)alloc(64 * 16 * 2048); a.MZX = (float*)alloc(64 * 16 * 256); a.PA = (u16*)alloc(56 * 32768);
    a.TS = (unsigned*)alloc(768 * 1280);
    (void)hipFuncSetAttribute((const void*)fused_kernel, hipFuncAttributeMaxDynamicSharedMemorySize, 160 * 1024);
    fused_kernel<<<256, 256, 160 * 1024, stream>>>(a);
}
```

```cpp
#include <hip/hip_runtime.h>
#include <stdint.h>
typedef unsigned short u16;
typedef unsigned long long u64;
typedef __attribute__((ext_vector_type(8))) __bf16 bf8;
typedef __attribute__((ext_vector_type(16))) __bf16 bf16v;
typedef __attribute__((ext_vector_type(2))) __bf16 bf2;
typedef __attribute__((ext_vector_type(2))) float f2;
typedef __attribute__((ext_vector_type(4))) float f4;
typedef __attribute__((ext_vector_type(4))) unsigned u4;
typedef __attribute__((ext_vector_type(2))) unsigned u2;
typedef __attribute__((ext_vector_type(8))) unsigned u8v;
typedef __attribute__((ext_vector_type(4))) short s4;
#define LAS __attribute__((address_space(3)))
#define DI __device__ __forceinline__
#define SB() __builtin_amdgcn_sched_barrier(0)

DI unsigned pk2(float a, float b) { f2 v = {a, b}; return __builtin_bit_cast(unsigned, __builtin_convertvector(v, bf2)); }
DI float bfr(float a) { return __uint_as_float(pk2(a, a) & 0xffff0000u); }
DI unsigned san(unsigned x) { return x < 0xFFFFFFFEu ? x : 0xFFFFFFFEu; }
DI void bar() { asm volatile("s_waitcnt lgkmcnt(0)\n\ts_barrier" ::: "memory"); SB(); }
DI float x32max(float a) { auto r = __builtin_amdgcn_permlane32_swap(__float_as_uint(a), __float_as_uint(a), false, false); return fmaxf(__uint_as_float(r[0]), __uint_as_float(r[1])); }
DI float x32add(float a) { auto r = __builtin_amdgcn_permlane32_swap(__float_as_uint(a), __float_as_uint(a), false, false); return __uint_as_float(r[0]) + __uint_as_float(r[1]); }
DI u2 trread(const char LAS* p) { return __builtin_bit_cast(u2, __builtin_amdgcn_ds_read_tr16_b64_v4i16((LAS s4*)p)); }
DI u4 mksrd(const void* p, unsigned bytes) { u64 x = (u64)p; return (u4){(unsigned)x, (unsigned)(x >> 32) & 0xffffu, bytes, 0x00020000u}; }
DI u4 ld128(u4 srd, int voff, int soff) { u4 v; asm volatile("buffer_load_dwordx4 %0, %1, %2, %3 offen sc1" : "=v"(v) : "v"(voff), "s"(srd), "s"(soff) : "memory"); return v; }
DI unsigned ld32(u4 srd, int voff, int soff) { unsigned v; asm volatile("buffer_load_dword %0, %1, %2, %3 offen sc1" : "=v"(v) : "v"(voff), "s"(srd), "s"(soff) : "memory"); return v; }
DI void st128(u4 v, u4 srd, int voff, int soff) { asm volatile("buffer_store_dwordx4 %0, %1, %2, %3 offen sc1\n\ts_nop 1" :: "v"(v), "v"(voff), "s"(srd), "s"(soff) : "memory"); }
DI void st32(unsigned v, u4 srd, int voff, int soff) { asm volatile("buffer_store_dword %0, %1, %2, %3 offen sc1" :: "v"(v), "v"(voff), "s"(srd), "s"(soff) : "memory"); }
#define PIN(x) asm volatile("" : "+v"(x))
#define WAITV(n, ...) asm volatile("s_waitcnt vmcnt(" #n ")" : __VA_ARGS__ :: "memory")

#define NSEG 64
#define O_MEM 0
#define O_SMM (2 * 32768)
#define O_MZ (4 * 32768)
#define O_MEMX 16384
#define RXW(n) ((n) < 7 ? (5 + (n)) * 32768 : 16384 + ((n) - 5) * 32768)
#define O_PROBE (15 * 32768)
#define O_FL (4 << 20)
#define O_PAR O_FL
#define O_SIDE (O_FL + 256)
#define O_WIN (O_FL + 384)
#define O_FPA (O_FL + 1024)
#define O_FE (O_FL + 2048)
#define O_FW (O_FL + 4096)
#define O_FT (O_FW + 16384)
#define O_REC (O_FT + 8192)
#define O_CNT (O_REC + 16384)
#define O_EPOCH (O_CNT + 32768)
#define O_RDY (O_EPOCH + 1024)
#define O_TICK (O_EPOCH + 128)
#define HB_SZ (5 << 20)
#define GU_H ((2 << 20) + 15360)
#define GU_SZ (32 << 20)
#define TU_P 524288
#define SPIN_MAX (1u << 17)
#define LOG2E 1.4426950408889634f
#define FF 0xFFFFFFFFu
#define OOB 0x7f000000

struct Args {
    const u16 *Au, *AlogA, *Aim, *ACr, *ACi, *Alogdt, *ADsk, *X, *mem0, *Wq, *Wk, *Wv, *Wo;
    u16* out;
    char* HB;
    u16 *WvoF, *WqkF, *GF, *UF, *EX, *UM, *TF, *PA; float *MZX;
    unsigned* TS;
};
typedef const Args __attribute__((address_space(4))) CArgs;
DI CArgs& get_args() { CArgs* p = (CArgs*)__builtin_amdgcn_kernarg_segment_ptr(); asm volatile("" : "+s"(p)); return *p; }
extern __shared__ __attribute__((aligned(16))) char smem_[];
DI unsigned now32() { u64 t; asm volatile("s_memtime %0\n\ts_waitcnt lgkmcnt(0)" : "=s"(t) :: "memory"); return (unsigned)t; }
DI unsigned rt_now() { u64 t; asm volatile("s_memrealtime %0\n\ts_waitcnt lgkmcnt(0)" : "=s"(t) :: "memory"); return (unsigned)t; }
#define TNOW() 0
#define REC_A 1140
#define REC_B 890
#define REC_0 150
#define LAT_R 1200u
#define OFF_P 60
#define OFF_X 20
#define TOL_C -100
#define SEQ_LO 0x5a928398a418820ull
#define SEQ_MI 0xb3bb56f28c9ca30ull
#define SEQ_HI 0xfddfeull
#define ASM_ROLES \
".p2align 6\n" \
"fk_role_S0:\n" \
"ds_read_b32 v200, v255 offset:0\n" \
"ds_read_b32 v201, v255 offset:256\n" \
"ds_read_b32 v202, v255 offset:512\n" \
"ds_read_b32 v203, v255 offset:768\n" \
"ds_read_b32 v204, v255 offset:1024\n" \
"ds_read_b32 v205, v255 offset:1280\n" \
"ds_read_b32 v206, v255 offset:1536\n" \
"ds_read_b32 v207, v255 offset:1792\n" \
"ds_read_b32 v208, v255 offset:2048\n" \
"ds_read_b32 v209, v255 offset:2304\n" \
"ds_read_b32 v210, v255 offset:2560\n" \
"ds_read_b32 v211, v255 offset:2816\n" \
"ds_read_b32 v212, v255 offset:3072\n" \
"ds_read_b32 v213, v255 offset:3328\n" \
"ds_read_b32 v214, v255 offset:3584\n" \
"ds_read_b32 v215, v255 offset:3840\n" \
"ds_read_b32 v254, v255 offset:7936\n" \
"s_waitcnt lgkmcnt(0)\n" \
"v_readlane_b32 s4, v254, 0\n" \
"v_readlane_b32 s5, v254, 1\n" \
"v_readlane_b32 s6, v254, 2\n" \
"v_readlane_b32 s7, v254, 3\n" \
"v_readlane_b32 s8, v254, 4\n" \
"v_readlane_b32 s9, v254, 5\n" \
"v_readlane_b32 s10, v254, 6\n" \
"v_readlane_b32 s11, v254, 7\n" \
"v_readlane_b32 s12, v254, 8\n" \
"v_readlane_b32 s13, v254, 9\n" \
"v_readlane_b32 s14, v254, 10\n" \
"v_readlane_b32 s15, v254, 11\n" \
"v_readlane_b32 s16, v254, 12\n" \
"v_readlane_b32 s17, v254, 13\n" \
"v_readlane_b32 s18, v254, 14\n" \
"v_readlane_b32 s19, v254, 15\n" \
"v_readlane_b32 s20, v254, 16\n" \
"v_readlane_b32 s21, v254, 17\n" \
"v_readlane_b32 s22, v254, 18\n" \
"v_readlane_b32 s23, v254, 19\n" \
"v_readlane_b32 s24, v254, 20\n" \
"v_readlane_b32 s25, v254, 21\n" \
"v_readlane_b32 s26, v254, 22\n" \
"v_readlane_b32 s27, v254, 23\n" \
"v_readlane_b32 s28, v254, 24\n" \
"v_readlane_b32 s29, v254, 25\n" \
"v_readlane_b32 s30, v254, 26\n" \
"v_readlane_b32 s31, v254, 27\n" \
"v_readlane_b32 s32, v254, 28\n" \
"v_readlane_b32 s33, v254, 29\n" \
"v_readlane_b32 s34, v254, 30\n" \
"v_readlane_b32 s35, v254, 31\n" \
"v_readlane_b32 s36, v254, 32\n" \
"v_readlane_b32 s37, v254, 33\n" \
"v_readlane_b32 s38, v254, 34\n" \
"v_readlane_b32 s39, v254, 35\n" \
"s_mov_b32 s40, 0\n" \
"s_mov_b32 s51, 0\n" \
"s_nop 0\n" \
"v_mov_b32 v252, s38\n" \
"s_nop 4\n" \
"v_mov_b32 v188, -1\n" \
"s_mov_b32 s28, s33\n" \
"s_mov_b32 s33, -1\n" \
"v_writelane_b32 v251, s6, 0\n" \
"v_writelane_b32 v251, s10, 1\n" \
"v_writelane_b32 v251, s14, 2\n" \
"v_writelane_b32 v251, s18, 3\n" \
"s_mov_b32 s6, 0\n" \
"s_mov_b32 s10, 0\n" \
"s_mov_b32 s14, 0\n" \
"s_mov_b32 s18, 0\n" \
"s_mov_b32 s40, -1\n" \
"s_branch .LS0_pre\n" \
".LS0_wet:\n" \
"s_waitcnt vmcnt(0)\n" \
"v_readlane_b32 s6, v251, 0\n" \
"v_readlane_b32 s10, v251, 1\n" \
"v_readlane_b32 s14, v251, 2\n" \
"v_readlane_b32 s18, v251, 3\n" \
"s_nop 4\n" \
"s_mov_b32 s33, s28\n" \
"s_add_u32 s34, s33, 2\n" \
"s_add_u32 s36, s33, 4\n" \
"s_add_u32 s43, s30, s33\n" \
"s_mov_b32 s50, 0\n" \
".LS0_w1l:\n" \
"buffer_load_dword v240, v204, s[4:7], 0 offen sc1\n" \
"s_waitcnt vmcnt(0)\n" \
".LS0_w1c:\n" \
"v_subrev_u32 v241, s43, v240\n" \
"v_cmp_gt_u32 vcc, 0x100, v241\n" \
"s_cmp_eq_u64 vcc, -1\n" \
"s_cbranch_scc1 .LS0_w1d\n" \
"s_sleep 2\n" \
"s_add_u32 s50, s50, 1\n" \
"s_cmp_lt_u32 s50, 0x20000\n" \
"s_cbranch_scc1 .LS0_w1l\n" \
"s_branch .LS0_end\n" \
".LS0_w1d:\n" \
"v_readlane_b32 s43, v202, 0\n" \
"s_nop 4\n" \
"buffer_load_dwordx4 a[0:3], v201, s[8:11], s43 offen sc1\n" \
"buffer_load_dwordx4 a[4:7], v201, s[8:11], s43 offen offset:1024 sc1\n" \
"v_readlane_b32 s43, v202, 1\n" \
"s_nop 4\n" \
"buffer_load_dwordx4 a[8:11], v201, s[8:11], s43 offen sc1\n" \
"buffer_load_dwordx4 a[12:15], v201, s[8:11], s43 offen offset:1024 sc1\n" \
"v_readlane_b32 s43, v202, 2\n" \
"s_nop 4\n" \
"buffer_load_dwordx4 a[16:19], v201, s[8:11], s43 offen sc1\n" \
"buffer_load_dwordx4 a[20:23], v201, s[8:11], s43 offen offset:1024 sc1\n" \
"v_readlane_b32 s43, v202, 3\n" \
"s_nop 4\n" \
"buffer_load_dwordx4 a[24:27], v201, s[8:11], s43 offen sc1\n" \
"buffer_load_dwordx4 a[28:31], v201, s[8:11], s43 offen offset:1024 sc1\n" \
"v_readlane_b32 s43, v202, 4\n" \
"s_nop 4\n" \
"buffer_load_dwordx4 a[32:35], v201, s[8:11], s43 offen sc1\n" \
"buffer_load_dwordx4 a[36:39], v201, s[8:11], s43 offen offset:1024 sc1\n" \
"v_readlane_b32 s43, v202, 5\n" \
"s_nop 4\n" \
"buffer_load_dwordx4 a[40:43], v201, s[8:11], s43 offen sc1\n" \
"buffer_load_dwordx4 a[44:47], v201, s[8:11], s43 offen offset:1024 sc1\n" \
"v_readlane_b32 s43, v202, 6\n" \
"s_nop 4\n" \
"buffer_load_dwordx4 a[48:51], v201, s[8:11], s43 offen sc1\n" \
"buffer_load_dwordx4 a[52:55], v201, s[8:11], s43 offen offset:1024 sc1\n" \
"v_readlane_b32 s43, v202, 7\n" \
"s_nop 4\n" \
"buffer_load_dwordx4 a[56:59], v201, s[8:11], s43 offen sc1\n" \
"buffer_load_dwordx4 a[60:63], v201, s[8:11], s43 offen offset:1024 sc1\n" \
"s_add_u32 s43, s30, s34\n" \
"s_mov_b32 s50, 0\n" \
"s_branch .LS0_w2c\n" \
".LS0_w2l:\n" \
"buffer_load_dword v240, v204, s[4:7], 0 offen sc1\n" \
"s_waitcnt vmcnt(0)\n" \
".LS0_w2c:\n" \
"v_subrev_u32 v241, s43, v240\n" \
"v_cmp_gt_u32 vcc, 0x100, v241\n" \
"s_cmp_eq_u64 vcc, -1\n" \
"s_cbranch_scc1 .LS0_w2d\n" \
"s_sleep 2\n" \
"s_add_u32 s50, s50, 1\n" \
"s_cmp_lt_u32 s50, 0x20000\n" \
"s_cbranch_scc1 .LS0_w2l\n" \
"s_branch .LS0_end\n" \
".LS0_w2d:\n" \
"v_readlane_b32 s43, v202, 8\n" \
"s_nop 4\n" \
"buffer_load_dwordx4 a[64:67], v201, s[8:11], s43 offen sc1\n" \
"buffer_load_dwordx4 a[68:71], v201, s[8:11], s43 offen offset:1024 sc1\n" \
"v_readlane_b32 s43, v202, 9\n" \
"s_nop 4\n" \
"buffer_load_dwordx4 a[72:75], v201, s[8:11], s43 offen sc1\n" \
"buffer_load_dwordx4 a[76:79], v201, s[8:11], s43 offen offset:1024 sc1\n" \
"v_readlane_b32 s43, v202, 10\n" \
"s_nop 4\n" \
"buffer_load_dwordx4 a[80:83], v201, s[8:11], s43 offen sc1\n" \
"buffer_load_dwordx4 a[84:87], v201, s[8:11], s43 offen offset:1024 sc1\n" \
"v_readlane_b32 s43, v202, 11\n" \
"s_nop 4\n" \
"buffer_load_dwordx4 a[88:91], v201, s[8:11], s43 offen sc1\n" \
"buffer_load_dwordx4 a[92:95], v201, s[8:11], s43 offen offset:1024 sc1\n" \
"v_readlane_b32 s43, v202, 16\n" \
"s_nop 4\n" \
"buffer_load_dwordx4 a[128:131], v201, s[8:11], s43 offen sc1\n" \
"buffer_load_dwordx4 a[132:135], v201, s[8:11], s43 offen offset:1024 sc1\n" \
"v_readlane_b32 s43, v202, 19\n" \
"s_nop 4\n" \
"buffer_load_dwordx4 a[152:155], v201, s[8:11], s43 offen sc1\n" \
"buffer_load_dwordx4 a[156:159], v201, s[8:11], s43 offen offset:1024 sc1\n" \
"v_readlane_b32 s43, v202, 22\n" \
"s_nop 4\n" \
"buffer_load_dwordx4 a[176:179], v201, s[8:11], s43 offen sc1\n" \
"buffer_load_dwordx4 a[180:183], v201, s[8:11], s43 offen offset:1024 sc1\n" \
"v_readlane_b32 s43, v202, 25\n" \
"s_nop 4\n" \
"buffer_load_dwordx4 a[200:203], v201, s[8:11], s43 offen sc1\n" \
"buffer_load_dwordx4 a[204:207], v201, s[8:11], s43 offen offset:1024 sc1\n" \
"s_add_u32 s43, s30, s36\n" \
"s_mov_b32 s50, 0\n" \
"s_branch .LS0_w3c\n" \
".LS0_w3l:\n" \
"buffer_load_dword v240, v204, s[4:7], 0 offen sc1\n" \
"s_waitcnt vmcnt(0)\n" \
".LS0_w3c:\n" \
"v_subrev_u32 v241, s43, v240\n" \
"v_cmp_gt_u32 vcc, 0x100, v241\n" \
"s_cmp_eq_u64 vcc, -1\n" \
"s_cbranch_scc1 .LS0_w3d\n" \
"s_sleep 2\n" \
"s_add_u32 s50, s50, 1\n" \
"s_cmp_lt_u32 s50, 0x20000\n" \
"s_cbranch_scc1 .LS0_w3l\n" \
"s_branch .LS0_end\n" \
".LS0_w3d:\n" \
"v_readlane_b32 s43, v202, 12\n" \
"s_nop 4\n" \
"buffer_load_dwordx4 a[96:99], v201, s[8:11], s43 offen sc1\n" \
"buffer_load_dwordx4 a[100:103], v201, s[8:11], s43 offen offset:1024 sc1\n" \
"v_readlane_b32 s43, v202, 13\n" \
"s_nop 4\n" \
"buffer_load_dwordx4 a[104:107], v201, s[8:11], s43 offen sc1\n" \
"buffer_load_dwordx4 a[108:111], v201, s[8:11], s43 offen offset:1024 sc1\n" \
"v_readlane_b32 s43, v202, 14\n" \
"s_nop 4\n" \
"buffer_load_dwordx4 a[112:115], v201, s[8:11], s43 offen sc1\n" \
"buffer_load_dwordx4 a[116:119], v201, s[8:11], s43 offen offset:1024 sc1\n" \
"v_readlane_b32 s43, v202, 15\n" \
"s_nop 4\n" \
"buffer_load_dwordx4 a[120:123], v201, s[8:11], s43 offen sc1\n" \
"buffer_load_dwordx4 a[124:127], v201, s[8:11], s43 offen offset:1024 sc1\n" \
"v_readlane_b32 s43, v202, 17\n" \
"s_nop 4\n" \
"buffer_load_dwordx4 a[136:139], v201, s[8:11], s43 offen sc1\n" \
"buffer_load_dwordx4 a[140:143], v201, s[8:11], s43 offen offset:1024 sc1\n" \
"v_readlane_b32 s43, v202, 20\n" \
"s_nop 4\n" \
"buffer_load_dwordx4 a[160:163], v201, s[8:11], s43 offen sc1\n" \
"buffer_load_dwordx4 a[164:167], v201, s[8:11], s43 offen offset:1024 sc1\n" \
"v_readlane_b32 s43, v202, 23\n" \
"s_nop 4\n" \
"buffer_load_dwordx4 a[184:187], v201, s[8:11], s43 offen sc1\n" \
"buffer_load_dwordx4 a[188:191], v201, s[8:11], s43 offen offset:1024 sc1\n" \
"v_readlane_b32 s43, v202, 26\n" \
"s_nop 4\n" \
"buffer_load_dwordx4 a[208:211], v201, s[8:11], s43 offen sc1\n" \
"buffer_load_dwordx4 a[212:215], v201, s[8:11], s43 offen offset:1024 sc1\n" \
"s_add_u32 s43, s30, 7\n" \
"s_mov_b32 s50, 0\n" \
"s_branch .LS0_w4c\n" \
".LS0_w4l:\n" \
"buffer_load_dword v240, v204, s[4:7], 0 offen sc1\n" \
"s_waitcnt vmcnt(0)\n" \
".LS0_w4c:\n" \
"v_subrev_u32 v241, s43, v240\n" \
"v_cmp_gt_u32 vcc, 0x100, v241\n" \
"s_cmp_eq_u64 vcc, -1\n" \
"s_cbranch_scc1 .LS0_w4d\n" \
"s_sleep 2\n" \
"s_add_u32 s50, s50, 1\n" \
"s_cmp_lt_u32 s50, 0x20000\n" \
"s_cbranch_scc1 .LS0_w4l\n" \
"s_branch .LS0_end\n" \
".LS0_w4d:\n" \
"v_readlane_b32 s43, v202, 18\n" \
"s_nop 4\n" \
"buffer_load_dwordx4 a[144:147], v201, s[8:11], s43 offen sc1\n" \
"buffer_load_dwordx4 a[148:151], v201, s[8:11], s43 offen offset:1024 sc1\n" \
"v_readlane_b32 s43, v202, 21\n" \
"s_nop 4\n" \
"buffer_load_dwordx4 a[168:171], v201, s[8:11], s43 offen sc1\n" \
"buffer_load_dwordx4 a[172:175], v201, s[8:11], s43 offen offset:1024 sc1\n" \
"v_readlane_b32 s43, v202, 24\n" \
"s_nop 4\n" \
"buffer_load_dwordx4 a[192:195], v201, s[8:11], s43 offen sc1\n" \
"buffer_load_dwordx4 a[196:199], v201, s[8:11], s43 offen offset:1024 sc1\n" \
"v_readlane_b32 s43, v202, 27\n" \
"s_nop 4\n" \
"buffer_load_dwordx4 a[216:219], v201, s[8:11], s43 offen sc1\n" \
"buffer_load_dwordx4 a[220:223], v201, s[8:11], s43 offen offset:1024 sc1\n" \
"s_mov_b32 s33, -1\n" \
"s_add_u32 s43, s30, 1\n" \
"s_mov_b32 s50, 0\n" \
".LS0_w5l:\n" \
"buffer_load_dword v240, v205, s[4:7], 0 offen sc1\n" \
"s_waitcnt vmcnt(0)\n" \
".LS0_w5c:\n" \
"v_subrev_u32 v241, s43, v240\n" \
"v_cmp_gt_u32 vcc, 0x100, v241\n" \
"s_cmp_eq_u64 vcc, -1\n" \
"s_cbranch_scc1 .LS0_w5d\n" \
"s_sleep 2\n" \
"s_add_u32 s50, s50, 1\n" \
"s_cmp_lt_u32 s50, 0x20000\n" \
"s_cbranch_scc1 .LS0_w5l\n" \
"s_branch .LS0_end\n" \
".LS0_w5d:\n" \
"v_readfirstlane_b32 s31, v241\n" \
"s_lshr_b32 s32, s31, 2\n" \
"s_xor_b32 s29, s31, s32\n" \
"s_and_b32 s29, s29, 3\n" \
"s_lshl_b32 s29, s29, 13\n" \
"s_bitset1_b32 s29, 21\n" \
"s_and_b32 s31, s31, 3\n" \
"s_lshl_b32 s31, s31, 13\n" \
"s_bitcmp1_b32 s39, 0\n" \
"s_cselect_b32 s43, s29, 0\n" \
"s_xor_b32 s31, s31, s43\n" \
"s_xor_b32 s32, s31, 0x4000\n" \
"s_mov_b32 s39, 0\n" \
".LS0_pre:\n" \
"s_barrier\n" \
"ds_read_b96 v[248:250], v252\n" \
"s_waitcnt lgkmcnt(0)\n" \
"v_readfirstlane_b32 s44, v248\n" \
"v_readfirstlane_b32 s45, v249\n" \
"v_readfirstlane_b32 s46, v250\n" \
"s_mul_i32 s42, s25, s46\n" \
"s_lshr_b32 s42, s42, 12\n" \
".LS0_cu:\n" \
"s_sub_u32 s43, s24, s44\n" \
"s_cmp_lt_i32 s43, 0\n" \
"s_cbranch_scc0 .LS0_cud\n" \
"s_add_u32 s24, s24, s25\n" \
"s_xor_b32 s31, s31, s29\n" \
"s_xor_b32 s32, s32, s29\n" \
"s_xor_b32 s47, s47, s29\n" \
"s_add_u32 s39, s39, 1\n" \
"s_branch .LS0_cu\n" \
".LS0_cud:\n" \
".LS0_loop:\n" \
"s_add_u32 s43, s40, 0\n" \
"s_and_b32 s43, s43, 3\n" \
"s_bitcmp1_b32 s43, 0\n" \
"s_cselect_b32 s47, s32, s31\n" \
"s_lshl_b32 s43, s43, 19\n" \
"s_add_u32 s47, s47, s43\n" \
"s_sub_u32 s43, s24, s44\n" \
"s_max_i32 s43, s43, 0xfffd0000\n" \
"s_min_i32 s43, s43, 0x4000\n" \
"s_mul_i32 s43, s43, s46\n" \
"s_ashr_i32 s43, s43, 12\n" \
"s_add_u32 s41, s45, s43\n" \
"s_branch .LS0_a1\n" \
".LS0_retry:\n" \
"s_add_u32 s51, s51, 1\n" \
"s_cmp_gt_u32 s51, 0x20000\n" \
"s_cbranch_scc1 .LS0_end\n" \
".LS0_a0:\n" \
"s_add_u32 s41, s41, s42\n" \
"s_add_u32 s24, s24, s25\n" \
"s_xor_b32 s31, s31, s29\n" \
"s_xor_b32 s32, s32, s29\n" \
"s_xor_b32 s47, s47, s29\n" \
"s_xor_b32 s48, s48, s29\n" \
"s_add_u32 s39, s39, 1\n" \
".p2align 6\n" \
".LS0_a1:\n" \
"s_memtime vcc\n" \
"s_waitcnt lgkmcnt(0)\n" \
"s_sub_u32 s43, s41, vcc_lo\n" \
"s_cmp_gt_i32 s43, 128\n" \
"s_cbranch_scc1 .LS0_a1\n" \
"s_cmp_lt_i32 s43, s37\n" \
"s_cbranch_scc1 .LS0_a0\n" \
"s_max_i32 s43, s43, 0\n" \
"s_and_b32 s43, s43, -4\n" \
"s_getpc_b64 vcc\n" \
".LS0_a3:\n" \
"s_sub_u32 s43, .LS0_a4-.LS0_a3, s43\n" \
"s_add_u32 vcc_lo, vcc_lo, s43\n" \
"s_addc_u32 vcc_hi, vcc_hi, 0\n" \
"s_setpc_b64 vcc\n" \
".rept 32\n" \
"s_nop 0\n" \
".endr\n" \
".LS0_a4:\n" \
"buffer_load_dwordx4 v[0:3], v208, s[4:7], s47 offen sc1\n" \
"s_nop 9\n" \
"buffer_load_dwordx4 v[4:7], v208, s[4:7], s47 offen offset:1024 sc1\n" \
"s_nop 9\n" \
"buffer_load_dwordx4 v[8:11], v208, s[4:7], s47 offen offset:2048 sc1\n" \
"s_nop 9\n" \
"buffer_load_dwordx4 v[12:15], v210, s[4:7], s47 offen sc1\n" \
"s_add_u32 s35, s35, 1\n" \
"v_mov_b32 v28, 0\n" \
"v_mov_b32 v29, 0\n" \
"v_mov_b32 v30, 0\n" \
"v_mov_b32 v31, 0\n" \
"v_mov_b32 v32, 0\n" \
"v_mov_b32 v33, 0\n" \
"v_mov_b32 v34, 0\n" \
"v_mov_b32 v35, 0\n" \
"v_mov_b32 v36, 0\n" \
"v_mov_b32 v37, 0\n" \
"v_mov_b32 v38, 0\n" \
"v_mov_b32 v39, 0\n" \
"v_mov_b32 v40, 0\n" \
"v_mov_b32 v41, 0\n" \
"v_mov_b32 v42, 0\n" \
"v_mov_b32 v43, 0\n" \
"v_mov_b32 v44, 0\n" \
"v_mov_b32 v45, 0\n" \
"v_mov_b32 v46, 0\n" \
"v_mov_b32 v47, 0\n" \
"v_mov_b32 v48, 0\n" \
"v_mov_b32 v49, 0\n" \
"v_mov_b32 v50, 0\n" \
"v_mov_b32 v51, 0\n" \
"v_mov_b32 v52, 0\n" \
"v_mov_b32 v53, 0\n" \
"v_mov_b32 v54, 0\n" \
"v_mov_b32 v55, 0\n" \
"v_mov_b32 v56, 0\n" \
"v_mov_b32 v57, 0\n" \
"v_mov_b32 v58, 0\n" \
"v_mov_b32 v59, 0\n" \
"s_waitcnt vmcnt(3)\n" \
"v_smfmac_f32_16x16x64_bf16 v[28:31], v[0:3], a[0:7], v200\n" \
"v_smfmac_f32_16x16x64_bf16 v[32:35], v[0:3], a[8:15], v200\n" \
"v_smfmac_f32_16x16x64_bf16 v[36:39], v[0:3], a[16:23], v200\n" \
"v_smfmac_f32_16x16x64_bf16 v[40:43], v[0:3], a[24:31], v200\n" \
"v_smfmac_f32_16x16x64_bf16 v[44:47], v[0:3], a[32:39], v200\n" \
"v_smfmac_f32_16x16x64_bf16 v[48:51], v[0:3], a[40:47], v200\n" \
"v_smfmac_f32_16x16x64_bf16 v[52:55], v[0:3], a[48:55], v200\n" \
"v_smfmac_f32_16x16x64_bf16 v[56:59], v[0:3], a[56:63], v200\n" \
"s_waitcnt vmcnt(2)\n" \
"v_smfmac_f32_16x16x64_bf16 v[28:31], v[4:7], a[64:71], v200\n" \
"v_smfmac_f32_16x16x64_bf16 v[32:35], v[4:7], a[72:79], v200\n" \
"v_smfmac_f32_16x16x64_bf16 v[36:39], v[4:7], a[80:87], v200\n" \
"v_smfmac_f32_16x16x64_bf16 v[40:43], v[4:7], a[88:95], v200\n" \
"s_waitcnt vmcnt(1)\n" \
"v_smfmac_f32_16x16x64_bf16 v[28:31], v[8:11], a[96:103], v200\n" \
"s_nop 2\n" \
"v_smfmac_f32_16x16x64_bf16 v[32:35], v[8:11], a[104:111], v200\n" \
"s_nop 2\n" \
"v_smfmac_f32_16x16x64_bf16 v[36:39], v[8:11], a[112:119], v200\n" \
"s_nop 0\n" \
"v_cvt_pk_bf16_f32 v128, v28, v29\n" \
"v_cvt_pk_bf16_f32 v129, v30, v31\n" \
"v_smfmac_f32_16x16x64_bf16 v[40:43], v[8:11], a[120:127], v200\n" \
"ds_write_b64 v211, v[128:129] offset:0\n" \
"v_cvt_pk_bf16_f32 v130, v32, v33\n" \
"v_cvt_pk_bf16_f32 v131, v34, v35\n" \
"v_smfmac_f32_16x16x64_bf16 v[44:47], v[4:7], a[128:135], v200\n" \
"ds_write_b64 v211, v[130:131] offset:384\n" \
"v_cvt_pk_bf16_f32 v132, v36, v37\n" \
"v_cvt_pk_bf16_f32 v133, v38, v39\n" \
"v_smfmac_f32_16x16x64_bf16 v[44:47], v[8:11], a[136:143], v200\n" \
"s_waitcnt vmcnt(0)\n" \
"ds_write_b64 v211, v[132:133] offset:2560\n" \
"v_cvt_pk_bf16_f32 v134, v40, v41\n" \
"v_cvt_pk_bf16_f32 v135, v42, v43\n" \
"v_smfmac_f32_16x16x64_bf16 v[44:47], v[12:15], a[144:151], v200\n" \
"ds_write_b64 v211, v[134:135] offset:4736\n" \
"s_nop 1\n" \
"v_smfmac_f32_16x16x64_bf16 v[48:51], v[4:7], a[152:159], v200\n" \
"s_nop 2\n" \
"v_smfmac_f32_16x16x64_bf16 v[48:51], v[8:11], a[160:167], v200\n" \
"s_nop 0\n" \
"v_cvt_pk_bf16_f32 v136, v44, v45\n" \
"v_cvt_pk_bf16_f32 v137, v46, v47\n" \
"v_smfmac_f32_16x16x64_bf16 v[48:51], v[12:15], a[168:175], v200\n" \
"ds_write_b64 v211, v[136:137] offset:9216\n" \
"s_nop 1\n" \
"v_smfmac_f32_16x16x64_bf16 v[52:55], v[4:7], a[176:183], v200\n" \
"s_waitcnt lgkmcnt(1)\n" \
"s_barrier\n" \
"s_nop 0\n" \
"v_smfmac_f32_16x16x64_bf16 v[52:55], v[8:11], a[184:191], v200\n" \
"s_nop 0\n" \
"v_cvt_pk_bf16_f32 v138, v48, v49\n" \
"v_cvt_pk_bf16_f32 v139, v50, v51\n" \
"v_smfmac_f32_16x16x64_bf16 v[52:55], v[12:15], a[192:199], v200\n" \
"ds_write_b64 v211, v[138:139] offset:9600\n" \
"s_nop 1\n" \
"v_smfmac_f32_16x16x64_bf16 v[56:59], v[4:7], a[200:207], v200\n" \
"s_nop 2\n" \
"v_smfmac_f32_16x16x64_bf16 v[56:59], v[8:11], a[208:215], v200\n" \
"s_nop 2\n" \
"v_smfmac_f32_16x16x64_bf16 v[56:59], v[12:15], a[216:223], v200\n" \
"v_cvt_pk_bf16_f32 v140, v52, v53\n" \
"v_cvt_pk_bf16_f32 v141, v54, v55\n" \
"s_nop 1\n" \
"ds_write_b64 v211, v[140:141] offset:11776\n" \
"s_nop 2\n" \
"v_cvt_pk_bf16_f32 v142, v56, v57\n" \
"v_cvt_pk_bf16_f32 v143, v58, v59\n" \
"ds_write_b64 v211, v[142:143] offset:13952\n" \
"s_waitcnt lgkmcnt(1)\n" \
"s_barrier\n" \
"s_waitcnt lgkmcnt(0)\n" \
"s_barrier\n" \
"s_nop 3\n" \
"ds_read_b32 v64, v252 offset:20\n" \
"s_waitcnt lgkmcnt(0)\n" \
"v_readfirstlane_b32 s43, v64\n" \
"s_cmp_eq_u32 s43, s35\n" \
"s_cbranch_scc1 .LS0_retry\n" \
"s_nop 15\n" \
"v_max3_u32 v64, v0, v0, v1\n" \
"v_max3_u32 v64, v64, v2, v3\n" \
"v_max3_u32 v64, v64, v4, v5\n" \
"v_max3_u32 v64, v64, v6, v7\n" \
"v_max3_u32 v64, v64, v8, v9\n" \
"v_max3_u32 v64, v64, v10, v11\n" \
"v_max3_u32 v64, v64, v12, v13\n" \
"v_max3_u32 v64, v64, v14, v15\n" \
"v_cmp_eq_u32 vcc, -1, v64\n" \
"s_cbranch_vccnz .LS0_veto\n" \
".LS0_back:\n" \
"s_waitcnt lgkmcnt(0)\n" \
"s_barrier\n" \
"ds_read_b32 v64, v252 offset:16\n" \
"ds_read_b64 v[246:247], v252 offset:24\n" \
"ds_read_b96 v[248:250], v252\n" \
"s_waitcnt lgkmcnt(0)\n" \
"v_readfirstlane_b32 s44, v248\n" \
"v_readfirstlane_b32 s45, v249\n" \
"v_readfirstlane_b32 s46, v250\n" \
"v_readfirstlane_b32 s26, v246\n" \
"v_readfirstlane_b32 s27, v247\n" \
"s_mul_i32 s42, s25, s46\n" \
"s_lshr_b32 s42, s42, 12\n" \
"v_readfirstlane_b32 s43, v64\n" \
"s_cmp_eq_u32 s43, s35\n" \
"s_cbranch_scc1 .LS0_retry\n" \
"s_add_u32 s40, s40, 1\n" \
"s_add_u32 s24, s24, s26\n" \
"s_add_u32 s24, s24, s27\n" \
"s_cmp_eq_u32 s40, 0\n" \
"s_cbranch_scc1 .LS0_wet\n" \
"s_cmp_lt_u32 s40, 63\n" \
"s_cbranch_scc1 .LS0_loop\n" \
".LS0_end:\n" \
"s_endpgm\n" \
".LS0_veto:\n" \
"v_mov_b32 v64, s35\n" \
"ds_write_b32 v252, v64 offset:16\n" \
"s_branch .LS0_back\n" \
".p2align 6\n" \
"fk_role_S1:\n" \
"ds_read_b32 v200, v255 offset:0\n" \
"ds_read_b32 v201, v255 offset:256\n" \
"ds_read_b32 v202, v255 offset:512\n" \
"ds_read_b32 v203, v255 offset:768\n" \
"ds_read_b32 v204, v255 offset:1024\n" \
"ds_read_b32 v205, v255 offset:1280\n" \
"ds_read_b32 v206, v255 offset:1536\n" \
"ds_read_b32 v207, v255 offset:1792\n" \
"ds_read_b32 v208, v255 offset:2048\n" \
"ds_read_b32 v209, v255 offset:2304\n" \
"ds_read_b32 v210, v255 offset:2560\n" \
"ds_read_b32 v211, v255 offset:2816\n" \
"ds_read_b32 v212, v255 offset:3072\n" \
"ds_read_b32 v213, v255 offset:3328\n" \
"ds_read_b32 v214, v255 offset:3584\n" \
"ds_read_b32 v215, v255 offset:3840\n" \
"ds_read_b32 v254, v255 offset:7936\n" \
"s_waitcnt lgkmcnt(0)\n" \
"v_readlane_b32 s4, v254, 0\n" \
"v_readlane_b32 s5, v254, 1\n" \
"v_readlane_b32 s6, v254, 2\n" \
"v_readlane_b32 s7, v254, 3\n" \
"v_readlane_b32 s8, v254, 4\n" \
"v_readlane_b32 s9, v254, 5\n" \
"v_readlane_b32 s10, v254, 6\n" \
"v_readlane_b32 s11, v254, 7\n" \
"v_readlane_b32 s12, v254, 8\n" \
"v_readlane_b32 s13, v254, 9\n" \
"v_readlane_b32 s14, v254, 10\n" \
"v_readlane_b32 s15, v254, 11\n" \
"v_readlane_b32 s16, v254, 12\n" \
"v_readlane_b32 s17, v254, 13\n" \
"v_readlane_b32 s18, v254, 14\n" \
"v_readlane_b32 s19, v254, 15\n" \
"v_readlane_b32 s20, v254, 16\n" \
"v_readlane_b32 s21, v254, 17\n" \
"v_readlane_b32 s22, v254, 18\n" \
"v_readlane_b32 s23, v254, 19\n" \
"v_readlane_b32 s24, v254, 20\n" \
"v_readlane_b32 s25, v254, 21\n" \
"v_readlane_b32 s26, v254, 22\n" \
"v_readlane_b32 s27, v254, 23\n" \
"v_readlane_b32 s28, v254, 24\n" \
"v_readlane_b32 s29, v254, 25\n" \
"v_readlane_b32 s30, v254, 26\n" \
"v_readlane_b32 s31, v254, 27\n" \
"v_readlane_b32 s32, v254, 28\n" \
"v_readlane_b32 s33, v254, 29\n" \
"v_readlane_b32 s34, v254, 30\n" \
"v_readlane_b32 s35, v254, 31\n" \
"v_readlane_b32 s36, v254, 32\n" \
"v_readlane_b32 s37, v254, 33\n" \
"v_readlane_b32 s38, v254, 34\n" \
"v_readlane_b32 s39, v254, 35\n" \
"s_mov_b32 s40, 0\n" \
"s_mov_b32 s51, 0\n" \
"s_nop 0\n" \
"v_mov_b32 v252, s38\n" \
"s_nop 4\n" \
"v_mov_b32 v188, -1\n" \
"s_mov_b32 s28, s33\n" \
"s_mov_b32 s33, -1\n" \
"v_writelane_b32 v251, s6, 0\n" \
"v_writelane_b32 v251, s10, 1\n" \
"v_writelane_b32 v251, s14, 2\n" \
"v_writelane_b32 v251, s18, 3\n" \
"s_mov_b32 s6, 0\n" \
"s_mov_b32 s10, 0\n" \
"s_mov_b32 s14, 0\n" \
"s_mov_b32 s18, 0\n" \
"s_mov_b32 s40, -1\n" \
"s_branch .LS1_pre\n" \
".LS1_wet:\n" \
"s_waitcnt vmcnt(0)\n" \
"v_readlane_b32 s6, v251, 0\n" \
"v_readlane_b32 s10, v251, 1\n" \
"v_readlane_b32 s14, v251, 2\n" \
"v_readlane_b32 s18, v251, 3\n" \
"s_nop 4\n" \
"s_mov_b32 s33, s28\n" \
"s_add_u32 s34, s33, 2\n" \
"s_add_u32 s36, s33, 4\n" \
"s_add_u32 s43, s30, s33\n" \
"s_mov_b32 s50, 0\n" \
".LS1_w6l:\n" \
"buffer_load_dword v240, v204, s[4:7], 0 offen sc1\n" \
"s_waitcnt vmcnt(0)\n" \
".LS1_w6c:\n" \
"v_subrev_u32 v241, s43, v240\n" \
"v_cmp_gt_u32 vcc, 0x100, v241\n" \
"s_cmp_eq_u64 vcc, -1\n" \
"s_cbranch_scc1 .LS1_w6d\n" \
"s_sleep 2\n" \
"s_add_u32 s50, s50, 1\n" \
"s_cmp_lt_u32 s50, 0x20000\n" \
"s_cbranch_scc1 .LS1_w6l\n" \
"s_branch .LS1_end\n" \
".LS1_w6d:\n" \
"v_readlane_b32 s43, v202, 12\n" \
"s_nop 4\n" \
"buffer_load_dwordx4 a[96:99], v201, s[8:11], s43 offen sc1\n" \
"buffer_load_dwordx4 a[100:103], v201, s[8:11], s43 offen offset:1024 sc1\n" \
"v_readlane_b32 s43, v202, 13\n" \
"s_nop 4\n" \
"buffer_load_dwordx4 a[104:107], v201, s[8:11], s43 offen sc1\n" \
"buffer_load_dwordx4 a[108:111], v201, s[8:11], s43 offen offset:1024 sc1\n" \
"v_readlane_b32 s43, v202, 14\n" \
"s_nop 4\n" \
"buffer_load_dwordx4 a[112:115], v201, s[8:11], s43 offen sc1\n" \
"buffer_load_dwordx4 a[116:119], v201, s[8:11], s43 offen offset:1024 sc1\n" \
"v_readlane_b32 s43, v202, 15\n" \
"s_nop 4\n" \
"buffer_load_dwordx4 a[120:123], v201, s[8:11], s43 offen sc1\n" \
"buffer_load_dwordx4 a[124:127], v201, s[8:11], s43 offen offset:1024 sc1\n" \
"v_readlane_b32 s43, v202, 17\n" \
"s_nop 4\n" \
"buffer_load_dwordx4 a[136:139], v201, s[8:11], s43 offen sc1\n" \
"buffer_load_dwordx4 a[140:143], v201, s[8:11], s43 offen offset:1024 sc1\n" \
"v_readlane_b32 s43, v202, 20\n" \
"s_nop 4\n" \
"buffer_load_dwordx4 a[160:163], v201, s[8:11], s43 offen sc1\n" \
"buffer_load_dwordx4 a[164:167], v201, s[8:11], s43 offen offset:1024 sc1\n" \
"v_readlane_b32 s43, v202, 23\n" \
"s_nop 4\n" \
"buffer_load_dwordx4 a[184:187], v201, s[8:11], s43 offen sc1\n" \
"buffer_load_dwordx4 a[188:191], v201, s[8:11], s43 offen offset:1024 sc1\n" \
"v_readlane_b32 s43, v202, 26\n" \
"s_nop 4\n" \
"buffer_load_dwordx4 a[208:211], v201, s[8:11], s43 offen sc1\n" \
"buffer_load_dwordx4 a[212:215], v201, s[8:11], s43 offen offset:1024 sc1\n" \
"s_add_u32 s43, s30, s34\n" \
"s_mov_b32 s50, 0\n" \
"s_branch .LS1_w7c\n" \
".LS1_w7l:\n" \
"buffer_load_dword v240, v204, s[4:7], 0 offen sc1\n" \
"s_waitcnt vmcnt(0)\n" \
".LS1_w7c:\n" \
"v_subrev_u32 v241, s43, v240\n" \
"v_cmp_gt_u32 vcc, 0x100, v241\n" \
"s_cmp_eq_u64 vcc, -1\n" \
"s_cbranch_scc1 .LS1_w7d\n" \
"s_sleep 2\n" \
"s_add_u32 s50, s50, 1\n" \
"s_cmp_lt_u32 s50, 0x20000\n" \
"s_cbranch_scc1 .LS1_w7l\n" \
"s_branch .LS1_end\n" \
".LS1_w7d:\n" \
"v_readlane_b32 s43, v202, 0\n" \
"s_nop 4\n" \
"buffer_load_dwordx4 a[0:3], v201, s[8:11], s43 offen sc1\n" \
"buffer_load_dwordx4 a[4:7], v201, s[8:11], s43 offen offset:1024 sc1\n" \
"v_readlane_b32 s43, v202, 1\n" \
"s_nop 4\n" \
"buffer_load_dwordx4 a[8:11], v201, s[8:11], s43 offen sc1\n" \
"buffer_load_dwordx4 a[12:15], v201, s[8:11], s43 offen offset:1024 sc1\n" \
"v_readlane_b32 s43, v202, 2\n" \
"s_nop 4\n" \
"buffer_load_dwordx4 a[16:19], v201, s[8:11], s43 offen sc1\n" \
"buffer_load_dwordx4 a[20:23], v201, s[8:11], s43 offen offset:1024 sc1\n" \
"v_readlane_b32 s43, v202, 3\n" \
"s_nop 4\n" \
"buffer_load_dwordx4 a[24:27], v201, s[8:11], s43 offen sc1\n" \
"buffer_load_dwordx4 a[28:31], v201, s[8:11], s43 offen offset:1024 sc1\n" \
"v_readlane_b32 s43, v202, 4\n" \
"s_nop 4\n" \
"buffer_load_dwordx4 a[32:35], v201, s[8:11], s43 offen sc1\n" \
"buffer_load_dwordx4 a[36:39], v201, s[8:11], s43 offen offset:1024 sc1\n" \
"v_readlane_b32 s43, v202, 5\n" \
"s_nop 4\n" \
"buffer_load_dwordx4 a[40:43], v201, s[8:11], s43 offen sc1\n" \
"buffer_load_dwordx4 a[44:47], v201, s[8:11], s43 offen offset:1024 sc1\n" \
"v_readlane_b32 s43, v202, 6\n" \
"s_nop 4\n" \
"buffer_load_dwordx4 a[48:51], v201, s[8:11], s43 offen sc1\n" \
"buffer_load_dwordx4 a[52:55], v201, s[8:11], s43 offen offset:1024 sc1\n" \
"v_readlane_b32 s43, v202, 7\n" \
"s_nop 4\n" \
"buffer_load_dwordx4 a[56:59], v201, s[8:11], s43 offen sc1\n" \
"buffer_load_dwordx4 a[60:63], v201, s[8:11], s43 offen offset:1024 sc1\n" \
"s_add_u32 s43, s30, s36\n" \
"s_mov_b32 s50, 0\n" \
"s_branch .LS1_w8c\n" \
".LS1_w8l:\n" \
"buffer_load_dword v240, v204, s[4:7], 0 offen sc1\n" \
"s_waitcnt vmcnt(0)\n" \
".LS1_w8c:\n" \
"v_subrev_u32 v241, s43, v240\n" \
"v_cmp_gt_u32 vcc, 0x100, v241\n" \
"s_cmp_eq_u64 vcc, -1\n" \
"s_cbranch_scc1 .LS1_w8d\n" \
"s_sleep 2\n" \
"s_add_u32 s50, s50, 1\n" \
"s_cmp_lt_u32 s50, 0x20000\n" \
"s_cbranch_scc1 .LS1_w8l\n" \
"s_branch .LS1_end\n" \
".LS1_w8d:\n" \
"v_readlane_b32 s43, v202, 8\n" \
"s_nop 4\n" \
"buffer_load_dwordx4 a[64:67], v201, s[8:11], s43 offen sc1\n" \
"buffer_load_dwordx4 a[68:71], v201, s[8:11], s43 offen offset:1024 sc1\n" \
"v_readlane_b32 s43, v202, 9\n" \
"s_nop 4\n" \
"buffer_load_dwordx4 a[72:75], v201, s[8:11], s43 offen sc1\n" \
"buffer_load_dwordx4 a[76:79], v201, s[8:11], s43 offen offset:1024 sc1\n" \
"v_readlane_b32 s43, v202, 10\n" \
"s_nop 4\n" \
"buffer_load_dwordx4 a[80:83], v201, s[8:11], s43 offen sc1\n" \
"buffer_load_dwordx4 a[84:87], v201, s[8:11], s43 offen offset:1024 sc1\n" \
"v_readlane_b32 s43, v202, 11\n" \
"s_nop 4\n" \
"buffer_load_dwordx4 a[88:91], v201, s[8:11], s43 offen sc1\n" \
"buffer_load_dwordx4 a[92:95], v201, s[8:11], s43 offen offset:1024 sc1\n" \
"v_readlane_b32 s43, v202, 16\n" \
"s_nop 4\n" \
"buffer_load_dwordx4 a[128:131], v201, s[8:11], s43 offen sc1\n" \
"buffer_load_dwordx4 a[132:135], v201, s[8:11], s43 offen offset:1024 sc1\n" \
"v_readlane_b32 s43, v202, 19\n" \
"s_nop 4\n" \
"buffer_load_dwordx4 a[152:155], v201, s[8:11], s43 offen sc1\n" \
"buffer_load_dwordx4 a[156:159], v201, s[8:11], s43 offen offset:1024 sc1\n" \
"v_readlane_b32 s43, v202, 22\n" \
"s_nop 4\n" \
"buffer_load_dwordx4 a[176:179], v201, s[8:11], s43 offen sc1\n" \
"buffer_load_dwordx4 a[180:183], v201, s[8:11], s43 offen offset:1024 sc1\n" \
"v_readlane_b32 s43, v202, 25\n" \
"s_nop 4\n" \
"buffer_load_dwordx4 a[200:203], v201, s[8:11], s43 offen sc1\n" \
"buffer_load_dwordx4 a[204:207], v201, s[8:11], s43 offen offset:1024 sc1\n" \
"s_add_u32 s43, s30, 7\n" \
"s_mov_b32 s50, 0\n" \
"s_branch .LS1_w9c\n" \
".LS1_w9l:\n" \
"buffer_load_dword v240, v204, s[4:7], 0 offen sc1\n" \
"s_waitcnt vmcnt(0)\n" \
".LS1_w9c:\n" \
"v_subrev_u32 v241, s43, v240\n" \
"v_cmp_gt_u32 vcc, 0x100, v241\n" \
"s_cmp_eq_u64 vcc, -1\n" \
"s_cbranch_scc1 .LS1_w9d\n" \
"s_sleep 2\n" \
"s_add_u32 s50, s50, 1\n" \
"s_cmp_lt_u32 s50, 0x20000\n" \
"s_cbranch_scc1 .LS1_w9l\n" \
"s_branch .LS1_end\n" \
".LS1_w9d:\n" \
"v_readlane_b32 s43, v202, 18\n" \
"s_nop 4\n" \
"buffer_load_dwordx4 a[144:147], v201, s[8:11], s43 offen sc1\n" \
"buffer_load_dwordx4 a[148:151], v201, s[8:11], s43 offen offset:1024 sc1\n" \
"v_readlane_b32 s43, v202, 21\n" \
"s_nop 4\n" \
"buffer_load_dwordx4 a[168:171], v201, s[8:11], s43 offen sc1\n" \
"buffer_load_dwordx4 a[172:175], v201, s[8:11], s43 offen offset:1024 sc1\n" \
"v_readlane_b32 s43, v202, 24\n" \
"s_nop 4\n" \
"buffer_load_dwordx4 a[192:195], v201, s[8:11], s43 offen sc1\n" \
"buffer_load_dwordx4 a[196:199], v201, s[8:11], s43 offen offset:1024 sc1\n" \
"v_readlane_b32 s43, v202, 27\n" \
"s_nop 4\n" \
"buffer_load_dwordx4 a[216:219], v201, s[8:11], s43 offen sc1\n" \
"buffer_load_dwordx4 a[220:223], v201, s[8:11], s43 offen offset:1024 sc1\n" \
"s_mov_b32 s33, -1\n" \
"s_add_u32 s43, s30, 1\n" \
"s_mov_b32 s50, 0\n" \
".LS1_w10l:\n" \
"buffer_load_dword v240, v205, s[4:7], 0 offen sc1\n" \
"s_waitcnt vmcnt(0)\n" \
".LS1_w10c:\n" \
"v_subrev_u32 v241, s43, v240\n" \
"v_cmp_gt_u32 vcc, 0x100, v241\n" \
"s_cmp_eq_u64 vcc, -1\n" \
"s_cbranch_scc1 .LS1_w10d\n" \
"s_sleep 2\n" \
"s_add_u32 s50, s50, 1\n" \
"s_cmp_lt_u32 s50, 0x20000\n" \
"s_cbranch_scc1 .LS1_w10l\n" \
"s_branch .LS1_end\n" \
".LS1_w10d:\n" \
"v_readfirstlane_b32 s31, v241\n" \
"s_lshr_b32 s32, s31, 2\n" \
"s_xor_b32 s29, s31, s32\n" \
"s_and_b32 s29, s29, 3\n" \
"s_lshl_b32 s29, s29, 13\n" \
"s_bitset1_b32 s29, 21\n" \
"s_and_b32 s31, s31, 3\n" \
"s_lshl_b32 s31, s31, 13\n" \
"s_bitcmp1_b32 s39, 0\n" \
"s_cselect_b32 s43, s29, 0\n" \
"s_xor_b32 s31, s31, s43\n" \
"s_xor_b32 s32, s31, 0x4000\n" \
"s_mov_b32 s39, 0\n" \
".LS1_pre:\n" \
"s_barrier\n" \
"ds_read_b96 v[248:250], v252\n" \
"s_waitcnt lgkmcnt(0)\n" \
"v_readfirstlane_b32 s44, v248\n" \
"v_readfirstlane_b32 s45, v249\n" \
"v_readfirstlane_b32 s46, v250\n" \
"s_mul_i32 s42, s25, s46\n" \
"s_lshr_b32 s42, s42, 12\n" \
".LS1_cu:\n" \
"s_sub_u32 s43, s24, s44\n" \
"s_cmp_lt_i32 s43, 0\n" \
"s_cbranch_scc0 .LS1_cud\n" \
"s_add_u32 s24, s24, s25\n" \
"s_xor_b32 s31, s31, s29\n" \
"s_xor_b32 s32, s32, s29\n" \
"s_xor_b32 s47, s47, s29\n" \
"s_add_u32 s39, s39, 1\n" \
"s_branch .LS1_cu\n" \
".LS1_cud:\n" \
".LS1_loop:\n" \
"s_add_u32 s43, s40, 0\n" \
"s_and_b32 s43, s43, 3\n" \
"s_bitcmp1_b32 s43, 0\n" \
"s_cselect_b32 s47, s32, s31\n" \
"s_lshl_b32 s43, s43, 19\n" \
"s_add_u32 s47, s47, s43\n" \
"s_sub_u32 s43, s24, s44\n" \
"s_max_i32 s43, s43, 0xfffd0000\n" \
"s_min_i32 s43, s43, 0x4000\n" \
"s_mul_i32 s43, s43, s46\n" \
"s_ashr_i32 s43, s43, 12\n" \
"s_add_u32 s41, s45, s43\n" \
"s_add_u32 s41, s41, 4\n" \
"s_branch .LS1_a1\n" \
".LS1_retry:\n" \
"s_add_u32 s51, s51, 1\n" \
"s_cmp_gt_u32 s51, 0x20000\n" \
"s_cbranch_scc1 .LS1_end\n" \
".LS1_a0:\n" \
"s_add_u32 s41, s41, s42\n" \
"s_add_u32 s24, s24, s25\n" \
"s_xor_b32 s31, s31, s29\n" \
"s_xor_b32 s32, s32, s29\n" \
"s_xor_b32 s47, s47, s29\n" \
"s_xor_b32 s48, s48, s29\n" \
"s_add_u32 s39, s39, 1\n" \
".p2align 6\n" \
".LS1_a1:\n" \
"s_memtime vcc\n" \
"s_waitcnt lgkmcnt(0)\n" \
"s_sub_u32 s43, s41, vcc_lo\n" \
"s_cmp_gt_i32 s43, 128\n" \
"s_cbranch_scc1 .LS1_a1\n" \
"s_cmp_lt_i32 s43, s37\n" \
"s_cbranch_scc1 .LS1_a0\n" \
"s_max_i32 s43, s43, 0\n" \
"s_and_b32 s43, s43, -4\n" \
"s_getpc_b64 vcc\n" \
".LS1_a3:\n" \
"s_sub_u32 s43, .LS1_a4-.LS1_a3, s43\n" \
"s_add_u32 vcc_lo, vcc_lo, s43\n" \
"s_addc_u32 vcc_hi, vcc_hi, 0\n" \
"s_setpc_b64 vcc\n" \
".rept 32\n" \
"s_nop 0\n" \
".endr\n" \
".LS1_a4:\n" \
"buffer_load_dwordx4 v[0:3], v208, s[4:7], s47 offen offset:1024 sc1\n" \
"s_nop 9\n" \
"buffer_load_dwordx4 v[4:7], v208, s[4:7], s47 offen offset:2048 sc1\n" \
"s_nop 9\n" \
"buffer_load_dwordx4 v[8:11], v208, s[4:7], s47 offen sc1\n" \
"s_nop 9\n" \
"buffer_load_dwordx4 v[12:15], v210, s[4:7], s47 offen sc1\n" \
"s_add_u32 s35, s35, 1\n" \
"v_mov_b32 v28, 0\n" \
"v_mov_b32 v29, 0\n" \
"v_mov_b32 v30, 0\n" \
"v_mov_b32 v31, 0\n" \
"v_mov_b32 v32, 0\n" \
"v_mov_b32 v33, 0\n" \
"v_mov_b32 v34, 0\n" \
"v_mov_b32 v35, 0\n" \
"v_mov_b32 v36, 0\n" \
"v_mov_b32 v37, 0\n" \
"v_mov_b32 v38, 0\n" \
"v_mov_b32 v39, 0\n" \
"v_mov_b32 v40, 0\n" \
"v_mov_b32 v41, 0\n" \
"v_mov_b32 v42, 0\n" \
"v_mov_b32 v43, 0\n" \
"v_mov_b32 v44, 0\n" \
"v_mov_b32 v45, 0\n" \
"v_mov_b32 v46, 0\n" \
"v_mov_b32 v47, 0\n" \
"v_mov_b32 v48, 0\n" \
"v_mov_b32 v49, 0\n" \
"v_mov_b32 v50, 0\n" \
"v_mov_b32 v51, 0\n" \
"v_mov_b32 v52, 0\n" \
"v_mov_b32 v53, 0\n" \
"v_mov_b32 v54, 0\n" \
"v_mov_b32 v55, 0\n" \
"v_mov_b32 v56, 0\n" \
"v_mov_b32 v57, 0\n" \
"v_mov_b32 v58, 0\n" \
"v_mov_b32 v59, 0\n" \
"s_waitcnt vmcnt(3)\n" \
"v_smfmac_f32_16x16x64_bf16 v[28:31], v[0:3], a[0:7], v200\n" \
"v_smfmac_f32_16x16x64_bf16 v[32:35], v[0:3], a[8:15], v200\n" \
"v_smfmac_f32_16x16x64_bf16 v[36:39], v[0:3], a[16:23], v200\n" \
"v_smfmac_f32_16x16x64_bf16 v[40:43], v[0:3], a[24:31], v200\n" \
"v_smfmac_f32_16x16x64_bf16 v[44:47], v[0:3], a[32:39], v200\n" \
"v_smfmac_f32_16x16x64_bf16 v[48:51], v[0:3], a[40:47], v200\n" \
"v_smfmac_f32_16x16x64_bf16 v[52:55], v[0:3], a[48:55], v200\n" \
"v_smfmac_f32_16x16x64_bf16 v[56:59], v[0:3], a[56:63], v200\n" \
"s_waitcnt vmcnt(2)\n" \
"v_smfmac_f32_16x16x64_bf16 v[28:31], v[4:7], a[64:71], v200\n" \
"v_smfmac_f32_16x16x64_bf16 v[32:35], v[4:7], a[72:79], v200\n" \
"v_smfmac_f32_16x16x64_bf16 v[36:39], v[4:7], a[80:87], v200\n" \
"v_smfmac_f32_16x16x64_bf16 v[40:43], v[4:7], a[88:95], v200\n" \
"s_waitcnt vmcnt(1)\n" \
"v_smfmac_f32_16x16x64_bf16 v[28:31], v[8:11], a[96:103], v200\n" \
"s_nop 2\n" \
"v_smfmac_f32_16x16x64_bf16 v[32:35], v[8:11], a[104:111], v200\n" \
"s_nop 2\n" \
"v_smfmac_f32_16x16x64_bf16 v[36:39], v[8:11], a[112:119], v200\n" \
"s_nop 0\n" \
"v_cvt_pk_bf16_f32 v128, v28, v29\n" \
"v_cvt_pk_bf16_f32 v129, v30, v31\n" \
"v_smfmac_f32_16x16x64_bf16 v[40:43], v[8:11], a[120:127], v200\n" \
"ds_write_b64 v211, v[128:129] offset:128\n" \
"v_cvt_pk_bf16_f32 v130, v32, v33\n" \
"v_cvt_pk_bf16_f32 v131, v34, v35\n" \
"v_smfmac_f32_16x16x64_bf16 v[44:47], v[4:7], a[128:135], v200\n" \
"ds_write_b64 v211, v[130:131] offset:2304\n" \
"v_cvt_pk_bf16_f32 v132, v36, v37\n" \
"v_cvt_pk_bf16_f32 v133, v38, v39\n" \
"v_smfmac_f32_16x16x64_bf16 v[44:47], v[8:11], a[136:143], v200\n" \
"s_waitcnt vmcnt(0)\n" \
"ds_write_b64 v211, v[132:133] offset:2688\n" \
"v_cvt_pk_bf16_f32 v134, v40, v41\n" \
"v_cvt_pk_bf16_f32 v135, v42, v43\n" \
"v_smfmac_f32_16x16x64_bf16 v[44:47], v[12:15], a[144:151], v200\n" \
"ds_write_b64 v211, v[134:135] offset:4864\n" \
"s_nop 1\n" \
"v_smfmac_f32_16x16x64_bf16 v[48:51], v[4:7], a[152:159], v200\n" \
"s_nop 2\n" \
"v_smfmac_f32_16x16x64_bf16 v[48:51], v[8:11], a[160:167], v200\n" \
"s_nop 0\n" \
"v_cvt_pk_bf16_f32 v136, v44, v45\n" \
"v_cvt_pk_bf16_f32 v137, v46, v47\n" \
"v_smfmac_f32_16x16x64_bf16 v[48:51], v[12:15], a[168:175], v200\n" \
"ds_write_b64 v211, v[136:137] offset:9344\n" \
"s_nop 1\n" \
"v_smfmac_f32_16x16x64_bf16 v[52:55], v[4:7], a[176:183], v200\n" \
"s_waitcnt lgkmcnt(1)\n" \
"s_barrier\n" \
"s_nop 0\n" \
"v_smfmac_f32_16x16x64_bf16 v[52:55], v[8:11], a[184:191], v200\n" \
"s_nop 0\n" \
"v_cvt_pk_bf16_f32 v138, v48, v49\n" \
"v_cvt_pk_bf16_f32 v139, v50, v51\n" \
"v_smfmac_f32_16x16x64_bf16 v[52:55], v[12:15], a[192:199], v200\n" \
"ds_write_b64 v211, v[138:139] offset:11520\n" \
"s_nop 1\n" \
"v_smfmac_f32_16x16x64_bf16 v[56:59], v[4:7], a[200:207], v200\n" \
"s_nop 2\n" \
"v_smfmac_f32_16x16x64_bf16 v[56:59], v[8:11], a[208:215], v200\n" \
"s_nop 2\n" \
"v_smfmac_f32_16x16x64_bf16 v[56:59], v[12:15], a[216:223], v200\n" \
"v_cvt_pk_bf16_f32 v140, v52, v53\n" \
"v_cvt_pk_bf16_f32 v141, v54, v55\n" \
"s_nop 1\n" \
"ds_write_b64 v211, v[140:141] offset:11904\n" \
"s_nop 2\n" \
"v_cvt_pk_bf16_f32 v142, v56, v57\n" \
"v_cvt_pk_bf16_f32 v143, v58, v59\n" \
"ds_write_b64 v211, v[142:143] offset:14080\n" \
"s_waitcnt lgkmcnt(1)\n" \
"s_barrier\n" \
"s_waitcnt lgkmcnt(0)\n" \
"s_barrier\n" \
"s_nop 3\n" \
"ds_read_b32 v64, v252 offset:20\n" \
"s_waitcnt lgkmcnt(0)\n" \
"v_readfirstlane_b32 s43, v64\n" \
"s_cmp_eq_u32 s43, s35\n" \
"s_cbranch_scc1 .LS1_retry\n" \
"s_nop 15\n" \
"v_max3_u32 v64, v0, v0, v1\n" \
"v_max3_u32 v64, v64, v2, v3\n" \
"v_max3_u32 v64, v64, v4, v5\n" \
"v_max3_u32 v64, v64, v6, v7\n" \
"v_max3_u32 v64, v64, v8, v9\n" \
"v_max3_u32 v64, v64, v10, v11\n" \
"v_max3_u32 v64, v64, v12, v13\n" \
"v_max3_u32 v64, v64, v14, v15\n" \
"v_cmp_eq_u32 vcc, -1, v64\n" \
"s_cbranch_vccnz .LS1_veto\n" \
".LS1_back:\n" \
"s_waitcnt lgkmcnt(0)\n" \
"s_barrier\n" \
"ds_read_b32 v64, v252 offset:16\n" \
"ds_read_b64 v[246:247], v252 offset:24\n" \
"ds_read_b96 v[248:250], v252\n" \
"s_waitcnt lgkmcnt(0)\n" \
"v_readfirstlane_b32 s44, v248\n" \
"v_readfirstlane_b32 s45, v249\n" \
"v_readfirstlane_b32 s46, v250\n" \
"v_readfirstlane_b32 s26, v246\n" \
"v_readfirstlane_b32 s27, v247\n" \
"s_mul_i32 s42, s25, s46\n" \
"s_lshr_b32 s42, s42, 12\n" \
"v_readfirstlane_b32 s43, v64\n" \
"s_cmp_eq_u32 s43, s35\n" \
"s_cbranch_scc1 .LS1_retry\n" \
"s_add_u32 s40, s40, 1\n" \
"s_add_u32 s24, s24, s26\n" \
"s_add_u32 s24, s24, s27\n" \
"s_cmp_eq_u32 s40, 0\n" \
"s_cbranch_scc1 .LS1_wet\n" \
"s_cmp_lt_u32 s40, 63\n" \
"s_cbranch_scc1 .LS1_loop\n" \
".LS1_end:\n" \
"s_endpgm\n" \
".LS1_veto:\n" \
"v_mov_b32 v64, s35\n" \
"ds_write_b32 v252, v64 offset:16\n" \
"s_branch .LS1_back\n" \
".p2align 6\n" \
"fk_role_S2:\n" \
"ds_read_b32 v200, v255 offset:0\n" \
"ds_read_b32 v201, v255 offset:256\n" \
"ds_read_b32 v202, v255 offset:512\n" \
"ds_read_b32 v203, v255 offset:768\n" \
"ds_read_b32 v204, v255 offset:1024\n" \
"ds_read_b32 v205, v255 offset:1280\n" \
"ds_read_b32 v206, v255 offset:1536\n" \
"ds_read_b32 v207, v255 offset:1792\n" \
"ds_read_b32 v208, v255 offset:2048\n" \
"ds_read_b32 v209, v255 offset:2304\n" \
"ds_read_b32 v210, v255 offset:2560\n" \
"ds_read_b32 v211, v255 offset:2816\n" \
"ds_read_b32 v212, v255 offset:3072\n" \
"ds_read_b32 v213, v255 offset:3328\n" \
"ds_read_b32 v214, v255 offset:3584\n" \
"ds_read_b32 v215, v255 offset:3840\n" \
"ds_read_b32 v254, v255 offset:7936\n" \
"s_waitcnt lgkmcnt(0)\n" \
"v_readlane_b32 s4, v254, 0\n" \
"v_readlane_b32 s5, v254, 1\n" \
"v_readlane_b32 s6, v254, 2\n" \
"v_readlane_b32 s7, v254, 3\n" \
"v_readlane_b32 s8, v254, 4\n" \
"v_readlane_b32 s9, v254, 5\n" \
"v_readlane_b32 s10, v254, 6\n" \
"v_readlane_b32 s11, v254, 7\n" \
"v_readlane_b32 s12, v254, 8\n" \
"v_readlane_b32 s13, v254, 9\n" \
"v_readlane_b32 s14, v254, 10\n" \
"v_readlane_b32 s15, v254, 11\n" \
"v_readlane_b32 s16, v254, 12\n" \
"v_readlane_b32 s17, v254, 13\n" \
"v_readlane_b32 s18, v254, 14\n" \
"v_readlane_b32 s19, v254, 15\n" \
"v_readlane_b32 s20, v254, 16\n" \
"v_readlane_b32 s21, v254, 17\n" \
"v_readlane_b32 s22, v254, 18\n" \
"v_readlane_b32 s23, v254, 19\n" \
"v_readlane_b32 s24, v254, 20\n" \
"v_readlane_b32 s25, v254, 21\n" \
"v_readlane_b32 s26, v254, 22\n" \
"v_readlane_b32 s27, v254, 23\n" \
"v_readlane_b32 s28, v254, 24\n" \
"v_readlane_b32 s29, v254, 25\n" \
"v_readlane_b32 s30, v254, 26\n" \
"v_readlane_b32 s31, v254, 27\n" \
"v_readlane_b32 s32, v254, 28\n" \
"v_readlane_b32 s33, v254, 29\n" \
"v_readlane_b32 s34, v254, 30\n" \
"v_readlane_b32 s35, v254, 31\n" \
"v_readlane_b32 s36, v254, 32\n" \
"v_readlane_b32 s37, v254, 33\n" \
"v_readlane_b32 s38, v254, 34\n" \
"v_readlane_b32 s39, v254, 35\n" \
"s_mov_b32 s40, 0\n" \
"s_mov_b32 s51, 0\n" \
"s_nop 0\n" \
"v_mov_b32 v252, s38\n" \
"s_nop 4\n" \
"v_mov_b32 v188, -1\n" \
"s_mov_b32 s28, s33\n" \
"s_mov_b32 s33, -1\n" \
"v_writelane_b32 v251, s6, 0\n" \
"v_writelane_b32 v251, s10, 1\n" \
"v_writelane_b32 v251, s14, 2\n" \
"v_writelane_b32 v251, s18, 3\n" \
"s_mov_b32 s6, 0\n" \
"s_mov_b32 s10, 0\n" \
"s_mov_b32 s14, 0\n" \
"s_mov_b32 s18, 0\n" \
"s_mov_b32 s40, -1\n" \
"s_branch .LS2_pre\n" \
".LS2_wet:\n" \
"s_waitcnt vmcnt(0)\n" \
"v_readlane_b32 s6, v251, 0\n" \
"v_readlane_b32 s10, v251, 1\n" \
"v_readlane_b32 s14, v251, 2\n" \
"v_readlane_b32 s18, v251, 3\n" \
"s_nop 4\n" \
"s_mov_b32 s33, s28\n" \
"s_add_u32 s34, s33, 2\n" \
"s_add_u32 s36, s33, 4\n" \
"s_add_u32 s43, s30, s33\n" \
"s_mov_b32 s50, 0\n" \
".LS2_w11l:\n" \
"buffer_load_dword v240, v204, s[4:7], 0 offen sc1\n" \
"s_waitcnt vmcnt(0)\n" \
".LS2_w11c:\n" \
"v_subrev_u32 v241, s43, v240\n" \
"v_cmp_gt_u32 vcc, 0x100, v241\n" \
"s_cmp_eq_u64 vcc, -1\n" \
"s_cbranch_scc1 .LS2_w11d\n" \
"s_sleep 2\n" \
"s_add_u32 s50, s50, 1\n" \
"s_cmp_lt_u32 s50, 0x20000\n" \
"s_cbranch_scc1 .LS2_w11l\n" \
"s_branch .LS2_end\n" \
".LS2_w11d:\n" \
"v_readlane_b32 s43, v202, 8\n" \
"s_nop 4\n" \
"buffer_load_dwordx4 a[64:67], v201, s[8:11], s43 offen sc1\n" \
"buffer_load_dwordx4 a[68:71], v201, s[8:11], s43 offen offset:1024 sc1\n" \
"v_readlane_b32 s43, v202, 9\n" \
"s_nop 4\n" \
"buffer_load_dwordx4 a[72:75], v201, s[8:11], s43 offen sc1\n" \
"buffer_load_dwordx4 a[76:79], v201, s[8:11], s43 offen offset:1024 sc1\n" \
"v_readlane_b32 s43, v202, 10\n" \
"s_nop 4\n" \
"buffer_load_dwordx4 a[80:83], v201, s[8:11], s43 offen sc1\n" \
"buffer_load_dwordx4 a[84:87], v201, s[8:11], s43 offen offset:1024 sc1\n" \
"v_readlane_b32 s43, v202, 11\n" \
"s_nop 4\n" \
"buffer_load_dwordx4 a[88:91], v201, s[8:11], s43 offen sc1\n" \
"buffer_load_dwordx4 a[92:95], v201, s[8:11], s43 offen offset:1024 sc1\n" \
"v_readlane_b32 s43, v202, 16\n" \
"s_nop 4\n" \
"buffer_load_dwordx4 a[128:131], v201, s[8:11], s43 offen sc1\n" \
"buffer_load_dwordx4 a[132:135], v201, s[8:11], s43 offen offset:1024 sc1\n" \
"v_readlane_b32 s43, v202, 19\n" \
"s_nop 4\n" \
"buffer_load_dwordx4 a[152:155], v201, s[8:11], s43 offen sc1\n" \
"buffer_load_dwordx4 a[156:159], v201, s[8:11], s43 offen offset:1024 sc1\n" \
"v_readlane_b32 s43, v202, 22\n" \
"s_nop 4\n" \
"buffer_load_dwordx4 a[176:179], v201, s[8:11], s43 offen sc1\n" \
"buffer_load_dwordx4 a[180:183], v201, s[8:11], s43 offen offset:1024 sc1\n" \
"v_readlane_b32 s43, v202, 25\n" \
"s_nop 4\n" \
"buffer_load_dwordx4 a[200:203], v201, s[8:11], s43 offen sc1\n" \
"buffer_load_dwordx4 a[204:207], v201, s[8:11], s43 offen offset:1024 sc1\n" \
"s_add_u32 s43, s30, s34\n" \
"s_mov_b32 s50, 0\n" \
"s_branch .LS2_w12c\n" \
".LS2_w12l:\n" \
"buffer_load_dword v240, v204, s[4:7], 0 offen sc1\n" \
"s_waitcnt vmcnt(0)\n" \
".LS2_w12c:\n" \
"v_subrev_u32 v241, s43, v240\n" \
"v_cmp_gt_u32 vcc, 0x100, v241\n" \
"s_cmp_eq_u64 vcc, -1\n" \
"s_cbranch_scc1 .LS2_w12d\n" \
"s_sleep 2\n" \
"s_add_u32 s50, s50, 1\n" \
"s_cmp_lt_u32 s50, 0x20000\n" \
"s_cbranch_scc1 .LS2_w12l\n" \
"s_branch .LS2_end\n" \
".LS2_w12d:\n" \
"v_readlane_b32 s43, v202, 12\n" \
"s_nop 4\n" \
"buffer_load_dwordx4 a[96:99], v201, s[8:11], s43 offen sc1\n" \
"buffer_load_dwordx4 a[100:103], v201, s[8:11], s43 offen offset:1024 sc1\n" \
"v_readlane_b32 s43, v202, 13\n" \
"s_nop 4\n" \
"buffer_load_dwordx4 a[104:107], v201, s[8:11], s43 offen sc1\n" \
"buffer_load_dwordx4 a[108:111], v201, s[8:11], s43 offen offset:1024 sc1\n" \
"v_readlane_b32 s43, v202, 14\n" \
"s_nop 4\n" \
"buffer_load_dwordx4 a[112:115], v201, s[8:11], s43 offen sc1\n" \
"buffer_load_dwordx4 a[116:119], v201, s[8:11], s43 offen offset:1024 sc1\n" \
"v_readlane_b32 s43, v202, 15\n" \
"s_nop 4\n" \
"buffer_load_dwordx4 a[120:123], v201, s[8:11], s43 offen sc1\n" \
"buffer_load_dwordx4 a[124:127], v201, s[8:11], s43 offen offset:1024 sc1\n" \
"v_readlane_b32 s43, v202, 17\n" \
"s_nop 4\n" \
"buffer_load_dwordx4 a[136:139], v201, s[8:11], s43 offen sc1\n" \
"buffer_load_dwordx4 a[140:143], v201, s[8:11], s43 offen offset:1024 sc1\n" \
"v_readlane_b32 s43, v202, 20\n" \
"s_nop 4\n" \
"buffer_load_dwordx4 a[160:163], v201, s[8:11], s43 offen sc1\n" \
"buffer_load_dwordx4 a[164:167], v201, s[8:11], s43 offen offset:1024 sc1\n" \
"v_readlane_b32 s43, v202, 23\n" \
"s_nop 4\n" \
"buffer_load_dwordx4 a[184:187], v201, s[8:11], s43 offen sc1\n" \
"buffer_load_dwordx4 a[188:191], v201, s[8:11], s43 offen offset:1024 sc1\n" \
"v_readlane_b32 s43, v202, 26\n" \
"s_nop 4\n" \
"buffer_load_dwordx4 a[208:211], v201, s[8:11], s43 offen sc1\n" \
"buffer_load_dwordx4 a[212:215], v201, s[8:11], s43 offen offset:1024 sc1\n" \
"s_add_u32 s43, s30, s36\n" \
"s_mov_b32 s50, 0\n" \
"s_branch .LS2_w13c\n" \
".LS2_w13l:\n" \
"buffer_load_dword v240, v204, s[4:7], 0 offen sc1\n" \
"s_waitcnt vmcnt(0)\n" \
".LS2_w13c:\n" \
"v_subrev_u32 v241, s43, v240\n" \
"v_cmp_gt_u32 vcc, 0x100, v241\n" \
"s_cmp_eq_u64 vcc, -1\n" \
"s_cbranch_scc1 .LS2_w13d\n" \
"s_sleep 2\n" \
"s_add_u32 s50, s50, 1\n" \
"s_cmp_lt_u32 s50, 0x20000\n" \
"s_cbranch_scc1 .LS2_w13l\n" \
"s_branch .LS2_end\n" \
".LS2_w13d:\n" \
"v_readlane_b32 s43, v202, 0\n" \
"s_nop 4\n" \
"buffer_load_dwordx4 a[0:3], v201, s[8:11], s43 offen sc1\n" \
"buffer_load_dwordx4 a[4:7], v201, s[8:11], s43 offen offset:1024 sc1\n" \
"v_readlane_b32 s43, v202, 1\n" \
"s_nop 4\n" \
"buffer_load_dwordx4 a[8:11], v201, s[8:11], s43 offen sc1\n" \
"buffer_load_dwordx4 a[12:15], v201, s[8:11], s43 offen offset:1024 sc1\n" \
"v_readlane_b32 s43, v202, 2\n" \
"s_nop 4\n" \
"buffer_load_dwordx4 a[16:19], v201, s[8:11], s43 offen sc1\n" \
"buffer_load_dwordx4 a[20:23], v201, s[8:11], s43 offen offset:1024 sc1\n" \
"v_readlane_b32 s43, v202, 3\n" \
"s_nop 4\n" \
"buffer_load_dwordx4 a[24:27], v201, s[8:11], s43 offen sc1\n" \
"buffer_load_dwordx4 a[28:31], v201, s[8:11], s43 offen offset:1024 sc1\n" \
"v_readlane_b32 s43, v202, 4\n" \
"s_nop 4\n" \
"buffer_load_dwordx4 a[32:35], v201, s[8:11], s43 offen sc1\n" \
"buffer_load_dwordx4 a[36:39], v201, s[8:11], s43 offen offset:1024 sc1\n" \
"v_readlane_b32 s43, v202, 5\n" \
"s_nop 4\n" \
"buffer_load_dwordx4 a[40:43], v201, s[8:11], s43 offen sc1\n" \
"buffer_load_dwordx4 a[44:47], v201, s[8:11], s43 offen offset:1024 sc1\n" \
"v_readlane_b32 s43, v202, 6\n" \
"s_nop 4\n" \
"buffer_load_dwordx4 a[48:51], v201, s[8:11], s43 offen sc1\n" \
"buffer_load_dwordx4 a[52:55], v201, s[8:11], s43 offen offset:1024 sc1\n" \
"v_readlane_b32 s43, v202, 7\n" \
"s_nop 4\n" \
"buffer_load_dwordx4 a[56:59], v201, s[8:11], s43 offen sc1\n" \
"buffer_load_dwordx4 a[60:63], v201, s[8:11], s43 offen offset:1024 sc1\n" \
"s_add_u32 s43, s30, 7\n" \
"s_mov_b32 s50, 0\n" \
"s_branch .LS2_w14c\n" \
".LS2_w14l:\n" \
"buffer_load_dword v240, v204, s[4:7], 0 offen sc1\n" \
"s_waitcnt vmcnt(0)\n" \
".LS2_w14c:\n" \
"v_subrev_u32 v241, s43, v240\n" \
"v_cmp_gt_u32 vcc, 0x100, v241\n" \
"s_cmp_eq_u64 vcc, -1\n" \
"s_cbranch_scc1 .LS2_w14d\n" \
"s_sleep 2\n" \
"s_add_u32 s50, s50, 1\n" \
"s_cmp_lt_u32 s50, 0x20000\n" \
"s_cbranch_scc1 .LS2_w14l\n" \
"s_branch .LS2_end\n" \
".LS2_w14d:\n" \
"v_readlane_b32 s43, v202, 18\n" \
"s_nop 4\n" \
"buffer_load_dwordx4 a[144:147], v201, s[8:11], s43 offen sc1\n" \
"buffer_load_dwordx4 a[148:151], v201, s[8:11], s43 offen offset:1024 sc1\n" \
"v_readlane_b32 s43, v202, 21\n" \
"s_nop 4\n" \
"buffer_load_dwordx4 a[168:171], v201, s[8:11], s43 offen sc1\n" \
"buffer_load_dwordx4 a[172:175], v201, s[8:11], s43 offen offset:1024 sc1\n" \
"v_readlane_b32 s43, v202, 24\n" \
"s_nop 4\n" \
"buffer_load_dwordx4 a[192:195], v201, s[8:11], s43 offen sc1\n" \
"buffer_load_dwordx4 a[196:199], v201, s[8:11], s43 offen offset:1024 sc1\n" \
"v_readlane_b32 s43, v202, 27\n" \
"s_nop 4\n" \
"buffer_load_dwordx4 a[216:219], v201, s[8:11], s43 offen sc1\n" \
"buffer_load_dwordx4 a[220:223], v201, s[8:11], s43 offen offset:1024 sc1\n" \
"s_mov_b32 s33, -1\n" \
"ds_read_b96 v[248:250], v252\n" \
"s_waitcnt lgkmcnt(0)\n" \
"v_readfirstlane_b32 s44, v248\n" \
"v_readfirstlane_b32 s45, v249\n" \
"v_readfirstlane_b32 s46, v250\n" \
"s_mul_i32 s42, s25, s46\n" \
"s_lshr_b32 s42, s42, 12\n" \
"s_mov_b32 s26, s44\n" \
"s_mov_b32 s27, s45\n" \
".p2align 3\n" \
"s_memtime s[54:55]\n" \
"s_memrealtime s[56:57]\n" \
"s_memtime s[58:59]\n" \
"s_memrealtime s[60:61]\n" \
"s_nop 0\n" \
"s_nop 0\n" \
"s_memtime s[62:63]\n" \
"s_memrealtime s[64:65]\n" \
"s_memtime s[66:67]\n" \
"s_memrealtime s[68:69]\n" \
"s_memtime s[70:71]\n" \
"s_memrealtime s[72:73]\n" \
"s_memtime s[74:75]\n" \
"s_memrealtime s[76:77]\n" \
"s_nop 0\n" \
"s_nop 0\n" \
"s_memtime s[78:79]\n" \
"s_memrealtime s[80:81]\n" \
"s_memtime s[82:83]\n" \
"s_memrealtime s[84:85]\n" \
"s_memtime s[86:87]\n" \
"s_memrealtime s[88:89]\n" \
"s_memtime s[90:91]\n" \
"s_memrealtime s[92:93]\n" \
"s_nop 0\n" \
"s_nop 0\n" \
"s_memtime s[94:95]\n" \
"s_memrealtime s[96:97]\n" \
"s_memtime s[98:99]\n" \
"s_mov_b32 s52, 0\n" \
"s_mov_b32 s53, 4096\n" \
"s_waitcnt lgkmcnt(0)\n" \
"s_cmp_lg_u32 s96, s56\n" \
"s_cselect_b32 s52, s90, s52\n" \
"s_cselect_b32 s53, s98, s53\n" \
"s_cmp_lg_u32 s92, s56\n" \
"s_cselect_b32 s52, s86, s52\n" \
"s_cselect_b32 s53, s94, s53\n" \
"s_cmp_lg_u32 s88, s56\n" \
"s_cselect_b32 s52, s82, s52\n" \
"s_cselect_b32 s53, s90, s53\n" \
"s_cmp_lg_u32 s84, s56\n" \
"s_cselect_b32 s52, s78, s52\n" \
"s_cselect_b32 s53, s86, s53\n" \
"s_cmp_lg_u32 s80, s56\n" \
"s_cselect_b32 s52, s74, s52\n" \
"s_cselect_b32 s53, s82, s53\n" \
"s_cmp_lg_u32 s76, s56\n" \
"s_cselect_b32 s52, s70, s52\n" \
"s_cselect_b32 s53, s78, s53\n" \
"s_cmp_lg_u32 s72, s56\n" \
"s_cselect_b32 s52, s66, s52\n" \
"s_cselect_b32 s53, s74, s53\n" \
"s_cmp_lg_u32 s68, s56\n" \
"s_cselect_b32 s52, s62, s52\n" \
"s_cselect_b32 s53, s70, s53\n" \
"s_cmp_lg_u32 s64, s56\n" \
"s_cselect_b32 s52, s58, s52\n" \
"s_cselect_b32 s53, s66, s53\n" \
"s_cmp_lg_u32 s60, s56\n" \
"s_cselect_b32 s52, s54, s52\n" \
"s_cselect_b32 s53, s62, s53\n" \
"s_sub_u32 s53, s53, s52\n" \
"s_lshr_b32 s55, s53, 1\n" \
"s_add_u32 s52, s52, s55\n" \
"s_add_u32 s55, s56, 4\n" \
"s_lshl_b32 s55, s55, 4\n" \
"s_cmp_le_u32 s53, 40\n" \
"s_cselect_b32 s44, s55, s44\n" \
"s_cselect_b32 s45, s52, s45\n" \
"v_mov_b32 v248, s44\n" \
"v_mov_b32 v249, s45\n" \
"v_mov_b32 v250, s46\n" \
"s_mov_b64 exec, 1\n" \
"ds_write_b96 v252, v[248:250]\n" \
"s_mov_b64 exec, -1\n" \
"s_sub_u32 s26, s44, s26\n" \
"s_sub_u32 s27, s45, s27\n" \
"v_cvt_f32_u32 v248, s26\n" \
"v_cvt_f32_u32 v249, s27\n" \
"v_rcp_f32 v248, v248\n" \
"v_mul_f32 v249, 0x457ae148, v249\n" \
"v_mul_f32 v248, v248, v249\n" \
"v_cvt_u32_f32 v248, v248\n" \
"s_nop 0\n" \
"v_readfirstlane_b32 s27, v248\n" \
"s_max_u32 s27, s27, 0x800\n" \
"s_min_u32 s27, s27, 0x2000\n" \
"s_cmp_gt_u32 s26, 1600\n" \
"s_cselect_b32 s46, s27, s46\n" \
".p2align 3\n" \
"s_memtime s[54:55]\n" \
"s_memrealtime s[56:57]\n" \
"s_memtime s[58:59]\n" \
"s_memrealtime s[60:61]\n" \
"s_nop 0\n" \
"s_nop 0\n" \
"s_memtime s[62:63]\n" \
"s_memrealtime s[64:65]\n" \
"s_memtime s[66:67]\n" \
"s_memrealtime s[68:69]\n" \
"s_memtime s[70:71]\n" \
"s_memrealtime s[72:73]\n" \
"s_memtime s[74:75]\n" \
"s_memrealtime s[76:77]\n" \
"s_nop 0\n" \
"s_nop 0\n" \
"s_memtime s[78:79]\n" \
"s_memrealtime s[80:81]\n" \
"s_memtime s[82:83]\n" \
"s_memrealtime s[84:85]\n" \
"s_memtime s[86:87]\n" \
"s_memrealtime s[88:89]\n" \
"s_memtime s[90:91]\n" \
"s_memrealtime s[92:93]\n" \
"s_nop 0\n" \
"s_nop 0\n" \
"s_memtime s[94:95]\n" \
"s_memrealtime s[96:97]\n" \
"s_memtime s[98:99]\n" \
"s_mov_b32 s52, 0\n" \
"s_mov_b32 s53, 4096\n" \
"s_waitcnt lgkmcnt(0)\n" \
"s_cmp_lg_u32 s96, s56\n" \
"s_cselect_b32 s52, s90, s52\n" \
"s_cselect_b32 s53, s98, s53\n" \
"s_cmp_lg_u32 s92, s56\n" \
"s_cselect_b32 s52, s86, s52\n" \
"s_cselect_b32 s53, s94, s53\n" \
"s_cmp_lg_u32 s88, s56\n" \
"s_cselect_b32 s52, s82, s52\n" \
"s_cselect_b32 s53, s90, s53\n" \
"s_cmp_lg_u32 s84, s56\n" \
"s_cselect_b32 s52, s78, s52\n" \
"s_cselect_b32 s53, s86, s53\n" \
"s_cmp_lg_u32 s80, s56\n" \
"s_cselect_b32 s52, s74, s52\n" \
"s_cselect_b32 s53, s82, s53\n" \
"s_cmp_lg_u32 s76, s56\n" \
"s_cselect_b32 s52, s70, s52\n" \
"s_cselect_b32 s53, s78, s53\n" \
"s_cmp_lg_u32 s72, s56\n" \
"s_cselect_b32 s52, s66, s52\n" \
"s_cselect_b32 s53, s74, s53\n" \
"s_cmp_lg_u32 s68, s56\n" \
"s_cselect_b32 s52, s62, s52\n" \
"s_cselect_b32 s53, s70, s53\n" \
"s_cmp_lg_u32 s64, s56\n" \
"s_cselect_b32 s52, s58, s52\n" \
"s_cselect_b32 s53, s66, s53\n" \
"s_cmp_lg_u32 s60, s56\n" \
"s_cselect_b32 s52, s54, s52\n" \
"s_cselect_b32 s53, s62, s53\n" \
"s_sub_u32 s53, s53, s52\n" \
"s_lshr_b32 s55, s53, 1\n" \
"s_add_u32 s52, s52, s55\n" \
"s_add_u32 s55, s56, 4\n" \
"s_lshl_b32 s55, s55, 4\n" \
"s_cmp_le_u32 s53, 40\n" \
"s_cselect_b32 s44, s55, s44\n" \
"s_cselect_b32 s45, s52, s45\n" \
"v_mov_b32 v248, s44\n" \
"v_mov_b32 v249, s45\n" \
"v_mov_b32 v250, s46\n" \
"s_mov_b64 exec, 1\n" \
"ds_write_b96 v252, v[248:250]\n" \
"s_mov_b64 exec, -1\n" \
"s_waitcnt lgkmcnt(0)\n" \
"s_add_u32 s43, s30, 1\n" \
"s_mov_b32 s50, 0\n" \
".LS2_w15l:\n" \
"buffer_load_dword v240, v205, s[4:7], 0 offen sc1\n" \
"s_waitcnt vmcnt(0)\n" \
".LS2_w15c:\n" \
"v_subrev_u32 v241, s43, v240\n" \
"v_cmp_gt_u32 vcc, 0x100, v241\n" \
"s_cmp_eq_u64 vcc, -1\n" \
"s_cbranch_scc1 .LS2_w15d\n" \
"s_sleep 2\n" \
"s_add_u32 s50, s50, 1\n" \
"s_cmp_lt_u32 s50, 0x20000\n" \
"s_cbranch_scc1 .LS2_w15l\n" \
"s_branch .LS2_end\n" \
".LS2_w15d:\n" \
"v_readfirstlane_b32 s31, v241\n" \
"s_lshr_b32 s32, s31, 2\n" \
"s_xor_b32 s29, s31, s32\n" \
"s_and_b32 s29, s29, 3\n" \
"s_lshl_b32 s29, s29, 13\n" \
"s_bitset1_b32 s29, 21\n" \
"s_and_b32 s31, s31, 3\n" \
"s_lshl_b32 s31, s31, 13\n" \
"s_bitcmp1_b32 s39, 0\n" \
"s_cselect_b32 s43, s29, 0\n" \
"s_xor_b32 s31, s31, s43\n" \
"s_xor_b32 s32, s31, 0x4000\n" \
"s_mov_b32 s39, 0\n" \
".LS2_pre:\n" \
"s_barrier\n" \
"ds_read_b96 v[248:250], v252\n" \
"s_waitcnt lgkmcnt(0)\n" \
"v_readfirstlane_b32 s44, v248\n" \
"v_readfirstlane_b32 s45, v249\n" \
"v_readfirstlane_b32 s46, v250\n" \
"s_mul_i32 s42, s25, s46\n" \
"s_lshr_b32 s42, s42, 12\n" \
".LS2_cu:\n" \
"s_sub_u32 s43, s24, s44\n" \
"s_cmp_lt_i32 s43, 0\n" \
"s_cbranch_scc0 .LS2_cud\n" \
"s_add_u32 s24, s24, s25\n" \
"s_xor_b32 s31, s31, s29\n" \
"s_xor_b32 s32, s32, s29\n" \
"s_xor_b32 s47, s47, s29\n" \
"s_add_u32 s39, s39, 1\n" \
"s_branch .LS2_cu\n" \
".LS2_cud:\n" \
".LS2_loop:\n" \
"s_add_u32 s43, s40, 0\n" \
"s_and_b32 s43, s43, 3\n" \
"s_bitcmp1_b32 s43, 0\n" \
"s_cselect_b32 s47, s32, s31\n" \
"s_lshl_b32 s43, s43, 19\n" \
"s_add_u32 s47, s47, s43\n" \
"s_sub_u32 s43, s24, s44\n" \
"s_max_i32 s43, s43, 0xfffd0000\n" \
"s_min_i32 s43, s43, 0x4000\n" \
"s_mul_i32 s43, s43, s46\n" \
"s_ashr_i32 s43, s43, 12\n" \
"s_add_u32 s41, s45, s43\n" \
"s_add_u32 s41, s41, 8\n" \
"s_branch .LS2_a1\n" \
".LS2_retry:\n" \
"s_add_u32 s51, s51, 1\n" \
"s_cmp_gt_u32 s51, 0x20000\n" \
"s_cbranch_scc1 .LS2_end\n" \
".LS2_a0:\n" \
"s_add_u32 s41, s41, s42\n" \
"s_add_u32 s24, s24, s25\n" \
"s_xor_b32 s31, s31, s29\n" \
"s_xor_b32 s32, s32, s29\n" \
"s_xor_b32 s47, s47, s29\n" \
"s_xor_b32 s48, s48, s29\n" \
"s_add_u32 s39, s39, 1\n" \
".p2align 6\n" \
".LS2_a1:\n" \
"s_memtime vcc\n" \
"s_waitcnt lgkmcnt(0)\n" \
"s_sub_u32 s43, s41, vcc_lo\n" \
"s_cmp_gt_i32 s43, 128\n" \
"s_cbranch_scc1 .LS2_a1\n" \
"s_cmp_lt_i32 s43, s37\n" \
"s_cbranch_scc1 .LS2_a0\n" \
"s_max_i32 s43, s43, 0\n" \
"s_and_b32 s43, s43, -4\n" \
"s_getpc_b64 vcc\n" \
".LS2_a3:\n" \
"s_sub_u32 s43, .LS2_a4-.LS2_a3, s43\n" \
"s_add_u32 vcc_lo, vcc_lo, s43\n" \
"s_addc_u32 vcc_hi, vcc_hi, 0\n" \
"s_setpc_b64 vcc\n" \
".rept 32\n" \
"s_nop 0\n" \
".endr\n" \
".LS2_a4:\n" \
"buffer_load_dwordx4 v[0:3], v208, s[4:7], s47 offen offset:2048 sc1\n" \
"s_nop 9\n" \
"buffer_load_dwordx4 v[4:7], v208, s[4:7], s47 offen sc1\n" \
"s_nop 9\n" \
"buffer_load_dwordx4 v[8:11], v208, s[4:7], s47 offen offset:1024 sc1\n" \
"s_nop 9\n" \
"buffer_load_dwordx4 v[12:15], v210, s[4:7], s47 offen sc1\n" \
"s_add_u32 s35, s35, 1\n" \
"v_mov_b32 v28, 0\n" \
"v_mov_b32 v29, 0\n" \
"v_mov_b32 v30, 0\n" \
"v_mov_b32 v31, 0\n" \
"v_mov_b32 v32, 0\n" \
"v_mov_b32 v33, 0\n" \
"v_mov_b32 v34, 0\n" \
"v_mov_b32 v35, 0\n" \
"v_mov_b32 v36, 0\n" \
"v_mov_b32 v37, 0\n" \
"v_mov_b32 v38, 0\n" \
"v_mov_b32 v39, 0\n" \
"v_mov_b32 v40, 0\n" \
"v_mov_b32 v41, 0\n" \
"v_mov_b32 v42, 0\n" \
"v_mov_b32 v43, 0\n" \
"v_mov_b32 v44, 0\n" \
"v_mov_b32 v45, 0\n" \
"v_mov_b32 v46, 0\n" \
"v_mov_b32 v47, 0\n" \
"v_mov_b32 v48, 0\n" \
"v_mov_b32 v49, 0\n" \
"v_mov_b32 v50, 0\n" \
"v_mov_b32 v51, 0\n" \
"v_mov_b32 v52, 0\n" \
"v_mov_b32 v53, 0\n" \
"v_mov_b32 v54, 0\n" \
"v_mov_b32 v55, 0\n" \
"v_mov_b32 v56, 0\n" \
"v_mov_b32 v57, 0\n" \
"v_mov_b32 v58, 0\n" \
"v_mov_b32 v59, 0\n" \
"s_waitcnt vmcnt(3)\n" \
"v_smfmac_f32_16x16x64_bf16 v[28:31], v[0:3], a[0:7], v200\n" \
"v_smfmac_f32_16x16x64_bf16 v[32:35], v[0:3], a[8:15], v200\n" \
"v_smfmac_f32_16x16x64_bf16 v[36:39], v[0:3], a[16:23], v200\n" \
"v_smfmac_f32_16x16x64_bf16 v[40:43], v[0:3], a[24:31], v200\n" \
"v_smfmac_f32_16x16x64_bf16 v[44:47], v[0:3], a[32:39], v200\n" \
"v_smfmac_f32_16x16x64_bf16 v[48:51], v[0:3], a[40:47], v200\n" \
"v_smfmac_f32_16x16x64_bf16 v[52:55], v[0:3], a[48:55], v200\n" \
"v_smfmac_f32_16x16x64_bf16 v[56:59], v[0:3], a[56:63], v200\n" \
"s_waitcnt vmcnt(2)\n" \
"v_smfmac_f32_16x16x64_bf16 v[28:31], v[4:7], a[64:71], v200\n" \
"v_smfmac_f32_16x16x64_bf16 v[32:35], v[4:7], a[72:79], v200\n" \
"v_smfmac_f32_16x16x64_bf16 v[36:39], v[4:7], a[80:87], v200\n" \
"v_smfmac_f32_16x16x64_bf16 v[40:43], v[4:7], a[88:95], v200\n" \
"s_waitcnt vmcnt(1)\n" \
"v_smfmac_f32_16x16x64_bf16 v[28:31], v[8:11], a[96:103], v200\n" \
"s_nop 2\n" \
"v_smfmac_f32_16x16x64_bf16 v[32:35], v[8:11], a[104:111], v200\n" \
"s_nop 2\n" \
"v_smfmac_f32_16x16x64_bf16 v[36:39], v[8:11], a[112:119], v200\n" \
"s_nop 0\n" \
"v_cvt_pk_bf16_f32 v128, v28, v29\n" \
"v_cvt_pk_bf16_f32 v129, v30, v31\n" \
"v_smfmac_f32_16x16x64_bf16 v[40:43], v[8:11], a[120:127], v200\n" \
"ds_write_b64 v211, v[128:129] offset:256\n" \
"v_cvt_pk_bf16_f32 v130, v32, v33\n" \
"v_cvt_pk_bf16_f32 v131, v34, v35\n" \
"v_smfmac_f32_16x16x64_bf16 v[44:47], v[4:7], a[128:135], v200\n" \
"ds_write_b64 v211, v[130:131] offset:2432\n" \
"v_cvt_pk_bf16_f32 v132, v36, v37\n" \
"v_cvt_pk_bf16_f32 v133, v38, v39\n" \
"v_smfmac_f32_16x16x64_bf16 v[44:47], v[8:11], a[136:143], v200\n" \
"s_waitcnt vmcnt(0)\n" \
"ds_write_b64 v211, v[132:133] offset:4608\n" \
"v_cvt_pk_bf16_f32 v134, v40, v41\n" \
"v_cvt_pk_bf16_f32 v135, v42, v43\n" \
"v_smfmac_f32_16x16x64_bf16 v[44:47], v[12:15], a[144:151], v200\n" \
"ds_write_b64 v211, v[134:135] offset:4992\n" \
"s_nop 1\n" \
"v_smfmac_f32_16x16x64_bf16 v[48:51], v[4:7], a[152:159], v200\n" \
"s_nop 2\n" \
"v_smfmac_f32_16x16x64_bf16 v[48:51], v[8:11], a[160:167], v200\n" \
"s_nop 0\n" \
"v_cvt_pk_bf16_f32 v136, v44, v45\n" \
"v_cvt_pk_bf16_f32 v137, v46, v47\n" \
"v_smfmac_f32_16x16x64_bf16 v[48:51], v[12:15], a[168:175], v200\n" \
"ds_write_b64 v211, v[136:137] offset:9472\n" \
"s_nop 1\n" \
"v_smfmac_f32_16x16x64_bf16 v[52:55], v[4:7], a[176:183], v200\n" \
"s_waitcnt lgkmcnt(1)\n" \
"s_barrier\n" \
"s_nop 0\n" \
"v_smfmac_f32_16x16x64_bf16 v[52:55], v[8:11], a[184:191], v200\n" \
"s_nop 0\n" \
"v_cvt_pk_bf16_f32 v138, v48, v49\n" \
"v_cvt_pk_bf16_f32 v139, v50, v51\n" \
"v_smfmac_f32_16x16x64_bf16 v[52:55], v[12:15], a[192:199], v200\n" \
"ds_write_b64 v211, v[138:139] offset:11648\n" \
"s_nop 1\n" \
"v_smfmac_f32_16x16x64_bf16 v[56:59], v[4:7], a[200:207], v200\n" \
"s_nop 2\n" \
"v_smfmac_f32_16x16x64_bf16 v[56:59], v[8:11], a[208:215], v200\n" \
"s_nop 2\n" \
"v_smfmac_f32_16x16x64_bf16 v[56:59], v[12:15], a[216:223], v200\n" \
"v_cvt_pk_bf16_f32 v140, v52, v53\n" \
"v_cvt_pk_bf16_f32 v141, v54, v55\n" \
"s_nop 1\n" \
"ds_write_b64 v211, v[140:141] offset:13824\n" \
"s_nop 2\n" \
"v_cvt_pk_bf16_f32 v142, v56, v57\n" \
"v_cvt_pk_bf16_f32 v143, v58, v59\n" \
"ds_write_b64 v211, v[142:143] offset:14208\n" \
"s_waitcnt lgkmcnt(1)\n" \
"s_barrier\n" \
"s_waitcnt lgkmcnt(0)\n" \
"s_barrier\n" \
"s_nop 3\n" \
"ds_read_b32 v64, v252 offset:20\n" \
"s_waitcnt lgkmcnt(0)\n" \
"v_readfirstlane_b32 s43, v64\n" \
"s_cmp_eq_u32 s43, s35\n" \
"s_cbranch_scc1 .LS2_retry\n" \
".p2align 3\n" \
"s_memtime s[54:55]\n" \
"s_memrealtime s[56:57]\n" \
"s_memtime s[58:59]\n" \
"s_memrealtime s[60:61]\n" \
"s_nop 0\n" \
"s_nop 0\n" \
"s_memtime s[62:63]\n" \
"s_memrealtime s[64:65]\n" \
"s_memtime s[66:67]\n" \
"s_memrealtime s[68:69]\n" \
"s_memtime s[70:71]\n" \
"s_memrealtime s[72:73]\n" \
"s_memtime s[74:75]\n" \
"s_memrealtime s[76:77]\n" \
"s_nop 0\n" \
"s_nop 0\n" \
"s_memtime s[78:79]\n" \
"s_memrealtime s[80:81]\n" \
"s_memtime s[82:83]\n" \
"s_memrealtime s[84:85]\n" \
"s_memtime s[86:87]\n" \
"s_memrealtime s[88:89]\n" \
"s_memtime s[90:91]\n" \
"s_memrealtime s[92:93]\n" \
"s_nop 0\n" \
"s_nop 0\n" \
"s_memtime s[94:95]\n" \
"s_memrealtime s[96:97]\n" \
"s_memtime s[98:99]\n" \
"s_mov_b32 s52, 0\n" \
"s_mov_b32 s53, 4096\n" \
"s_waitcnt lgkmcnt(0)\n" \
"s_cmp_lg_u32 s96, s56\n" \
"s_cselect_b32 s52, s90, s52\n" \
"s_cselect_b32 s53, s98, s53\n" \
"s_cmp_lg_u32 s92, s56\n" \
"s_cselect_b32 s52, s86, s52\n" \
"s_cselect_b32 s53, s94, s53\n" \
"s_cmp_lg_u32 s88, s56\n" \
"s_cselect_b32 s52, s82, s52\n" \
"s_cselect_b32 s53, s90, s53\n" \
"s_cmp_lg_u32 s84, s56\n" \
"s_cselect_b32 s52, s78, s52\n" \
"s_cselect_b32 s53, s86, s53\n" \
"s_cmp_lg_u32 s80, s56\n" \
"s_cselect_b32 s52, s74, s52\n" \
"s_cselect_b32 s53, s82, s53\n" \
"s_cmp_lg_u32 s76, s56\n" \
"s_cselect_b32 s52, s70, s52\n" \
"s_cselect_b32 s53, s78, s53\n" \
"s_cmp_lg_u32 s72, s56\n" \
"s_cselect_b32 s52, s66, s52\n" \
"s_cselect_b32 s53, s74, s53\n" \
"s_cmp_lg_u32 s68, s56\n" \
"s_cselect_b32 s52, s62, s52\n" \
"s_cselect_b32 s53, s70, s53\n" \
"s_cmp_lg_u32 s64, s56\n" \
"s_cselect_b32 s52, s58, s52\n" \
"s_cselect_b32 s53, s66, s53\n" \
"s_cmp_lg_u32 s60, s56\n" \
"s_cselect_b32 s52, s54, s52\n" \
"s_cselect_b32 s53, s62, s53\n" \
"s_sub_u32 s53, s53, s52\n" \
"s_lshr_b32 s55, s53, 1\n" \
"s_add_u32 s52, s52, s55\n" \
"s_add_u32 s55, s56, 4\n" \
"s_lshl_b32 s55, s55, 4\n" \
"s_sub_u32 s59, s55, s44\n" \
"s_cmp_lt_u32 s59, 2600\n" \
"s_cselect_b32 s63, 1, 2\n" \
"s_mul_i32 s59, s59, s46\n" \
"s_ashr_i32 s59, s59, 12\n" \
"s_add_u32 s59, s59, s45\n" \
"s_sub_u32 s59, s52, s59\n" \
"s_max_i32 s59, s59, -96\n" \
"s_min_i32 s59, s59, 96\n" \
"s_ashr_i32 s63, s59, s63\n" \
"s_add_u32 s63, s63, s46\n" \
"s_max_i32 s63, s63, 0x800\n" \
"s_min_i32 s63, s63, 0x2000\n" \
"s_cmp_le_u32 s53, 40\n" \
"s_cselect_b32 s44, s55, s44\n" \
"s_cselect_b32 s45, s52, s45\n" \
"s_cselect_b32 s46, s63, s46\n" \
"v_mov_b32 v248, s44\n" \
"v_mov_b32 v249, s45\n" \
"v_mov_b32 v250, s46\n" \
"s_mov_b64 exec, 1\n" \
"ds_write_b96 v252, v[248:250]\n" \
"s_mov_b64 exec, -1\n" \
"v_max3_u32 v64, v0, v0, v1\n" \
"v_max3_u32 v64, v64, v2, v3\n" \
"v_max3_u32 v64, v64, v4, v5\n" \
"v_max3_u32 v64, v64, v6, v7\n" \
"v_max3_u32 v64, v64, v8, v9\n" \
"v_max3_u32 v64, v64, v10, v11\n" \
"v_max3_u32 v64, v64, v12, v13\n" \
"v_max3_u32 v64, v64, v14, v15\n" \
"v_cmp_eq_u32 vcc, -1, v64\n" \
"s_cbranch_vccnz .LS2_veto\n" \
".LS2_back:\n" \
"s_waitcnt lgkmcnt(0)\n" \
"s_barrier\n" \
"ds_read_b32 v64, v252 offset:16\n" \
"ds_read_b64 v[246:247], v252 offset:24\n" \
"ds_read_b96 v[248:250], v252\n" \
"s_waitcnt lgkmcnt(0)\n" \
"v_readfirstlane_b32 s44, v248\n" \
"v_readfirstlane_b32 s45, v249\n" \
"v_readfirstlane_b32 s46, v250\n" \
"v_readfirstlane_b32 s26, v246\n" \
"v_readfirstlane_b32 s27, v247\n" \
"s_mul_i32 s42, s25, s46\n" \
"s_lshr_b32 s42, s42, 12\n" \
"v_readfirstlane_b32 s43, v64\n" \
"s_cmp_eq_u32 s43, s35\n" \
"s_cbranch_scc1 .LS2_retry\n" \
"s_add_u32 s40, s40, 1\n" \
"s_add_u32 s24, s24, s26\n" \
"s_add_u32 s24, s24, s27\n" \
"s_cmp_eq_u32 s40, 0\n" \
"s_cbranch_scc1 .LS2_wet\n" \
"s_cmp_lt_u32 s40, 63\n" \
"s_cbranch_scc1 .LS2_loop\n" \
".LS2_end:\n" \
"s_endpgm\n" \
".LS2_veto:\n" \
"v_mov_b32 v64, s35\n" \
"ds_write_b32 v252, v64 offset:16\n" \
"s_branch .LS2_back\n" \
".p2align 6\n" \
"fk_role_S3:\n" \
"ds_read_b32 v200, v255 offset:0\n" \
"ds_read_b32 v201, v255 offset:256\n" \
"ds_read_b32 v202, v255 offset:512\n" \
"ds_read_b32 v203, v255 offset:768\n" \
"ds_read_b32 v204, v255 offset:1024\n" \
"ds_read_b32 v205, v255 offset:1280\n" \
"ds_read_b32 v206, v255 offset:1536\n" \
"ds_read_b32 v207, v255 offset:1792\n" \
"ds_read_b32 v208, v255 offset:2048\n" \
"ds_read_b32 v209, v255 offset:2304\n" \
"ds_read_b32 v210, v255 offset:2560\n" \
"ds_read_b32 v211, v255 offset:2816\n" \
"ds_read_b32 v212, v255 offset:3072\n" \
"ds_read_b32 v213, v255 offset:3328\n" \
"ds_read_b32 v214, v255 offset:3584\n" \
"ds_read_b32 v215, v255 offset:3840\n" \
"ds_read_b32 v254, v255 offset:7936\n" \
"s_waitcnt lgkmcnt(0)\n" \
"v_readlane_b32 s4, v254, 0\n" \
"v_readlane_b32 s5, v254, 1\n" \
"v_readlane_b32 s6, v254, 2\n" \
"v_readlane_b32 s7, v254, 3\n" \
"v_readlane_b32 s8, v254, 4\n" \
"v_readlane_b32 s9, v254, 5\n" \
"v_readlane_b32 s10, v254, 6\n" \
"v_readlane_b32 s11, v254, 7\n" \
"v_readlane_b32 s12, v254, 8\n" \
"v_readlane_b32 s13, v254, 9\n" \
"v_readlane_b32 s14, v254, 10\n" \
"v_readlane_b32 s15, v254, 11\n" \
"v_readlane_b32 s16, v254, 12\n" \
"v_readlane_b32 s17, v254, 13\n" \
"v_readlane_b32 s18, v254, 14\n" \
"v_readlane_b32 s19, v254, 15\n" \
"v_readlane_b32 s20, v254, 16\n" \
"v_readlane_b32 s21, v254, 17\n" \
"v_readlane_b32 s22, v254, 18\n" \
"v_readlane_b32 s23, v254, 19\n" \
"v_readlane_b32 s24, v254, 20\n" \
"v_readlane_b32 s25, v254, 21\n" \
"v_readlane_b32 s26, v254, 22\n" \
"v_readlane_b32 s27, v254, 23\n" \
"v_readlane_b32 s28, v254, 24\n" \
"v_readlane_b32 s29, v254, 25\n" \
"v_readlane_b32 s30, v254, 26\n" \
"v_readlane_b32 s31, v254, 27\n" \
"v_readlane_b32 s32, v254, 28\n" \
"v_readlane_b32 s33, v254, 29\n" \
"v_readlane_b32 s34, v254, 30\n" \
"v_readlane_b32 s35, v254, 31\n" \
"v_readlane_b32 s36, v254, 32\n" \
"v_readlane_b32 s37, v254, 33\n" \
"v_readlane_b32 s38, v254, 34\n" \
"v_readlane_b32 s39, v254, 35\n" \
"s_mov_b32 s40, 0\n" \
"s_mov_b32 s51, 0\n" \
"s_nop 0\n" \
"v_mov_b32 v252, s38\n" \
"s_nop 4\n" \
"v_mov_b32 v188, -1\n" \
"s_mov_b32 s28, s33\n" \
"s_mov_b32 s33, -1\n" \
"v_writelane_b32 v251, s6, 0\n" \
"v_writelane_b32 v251, s10, 1\n" \
"v_writelane_b32 v251, s14, 2\n" \
"v_writelane_b32 v251, s18, 3\n" \
"s_mov_b32 s6, 0\n" \
"s_mov_b32 s10, 0\n" \
"s_mov_b32 s14, 0\n" \
"s_mov_b32 s18, 0\n" \
"s_mov_b32 s40, -1\n" \
"s_branch .LS3_pre\n" \
".LS3_wet:\n" \
"s_waitcnt vmcnt(0)\n" \
"v_readlane_b32 s6, v251, 0\n" \
"v_readlane_b32 s10, v251, 1\n" \
"v_readlane_b32 s14, v251, 2\n" \
"v_readlane_b32 s18, v251, 3\n" \
"s_nop 4\n" \
"s_mov_b32 s33, s28\n" \
"s_add_u32 s34, s33, 2\n" \
"s_add_u32 s36, s33, 4\n" \
"s_add_u32 s43, s30, s33\n" \
"s_mov_b32 s50, 0\n" \
".LS3_w16l:\n" \
"buffer_load_dword v240, v204, s[4:7], 0 offen sc1\n" \
"s_waitcnt vmcnt(0)\n" \
".LS3_w16c:\n" \
"v_subrev_u32 v241, s43, v240\n" \
"v_cmp_gt_u32 vcc, 0x100, v241\n" \
"s_cmp_eq_u64 vcc, -1\n" \
"s_cbranch_scc1 .LS3_w16d\n" \
"s_sleep 2\n" \
"s_add_u32 s50, s50, 1\n" \
"s_cmp_lt_u32 s50, 0x20000\n" \
"s_cbranch_scc1 .LS3_w16l\n" \
"s_branch .LS3_end\n" \
".LS3_w16d:\n" \
"v_readlane_b32 s43, v202, 2\n" \
"s_nop 4\n" \
"buffer_load_dwordx4 a[16:19], v201, s[8:11], s43 offen sc1\n" \
"buffer_load_dwordx4 a[20:23], v201, s[8:11], s43 offen offset:1024 sc1\n" \
"v_readlane_b32 s43, v202, 3\n" \
"s_nop 4\n" \
"buffer_load_dwordx4 a[24:27], v201, s[8:11], s43 offen sc1\n" \
"buffer_load_dwordx4 a[28:31], v201, s[8:11], s43 offen offset:1024 sc1\n" \
"v_readlane_b32 s43, v202, 4\n" \
"s_nop 4\n" \
"buffer_load_dwordx4 a[32:35], v201, s[8:11], s43 offen sc1\n" \
"buffer_load_dwordx4 a[36:39], v201, s[8:11], s43 offen offset:1024 sc1\n" \
"v_readlane_b32 s43, v202, 5\n" \
"s_nop 4\n" \
"buffer_load_dwordx4 a[40:43], v201, s[8:11], s43 offen sc1\n" \
"buffer_load_dwordx4 a[44:47], v201, s[8:11], s43 offen offset:1024 sc1\n" \
"s_add_u32 s43, s30, s34\n" \
"s_mov_b32 s50, 0\n" \
"s_branch .LS3_w17c\n" \
".LS3_w17l:\n" \
"buffer_load_dword v240, v204, s[4:7], 0 offen sc1\n" \
"s_waitcnt vmcnt(0)\n" \
".LS3_w17c:\n" \
"v_subrev_u32 v241, s43, v240\n" \
"v_cmp_gt_u32 vcc, 0x100, v241\n" \
"s_cmp_eq_u64 vcc, -1\n" \
"s_cbranch_scc1 .LS3_w17d\n" \
"s_sleep 2\n" \
"s_add_u32 s50, s50, 1\n" \
"s_cmp_lt_u32 s50, 0x20000\n" \
"s_cbranch_scc1 .LS3_w17l\n" \
"s_branch .LS3_end\n" \
".LS3_w17d:\n" \
"v_readlane_b32 s43, v202, 6\n" \
"s_nop 4\n" \
"buffer_load_dwordx4 a[48:51], v201, s[8:11], s43 offen sc1\n" \
"buffer_load_dwordx4 a[52:55], v201, s[8:11], s43 offen offset:1024 sc1\n" \
"v_readlane_b32 s43, v202, 7\n" \
"s_nop 4\n" \
"buffer_load_dwordx4 a[56:59], v201, s[8:11], s43 offen sc1\n" \
"buffer_load_dwordx4 a[60:63], v201, s[8:11], s43 offen offset:1024 sc1\n" \
"v_readlane_b32 s43, v202, 8\n" \
"s_nop 4\n" \
"buffer_load_dwordx4 a[64:67], v201, s[8:11], s43 offen sc1\n" \
"buffer_load_dwordx4 a[68:71], v201, s[8:11], s43 offen offset:1024 sc1\n" \
"v_readlane_b32 s43, v202, 9\n" \
"s_nop 4\n" \
"buffer_load_dwordx4 a[72:75], v201, s[8:11], s43 offen sc1\n" \
"buffer_load_dwordx4 a[76:79], v201, s[8:11], s43 offen offset:1024 sc1\n" \
"s_add_u32 s43, s30, s36\n" \
"s_mov_b32 s50, 0\n" \
"s_branch .LS3_w18c\n" \
".LS3_w18l:\n" \
"buffer_load_dword v240, v204, s[4:7], 0 offen sc1\n" \
"s_waitcnt vmcnt(0)\n" \
".LS3_w18c:\n" \
"v_subrev_u32 v241, s43, v240\n" \
"v_cmp_gt_u32 vcc, 0x100, v241\n" \
"s_cmp_eq_u64 vcc, -1\n" \
"s_cbranch_scc1 .LS3_w18d\n" \
"s_sleep 2\n" \
"s_add_u32 s50, s50, 1\n" \
"s_cmp_lt_u32 s50, 0x20000\n" \
"s_cbranch_scc1 .LS3_w18l\n" \
"s_branch .LS3_end\n" \
".LS3_w18d:\n" \
"v_readlane_b32 s43, v202, 10\n" \
"s_nop 4\n" \
"buffer_load_dwordx4 a[80:83], v201, s[8:11], s43 offen sc1\n" \
"buffer_load_dwordx4 a[84:87], v201, s[8:11], s43 offen offset:1024 sc1\n" \
"v_readlane_b32 s43, v202, 11\n" \
"s_nop 4\n" \
"buffer_load_dwordx4 a[88:91], v201, s[8:11], s43 offen sc1\n" \
"buffer_load_dwordx4 a[92:95], v201, s[8:11], s43 offen offset:1024 sc1\n" \
"v_readlane_b32 s43, v202, 12\n" \
"s_nop 4\n" \
"buffer_load_dwordx4 a[96:99], v201, s[8:11], s43 offen sc1\n" \
"buffer_load_dwordx4 a[100:103], v201, s[8:11], s43 offen offset:1024 sc1\n" \
"v_readlane_b32 s43, v202, 13\n" \
"s_nop 4\n" \
"buffer_load_dwordx4 a[104:107], v201, s[8:11], s43 offen sc1\n" \
"buffer_load_dwordx4 a[108:111], v201, s[8:11], s43 offen offset:1024 sc1\n" \
"s_add_u32 s43, s30, 7\n" \
"s_mov_b32 s50, 0\n" \
"s_branch .LS3_w19c\n" \
".LS3_w19l:\n" \
"buffer_load_dword v240, v204, s[4:7], 0 offen sc1\n" \
"s_waitcnt vmcnt(0)\n" \
".LS3_w19c:\n" \
"v_subrev_u32 v241, s43, v240\n" \
"v_cmp_gt_u32 vcc, 0x100, v241\n" \
"s_cmp_eq_u64 vcc, -1\n" \
"s_cbranch_scc1 .LS3_w19d\n" \
"s_sleep 2\n" \
"s_add_u32 s50, s50, 1\n" \
"s_cmp_lt_u32 s50, 0x20000\n" \
"s_cbranch_scc1 .LS3_w19l\n" \
"s_branch .LS3_end\n" \
".LS3_w19d:\n" \
"v_readlane_b32 s43, v202, 0\n" \
"s_nop 4\n" \
"buffer_load_dwordx4 a[0:3], v201, s[8:11], s43 offen sc1\n" \
"buffer_load_dwordx4 a[4:7], v201, s[8:11], s43 offen offset:1024 sc1\n" \
"v_readlane_b32 s43, v202, 1\n" \
"s_nop 4\n" \
"buffer_load_dwordx4 a[8:11], v201, s[8:11], s43 offen sc1\n" \
"buffer_load_dwordx4 a[12:15], v201, s[8:11], s43 offen offset:1024 sc1\n" \
"s_mov_b32 s33, -1\n" \
"s_add_u32 s43, s30, 1\n" \
"s_mov_b32 s50, 0\n" \
".LS3_w20l:\n" \
"buffer_load_dword v240, v205, s[4:7], 0 offen sc1\n" \
"s_waitcnt vmcnt(0)\n" \
".LS3_w20c:\n" \
"v_subrev_u32 v241, s43, v240\n" \
"v_cmp_gt_u32 vcc, 0x100, v241\n" \
"s_cmp_eq_u64 vcc, -1\n" \
"s_cbranch_scc1 .LS3_w20d\n" \
"s_sleep 2\n" \
"s_add_u32 s50, s50, 1\n" \
"s_cmp_lt_u32 s50, 0x20000\n" \
"s_cbranch_scc1 .LS3_w20l\n" \
"s_branch .LS3_end\n" \
".LS3_w20d:\n" \
"v_readfirstlane_b32 s31, v241\n" \
"s_lshr_b32 s32, s31, 2\n" \
"s_xor_b32 s29, s31, s32\n" \
"s_and_b32 s29, s29, 3\n" \
"s_lshl_b32 s29, s29, 13\n" \
"s_bitset1_b32 s29, 21\n" \
"s_and_b32 s31, s31, 3\n" \
"s_lshl_b32 s31, s31, 13\n" \
"s_bitcmp1_b32 s39, 0\n" \
"s_cselect_b32 s43, s29, 0\n" \
"s_xor_b32 s31, s31, s43\n" \
"s_xor_b32 s32, s31, 0x4000\n" \
"s_mov_b32 s39, 0\n" \
".LS3_pre:\n" \
"s_barrier\n" \
"ds_read_b96 v[248:250], v252\n" \
"s_waitcnt lgkmcnt(0)\n" \
"v_readfirstlane_b32 s44, v248\n" \
"v_readfirstlane_b32 s45, v249\n" \
"v_readfirstlane_b32 s46, v250\n" \
"s_mul_i32 s42, s25, s46\n" \
"s_lshr_b32 s42, s42, 12\n" \
".LS3_cu:\n" \
"s_sub_u32 s43, s24, s44\n" \
"s_cmp_lt_i32 s43, 0\n" \
"s_cbranch_scc0 .LS3_cud\n" \
"s_add_u32 s24, s24, s25\n" \
"s_xor_b32 s31, s31, s29\n" \
"s_xor_b32 s32, s32, s29\n" \
"s_xor_b32 s47, s47, s29\n" \
"s_add_u32 s39, s39, 1\n" \
"s_branch .LS3_cu\n" \
".LS3_cud:\n" \
".LS3_loop:\n" \
"s_add_u32 s43, s40, 0\n" \
"s_and_b32 s43, s43, 3\n" \
"s_bitcmp1_b32 s43, 0\n" \
"s_cselect_b32 s47, s32, s31\n" \
"s_lshl_b32 s43, s43, 19\n" \
"s_add_u32 s47, s47, s43\n" \
"s_add_u32 s43, s40, 2\n" \
"s_and_b32 s43, s43, 3\n" \
"s_bitcmp1_b32 s43, 0\n" \
"s_cselect_b32 s49, s32, s31\n" \
"s_lshl_b32 s43, s43, 19\n" \
"s_add_u32 s49, s49, s43\n" \
"s_sub_u32 s43, s24, s44\n" \
"s_max_i32 s43, s43, 0xfffd0000\n" \
"s_min_i32 s43, s43, 0x4000\n" \
"s_mul_i32 s43, s43, s46\n" \
"s_ashr_i32 s43, s43, 12\n" \
"s_add_u32 s41, s45, s43\n" \
"s_add_u32 s41, s41, 12\n" \
"s_branch .LS3_a1\n" \
".LS3_retry:\n" \
"s_add_u32 s51, s51, 1\n" \
"s_cmp_gt_u32 s51, 0x20000\n" \
"s_cbranch_scc1 .LS3_end\n" \
".LS3_a0:\n" \
"s_add_u32 s41, s41, s42\n" \
"s_add_u32 s24, s24, s25\n" \
"s_xor_b32 s31, s31, s29\n" \
"s_xor_b32 s32, s32, s29\n" \
"s_xor_b32 s47, s47, s29\n" \
"s_xor_b32 s48, s48, s29\n" \
"s_add_u32 s39, s39, 1\n" \
"buffer_store_dword v188, v213, s[4:7], s47 offen sc1\n" \
"buffer_store_dword v188, v215, s[4:7], s47 offen sc1\n" \
".p2align 6\n" \
".LS3_a1:\n" \
"s_memtime vcc\n" \
"s_waitcnt lgkmcnt(0)\n" \
"s_sub_u32 s43, s41, vcc_lo\n" \
"s_cmp_gt_i32 s43, 128\n" \
"s_cbranch_scc1 .LS3_a1\n" \
"s_cmp_lt_i32 s43, s37\n" \
"s_cbranch_scc1 .LS3_a0\n" \
"s_max_i32 s43, s43, 0\n" \
"s_and_b32 s43, s43, -4\n" \
"s_getpc_b64 vcc\n" \
".LS3_a3:\n" \
"s_sub_u32 s43, .LS3_a4-.LS3_a3, s43\n" \
"s_add_u32 vcc_lo, vcc_lo, s43\n" \
"s_addc_u32 vcc_hi, vcc_hi, 0\n" \
"s_setpc_b64 vcc\n" \
".rept 32\n" \
"s_nop 0\n" \
".endr\n" \
".LS3_a4:\n" \
"buffer_load_dwordx4 v[12:15], v210, s[4:7], s47 offen sc1\n" \
"s_nop 3\n" \
"buffer_load_dwordx4 v[0:3], v208, s[4:7], s47 offen sc1\n" \
"s_nop 3\n" \
"buffer_load_dwordx4 v[4:7], v208, s[4:7], s47 offen offset:1024 sc1\n" \
"s_nop 3\n" \
"buffer_load_dwordx4 v[8:11], v208, s[4:7], s47 offen offset:2048 sc1\n" \
"s_nop 3\n" \
"buffer_load_dwordx4 v[16:19], v209, s[4:7], s47 offen sc1\n" \
"s_nop 3\n" \
"buffer_load_dwordx4 v[20:23], v209, s[4:7], s47 offen offset:1024 sc1\n" \
"s_nop 3\n" \
"buffer_load_dwordx4 v[24:27], v209, s[4:7], s47 offen offset:2048 sc1\n" \
"s_lshl_b32 s43, s40, 7\n" \
"buffer_load_dwordx4 v[236:239], v207, s[4:7], s43 offen sc1\n" \
"s_add_u32 s35, s35, 1\n" \
"v_mov_b32 v28, 0\n" \
"v_mov_b32 v29, 0\n" \
"v_mov_b32 v30, 0\n" \
"v_mov_b32 v31, 0\n" \
"v_mov_b32 v32, 0\n" \
"v_mov_b32 v33, 0\n" \
"v_mov_b32 v34, 0\n" \
"v_mov_b32 v35, 0\n" \
"v_mov_b32 v36, 0\n" \
"v_mov_b32 v37, 0\n" \
"v_mov_b32 v38, 0\n" \
"v_mov_b32 v39, 0\n" \
"v_mov_b32 v40, 0\n" \
"v_mov_b32 v41, 0\n" \
"v_mov_b32 v42, 0\n" \
"v_mov_b32 v43, 0\n" \
"v_mov_b32 v68, 0\n" \
"v_mov_b32 v69, 0\n" \
"v_mov_b32 v70, 0\n" \
"v_mov_b32 v71, 0\n" \
"s_waitcnt vmcnt(7)\n" \
"v_smfmac_f32_16x16x64_bf16 v[36:39], v[12:15], a[0:7], v200\n" \
"v_smfmac_f32_16x16x64_bf16 v[40:43], v[12:15], a[8:15], v200\n" \
"s_waitcnt vmcnt(6)\n" \
"v_smfmac_f32_16x16x64_bf16 v[28:31], v[0:3], a[16:23], v200\n" \
"v_smfmac_f32_16x16x64_bf16 v[32:35], v[0:3], a[24:31], v200\n" \
"v_smfmac_f32_16x16x64_bf16 v[36:39], v[0:3], a[32:39], v200\n" \
"v_smfmac_f32_16x16x64_bf16 v[40:43], v[0:3], a[40:47], v200\n" \
"s_waitcnt vmcnt(5)\n" \
"v_smfmac_f32_16x16x64_bf16 v[28:31], v[4:7], a[48:55], v200\n" \
"v_smfmac_f32_16x16x64_bf16 v[32:35], v[4:7], a[56:63], v200\n" \
"v_smfmac_f32_16x16x64_bf16 v[36:39], v[4:7], a[64:71], v200\n" \
"v_smfmac_f32_16x16x64_bf16 v[40:43], v[4:7], a[72:79], v200\n" \
"s_waitcnt vmcnt(4)\n" \
"v_smfmac_f32_16x16x64_bf16 v[28:31], v[8:11], a[80:87], v200\n" \
"v_smfmac_f32_16x16x64_bf16 v[32:35], v[8:11], a[88:95], v200\n" \
"v_smfmac_f32_16x16x64_bf16 v[36:39], v[8:11], a[96:103], v200\n" \
"v_smfmac_f32_16x16x64_bf16 v[40:43], v[8:11], a[104:111], v200\n" \
"s_nop 4\n" \
"v_cvt_pk_bf16_f32 v128, v28, v29\n" \
"v_cvt_pk_bf16_f32 v129, v30, v31\n" \
"v_cvt_pk_bf16_f32 v130, v32, v33\n" \
"v_cvt_pk_bf16_f32 v131, v34, v35\n" \
"v_cvt_pk_bf16_f32 v132, v36, v37\n" \
"v_cvt_pk_bf16_f32 v133, v38, v39\n" \
"ds_write2_b64 v211, v[128:129], v[130:131] offset0:0 offset1:16\n" \
"v_cvt_pk_bf16_f32 v134, v40, v41\n" \
"v_cvt_pk_bf16_f32 v135, v42, v43\n" \
"ds_write2_b64 v214, v[132:133], v[134:135] offset0:0 offset1:16\n" \
"ds_read_b64_tr_b16 v[96:97], v212 offset:6912\n" \
"ds_read_b64_tr_b16 v[98:99], v212 offset:6928\n" \
"ds_read_b64_tr_b16 v[100:101], v212 offset:7168\n" \
"ds_read_b64_tr_b16 v[102:103], v212 offset:7184\n" \
"s_waitcnt vmcnt(1)\n" \
"v_max3_u32 v64, v16, v16, v17\n" \
"v_max3_u32 v64, v64, v18, v19\n" \
"v_max3_u32 v64, v64, v20, v21\n" \
"v_max3_u32 v64, v64, v22, v23\n" \
"v_max3_u32 v64, v64, v24, v25\n" \
"v_max3_u32 v64, v64, v26, v27\n" \
"v_cmp_eq_u32 vcc, -1, v64\n" \
"s_barrier\n" \
"ds_read_b64_tr_b16 v[72:73], v212 offset:0\n" \
"ds_read_b64_tr_b16 v[74:75], v212 offset:16\n" \
"ds_read_b64_tr_b16 v[76:77], v212 offset:256\n" \
"ds_read_b64_tr_b16 v[78:79], v212 offset:272\n" \
"ds_read_b64_tr_b16 v[80:81], v212 offset:2304\n" \
"ds_read_b64_tr_b16 v[82:83], v212 offset:2320\n" \
"ds_read_b64_tr_b16 v[84:85], v212 offset:2560\n" \
"ds_read_b64_tr_b16 v[86:87], v212 offset:2576\n" \
"s_cbranch_vccnz .LS3_veto\n" \
"s_waitcnt lgkmcnt(8)\n" \
"v_smfmac_f32_16x16x64_bf16 v[68:71], v[12:15], v[96:103], v200\n" \
"ds_read_b64_tr_b16 v[88:89], v212 offset:4608\n" \
"ds_read_b64_tr_b16 v[90:91], v212 offset:4624\n" \
"ds_read_b64_tr_b16 v[92:93], v212 offset:4864\n" \
"ds_read_b64_tr_b16 v[94:95], v212 offset:4880\n" \
"s_waitcnt lgkmcnt(8)\n" \
"v_smfmac_f32_16x16x64_bf16 v[68:71], v[0:3], v[72:79], v200\n" \
"s_waitcnt lgkmcnt(4)\n" \
"v_smfmac_f32_16x16x64_bf16 v[68:71], v[4:7], v[80:87], v200\n" \
"s_waitcnt lgkmcnt(0)\n" \
"v_smfmac_f32_16x16x64_bf16 v[68:71], v[8:11], v[88:95], v200\n" \
"s_nop 7\n" \
"v_cmp_u_f32 vcc, v68, v68\n" \
"s_cbranch_vccnz .LS3_veto2\n" \
".LS3_veto2r:\n" \
"s_barrier\n" \
"ds_read_b64_tr_b16 v[104:105], v212 offset:9216\n" \
"ds_read_b64_tr_b16 v[106:107], v212 offset:9232\n" \
"ds_read_b64_tr_b16 v[108:109], v212 offset:9472\n" \
"ds_read_b64_tr_b16 v[110:111], v212 offset:9488\n" \
"ds_read_b64_tr_b16 v[112:113], v212 offset:11520\n" \
"ds_read_b64_tr_b16 v[114:115], v212 offset:11536\n" \
"ds_read_b64_tr_b16 v[116:117], v212 offset:11776\n" \
"ds_read_b64_tr_b16 v[118:119], v212 offset:11792\n" \
"s_barrier\n" \
"ds_read_b64_tr_b16 v[120:121], v212 offset:13824\n" \
"ds_read_b64_tr_b16 v[122:123], v212 offset:13840\n" \
"ds_read_b64_tr_b16 v[124:125], v212 offset:14080\n" \
"ds_read_b64_tr_b16 v[126:127], v212 offset:14096\n" \
"s_waitcnt lgkmcnt(8)\n" \
"v_smfmac_f32_16x16x64_bf16 v[68:71], v[16:19], v[104:111], v200\n" \
"s_waitcnt lgkmcnt(4)\n" \
"v_smfmac_f32_16x16x64_bf16 v[68:71], v[20:23], v[112:119], v200\n" \
"s_waitcnt lgkmcnt(0)\n" \
"v_smfmac_f32_16x16x64_bf16 v[68:71], v[24:27], v[120:127], v200\n" \
"s_nop 7\n" \
"v_add_f32_dpp v128, v68, v68 row_shl:4 row_mask:0xf bank_mask:0x5\n" \
"v_add_f32_dpp v129, v70, v70 row_shl:4 row_mask:0xf bank_mask:0x5\n" \
"v_add_f32_dpp v128, v69, v69 row_shr:4 row_mask:0xf bank_mask:0xa\n" \
"v_add_f32_dpp v129, v71, v71 row_shr:4 row_mask:0xf bank_mask:0xa\n" \
"s_nop 1\n" \
"v_permlane32_swap_b32 v128, v129\n" \
"v_add_f32 v128, v128, v129\n" \
"buffer_store_dword v128, v213, s[4:7], s47 offen sc1\n" \
"buffer_store_dword v128, v215, s[4:7], s47 offen sc1\n" \
"s_xor_b32 s43, s47, s29\n" \
"buffer_store_dword v128, v213, s[4:7], s43 offen sc1\n" \
"buffer_store_dword v128, v215, s[4:7], s43 offen sc1\n" \
"s_waitcnt vmcnt(4)\n" \
"v_max_u32 v236, v236, v238\n" \
"v_max_u32 v237, v237, v239\n" \
"v_and_b32 v236, 0xfff, v236\n" \
"v_and_b32 v237, 0xfff, v237\n" \
"s_mov_b64 exec, 1\n" \
"ds_write_b64 v252, v[236:237] offset:24\n" \
"s_mov_b64 exec, -1\n" \
"s_nop 15\n" \
"s_nop 13\n" \
"buffer_store_dword v188, v213, s[4:7], s49 offen sc1\n" \
"buffer_store_dword v188, v215, s[4:7], s49 offen sc1\n" \
"s_xor_b32 s43, s49, s29\n" \
"buffer_store_dword v188, v213, s[4:7], s43 offen sc1\n" \
"buffer_store_dword v188, v215, s[4:7], s43 offen sc1\n" \
"s_waitcnt lgkmcnt(0)\n" \
"s_barrier\n" \
"ds_read_b32 v64, v252 offset:16\n" \
"ds_read_b64 v[246:247], v252 offset:24\n" \
"ds_read_b96 v[248:250], v252\n" \
"s_waitcnt lgkmcnt(0)\n" \
"v_readfirstlane_b32 s44, v248\n" \
"v_readfirstlane_b32 s45, v249\n" \
"v_readfirstlane_b32 s46, v250\n" \
"v_readfirstlane_b32 s26, v246\n" \
"v_readfirstlane_b32 s27, v247\n" \
"s_mul_i32 s42, s25, s46\n" \
"s_lshr_b32 s42, s42, 12\n" \
"v_readfirstlane_b32 s43, v64\n" \
"s_cmp_eq_u32 s43, s35\n" \
"s_cbranch_scc1 .LS3_retry\n" \
"v_cmp_u_f32 vcc, v128, v128\n" \
"s_cbranch_vccnz .LS3_nan\n" \
".LS3_nanr:\n" \
"s_add_u32 s40, s40, 1\n" \
"s_add_u32 s24, s24, s26\n" \
"s_add_u32 s24, s24, s27\n" \
"s_cmp_eq_u32 s40, 0\n" \
"s_cbranch_scc1 .LS3_wet\n" \
"s_cmp_lt_u32 s40, 63\n" \
"s_cbranch_scc1 .LS3_loop\n" \
".LS3_end:\n" \
"s_endpgm\n" \
".LS3_nan:\n" \
"v_cmp_u_f32 vcc, v128, v128\n" \
"v_mov_b32 v129, 0x7fc00001\n" \
"s_nop 1\n" \
"v_cndmask_b32 v128, v128, v129, vcc\n" \
"buffer_store_dword v128, v213, s[4:7], s47 offen sc1\n" \
"buffer_store_dword v128, v215, s[4:7], s47 offen sc1\n" \
"s_xor_b32 s43, s47, s29\n" \
"buffer_store_dword v128, v213, s[4:7], s43 offen sc1\n" \
"buffer_store_dword v128, v215, s[4:7], s43 offen sc1\n" \
"s_branch .LS3_nanr\n" \
".LS3_veto:\n" \
"v_mov_b32 v64, s35\n" \
"ds_write_b32 v252, v64 offset:20\n" \
"s_barrier\n" \
"s_waitcnt lgkmcnt(0)\n" \
"s_barrier\n" \
"s_branch .LS3_retry\n" \
".LS3_veto2:\n" \
"v_max3_u32 v64, v0, v0, v1\n" \
"v_max3_u32 v64, v64, v2, v3\n" \
"v_max3_u32 v64, v64, v4, v5\n" \
"v_max3_u32 v64, v64, v6, v7\n" \
"v_max3_u32 v64, v64, v8, v9\n" \
"v_max3_u32 v64, v64, v10, v11\n" \
"v_max3_u32 v64, v64, v12, v13\n" \
"v_max3_u32 v64, v64, v14, v15\n" \
"v_cmp_eq_u32 vcc, -1, v64\n" \
"s_cbranch_vccnz .LS3_veto3\n" \
"s_cmp_eq_u32 s33, s40\n" \
"s_cbranch_scc1 .LS3_veto2r\n" \
"s_mov_b32 s33, s40\n" \
".LS3_veto3:\n" \
"v_mov_b32 v64, s35\n" \
"ds_write_b32 v252, v64 offset:20\n" \
"s_barrier\n" \
"s_waitcnt lgkmcnt(0)\n" \
"s_barrier\n" \
"s_branch .LS3_retry\n" \
".p2align 6\n" \
"fk_role_P0:\n" \
"ds_read_b32 v200, v255 offset:0\n" \
"ds_read_b32 v201, v255 offset:256\n" \
"ds_read_b32 v202, v255 offset:512\n" \
"ds_read_b32 v203, v255 offset:768\n" \
"ds_read_b32 v204, v255 offset:1024\n" \
"ds_read_b32 v205, v255 offset:1280\n" \
"ds_read_b32 v206, v255 offset:1536\n" \
"ds_read_b32 v207, v255 offset:1792\n" \
"ds_read_b32 v208, v255 offset:2048\n" \
"ds_read_b32 v209, v255 offset:2304\n" \
"ds_read_b32 v210, v255 offset:2560\n" \
"ds_read_b32 v211, v255 offset:2816\n" \
"ds_read_b32 v212, v255 offset:3072\n" \
"ds_read_b32 v213, v255 offset:3328\n" \
"ds_read_b32 v214, v255 offset:3584\n" \
"ds_read_b32 v215, v255 offset:3840\n" \
"ds_read_b32 v216, v255 offset:4096\n" \
"ds_read_b32 v217, v255 offset:4352\n" \
"ds_read_b32 v218, v255 offset:4608\n" \
"ds_read_b32 v219, v255 offset:4864\n" \
"ds_read_b32 v220, v255 offset:5120\n" \
"ds_read_b32 v221, v255 offset:5376\n" \
"ds_read_b32 v222, v255 offset:5632\n" \
"ds_read_b32 v223, v255 offset:5888\n" \
"ds_read_b32 v254, v255 offset:7936\n" \
"s_waitcnt lgkmcnt(0)\n" \
"v_readlane_b32 s4, v254, 0\n" \
"v_readlane_b32 s5, v254, 1\n" \
"v_readlane_b32 s6, v254, 2\n" \
"v_readlane_b32 s7, v254, 3\n" \
"v_readlane_b32 s8, v254, 4\n" \
"v_readlane_b32 s9, v254, 5\n" \
"v_readlane_b32 s10, v254, 6\n" \
"v_readlane_b32 s11, v254, 7\n" \
"v_readlane_b32 s12, v254, 8\n" \
"v_readlane_b32 s13, v254, 9\n" \
"v_readlane_b32 s14, v254, 10\n" \
"v_readlane_b32 s15, v254, 11\n" \
"v_readlane_b32 s16, v254, 12\n" \
"v_readlane_b32 s17, v254, 13\n" \
"v_readlane_b32 s18, v254, 14\n" \
"v_readlane_b32 s19, v254, 15\n" \
"v_readlane_b32 s20, v254, 16\n" \
"v_readlane_b32 s21, v254, 17\n" \
"v_readlane_b32 s22, v254, 18\n" \
"v_readlane_b32 s23, v254, 19\n" \
"v_readlane_b32 s24, v254, 20\n" \
"v_readlane_b32 s25, v254, 21\n" \
"v_readlane_b32 s26, v254, 22\n" \
"v_readlane_b32 s27, v254, 23\n" \
"v_readlane_b32 s28, v254, 24\n" \
"v_readlane_b32 s29, v254, 25\n" \
"v_readlane_b32 s30, v254, 26\n" \
"v_readlane_b32 s31, v254, 27\n" \
"v_readlane_b32 s32, v254, 28\n" \
"v_readlane_b32 s33, v254, 29\n" \
"v_readlane_b32 s34, v254, 30\n" \
"v_readlane_b32 s35, v254, 31\n" \
"v_readlane_b32 s36, v254, 32\n" \
"v_readlane_b32 s37, v254, 33\n" \
"v_readlane_b32 s38, v254, 34\n" \
"v_readlane_b32 s39, v254, 35\n" \
"s_mov_b32 s40, 0\n" \
"s_mov_b32 s51, 0\n" \
"s_nop 0\n" \
"v_mov_b32 v252, s38\n" \
"s_nop 4\n" \
"v_mov_b32 v112, -1\n" \
"v_mov_b32 v113, -1\n" \
"v_mov_b32 v114, -1\n" \
"v_mov_b32 v115, -1\n" \
"v_mov_b32 v109, 0x7fc00001\n" \
"s_mov_b32 s52, 0x8000\n" \
"s_mov_b32 s53, 0x4000\n" \
"s_mov_b32 s54, 0xc000\n" \
"v_writelane_b32 v251, s6, 0\n" \
"v_writelane_b32 v251, s10, 1\n" \
"v_writelane_b32 v251, s14, 2\n" \
"v_writelane_b32 v251, s18, 3\n" \
"s_mov_b32 s6, 0\n" \
"s_mov_b32 s10, 0\n" \
"s_mov_b32 s14, 0\n" \
"s_mov_b32 s18, 0\n" \
"s_mov_b32 s40, -1\n" \
"s_branch .LP0_pre\n" \
".LP0_wet:\n" \
"s_waitcnt vmcnt(0)\n" \
"v_readlane_b32 s6, v251, 0\n" \
"v_readlane_b32 s10, v251, 1\n" \
"v_readlane_b32 s14, v251, 2\n" \
"v_readlane_b32 s18, v251, 3\n" \
"s_nop 4\n" \
"s_mov_b32 s33, 20\n" \
"s_mov_b32 s34, 0\n" \
"s_add_u32 s43, s30, 1\n" \
"s_mov_b32 s50, 0\n" \
".LP0_w21l:\n" \
"buffer_load_dword v240, v204, s[4:7], 0 offen sc1\n" \
"s_waitcnt vmcnt(0)\n" \
".LP0_w21c:\n" \
"v_subrev_u32 v241, s43, v240\n" \
"v_cmp_gt_u32 vcc, 0x100, v241\n" \
"s_cmp_eq_u64 vcc, -1\n" \
"s_cbranch_scc1 .LP0_w21d\n" \
"s_sleep 2\n" \
"s_add_u32 s50, s50, 1\n" \
"s_cmp_lt_u32 s50, 0x20000\n" \
"s_cbranch_scc1 .LP0_w21l\n" \
"s_branch .LP0_end\n" \
".LP0_w21d:\n" \
"v_readlane_b32 s43, v202, 0\n" \
"s_nop 4\n" \
"buffer_load_dwordx4 a[0:3], v201, s[8:11], s43 offen sc1\n" \
"buffer_load_dwordx4 a[4:7], v201, s[8:11], s43 offen offset:1024 sc1\n" \
"v_readlane_b32 s43, v202, 7\n" \
"s_nop 4\n" \
"buffer_load_dwordx4 a[56:59], v201, s[8:11], s43 offen sc1\n" \
"buffer_load_dwordx4 a[60:63], v201, s[8:11], s43 offen offset:1024 sc1\n" \
"v_readlane_b32 s43, v202, 14\n" \
"s_nop 4\n" \
"buffer_load_dwordx4 a[112:115], v201, s[8:11], s43 offen sc1\n" \
"buffer_load_dwordx4 a[116:119], v201, s[8:11], s43 offen offset:1024 sc1\n" \
"v_readlane_b32 s43, v202, 21\n" \
"s_nop 4\n" \
"buffer_load_dwordx4 a[168:171], v201, s[8:11], s43 offen sc1\n" \
"buffer_load_dwordx4 a[172:175], v201, s[8:11], s43 offen offset:1024 sc1\n" \
"s_add_u32 s43, s30, 2\n" \
"s_mov_b32 s50, 0\n" \
"s_branch .LP0_w22c\n" \
".LP0_w22l:\n" \
"buffer_load_dword v240, v204, s[4:7], 0 offen sc1\n" \
"s_waitcnt vmcnt(0)\n" \
".LP0_w22c:\n" \
"v_subrev_u32 v241, s43, v240\n" \
"v_cmp_gt_u32 vcc, 0x100, v241\n" \
"s_cmp_eq_u64 vcc, -1\n" \
"s_cbranch_scc1 .LP0_w22d\n" \
"s_sleep 2\n" \
"s_add_u32 s50, s50, 1\n" \
"s_cmp_lt_u32 s50, 0x20000\n" \
"s_cbranch_scc1 .LP0_w22l\n" \
"s_branch .LP0_end\n" \
".LP0_w22d:\n" \
"v_readlane_b32 s43, v202, 4\n" \
"s_nop 4\n" \
"buffer_load_dwordx4 a[32:35], v201, s[8:11], s43 offen sc1\n" \
"buffer_load_dwordx4 a[36:39], v201, s[8:11], s43 offen offset:1024 sc1\n" \
"v_readlane_b32 s43, v202, 11\n" \
"s_nop 4\n" \
"buffer_load_dwordx4 a[88:91], v201, s[8:11], s43 offen sc1\n" \
"buffer_load_dwordx4 a[92:95], v201, s[8:11], s43 offen offset:1024 sc1\n" \
"v_readlane_b32 s43, v202, 18\n" \
"s_nop 4\n" \
"buffer_load_dwordx4 a[144:147], v201, s[8:11], s43 offen sc1\n" \
"buffer_load_dwordx4 a[148:151], v201, s[8:11], s43 offen offset:1024 sc1\n" \
"v_readlane_b32 s43, v202, 25\n" \
"s_nop 4\n" \
"buffer_load_dwordx4 a[200:203], v201, s[8:11], s43 offen sc1\n" \
"buffer_load_dwordx4 a[204:207], v201, s[8:11], s43 offen offset:1024 sc1\n" \
"s_add_u32 s43, s30, 3\n" \
"s_mov_b32 s50, 0\n" \
"s_branch .LP0_w23c\n" \
".LP0_w23l:\n" \
"buffer_load_dword v240, v204, s[4:7], 0 offen sc1\n" \
"s_waitcnt vmcnt(0)\n" \
".LP0_w23c:\n" \
"v_subrev_u32 v241, s43, v240\n" \
"v_cmp_gt_u32 vcc, 0x100, v241\n" \
"s_cmp_eq_u64 vcc, -1\n" \
"s_cbranch_scc1 .LP0_w23d\n" \
"s_sleep 2\n" \
"s_add_u32 s50, s50, 1\n" \
"s_cmp_lt_u32 s50, 0x20000\n" \
"s_cbranch_scc1 .LP0_w23l\n" \
"s_branch .LP0_end\n" \
".LP0_w23d:\n" \
"v_readlane_b32 s43, v202, 1\n" \
"s_nop 4\n" \
"buffer_load_dwordx4 a[8:11], v201, s[8:11], s43 offen sc1\n" \
"buffer_load_dwordx4 a[12:15], v201, s[8:11], s43 offen offset:1024 sc1\n" \
"v_readlane_b32 s43, v202, 8\n" \
"s_nop 4\n" \
"buffer_load_dwordx4 a[64:67], v201, s[8:11], s43 offen sc1\n" \
"buffer_load_dwordx4 a[68:71], v201, s[8:11], s43 offen offset:1024 sc1\n" \
"v_readlane_b32 s43, v202, 15\n" \
"s_nop 4\n" \
"buffer_load_dwordx4 a[120:123], v201, s[8:11], s43 offen sc1\n" \
"buffer_load_dwordx4 a[124:127], v201, s[8:11], s43 offen offset:1024 sc1\n" \
"v_readlane_b32 s43, v202, 22\n" \
"s_nop 4\n" \
"buffer_load_dwordx4 a[176:179], v201, s[8:11], s43 offen sc1\n" \
"buffer_load_dwordx4 a[180:183], v201, s[8:11], s43 offen offset:1024 sc1\n" \
"s_add_u32 s43, s30, 4\n" \
"s_mov_b32 s50, 0\n" \
"s_branch .LP0_w24c\n" \
".LP0_w24l:\n" \
"buffer_load_dword v240, v204, s[4:7], 0 offen sc1\n" \
"s_waitcnt vmcnt(0)\n" \
".LP0_w24c:\n" \
"v_subrev_u32 v241, s43, v240\n" \
"v_cmp_gt_u32 vcc, 0x100, v241\n" \
"s_cmp_eq_u64 vcc, -1\n" \
"s_cbranch_scc1 .LP0_w24d\n" \
"s_sleep 2\n" \
"s_add_u32 s50, s50, 1\n" \
"s_cmp_lt_u32 s50, 0x20000\n" \
"s_cbranch_scc1 .LP0_w24l\n" \
"s_branch .LP0_end\n" \
".LP0_w24d:\n" \
"v_readlane_b32 s43, v202, 5\n" \
"s_nop 4\n" \
"buffer_load_dwordx4 a[40:43], v201, s[8:11], s43 offen sc1\n" \
"buffer_load_dwordx4 a[44:47], v201, s[8:11], s43 offen offset:1024 sc1\n" \
"v_readlane_b32 s43, v202, 12\n" \
"s_nop 4\n" \
"buffer_load_dwordx4 a[96:99], v201, s[8:11], s43 offen sc1\n" \
"buffer_load_dwordx4 a[100:103], v201, s[8:11], s43 offen offset:1024 sc1\n" \
"v_readlane_b32 s43, v202, 19\n" \
"s_nop 4\n" \
"buffer_load_dwordx4 a[152:155], v201, s[8:11], s43 offen sc1\n" \
"buffer_load_dwordx4 a[156:159], v201, s[8:11], s43 offen offset:1024 sc1\n" \
"v_readlane_b32 s43, v202, 26\n" \
"s_nop 4\n" \
"buffer_load_dwordx4 a[208:211], v201, s[8:11], s43 offen sc1\n" \
"buffer_load_dwordx4 a[212:215], v201, s[8:11], s43 offen offset:1024 sc1\n" \
"s_add_u32 s43, s30, 5\n" \
"s_mov_b32 s50, 0\n" \
"s_branch .LP0_w25c\n" \
".LP0_w25l:\n" \
"buffer_load_dword v240, v204, s[4:7], 0 offen sc1\n" \
"s_waitcnt vmcnt(0)\n" \
".LP0_w25c:\n" \
"v_subrev_u32 v241, s43, v240\n" \
"v_cmp_gt_u32 vcc, 0x100, v241\n" \
"s_cmp_eq_u64 vcc, -1\n" \
"s_cbranch_scc1 .LP0_w25d\n" \
"s_sleep 2\n" \
"s_add_u32 s50, s50, 1\n" \
"s_cmp_lt_u32 s50, 0x20000\n" \
"s_cbranch_scc1 .LP0_w25l\n" \
"s_branch .LP0_end\n" \
".LP0_w25d:\n" \
"v_readlane_b32 s43, v202, 2\n" \
"s_nop 4\n" \
"buffer_load_dwordx4 a[16:19], v201, s[8:11], s43 offen sc1\n" \
"buffer_load_dwordx4 a[20:23], v201, s[8:11], s43 offen offset:1024 sc1\n" \
"v_readlane_b32 s43, v202, 9\n" \
"s_nop 4\n" \
"buffer_load_dwordx4 a[72:75], v201, s[8:11], s43 offen sc1\n" \
"buffer_load_dwordx4 a[76:79], v201, s[8:11], s43 offen offset:1024 sc1\n" \
"v_readlane_b32 s43, v202, 16\n" \
"s_nop 4\n" \
"buffer_load_dwordx4 a[128:131], v201, s[8:11], s43 offen sc1\n" \
"buffer_load_dwordx4 a[132:135], v201, s[8:11], s43 offen offset:1024 sc1\n" \
"v_readlane_b32 s43, v202, 23\n" \
"s_nop 4\n" \
"buffer_load_dwordx4 a[184:187], v201, s[8:11], s43 offen sc1\n" \
"buffer_load_dwordx4 a[188:191], v201, s[8:11], s43 offen offset:1024 sc1\n" \
"s_add_u32 s43, s30, 6\n" \
"s_mov_b32 s50, 0\n" \
"s_branch .LP0_w26c\n" \
".LP0_w26l:\n" \
"buffer_load_dword v240, v204, s[4:7], 0 offen sc1\n" \
"s_waitcnt vmcnt(0)\n" \
".LP0_w26c:\n" \
"v_subrev_u32 v241, s43, v240\n" \
"v_cmp_gt_u32 vcc, 0x100, v241\n" \
"s_cmp_eq_u64 vcc, -1\n" \
"s_cbranch_scc1 .LP0_w26d\n" \
"s_sleep 2\n" \
"s_add_u32 s50, s50, 1\n" \
"s_cmp_lt_u32 s50, 0x20000\n" \
"s_cbranch_scc1 .LP0_w26l\n" \
"s_branch .LP0_end\n" \
".LP0_w26d:\n" \
"v_readlane_b32 s43, v202, 6\n" \
"s_nop 4\n" \
"buffer_load_dwordx4 a[48:51], v201, s[8:11], s43 offen sc1\n" \
"buffer_load_dwordx4 a[52:55], v201, s[8:11], s43 offen offset:1024 sc1\n" \
"v_readlane_b32 s43, v202, 13\n" \
"s_nop 4\n" \
"buffer_load_dwordx4 a[104:107], v201, s[8:11], s43 offen sc1\n" \
"buffer_load_dwordx4 a[108:111], v201, s[8:11], s43 offen offset:1024 sc1\n" \
"v_readlane_b32 s43, v202, 20\n" \
"s_nop 4\n" \
"buffer_load_dwordx4 a[160:163], v201, s[8:11], s43 offen sc1\n" \
"buffer_load_dwordx4 a[164:167], v201, s[8:11], s43 offen offset:1024 sc1\n" \
"v_readlane_b32 s43, v202, 27\n" \
"s_nop 4\n" \
"buffer_load_dwordx4 a[216:219], v201, s[8:11], s43 offen sc1\n" \
"buffer_load_dwordx4 a[220:223], v201, s[8:11], s43 offen offset:1024 sc1\n" \
"s_add_u32 s43, s30, 7\n" \
"s_mov_b32 s50, 0\n" \
"s_branch .LP0_w27c\n" \
".LP0_w27l:\n" \
"buffer_load_dword v240, v204, s[4:7], 0 offen sc1\n" \
"s_waitcnt vmcnt(0)\n" \
".LP0_w27c:\n" \
"v_subrev_u32 v241, s43, v240\n" \
"v_cmp_gt_u32 vcc, 0x100, v241\n" \
"s_cmp_eq_u64 vcc, -1\n" \
"s_cbranch_scc1 .LP0_w27d\n" \
"s_sleep 2\n" \
"s_add_u32 s50, s50, 1\n" \
"s_cmp_lt_u32 s50, 0x20000\n" \
"s_cbranch_scc1 .LP0_w27l\n" \
"s_branch .LP0_end\n" \
".LP0_w27d:\n" \
"v_readlane_b32 s43, v202, 3\n" \
"s_nop 4\n" \
"buffer_load_dwordx4 a[24:27], v201, s[8:11], s43 offen sc1\n" \
"buffer_load_dwordx4 a[28:31], v201, s[8:11], s43 offen offset:1024 sc1\n" \
"v_readlane_b32 s43, v202, 10\n" \
"s_nop 4\n" \
"buffer_load_dwordx4 a[80:83], v201, s[8:11], s43 offen sc1\n" \
"buffer_load_dwordx4 a[84:87], v201, s[8:11], s43 offen offset:1024 sc1\n" \
"v_readlane_b32 s43, v202, 17\n" \
"s_nop 4\n" \
"buffer_load_dwordx4 a[136:139], v201, s[8:11], s43 offen sc1\n" \
"buffer_load_dwordx4 a[140:143], v201, s[8:11], s43 offen offset:1024 sc1\n" \
"v_readlane_b32 s43, v202, 24\n" \
"s_nop 4\n" \
"buffer_load_dwordx4 a[192:195], v201, s[8:11], s43 offen sc1\n" \
"buffer_load_dwordx4 a[196:199], v201, s[8:11], s43 offen offset:1024 sc1\n" \
"s_add_u32 s43, s30, 1\n" \
"s_mov_b32 s50, 0\n" \
".LP0_w28l:\n" \
"buffer_load_dword v240, v205, s[4:7], 0 offen sc1\n" \
"s_waitcnt vmcnt(0)\n" \
".LP0_w28c:\n" \
"v_subrev_u32 v241, s43, v240\n" \
"v_cmp_gt_u32 vcc, 0x100, v241\n" \
"s_cmp_eq_u64 vcc, -1\n" \
"s_cbranch_scc1 .LP0_w28d\n" \
"s_sleep 2\n" \
"s_add_u32 s50, s50, 1\n" \
"s_cmp_lt_u32 s50, 0x20000\n" \
"s_cbranch_scc1 .LP0_w28l\n" \
"s_branch .LP0_end\n" \
".LP0_w28d:\n" \
"v_readfirstlane_b32 s31, v241\n" \
"s_lshr_b32 s32, s31, 2\n" \
"s_xor_b32 s29, s31, s32\n" \
"s_and_b32 s29, s29, 3\n" \
"s_lshl_b32 s29, s29, 13\n" \
"s_bitset1_b32 s29, 21\n" \
"s_and_b32 s31, s31, 3\n" \
"s_lshl_b32 s31, s31, 13\n" \
"s_bitcmp1_b32 s39, 0\n" \
"s_cselect_b32 s43, s29, 0\n" \
"s_xor_b32 s31, s31, s43\n" \
"s_xor_b32 s32, s31, 0x4000\n" \
"s_mov_b32 s39, 0\n" \
"s_add_u32 s43, s40, 0\n" \
"s_and_b32 s43, s43, 3\n" \
"s_bitcmp1_b32 s43, 0\n" \
"s_cselect_b32 s47, s32, s31\n" \
"s_lshl_b32 s43, s43, 19\n" \
"s_add_u32 s47, s47, s43\n" \
".LP0_pre:\n" \
"s_barrier\n" \
"ds_read_b96 v[248:250], v252\n" \
"s_waitcnt lgkmcnt(0)\n" \
"v_readfirstlane_b32 s44, v248\n" \
"v_readfirstlane_b32 s45, v249\n" \
"v_readfirstlane_b32 s46, v250\n" \
"s_mul_i32 s42, s25, s46\n" \
"s_lshr_b32 s42, s42, 12\n" \
".LP0_cu:\n" \
"s_sub_u32 s43, s24, s44\n" \
"s_cmp_lt_i32 s43, 0\n" \
"s_cbranch_scc0 .LP0_cud\n" \
"s_add_u32 s24, s24, s25\n" \
"s_xor_b32 s31, s31, s29\n" \
"s_xor_b32 s32, s32, s29\n" \
"s_xor_b32 s47, s47, s29\n" \
"s_add_u32 s39, s39, 1\n" \
"s_branch .LP0_cu\n" \
".LP0_cud:\n" \
"s_mov_b32 s3, s46\n" \
"s_add_u32 s43, s40, 1\n" \
"s_and_b32 s43, s43, 3\n" \
"s_bitcmp1_b32 s43, 0\n" \
"s_cselect_b32 s48, s32, s31\n" \
"s_lshl_b32 s43, s43, 19\n" \
"s_add_u32 s48, s48, s43\n" \
"s_add_u32 s43, s40, 2\n" \
"s_and_b32 s43, s43, 3\n" \
"s_bitcmp1_b32 s43, 0\n" \
"s_cselect_b32 s49, s32, s31\n" \
"s_lshl_b32 s43, s43, 19\n" \
"s_add_u32 s49, s49, s43\n" \
"s_sub_u32 s43, s24, s44\n" \
"s_add_u32 s43, s43, s28\n" \
"s_max_i32 s43, s43, 0xfffd0000\n" \
"s_min_i32 s43, s43, 0x4000\n" \
"s_mul_i32 s43, s43, s46\n" \
"s_ashr_i32 s43, s43, 12\n" \
"s_add_u32 s41, s45, s43\n" \
"s_lshr_b32 s35, s42, 1\n" \
"s_sub_u32 s35, 0, s35\n" \
".LP0_loop:\n" \
"s_branch .LP0_m1\n" \
".LP0_retry1:\n" \
"s_add_u32 s51, s51, 1\n" \
"s_cmp_gt_u32 s51, 0x20000\n" \
"s_cbranch_scc1 .LP0_end\n" \
".LP0_m0:\n" \
"s_add_u32 s41, s41, s42\n" \
"s_add_u32 s24, s24, s25\n" \
"s_xor_b32 s31, s31, s29\n" \
"s_xor_b32 s32, s32, s29\n" \
"s_xor_b32 s47, s47, s29\n" \
"s_xor_b32 s48, s48, s29\n" \
"s_add_u32 s39, s39, 1\n" \
"s_bitcmp1_b32 s31, 21\n" \
"s_cselect_b32 s43, s29, 0\n" \
"v_mov_b32 v116, s43\n" \
"ds_write_b32 v252, v116 offset:16\n" \
".LP0_m1:\n" \
"s_memtime vcc\n" \
"s_waitcnt lgkmcnt(0)\n" \
"s_sub_u32 s43, s41, vcc_lo\n" \
"s_cmp_gt_i32 s43, 0\n" \
"s_cbranch_scc1 .LP0_m1\n" \
"s_cmp_lt_i32 s43, s35\n" \
"s_cbranch_scc1 .LP0_m0\n" \
"buffer_load_dwordx4 v[0:3], v208, s[4:7], s47 offen sc1\n" \
"buffer_load_dwordx4 v[4:7], v208, s[4:7], s47 offen offset:1024 sc1\n" \
"s_nop 15\n" \
"s_nop 13\n" \
"s_mul_i32 s50, s40, 4096\n" \
"buffer_store_dwordx4 v[68:71], v218, s[12:15], s50 offen sc1\n" \
"buffer_store_dword v112, v213, s[4:7], s49 offen sc1\n" \
"s_xor_b32 s43, s49, s29\n" \
"buffer_store_dword v112, v213, s[4:7], s43 offen sc1\n" \
"s_add_u32 s43, s30, s40\n" \
"s_add_u32 s43, s43, -1\n" \
"v_mov_b32 v241, s43\n" \
"buffer_store_dword v241, v221, s[4:7], 0 offen sc1\n" \
"s_bitcmp1_b32 s31, 21\n" \
"s_cselect_b32 s43, s29, 0\n" \
"v_mov_b32 v116, s43\n" \
"ds_write_b32 v252, v116 offset:16\n" \
"v_mov_b32 v28, 0\n" \
"v_mov_b32 v29, 0\n" \
"v_mov_b32 v30, 0\n" \
"v_mov_b32 v31, 0\n" \
"v_mov_b32 v32, 0\n" \
"v_mov_b32 v33, 0\n" \
"v_mov_b32 v34, 0\n" \
"v_mov_b32 v35, 0\n" \
"v_mov_b32 v36, 0\n" \
"v_mov_b32 v37, 0\n" \
"v_mov_b32 v38, 0\n" \
"v_mov_b32 v39, 0\n" \
"v_mov_b32 v40, 0\n" \
"v_mov_b32 v41, 0\n" \
"v_mov_b32 v42, 0\n" \
"v_mov_b32 v43, 0\n" \
"s_waitcnt vmcnt(5)\n" \
"ds_write_b128 v220, v[0:3] offset:0\n" \
"s_waitcnt vmcnt(4)\n" \
"ds_write_b128 v220, v[4:7] offset:1024\n" \
"v_max3_u32 v44, v0, v0, v1\n" \
"v_max3_u32 v44, v44, v2, v3\n" \
"v_max3_u32 v44, v44, v4, v5\n" \
"v_max3_u32 v44, v44, v6, v7\n" \
"v_cmp_eq_u32 vcc, -1, v44\n" \
"v_smfmac_f32_16x16x64_bf16 v[28:31], v[0:3], a[0:7], v200\n" \
"v_smfmac_f32_16x16x64_bf16 v[32:35], v[0:3], a[56:63], v200\n" \
"s_cbranch_vccnz .LP0_retry1\n" \
"s_waitcnt lgkmcnt(0)\n" \
"s_barrier\n" \
"ds_read_b128 v[8:11], v220 offset:2048\n" \
"ds_read_b128 v[12:15], v220 offset:3072\n" \
"ds_read_b128 v[16:19], v220 offset:4096\n" \
"ds_read_b128 v[20:23], v220 offset:5120\n" \
"ds_read_b128 v[24:27], v220 offset:6144\n" \
"ds_read_b64 v[246:247], v252 offset:24\n" \
"ds_read_b96 v[248:250], v252\n" \
"v_smfmac_f32_16x16x64_bf16 v[36:39], v[0:3], a[112:119], v200\n" \
"v_smfmac_f32_16x16x64_bf16 v[40:43], v[0:3], a[168:175], v200\n" \
"v_smfmac_f32_16x16x64_bf16 v[28:31], v[4:7], a[8:15], v200\n" \
"v_smfmac_f32_16x16x64_bf16 v[32:35], v[4:7], a[64:71], v200\n" \
"v_smfmac_f32_16x16x64_bf16 v[36:39], v[4:7], a[120:127], v200\n" \
"v_smfmac_f32_16x16x64_bf16 v[40:43], v[4:7], a[176:183], v200\n" \
"s_waitcnt lgkmcnt(6)\n" \
"v_smfmac_f32_16x16x64_bf16 v[28:31], v[8:11], a[16:23], v200\n" \
"v_smfmac_f32_16x16x64_bf16 v[32:35], v[8:11], a[72:79], v200\n" \
"v_smfmac_f32_16x16x64_bf16 v[36:39], v[8:11], a[128:135], v200\n" \
"v_smfmac_f32_16x16x64_bf16 v[40:43], v[8:11], a[184:191], v200\n" \
"s_waitcnt lgkmcnt(0)\n" \
"v_readfirstlane_b32 s44, v248\n" \
"v_readfirstlane_b32 s45, v249\n" \
"v_smfmac_f32_16x16x64_bf16 v[28:31], v[12:15], a[24:31], v200\n" \
"v_readfirstlane_b32 s46, v250\n" \
"v_readfirstlane_b32 s26, v246\n" \
"v_readfirstlane_b32 s27, v247\n" \
"v_smfmac_f32_16x16x64_bf16 v[32:35], v[12:15], a[80:87], v200\n" \
"s_mul_i32 s42, s25, s46\n" \
"s_lshr_b32 s42, s42, 12\n" \
"s_add_u32 s24, s24, s26\n" \
"v_smfmac_f32_16x16x64_bf16 v[36:39], v[12:15], a[136:143], v200\n" \
"s_sub_u32 s43, s24, s44\n" \
"s_max_i32 s43, s43, 0xfffd0000\n" \
"s_min_i32 s43, s43, 0x4000\n" \
"v_smfmac_f32_16x16x64_bf16 v[40:43], v[12:15], a[192:199], v200\n" \
"s_mul_i32 s43, s43, s46\n" \
"s_ashr_i32 s43, s43, 12\n" \
"s_add_u32 s41, s45, s43\n" \
"s_cmp_eq_u32 s40, 63\n" \
"s_cbranch_scc1 .LP0_last\n" \
"s_branch .LP0_z1\n" \
".LP0_z0:\n" \
"s_add_u32 s41, s41, s42\n" \
"s_add_u32 s24, s24, s25\n" \
"s_xor_b32 s31, s31, s29\n" \
"s_xor_b32 s32, s32, s29\n" \
"s_xor_b32 s47, s47, s29\n" \
"s_xor_b32 s48, s48, s29\n" \
"s_add_u32 s39, s39, 1\n" \
"s_bitcmp1_b32 s31, 21\n" \
"s_cselect_b32 s43, s29, 0\n" \
"v_mov_b32 v116, s43\n" \
"ds_write_b32 v252, v116 offset:16\n" \
".LP0_z1:\n" \
"s_memtime vcc\n" \
"s_waitcnt lgkmcnt(0)\n" \
"s_sub_u32 s43, s41, vcc_lo\n" \
"s_cmp_gt_i32 s43, 160\n" \
"s_cbranch_scc1 .LP0_z1\n" \
"s_cmp_lt_i32 s43, s37\n" \
"s_cbranch_scc1 .LP0_z0\n" \
"buffer_load_dwordx2 v[64:65], v211, s[4:7], s47 offen sc1\n" \
".p2align 6\n" \
".LP0_b1:\n" \
"s_memtime vcc\n" \
"s_waitcnt lgkmcnt(0)\n" \
"s_sub_u32 s43, s41, vcc_lo\n" \
"s_cmp_gt_i32 s43, 128\n" \
"s_cbranch_scc1 .LP0_b1\n" \
"s_cmp_lt_i32 s43, s37\n" \
"s_cbranch_scc1 .LP0_z0\n" \
"s_max_i32 s43, s43, 0\n" \
"s_and_b32 s43, s43, -4\n" \
"s_getpc_b64 vcc\n" \
".LP0_b3:\n" \
"s_sub_u32 s43, .LP0_b4-.LP0_b3, s43\n" \
"s_add_u32 vcc_lo, vcc_lo, s43\n" \
"s_addc_u32 vcc_hi, vcc_hi, 0\n" \
"s_setpc_b64 vcc\n" \
".rept 32\n" \
"s_nop 0\n" \
".endr\n" \
".LP0_b4:\n" \
"v_smfmac_f32_16x16x64_bf16 v[28:31], v[16:19], a[32:39], v200\n" \
"buffer_load_dwordx4 v[56:59], v210, s[4:7], s47 offen sc1\n" \
"buffer_load_dwordx4 v[60:63], v210, s[4:7], s47 offen offset:256 sc1\n" \
"v_smfmac_f32_16x16x64_bf16 v[32:35], v[16:19], a[88:95], v200\n" \
"v_smfmac_f32_16x16x64_bf16 v[36:39], v[16:19], a[144:151], v200\n" \
"v_smfmac_f32_16x16x64_bf16 v[40:43], v[16:19], a[200:207], v200\n" \
"v_smfmac_f32_16x16x64_bf16 v[28:31], v[20:23], a[40:47], v200\n" \
"buffer_load_dwordx4 v[50:53], v212, s[4:7], s47 offen sc1\n" \
"v_smfmac_f32_16x16x64_bf16 v[32:35], v[20:23], a[96:103], v200\n" \
"v_smfmac_f32_16x16x64_bf16 v[36:39], v[20:23], a[152:159], v200\n" \
"v_smfmac_f32_16x16x64_bf16 v[40:43], v[20:23], a[208:215], v200\n" \
"v_smfmac_f32_16x16x64_bf16 v[28:31], v[24:27], a[48:55], v200\n" \
"v_smfmac_f32_16x16x64_bf16 v[32:35], v[24:27], a[104:111], v200\n" \
"v_smfmac_f32_16x16x64_bf16 v[36:39], v[24:27], a[160:167], v200\n" \
"v_smfmac_f32_16x16x64_bf16 v[40:43], v[24:27], a[216:223], v200\n" \
"s_nop 2\n" \
"v_mov_b32 v245, v245\n" \
"v_mov_b32 v245, v245\n" \
"v_mov_b32 v245, v245\n" \
"v_permlane32_swap_b32 v28, v32\n" \
"v_permlane32_swap_b32 v29, v33\n" \
"v_permlane32_swap_b32 v30, v34\n" \
"v_permlane32_swap_b32 v31, v35\n" \
"v_add_f32 v28, v28, v32\n" \
"v_add_f32 v29, v29, v33\n" \
"v_add_f32 v30, v30, v34\n" \
"v_add_f32 v31, v31, v35\n" \
"v_cvt_pk_bf16_f32 v68, v28, v29\n" \
"v_cvt_pk_bf16_f32 v69, v30, v31\n" \
"v_permlane32_swap_b32 v36, v40\n" \
"v_permlane32_swap_b32 v37, v41\n" \
"v_permlane32_swap_b32 v38, v42\n" \
"v_permlane32_swap_b32 v39, v43\n" \
"v_add_f32 v36, v36, v40\n" \
"v_add_f32 v37, v37, v41\n" \
"v_add_f32 v38, v38, v42\n" \
"v_add_f32 v39, v39, v43\n" \
"v_cvt_pk_bf16_f32 v70, v36, v37\n" \
"v_cvt_pk_bf16_f32 v71, v38, v39\n" \
"v_mov_b32 v48, v68\n" \
"v_mov_b32 v49, v69\n" \
"v_mov_b32 v54, v70\n" \
"v_mov_b32 v55, v71\n" \
"s_nop 1\n" \
"v_permlane16_swap_b32 v48, v54\n" \
"v_permlane16_swap_b32 v49, v55\n" \
"s_nop 0\n" \
"v_mov_b32 v245, v245\n" \
"s_nop 2\n" \
"v_mov_b32 v245, v245\n" \
"s_nop 2\n" \
"v_mov_b32 v245, v245\n" \
"s_nop 2\n" \
"v_mov_b32 v245, v245\n" \
"s_nop 2\n" \
"v_mov_b32 v245, v245\n" \
"s_nop 2\n" \
"v_mov_b32 v245, v245\n" \
".LP0_wait:\n" \
"s_waitcnt vmcnt(1)\n" \
"v_pk_add_f32 v[72:73], v[56:57], v[60:61]\n" \
"v_pk_add_f32 v[74:75], v[58:59], v[62:63]\n" \
"v_max3_f32 v77, v72, v73, v64\n" \
"v_max3_f32 v77, v77, v74, v75\n" \
"s_nop 1\n" \
"v_max_f32_dpp v76, v77, v77 row_ror:8 row_mask:0xf bank_mask:0xf\n" \
"v_pk_add_f32 v[72:73], v[72:73], v[76:77] op_sel_hi:[1,0] neg_lo:[0,1] neg_hi:[0,1]\n" \
"v_pk_add_f32 v[74:75], v[74:75], v[76:77] op_sel_hi:[1,0] neg_lo:[0,1] neg_hi:[0,1]\n" \
"v_sub_f32 v78, v64, v76\n" \
"v_exp_f32 v72, v72\n" \
"v_exp_f32 v73, v73\n" \
"v_exp_f32 v74, v74\n" \
"v_exp_f32 v75, v75\n" \
"v_exp_f32 v78, v78\n" \
"v_pk_add_f32 v[80:81], v[72:73], v[74:75]\n" \
"v_fma_f32 v80, v78, v65, v80\n" \
"v_add_f32 v81, v80, v81\n" \
"s_nop 1\n" \
"v_add_f32_dpp v80, v81, v81 row_ror:8 row_mask:0xf bank_mask:0xf\n" \
"v_rcp_f32 v80, v80\n" \
"s_nop 0\n" \
"v_mul_f32 v78, v78, v80\n" \
"v_pk_mul_f32 v[72:73], v[72:73], v[80:81] op_sel_hi:[1,0]\n" \
"v_pk_mul_f32 v[74:75], v[74:75], v[80:81] op_sel_hi:[1,0]\n" \
"v_cvt_pk_bf16_f32 v78, v78, v78\n" \
"v_cvt_pk_bf16_f32 v82, v72, v73\n" \
"v_cvt_pk_bf16_f32 v83, v74, v75\n" \
"v_and_b32 v84, v78, v214\n" \
"v_and_b32 v85, v78, v215\n" \
"v_cvt_pk_bf16_f32 v86, v72, v73\n" \
"s_waitcnt vmcnt(0)\n" \
"v_mfma_f32_16x16x32_bf16 v[88:91], v[48:51], v[82:85], 0\n" \
"v_cvt_pk_bf16_f32 v87, v74, v75\n" \
"s_nop 1\n" \
"v_mfma_f32_16x16x32_bf16 v[92:95], v[52:55], v[84:87], 0\n" \
"s_bitcmp1_b32 s31, 21\n" \
"s_cselect_b32 s18, s29, 0\n" \
"s_xor_b32 s18, s18, s48\n" \
"v_mov_b32 v245, v245\n" \
"s_xor_b32 s19, s18, s29\n" \
"s_nop 0\n" \
"s_nop 0\n" \
"v_mov_b32 v245, v245\n" \
"v_add_f32_dpp v96, v92, v88 row_ror:8 row_mask:0xf bank_mask:0x3\n" \
"v_add_f32_dpp v97, v93, v89 row_ror:8 row_mask:0xf bank_mask:0x3\n" \
"v_add_f32_dpp v96, v90, v94 row_ror:8 row_mask:0xf bank_mask:0xc\n" \
"v_add_f32_dpp v97, v91, v95 row_ror:8 row_mask:0xf bank_mask:0xc\n" \
"ds_write_b64 v216, v[96:97]\n" \
"v_cmp_u_f32 vcc, v96, v97\n" \
"s_cbranch_vccnz .LP0_cls\n" \
".LP0_clsr:\n" \
"s_barrier\n" \
"ds_read_b64 v[100:101], v217 offset:0\n" \
"ds_read_b64 v[102:103], v217 offset:512\n" \
"ds_read_b64 v[104:105], v217 offset:1024\n" \
"ds_read_b64 v[106:107], v217 offset:1536\n" \
"ds_read_b32 v116, v252 offset:16\n" \
"s_add_u32 s40, s40, 1\n" \
"s_add_u32 s24, s24, s27\n" \
"s_mov_b32 s47, s48\n" \
"s_add_u32 s43, s40, 1\n" \
"s_and_b32 s43, s43, 3\n" \
"s_bitcmp1_b32 s43, 0\n" \
"s_cselect_b32 s48, s32, s31\n" \
"s_lshl_b32 s43, s43, 19\n" \
"s_waitcnt lgkmcnt(3)\n" \
"v_pk_add_f32 v[100:101], v[100:101], v[102:103]\n" \
"s_waitcnt lgkmcnt(0)\n" \
"v_pk_add_f32 v[104:105], v[104:105], v[106:107]\n" \
"v_pk_add_f32 v[100:101], v[100:101], v[104:105]\n" \
"v_xad_u32 v117, v116, s18, v213\n" \
"v_cvt_pk_bf16_f32 v96, v100, v101\n" \
"buffer_store_dword v96, v117, s[4:7], 0 offen sc1\n" \
"buffer_store_dword v96, v117, s[4:7], s52 offen sc1\n" \
"buffer_store_dword v96, v117, s[4:7], s53 offen sc1\n" \
"buffer_store_dword v96, v117, s[4:7], s54 offen sc1\n" \
"v_xad_u32 v116, v116, s19, v213\n" \
"s_add_u32 s48, s48, s43\n" \
"s_add_u32 s43, s40, 2\n" \
"s_and_b32 s43, s43, 3\n" \
"s_bitcmp1_b32 s43, 0\n" \
"s_cselect_b32 s49, s32, s31\n" \
"s_lshl_b32 s43, s43, 19\n" \
"s_add_u32 s49, s49, s43\n" \
"s_sub_u32 s43, s24, s44\n" \
"s_add_u32 s43, s43, s28\n" \
"s_max_i32 s43, s43, 0xfffd0000\n" \
"s_min_i32 s43, s43, 0x4000\n" \
"s_mul_i32 s43, s43, s46\n" \
"s_ashr_i32 s43, s43, 12\n" \
"s_add_u32 s41, s45, s43\n" \
"s_lshr_b32 s35, s42, 1\n" \
"s_sub_u32 s35, 0, s35\n" \
"buffer_store_dword v96, v116, s[4:7], 0 offen sc1\n" \
"buffer_store_dword v96, v116, s[4:7], s52 offen sc1\n" \
"buffer_store_dword v96, v116, s[4:7], s53 offen sc1\n" \
"buffer_store_dword v96, v116, s[4:7], s54 offen sc1\n" \
"s_cmp_eq_u32 s40, 0\n" \
"s_cbranch_scc1 .LP0_wet\n" \
"s_branch .LP0_loop\n" \
"s_branch .LP0_y1\n" \
".LP0_retry2:\n" \
"s_add_u32 s51, s51, 1\n" \
"s_cmp_gt_u32 s51, 0x20000\n" \
"s_cbranch_scc1 .LP0_end\n" \
".LP0_y0:\n" \
"s_add_u32 s41, s41, s42\n" \
"s_add_u32 s24, s24, s25\n" \
"s_xor_b32 s31, s31, s29\n" \
"s_xor_b32 s32, s32, s29\n" \
"s_xor_b32 s47, s47, s29\n" \
"s_xor_b32 s48, s48, s29\n" \
"s_add_u32 s39, s39, 1\n" \
"buffer_store_dwordx4 v[112:115], v223, s[4:7], s48 offen sc1\n" \
"s_bitcmp1_b32 s31, 21\n" \
"s_cselect_b32 s43, s29, 0\n" \
"v_mov_b32 v116, s43\n" \
"ds_write_b32 v252, v116 offset:16\n" \
".LP0_y1:\n" \
"s_memtime vcc\n" \
"s_waitcnt lgkmcnt(0)\n" \
"s_sub_u32 s43, s41, vcc_lo\n" \
"s_cmp_gt_i32 s43, 160\n" \
"s_cbranch_scc1 .LP0_y1\n" \
"s_cmp_lt_i32 s43, s37\n" \
"s_cbranch_scc1 .LP0_y0\n" \
"buffer_load_dwordx2 v[64:65], v211, s[4:7], s47 offen sc1\n" \
".p2align 6\n" \
".LP0_c1:\n" \
"s_memtime vcc\n" \
"s_waitcnt lgkmcnt(0)\n" \
"s_sub_u32 s43, s41, vcc_lo\n" \
"s_cmp_gt_i32 s43, 128\n" \
"s_cbranch_scc1 .LP0_c1\n" \
"s_cmp_lt_i32 s43, s37\n" \
"s_cbranch_scc1 .LP0_y0\n" \
"s_max_i32 s43, s43, 0\n" \
"s_and_b32 s43, s43, -4\n" \
"s_getpc_b64 vcc\n" \
".LP0_c3:\n" \
"s_sub_u32 s43, .LP0_c4-.LP0_c3, s43\n" \
"s_add_u32 vcc_lo, vcc_lo, s43\n" \
"s_addc_u32 vcc_hi, vcc_hi, 0\n" \
"s_setpc_b64 vcc\n" \
".rept 32\n" \
"s_nop 0\n" \
".endr\n" \
".LP0_c4:\n" \
"s_nop 2\n" \
"v_mov_b32 v245, v245\n" \
"buffer_load_dwordx4 v[56:59], v210, s[4:7], s47 offen sc1\n" \
"buffer_load_dwordx4 v[60:63], v210, s[4:7], s47 offen offset:256 sc1\n" \
"s_nop 2\n" \
"v_mov_b32 v245, v245\n" \
"s_nop 2\n" \
"v_mov_b32 v245, v245\n" \
"s_nop 2\n" \
"v_mov_b32 v245, v245\n" \
"s_nop 2\n" \
"v_mov_b32 v245, v245\n" \
"buffer_load_dwordx4 v[50:53], v212, s[4:7], s47 offen sc1\n" \
"s_nop 2\n" \
"v_mov_b32 v245, v245\n" \
"s_nop 2\n" \
"v_mov_b32 v245, v245\n" \
"s_nop 2\n" \
"v_mov_b32 v245, v245\n" \
"s_nop 2\n" \
"v_mov_b32 v245, v245\n" \
"s_nop 2\n" \
"v_mov_b32 v245, v245\n" \
"s_nop 2\n" \
"v_mov_b32 v245, v245\n" \
"s_nop 2\n" \
"v_mov_b32 v245, v245\n" \
"s_nop 2\n" \
"v_mov_b32 v245, v245\n" \
"s_nop 2\n" \
"v_mov_b32 v245, v245\n" \
"s_nop 2\n" \
"v_mov_b32 v245, v245\n" \
"s_nop 2\n" \
"v_mov_b32 v245, v245\n" \
"s_nop 2\n" \
"v_mov_b32 v245, v245\n" \
"s_nop 2\n" \
"v_mov_b32 v245, v245\n" \
"s_nop 2\n" \
"v_mov_b32 v245, v245\n" \
"s_nop 2\n" \
"v_mov_b32 v245, v245\n" \
"s_nop 2\n" \
"v_mov_b32 v245, v245\n" \
"s_nop 2\n" \
"v_mov_b32 v245, v245\n" \
"s_nop 2\n" \
"v_mov_b32 v245, v245\n" \
"s_nop 0\n" \
"v_mov_b32 v245, v245\n" \
"s_nop 2\n" \
"v_mov_b32 v245, v245\n" \
"s_nop 2\n" \
"v_mov_b32 v245, v245\n" \
"s_nop 2\n" \
"v_mov_b32 v245, v245\n" \
"s_nop 2\n" \
"v_mov_b32 v245, v245\n" \
"s_nop 2\n" \
"v_mov_b32 v245, v245\n" \
"s_branch .LP0_wait\n" \
".LP0_cls:\n" \
"s_waitcnt vmcnt(0)\n" \
"v_max3_u32 v44, v64, v64, v65\n" \
"v_max3_u32 v44, v44, v50, v51\n" \
"v_max3_u32 v44, v44, v52, v53\n" \
"v_cmp_eq_u32 vcc, -1, v44\n" \
"s_cbranch_vccnz .LP0_retry2\n" \
"v_cmp_u_f32 vcc, v56, v56\n" \
"v_cmp_ne_u32 s[56:57], v56, v109\n" \
"s_and_b64 vcc, vcc, s[56:57]\n" \
"s_cbranch_vccnz .LP0_retry2\n" \
"v_cmp_u_f32 vcc, v57, v57\n" \
"v_cmp_ne_u32 s[56:57], v57, v109\n" \
"s_and_b64 vcc, vcc, s[56:57]\n" \
"s_cbranch_vccnz .LP0_retry2\n" \
"v_cmp_u_f32 vcc, v58, v58\n" \
"v_cmp_ne_u32 s[56:57], v58, v109\n" \
"s_and_b64 vcc, vcc, s[56:57]\n" \
"s_cbranch_vccnz .LP0_retry2\n" \
"v_cmp_u_f32 vcc, v59, v59\n" \
"v_cmp_ne_u32 s[56:57], v59, v109\n" \
"s_and_b64 vcc, vcc, s[56:57]\n" \
"s_cbranch_vccnz .LP0_retry2\n" \
"v_cmp_u_f32 vcc, v60, v60\n" \
"v_cmp_ne_u32 s[56:57], v60, v109\n" \
"s_and_b64 vcc, vcc, s[56:57]\n" \
"s_cbranch_vccnz .LP0_retry2\n" \
"v_cmp_u_f32 vcc, v61, v61\n" \
"v_cmp_ne_u32 s[56:57], v61, v109\n" \
"s_and_b64 vcc, vcc, s[56:57]\n" \
"s_cbranch_vccnz .LP0_retry2\n" \
"v_cmp_u_f32 vcc, v62, v62\n" \
"v_cmp_ne_u32 s[56:57], v62, v109\n" \
"s_and_b64 vcc, vcc, s[56:57]\n" \
"s_cbranch_vccnz .LP0_retry2\n" \
"v_cmp_u_f32 vcc, v63, v63\n" \
"v_cmp_ne_u32 s[56:57], v63, v109\n" \
"s_and_b64 vcc, vcc, s[56:57]\n" \
"s_cbranch_vccnz .LP0_retry2\n" \
"s_branch .LP0_clsr\n" \
".LP0_last:\n" \
"v_smfmac_f32_16x16x64_bf16 v[28:31], v[16:19], a[32:39], v200\n" \
"v_smfmac_f32_16x16x64_bf16 v[32:35], v[16:19], a[88:95], v200\n" \
"v_smfmac_f32_16x16x64_bf16 v[36:39], v[16:19], a[144:151], v200\n" \
"v_smfmac_f32_16x16x64_bf16 v[40:43], v[16:19], a[200:207], v200\n" \
"v_smfmac_f32_16x16x64_bf16 v[28:31], v[20:23], a[40:47], v200\n" \
"v_smfmac_f32_16x16x64_bf16 v[32:35], v[20:23], a[96:103], v200\n" \
"v_smfmac_f32_16x16x64_bf16 v[36:39], v[20:23], a[152:159], v200\n" \
"v_smfmac_f32_16x16x64_bf16 v[40:43], v[20:23], a[208:215], v200\n" \
"v_smfmac_f32_16x16x64_bf16 v[28:31], v[24:27], a[48:55], v200\n" \
"v_smfmac_f32_16x16x64_bf16 v[32:35], v[24:27], a[104:111], v200\n" \
"v_smfmac_f32_16x16x64_bf16 v[36:39], v[24:27], a[160:167], v200\n" \
"v_smfmac_f32_16x16x64_bf16 v[40:43], v[24:27], a[216:223], v200\n" \
"s_waitcnt vmcnt(0)\n" \
"s_nop 7\n" \
"v_permlane32_swap_b32 v28, v32\n" \
"v_permlane32_swap_b32 v29, v33\n" \
"v_permlane32_swap_b32 v30, v34\n" \
"v_permlane32_swap_b32 v31, v35\n" \
"v_add_f32 v28, v28, v32\n" \
"v_add_f32 v29, v29, v33\n" \
"v_add_f32 v30, v30, v34\n" \
"v_add_f32 v31, v31, v35\n" \
"v_cvt_pk_bf16_f32 v68, v28, v29\n" \
"v_cvt_pk_bf16_f32 v69, v30, v31\n" \
"v_permlane32_swap_b32 v36, v40\n" \
"v_permlane32_swap_b32 v37, v41\n" \
"v_permlane32_swap_b32 v38, v42\n" \
"v_permlane32_swap_b32 v39, v43\n" \
"v_add_f32 v36, v36, v40\n" \
"v_add_f32 v37, v37, v41\n" \
"v_add_f32 v38, v38, v42\n" \
"v_add_f32 v39, v39, v43\n" \
"v_cvt_pk_bf16_f32 v70, v36, v37\n" \
"v_cvt_pk_bf16_f32 v71, v38, v39\n" \
"s_mov_b32 s50, 262144\n" \
"buffer_store_dwordx4 v[68:71], v218, s[12:15], s50 offen sc1\n" \
"s_waitcnt vmcnt(0)\n" \
"s_barrier\n" \
"s_add_u32 s43, s30, s40\n" \
"s_add_u32 s43, s43, 1\n" \
"v_mov_b32 v241, s43\n" \
"buffer_store_dword v241, v221, s[4:7], 0 offen sc1\n" \
".LP0_end:\n" \
"s_endpgm\n" \
".p2align 6\n" \
"fk_role_P1:\n" \
"ds_read_b32 v200, v255 offset:0\n" \
"ds_read_b32 v201, v255 offset:256\n" \
"ds_read_b32 v202, v255 offset:512\n" \
"ds_read_b32 v203, v255 offset:768\n" \
"ds_read_b32 v204, v255 offset:1024\n" \
"ds_read_b32 v205, v255 offset:1280\n" \
"ds_read_b32 v206, v255 offset:1536\n" \
"ds_read_b32 v207, v255 offset:1792\n" \
"ds_read_b32 v208, v255 offset:2048\n" \
"ds_read_b32 v209, v255 offset:2304\n" \
"ds_read_b32 v210, v255 offset:2560\n" \
"ds_read_b32 v211, v255 offset:2816\n" \
"ds_read_b32 v212, v255 offset:3072\n" \
"ds_read_b32 v213, v255 offset:3328\n" \
"ds_read_b32 v214, v255 offset:3584\n" \
"ds_read_b32 v215, v255 offset:3840\n" \
"ds_read_b32 v216, v255 offset:4096\n" \
"ds_read_b32 v217, v255 offset:4352\n" \
"ds_read_b32 v218, v255 offset:4608\n" \
"ds_read_b32 v219, v255 offset:4864\n" \
"ds_read_b32 v220, v255 offset:5120\n" \
"ds_read_b32 v221, v255 offset:5376\n" \
"ds_read_b32 v222, v255 offset:5632\n" \
"ds_read_b32 v223, v255 offset:5888\n" \
"ds_read_b32 v254, v255 offset:7936\n" \
"s_waitcnt lgkmcnt(0)\n" \
"v_readlane_b32 s4, v254, 0\n" \
"v_readlane_b32 s5, v254, 1\n" \
"v_readlane_b32 s6, v254, 2\n" \
"v_readlane_b32 s7, v254, 3\n" \
"v_readlane_b32 s8, v254, 4\n" \
"v_readlane_b32 s9, v254, 5\n" \
"v_readlane_b32 s10, v254, 6\n" \
"v_readlane_b32 s11, v254, 7\n" \
"v_readlane_b32 s12, v254, 8\n" \
"v_readlane_b32 s13, v254, 9\n" \
"v_readlane_b32 s14, v254, 10\n" \
"v_readlane_b32 s15, v254, 11\n" \
"v_readlane_b32 s16, v254, 12\n" \
"v_readlane_b32 s17, v254, 13\n" \
"v_readlane_b32 s18, v254, 14\n" \
"v_readlane_b32 s19, v254, 15\n" \
"v_readlane_b32 s20, v254, 16\n" \
"v_readlane_b32 s21, v254, 17\n" \
"v_readlane_b32 s22, v254, 18\n" \
"v_readlane_b32 s23, v254, 19\n" \
"v_readlane_b32 s24, v254, 20\n" \
"v_readlane_b32 s25, v254, 21\n" \
"v_readlane_b32 s26, v254, 22\n" \
"v_readlane_b32 s27, v254, 23\n" \
"v_readlane_b32 s28, v254, 24\n" \
"v_readlane_b32 s29, v254, 25\n" \
"v_readlane_b32 s30, v254, 26\n" \
"v_readlane_b32 s31, v254, 27\n" \
"v_readlane_b32 s32, v254, 28\n" \
"v_readlane_b32 s33, v254, 29\n" \
"v_readlane_b32 s34, v254, 30\n" \
"v_readlane_b32 s35, v254, 31\n" \
"v_readlane_b32 s36, v254, 32\n" \
"v_readlane_b32 s37, v254, 33\n" \
"v_readlane_b32 s38, v254, 34\n" \
"v_readlane_b32 s39, v254, 35\n" \
"s_mov_b32 s40, 0\n" \
"s_mov_b32 s51, 0\n" \
"s_nop 0\n" \
"v_mov_b32 v252, s38\n" \
"s_nop 4\n" \
"v_mov_b32 v112, -1\n" \
"v_mov_b32 v113, -1\n" \
"v_mov_b32 v114, -1\n" \
"v_mov_b32 v115, -1\n" \
"v_mov_b32 v109, 0x7fc00001\n" \
"s_mov_b32 s52, 0x8000\n" \
"s_mov_b32 s53, 0x4000\n" \
"s_mov_b32 s54, 0xc000\n" \
"v_writelane_b32 v251, s6, 0\n" \
"v_writelane_b32 v251, s10, 1\n" \
"v_writelane_b32 v251, s14, 2\n" \
"v_writelane_b32 v251, s18, 3\n" \
"s_mov_b32 s6, 0\n" \
"s_mov_b32 s10, 0\n" \
"s_mov_b32 s14, 0\n" \
"s_mov_b32 s18, 0\n" \
"s_mov_b32 s40, -1\n" \
"s_branch .LP1_pre\n" \
".LP1_wet:\n" \
"s_waitcnt vmcnt(0)\n" \
"v_readlane_b32 s6, v251, 0\n" \
"v_readlane_b32 s10, v251, 1\n" \
"v_readlane_b32 s14, v251, 2\n" \
"v_readlane_b32 s18, v251, 3\n" \
"s_nop 4\n" \
"s_mov_b32 s33, 20\n" \
"s_mov_b32 s34, 0\n" \
"s_add_u32 s43, s30, 1\n" \
"s_mov_b32 s50, 0\n" \
".LP1_w29l:\n" \
"buffer_load_dword v240, v204, s[4:7], 0 offen sc1\n" \
"s_waitcnt vmcnt(0)\n" \
".LP1_w29c:\n" \
"v_subrev_u32 v241, s43, v240\n" \
"v_cmp_gt_u32 vcc, 0x100, v241\n" \
"s_cmp_eq_u64 vcc, -1\n" \
"s_cbranch_scc1 .LP1_w29d\n" \
"s_sleep 2\n" \
"s_add_u32 s50, s50, 1\n" \
"s_cmp_lt_u32 s50, 0x20000\n" \
"s_cbranch_scc1 .LP1_w29l\n" \
"s_branch .LP1_end\n" \
".LP1_w29d:\n" \
"v_readlane_b32 s43, v202, 4\n" \
"s_nop 4\n" \
"buffer_load_dwordx4 a[32:35], v201, s[8:11], s43 offen sc1\n" \
"buffer_load_dwordx4 a[36:39], v201, s[8:11], s43 offen offset:1024 sc1\n" \
"v_readlane_b32 s43, v202, 11\n" \
"s_nop 4\n" \
"buffer_load_dwordx4 a[88:91], v201, s[8:11], s43 offen sc1\n" \
"buffer_load_dwordx4 a[92:95], v201, s[8:11], s43 offen offset:1024 sc1\n" \
"v_readlane_b32 s43, v202, 18\n" \
"s_nop 4\n" \
"buffer_load_dwordx4 a[144:147], v201, s[8:11], s43 offen sc1\n" \
"buffer_load_dwordx4 a[148:151], v201, s[8:11], s43 offen offset:1024 sc1\n" \
"v_readlane_b32 s43, v202, 25\n" \
"s_nop 4\n" \
"buffer_load_dwordx4 a[200:203], v201, s[8:11], s43 offen sc1\n" \
"buffer_load_dwordx4 a[204:207], v201, s[8:11], s43 offen offset:1024 sc1\n" \
"s_add_u32 s43, s30, 2\n" \
"s_mov_b32 s50, 0\n" \
"s_branch .LP1_w30c\n" \
".LP1_w30l:\n" \
"buffer_load_dword v240, v204, s[4:7], 0 offen sc1\n" \
"s_waitcnt vmcnt(0)\n" \
".LP1_w30c:\n" \
"v_subrev_u32 v241, s43, v240\n" \
"v_cmp_gt_u32 vcc, 0x100, v241\n" \
"s_cmp_eq_u64 vcc, -1\n" \
"s_cbranch_scc1 .LP1_w30d\n" \
"s_sleep 2\n" \
"s_add_u32 s50, s50, 1\n" \
"s_cmp_lt_u32 s50, 0x20000\n" \
"s_cbranch_scc1 .LP1_w30l\n" \
"s_branch .LP1_end\n" \
".LP1_w30d:\n" \
"v_readlane_b32 s43, v202, 1\n" \
"s_nop 4\n" \
"buffer_load_dwordx4 a[8:11], v201, s[8:11], s43 offen sc1\n" \
"buffer_load_dwordx4 a[12:15], v201, s[8:11], s43 offen offset:1024 sc1\n" \
"v_readlane_b32 s43, v202, 8\n" \
"s_nop 4\n" \
"buffer_load_dwordx4 a[64:67], v201, s[8:11], s43 offen sc1\n" \
"buffer_load_dwordx4 a[68:71], v201, s[8:11], s43 offen offset:1024 sc1\n" \
"v_readlane_b32 s43, v202, 15\n" \
"s_nop 4\n" \
"buffer_load_dwordx4 a[120:123], v201, s[8:11], s43 offen sc1\n" \
"buffer_load_dwordx4 a[124:127], v201, s[8:11], s43 offen offset:1024 sc1\n" \
"v_readlane_b32 s43, v202, 22\n" \
"s_nop 4\n" \
"buffer_load_dwordx4 a[176:179], v201, s[8:11], s43 offen sc1\n" \
"buffer_load_dwordx4 a[180:183], v201, s[8:11], s43 offen offset:1024 sc1\n" \
"s_add_u32 s43, s30, 3\n" \
"s_mov_b32 s50, 0\n" \
"s_branch .LP1_w31c\n" \
".LP1_w31l:\n" \
"buffer_load_dword v240, v204, s[4:7], 0 offen sc1\n" \
"s_waitcnt vmcnt(0)\n" \
".LP1_w31c:\n" \
"v_subrev_u32 v241, s43, v240\n" \
"v_cmp_gt_u32 vcc, 0x100, v241\n" \
"s_cmp_eq_u64 vcc, -1\n" \
"s_cbranch_scc1 .LP1_w31d\n" \
"s_sleep 2\n" \
"s_add_u32 s50, s50, 1\n" \
"s_cmp_lt_u32 s50, 0x20000\n" \
"s_cbranch_scc1 .LP1_w31l\n" \
"s_branch .LP1_end\n" \
".LP1_w31d:\n" \
"v_readlane_b32 s43, v202, 5\n" \
"s_nop 4\n" \
"buffer_load_dwordx4 a[40:43], v201, s[8:11], s43 offen sc1\n" \
"buffer_load_dwordx4 a[44:47], v201, s[8:11], s43 offen offset:1024 sc1\n" \
"v_readlane_b32 s43, v202, 12\n" \
"s_nop 4\n" \
"buffer_load_dwordx4 a[96:99], v201, s[8:11], s43 offen sc1\n" \
"buffer_load_dwordx4 a[100:103], v201, s[8:11], s43 offen offset:1024 sc1\n" \
"v_readlane_b32 s43, v202, 19\n" \
"s_nop 4\n" \
"buffer_load_dwordx4 a[152:155], v201, s[8:11], s43 offen sc1\n" \
"buffer_load_dwordx4 a[156:159], v201, s[8:11], s43 offen offset:1024 sc1\n" \
"v_readlane_b32 s43, v202, 26\n" \
"s_nop 4\n" \
"buffer_load_dwordx4 a[208:211], v201, s[8:11], s43 offen sc1\n" \
"buffer_load_dwordx4 a[212:215], v201, s[8:11], s43 offen offset:1024 sc1\n" \
"s_add_u32 s43, s30, 4\n" \
"s_mov_b32 s50, 0\n" \
"s_branch .LP1_w32c\n" \
".LP1_w32l:\n" \
"buffer_load_dword v240, v204, s[4:7], 0 offen sc1\n" \
"s_waitcnt vmcnt(0)\n" \
".LP1_w32c:\n" \
"v_subrev_u32 v241, s43, v240\n" \
"v_cmp_gt_u32 vcc, 0x100, v241\n" \
"s_cmp_eq_u64 vcc, -1\n" \
"s_cbranch_scc1 .LP1_w32d\n" \
"s_sleep 2\n" \
"s_add_u32 s50, s50, 1\n" \
"s_cmp_lt_u32 s50, 0x20000\n" \
"s_cbranch_scc1 .LP1_w32l\n" \
"s_branch .LP1_end\n" \
".LP1_w32d:\n" \
"v_readlane_b32 s43, v202, 2\n" \
"s_nop 4\n" \
"buffer_load_dwordx4 a[16:19], v201, s[8:11], s43 offen sc1\n" \
"buffer_load_dwordx4 a[20:23], v201, s[8:11], s43 offen offset:1024 sc1\n" \
"v_readlane_b32 s43, v202, 9\n" \
"s_nop 4\n" \
"buffer_load_dwordx4 a[72:75], v201, s[8:11], s43 offen sc1\n" \
"buffer_load_dwordx4 a[76:79], v201, s[8:11], s43 offen offset:1024 sc1\n" \
"v_readlane_b32 s43, v202, 16\n" \
"s_nop 4\n" \
"buffer_load_dwordx4 a[128:131], v201, s[8:11], s43 offen sc1\n" \
"buffer_load_dwordx4 a[132:135], v201, s[8:11], s43 offen offset:1024 sc1\n" \
"v_readlane_b32 s43, v202, 23\n" \
"s_nop 4\n" \
"buffer_load_dwordx4 a[184:187], v201, s[8:11], s43 offen sc1\n" \
"buffer_load_dwordx4 a[188:191], v201, s[8:11], s43 offen offset:1024 sc1\n" \
"s_add_u32 s43, s30, 5\n" \
"s_mov_b32 s50, 0\n" \
"s_branch .LP1_w33c\n" \
".LP1_w33l:\n" \
"buffer_load_dword v240, v204, s[4:7], 0 offen sc1\n" \
"s_waitcnt vmcnt(0)\n" \
".LP1_w33c:\n" \
"v_subrev_u32 v241, s43, v240\n" \
"v_cmp_gt_u32 vcc, 0x100, v241\n" \
"s_cmp_eq_u64 vcc, -1\n" \
"s_cbranch_scc1 .LP1_w33d\n" \
"s_sleep 2\n" \
"s_add_u32 s50, s50, 1\n" \
"s_cmp_lt_u32 s50, 0x20000\n" \
"s_cbranch_scc1 .LP1_w33l\n" \
"s_branch .LP1_end\n" \
".LP1_w33d:\n" \
"v_readlane_b32 s43, v202, 0\n" \
"s_nop 4\n" \
"buffer_load_dwordx4 a[0:3], v201, s[8:11], s43 offen sc1\n" \
"buffer_load_dwordx4 a[4:7], v201, s[8:11], s43 offen offset:1024 sc1\n" \
"v_readlane_b32 s43, v202, 7\n" \
"s_nop 4\n" \
"buffer_load_dwordx4 a[56:59], v201, s[8:11], s43 offen sc1\n" \
"buffer_load_dwordx4 a[60:63], v201, s[8:11], s43 offen offset:1024 sc1\n" \
"v_readlane_b32 s43, v202, 14\n" \
"s_nop 4\n" \
"buffer_load_dwordx4 a[112:115], v201, s[8:11], s43 offen sc1\n" \
"buffer_load_dwordx4 a[116:119], v201, s[8:11], s43 offen offset:1024 sc1\n" \
"v_readlane_b32 s43, v202, 21\n" \
"s_nop 4\n" \
"buffer_load_dwordx4 a[168:171], v201, s[8:11], s43 offen sc1\n" \
"buffer_load_dwordx4 a[172:175], v201, s[8:11], s43 offen offset:1024 sc1\n" \
"s_add_u32 s43, s30, 6\n" \
"s_mov_b32 s50, 0\n" \
"s_branch .LP1_w34c\n" \
".LP1_w34l:\n" \
"buffer_load_dword v240, v204, s[4:7], 0 offen sc1\n" \
"s_waitcnt vmcnt(0)\n" \
".LP1_w34c:\n" \
"v_subrev_u32 v241, s43, v240\n" \
"v_cmp_gt_u32 vcc, 0x100, v241\n" \
"s_cmp_eq_u64 vcc, -1\n" \
"s_cbranch_scc1 .LP1_w34d\n" \
"s_sleep 2\n" \
"s_add_u32 s50, s50, 1\n" \
"s_cmp_lt_u32 s50, 0x20000\n" \
"s_cbranch_scc1 .LP1_w34l\n" \
"s_branch .LP1_end\n" \
".LP1_w34d:\n" \
"v_readlane_b32 s43, v202, 3\n" \
"s_nop 4\n" \
"buffer_load_dwordx4 a[24:27], v201, s[8:11], s43 offen sc1\n" \
"buffer_load_dwordx4 a[28:31], v201, s[8:11], s43 offen offset:1024 sc1\n" \
"v_readlane_b32 s43, v202, 10\n" \
"s_nop 4\n" \
"buffer_load_dwordx4 a[80:83], v201, s[8:11], s43 offen sc1\n" \
"buffer_load_dwordx4 a[84:87], v201, s[8:11], s43 offen offset:1024 sc1\n" \
"v_readlane_b32 s43, v202, 17\n" \
"s_nop 4\n" \
"buffer_load_dwordx4 a[136:139], v201, s[8:11], s43 offen sc1\n" \
"buffer_load_dwordx4 a[140:143], v201, s[8:11], s43 offen offset:1024 sc1\n" \
"v_readlane_b32 s43, v202, 24\n" \
"s_nop 4\n" \
"buffer_load_dwordx4 a[192:195], v201, s[8:11], s43 offen sc1\n" \
"buffer_load_dwordx4 a[196:199], v201, s[8:11], s43 offen offset:1024 sc1\n" \
"s_add_u32 s43, s30, 7\n" \
"s_mov_b32 s50, 0\n" \
"s_branch .LP1_w35c\n" \
".LP1_w35l:\n" \
"buffer_load_dword v240, v204, s[4:7], 0 offen sc1\n" \
"s_waitcnt vmcnt(0)\n" \
".LP1_w35c:\n" \
"v_subrev_u32 v241, s43, v240\n" \
"v_cmp_gt_u32 vcc, 0x100, v241\n" \
"s_cmp_eq_u64 vcc, -1\n" \
"s_cbranch_scc1 .LP1_w35d\n" \
"s_sleep 2\n" \
"s_add_u32 s50, s50, 1\n" \
"s_cmp_lt_u32 s50, 0x20000\n" \
"s_cbranch_scc1 .LP1_w35l\n" \
"s_branch .LP1_end\n" \
".LP1_w35d:\n" \
"v_readlane_b32 s43, v202, 6\n" \
"s_nop 4\n" \
"buffer_load_dwordx4 a[48:51], v201, s[8:11], s43 offen sc1\n" \
"buffer_load_dwordx4 a[52:55], v201, s[8:11], s43 offen offset:1024 sc1\n" \
"v_readlane_b32 s43, v202, 13\n" \
"s_nop 4\n" \
"buffer_load_dwordx4 a[104:107], v201, s[8:11], s43 offen sc1\n" \
"buffer_load_dwordx4 a[108:111], v201, s[8:11], s43 offen offset:1024 sc1\n" \
"v_readlane_b32 s43, v202, 20\n" \
"s_nop 4\n" \
"buffer_load_dwordx4 a[160:163], v201, s[8:11], s43 offen sc1\n" \
"buffer_load_dwordx4 a[164:167], v201, s[8:11], s43 offen offset:1024 sc1\n" \
"v_readlane_b32 s43, v202, 27\n" \
"s_nop 4\n" \
"buffer_load_dwordx4 a[216:219], v201, s[8:11], s43 offen sc1\n" \
"buffer_load_dwordx4 a[220:223], v201, s[8:11], s43 offen offset:1024 sc1\n" \
"s_add_u32 s43, s30, 1\n" \
"s_mov_b32 s50, 0\n" \
".LP1_w36l:\n" \
"buffer_load_dword v240, v205, s[4:7], 0 offen sc1\n" \
"s_waitcnt vmcnt(0)\n" \
".LP1_w36c:\n" \
"v_subrev_u32 v241, s43, v240\n" \
"v_cmp_gt_u32 vcc, 0x100, v241\n" \
"s_cmp_eq_u64 vcc, -1\n" \
"s_cbranch_scc1 .LP1_w36d\n" \
"s_sleep 2\n" \
"s_add_u32 s50, s50, 1\n" \
"s_cmp_lt_u32 s50, 0x20000\n" \
"s_cbranch_scc1 .LP1_w36l\n" \
"s_branch .LP1_end\n" \
".LP1_w36d:\n" \
"v_readfirstlane_b32 s31, v241\n" \
"s_lshr_b32 s32, s31, 2\n" \
"s_xor_b32 s29, s31, s32\n" \
"s_and_b32 s29, s29, 3\n" \
"s_lshl_b32 s29, s29, 13\n" \
"s_bitset1_b32 s29, 21\n" \
"s_and_b32 s31, s31, 3\n" \
"s_lshl_b32 s31, s31, 13\n" \
"s_bitcmp1_b32 s39, 0\n" \
"s_cselect_b32 s43, s29, 0\n" \
"s_xor_b32 s31, s31, s43\n" \
"s_xor_b32 s32, s31, 0x4000\n" \
"s_mov_b32 s39, 0\n" \
"s_add_u32 s43, s40, 0\n" \
"s_and_b32 s43, s43, 3\n" \
"s_bitcmp1_b32 s43, 0\n" \
"s_cselect_b32 s47, s32, s31\n" \
"s_lshl_b32 s43, s43, 19\n" \
"s_add_u32 s47, s47, s43\n" \
".LP1_pre:\n" \
"v_mov_b32 v236, 1290\n" \
"v_mov_b32 v237, 1040\n" \
"v_mov_b32 v238, 1290\n" \
"v_mov_b32 v239, 1040\n" \
"s_cmp_eq_u32 s40, 0\n" \
"s_cselect_b32 s43, -1, 0\n" \
"v_and_b32 v236, s43, v236\n" \
"v_and_b32 v237, s43, v237\n" \
"v_and_b32 v238, s43, v238\n" \
"v_and_b32 v239, s43, v239\n" \
"s_barrier\n" \
"ds_read_b96 v[248:250], v252\n" \
"s_waitcnt lgkmcnt(0)\n" \
"v_readfirstlane_b32 s44, v248\n" \
"v_readfirstlane_b32 s45, v249\n" \
"v_readfirstlane_b32 s46, v250\n" \
"s_mul_i32 s42, s25, s46\n" \
"s_lshr_b32 s42, s42, 12\n" \
".LP1_cu:\n" \
"s_sub_u32 s43, s24, s44\n" \
"s_cmp_lt_i32 s43, 0\n" \
"s_cbranch_scc0 .LP1_cud\n" \
"s_add_u32 s24, s24, s25\n" \
"s_xor_b32 s31, s31, s29\n" \
"s_xor_b32 s32, s32, s29\n" \
"s_xor_b32 s47, s47, s29\n" \
"s_add_u32 s39, s39, 1\n" \
"s_branch .LP1_cu\n" \
".LP1_cud:\n" \
"s_mov_b32 s3, s46\n" \
"s_add_u32 s43, s40, 1\n" \
"s_and_b32 s43, s43, 3\n" \
"s_bitcmp1_b32 s43, 0\n" \
"s_cselect_b32 s48, s32, s31\n" \
"s_lshl_b32 s43, s43, 19\n" \
"s_add_u32 s48, s48, s43\n" \
"s_add_u32 s43, s40, 2\n" \
"s_and_b32 s43, s43, 3\n" \
"s_bitcmp1_b32 s43, 0\n" \
"s_cselect_b32 s49, s32, s31\n" \
"s_lshl_b32 s43, s43, 19\n" \
"s_add_u32 s49, s49, s43\n" \
"s_sub_u32 s43, s24, s44\n" \
"s_add_u32 s43, s43, s28\n" \
"s_max_i32 s43, s43, 0xfffd0000\n" \
"s_min_i32 s43, s43, 0x4000\n" \
"s_mul_i32 s43, s43, s46\n" \
"s_ashr_i32 s43, s43, 12\n" \
"s_add_u32 s41, s45, s43\n" \
"s_lshr_b32 s35, s42, 1\n" \
"s_sub_u32 s35, 0, s35\n" \
"v_max_u32 v236, v236, v238\n" \
"v_max_u32 v237, v237, v239\n" \
"v_and_b32 v236, 0xfff, v236\n" \
"v_and_b32 v237, 0xfff, v237\n" \
"s_mov_b64 exec, 1\n" \
"ds_write_b64 v252, v[236:237] offset:24\n" \
"s_mov_b64 exec, -1\n" \
".LP1_loop:\n" \
"s_branch .LP1_m1\n" \
".LP1_retry1:\n" \
"s_add_u32 s51, s51, 1\n" \
"s_cmp_gt_u32 s51, 0x20000\n" \
"s_cbranch_scc1 .LP1_end\n" \
".LP1_m0:\n" \
"s_add_u32 s41, s41, s42\n" \
"s_add_u32 s24, s24, s25\n" \
"s_xor_b32 s31, s31, s29\n" \
"s_xor_b32 s32, s32, s29\n" \
"s_xor_b32 s47, s47, s29\n" \
"s_xor_b32 s48, s48, s29\n" \
"s_add_u32 s39, s39, 1\n" \
"s_bitcmp1_b32 s31, 21\n" \
"s_cselect_b32 s43, s29, 0\n" \
"v_mov_b32 v116, s43\n" \
"ds_write_b32 v252, v116 offset:16\n" \
".LP1_m1:\n" \
"s_memtime vcc\n" \
"s_waitcnt lgkmcnt(0)\n" \
"s_sub_u32 s43, s41, vcc_lo\n" \
"s_cmp_gt_i32 s43, 0\n" \
"s_cbranch_scc1 .LP1_m1\n" \
"s_cmp_lt_i32 s43, s35\n" \
"s_cbranch_scc1 .LP1_m0\n" \
"buffer_load_dwordx4 v[0:3], v208, s[4:7], s47 offen offset:2048 sc1\n" \
"buffer_load_dwordx4 v[4:7], v209, s[4:7], s47 offen sc1\n" \
"s_nop 15\n" \
"s_nop 13\n" \
"s_mul_i32 s50, s40, 4096\n" \
"buffer_store_dwordx4 v[68:71], v218, s[12:15], s50 offen sc1\n" \
"buffer_store_dword v112, v213, s[4:7], s49 offen sc1\n" \
"s_xor_b32 s43, s49, s29\n" \
"buffer_store_dword v112, v213, s[4:7], s43 offen sc1\n" \
"s_lshl_b32 s43, s40, 7\n" \
"buffer_load_dwordx4 v[236:239], v207, s[4:7], s43 offen offset:128 sc1\n" \
"v_mov_b32 v28, 0\n" \
"v_mov_b32 v29, 0\n" \
"v_mov_b32 v30, 0\n" \
"v_mov_b32 v31, 0\n" \
"v_mov_b32 v32, 0\n" \
"v_mov_b32 v33, 0\n" \
"v_mov_b32 v34, 0\n" \
"v_mov_b32 v35, 0\n" \
"v_mov_b32 v36, 0\n" \
"v_mov_b32 v37, 0\n" \
"v_mov_b32 v38, 0\n" \
"v_mov_b32 v39, 0\n" \
"v_mov_b32 v40, 0\n" \
"v_mov_b32 v41, 0\n" \
"v_mov_b32 v42, 0\n" \
"v_mov_b32 v43, 0\n" \
"s_waitcnt vmcnt(5)\n" \
"ds_write_b128 v220, v[0:3] offset:2048\n" \
"s_waitcnt vmcnt(4)\n" \
"ds_write_b128 v220, v[4:7] offset:4096\n" \
"v_max3_u32 v44, v0, v0, v1\n" \
"v_max3_u32 v44, v44, v2, v3\n" \
"v_max3_u32 v44, v44, v4, v5\n" \
"v_max3_u32 v44, v44, v6, v7\n" \
"v_cmp_eq_u32 vcc, -1, v44\n" \
"v_smfmac_f32_16x16x64_bf16 v[28:31], v[0:3], a[0:7], v200\n" \
"v_smfmac_f32_16x16x64_bf16 v[32:35], v[0:3], a[56:63], v200\n" \
"s_cbranch_vccnz .LP1_retry1\n" \
"s_waitcnt lgkmcnt(0)\n" \
"s_barrier\n" \
"ds_read_b128 v[8:11], v220 offset:5120\n" \
"ds_read_b128 v[12:15], v220 offset:6144\n" \
"ds_read_b128 v[16:19], v220 offset:0\n" \
"ds_read_b128 v[20:23], v220 offset:1024\n" \
"ds_read_b128 v[24:27], v220 offset:3072\n" \
"ds_read_b64 v[246:247], v252 offset:24\n" \
"ds_read_b96 v[248:250], v252\n" \
"v_smfmac_f32_16x16x64_bf16 v[36:39], v[0:3], a[112:119], v200\n" \
"v_smfmac_f32_16x16x64_bf16 v[40:43], v[0:3], a[168:175], v200\n" \
"v_smfmac_f32_16x16x64_bf16 v[28:31], v[4:7], a[8:15], v200\n" \
"v_smfmac_f32_16x16x64_bf16 v[32:35], v[4:7], a[64:71], v200\n" \
"v_smfmac_f32_16x16x64_bf16 v[36:39], v[4:7], a[120:127], v200\n" \
"v_smfmac_f32_16x16x64_bf16 v[40:43], v[4:7], a[176:183], v200\n" \
"s_waitcnt lgkmcnt(6)\n" \
"v_smfmac_f32_16x16x64_bf16 v[28:31], v[8:11], a[16:23], v200\n" \
"v_smfmac_f32_16x16x64_bf16 v[32:35], v[8:11], a[72:79], v200\n" \
"v_smfmac_f32_16x16x64_bf16 v[36:39], v[8:11], a[128:135], v200\n" \
"v_smfmac_f32_16x16x64_bf16 v[40:43], v[8:11], a[184:191], v200\n" \
"s_waitcnt lgkmcnt(0)\n" \
"v_readfirstlane_b32 s44, v248\n" \
"v_readfirstlane_b32 s45, v249\n" \
"v_smfmac_f32_16x16x64_bf16 v[28:31], v[12:15], a[24:31], v200\n" \
"v_readfirstlane_b32 s46, v250\n" \
"v_readfirstlane_b32 s26, v246\n" \
"v_readfirstlane_b32 s27, v247\n" \
"v_smfmac_f32_16x16x64_bf16 v[32:35], v[12:15], a[80:87], v200\n" \
"s_mul_i32 s42, s25, s46\n" \
"s_lshr_b32 s42, s42, 12\n" \
"s_add_u32 s24, s24, s26\n" \
"v_smfmac_f32_16x16x64_bf16 v[36:39], v[12:15], a[136:143], v200\n" \
"s_sub_u32 s43, s24, s44\n" \
"s_max_i32 s43, s43, 0xfffd0000\n" \
"s_min_i32 s43, s43, 0x4000\n" \
"v_smfmac_f32_16x16x64_bf16 v[40:43], v[12:15], a[192:199], v200\n" \
"s_mul_i32 s43, s43, s46\n" \
"s_ashr_i32 s43, s43, 12\n" \
"s_add_u32 s41, s45, s43\n" \
"s_cmp_eq_u32 s40, 63\n" \
"s_cbranch_scc1 .LP1_last\n" \
"s_branch .LP1_z1\n" \
".LP1_z0:\n" \
"s_add_u32 s41, s41, s42\n" \
"s_add_u32 s24, s24, s25\n" \
"s_xor_b32 s31, s31, s29\n" \
"s_xor_b32 s32, s32, s29\n" \
"s_xor_b32 s47, s47, s29\n" \
"s_xor_b32 s48, s48, s29\n" \
"s_add_u32 s39, s39, 1\n" \
"s_bitcmp1_b32 s31, 21\n" \
"s_cselect_b32 s43, s29, 0\n" \
"v_mov_b32 v116, s43\n" \
"ds_write_b32 v252, v116 offset:16\n" \
".LP1_z1:\n" \
"s_memtime vcc\n" \
"s_waitcnt lgkmcnt(0)\n" \
"s_sub_u32 s43, s41, vcc_lo\n" \
"s_cmp_gt_i32 s43, 160\n" \
"s_cbranch_scc1 .LP1_z1\n" \
"s_cmp_lt_i32 s43, s37\n" \
"s_cbranch_scc1 .LP1_z0\n" \
"buffer_load_dwordx2 v[64:65], v211, s[4:7], s47 offen sc1\n" \
".p2align 6\n" \
".LP1_b1:\n" \
"s_memtime vcc\n" \
"s_waitcnt lgkmcnt(0)\n" \
"s_sub_u32 s43, s41, vcc_lo\n" \
"s_cmp_gt_i32 s43, 128\n" \
"s_cbranch_scc1 .LP1_b1\n" \
"s_cmp_lt_i32 s43, s37\n" \
"s_cbranch_scc1 .LP1_z0\n" \
"s_max_i32 s43, s43, 0\n" \
"s_and_b32 s43, s43, -4\n" \
"s_getpc_b64 vcc\n" \
".LP1_b3:\n" \
"s_sub_u32 s43, .LP1_b4-.LP1_b3, s43\n" \
"s_add_u32 vcc_lo, vcc_lo, s43\n" \
"s_addc_u32 vcc_hi, vcc_hi, 0\n" \
"s_setpc_b64 vcc\n" \
".rept 32\n" \
"s_nop 0\n" \
".endr\n" \
".LP1_b4:\n" \
"buffer_load_dwordx4 v[56:59], v210, s[4:7], s47 offen sc1\n" \
"buffer_load_dwordx4 v[60:63], v210, s[4:7], s47 offen offset:256 sc1\n" \
"v_smfmac_f32_16x16x64_bf16 v[28:31], v[16:19], a[32:39], v200\n" \
"v_smfmac_f32_16x16x64_bf16 v[32:35], v[16:19], a[88:95], v200\n" \
"v_smfmac_f32_16x16x64_bf16 v[36:39], v[16:19], a[144:151], v200\n" \
"v_smfmac_f32_16x16x64_bf16 v[40:43], v[16:19], a[200:207], v200\n" \
"buffer_load_dwordx4 v[50:53], v212, s[4:7], s47 offen sc1\n" \
"v_smfmac_f32_16x16x64_bf16 v[28:31], v[20:23], a[40:47], v200\n" \
"v_smfmac_f32_16x16x64_bf16 v[32:35], v[20:23], a[96:103], v200\n" \
"v_smfmac_f32_16x16x64_bf16 v[36:39], v[20:23], a[152:159], v200\n" \
"v_smfmac_f32_16x16x64_bf16 v[40:43], v[20:23], a[208:215], v200\n" \
"v_smfmac_f32_16x16x64_bf16 v[28:31], v[24:27], a[48:55], v200\n" \
"v_smfmac_f32_16x16x64_bf16 v[32:35], v[24:27], a[104:111], v200\n" \
"v_smfmac_f32_16x16x64_bf16 v[36:39], v[24:27], a[160:167], v200\n" \
"v_smfmac_f32_16x16x64_bf16 v[40:43], v[24:27], a[216:223], v200\n" \
"s_nop 2\n" \
"v_mov_b32 v245, v245\n" \
"v_mov_b32 v245, v245\n" \
"v_mov_b32 v245, v245\n" \
"v_permlane32_swap_b32 v28, v32\n" \
"v_permlane32_swap_b32 v29, v33\n" \
"v_permlane32_swap_b32 v30, v34\n" \
"v_permlane32_swap_b32 v31, v35\n" \
"v_add_f32 v28, v28, v32\n" \
"v_add_f32 v29, v29, v33\n" \
"v_add_f32 v30, v30, v34\n" \
"v_add_f32 v31, v31, v35\n" \
"v_cvt_pk_bf16_f32 v68, v28, v29\n" \
"v_cvt_pk_bf16_f32 v69, v30, v31\n" \
"v_permlane32_swap_b32 v36, v40\n" \
"v_permlane32_swap_b32 v37, v41\n" \
"v_permlane32_swap_b32 v38, v42\n" \
"v_permlane32_swap_b32 v39, v43\n" \
"v_add_f32 v36, v36, v40\n" \
"v_add_f32 v37, v37, v41\n" \
"v_add_f32 v38, v38, v42\n" \
"v_add_f32 v39, v39, v43\n" \
"v_cvt_pk_bf16_f32 v70, v36, v37\n" \
"v_cvt_pk_bf16_f32 v71, v38, v39\n" \
"v_mov_b32 v48, v68\n" \
"v_mov_b32 v49, v69\n" \
"v_mov_b32 v54, v70\n" \
"v_mov_b32 v55, v71\n" \
"s_nop 1\n" \
"v_permlane16_swap_b32 v48, v54\n" \
"v_permlane16_swap_b32 v49, v55\n" \
"s_nop 2\n" \
"v_mov_b32 v245, v245\n" \
".LP1_wait:\n" \
"s_waitcnt vmcnt(1)\n" \
"v_pk_add_f32 v[72:73], v[56:57], v[60:61]\n" \
"v_pk_add_f32 v[74:75], v[58:59], v[62:63]\n" \
"v_max3_f32 v77, v72, v73, v64\n" \
"v_max3_f32 v77, v77, v74, v75\n" \
"s_nop 1\n" \
"v_max_f32_dpp v76, v77, v77 row_ror:8 row_mask:0xf bank_mask:0xf\n" \
"v_pk_add_f32 v[72:73], v[72:73], v[76:77] op_sel_hi:[1,0] neg_lo:[0,1] neg_hi:[0,1]\n" \
"v_pk_add_f32 v[74:75], v[74:75], v[76:77] op_sel_hi:[1,0] neg_lo:[0,1] neg_hi:[0,1]\n" \
"v_sub_f32 v78, v64, v76\n" \
"v_exp_f32 v72, v72\n" \
"v_exp_f32 v73, v73\n" \
"v_exp_f32 v74, v74\n" \
"v_exp_f32 v75, v75\n" \
"v_exp_f32 v78, v78\n" \
"v_pk_add_f32 v[80:81], v[72:73], v[74:75]\n" \
"v_fma_f32 v80, v78, v65, v80\n" \
"v_add_f32 v81, v80, v81\n" \
"s_nop 1\n" \
"v_add_f32_dpp v80, v81, v81 row_ror:8 row_mask:0xf bank_mask:0xf\n" \
"v_rcp_f32 v80, v80\n" \
"s_nop 0\n" \
"v_mul_f32 v78, v78, v80\n" \
"v_pk_mul_f32 v[72:73], v[72:73], v[80:81] op_sel_hi:[1,0]\n" \
"v_pk_mul_f32 v[74:75], v[74:75], v[80:81] op_sel_hi:[1,0]\n" \
"v_cvt_pk_bf16_f32 v78, v78, v78\n" \
"v_cvt_pk_bf16_f32 v82, v72, v73\n" \
"v_cvt_pk_bf16_f32 v83, v74, v75\n" \
"v_and_b32 v84, v78, v214\n" \
"v_and_b32 v85, v78, v215\n" \
"v_cvt_pk_bf16_f32 v86, v72, v73\n" \
"s_waitcnt vmcnt(0)\n" \
"v_mfma_f32_16x16x32_bf16 v[88:91], v[48:51], v[82:85], 0\n" \
"v_cvt_pk_bf16_f32 v87, v74, v75\n" \
"s_nop 1\n" \
"v_mfma_f32_16x16x32_bf16 v[92:95], v[52:55], v[84:87], 0\n" \
"s_nop 0\n" \
"s_nop 0\n" \
"s_nop 0\n" \
"v_mov_b32 v245, v245\n" \
"s_nop 0\n" \
"s_nop 0\n" \
"s_nop 0\n" \
"v_mov_b32 v245, v245\n" \
"v_add_f32_dpp v96, v92, v88 row_ror:8 row_mask:0xf bank_mask:0x3\n" \
"v_add_f32_dpp v97, v93, v89 row_ror:8 row_mask:0xf bank_mask:0x3\n" \
"v_add_f32_dpp v96, v90, v94 row_ror:8 row_mask:0xf bank_mask:0xc\n" \
"v_add_f32_dpp v97, v91, v95 row_ror:8 row_mask:0xf bank_mask:0xc\n" \
"ds_write_b64 v216, v[96:97]\n" \
"v_cmp_u_f32 vcc, v96, v97\n" \
"s_nop 0\n" \
"v_mov_b32 v245, v245\n" \
"s_nop 2\n" \
"v_mov_b32 v245, v245\n" \
"s_cbranch_vccnz .LP1_cls\n" \
".LP1_clsr:\n" \
"s_waitcnt lgkmcnt(0)\n" \
"s_barrier\n" \
"s_add_u32 s40, s40, 1\n" \
"s_add_u32 s24, s24, s27\n" \
"s_mov_b32 s47, s48\n" \
"s_add_u32 s43, s40, 1\n" \
"s_and_b32 s43, s43, 3\n" \
"s_bitcmp1_b32 s43, 0\n" \
"s_cselect_b32 s48, s32, s31\n" \
"s_lshl_b32 s43, s43, 19\n" \
"s_add_u32 s48, s48, s43\n" \
"s_add_u32 s43, s40, 2\n" \
"s_and_b32 s43, s43, 3\n" \
"s_bitcmp1_b32 s43, 0\n" \
"s_cselect_b32 s49, s32, s31\n" \
"s_lshl_b32 s43, s43, 19\n" \
"s_add_u32 s49, s49, s43\n" \
"s_sub_u32 s43, s24, s44\n" \
"s_add_u32 s43, s43, s28\n" \
"s_max_i32 s43, s43, 0xfffd0000\n" \
"s_min_i32 s43, s43, 0x4000\n" \
"s_mul_i32 s43, s43, s46\n" \
"s_ashr_i32 s43, s43, 12\n" \
"s_add_u32 s41, s45, s43\n" \
"s_lshr_b32 s35, s42, 1\n" \
"s_sub_u32 s35, 0, s35\n" \
"s_nop 11\n" \
"v_max_u32 v236, v236, v238\n" \
"v_max_u32 v237, v237, v239\n" \
"v_and_b32 v236, 0xfff, v236\n" \
"v_and_b32 v237, 0xfff, v237\n" \
"s_mov_b64 exec, 1\n" \
"ds_write_b64 v252, v[236:237] offset:24\n" \
"s_mov_b64 exec, -1\n" \
"s_cmp_eq_u32 s40, 0\n" \
"s_cbranch_scc1 .LP1_wet\n" \
"s_branch .LP1_loop\n" \
"s_branch .LP1_y1\n" \
".LP1_retry2:\n" \
"s_add_u32 s51, s51, 1\n" \
"s_cmp_gt_u32 s51, 0x20000\n" \
"s_cbranch_scc1 .LP1_end\n" \
".LP1_y0:\n" \
"s_add_u32 s41, s41, s42\n" \
"s_add_u32 s24, s24, s25\n" \
"s_xor_b32 s31, s31, s29\n" \
"s_xor_b32 s32, s32, s29\n" \
"s_xor_b32 s47, s47, s29\n" \
"s_xor_b32 s48, s48, s29\n" \
"s_add_u32 s39, s39, 1\n" \
"buffer_store_dwordx4 v[112:115], v223, s[4:7], s48 offen sc1\n" \
"s_bitcmp1_b32 s31, 21\n" \
"s_cselect_b32 s43, s29, 0\n" \
"v_mov_b32 v116, s43\n" \
"ds_write_b32 v252, v116 offset:16\n" \
".LP1_y1:\n" \
"s_memtime vcc\n" \
"s_waitcnt lgkmcnt(0)\n" \
"s_sub_u32 s43, s41, vcc_lo\n" \
"s_cmp_gt_i32 s43, 160\n" \
"s_cbranch_scc1 .LP1_y1\n" \
"s_cmp_lt_i32 s43, s37\n" \
"s_cbranch_scc1 .LP1_y0\n" \
"buffer_load_dwordx2 v[64:65], v211, s[4:7], s47 offen sc1\n" \
".p2align 6\n" \
".LP1_c1:\n" \
"s_memtime vcc\n" \
"s_waitcnt lgkmcnt(0)\n" \
"s_sub_u32 s43, s41, vcc_lo\n" \
"s_cmp_gt_i32 s43, 128\n" \
"s_cbranch_scc1 .LP1_c1\n" \
"s_cmp_lt_i32 s43, s37\n" \
"s_cbranch_scc1 .LP1_y0\n" \
"s_max_i32 s43, s43, 0\n" \
"s_and_b32 s43, s43, -4\n" \
"s_getpc_b64 vcc\n" \
".LP1_c3:\n" \
"s_sub_u32 s43, .LP1_c4-.LP1_c3, s43\n" \
"s_add_u32 vcc_lo, vcc_lo, s43\n" \
"s_addc_u32 vcc_hi, vcc_hi, 0\n" \
"s_setpc_b64 vcc\n" \
".rept 32\n" \
"s_nop 0\n" \
".endr\n" \
".LP1_c4:\n" \
"buffer_load_dwordx4 v[56:59], v210, s[4:7], s47 offen sc1\n" \
"buffer_load_dwordx4 v[60:63], v210, s[4:7], s47 offen offset:256 sc1\n" \
"s_nop 2\n" \
"v_mov_b32 v245, v245\n" \
"s_nop 2\n" \
"v_mov_b32 v245, v245\n" \
"s_nop 2\n" \
"v_mov_b32 v245, v245\n" \
"s_nop 2\n" \
"v_mov_b32 v245, v245\n" \
"buffer_load_dwordx4 v[50:53], v212, s[4:7], s47 offen sc1\n" \
"s_nop 2\n" \
"v_mov_b32 v245, v245\n" \
"s_nop 2\n" \
"v_mov_b32 v245, v245\n" \
"s_nop 2\n" \
"v_mov_b32 v245, v245\n" \
"s_nop 2\n" \
"v_mov_b32 v245, v245\n" \
"s_nop 2\n" \
"v_mov_b32 v245, v245\n" \
"s_nop 2\n" \
"v_mov_b32 v245, v245\n" \
"s_nop 2\n" \
"v_mov_b32 v245, v245\n" \
"s_nop 2\n" \
"v_mov_b32 v245, v245\n" \
"s_nop 2\n" \
"v_mov_b32 v245, v245\n" \
"s_nop 2\n" \
"v_mov_b32 v245, v245\n" \
"s_nop 2\n" \
"v_mov_b32 v245, v245\n" \
"s_nop 2\n" \
"v_mov_b32 v245, v245\n" \
"s_nop 2\n" \
"v_mov_b32 v245, v245\n" \
"s_nop 2\n" \
"v_mov_b32 v245, v245\n" \
"s_nop 2\n" \
"v_mov_b32 v245, v245\n" \
"s_nop 2\n" \
"v_mov_b32 v245, v245\n" \
"s_nop 2\n" \
"v_mov_b32 v245, v245\n" \
"s_nop 2\n" \
"v_mov_b32 v245, v245\n" \
"s_nop 2\n" \
"v_mov_b32 v245, v245\n" \
"s_nop 2\n" \
"v_mov_b32 v245, v245\n" \
"s_branch .LP1_wait\n" \
".LP1_cls:\n" \
"s_waitcnt vmcnt(0)\n" \
"v_max3_u32 v44, v64, v64, v65\n" \
"v_max3_u32 v44, v44, v50, v51\n" \
"v_max3_u32 v44, v44, v52, v53\n" \
"v_cmp_eq_u32 vcc, -1, v44\n" \
"s_cbranch_vccnz .LP1_retry2\n" \
"v_cmp_u_f32 vcc, v56, v56\n" \
"v_cmp_ne_u32 s[56:57], v56, v109\n" \
"s_and_b64 vcc, vcc, s[56:57]\n" \
"s_cbranch_vccnz .LP1_retry2\n" \
"v_cmp_u_f32 vcc, v57, v57\n" \
"v_cmp_ne_u32 s[56:57], v57, v109\n" \
"s_and_b64 vcc, vcc, s[56:57]\n" \
"s_cbranch_vccnz .LP1_retry2\n" \
"v_cmp_u_f32 vcc, v58, v58\n" \
"v_cmp_ne_u32 s[56:57], v58, v109\n" \
"s_and_b64 vcc, vcc, s[56:57]\n" \
"s_cbranch_vccnz .LP1_retry2\n" \
"v_cmp_u_f32 vcc, v59, v59\n" \
"v_cmp_ne_u32 s[56:57], v59, v109\n" \
"s_and_b64 vcc, vcc, s[56:57]\n" \
"s_cbranch_vccnz .LP1_retry2\n" \
"v_cmp_u_f32 vcc, v60, v60\n" \
"v_cmp_ne_u32 s[56:57], v60, v109\n" \
"s_and_b64 vcc, vcc, s[56:57]\n" \
"s_cbranch_vccnz .LP1_retry2\n" \
"v_cmp_u_f32 vcc, v61, v61\n" \
"v_cmp_ne_u32 s[56:57], v61, v109\n" \
"s_and_b64 vcc, vcc, s[56:57]\n" \
"s_cbranch_vccnz .LP1_retry2\n" \
"v_cmp_u_f32 vcc, v62, v62\n" \
"v_cmp_ne_u32 s[56:57], v62, v109\n" \
"s_and_b64 vcc, vcc, s[56:57]\n" \
"s_cbranch_vccnz .LP1_retry2\n" \
"v_cmp_u_f32 vcc, v63, v63\n" \
"v_cmp_ne_u32 s[56:57], v63, v109\n" \
"s_and_b64 vcc, vcc, s[56:57]\n" \
"s_cbranch_vccnz .LP1_retry2\n" \
"s_branch .LP1_clsr\n" \
".LP1_last:\n" \
"v_smfmac_f32_16x16x64_bf16 v[28:31], v[16:19], a[32:39], v200\n" \
"v_smfmac_f32_16x16x64_bf16 v[32:35], v[16:19], a[88:95], v200\n" \
"v_smfmac_f32_16x16x64_bf16 v[36:39], v[16:19], a[144:151], v200\n" \
"v_smfmac_f32_16x16x64_bf16 v[40:43], v[16:19], a[200:207], v200\n" \
"v_smfmac_f32_16x16x64_bf16 v[28:31], v[20:23], a[40:47], v200\n" \
"v_smfmac_f32_16x16x64_bf16 v[32:35], v[20:23], a[96:103], v200\n" \
"v_smfmac_f32_16x16x64_bf16 v[36:39], v[20:23], a[152:159], v200\n" \
"v_smfmac_f32_16x16x64_bf16 v[40:43], v[20:23], a[208:215], v200\n" \
"v_smfmac_f32_16x16x64_bf16 v[28:31], v[24:27], a[48:55], v200\n" \
"v_smfmac_f32_16x16x64_bf16 v[32:35], v[24:27], a[104:111], v200\n" \
"v_smfmac_f32_16x16x64_bf16 v[36:39], v[24:27], a[160:167], v200\n" \
"v_smfmac_f32_16x16x64_bf16 v[40:43], v[24:27], a[216:223], v200\n" \
"s_waitcnt vmcnt(0)\n" \
"s_nop 7\n" \
"v_permlane32_swap_b32 v28, v32\n" \
"v_permlane32_swap_b32 v29, v33\n" \
"v_permlane32_swap_b32 v30, v34\n" \
"v_permlane32_swap_b32 v31, v35\n" \
"v_add_f32 v28, v28, v32\n" \
"v_add_f32 v29, v29, v33\n" \
"v_add_f32 v30, v30, v34\n" \
"v_add_f32 v31, v31, v35\n" \
"v_cvt_pk_bf16_f32 v68, v28, v29\n" \
"v_cvt_pk_bf16_f32 v69, v30, v31\n" \
"v_permlane32_swap_b32 v36, v40\n" \
"v_permlane32_swap_b32 v37, v41\n" \
"v_permlane32_swap_b32 v38, v42\n" \
"v_permlane32_swap_b32 v39, v43\n" \
"v_add_f32 v36, v36, v40\n" \
"v_add_f32 v37, v37, v41\n" \
"v_add_f32 v38, v38, v42\n" \
"v_add_f32 v39, v39, v43\n" \
"v_cvt_pk_bf16_f32 v70, v36, v37\n" \
"v_cvt_pk_bf16_f32 v71, v38, v39\n" \
"s_mov_b32 s50, 262144\n" \
"buffer_store_dwordx4 v[68:71], v218, s[12:15], s50 offen sc1\n" \
"s_waitcnt vmcnt(0)\n" \
"s_barrier\n" \
"s_add_u32 s43, s30, 1\n" \
"s_mov_b32 s50, 0\n" \
".LP1_w37l:\n" \
"buffer_load_dword v240, v206, s[4:7], 0 offen sc1\n" \
"s_waitcnt vmcnt(0)\n" \
".LP1_w37c:\n" \
"v_subrev_u32 v241, s43, v240\n" \
"v_cmp_gt_u32 vcc, 0x100, v241\n" \
"s_cmp_eq_u64 vcc, -1\n" \
"s_cbranch_scc1 .LP1_w37d\n" \
"s_sleep 2\n" \
"s_add_u32 s50, s50, 1\n" \
"s_cmp_lt_u32 s50, 0x20000\n" \
"s_cbranch_scc1 .LP1_w37l\n" \
"s_branch .LP1_end\n" \
".LP1_w37d:\n" \
"v_mov_b32 v241, s30\n" \
"buffer_store_dword v241, v222, s[4:7], 0 offen sc1\n" \
".LP1_end:\n" \
"s_endpgm\n" \
".p2align 6\n" \
"fk_role_P2:\n" \
"ds_read_b32 v200, v255 offset:0\n" \
"ds_read_b32 v201, v255 offset:256\n" \
"ds_read_b32 v202, v255 offset:512\n" \
"ds_read_b32 v203, v255 offset:768\n" \
"ds_read_b32 v204, v255 offset:1024\n" \
"ds_read_b32 v205, v255 offset:1280\n" \
"ds_read_b32 v206, v255 offset:1536\n" \
"ds_read_b32 v207, v255 offset:1792\n" \
"ds_read_b32 v208, v255 offset:2048\n" \
"ds_read_b32 v209, v255 offset:2304\n" \
"ds_read_b32 v210, v255 offset:2560\n" \
"ds_read_b32 v211, v255 offset:2816\n" \
"ds_read_b32 v212, v255 offset:3072\n" \
"ds_read_b32 v213, v255 offset:3328\n" \
"ds_read_b32 v214, v255 offset:3584\n" \
"ds_read_b32 v215, v255 offset:3840\n" \
"ds_read_b32 v216, v255 offset:4096\n" \
"ds_read_b32 v217, v255 offset:4352\n" \
"ds_read_b32 v218, v255 offset:4608\n" \
"ds_read_b32 v219, v255 offset:4864\n" \
"ds_read_b32 v220, v255 offset:5120\n" \
"ds_read_b32 v221, v255 offset:5376\n" \
"ds_read_b32 v222, v255 offset:5632\n" \
"ds_read_b32 v223, v255 offset:5888\n" \
"ds_read_b32 v254, v255 offset:7936\n" \
"s_waitcnt lgkmcnt(0)\n" \
"v_readlane_b32 s4, v254, 0\n" \
"v_readlane_b32 s5, v254, 1\n" \
"v_readlane_b32 s6, v254, 2\n" \
"v_readlane_b32 s7, v254, 3\n" \
"v_readlane_b32 s8, v254, 4\n" \
"v_readlane_b32 s9, v254, 5\n" \
"v_readlane_b32 s10, v254, 6\n" \
"v_readlane_b32 s11, v254, 7\n" \
"v_readlane_b32 s12, v254, 8\n" \
"v_readlane_b32 s13, v254, 9\n" \
"v_readlane_b32 s14, v254, 10\n" \
"v_readlane_b32 s15, v254, 11\n" \
"v_readlane_b32 s16, v254, 12\n" \
"v_readlane_b32 s17, v254, 13\n" \
"v_readlane_b32 s18, v254, 14\n" \
"v_readlane_b32 s19, v254, 15\n" \
"v_readlane_b32 s20, v254, 16\n" \
"v_readlane_b32 s21, v254, 17\n" \
"v_readlane_b32 s22, v254, 18\n" \
"v_readlane_b32 s23, v254, 19\n" \
"v_readlane_b32 s24, v254, 20\n" \
"v_readlane_b32 s25, v254, 21\n" \
"v_readlane_b32 s26, v254, 22\n" \
"v_readlane_b32 s27, v254, 23\n" \
"v_readlane_b32 s28, v254, 24\n" \
"v_readlane_b32 s29, v254, 25\n" \
"v_readlane_b32 s30, v254, 26\n" \
"v_readlane_b32 s31, v254, 27\n" \
"v_readlane_b32 s32, v254, 28\n" \
"v_readlane_b32 s33, v254, 29\n" \
"v_readlane_b32 s34, v254, 30\n" \
"v_readlane_b32 s35, v254, 31\n" \
"v_readlane_b32 s36, v254, 32\n" \
"v_readlane_b32 s37, v254, 33\n" \
"v_readlane_b32 s38, v254, 34\n" \
"v_readlane_b32 s39, v254, 35\n" \
"s_mov_b32 s40, 0\n" \
"s_mov_b32 s51, 0\n" \
"s_nop 0\n" \
"v_mov_b32 v252, s38\n" \
"s_nop 4\n" \
"v_mov_b32 v112, -1\n" \
"v_mov_b32 v113, -1\n" \
"v_mov_b32 v114, -1\n" \
"v_mov_b32 v115, -1\n" \
"v_mov_b32 v109, 0x7fc00001\n" \
"s_mov_b32 s52, 0x8000\n" \
"s_mov_b32 s53, 0x4000\n" \
"s_mov_b32 s54, 0xc000\n" \
"v_writelane_b32 v251, s6, 0\n" \
"v_writelane_b32 v251, s10, 1\n" \
"v_writelane_b32 v251, s14, 2\n" \
"v_writelane_b32 v251, s18, 3\n" \
"s_mov_b32 s6, 0\n" \
"s_mov_b32 s10, 0\n" \
"s_mov_b32 s14, 0\n" \
"s_mov_b32 s18, 0\n" \
"s_mov_b32 s40, -1\n" \
"s_branch .LP2_pre\n" \
".LP2_wet:\n" \
"s_waitcnt vmcnt(0)\n" \
"v_readlane_b32 s6, v251, 0\n" \
"v_readlane_b32 s10, v251, 1\n" \
"v_readlane_b32 s14, v251, 2\n" \
"v_readlane_b32 s18, v251, 3\n" \
"s_nop 4\n" \
"s_mov_b32 s33, 20\n" \
"s_mov_b32 s34, 0\n" \
"s_add_u32 s43, s30, 1\n" \
"s_mov_b32 s50, 0\n" \
".LP2_w38l:\n" \
"buffer_load_dword v240, v204, s[4:7], 0 offen sc1\n" \
"s_waitcnt vmcnt(0)\n" \
".LP2_w38c:\n" \
"v_subrev_u32 v241, s43, v240\n" \
"v_cmp_gt_u32 vcc, 0x100, v241\n" \
"s_cmp_eq_u64 vcc, -1\n" \
"s_cbranch_scc1 .LP2_w38d\n" \
"s_sleep 2\n" \
"s_add_u32 s50, s50, 1\n" \
"s_cmp_lt_u32 s50, 0x20000\n" \
"s_cbranch_scc1 .LP2_w38l\n" \
"s_branch .LP2_end\n" \
".LP2_w38d:\n" \
"v_readlane_b32 s43, v202, 2\n" \
"s_nop 4\n" \
"buffer_load_dwordx4 a[16:19], v201, s[8:11], s43 offen sc1\n" \
"buffer_load_dwordx4 a[20:23], v201, s[8:11], s43 offen offset:1024 sc1\n" \
"v_readlane_b32 s43, v202, 9\n" \
"s_nop 4\n" \
"buffer_load_dwordx4 a[72:75], v201, s[8:11], s43 offen sc1\n" \
"buffer_load_dwordx4 a[76:79], v201, s[8:11], s43 offen offset:1024 sc1\n" \
"v_readlane_b32 s43, v202, 16\n" \
"s_nop 4\n" \
"buffer_load_dwordx4 a[128:131], v201, s[8:11], s43 offen sc1\n" \
"buffer_load_dwordx4 a[132:135], v201, s[8:11], s43 offen offset:1024 sc1\n" \
"v_readlane_b32 s43, v202, 23\n" \
"s_nop 4\n" \
"buffer_load_dwordx4 a[184:187], v201, s[8:11], s43 offen sc1\n" \
"buffer_load_dwordx4 a[188:191], v201, s[8:11], s43 offen offset:1024 sc1\n" \
"s_add_u32 s43, s30, 2\n" \
"s_mov_b32 s50, 0\n" \
"s_branch .LP2_w39c\n" \
".LP2_w39l:\n" \
"buffer_load_dword v240, v204, s[4:7], 0 offen sc1\n" \
"s_waitcnt vmcnt(0)\n" \
".LP2_w39c:\n" \
"v_subrev_u32 v241, s43, v240\n" \
"v_cmp_gt_u32 vcc, 0x100, v241\n" \
"s_cmp_eq_u64 vcc, -1\n" \
"s_cbranch_scc1 .LP2_w39d\n" \
"s_sleep 2\n" \
"s_add_u32 s50, s50, 1\n" \
"s_cmp_lt_u32 s50, 0x20000\n" \
"s_cbranch_scc1 .LP2_w39l\n" \
"s_branch .LP2_end\n" \
".LP2_w39d:\n" \
"v_readlane_b32 s43, v202, 6\n" \
"s_nop 4\n" \
"buffer_load_dwordx4 a[48:51], v201, s[8:11], s43 offen sc1\n" \
"buffer_load_dwordx4 a[52:55], v201, s[8:11], s43 offen offset:1024 sc1\n" \
"v_readlane_b32 s43, v202, 13\n" \
"s_nop 4\n" \
"buffer_load_dwordx4 a[104:107], v201, s[8:11], s43 offen sc1\n" \
"buffer_load_dwordx4 a[108:111], v201, s[8:11], s43 offen offset:1024 sc1\n" \
"v_readlane_b32 s43, v202, 20\n" \
"s_nop 4\n" \
"buffer_load_dwordx4 a[160:163], v201, s[8:11], s43 offen sc1\n" \
"buffer_load_dwordx4 a[164:167], v201, s[8:11], s43 offen offset:1024 sc1\n" \
"v_readlane_b32 s43, v202, 27\n" \
"s_nop 4\n" \
"buffer_load_dwordx4 a[216:219], v201, s[8:11], s43 offen sc1\n" \
"buffer_load_dwordx4 a[220:223], v201, s[8:11], s43 offen offset:1024 sc1\n" \
"s_add_u32 s43, s30, 3\n" \
"s_mov_b32 s50, 0\n" \
"s_branch .LP2_w40c\n" \
".LP2_w40l:\n" \
"buffer_load_dword v240, v204, s[4:7], 0 offen sc1\n" \
"s_waitcnt vmcnt(0)\n" \
".LP2_w40c:\n" \
"v_subrev_u32 v241, s43, v240\n" \
"v_cmp_gt_u32 vcc, 0x100, v241\n" \
"s_cmp_eq_u64 vcc, -1\n" \
"s_cbranch_scc1 .LP2_w40d\n" \
"s_sleep 2\n" \
"s_add_u32 s50, s50, 1\n" \
"s_cmp_lt_u32 s50, 0x20000\n" \
"s_cbranch_scc1 .LP2_w40l\n" \
"s_branch .LP2_end\n" \
".LP2_w40d:\n" \
"v_readlane_b32 s43, v202, 3\n" \
"s_nop 4\n" \
"buffer_load_dwordx4 a[24:27], v201, s[8:11], s43 offen sc1\n" \
"buffer_load_dwordx4 a[28:31], v201, s[8:11], s43 offen offset:1024 sc1\n" \
"v_readlane_b32 s43, v202, 10\n" \
"s_nop 4\n" \
"buffer_load_dwordx4 a[80:83], v201, s[8:11], s43 offen sc1\n" \
"buffer_load_dwordx4 a[84:87], v201, s[8:11], s43 offen offset:1024 sc1\n" \
"v_readlane_b32 s43, v202, 17\n" \
"s_nop 4\n" \
"buffer_load_dwordx4 a[136:139], v201, s[8:11], s43 offen sc1\n" \
"buffer_load_dwordx4 a[140:143], v201, s[8:11], s43 offen offset:1024 sc1\n" \
"v_readlane_b32 s43, v202, 24\n" \
"s_nop 4\n" \
"buffer_load_dwordx4 a[192:195], v201, s[8:11], s43 offen sc1\n" \
"buffer_load_dwordx4 a[196:199], v201, s[8:11], s43 offen offset:1024 sc1\n" \
"s_add_u32 s43, s30, 4\n" \
"s_mov_b32 s50, 0\n" \
"s_branch .LP2_w41c\n" \
".LP2_w41l:\n" \
"buffer_load_dword v240, v204, s[4:7], 0 offen sc1\n" \
"s_waitcnt vmcnt(0)\n" \
".LP2_w41c:\n" \
"v_subrev_u32 v241, s43, v240\n" \
"v_cmp_gt_u32 vcc, 0x100, v241\n" \
"s_cmp_eq_u64 vcc, -1\n" \
"s_cbranch_scc1 .LP2_w41d\n" \
"s_sleep 2\n" \
"s_add_u32 s50, s50, 1\n" \
"s_cmp_lt_u32 s50, 0x20000\n" \
"s_cbranch_scc1 .LP2_w41l\n" \
"s_branch .LP2_end\n" \
".LP2_w41d:\n" \
"v_readlane_b32 s43, v202, 0\n" \
"s_nop 4\n" \
"buffer_load_dwordx4 a[0:3], v201, s[8:11], s43 offen sc1\n" \
"buffer_load_dwordx4 a[4:7], v201, s[8:11], s43 offen offset:1024 sc1\n" \
"v_readlane_b32 s43, v202, 7\n" \
"s_nop 4\n" \
"buffer_load_dwordx4 a[56:59], v201, s[8:11], s43 offen sc1\n" \
"buffer_load_dwordx4 a[60:63], v201, s[8:11], s43 offen offset:1024 sc1\n" \
"v_readlane_b32 s43, v202, 14\n" \
"s_nop 4\n" \
"buffer_load_dwordx4 a[112:115], v201, s[8:11], s43 offen sc1\n" \
"buffer_load_dwordx4 a[116:119], v201, s[8:11], s43 offen offset:1024 sc1\n" \
"v_readlane_b32 s43, v202, 21\n" \
"s_nop 4\n" \
"buffer_load_dwordx4 a[168:171], v201, s[8:11], s43 offen sc1\n" \
"buffer_load_dwordx4 a[172:175], v201, s[8:11], s43 offen offset:1024 sc1\n" \
"s_add_u32 s43, s30, 5\n" \
"s_mov_b32 s50, 0\n" \
"s_branch .LP2_w42c\n" \
".LP2_w42l:\n" \
"buffer_load_dword v240, v204, s[4:7], 0 offen sc1\n" \
"s_waitcnt vmcnt(0)\n" \
".LP2_w42c:\n" \
"v_subrev_u32 v241, s43, v240\n" \
"v_cmp_gt_u32 vcc, 0x100, v241\n" \
"s_cmp_eq_u64 vcc, -1\n" \
"s_cbranch_scc1 .LP2_w42d\n" \
"s_sleep 2\n" \
"s_add_u32 s50, s50, 1\n" \
"s_cmp_lt_u32 s50, 0x20000\n" \
"s_cbranch_scc1 .LP2_w42l\n" \
"s_branch .LP2_end\n" \
".LP2_w42d:\n" \
"v_readlane_b32 s43, v202, 4\n" \
"s_nop 4\n" \
"buffer_load_dwordx4 a[32:35], v201, s[8:11], s43 offen sc1\n" \
"buffer_load_dwordx4 a[36:39], v201, s[8:11], s43 offen offset:1024 sc1\n" \
"v_readlane_b32 s43, v202, 11\n" \
"s_nop 4\n" \
"buffer_load_dwordx4 a[88:91], v201, s[8:11], s43 offen sc1\n" \
"buffer_load_dwordx4 a[92:95], v201, s[8:11], s43 offen offset:1024 sc1\n" \
"v_readlane_b32 s43, v202, 18\n" \
"s_nop 4\n" \
"buffer_load_dwordx4 a[144:147], v201, s[8:11], s43 offen sc1\n" \
"buffer_load_dwordx4 a[148:151], v201, s[8:11], s43 offen offset:1024 sc1\n" \
"v_readlane_b32 s43, v202, 25\n" \
"s_nop 4\n" \
"buffer_load_dwordx4 a[200:203], v201, s[8:11], s43 offen sc1\n" \
"buffer_load_dwordx4 a[204:207], v201, s[8:11], s43 offen offset:1024 sc1\n" \
"s_add_u32 s43, s30, 6\n" \
"s_mov_b32 s50, 0\n" \
"s_branch .LP2_w43c\n" \
".LP2_w43l:\n" \
"buffer_load_dword v240, v204, s[4:7], 0 offen sc1\n" \
"s_waitcnt vmcnt(0)\n" \
".LP2_w43c:\n" \
"v_subrev_u32 v241, s43, v240\n" \
"v_cmp_gt_u32 vcc, 0x100, v241\n" \
"s_cmp_eq_u64 vcc, -1\n" \
"s_cbranch_scc1 .LP2_w43d\n" \
"s_sleep 2\n" \
"s_add_u32 s50, s50, 1\n" \
"s_cmp_lt_u32 s50, 0x20000\n" \
"s_cbranch_scc1 .LP2_w43l\n" \
"s_branch .LP2_end\n" \
".LP2_w43d:\n" \
"v_readlane_b32 s43, v202, 1\n" \
"s_nop 4\n" \
"buffer_load_dwordx4 a[8:11], v201, s[8:11], s43 offen sc1\n" \
"buffer_load_dwordx4 a[12:15], v201, s[8:11], s43 offen offset:1024 sc1\n" \
"v_readlane_b32 s43, v202, 8\n" \
"s_nop 4\n" \
"buffer_load_dwordx4 a[64:67], v201, s[8:11], s43 offen sc1\n" \
"buffer_load_dwordx4 a[68:71], v201, s[8:11], s43 offen offset:1024 sc1\n" \
"v_readlane_b32 s43, v202, 15\n" \
"s_nop 4\n" \
"buffer_load_dwordx4 a[120:123], v201, s[8:11], s43 offen sc1\n" \
"buffer_load_dwordx4 a[124:127], v201, s[8:11], s43 offen offset:1024 sc1\n" \
"v_readlane_b32 s43, v202, 22\n" \
"s_nop 4\n" \
"buffer_load_dwordx4 a[176:179], v201, s[8:11], s43 offen sc1\n" \
"buffer_load_dwordx4 a[180:183], v201, s[8:11], s43 offen offset:1024 sc1\n" \
"s_add_u32 s43, s30, 7\n" \
"s_mov_b32 s50, 0\n" \
"s_branch .LP2_w44c\n" \
".LP2_w44l:\n" \
"buffer_load_dword v240, v204, s[4:7], 0 offen sc1\n" \
"s_waitcnt vmcnt(0)\n" \
".LP2_w44c:\n" \
"v_subrev_u32 v241, s43, v240\n" \
"v_cmp_gt_u32 vcc, 0x100, v241\n" \
"s_cmp_eq_u64 vcc, -1\n" \
"s_cbranch_scc1 .LP2_w44d\n" \
"s_sleep 2\n" \
"s_add_u32 s50, s50, 1\n" \
"s_cmp_lt_u32 s50, 0x20000\n" \
"s_cbranch_scc1 .LP2_w44l\n" \
"s_branch .LP2_end\n" \
".LP2_w44d:\n" \
"v_readlane_b32 s43, v202, 5\n" \
"s_nop 4\n" \
"buffer_load_dwordx4 a[40:43], v201, s[8:11], s43 offen sc1\n" \
"buffer_load_dwordx4 a[44:47], v201, s[8:11], s43 offen offset:1024 sc1\n" \
"v_readlane_b32 s43, v202, 12\n" \
"s_nop 4\n" \
"buffer_load_dwordx4 a[96:99], v201, s[8:11], s43 offen sc1\n" \
"buffer_load_dwordx4 a[100:103], v201, s[8:11], s43 offen offset:1024 sc1\n" \
"v_readlane_b32 s43, v202, 19\n" \
"s_nop 4\n" \
"buffer_load_dwordx4 a[152:155], v201, s[8:11], s43 offen sc1\n" \
"buffer_load_dwordx4 a[156:159], v201, s[8:11], s43 offen offset:1024 sc1\n" \
"v_readlane_b32 s43, v202, 26\n" \
"s_nop 4\n" \
"buffer_load_dwordx4 a[208:211], v201, s[8:11], s43 offen sc1\n" \
"buffer_load_dwordx4 a[212:215], v201, s[8:11], s43 offen offset:1024 sc1\n" \
"s_add_u32 s43, s30, 1\n" \
"s_mov_b32 s50, 0\n" \
".LP2_w45l:\n" \
"buffer_load_dword v240, v205, s[4:7], 0 offen sc1\n" \
"s_waitcnt vmcnt(0)\n" \
".LP2_w45c:\n" \
"v_subrev_u32 v241, s43, v240\n" \
"v_cmp_gt_u32 vcc, 0x100, v241\n" \
"s_cmp_eq_u64 vcc, -1\n" \
"s_cbranch_scc1 .LP2_w45d\n" \
"s_sleep 2\n" \
"s_add_u32 s50, s50, 1\n" \
"s_cmp_lt_u32 s50, 0x20000\n" \
"s_cbranch_scc1 .LP2_w45l\n" \
"s_branch .LP2_end\n" \
".LP2_w45d:\n" \
"v_readfirstlane_b32 s31, v241\n" \
"s_lshr_b32 s32, s31, 2\n" \
"s_xor_b32 s29, s31, s32\n" \
"s_and_b32 s29, s29, 3\n" \
"s_lshl_b32 s29, s29, 13\n" \
"s_bitset1_b32 s29, 21\n" \
"s_and_b32 s31, s31, 3\n" \
"s_lshl_b32 s31, s31, 13\n" \
"s_bitcmp1_b32 s39, 0\n" \
"s_cselect_b32 s43, s29, 0\n" \
"s_xor_b32 s31, s31, s43\n" \
"s_xor_b32 s32, s31, 0x4000\n" \
"s_mov_b32 s39, 0\n" \
"s_add_u32 s43, s40, 0\n" \
"s_and_b32 s43, s43, 3\n" \
"s_bitcmp1_b32 s43, 0\n" \
"s_cselect_b32 s47, s32, s31\n" \
"s_lshl_b32 s43, s43, 19\n" \
"s_add_u32 s47, s47, s43\n" \
".LP2_pre:\n" \
"s_barrier\n" \
"ds_read_b96 v[248:250], v252\n" \
"s_waitcnt lgkmcnt(0)\n" \
"v_readfirstlane_b32 s44, v248\n" \
"v_readfirstlane_b32 s45, v249\n" \
"v_readfirstlane_b32 s46, v250\n" \
"s_mul_i32 s42, s25, s46\n" \
"s_lshr_b32 s42, s42, 12\n" \
".LP2_cu:\n" \
"s_sub_u32 s43, s24, s44\n" \
"s_cmp_lt_i32 s43, 0\n" \
"s_cbranch_scc0 .LP2_cud\n" \
"s_add_u32 s24, s24, s25\n" \
"s_xor_b32 s31, s31, s29\n" \
"s_xor_b32 s32, s32, s29\n" \
"s_xor_b32 s47, s47, s29\n" \
"s_add_u32 s39, s39, 1\n" \
"s_branch .LP2_cu\n" \
".LP2_cud:\n" \
"s_mov_b32 s3, s46\n" \
"s_add_u32 s43, s40, 1\n" \
"s_and_b32 s43, s43, 3\n" \
"s_bitcmp1_b32 s43, 0\n" \
"s_cselect_b32 s48, s32, s31\n" \
"s_lshl_b32 s43, s43, 19\n" \
"s_add_u32 s48, s48, s43\n" \
"s_add_u32 s43, s40, 2\n" \
"s_and_b32 s43, s43, 3\n" \
"s_bitcmp1_b32 s43, 0\n" \
"s_cselect_b32 s49, s32, s31\n" \
"s_lshl_b32 s43, s43, 19\n" \
"s_add_u32 s49, s49, s43\n" \
"s_sub_u32 s43, s24, s44\n" \
"s_add_u32 s43, s43, s28\n" \
"s_max_i32 s43, s43, 0xfffd0000\n" \
"s_min_i32 s43, s43, 0x4000\n" \
"s_mul_i32 s43, s43, s46\n" \
"s_ashr_i32 s43, s43, 12\n" \
"s_add_u32 s41, s45, s43\n" \
"s_lshr_b32 s35, s42, 1\n" \
"s_sub_u32 s35, 0, s35\n" \
".LP2_loop:\n" \
"s_branch .LP2_m1\n" \
".LP2_retry1:\n" \
"s_add_u32 s51, s51, 1\n" \
"s_cmp_gt_u32 s51, 0x20000\n" \
"s_cbranch_scc1 .LP2_end\n" \
".LP2_m0:\n" \
"s_add_u32 s41, s41, s42\n" \
"s_add_u32 s24, s24, s25\n" \
"s_xor_b32 s31, s31, s29\n" \
"s_xor_b32 s32, s32, s29\n" \
"s_xor_b32 s47, s47, s29\n" \
"s_xor_b32 s48, s48, s29\n" \
"s_add_u32 s39, s39, 1\n" \
"s_bitcmp1_b32 s31, 21\n" \
"s_cselect_b32 s43, s29, 0\n" \
"v_mov_b32 v116, s43\n" \
"ds_write_b32 v252, v116 offset:16\n" \
".LP2_m1:\n" \
"s_memtime vcc\n" \
"s_waitcnt lgkmcnt(0)\n" \
"s_sub_u32 s43, s41, vcc_lo\n" \
"s_cmp_gt_i32 s43, 0\n" \
"s_cbranch_scc1 .LP2_m1\n" \
"s_cmp_lt_i32 s43, s35\n" \
"s_cbranch_scc1 .LP2_m0\n" \
"buffer_load_dwordx4 v[0:3], v209, s[4:7], s47 offen offset:1024 sc1\n" \
"buffer_load_dwordx4 v[4:7], v209, s[4:7], s47 offen offset:2048 sc1\n" \
"s_cmp_lg_u32 s39, s34\n" \
"s_cselect_b32 s43, 20, -4\n" \
"s_add_u32 s33, s33, s43\n" \
"s_max_i32 s33, s33, 0\n" \
"s_min_i32 s33, s33, 60\n" \
"s_mov_b32 s34, s39\n" \
"s_add_u32 s3, s3, 10\n" \
"s_min_u32 s3, s3, s46\n" \
"s_sub_u32 s43, 6042, s3\n" \
"s_mov_b32 s50, s33\n" \
"s_mul_i32 s36, s43, 185\n" \
"s_ashr_i32 s36, s36, 10\n" \
"s_add_u32 s36, s36, s50\n" \
"s_add_u32 s36, s36, 1040\n" \
"v_mov_b32 v110, s36\n" \
"s_mul_i32 s36, s43, 138\n" \
"s_ashr_i32 s36, s36, 10\n" \
"s_add_u32 s36, s36, s50\n" \
"s_add_u32 s36, s36, 790\n" \
"v_mov_b32 v111, s36\n" \
"s_nop 15\n" \
"s_nop 13\n" \
"s_mul_i32 s50, s40, 4096\n" \
"buffer_store_dwordx4 v[68:71], v218, s[12:15], s50 offen sc1\n" \
"buffer_store_dword v112, v213, s[4:7], s49 offen sc1\n" \
"s_xor_b32 s43, s49, s29\n" \
"buffer_store_dword v112, v213, s[4:7], s43 offen sc1\n" \
"s_lshl_b32 s43, s40, 7\n" \
"buffer_store_dwordx2 v[110:111], v222, s[4:7], s43 offen offset:384 sc1\n" \
"v_mov_b32 v28, 0\n" \
"v_mov_b32 v29, 0\n" \
"v_mov_b32 v30, 0\n" \
"v_mov_b32 v31, 0\n" \
"v_mov_b32 v32, 0\n" \
"v_mov_b32 v33, 0\n" \
"v_mov_b32 v34, 0\n" \
"v_mov_b32 v35, 0\n" \
"v_mov_b32 v36, 0\n" \
"v_mov_b32 v37, 0\n" \
"v_mov_b32 v38, 0\n" \
"v_mov_b32 v39, 0\n" \
"v_mov_b32 v40, 0\n" \
"v_mov_b32 v41, 0\n" \
"v_mov_b32 v42, 0\n" \
"v_mov_b32 v43, 0\n" \
"s_waitcnt vmcnt(5)\n" \
"ds_write_b128 v220, v[0:3] offset:5120\n" \
"s_waitcnt vmcnt(4)\n" \
"ds_write_b128 v220, v[4:7] offset:6144\n" \
"v_max3_u32 v44, v0, v0, v1\n" \
"v_max3_u32 v44, v44, v2, v3\n" \
"v_max3_u32 v44, v44, v4, v5\n" \
"v_max3_u32 v44, v44, v6, v7\n" \
"v_cmp_eq_u32 vcc, -1, v44\n" \
"v_smfmac_f32_16x16x64_bf16 v[28:31], v[0:3], a[0:7], v200\n" \
"v_smfmac_f32_16x16x64_bf16 v[32:35], v[0:3], a[56:63], v200\n" \
"s_cbranch_vccnz .LP2_retry1\n" \
"s_waitcnt lgkmcnt(0)\n" \
"s_barrier\n" \
"ds_read_b128 v[8:11], v220 offset:0\n" \
"ds_read_b128 v[12:15], v220 offset:1024\n" \
"ds_read_b128 v[16:19], v220 offset:2048\n" \
"ds_read_b128 v[20:23], v220 offset:3072\n" \
"ds_read_b128 v[24:27], v220 offset:4096\n" \
"ds_read_b64 v[246:247], v252 offset:24\n" \
"ds_read_b96 v[248:250], v252\n" \
"v_smfmac_f32_16x16x64_bf16 v[36:39], v[0:3], a[112:119], v200\n" \
"v_smfmac_f32_16x16x64_bf16 v[40:43], v[0:3], a[168:175], v200\n" \
"v_smfmac_f32_16x16x64_bf16 v[28:31], v[4:7], a[8:15], v200\n" \
"v_smfmac_f32_16x16x64_bf16 v[32:35], v[4:7], a[64:71], v200\n" \
"v_smfmac_f32_16x16x64_bf16 v[36:39], v[4:7], a[120:127], v200\n" \
"v_smfmac_f32_16x16x64_bf16 v[40:43], v[4:7], a[176:183], v200\n" \
"s_waitcnt lgkmcnt(6)\n" \
"v_smfmac_f32_16x16x64_bf16 v[28:31], v[8:11], a[16:23], v200\n" \
"v_smfmac_f32_16x16x64_bf16 v[32:35], v[8:11], a[72:79], v200\n" \
"v_smfmac_f32_16x16x64_bf16 v[36:39], v[8:11], a[128:135], v200\n" \
"v_smfmac_f32_16x16x64_bf16 v[40:43], v[8:11], a[184:191], v200\n" \
"s_waitcnt lgkmcnt(0)\n" \
"v_readfirstlane_b32 s44, v248\n" \
"v_readfirstlane_b32 s45, v249\n" \
"v_smfmac_f32_16x16x64_bf16 v[28:31], v[12:15], a[24:31], v200\n" \
"v_readfirstlane_b32 s46, v250\n" \
"v_readfirstlane_b32 s26, v246\n" \
"v_readfirstlane_b32 s27, v247\n" \
"v_smfmac_f32_16x16x64_bf16 v[32:35], v[12:15], a[80:87], v200\n" \
"s_mul_i32 s42, s25, s46\n" \
"s_lshr_b32 s42, s42, 12\n" \
"s_add_u32 s24, s24, s26\n" \
"v_smfmac_f32_16x16x64_bf16 v[36:39], v[12:15], a[136:143], v200\n" \
"s_sub_u32 s43, s24, s44\n" \
"s_max_i32 s43, s43, 0xfffd0000\n" \
"s_min_i32 s43, s43, 0x4000\n" \
"v_smfmac_f32_16x16x64_bf16 v[40:43], v[12:15], a[192:199], v200\n" \
"s_mul_i32 s43, s43, s46\n" \
"s_ashr_i32 s43, s43, 12\n" \
"s_add_u32 s41, s45, s43\n" \
"s_cmp_eq_u32 s40, 63\n" \
"s_cbranch_scc1 .LP2_last\n" \
"s_branch .LP2_z1\n" \
".LP2_z0:\n" \
"s_add_u32 s41, s41, s42\n" \
"s_add_u32 s24, s24, s25\n" \
"s_xor_b32 s31, s31, s29\n" \
"s_xor_b32 s32, s32, s29\n" \
"s_xor_b32 s47, s47, s29\n" \
"s_xor_b32 s48, s48, s29\n" \
"s_add_u32 s39, s39, 1\n" \
"s_bitcmp1_b32 s31, 21\n" \
"s_cselect_b32 s43, s29, 0\n" \
"v_mov_b32 v116, s43\n" \
"ds_write_b32 v252, v116 offset:16\n" \
".LP2_z1:\n" \
"s_memtime vcc\n" \
"s_waitcnt lgkmcnt(0)\n" \
"s_sub_u32 s43, s41, vcc_lo\n" \
"s_cmp_gt_i32 s43, 160\n" \
"s_cbranch_scc1 .LP2_z1\n" \
"s_cmp_lt_i32 s43, s37\n" \
"s_cbranch_scc1 .LP2_z0\n" \
"buffer_load_dwordx2 v[64:65], v211, s[4:7], s47 offen sc1\n" \
".p2align 6\n" \
".LP2_b1:\n" \
"s_memtime vcc\n" \
"s_waitcnt lgkmcnt(0)\n" \
"s_sub_u32 s43, s41, vcc_lo\n" \
"s_cmp_gt_i32 s43, 128\n" \
"s_cbranch_scc1 .LP2_b1\n" \
"s_cmp_lt_i32 s43, s37\n" \
"s_cbranch_scc1 .LP2_z0\n" \
"s_max_i32 s43, s43, 0\n" \
"s_and_b32 s43, s43, -4\n" \
"s_getpc_b64 vcc\n" \
".LP2_b3:\n" \
"s_sub_u32 s43, .LP2_b4-.LP2_b3, s43\n" \
"s_add_u32 vcc_lo, vcc_lo, s43\n" \
"s_addc_u32 vcc_hi, vcc_hi, 0\n" \
"s_setpc_b64 vcc\n" \
".rept 32\n" \
"s_nop 0\n" \
".endr\n" \
".LP2_b4:\n" \
"buffer_load_dwordx4 v[56:59], v210, s[4:7], s47 offen sc1\n" \
"buffer_load_dwordx4 v[60:63], v210, s[4:7], s47 offen offset:256 sc1\n" \
"v_smfmac_f32_16x16x64_bf16 v[28:31], v[16:19], a[32:39], v200\n" \
"v_smfmac_f32_16x16x64_bf16 v[32:35], v[16:19], a[88:95], v200\n" \
"v_smfmac_f32_16x16x64_bf16 v[36:39], v[16:19], a[144:151], v200\n" \
"v_smfmac_f32_16x16x64_bf16 v[40:43], v[16:19], a[200:207], v200\n" \
"buffer_load_dwordx4 v[50:53], v212, s[4:7], s47 offen sc1\n" \
"v_smfmac_f32_16x16x64_bf16 v[28:31], v[20:23], a[40:47], v200\n" \
"v_smfmac_f32_16x16x64_bf16 v[32:35], v[20:23], a[96:103], v200\n" \
"v_smfmac_f32_16x16x64_bf16 v[36:39], v[20:23], a[152:159], v200\n" \
"v_smfmac_f32_16x16x64_bf16 v[40:43], v[20:23], a[208:215], v200\n" \
"v_smfmac_f32_16x16x64_bf16 v[28:31], v[24:27], a[48:55], v200\n" \
"v_smfmac_f32_16x16x64_bf16 v[32:35], v[24:27], a[104:111], v200\n" \
"v_smfmac_f32_16x16x64_bf16 v[36:39], v[24:27], a[160:167], v200\n" \
"v_smfmac_f32_16x16x64_bf16 v[40:43], v[24:27], a[216:223], v200\n" \
"s_nop 2\n" \
"v_mov_b32 v245, v245\n" \
"v_mov_b32 v245, v245\n" \
"v_mov_b32 v245, v245\n" \
"v_permlane32_swap_b32 v28, v32\n" \
"v_permlane32_swap_b32 v29, v33\n" \
"v_permlane32_swap_b32 v30, v34\n" \
"v_permlane32_swap_b32 v31, v35\n" \
"v_add_f32 v28, v28, v32\n" \
"v_add_f32 v29, v29, v33\n" \
"v_add_f32 v30, v30, v34\n" \
"v_add_f32 v31, v31, v35\n" \
"v_cvt_pk_bf16_f32 v68, v28, v29\n" \
"v_cvt_pk_bf16_f32 v69, v30, v31\n" \
"v_permlane32_swap_b32 v36, v40\n" \
"v_permlane32_swap_b32 v37, v41\n" \
"v_permlane32_swap_b32 v38, v42\n" \
"v_permlane32_swap_b32 v39, v43\n" \
"v_add_f32 v36, v36, v40\n" \
"v_add_f32 v37, v37, v41\n" \
"v_add_f32 v38, v38, v42\n" \
"v_add_f32 v39, v39, v43\n" \
"v_cvt_pk_bf16_f32 v70, v36, v37\n" \
"v_cvt_pk_bf16_f32 v71, v38, v39\n" \
"v_mov_b32 v48, v68\n" \
"v_mov_b32 v49, v69\n" \
"v_mov_b32 v54, v70\n" \
"v_mov_b32 v55, v71\n" \
"s_nop 1\n" \
"v_permlane16_swap_b32 v48, v54\n" \
"v_permlane16_swap_b32 v49, v55\n" \
"s_nop 2\n" \
"v_mov_b32 v245, v245\n" \
".LP2_wait:\n" \
"s_waitcnt vmcnt(1)\n" \
"v_pk_add_f32 v[72:73], v[56:57], v[60:61]\n" \
"v_pk_add_f32 v[74:75], v[58:59], v[62:63]\n" \
"v_max3_f32 v77, v72, v73, v64\n" \
"v_max3_f32 v77, v77, v74, v75\n" \
"s_nop 1\n" \
"v_max_f32_dpp v76, v77, v77 row_ror:8 row_mask:0xf bank_mask:0xf\n" \
"v_pk_add_f32 v[72:73], v[72:73], v[76:77] op_sel_hi:[1,0] neg_lo:[0,1] neg_hi:[0,1]\n" \
"v_pk_add_f32 v[74:75], v[74:75], v[76:77] op_sel_hi:[1,0] neg_lo:[0,1] neg_hi:[0,1]\n" \
"v_sub_f32 v78, v64, v76\n" \
"v_exp_f32 v72, v72\n" \
"v_exp_f32 v73, v73\n" \
"v_exp_f32 v74, v74\n" \
"v_exp_f32 v75, v75\n" \
"v_exp_f32 v78, v78\n" \
"v_pk_add_f32 v[80:81], v[72:73], v[74:75]\n" \
"v_fma_f32 v80, v78, v65, v80\n" \
"v_add_f32 v81, v80, v81\n" \
"s_nop 1\n" \
"v_add_f32_dpp v80, v81, v81 row_ror:8 row_mask:0xf bank_mask:0xf\n" \
"v_rcp_f32 v80, v80\n" \
"s_nop 0\n" \
"v_mul_f32 v78, v78, v80\n" \
"v_pk_mul_f32 v[72:73], v[72:73], v[80:81] op_sel_hi:[1,0]\n" \
"v_pk_mul_f32 v[74:75], v[74:75], v[80:81] op_sel_hi:[1,0]\n" \
"v_cvt_pk_bf16_f32 v78, v78, v78\n" \
"v_cvt_pk_bf16_f32 v82, v72, v73\n" \
"v_cvt_pk_bf16_f32 v83, v74, v75\n" \
"v_and_b32 v84, v78, v214\n" \
"v_and_b32 v85, v78, v215\n" \
"v_cvt_pk_bf16_f32 v86, v72, v73\n" \
"s_waitcnt vmcnt(0)\n" \
"v_mfma_f32_16x16x32_bf16 v[88:91], v[48:51], v[82:85], 0\n" \
"v_cvt_pk_bf16_f32 v87, v74, v75\n" \
"s_nop 1\n" \
"v_mfma_f32_16x16x32_bf16 v[92:95], v[52:55], v[84:87], 0\n" \
"s_nop 0\n" \
"s_nop 0\n" \
"s_nop 0\n" \
"v_mov_b32 v245, v245\n" \
"s_nop 0\n" \
"s_nop 0\n" \
"s_nop 0\n" \
"v_mov_b32 v245, v245\n" \
"v_add_f32_dpp v96, v92, v88 row_ror:8 row_mask:0xf bank_mask:0x3\n" \
"v_add_f32_dpp v97, v93, v89 row_ror:8 row_mask:0xf bank_mask:0x3\n" \
"v_add_f32_dpp v96, v90, v94 row_ror:8 row_mask:0xf bank_mask:0xc\n" \
"v_add_f32_dpp v97, v91, v95 row_ror:8 row_mask:0xf bank_mask:0xc\n" \
"ds_write_b64 v216, v[96:97]\n" \
"v_cmp_u_f32 vcc, v96, v97\n" \
"s_nop 0\n" \
"v_mov_b32 v245, v245\n" \
"s_nop 2\n" \
"v_mov_b32 v245, v245\n" \
"s_cbranch_vccnz .LP2_cls\n" \
".LP2_clsr:\n" \
"s_waitcnt lgkmcnt(0)\n" \
"s_barrier\n" \
"s_add_u32 s40, s40, 1\n" \
"s_add_u32 s24, s24, s27\n" \
"s_mov_b32 s47, s48\n" \
"s_add_u32 s43, s40, 1\n" \
"s_and_b32 s43, s43, 3\n" \
"s_bitcmp1_b32 s43, 0\n" \
"s_cselect_b32 s48, s32, s31\n" \
"s_lshl_b32 s43, s43, 19\n" \
"s_add_u32 s48, s48, s43\n" \
"s_add_u32 s43, s40, 2\n" \
"s_and_b32 s43, s43, 3\n" \
"s_bitcmp1_b32 s43, 0\n" \
"s_cselect_b32 s49, s32, s31\n" \
"s_lshl_b32 s43, s43, 19\n" \
"s_add_u32 s49, s49, s43\n" \
"s_sub_u32 s43, s24, s44\n" \
"s_add_u32 s43, s43, s28\n" \
"s_max_i32 s43, s43, 0xfffd0000\n" \
"s_min_i32 s43, s43, 0x4000\n" \
"s_mul_i32 s43, s43, s46\n" \
"s_ashr_i32 s43, s43, 12\n" \
"s_add_u32 s41, s45, s43\n" \
"s_lshr_b32 s35, s42, 1\n" \
"s_sub_u32 s35, 0, s35\n" \
"s_cmp_eq_u32 s40, 0\n" \
"s_cbranch_scc1 .LP2_wet\n" \
"s_branch .LP2_loop\n" \
"s_branch .LP2_y1\n" \
".LP2_retry2:\n" \
"s_add_u32 s51, s51, 1\n" \
"s_cmp_gt_u32 s51, 0x20000\n" \
"s_cbranch_scc1 .LP2_end\n" \
".LP2_y0:\n" \
"s_add_u32 s41, s41, s42\n" \
"s_add_u32 s24, s24, s25\n" \
"s_xor_b32 s31, s31, s29\n" \
"s_xor_b32 s32, s32, s29\n" \
"s_xor_b32 s47, s47, s29\n" \
"s_xor_b32 s48, s48, s29\n" \
"s_add_u32 s39, s39, 1\n" \
"buffer_store_dwordx4 v[112:115], v223, s[4:7], s48 offen sc1\n" \
"s_bitcmp1_b32 s31, 21\n" \
"s_cselect_b32 s43, s29, 0\n" \
"v_mov_b32 v116, s43\n" \
"ds_write_b32 v252, v116 offset:16\n" \
".LP2_y1:\n" \
"s_memtime vcc\n" \
"s_waitcnt lgkmcnt(0)\n" \
"s_sub_u32 s43, s41, vcc_lo\n" \
"s_cmp_gt_i32 s43, 160\n" \
"s_cbranch_scc1 .LP2_y1\n" \
"s_cmp_lt_i32 s43, s37\n" \
"s_cbranch_scc1 .LP2_y0\n" \
"buffer_load_dwordx2 v[64:65], v211, s[4:7], s47 offen sc1\n" \
".p2align 6\n" \
".LP2_c1:\n" \
"s_memtime vcc\n" \
"s_waitcnt lgkmcnt(0)\n" \
"s_sub_u32 s43, s41, vcc_lo\n" \
"s_cmp_gt_i32 s43, 128\n" \
"s_cbranch_scc1 .LP2_c1\n" \
"s_cmp_lt_i32 s43, s37\n" \
"s_cbranch_scc1 .LP2_y0\n" \
"s_max_i32 s43, s43, 0\n" \
"s_and_b32 s43, s43, -4\n" \
"s_getpc_b64 vcc\n" \
".LP2_c3:\n" \
"s_sub_u32 s43, .LP2_c4-.LP2_c3, s43\n" \
"s_add_u32 vcc_lo, vcc_lo, s43\n" \
"s_addc_u32 vcc_hi, vcc_hi, 0\n" \
"s_setpc_b64 vcc\n" \
".rept 32\n" \
"s_nop 0\n" \
".endr\n" \
".LP2_c4:\n" \
"buffer_load_dwordx4 v[56:59], v210, s[4:7], s47 offen sc1\n" \
"buffer_load_dwordx4 v[60:63], v210, s[4:7], s47 offen offset:256 sc1\n" \
"s_nop 2\n" \
"v_mov_b32 v245, v245\n" \
"s_nop 2\n" \
"v_mov_b32 v245, v245\n" \
"s_nop 2\n" \
"v_mov_b32 v245, v245\n" \
"s_nop 2\n" \
"v_mov_b32 v245, v245\n" \
"buffer_load_dwordx4 v[50:53], v212, s[4:7], s47 offen sc1\n" \
"s_nop 2\n" \
"v_mov_b32 v245, v245\n" \
"s_nop 2\n" \
"v_mov_b32 v245, v245\n" \
"s_nop 2\n" \
"v_mov_b32 v245, v245\n" \
"s_nop 2\n" \
"v_mov_b32 v245, v245\n" \
"s_nop 2\n" \
"v_mov_b32 v245, v245\n" \
"s_nop 2\n" \
"v_mov_b32 v245, v245\n" \
"s_nop 2\n" \
"v_mov_b32 v245, v245\n" \
"s_nop 2\n" \
"v_mov_b32 v245, v245\n" \
"s_nop 2\n" \
"v_mov_b32 v245, v245\n" \
"s_nop 2\n" \
"v_mov_b32 v245, v245\n" \
"s_nop 2\n" \
"v_mov_b32 v245, v245\n" \
"s_nop 2\n" \
"v_mov_b32 v245, v245\n" \
"s_nop 2\n" \
"v_mov_b32 v245, v245\n" \
"s_nop 2\n" \
"v_mov_b32 v245, v245\n" \
"s_nop 2\n" \
"v_mov_b32 v245, v245\n" \
"s_nop 2\n" \
"v_mov_b32 v245, v245\n" \
"s_nop 2\n" \
"v_mov_b32 v245, v245\n" \
"s_nop 2\n" \
"v_mov_b32 v245, v245\n" \
"s_nop 2\n" \
"v_mov_b32 v245, v245\n" \
"s_nop 2\n" \
"v_mov_b32 v245, v245\n" \
"s_branch .LP2_wait\n" \
".LP2_cls:\n" \
"s_waitcnt vmcnt(0)\n" \
"v_max3_u32 v44, v64, v64, v65\n" \
"v_max3_u32 v44, v44, v50, v51\n" \
"v_max3_u32 v44, v44, v52, v53\n" \
"v_cmp_eq_u32 vcc, -1, v44\n" \
"s_cbranch_vccnz .LP2_retry2\n" \
"v_cmp_u_f32 vcc, v56, v56\n" \
"v_cmp_ne_u32 s[56:57], v56, v109\n" \
"s_and_b64 vcc, vcc, s[56:57]\n" \
"s_cbranch_vccnz .LP2_retry2\n" \
"v_cmp_u_f32 vcc, v57, v57\n" \
"v_cmp_ne_u32 s[56:57], v57, v109\n" \
"s_and_b64 vcc, vcc, s[56:57]\n" \
"s_cbranch_vccnz .LP2_retry2\n" \
"v_cmp_u_f32 vcc, v58, v58\n" \
"v_cmp_ne_u32 s[56:57], v58, v109\n" \
"s_and_b64 vcc, vcc, s[56:57]\n" \
"s_cbranch_vccnz .LP2_retry2\n" \
"v_cmp_u_f32 vcc, v59, v59\n" \
"v_cmp_ne_u32 s[56:57], v59, v109\n" \
"s_and_b64 vcc, vcc, s[56:57]\n" \
"s_cbranch_vccnz .LP2_retry2\n" \
"v_cmp_u_f32 vcc, v60, v60\n" \
"v_cmp_ne_u32 s[56:57], v60, v109\n" \
"s_and_b64 vcc, vcc, s[56:57]\n" \
"s_cbranch_vccnz .LP2_retry2\n" \
"v_cmp_u_f32 vcc, v61, v61\n" \
"v_cmp_ne_u32 s[56:57], v61, v109\n" \
"s_and_b64 vcc, vcc, s[56:57]\n" \
"s_cbranch_vccnz .LP2_retry2\n" \
"v_cmp_u_f32 vcc, v62, v62\n" \
"v_cmp_ne_u32 s[56:57], v62, v109\n" \
"s_and_b64 vcc, vcc, s[56:57]\n" \
"s_cbranch_vccnz .LP2_retry2\n" \
"v_cmp_u_f32 vcc, v63, v63\n" \
"v_cmp_ne_u32 s[56:57], v63, v109\n" \
"s_and_b64 vcc, vcc, s[56:57]\n" \
"s_cbranch_vccnz .LP2_retry2\n" \
"s_branch .LP2_clsr\n" \
".LP2_last:\n" \
"v_smfmac_f32_16x16x64_bf16 v[28:31], v[16:19], a[32:39], v200\n" \
"v_smfmac_f32_16x16x64_bf16 v[32:35], v[16:19], a[88:95], v200\n" \
"v_smfmac_f32_16x16x64_bf16 v[36:39], v[16:19], a[144:151], v200\n" \
"v_smfmac_f32_16x16x64_bf16 v[40:43], v[16:19], a[200:207], v200\n" \
"v_smfmac_f32_16x16x64_bf16 v[28:31], v[20:23], a[40:47], v200\n" \
"v_smfmac_f32_16x16x64_bf16 v[32:35], v[20:23], a[96:103], v200\n" \
"v_smfmac_f32_16x16x64_bf16 v[36:39], v[20:23], a[152:159], v200\n" \
"v_smfmac_f32_16x16x64_bf16 v[40:43], v[20:23], a[208:215], v200\n" \
"v_smfmac_f32_16x16x64_bf16 v[28:31], v[24:27], a[48:55], v200\n" \
"v_smfmac_f32_16x16x64_bf16 v[32:35], v[24:27], a[104:111], v200\n" \
"v_smfmac_f32_16x16x64_bf16 v[36:39], v[24:27], a[160:167], v200\n" \
"v_smfmac_f32_16x16x64_bf16 v[40:43], v[24:27], a[216:223], v200\n" \
"s_waitcnt vmcnt(0)\n" \
"s_nop 7\n" \
"v_permlane32_swap_b32 v28, v32\n" \
"v_permlane32_swap_b32 v29, v33\n" \
"v_permlane32_swap_b32 v30, v34\n" \
"v_permlane32_swap_b32 v31, v35\n" \
"v_add_f32 v28, v28, v32\n" \
"v_add_f32 v29, v29, v33\n" \
"v_add_f32 v30, v30, v34\n" \
"v_add_f32 v31, v31, v35\n" \
"v_cvt_pk_bf16_f32 v68, v28, v29\n" \
"v_cvt_pk_bf16_f32 v69, v30, v31\n" \
"v_permlane32_swap_b32 v36, v40\n" \
"v_permlane32_swap_b32 v37, v41\n" \
"v_permlane32_swap_b32 v38, v42\n" \
"v_permlane32_swap_b32 v39, v43\n" \
"v_add_f32 v36, v36, v40\n" \
"v_add_f32 v37, v37, v41\n" \
"v_add_f32 v38, v38, v42\n" \
"v_add_f32 v39, v39, v43\n" \
"v_cvt_pk_bf16_f32 v70, v36, v37\n" \
"v_cvt_pk_bf16_f32 v71, v38, v39\n" \
"s_mov_b32 s50, 262144\n" \
"buffer_store_dwordx4 v[68:71], v218, s[12:15], s50 offen sc1\n" \
"s_waitcnt vmcnt(0)\n" \
"s_barrier\n" \
".LP2_end:\n" \
"s_endpgm\n" \
".p2align 6\n" \
"fk_role_P3:\n" \
"ds_read_b32 v200, v255 offset:0\n" \
"ds_read_b32 v201, v255 offset:256\n" \
"ds_read_b32 v202, v255 offset:512\n" \
"ds_read_b32 v203, v255 offset:768\n" \
"ds_read_b32 v204, v255 offset:1024\n" \
"ds_read_b32 v205, v255 offset:1280\n" \
"ds_read_b32 v206, v255 offset:1536\n" \
"ds_read_b32 v207, v255 offset:1792\n" \
"ds_read_b32 v208, v255 offset:2048\n" \
"ds_read_b32 v209, v255 offset:2304\n" \
"ds_read_b32 v210, v255 offset:2560\n" \
"ds_read_b32 v211, v255 offset:2816\n" \
"ds_read_b32 v212, v255 offset:3072\n" \
"ds_read_b32 v213, v255 offset:3328\n" \
"ds_read_b32 v214, v255 offset:3584\n" \
"ds_read_b32 v215, v255 offset:3840\n" \
"ds_read_b32 v216, v255 offset:4096\n" \
"ds_read_b32 v217, v255 offset:4352\n" \
"ds_read_b32 v218, v255 offset:4608\n" \
"ds_read_b32 v219, v255 offset:4864\n" \
"ds_read_b32 v220, v255 offset:5120\n" \
"ds_read_b32 v221, v255 offset:5376\n" \
"ds_read_b32 v222, v255 offset:5632\n" \
"ds_read_b32 v223, v255 offset:5888\n" \
"ds_read_b32 v254, v255 offset:7936\n" \
"s_waitcnt lgkmcnt(0)\n" \
"v_readlane_b32 s4, v254, 0\n" \
"v_readlane_b32 s5, v254, 1\n" \
"v_readlane_b32 s6, v254, 2\n" \
"v_readlane_b32 s7, v254, 3\n" \
"v_readlane_b32 s8, v254, 4\n" \
"v_readlane_b32 s9, v254, 5\n" \
"v_readlane_b32 s10, v254, 6\n" \
"v_readlane_b32 s11, v254, 7\n" \
"v_readlane_b32 s12, v254, 8\n" \
"v_readlane_b32 s13, v254, 9\n" \
"v_readlane_b32 s14, v254, 10\n" \
"v_readlane_b32 s15, v254, 11\n" \
"v_readlane_b32 s16, v254, 12\n" \
"v_readlane_b32 s17, v254, 13\n" \
"v_readlane_b32 s18, v254, 14\n" \
"v_readlane_b32 s19, v254, 15\n" \
"v_readlane_b32 s20, v254, 16\n" \
"v_readlane_b32 s21, v254, 17\n" \
"v_readlane_b32 s22, v254, 18\n" \
"v_readlane_b32 s23, v254, 19\n" \
"v_readlane_b32 s24, v254, 20\n" \
"v_readlane_b32 s25, v254, 21\n" \
"v_readlane_b32 s26, v254, 22\n" \
"v_readlane_b32 s27, v254, 23\n" \
"v_readlane_b32 s28, v254, 24\n" \
"v_readlane_b32 s29, v254, 25\n" \
"v_readlane_b32 s30, v254, 26\n" \
"v_readlane_b32 s31, v254, 27\n" \
"v_readlane_b32 s32, v254, 28\n" \
"v_readlane_b32 s33, v254, 29\n" \
"v_readlane_b32 s34, v254, 30\n" \
"v_readlane_b32 s35, v254, 31\n" \
"v_readlane_b32 s36, v254, 32\n" \
"v_readlane_b32 s37, v254, 33\n" \
"v_readlane_b32 s38, v254, 34\n" \
"v_readlane_b32 s39, v254, 35\n" \
"s_mov_b32 s40, 0\n" \
"s_mov_b32 s51, 0\n" \
"s_nop 0\n" \
"v_mov_b32 v252, s38\n" \
"s_nop 4\n" \
"v_mov_b32 v112, -1\n" \
"v_mov_b32 v113, -1\n" \
"v_mov_b32 v114, -1\n" \
"v_mov_b32 v115, -1\n" \
"v_mov_b32 v109, 0x7fc00001\n" \
"s_mov_b32 s52, 0x8000\n" \
"s_mov_b32 s53, 0x4000\n" \
"s_mov_b32 s54, 0xc000\n" \
"v_writelane_b32 v251, s6, 0\n" \
"v_writelane_b32 v251, s10, 1\n" \
"v_writelane_b32 v251, s14, 2\n" \
"v_writelane_b32 v251, s18, 3\n" \
"s_mov_b32 s6, 0\n" \
"s_mov_b32 s10, 0\n" \
"s_mov_b32 s14, 0\n" \
"s_mov_b32 s18, 0\n" \
"s_mov_b32 s40, -1\n" \
"s_branch .LP3_pre\n" \
".LP3_wet:\n" \
"s_waitcnt vmcnt(0)\n" \
"v_readlane_b32 s6, v251, 0\n" \
"v_readlane_b32 s10, v251, 1\n" \
"v_readlane_b32 s14, v251, 2\n" \
"v_readlane_b32 s18, v251, 3\n" \
"s_nop 4\n" \
"s_mov_b32 s33, 20\n" \
"s_mov_b32 s34, 0\n" \
"s_add_u32 s43, s30, 1\n" \
"s_mov_b32 s50, 0\n" \
".LP3_w46l:\n" \
"buffer_load_dword v240, v204, s[4:7], 0 offen sc1\n" \
"s_waitcnt vmcnt(0)\n" \
".LP3_w46c:\n" \
"v_subrev_u32 v241, s43, v240\n" \
"v_cmp_gt_u32 vcc, 0x100, v241\n" \
"s_cmp_eq_u64 vcc, -1\n" \
"s_cbranch_scc1 .LP3_w46d\n" \
"s_sleep 2\n" \
"s_add_u32 s50, s50, 1\n" \
"s_cmp_lt_u32 s50, 0x20000\n" \
"s_cbranch_scc1 .LP3_w46l\n" \
"s_branch .LP3_end\n" \
".LP3_w46d:\n" \
"v_readlane_b32 s43, v202, 1\n" \
"s_nop 4\n" \
"buffer_load_dwordx4 a[8:11], v201, s[8:11], s43 offen sc1\n" \
"buffer_load_dwordx4 a[12:15], v201, s[8:11], s43 offen offset:1024 sc1\n" \
"v_readlane_b32 s43, v202, 8\n" \
"s_nop 4\n" \
"buffer_load_dwordx4 a[64:67], v201, s[8:11], s43 offen sc1\n" \
"buffer_load_dwordx4 a[68:71], v201, s[8:11], s43 offen offset:1024 sc1\n" \
"v_readlane_b32 s43, v202, 15\n" \
"s_nop 4\n" \
"buffer_load_dwordx4 a[120:123], v201, s[8:11], s43 offen sc1\n" \
"buffer_load_dwordx4 a[124:127], v201, s[8:11], s43 offen offset:1024 sc1\n" \
"v_readlane_b32 s43, v202, 22\n" \
"s_nop 4\n" \
"buffer_load_dwordx4 a[176:179], v201, s[8:11], s43 offen sc1\n" \
"buffer_load_dwordx4 a[180:183], v201, s[8:11], s43 offen offset:1024 sc1\n" \
"s_add_u32 s43, s30, 2\n" \
"s_mov_b32 s50, 0\n" \
"s_branch .LP3_w47c\n" \
".LP3_w47l:\n" \
"buffer_load_dword v240, v204, s[4:7], 0 offen sc1\n" \
"s_waitcnt vmcnt(0)\n" \
".LP3_w47c:\n" \
"v_subrev_u32 v241, s43, v240\n" \
"v_cmp_gt_u32 vcc, 0x100, v241\n" \
"s_cmp_eq_u64 vcc, -1\n" \
"s_cbranch_scc1 .LP3_w47d\n" \
"s_sleep 2\n" \
"s_add_u32 s50, s50, 1\n" \
"s_cmp_lt_u32 s50, 0x20000\n" \
"s_cbranch_scc1 .LP3_w47l\n" \
"s_branch .LP3_end\n" \
".LP3_w47d:\n" \
"v_readlane_b32 s43, v202, 4\n" \
"s_nop 4\n" \
"buffer_load_dwordx4 a[32:35], v201, s[8:11], s43 offen sc1\n" \
"buffer_load_dwordx4 a[36:39], v201, s[8:11], s43 offen offset:1024 sc1\n" \
"v_readlane_b32 s43, v202, 11\n" \
"s_nop 4\n" \
"buffer_load_dwordx4 a[88:91], v201, s[8:11], s43 offen sc1\n" \
"buffer_load_dwordx4 a[92:95], v201, s[8:11], s43 offen offset:1024 sc1\n" \
"v_readlane_b32 s43, v202, 18\n" \
"s_nop 4\n" \
"buffer_load_dwordx4 a[144:147], v201, s[8:11], s43 offen sc1\n" \
"buffer_load_dwordx4 a[148:151], v201, s[8:11], s43 offen offset:1024 sc1\n" \
"v_readlane_b32 s43, v202, 25\n" \
"s_nop 4\n" \
"buffer_load_dwordx4 a[200:203], v201, s[8:11], s43 offen sc1\n" \
"buffer_load_dwordx4 a[204:207], v201, s[8:11], s43 offen offset:1024 sc1\n" \
"s_add_u32 s43, s30, 3\n" \
"s_mov_b32 s50, 0\n" \
"s_branch .LP3_w48c\n" \
".LP3_w48l:\n" \
"buffer_load_dword v240, v204, s[4:7], 0 offen sc1\n" \
"s_waitcnt vmcnt(0)\n" \
".LP3_w48c:\n" \
"v_subrev_u32 v241, s43, v240\n" \
"v_cmp_gt_u32 vcc, 0x100, v241\n" \
"s_cmp_eq_u64 vcc, -1\n" \
"s_cbranch_scc1 .LP3_w48d\n" \
"s_sleep 2\n" \
"s_add_u32 s50, s50, 1\n" \
"s_cmp_lt_u32 s50, 0x20000\n" \
"s_cbranch_scc1 .LP3_w48l\n" \
"s_branch .LP3_end\n" \
".LP3_w48d:\n" \
"v_readlane_b32 s43, v202, 2\n" \
"s_nop 4\n" \
"buffer_load_dwordx4 a[16:19], v201, s[8:11], s43 offen sc1\n" \
"buffer_load_dwordx4 a[20:23], v201, s[8:11], s43 offen offset:1024 sc1\n" \
"v_readlane_b32 s43, v202, 9\n" \
"s_nop 4\n" \
"buffer_load_dwordx4 a[72:75], v201, s[8:11], s43 offen sc1\n" \
"buffer_load_dwordx4 a[76:79], v201, s[8:11], s43 offen offset:1024 sc1\n" \
"v_readlane_b32 s43, v202, 16\n" \
"s_nop 4\n" \
"buffer_load_dwordx4 a[128:131], v201, s[8:11], s43 offen sc1\n" \
"buffer_load_dwordx4 a[132:135], v201, s[8:11], s43 offen offset:1024 sc1\n" \
"v_readlane_b32 s43, v202, 23\n" \
"s_nop 4\n" \
"buffer_load_dwordx4 a[184:187], v201, s[8:11], s43 offen sc1\n" \
"buffer_load_dwordx4 a[188:191], v201, s[8:11], s43 offen offset:1024 sc1\n" \
"s_add_u32 s43, s30, 4\n" \
"s_mov_b32 s50, 0\n" \
"s_branch .LP3_w49c\n" \
".LP3_w49l:\n" \
"buffer_load_dword v240, v204, s[4:7], 0 offen sc1\n" \
"s_waitcnt vmcnt(0)\n" \
".LP3_w49c:\n" \
"v_subrev_u32 v241, s43, v240\n" \
"v_cmp_gt_u32 vcc, 0x100, v241\n" \
"s_cmp_eq_u64 vcc, -1\n" \
"s_cbranch_scc1 .LP3_w49d\n" \
"s_sleep 2\n" \
"s_add_u32 s50, s50, 1\n" \
"s_cmp_lt_u32 s50, 0x20000\n" \
"s_cbranch_scc1 .LP3_w49l\n" \
"s_branch .LP3_end\n" \
".LP3_w49d:\n" \
"v_readlane_b32 s43, v202, 5\n" \
"s_nop 4\n" \
"buffer_load_dwordx4 a[40:43], v201, s[8:11], s43 offen sc1\n" \
"buffer_load_dwordx4 a[44:47], v201, s[8:11], s43 offen offset:1024 sc1\n" \
"v_readlane_b32 s43, v202, 12\n" \
"s_nop 4\n" \
"buffer_load_dwordx4 a[96:99], v201, s[8:11], s43 offen sc1\n" \
"buffer_load_dwordx4 a[100:103], v201, s[8:11], s43 offen offset:1024 sc1\n" \
"v_readlane_b32 s43, v202, 19\n" \
"s_nop 4\n" \
"buffer_load_dwordx4 a[152:155], v201, s[8:11], s43 offen sc1\n" \
"buffer_load_dwordx4 a[156:159], v201, s[8:11], s43 offen offset:1024 sc1\n" \
"v_readlane_b32 s43, v202, 26\n" \
"s_nop 4\n" \
"buffer_load_dwordx4 a[208:211], v201, s[8:11], s43 offen sc1\n" \
"buffer_load_dwordx4 a[212:215], v201, s[8:11], s43 offen offset:1024 sc1\n" \
"s_add_u32 s43, s30, 5\n" \
"s_mov_b32 s50, 0\n" \
"s_branch .LP3_w50c\n" \
".LP3_w50l:\n" \
"buffer_load_dword v240, v204, s[4:7], 0 offen sc1\n" \
"s_waitcnt vmcnt(0)\n" \
".LP3_w50c:\n" \
"v_subrev_u32 v241, s43, v240\n" \
"v_cmp_gt_u32 vcc, 0x100, v241\n" \
"s_cmp_eq_u64 vcc, -1\n" \
"s_cbranch_scc1 .LP3_w50d\n" \
"s_sleep 2\n" \
"s_add_u32 s50, s50, 1\n" \
"s_cmp_lt_u32 s50, 0x20000\n" \
"s_cbranch_scc1 .LP3_w50l\n" \
"s_branch .LP3_end\n" \
".LP3_w50d:\n" \
"v_readlane_b32 s43, v202, 3\n" \
"s_nop 4\n" \
"buffer_load_dwordx4 a[24:27], v201, s[8:11], s43 offen sc1\n" \
"buffer_load_dwordx4 a[28:31], v201, s[8:11], s43 offen offset:1024 sc1\n" \
"v_readlane_b32 s43, v202, 10\n" \
"s_nop 4\n" \
"buffer_load_dwordx4 a[80:83], v201, s[8:11], s43 offen sc1\n" \
"buffer_load_dwordx4 a[84:87], v201, s[8:11], s43 offen offset:1024 sc1\n" \
"v_readlane_b32 s43, v202, 17\n" \
"s_nop 4\n" \
"buffer_load_dwordx4 a[136:139], v201, s[8:11], s43 offen sc1\n" \
"buffer_load_dwordx4 a[140:143], v201, s[8:11], s43 offen offset:1024 sc1\n" \
"v_readlane_b32 s43, v202, 24\n" \
"s_nop 4\n" \
"buffer_load_dwordx4 a[192:195], v201, s[8:11], s43 offen sc1\n" \
"buffer_load_dwordx4 a[196:199], v201, s[8:11], s43 offen offset:1024 sc1\n" \
"s_add_u32 s43, s30, 6\n" \
"s_mov_b32 s50, 0\n" \
"s_branch .LP3_w51c\n" \
".LP3_w51l:\n" \
"buffer_load_dword v240, v204, s[4:7], 0 offen sc1\n" \
"s_waitcnt vmcnt(0)\n" \
".LP3_w51c:\n" \
"v_subrev_u32 v241, s43, v240\n" \
"v_cmp_gt_u32 vcc, 0x100, v241\n" \
"s_cmp_eq_u64 vcc, -1\n" \
"s_cbranch_scc1 .LP3_w51d\n" \
"s_sleep 2\n" \
"s_add_u32 s50, s50, 1\n" \
"s_cmp_lt_u32 s50, 0x20000\n" \
"s_cbranch_scc1 .LP3_w51l\n" \
"s_branch .LP3_end\n" \
".LP3_w51d:\n" \
"v_readlane_b32 s43, v202, 6\n" \
"s_nop 4\n" \
"buffer_load_dwordx4 a[48:51], v201, s[8:11], s43 offen sc1\n" \
"buffer_load_dwordx4 a[52:55], v201, s[8:11], s43 offen offset:1024 sc1\n" \
"v_readlane_b32 s43, v202, 13\n" \
"s_nop 4\n" \
"buffer_load_dwordx4 a[104:107], v201, s[8:11], s43 offen sc1\n" \
"buffer_load_dwordx4 a[108:111], v201, s[8:11], s43 offen offset:1024 sc1\n" \
"v_readlane_b32 s43, v202, 20\n" \
"s_nop 4\n" \
"buffer_load_dwordx4 a[160:163], v201, s[8:11], s43 offen sc1\n" \
"buffer_load_dwordx4 a[164:167], v201, s[8:11], s43 offen offset:1024 sc1\n" \
"v_readlane_b32 s43, v202, 27\n" \
"s_nop 4\n" \
"buffer_load_dwordx4 a[216:219], v201, s[8:11], s43 offen sc1\n" \
"buffer_load_dwordx4 a[220:223], v201, s[8:11], s43 offen offset:1024 sc1\n" \
"s_add_u32 s43, s30, 7\n" \
"s_mov_b32 s50, 0\n" \
"s_branch .LP3_w52c\n" \
".LP3_w52l:\n" \
"buffer_load_dword v240, v204, s[4:7], 0 offen sc1\n" \
"s_waitcnt vmcnt(0)\n" \
".LP3_w52c:\n" \
"v_subrev_u32 v241, s43, v240\n" \
"v_cmp_gt_u32 vcc, 0x100, v241\n" \
"s_cmp_eq_u64 vcc, -1\n" \
"s_cbranch_scc1 .LP3_w52d\n" \
"s_sleep 2\n" \
"s_add_u32 s50, s50, 1\n" \
"s_cmp_lt_u32 s50, 0x20000\n" \
"s_cbranch_scc1 .LP3_w52l\n" \
"s_branch .LP3_end\n" \
".LP3_w52d:\n" \
"v_readlane_b32 s43, v202, 0\n" \
"s_nop 4\n" \
"buffer_load_dwordx4 a[0:3], v201, s[8:11], s43 offen sc1\n" \
"buffer_load_dwordx4 a[4:7], v201, s[8:11], s43 offen offset:1024 sc1\n" \
"v_readlane_b32 s43, v202, 7\n" \
"s_nop 4\n" \
"buffer_load_dwordx4 a[56:59], v201, s[8:11], s43 offen sc1\n" \
"buffer_load_dwordx4 a[60:63], v201, s[8:11], s43 offen offset:1024 sc1\n" \
"v_readlane_b32 s43, v202, 14\n" \
"s_nop 4\n" \
"buffer_load_dwordx4 a[112:115], v201, s[8:11], s43 offen sc1\n" \
"buffer_load_dwordx4 a[116:119], v201, s[8:11], s43 offen offset:1024 sc1\n" \
"v_readlane_b32 s43, v202, 21\n" \
"s_nop 4\n" \
"buffer_load_dwordx4 a[168:171], v201, s[8:11], s43 offen sc1\n" \
"buffer_load_dwordx4 a[172:175], v201, s[8:11], s43 offen offset:1024 sc1\n" \
"ds_read_b96 v[248:250], v252\n" \
"s_waitcnt lgkmcnt(0)\n" \
"v_readfirstlane_b32 s44, v248\n" \
"v_readfirstlane_b32 s45, v249\n" \
"v_readfirstlane_b32 s46, v250\n" \
"s_mul_i32 s42, s25, s46\n" \
"s_lshr_b32 s42, s42, 12\n" \
"s_mov_b32 s26, s44\n" \
"s_mov_b32 s27, s45\n" \
".p2align 3\n" \
"s_memtime s[54:55]\n" \
"s_memrealtime s[56:57]\n" \
"s_memtime s[58:59]\n" \
"s_memrealtime s[60:61]\n" \
"s_nop 0\n" \
"s_nop 0\n" \
"s_memtime s[62:63]\n" \
"s_memrealtime s[64:65]\n" \
"s_memtime s[66:67]\n" \
"s_memrealtime s[68:69]\n" \
"s_memtime s[70:71]\n" \
"s_memrealtime s[72:73]\n" \
"s_memtime s[74:75]\n" \
"s_memrealtime s[76:77]\n" \
"s_nop 0\n" \
"s_nop 0\n" \
"s_memtime s[78:79]\n" \
"s_memrealtime s[80:81]\n" \
"s_memtime s[82:83]\n" \
"s_memrealtime s[84:85]\n" \
"s_memtime s[86:87]\n" \
"s_memrealtime s[88:89]\n" \
"s_memtime s[90:91]\n" \
"s_memrealtime s[92:93]\n" \
"s_nop 0\n" \
"s_nop 0\n" \
"s_memtime s[94:95]\n" \
"s_memrealtime s[96:97]\n" \
"s_memtime s[98:99]\n" \
"s_mov_b32 s52, 0\n" \
"s_mov_b32 s53, 4096\n" \
"s_waitcnt lgkmcnt(0)\n" \
"s_cmp_lg_u32 s96, s56\n" \
"s_cselect_b32 s52, s90, s52\n" \
"s_cselect_b32 s53, s98, s53\n" \
"s_cmp_lg_u32 s92, s56\n" \
"s_cselect_b32 s52, s86, s52\n" \
"s_cselect_b32 s53, s94, s53\n" \
"s_cmp_lg_u32 s88, s56\n" \
"s_cselect_b32 s52, s82, s52\n" \
"s_cselect_b32 s53, s90, s53\n" \
"s_cmp_lg_u32 s84, s56\n" \
"s_cselect_b32 s52, s78, s52\n" \
"s_cselect_b32 s53, s86, s53\n" \
"s_cmp_lg_u32 s80, s56\n" \
"s_cselect_b32 s52, s74, s52\n" \
"s_cselect_b32 s53, s82, s53\n" \
"s_cmp_lg_u32 s76, s56\n" \
"s_cselect_b32 s52, s70, s52\n" \
"s_cselect_b32 s53, s78, s53\n" \
"s_cmp_lg_u32 s72, s56\n" \
"s_cselect_b32 s52, s66, s52\n" \
"s_cselect_b32 s53, s74, s53\n" \
"s_cmp_lg_u32 s68, s56\n" \
"s_cselect_b32 s52, s62, s52\n" \
"s_cselect_b32 s53, s70, s53\n" \
"s_cmp_lg_u32 s64, s56\n" \
"s_cselect_b32 s52, s58, s52\n" \
"s_cselect_b32 s53, s66, s53\n" \
"s_cmp_lg_u32 s60, s56\n" \
"s_cselect_b32 s52, s54, s52\n" \
"s_cselect_b32 s53, s62, s53\n" \
"s_sub_u32 s53, s53, s52\n" \
"s_lshr_b32 s55, s53, 1\n" \
"s_add_u32 s52, s52, s55\n" \
"s_add_u32 s55, s56, 4\n" \
"s_lshl_b32 s55, s55, 4\n" \
"s_cmp_le_u32 s53, 40\n" \
"s_cselect_b32 s44, s55, s44\n" \
"s_cselect_b32 s45, s52, s45\n" \
"v_mov_b32 v248, s44\n" \
"v_mov_b32 v249, s45\n" \
"v_mov_b32 v250, s46\n" \
"s_mov_b64 exec, 1\n" \
"ds_write_b96 v252, v[248:250]\n" \
"s_mov_b64 exec, -1\n" \
"s_sub_u32 s26, s44, s26\n" \
"s_sub_u32 s27, s45, s27\n" \
"v_cvt_f32_u32 v248, s26\n" \
"v_cvt_f32_u32 v249, s27\n" \
"v_rcp_f32 v248, v248\n" \
"v_mul_f32 v249, 0x457ae148, v249\n" \
"v_mul_f32 v248, v248, v249\n" \
"v_cvt_u32_f32 v248, v248\n" \
"s_nop 0\n" \
"v_readfirstlane_b32 s27, v248\n" \
"s_max_u32 s27, s27, 0x800\n" \
"s_min_u32 s27, s27, 0x2000\n" \
"s_cmp_gt_u32 s26, 1600\n" \
"s_cselect_b32 s46, s27, s46\n" \
".p2align 3\n" \
"s_memtime s[54:55]\n" \
"s_memrealtime s[56:57]\n" \
"s_memtime s[58:59]\n" \
"s_memrealtime s[60:61]\n" \
"s_nop 0\n" \
"s_nop 0\n" \
"s_memtime s[62:63]\n" \
"s_memrealtime s[64:65]\n" \
"s_memtime s[66:67]\n" \
"s_memrealtime s[68:69]\n" \
"s_memtime s[70:71]\n" \
"s_memrealtime s[72:73]\n" \
"s_memtime s[74:75]\n" \
"s_memrealtime s[76:77]\n" \
"s_nop 0\n" \
"s_nop 0\n" \
"s_memtime s[78:79]\n" \
"s_memrealtime s[80:81]\n" \
"s_memtime s[82:83]\n" \
"s_memrealtime s[84:85]\n" \
"s_memtime s[86:87]\n" \
"s_memrealtime s[88:89]\n" \
"s_memtime s[90:91]\n" \
"s_memrealtime s[92:93]\n" \
"s_nop 0\n" \
"s_nop 0\n" \
"s_memtime s[94:95]\n" \
"s_memrealtime s[96:97]\n" \
"s_memtime s[98:99]\n" \
"s_mov_b32 s52, 0\n" \
"s_mov_b32 s53, 4096\n" \
"s_waitcnt lgkmcnt(0)\n" \
"s_cmp_lg_u32 s96, s56\n" \
"s_cselect_b32 s52, s90, s52\n" \
"s_cselect_b32 s53, s98, s53\n" \
"s_cmp_lg_u32 s92, s56\n" \
"s_cselect_b32 s52, s86, s52\n" \
"s_cselect_b32 s53, s94, s53\n" \
"s_cmp_lg_u32 s88, s56\n" \
"s_cselect_b32 s52, s82, s52\n" \
"s_cselect_b32 s53, s90, s53\n" \
"s_cmp_lg_u32 s84, s56\n" \
"s_cselect_b32 s52, s78, s52\n" \
"s_cselect_b32 s53, s86, s53\n" \
"s_cmp_lg_u32 s80, s56\n" \
"s_cselect_b32 s52, s74, s52\n" \
"s_cselect_b32 s53, s82, s53\n" \
"s_cmp_lg_u32 s76, s56\n" \
"s_cselect_b32 s52, s70, s52\n" \
"s_cselect_b32 s53, s78, s53\n" \
"s_cmp_lg_u32 s72, s56\n" \
"s_cselect_b32 s52, s66, s52\n" \
"s_cselect_b32 s53, s74, s53\n" \
"s_cmp_lg_u32 s68, s56\n" \
"s_cselect_b32 s52, s62, s52\n" \
"s_cselect_b32 s53, s70, s53\n" \
"s_cmp_lg_u32 s64, s56\n" \
"s_cselect_b32 s52, s58, s52\n" \
"s_cselect_b32 s53, s66, s53\n" \
"s_cmp_lg_u32 s60, s56\n" \
"s_cselect_b32 s52, s54, s52\n" \
"s_cselect_b32 s53, s62, s53\n" \
"s_sub_u32 s53, s53, s52\n" \
"s_lshr_b32 s55, s53, 1\n" \
"s_add_u32 s52, s52, s55\n" \
"s_add_u32 s55, s56, 4\n" \
"s_lshl_b32 s55, s55, 4\n" \
"s_cmp_le_u32 s53, 40\n" \
"s_cselect_b32 s44, s55, s44\n" \
"s_cselect_b32 s45, s52, s45\n" \
"v_mov_b32 v248, s44\n" \
"v_mov_b32 v249, s45\n" \
"v_mov_b32 v250, s46\n" \
"s_mov_b64 exec, 1\n" \
"ds_write_b96 v252, v[248:250]\n" \
"s_mov_b64 exec, -1\n" \
"s_waitcnt lgkmcnt(0)\n" \
"s_add_u32 s43, s30, 1\n" \
"s_mov_b32 s50, 0\n" \
".LP3_w53l:\n" \
"buffer_load_dword v240, v205, s[4:7], 0 offen sc1\n" \
"s_waitcnt vmcnt(0)\n" \
".LP3_w53c:\n" \
"v_subrev_u32 v241, s43, v240\n" \
"v_cmp_gt_u32 vcc, 0x100, v241\n" \
"s_cmp_eq_u64 vcc, -1\n" \
"s_cbranch_scc1 .LP3_w53d\n" \
"s_sleep 2\n" \
"s_add_u32 s50, s50, 1\n" \
"s_cmp_lt_u32 s50, 0x20000\n" \
"s_cbranch_scc1 .LP3_w53l\n" \
"s_branch .LP3_end\n" \
".LP3_w53d:\n" \
"v_readfirstlane_b32 s31, v241\n" \
"s_lshr_b32 s32, s31, 2\n" \
"s_xor_b32 s29, s31, s32\n" \
"s_and_b32 s29, s29, 3\n" \
"s_lshl_b32 s29, s29, 13\n" \
"s_bitset1_b32 s29, 21\n" \
"s_and_b32 s31, s31, 3\n" \
"s_lshl_b32 s31, s31, 13\n" \
"s_bitcmp1_b32 s39, 0\n" \
"s_cselect_b32 s43, s29, 0\n" \
"s_xor_b32 s31, s31, s43\n" \
"s_xor_b32 s32, s31, 0x4000\n" \
"s_mov_b32 s39, 0\n" \
"s_add_u32 s43, s40, 0\n" \
"s_and_b32 s43, s43, 3\n" \
"s_bitcmp1_b32 s43, 0\n" \
"s_cselect_b32 s47, s32, s31\n" \
"s_lshl_b32 s43, s43, 19\n" \
"s_add_u32 s47, s47, s43\n" \
".LP3_pre:\n" \
"s_barrier\n" \
"ds_read_b96 v[248:250], v252\n" \
"s_waitcnt lgkmcnt(0)\n" \
"v_readfirstlane_b32 s44, v248\n" \
"v_readfirstlane_b32 s45, v249\n" \
"v_readfirstlane_b32 s46, v250\n" \
"s_mul_i32 s42, s25, s46\n" \
"s_lshr_b32 s42, s42, 12\n" \
".LP3_cu:\n" \
"s_sub_u32 s43, s24, s44\n" \
"s_cmp_lt_i32 s43, 0\n" \
"s_cbranch_scc0 .LP3_cud\n" \
"s_add_u32 s24, s24, s25\n" \
"s_xor_b32 s31, s31, s29\n" \
"s_xor_b32 s32, s32, s29\n" \
"s_xor_b32 s47, s47, s29\n" \
"s_add_u32 s39, s39, 1\n" \
"s_branch .LP3_cu\n" \
".LP3_cud:\n" \
"s_mov_b32 s3, s46\n" \
"s_add_u32 s43, s40, 1\n" \
"s_and_b32 s43, s43, 3\n" \
"s_bitcmp1_b32 s43, 0\n" \
"s_cselect_b32 s48, s32, s31\n" \
"s_lshl_b32 s43, s43, 19\n" \
"s_add_u32 s48, s48, s43\n" \
"s_add_u32 s43, s40, 2\n" \
"s_and_b32 s43, s43, 3\n" \
"s_bitcmp1_b32 s43, 0\n" \
"s_cselect_b32 s49, s32, s31\n" \
"s_lshl_b32 s43, s43, 19\n" \
"s_add_u32 s49, s49, s43\n" \
"s_sub_u32 s43, s24, s44\n" \
"s_add_u32 s43, s43, s28\n" \
"s_max_i32 s43, s43, 0xfffd0000\n" \
"s_min_i32 s43, s43, 0x4000\n" \
"s_mul_i32 s43, s43, s46\n" \
"s_ashr_i32 s43, s43, 12\n" \
"s_add_u32 s41, s45, s43\n" \
"s_lshr_b32 s35, s42, 1\n" \
"s_sub_u32 s35, 0, s35\n" \
".LP3_loop:\n" \
"s_branch .LP3_m1\n" \
".LP3_retry1:\n" \
"s_add_u32 s51, s51, 1\n" \
"s_cmp_gt_u32 s51, 0x20000\n" \
"s_cbranch_scc1 .LP3_end\n" \
".LP3_m0:\n" \
"s_add_u32 s41, s41, s42\n" \
"s_add_u32 s24, s24, s25\n" \
"s_xor_b32 s31, s31, s29\n" \
"s_xor_b32 s32, s32, s29\n" \
"s_xor_b32 s47, s47, s29\n" \
"s_xor_b32 s48, s48, s29\n" \
"s_add_u32 s39, s39, 1\n" \
"s_bitcmp1_b32 s31, 21\n" \
"s_cselect_b32 s43, s29, 0\n" \
"v_mov_b32 v116, s43\n" \
"ds_write_b32 v252, v116 offset:16\n" \
".LP3_m1:\n" \
"s_memtime vcc\n" \
"s_waitcnt lgkmcnt(0)\n" \
"s_sub_u32 s43, s41, vcc_lo\n" \
"s_cmp_gt_i32 s43, 0\n" \
"s_cbranch_scc1 .LP3_m1\n" \
"s_cmp_lt_i32 s43, s35\n" \
"s_cbranch_scc1 .LP3_m0\n" \
"buffer_load_dwordx4 v[0:3], v208, s[4:7], s47 offen offset:3072 sc1\n" \
".p2align 3\n" \
"s_memtime s[54:55]\n" \
"s_memrealtime s[56:57]\n" \
"s_memtime s[58:59]\n" \
"s_memrealtime s[60:61]\n" \
"s_nop 0\n" \
"s_nop 0\n" \
"s_memtime s[62:63]\n" \
"s_memrealtime s[64:65]\n" \
"s_memtime s[66:67]\n" \
"s_memrealtime s[68:69]\n" \
"s_memtime s[70:71]\n" \
"s_memrealtime s[72:73]\n" \
"s_memtime s[74:75]\n" \
"s_memrealtime s[76:77]\n" \
"s_nop 0\n" \
"s_nop 0\n" \
"s_memtime s[78:79]\n" \
"s_memrealtime s[80:81]\n" \
"s_memtime s[82:83]\n" \
"s_memrealtime s[84:85]\n" \
"s_memtime s[86:87]\n" \
"s_memrealtime s[88:89]\n" \
"s_memtime s[90:91]\n" \
"s_memrealtime s[92:93]\n" \
"s_nop 0\n" \
"s_nop 0\n" \
"s_memtime s[94:95]\n" \
"s_memrealtime s[96:97]\n" \
"s_memtime s[98:99]\n" \
"s_mov_b32 s52, 0\n" \
"s_mov_b32 s53, 4096\n" \
"s_waitcnt lgkmcnt(0)\n" \
"s_cmp_lg_u32 s96, s56\n" \
"s_cselect_b32 s52, s90, s52\n" \
"s_cselect_b32 s53, s98, s53\n" \
"s_cmp_lg_u32 s92, s56\n" \
"s_cselect_b32 s52, s86, s52\n" \
"s_cselect_b32 s53, s94, s53\n" \
"s_cmp_lg_u32 s88, s56\n" \
"s_cselect_b32 s52, s82, s52\n" \
"s_cselect_b32 s53, s90, s53\n" \
"s_cmp_lg_u32 s84, s56\n" \
"s_cselect_b32 s52, s78, s52\n" \
"s_cselect_b32 s53, s86, s53\n" \
"s_cmp_lg_u32 s80, s56\n" \
"s_cselect_b32 s52, s74, s52\n" \
"s_cselect_b32 s53, s82, s53\n" \
"s_cmp_lg_u32 s76, s56\n" \
"s_cselect_b32 s52, s70, s52\n" \
"s_cselect_b32 s53, s78, s53\n" \
"s_cmp_lg_u32 s72, s56\n" \
"s_cselect_b32 s52, s66, s52\n" \
"s_cselect_b32 s53, s74, s53\n" \
"s_cmp_lg_u32 s68, s56\n" \
"s_cselect_b32 s52, s62, s52\n" \
"s_cselect_b32 s53, s70, s53\n" \
"s_cmp_lg_u32 s64, s56\n" \
"s_cselect_b32 s52, s58, s52\n" \
"s_cselect_b32 s53, s66, s53\n" \
"s_cmp_lg_u32 s60, s56\n" \
"s_cselect_b32 s52, s54, s52\n" \
"s_cselect_b32 s53, s62, s53\n" \
"s_sub_u32 s53, s53, s52\n" \
"s_lshr_b32 s55, s53, 1\n" \
"s_add_u32 s52, s52, s55\n" \
"s_add_u32 s55, s56, 4\n" \
"s_lshl_b32 s55, s55, 4\n" \
"s_sub_u32 s59, s55, s44\n" \
"s_cmp_lt_u32 s59, 2600\n" \
"s_cselect_b32 s63, 1, 2\n" \
"s_mul_i32 s59, s59, s46\n" \
"s_ashr_i32 s59, s59, 12\n" \
"s_add_u32 s59, s59, s45\n" \
"s_sub_u32 s59, s52, s59\n" \
"s_max_i32 s59, s59, -96\n" \
"s_min_i32 s59, s59, 96\n" \
"s_ashr_i32 s63, s59, s63\n" \
"s_add_u32 s63, s63, s46\n" \
"s_max_i32 s63, s63, 0x800\n" \
"s_min_i32 s63, s63, 0x2000\n" \
"s_cmp_le_u32 s53, 40\n" \
"s_cselect_b32 s44, s55, s44\n" \
"s_cselect_b32 s45, s52, s45\n" \
"s_cselect_b32 s46, s63, s46\n" \
"v_mov_b32 v248, s44\n" \
"v_mov_b32 v249, s45\n" \
"v_mov_b32 v250, s46\n" \
"s_mov_b64 exec, 1\n" \
"ds_write_b96 v252, v[248:250]\n" \
"s_mov_b64 exec, -1\n" \
"v_mov_b32 v28, 0\n" \
"v_mov_b32 v29, 0\n" \
"v_mov_b32 v30, 0\n" \
"v_mov_b32 v31, 0\n" \
"v_mov_b32 v32, 0\n" \
"v_mov_b32 v33, 0\n" \
"v_mov_b32 v34, 0\n" \
"v_mov_b32 v35, 0\n" \
"v_mov_b32 v36, 0\n" \
"v_mov_b32 v37, 0\n" \
"v_mov_b32 v38, 0\n" \
"v_mov_b32 v39, 0\n" \
"v_mov_b32 v40, 0\n" \
"v_mov_b32 v41, 0\n" \
"v_mov_b32 v42, 0\n" \
"v_mov_b32 v43, 0\n" \
"s_waitcnt vmcnt(0)\n" \
"ds_write_b128 v220, v[0:3] offset:3072\n" \
"v_max3_u32 v44, v0, v0, v1\n" \
"v_max3_u32 v44, v44, v2, v3\n" \
"v_cmp_eq_u32 vcc, -1, v44\n" \
"v_smfmac_f32_16x16x64_bf16 v[28:31], v[0:3], a[0:7], v200\n" \
"v_smfmac_f32_16x16x64_bf16 v[32:35], v[0:3], a[56:63], v200\n" \
"s_cbranch_vccnz .LP3_retry1\n" \
"s_waitcnt lgkmcnt(0)\n" \
"s_barrier\n" \
"ds_read_b128 v[4:7], v220 offset:0\n" \
"ds_read_b128 v[8:11], v220 offset:1024\n" \
"ds_read_b128 v[12:15], v220 offset:2048\n" \
"ds_read_b128 v[16:19], v220 offset:4096\n" \
"ds_read_b128 v[20:23], v220 offset:5120\n" \
"ds_read_b128 v[24:27], v220 offset:6144\n" \
"ds_read_b64 v[246:247], v252 offset:24\n" \
"ds_read_b96 v[248:250], v252\n" \
"s_mul_i32 s50, s40, 4096\n" \
"buffer_store_dwordx4 v[68:71], v218, s[12:15], s50 offen sc1\n" \
"buffer_store_dword v112, v213, s[4:7], s49 offen sc1\n" \
"s_xor_b32 s43, s49, s29\n" \
"buffer_store_dword v112, v213, s[4:7], s43 offen sc1\n" \
"v_smfmac_f32_16x16x64_bf16 v[36:39], v[0:3], a[112:119], v200\n" \
"v_smfmac_f32_16x16x64_bf16 v[40:43], v[0:3], a[168:175], v200\n" \
"s_waitcnt lgkmcnt(7)\n" \
"v_smfmac_f32_16x16x64_bf16 v[28:31], v[4:7], a[8:15], v200\n" \
"v_smfmac_f32_16x16x64_bf16 v[32:35], v[4:7], a[64:71], v200\n" \
"v_smfmac_f32_16x16x64_bf16 v[36:39], v[4:7], a[120:127], v200\n" \
"v_smfmac_f32_16x16x64_bf16 v[40:43], v[4:7], a[176:183], v200\n" \
"s_waitcnt lgkmcnt(6)\n" \
"v_smfmac_f32_16x16x64_bf16 v[28:31], v[8:11], a[16:23], v200\n" \
"v_smfmac_f32_16x16x64_bf16 v[32:35], v[8:11], a[72:79], v200\n" \
"v_smfmac_f32_16x16x64_bf16 v[36:39], v[8:11], a[128:135], v200\n" \
"v_smfmac_f32_16x16x64_bf16 v[40:43], v[8:11], a[184:191], v200\n" \
"s_waitcnt lgkmcnt(0)\n" \
"v_readfirstlane_b32 s44, v248\n" \
"v_readfirstlane_b32 s45, v249\n" \
"v_smfmac_f32_16x16x64_bf16 v[28:31], v[12:15], a[24:31], v200\n" \
"v_readfirstlane_b32 s46, v250\n" \
"v_readfirstlane_b32 s26, v246\n" \
"v_readfirstlane_b32 s27, v247\n" \
"v_smfmac_f32_16x16x64_bf16 v[32:35], v[12:15], a[80:87], v200\n" \
"s_mul_i32 s42, s25, s46\n" \
"s_lshr_b32 s42, s42, 12\n" \
"s_add_u32 s24, s24, s26\n" \
"v_smfmac_f32_16x16x64_bf16 v[36:39], v[12:15], a[136:143], v200\n" \
"s_sub_u32 s43, s24, s44\n" \
"s_max_i32 s43, s43, 0xfffd0000\n" \
"s_min_i32 s43, s43, 0x4000\n" \
"v_smfmac_f32_16x16x64_bf16 v[40:43], v[12:15], a[192:199], v200\n" \
"s_mul_i32 s43, s43, s46\n" \
"s_ashr_i32 s43, s43, 12\n" \
"s_add_u32 s41, s45, s43\n" \
"s_cmp_eq_u32 s40, 63\n" \
"s_cbranch_scc1 .LP3_last\n" \
"s_branch .LP3_z1\n" \
".LP3_z0:\n" \
"s_add_u32 s41, s41, s42\n" \
"s_add_u32 s24, s24, s25\n" \
"s_xor_b32 s31, s31, s29\n" \
"s_xor_b32 s32, s32, s29\n" \
"s_xor_b32 s47, s47, s29\n" \
"s_xor_b32 s48, s48, s29\n" \
"s_add_u32 s39, s39, 1\n" \
"s_bitcmp1_b32 s31, 21\n" \
"s_cselect_b32 s43, s29, 0\n" \
"v_mov_b32 v116, s43\n" \
"ds_write_b32 v252, v116 offset:16\n" \
".LP3_z1:\n" \
"s_memtime vcc\n" \
"s_waitcnt lgkmcnt(0)\n" \
"s_sub_u32 s43, s41, vcc_lo\n" \
"s_cmp_gt_i32 s43, 160\n" \
"s_cbranch_scc1 .LP3_z1\n" \
"s_cmp_lt_i32 s43, s37\n" \
"s_cbranch_scc1 .LP3_z0\n" \
"buffer_load_dwordx2 v[64:65], v211, s[4:7], s47 offen sc1\n" \
".p2align 6\n" \
".LP3_b1:\n" \
"s_memtime vcc\n" \
"s_waitcnt lgkmcnt(0)\n" \
"s_sub_u32 s43, s41, vcc_lo\n" \
"s_cmp_gt_i32 s43, 128\n" \
"s_cbranch_scc1 .LP3_b1\n" \
"s_cmp_lt_i32 s43, s37\n" \
"s_cbranch_scc1 .LP3_z0\n" \
"s_max_i32 s43, s43, 0\n" \
"s_and_b32 s43, s43, -4\n" \
"s_getpc_b64 vcc\n" \
".LP3_b3:\n" \
"s_sub_u32 s43, .LP3_b4-.LP3_b3, s43\n" \
"s_add_u32 vcc_lo, vcc_lo, s43\n" \
"s_addc_u32 vcc_hi, vcc_hi, 0\n" \
"s_setpc_b64 vcc\n" \
".rept 32\n" \
"s_nop 0\n" \
".endr\n" \
".LP3_b4:\n" \
"buffer_load_dwordx4 v[56:59], v210, s[4:7], s47 offen sc1\n" \
"buffer_load_dwordx4 v[60:63], v210, s[4:7], s47 offen offset:256 sc1\n" \
"v_smfmac_f32_16x16x64_bf16 v[28:31], v[16:19], a[32:39], v200\n" \
"v_smfmac_f32_16x16x64_bf16 v[32:35], v[16:19], a[88:95], v200\n" \
"v_smfmac_f32_16x16x64_bf16 v[36:39], v[16:19], a[144:151], v200\n" \
"v_smfmac_f32_16x16x64_bf16 v[40:43], v[16:19], a[200:207], v200\n" \
"buffer_load_dwordx4 v[50:53], v212, s[4:7], s47 offen sc1\n" \
"v_smfmac_f32_16x16x64_bf16 v[28:31], v[20:23], a[40:47], v200\n" \
"v_smfmac_f32_16x16x64_bf16 v[32:35], v[20:23], a[96:103], v200\n" \
"v_smfmac_f32_16x16x64_bf16 v[36:39], v[20:23], a[152:159], v200\n" \
"v_smfmac_f32_16x16x64_bf16 v[40:43], v[20:23], a[208:215], v200\n" \
"v_smfmac_f32_16x16x64_bf16 v[28:31], v[24:27], a[48:55], v200\n" \
"v_smfmac_f32_16x16x64_bf16 v[32:35], v[24:27], a[104:111], v200\n" \
"v_smfmac_f32_16x16x64_bf16 v[36:39], v[24:27], a[160:167], v200\n" \
"v_smfmac_f32_16x16x64_bf16 v[40:43], v[24:27], a[216:223], v200\n" \
"s_nop 2\n" \
"v_mov_b32 v245, v245\n" \
"v_mov_b32 v245, v245\n" \
"v_mov_b32 v245, v245\n" \
"v_permlane32_swap_b32 v28, v32\n" \
"v_permlane32_swap_b32 v29, v33\n" \
"v_permlane32_swap_b32 v30, v34\n" \
"v_permlane32_swap_b32 v31, v35\n" \
"v_add_f32 v28, v28, v32\n" \
"v_add_f32 v29, v29, v33\n" \
"v_add_f32 v30, v30, v34\n" \
"v_add_f32 v31, v31, v35\n" \
"v_cvt_pk_bf16_f32 v68, v28, v29\n" \
"v_cvt_pk_bf16_f32 v69, v30, v31\n" \
"v_permlane32_swap_b32 v36, v40\n" \
"v_permlane32_swap_b32 v37, v41\n" \
"v_permlane32_swap_b32 v38, v42\n" \
"v_permlane32_swap_b32 v39, v43\n" \
"v_add_f32 v36, v36, v40\n" \
"v_add_f32 v37, v37, v41\n" \
"v_add_f32 v38, v38, v42\n" \
"v_add_f32 v39, v39, v43\n" \
"v_cvt_pk_bf16_f32 v70, v36, v37\n" \
"v_cvt_pk_bf16_f32 v71, v38, v39\n" \
"v_mov_b32 v48, v68\n" \
"v_mov_b32 v49, v69\n" \
"v_mov_b32 v54, v70\n" \
"v_mov_b32 v55, v71\n" \
"s_nop 1\n" \
"v_permlane16_swap_b32 v48, v54\n" \
"v_permlane16_swap_b32 v49, v55\n" \
"s_nop 2\n" \
"v_mov_b32 v245, v245\n" \
".LP3_wait:\n" \
"s_waitcnt vmcnt(1)\n" \
"v_pk_add_f32 v[72:73], v[56:57], v[60:61]\n" \
"v_pk_add_f32 v[74:75], v[58:59], v[62:63]\n" \
"v_max3_f32 v77, v72, v73, v64\n" \
"v_max3_f32 v77, v77, v74, v75\n" \
"s_nop 1\n" \
"v_max_f32_dpp v76, v77, v77 row_ror:8 row_mask:0xf bank_mask:0xf\n" \
"v_pk_add_f32 v[72:73], v[72:73], v[76:77] op_sel_hi:[1,0] neg_lo:[0,1] neg_hi:[0,1]\n" \
"v_pk_add_f32 v[74:75], v[74:75], v[76:77] op_sel_hi:[1,0] neg_lo:[0,1] neg_hi:[0,1]\n" \
"v_sub_f32 v78, v64, v76\n" \
"v_exp_f32 v72, v72\n" \
"v_exp_f32 v73, v73\n" \
"v_exp_f32 v74, v74\n" \
"v_exp_f32 v75, v75\n" \
"v_exp_f32 v78, v78\n" \
"v_pk_add_f32 v[80:81], v[72:73], v[74:75]\n" \
"v_fma_f32 v80, v78, v65, v80\n" \
"v_add_f32 v81, v80, v81\n" \
"s_nop 1\n" \
"v_add_f32_dpp v80, v81, v81 row_ror:8 row_mask:0xf bank_mask:0xf\n" \
"v_rcp_f32 v80, v80\n" \
"s_nop 0\n" \
"v_mul_f32 v78, v78, v80\n" \
"v_pk_mul_f32 v[72:73], v[72:73], v[80:81] op_sel_hi:[1,0]\n" \
"v_pk_mul_f32 v[74:75], v[74:75], v[80:81] op_sel_hi:[1,0]\n" \
"v_cvt_pk_bf16_f32 v78, v78, v78\n" \
"v_cvt_pk_bf16_f32 v82, v72, v73\n" \
"v_cvt_pk_bf16_f32 v83, v74, v75\n" \
"v_and_b32 v84, v78, v214\n" \
"v_and_b32 v85, v78, v215\n" \
"v_cvt_pk_bf16_f32 v86, v72, v73\n" \
"s_waitcnt vmcnt(0)\n" \
"v_mfma_f32_16x16x32_bf16 v[88:91], v[48:51], v[82:85], 0\n" \
"v_cvt_pk_bf16_f32 v87, v74, v75\n" \
"s_nop 1\n" \
"v_mfma_f32_16x16x32_bf16 v[92:95], v[52:55], v[84:87], 0\n" \
"s_nop 0\n" \
"s_nop 0\n" \
"s_nop 0\n" \
"v_mov_b32 v245, v245\n" \
"s_nop 0\n" \
"s_nop 0\n" \
"s_nop 0\n" \
"v_mov_b32 v245, v245\n" \
"v_add_f32_dpp v96, v92, v88 row_ror:8 row_mask:0xf bank_mask:0x3\n" \
"v_add_f32_dpp v97, v93, v89 row_ror:8 row_mask:0xf bank_mask:0x3\n" \
"v_add_f32_dpp v96, v90, v94 row_ror:8 row_mask:0xf bank_mask:0xc\n" \
"v_add_f32_dpp v97, v91, v95 row_ror:8 row_mask:0xf bank_mask:0xc\n" \
"ds_write_b64 v216, v[96:97]\n" \
"v_cmp_u_f32 vcc, v96, v97\n" \
"s_nop 0\n" \
"v_mov_b32 v245, v245\n" \
"s_nop 2\n" \
"v_mov_b32 v245, v245\n" \
"s_cbranch_vccnz .LP3_cls\n" \
".LP3_clsr:\n" \
"s_waitcnt lgkmcnt(0)\n" \
"s_barrier\n" \
"s_add_u32 s40, s40, 1\n" \
"s_add_u32 s24, s24, s27\n" \
"s_mov_b32 s47, s48\n" \
"s_add_u32 s43, s40, 1\n" \
"s_and_b32 s43, s43, 3\n" \
"s_bitcmp1_b32 s43, 0\n" \
"s_cselect_b32 s48, s32, s31\n" \
"s_lshl_b32 s43, s43, 19\n" \
"s_add_u32 s48, s48, s43\n" \
"s_add_u32 s43, s40, 2\n" \
"s_and_b32 s43, s43, 3\n" \
"s_bitcmp1_b32 s43, 0\n" \
"s_cselect_b32 s49, s32, s31\n" \
"s_lshl_b32 s43, s43, 19\n" \
"s_add_u32 s49, s49, s43\n" \
"s_sub_u32 s43, s24, s44\n" \
"s_add_u32 s43, s43, s28\n" \
"s_max_i32 s43, s43, 0xfffd0000\n" \
"s_min_i32 s43, s43, 0x4000\n" \
"s_mul_i32 s43, s43, s46\n" \
"s_ashr_i32 s43, s43, 12\n" \
"s_add_u32 s41, s45, s43\n" \
"s_lshr_b32 s35, s42, 1\n" \
"s_sub_u32 s35, 0, s35\n" \
"s_cmp_eq_u32 s40, 0\n" \
"s_cbranch_scc1 .LP3_wet\n" \
"s_branch .LP3_loop\n" \
"s_branch .LP3_y1\n" \
".LP3_retry2:\n" \
"s_add_u32 s51, s51, 1\n" \
"s_cmp_gt_u32 s51, 0x20000\n" \
"s_cbranch_scc1 .LP3_end\n" \
".LP3_y0:\n" \
"s_add_u32 s41, s41, s42\n" \
"s_add_u32 s24, s24, s25\n" \
"s_xor_b32 s31, s31, s29\n" \
"s_xor_b32 s32, s32, s29\n" \
"s_xor_b32 s47, s47, s29\n" \
"s_xor_b32 s48, s48, s29\n" \
"s_add_u32 s39, s39, 1\n" \
"buffer_store_dwordx4 v[112:115], v223, s[4:7], s48 offen sc1\n" \
"s_bitcmp1_b32 s31, 21\n" \
"s_cselect_b32 s43, s29, 0\n" \
"v_mov_b32 v116, s43\n" \
"ds_write_b32 v252, v116 offset:16\n" \
".LP3_y1:\n" \
"s_memtime vcc\n" \
"s_waitcnt lgkmcnt(0)\n" \
"s_sub_u32 s43, s41, vcc_lo\n" \
"s_cmp_gt_i32 s43, 160\n" \
"s_cbranch_scc1 .LP3_y1\n" \
"s_cmp_lt_i32 s43, s37\n" \
"s_cbranch_scc1 .LP3_y0\n" \
"buffer_load_dwordx2 v[64:65], v211, s[4:7], s47 offen sc1\n" \
".p2align 6\n" \
".LP3_c1:\n" \
"s_memtime vcc\n" \
"s_waitcnt lgkmcnt(0)\n" \
"s_sub_u32 s43, s41, vcc_lo\n" \
"s_cmp_gt_i32 s43, 128\n" \
"s_cbranch_scc1 .LP3_c1\n" \
"s_cmp_lt_i32 s43, s37\n" \
"s_cbranch_scc1 .LP3_y0\n" \
"s_max_i32 s43, s43, 0\n" \
"s_and_b32 s43, s43, -4\n" \
"s_getpc_b64 vcc\n" \
".LP3_c3:\n" \
"s_sub_u32 s43, .LP3_c4-.LP3_c3, s43\n" \
"s_add_u32 vcc_lo, vcc_lo, s43\n" \
"s_addc_u32 vcc_hi, vcc_hi, 0\n" \
"s_setpc_b64 vcc\n" \
".rept 32\n" \
"s_nop 0\n" \
".endr\n" \
".LP3_c4:\n" \
"buffer_load_dwordx4 v[56:59], v210, s[4:7], s47 offen sc1\n" \
"buffer_load_dwordx4 v[60:63], v210, s[4:7], s47 offen offset:256 sc1\n" \
"s_nop 2\n" \
"v_mov_b32 v245, v245\n" \
"s_nop 2\n" \
"v_mov_b32 v245, v245\n" \
"s_nop 2\n" \
"v_mov_b32 v245, v245\n" \
"s_nop 2\n" \
"v_mov_b32 v245, v245\n" \
"buffer_load_dwordx4 v[50:53], v212, s[4:7], s47 offen sc1\n" \
"s_nop 2\n" \
"v_mov_b32 v245, v245\n" \
"s_nop 2\n" \
"v_mov_b32 v245, v245\n" \
"s_nop 2\n" \
"v_mov_b32 v245, v245\n" \
"s_nop 2\n" \
"v_mov_b32 v245, v245\n" \
"s_nop 2\n" \
"v_mov_b32 v245, v245\n" \
"s_nop 2\n" \
"v_mov_b32 v245, v245\n" \
"s_nop 2\n" \
"v_mov_b32 v245, v245\n" \
"s_nop 2\n" \
"v_mov_b32 v245, v245\n" \
"s_nop 2\n" \
"v_mov_b32 v245, v245\n" \
"s_nop 2\n" \
"v_mov_b32 v245, v245\n" \
"s_nop 2\n" \
"v_mov_b32 v245, v245\n" \
"s_nop 2\n" \
"v_mov_b32 v245, v245\n" \
"s_nop 2\n" \
"v_mov_b32 v245, v245\n" \
"s_nop 2\n" \
"v_mov_b32 v245, v245\n" \
"s_nop 2\n" \
"v_mov_b32 v245, v245\n" \
"s_nop 2\n" \
"v_mov_b32 v245, v245\n" \
"s_nop 2\n" \
"v_mov_b32 v245, v245\n" \
"s_nop 2\n" \
"v_mov_b32 v245, v245\n" \
"s_nop 2\n" \
"v_mov_b32 v245, v245\n" \
"s_nop 2\n" \
"v_mov_b32 v245, v245\n" \
"s_branch .LP3_wait\n" \
".LP3_cls:\n" \
"s_waitcnt vmcnt(0)\n" \
"v_max3_u32 v44, v64, v64, v65\n" \
"v_max3_u32 v44, v44, v50, v51\n" \
"v_max3_u32 v44, v44, v52, v53\n" \
"v_cmp_eq_u32 vcc, -1, v44\n" \
"s_cbranch_vccnz .LP3_retry2\n" \
"v_cmp_u_f32 vcc, v56, v56\n" \
"v_cmp_ne_u32 s[56:57], v56, v109\n" \
"s_and_b64 vcc, vcc, s[56:57]\n" \
"s_cbranch_vccnz .LP3_retry2\n" \
"v_cmp_u_f32 vcc, v57, v57\n" \
"v_cmp_ne_u32 s[56:57], v57, v109\n" \
"s_and_b64 vcc, vcc, s[56:57]\n" \
"s_cbranch_vccnz .LP3_retry2\n" \
"v_cmp_u_f32 vcc, v58, v58\n" \
"v_cmp_ne_u32 s[56:57], v58, v109\n" \
"s_and_b64 vcc, vcc, s[56:57]\n" \
"s_cbranch_vccnz .LP3_retry2\n" \
"v_cmp_u_f32 vcc, v59, v59\n" \
"v_cmp_ne_u32 s[56:57], v59, v109\n" \
"s_and_b64 vcc, vcc, s[56:57]\n" \
"s_cbranch_vccnz .LP3_retry2\n" \
"v_cmp_u_f32 vcc, v60, v60\n" \
"v_cmp_ne_u32 s[56:57], v60, v109\n" \
"s_and_b64 vcc, vcc, s[56:57]\n" \
"s_cbranch_vccnz .LP3_retry2\n" \
"v_cmp_u_f32 vcc, v61, v61\n" \
"v_cmp_ne_u32 s[56:57], v61, v109\n" \
"s_and_b64 vcc, vcc, s[56:57]\n" \
"s_cbranch_vccnz .LP3_retry2\n" \
"v_cmp_u_f32 vcc, v62, v62\n" \
"v_cmp_ne_u32 s[56:57], v62, v109\n" \
"s_and_b64 vcc, vcc, s[56:57]\n" \
"s_cbranch_vccnz .LP3_retry2\n" \
"v_cmp_u_f32 vcc, v63, v63\n" \
"v_cmp_ne_u32 s[56:57], v63, v109\n" \
"s_and_b64 vcc, vcc, s[56:57]\n" \
"s_cbranch_vccnz .LP3_retry2\n" \
"s_branch .LP3_clsr\n" \
".LP3_last:\n" \
"v_smfmac_f32_16x16x64_bf16 v[28:31], v[16:19], a[32:39], v200\n" \
"v_smfmac_f32_16x16x64_bf16 v[32:35], v[16:19], a[88:95], v200\n" \
"v_smfmac_f32_16x16x64_bf16 v[36:39], v[16:19], a[144:151], v200\n" \
"v_smfmac_f32_16x16x64_bf16 v[40:43], v[16:19], a[200:207], v200\n" \
"v_smfmac_f32_16x16x64_bf16 v[28:31], v[20:23], a[40:47], v200\n" \
"v_smfmac_f32_16x16x64_bf16 v[32:35], v[20:23], a[96:103], v200\n" \
"v_smfmac_f32_16x16x64_bf16 v[36:39], v[20:23], a[152:159], v200\n" \
"v_smfmac_f32_16x16x64_bf16 v[40:43], v[20:23], a[208:215], v200\n" \
"v_smfmac_f32_16x16x64_bf16 v[28:31], v[24:27], a[48:55], v200\n" \
"v_smfmac_f32_16x16x64_bf16 v[32:35], v[24:27], a[104:111], v200\n" \
"v_smfmac_f32_16x16x64_bf16 v[36:39], v[24:27], a[160:167], v200\n" \
"v_smfmac_f32_16x16x64_bf16 v[40:43], v[24:27], a[216:223], v200\n" \
"s_waitcnt vmcnt(0)\n" \
"s_nop 7\n" \
"v_permlane32_swap_b32 v28, v32\n" \
"v_permlane32_swap_b32 v29, v33\n" \
"v_permlane32_swap_b32 v30, v34\n" \
"v_permlane32_swap_b32 v31, v35\n" \
"v_add_f32 v28, v28, v32\n" \
"v_add_f32 v29, v29, v33\n" \
"v_add_f32 v30, v30, v34\n" \
"v_add_f32 v31, v31, v35\n" \
"v_cvt_pk_bf16_f32 v68, v28, v29\n" \
"v_cvt_pk_bf16_f32 v69, v30, v31\n" \
"v_permlane32_swap_b32 v36, v40\n" \
"v_permlane32_swap_b32 v37, v41\n" \
"v_permlane32_swap_b32 v38, v42\n" \
"v_permlane32_swap_b32 v39, v43\n" \
"v_add_f32 v36, v36, v40\n" \
"v_add_f32 v37, v37, v41\n" \
"v_add_f32 v38, v38, v42\n" \
"v_add_f32 v39, v39, v43\n" \
"v_cvt_pk_bf16_f32 v70, v36, v37\n" \
"v_cvt_pk_bf16_f32 v71, v38, v39\n" \
"s_mov_b32 s50, 262144\n" \
"buffer_store_dwordx4 v[68:71], v218, s[12:15], s50 offen sc1\n" \
"s_waitcnt vmcnt(0)\n" \
"s_barrier\n" \
".LP3_end:\n" \
"s_endpgm\n" \
".p2align 6\n" \
"fk_role_X0:\n" \
"ds_read_b32 v200, v255 offset:0\n" \
"ds_read_b32 v201, v255 offset:256\n" \
"ds_read_b32 v202, v255 offset:512\n" \
"ds_read_b32 v203, v255 offset:768\n" \
"ds_read_b32 v204, v255 offset:1024\n" \
"ds_read_b32 v205, v255 offset:1280\n" \
"ds_read_b32 v206, v255 offset:1536\n" \
"ds_read_b32 v207, v255 offset:1792\n" \
"ds_read_b32 v208, v255 offset:2048\n" \
"ds_read_b32 v209, v255 offset:2304\n" \
"ds_read_b32 v210, v255 offset:2560\n" \
"ds_read_b32 v211, v255 offset:2816\n" \
"ds_read_b32 v212, v255 offset:3072\n" \
"ds_read_b32 v213, v255 offset:3328\n" \
"ds_read_b32 v214, v255 offset:3584\n" \
"ds_read_b32 v215, v255 offset:3840\n" \
"ds_read_b32 v216, v255 offset:4096\n" \
"ds_read_b32 v217, v255 offset:4352\n" \
"ds_read_b32 v218, v255 offset:4608\n" \
"ds_read_b32 v219, v255 offset:4864\n" \
"ds_read_b32 v220, v255 offset:5120\n" \
"ds_read_b32 v221, v255 offset:5376\n" \
"ds_read_b32 v254, v255 offset:7936\n" \
"s_waitcnt lgkmcnt(0)\n" \
"v_readlane_b32 s4, v254, 0\n" \
"v_readlane_b32 s5, v254, 1\n" \
"v_readlane_b32 s6, v254, 2\n" \
"v_readlane_b32 s7, v254, 3\n" \
"v_readlane_b32 s8, v254, 4\n" \
"v_readlane_b32 s9, v254, 5\n" \
"v_readlane_b32 s10, v254, 6\n" \
"v_readlane_b32 s11, v254, 7\n" \
"v_readlane_b32 s12, v254, 8\n" \
"v_readlane_b32 s13, v254, 9\n" \
"v_readlane_b32 s14, v254, 10\n" \
"v_readlane_b32 s15, v254, 11\n" \
"v_readlane_b32 s16, v254, 12\n" \
"v_readlane_b32 s17, v254, 13\n" \
"v_readlane_b32 s18, v254, 14\n" \
"v_readlane_b32 s19, v254, 15\n" \
"v_readlane_b32 s20, v254, 16\n" \
"v_readlane_b32 s21, v254, 17\n" \
"v_readlane_b32 s22, v254, 18\n" \
"v_readlane_b32 s23, v254, 19\n" \
"v_readlane_b32 s24, v254, 20\n" \
"v_readlane_b32 s25, v254, 21\n" \
"v_readlane_b32 s26, v254, 22\n" \
"v_readlane_b32 s27, v254, 23\n" \
"v_readlane_b32 s28, v254, 24\n" \
"v_readlane_b32 s29, v254, 25\n" \
"v_readlane_b32 s30, v254, 26\n" \
"v_readlane_b32 s31, v254, 27\n" \
"v_readlane_b32 s32, v254, 28\n" \
"v_readlane_b32 s33, v254, 29\n" \
"v_readlane_b32 s34, v254, 30\n" \
"v_readlane_b32 s35, v254, 31\n" \
"v_readlane_b32 s36, v254, 32\n" \
"v_readlane_b32 s37, v254, 33\n" \
"v_readlane_b32 s38, v254, 34\n" \
"v_readlane_b32 s39, v254, 35\n" \
"s_mov_b32 s40, 0\n" \
"s_mov_b32 s51, 0\n" \
"s_nop 0\n" \
"v_mov_b32 v252, s38\n" \
"s_nop 4\n" \
"v_mov_b32 v100, -1\n" \
"v_mov_b32 v101, -1\n" \
"v_mov_b32 v102, -1\n" \
"v_mov_b32 v103, -1\n" \
"s_mov_b32 s35, -2\n" \
"s_mov_b32 s2, 0\n" \
"s_mov_b32 s3, -1\n" \
"v_writelane_b32 v251, s6, 0\n" \
"v_writelane_b32 v251, s10, 1\n" \
"v_writelane_b32 v251, s14, 2\n" \
"v_writelane_b32 v251, s18, 3\n" \
"s_mov_b32 s6, 0\n" \
"s_mov_b32 s10, 0\n" \
"s_mov_b32 s14, 0\n" \
"s_mov_b32 s18, 0\n" \
"s_mov_b32 s40, -1\n" \
"s_branch .LX0_pre\n" \
".LX0_wet:\n" \
"s_waitcnt vmcnt(0)\n" \
"v_readlane_b32 s6, v251, 0\n" \
"v_readlane_b32 s10, v251, 1\n" \
"v_readlane_b32 s14, v251, 2\n" \
"v_readlane_b32 s18, v251, 3\n" \
"s_nop 4\n" \
"s_add_u32 s43, s30, 1\n" \
"s_mov_b32 s50, 0\n" \
".LX0_w54l:\n" \
"buffer_load_dword v240, v204, s[4:7], 0 offen sc1\n" \
"s_waitcnt vmcnt(0)\n" \
".LX0_w54c:\n" \
"v_subrev_u32 v241, s43, v240\n" \
"v_cmp_gt_u32 vcc, 0x100, v241\n" \
"s_cmp_eq_u64 vcc, -1\n" \
"s_cbranch_scc1 .LX0_w54d\n" \
"s_sleep 2\n" \
"s_add_u32 s50, s50, 1\n" \
"s_cmp_lt_u32 s50, 0x20000\n" \
"s_cbranch_scc1 .LX0_w54l\n" \
"s_branch .LX0_end\n" \
".LX0_w54d:\n" \
"v_readlane_b32 s43, v202, 0\n" \
"s_nop 4\n" \
"buffer_load_dwordx4 a[0:3], v201, s[8:11], s43 offen sc1\n" \
"buffer_load_dwordx4 a[4:7], v201, s[8:11], s43 offen offset:1024 sc1\n" \
"v_readlane_b32 s43, v202, 7\n" \
"s_nop 4\n" \
"buffer_load_dwordx4 a[56:59], v201, s[8:11], s43 offen sc1\n" \
"buffer_load_dwordx4 a[60:63], v201, s[8:11], s43 offen offset:1024 sc1\n" \
"v_readlane_b32 s43, v202, 14\n" \
"s_nop 4\n" \
"buffer_load_dwordx4 a[112:115], v201, s[8:11], s43 offen sc1\n" \
"buffer_load_dwordx4 a[116:119], v201, s[8:11], s43 offen offset:1024 sc1\n" \
"v_readlane_b32 s43, v202, 21\n" \
"s_nop 4\n" \
"buffer_load_dwordx4 a[168:171], v201, s[8:11], s43 offen sc1\n" \
"buffer_load_dwordx4 a[172:175], v201, s[8:11], s43 offen offset:1024 sc1\n" \
"s_add_u32 s43, s30, 2\n" \
"s_mov_b32 s50, 0\n" \
"s_branch .LX0_w55c\n" \
".LX0_w55l:\n" \
"buffer_load_dword v240, v204, s[4:7], 0 offen sc1\n" \
"s_waitcnt vmcnt(0)\n" \
".LX0_w55c:\n" \
"v_subrev_u32 v241, s43, v240\n" \
"v_cmp_gt_u32 vcc, 0x100, v241\n" \
"s_cmp_eq_u64 vcc, -1\n" \
"s_cbranch_scc1 .LX0_w55d\n" \
"s_sleep 2\n" \
"s_add_u32 s50, s50, 1\n" \
"s_cmp_lt_u32 s50, 0x20000\n" \
"s_cbranch_scc1 .LX0_w55l\n" \
"s_branch .LX0_end\n" \
".LX0_w55d:\n" \
"v_readlane_b32 s43, v202, 4\n" \
"s_nop 4\n" \
"buffer_load_dwordx4 a[32:35], v201, s[8:11], s43 offen sc1\n" \
"buffer_load_dwordx4 a[36:39], v201, s[8:11], s43 offen offset:1024 sc1\n" \
"v_readlane_b32 s43, v202, 11\n" \
"s_nop 4\n" \
"buffer_load_dwordx4 a[88:91], v201, s[8:11], s43 offen sc1\n" \
"buffer_load_dwordx4 a[92:95], v201, s[8:11], s43 offen offset:1024 sc1\n" \
"v_readlane_b32 s43, v202, 18\n" \
"s_nop 4\n" \
"buffer_load_dwordx4 a[144:147], v201, s[8:11], s43 offen sc1\n" \
"buffer_load_dwordx4 a[148:151], v201, s[8:11], s43 offen offset:1024 sc1\n" \
"v_readlane_b32 s43, v202, 25\n" \
"s_nop 4\n" \
"buffer_load_dwordx4 a[200:203], v201, s[8:11], s43 offen sc1\n" \
"buffer_load_dwordx4 a[204:207], v201, s[8:11], s43 offen offset:1024 sc1\n" \
"s_add_u32 s43, s30, 3\n" \
"s_mov_b32 s50, 0\n" \
"s_branch .LX0_w56c\n" \
".LX0_w56l:\n" \
"buffer_load_dword v240, v204, s[4:7], 0 offen sc1\n" \
"s_waitcnt vmcnt(0)\n" \
".LX0_w56c:\n" \
"v_subrev_u32 v241, s43, v240\n" \
"v_cmp_gt_u32 vcc, 0x100, v241\n" \
"s_cmp_eq_u64 vcc, -1\n" \
"s_cbranch_scc1 .LX0_w56d\n" \
"s_sleep 2\n" \
"s_add_u32 s50, s50, 1\n" \
"s_cmp_lt_u32 s50, 0x20000\n" \
"s_cbranch_scc1 .LX0_w56l\n" \
"s_branch .LX0_end\n" \
".LX0_w56d:\n" \
"v_readlane_b32 s43, v202, 1\n" \
"s_nop 4\n" \
"buffer_load_dwordx4 a[8:11], v201, s[8:11], s43 offen sc1\n" \
"buffer_load_dwordx4 a[12:15], v201, s[8:11], s43 offen offset:1024 sc1\n" \
"v_readlane_b32 s43, v202, 8\n" \
"s_nop 4\n" \
"buffer_load_dwordx4 a[64:67], v201, s[8:11], s43 offen sc1\n" \
"buffer_load_dwordx4 a[68:71], v201, s[8:11], s43 offen offset:1024 sc1\n" \
"v_readlane_b32 s43, v202, 15\n" \
"s_nop 4\n" \
"buffer_load_dwordx4 a[120:123], v201, s[8:11], s43 offen sc1\n" \
"buffer_load_dwordx4 a[124:127], v201, s[8:11], s43 offen offset:1024 sc1\n" \
"v_readlane_b32 s43, v202, 22\n" \
"s_nop 4\n" \
"buffer_load_dwordx4 a[176:179], v201, s[8:11], s43 offen sc1\n" \
"buffer_load_dwordx4 a[180:183], v201, s[8:11], s43 offen offset:1024 sc1\n" \
"s_add_u32 s43, s30, 4\n" \
"s_mov_b32 s50, 0\n" \
"s_branch .LX0_w57c\n" \
".LX0_w57l:\n" \
"buffer_load_dword v240, v204, s[4:7], 0 offen sc1\n" \
"s_waitcnt vmcnt(0)\n" \
".LX0_w57c:\n" \
"v_subrev_u32 v241, s43, v240\n" \
"v_cmp_gt_u32 vcc, 0x100, v241\n" \
"s_cmp_eq_u64 vcc, -1\n" \
"s_cbranch_scc1 .LX0_w57d\n" \
"s_sleep 2\n" \
"s_add_u32 s50, s50, 1\n" \
"s_cmp_lt_u32 s50, 0x20000\n" \
"s_cbranch_scc1 .LX0_w57l\n" \
"s_branch .LX0_end\n" \
".LX0_w57d:\n" \
"v_readlane_b32 s43, v202, 5\n" \
"s_nop 4\n" \
"buffer_load_dwordx4 a[40:43], v201, s[8:11], s43 offen sc1\n" \
"buffer_load_dwordx4 a[44:47], v201, s[8:11], s43 offen offset:1024 sc1\n" \
"v_readlane_b32 s43, v202, 12\n" \
"s_nop 4\n" \
"buffer_load_dwordx4 a[96:99], v201, s[8:11], s43 offen sc1\n" \
"buffer_load_dwordx4 a[100:103], v201, s[8:11], s43 offen offset:1024 sc1\n" \
"v_readlane_b32 s43, v202, 19\n" \
"s_nop 4\n" \
"buffer_load_dwordx4 a[152:155], v201, s[8:11], s43 offen sc1\n" \
"buffer_load_dwordx4 a[156:159], v201, s[8:11], s43 offen offset:1024 sc1\n" \
"v_readlane_b32 s43, v202, 26\n" \
"s_nop 4\n" \
"buffer_load_dwordx4 a[208:211], v201, s[8:11], s43 offen sc1\n" \
"buffer_load_dwordx4 a[212:215], v201, s[8:11], s43 offen offset:1024 sc1\n" \
"s_add_u32 s43, s30, 5\n" \
"s_mov_b32 s50, 0\n" \
"s_branch .LX0_w58c\n" \
".LX0_w58l:\n" \
"buffer_load_dword v240, v204, s[4:7], 0 offen sc1\n" \
"s_waitcnt vmcnt(0)\n" \
".LX0_w58c:\n" \
"v_subrev_u32 v241, s43, v240\n" \
"v_cmp_gt_u32 vcc, 0x100, v241\n" \
"s_cmp_eq_u64 vcc, -1\n" \
"s_cbranch_scc1 .LX0_w58d\n" \
"s_sleep 2\n" \
"s_add_u32 s50, s50, 1\n" \
"s_cmp_lt_u32 s50, 0x20000\n" \
"s_cbranch_scc1 .LX0_w58l\n" \
"s_branch .LX0_end\n" \
".LX0_w58d:\n" \
"v_readlane_b32 s43, v202, 2\n" \
"s_nop 4\n" \
"buffer_load_dwordx4 a[16:19], v201, s[8:11], s43 offen sc1\n" \
"buffer_load_dwordx4 a[20:23], v201, s[8:11], s43 offen offset:1024 sc1\n" \
"v_readlane_b32 s43, v202, 9\n" \
"s_nop 4\n" \
"buffer_load_dwordx4 a[72:75], v201, s[8:11], s43 offen sc1\n" \
"buffer_load_dwordx4 a[76:79], v201, s[8:11], s43 offen offset:1024 sc1\n" \
"v_readlane_b32 s43, v202, 16\n" \
"s_nop 4\n" \
"buffer_load_dwordx4 a[128:131], v201, s[8:11], s43 offen sc1\n" \
"buffer_load_dwordx4 a[132:135], v201, s[8:11], s43 offen offset:1024 sc1\n" \
"v_readlane_b32 s43, v202, 23\n" \
"s_nop 4\n" \
"buffer_load_dwordx4 a[184:187], v201, s[8:11], s43 offen sc1\n" \
"buffer_load_dwordx4 a[188:191], v201, s[8:11], s43 offen offset:1024 sc1\n" \
"s_add_u32 s43, s30, 6\n" \
"s_mov_b32 s50, 0\n" \
"s_branch .LX0_w59c\n" \
".LX0_w59l:\n" \
"buffer_load_dword v240, v204, s[4:7], 0 offen sc1\n" \
"s_waitcnt vmcnt(0)\n" \
".LX0_w59c:\n" \
"v_subrev_u32 v241, s43, v240\n" \
"v_cmp_gt_u32 vcc, 0x100, v241\n" \
"s_cmp_eq_u64 vcc, -1\n" \
"s_cbranch_scc1 .LX0_w59d\n" \
"s_sleep 2\n" \
"s_add_u32 s50, s50, 1\n" \
"s_cmp_lt_u32 s50, 0x20000\n" \
"s_cbranch_scc1 .LX0_w59l\n" \
"s_branch .LX0_end\n" \
".LX0_w59d:\n" \
"v_readlane_b32 s43, v202, 6\n" \
"s_nop 4\n" \
"buffer_load_dwordx4 a[48:51], v201, s[8:11], s43 offen sc1\n" \
"buffer_load_dwordx4 a[52:55], v201, s[8:11], s43 offen offset:1024 sc1\n" \
"v_readlane_b32 s43, v202, 13\n" \
"s_nop 4\n" \
"buffer_load_dwordx4 a[104:107], v201, s[8:11], s43 offen sc1\n" \
"buffer_load_dwordx4 a[108:111], v201, s[8:11], s43 offen offset:1024 sc1\n" \
"v_readlane_b32 s43, v202, 20\n" \
"s_nop 4\n" \
"buffer_load_dwordx4 a[160:163], v201, s[8:11], s43 offen sc1\n" \
"buffer_load_dwordx4 a[164:167], v201, s[8:11], s43 offen offset:1024 sc1\n" \
"v_readlane_b32 s43, v202, 27\n" \
"s_nop 4\n" \
"buffer_load_dwordx4 a[216:219], v201, s[8:11], s43 offen sc1\n" \
"buffer_load_dwordx4 a[220:223], v201, s[8:11], s43 offen offset:1024 sc1\n" \
"s_add_u32 s43, s30, 7\n" \
"s_mov_b32 s50, 0\n" \
"s_branch .LX0_w60c\n" \
".LX0_w60l:\n" \
"buffer_load_dword v240, v204, s[4:7], 0 offen sc1\n" \
"s_waitcnt vmcnt(0)\n" \
".LX0_w60c:\n" \
"v_subrev_u32 v241, s43, v240\n" \
"v_cmp_gt_u32 vcc, 0x100, v241\n" \
"s_cmp_eq_u64 vcc, -1\n" \
"s_cbranch_scc1 .LX0_w60d\n" \
"s_sleep 2\n" \
"s_add_u32 s50, s50, 1\n" \
"s_cmp_lt_u32 s50, 0x20000\n" \
"s_cbranch_scc1 .LX0_w60l\n" \
"s_branch .LX0_end\n" \
".LX0_w60d:\n" \
"v_readlane_b32 s43, v202, 3\n" \
"s_nop 4\n" \
"buffer_load_dwordx4 a[24:27], v201, s[8:11], s43 offen sc1\n" \
"buffer_load_dwordx4 a[28:31], v201, s[8:11], s43 offen offset:1024 sc1\n" \
"v_readlane_b32 s43, v202, 10\n" \
"s_nop 4\n" \
"buffer_load_dwordx4 a[80:83], v201, s[8:11], s43 offen sc1\n" \
"buffer_load_dwordx4 a[84:87], v201, s[8:11], s43 offen offset:1024 sc1\n" \
"v_readlane_b32 s43, v202, 17\n" \
"s_nop 4\n" \
"buffer_load_dwordx4 a[136:139], v201, s[8:11], s43 offen sc1\n" \
"buffer_load_dwordx4 a[140:143], v201, s[8:11], s43 offen offset:1024 sc1\n" \
"v_readlane_b32 s43, v202, 24\n" \
"s_nop 4\n" \
"buffer_load_dwordx4 a[192:195], v201, s[8:11], s43 offen sc1\n" \
"buffer_load_dwordx4 a[196:199], v201, s[8:11], s43 offen offset:1024 sc1\n" \
"s_add_u32 s43, s30, 1\n" \
"s_mov_b32 s50, 0\n" \
".LX0_w61l:\n" \
"buffer_load_dword v240, v205, s[4:7], 0 offen sc1\n" \
"s_waitcnt vmcnt(0)\n" \
".LX0_w61c:\n" \
"v_subrev_u32 v241, s43, v240\n" \
"v_cmp_gt_u32 vcc, 0x100, v241\n" \
"s_cmp_eq_u64 vcc, -1\n" \
"s_cbranch_scc1 .LX0_w61d\n" \
"s_sleep 2\n" \
"s_add_u32 s50, s50, 1\n" \
"s_cmp_lt_u32 s50, 0x20000\n" \
"s_cbranch_scc1 .LX0_w61l\n" \
"s_branch .LX0_end\n" \
".LX0_w61d:\n" \
"v_readfirstlane_b32 s31, v241\n" \
"s_lshr_b32 s32, s31, 2\n" \
"s_xor_b32 s29, s31, s32\n" \
"s_and_b32 s29, s29, 3\n" \
"s_lshl_b32 s29, s29, 13\n" \
"s_bitset1_b32 s29, 21\n" \
"s_and_b32 s31, s31, 3\n" \
"s_lshl_b32 s31, s31, 13\n" \
"s_bitcmp1_b32 s39, 0\n" \
"s_cselect_b32 s43, s29, 0\n" \
"s_xor_b32 s31, s31, s43\n" \
"s_xor_b32 s32, s31, 0x4000\n" \
"s_mov_b32 s39, 0\n" \
"s_add_u32 s43, s30, 8\n" \
"s_mov_b32 s50, 0\n" \
".LX0_w62l:\n" \
"buffer_load_dword v240, v215, s[4:7], 0 offen sc1\n" \
"s_waitcnt vmcnt(0)\n" \
".LX0_w62c:\n" \
"v_subrev_u32 v241, s43, v240\n" \
"v_cmp_gt_u32 vcc, 0x100, v241\n" \
"s_cmp_eq_u64 vcc, -1\n" \
"s_cbranch_scc1 .LX0_w62d\n" \
"s_sleep 2\n" \
"s_add_u32 s50, s50, 1\n" \
"s_cmp_lt_u32 s50, 0x20000\n" \
"s_cbranch_scc1 .LX0_w62l\n" \
"s_branch .LX0_end\n" \
".LX0_w62d:\n" \
"s_add_u32 s50, s40, 0\n" \
"s_mul_i32 s50, s50, 28672\n" \
"s_add_u32 s50, s50, s34\n" \
"buffer_load_dwordx4 v[28:31], v201, s[12:15], s50 offen sc1\n" \
"buffer_load_dwordx4 v[32:35], v201, s[12:15], s50 offen offset:1024 sc1\n" \
"buffer_load_dwordx4 v[36:39], v201, s[12:15], s50 offen offset:2048 sc1\n" \
"buffer_load_dwordx4 v[40:43], v201, s[12:15], s50 offen offset:3072 sc1\n" \
"ds_read_b96 v[248:250], v252\n" \
"s_waitcnt lgkmcnt(0)\n" \
"v_readfirstlane_b32 s44, v248\n" \
"v_readfirstlane_b32 s45, v249\n" \
"v_readfirstlane_b32 s46, v250\n" \
"s_mul_i32 s42, s25, s46\n" \
"s_lshr_b32 s42, s42, 12\n" \
"s_mov_b32 s26, s44\n" \
"s_mov_b32 s27, s45\n" \
".p2align 3\n" \
"s_memtime s[54:55]\n" \
"s_memrealtime s[56:57]\n" \
"s_memtime s[58:59]\n" \
"s_memrealtime s[60:61]\n" \
"s_nop 0\n" \
"s_nop 0\n" \
"s_memtime s[62:63]\n" \
"s_memrealtime s[64:65]\n" \
"s_memtime s[66:67]\n" \
"s_memrealtime s[68:69]\n" \
"s_memtime s[70:71]\n" \
"s_memrealtime s[72:73]\n" \
"s_memtime s[74:75]\n" \
"s_memrealtime s[76:77]\n" \
"s_nop 0\n" \
"s_nop 0\n" \
"s_memtime s[78:79]\n" \
"s_memrealtime s[80:81]\n" \
"s_memtime s[82:83]\n" \
"s_memrealtime s[84:85]\n" \
"s_memtime s[86:87]\n" \
"s_memrealtime s[88:89]\n" \
"s_memtime s[90:91]\n" \
"s_memrealtime s[92:93]\n" \
"s_nop 0\n" \
"s_nop 0\n" \
"s_memtime s[94:95]\n" \
"s_memrealtime s[96:97]\n" \
"s_memtime s[98:99]\n" \
"s_mov_b32 s52, 0\n" \
"s_mov_b32 s53, 4096\n" \
"s_waitcnt lgkmcnt(0)\n" \
"s_cmp_lg_u32 s96, s56\n" \
"s_cselect_b32 s52, s90, s52\n" \
"s_cselect_b32 s53, s98, s53\n" \
"s_cmp_lg_u32 s92, s56\n" \
"s_cselect_b32 s52, s86, s52\n" \
"s_cselect_b32 s53, s94, s53\n" \
"s_cmp_lg_u32 s88, s56\n" \
"s_cselect_b32 s52, s82, s52\n" \
"s_cselect_b32 s53, s90, s53\n" \
"s_cmp_lg_u32 s84, s56\n" \
"s_cselect_b32 s52, s78, s52\n" \
"s_cselect_b32 s53, s86, s53\n" \
"s_cmp_lg_u32 s80, s56\n" \
"s_cselect_b32 s52, s74, s52\n" \
"s_cselect_b32 s53, s82, s53\n" \
"s_cmp_lg_u32 s76, s56\n" \
"s_cselect_b32 s52, s70, s52\n" \
"s_cselect_b32 s53, s78, s53\n" \
"s_cmp_lg_u32 s72, s56\n" \
"s_cselect_b32 s52, s66, s52\n" \
"s_cselect_b32 s53, s74, s53\n" \
"s_cmp_lg_u32 s68, s56\n" \
"s_cselect_b32 s52, s62, s52\n" \
"s_cselect_b32 s53, s70, s53\n" \
"s_cmp_lg_u32 s64, s56\n" \
"s_cselect_b32 s52, s58, s52\n" \
"s_cselect_b32 s53, s66, s53\n" \
"s_cmp_lg_u32 s60, s56\n" \
"s_cselect_b32 s52, s54, s52\n" \
"s_cselect_b32 s53, s62, s53\n" \
"s_sub_u32 s53, s53, s52\n" \
"s_lshr_b32 s55, s53, 1\n" \
"s_add_u32 s52, s52, s55\n" \
"s_add_u32 s55, s56, 4\n" \
"s_lshl_b32 s55, s55, 4\n" \
"s_cmp_le_u32 s53, 40\n" \
"s_cselect_b32 s44, s55, s44\n" \
"s_cselect_b32 s45, s52, s45\n" \
"v_mov_b32 v248, s44\n" \
"v_mov_b32 v249, s45\n" \
"v_mov_b32 v250, s46\n" \
"s_mov_b64 exec, 1\n" \
"ds_write_b96 v252, v[248:250]\n" \
"s_mov_b64 exec, -1\n" \
"s_sub_u32 s26, s44, s26\n" \
"s_sub_u32 s27, s45, s27\n" \
"v_cvt_f32_u32 v248, s26\n" \
"v_cvt_f32_u32 v249, s27\n" \
"v_rcp_f32 v248, v248\n" \
"v_mul_f32 v249, 0x457ae148, v249\n" \
"v_mul_f32 v248, v248, v249\n" \
"v_cvt_u32_f32 v248, v248\n" \
"s_nop 0\n" \
"v_readfirstlane_b32 s27, v248\n" \
"s_max_u32 s27, s27, 0x800\n" \
"s_min_u32 s27, s27, 0x2000\n" \
"s_cmp_gt_u32 s26, 1600\n" \
"s_cselect_b32 s46, s27, s46\n" \
".p2align 3\n" \
"s_memtime s[54:55]\n" \
"s_memrealtime s[56:57]\n" \
"s_memtime s[58:59]\n" \
"s_memrealtime s[60:61]\n" \
"s_nop 0\n" \
"s_nop 0\n" \
"s_memtime s[62:63]\n" \
"s_memrealtime s[64:65]\n" \
"s_memtime s[66:67]\n" \
"s_memrealtime s[68:69]\n" \
"s_memtime s[70:71]\n" \
"s_memrealtime s[72:73]\n" \
"s_memtime s[74:75]\n" \
"s_memrealtime s[76:77]\n" \
"s_nop 0\n" \
"s_nop 0\n" \
"s_memtime s[78:79]\n" \
"s_memrealtime s[80:81]\n" \
"s_memtime s[82:83]\n" \
"s_memrealtime s[84:85]\n" \
"s_memtime s[86:87]\n" \
"s_memrealtime s[88:89]\n" \
"s_memtime s[90:91]\n" \
"s_memrealtime s[92:93]\n" \
"s_nop 0\n" \
"s_nop 0\n" \
"s_memtime s[94:95]\n" \
"s_memrealtime s[96:97]\n" \
"s_memtime s[98:99]\n" \
"s_mov_b32 s52, 0\n" \
"s_mov_b32 s53, 4096\n" \
"s_waitcnt lgkmcnt(0)\n" \
"s_cmp_lg_u32 s96, s56\n" \
"s_cselect_b32 s52, s90, s52\n" \
"s_cselect_b32 s53, s98, s53\n" \
"s_cmp_lg_u32 s92, s56\n" \
"s_cselect_b32 s52, s86, s52\n" \
"s_cselect_b32 s53, s94, s53\n" \
"s_cmp_lg_u32 s88, s56\n" \
"s_cselect_b32 s52, s82, s52\n" \
"s_cselect_b32 s53, s90, s53\n" \
"s_cmp_lg_u32 s84, s56\n" \
"s_cselect_b32 s52, s78, s52\n" \
"s_cselect_b32 s53, s86, s53\n" \
"s_cmp_lg_u32 s80, s56\n" \
"s_cselect_b32 s52, s74, s52\n" \
"s_cselect_b32 s53, s82, s53\n" \
"s_cmp_lg_u32 s76, s56\n" \
"s_cselect_b32 s52, s70, s52\n" \
"s_cselect_b32 s53, s78, s53\n" \
"s_cmp_lg_u32 s72, s56\n" \
"s_cselect_b32 s52, s66, s52\n" \
"s_cselect_b32 s53, s74, s53\n" \
"s_cmp_lg_u32 s68, s56\n" \
"s_cselect_b32 s52, s62, s52\n" \
"s_cselect_b32 s53, s70, s53\n" \
"s_cmp_lg_u32 s64, s56\n" \
"s_cselect_b32 s52, s58, s52\n" \
"s_cselect_b32 s53, s66, s53\n" \
"s_cmp_lg_u32 s60, s56\n" \
"s_cselect_b32 s52, s54, s52\n" \
"s_cselect_b32 s53, s62, s53\n" \
"s_sub_u32 s53, s53, s52\n" \
"s_lshr_b32 s55, s53, 1\n" \
"s_add_u32 s52, s52, s55\n" \
"s_add_u32 s55, s56, 4\n" \
"s_lshl_b32 s55, s55, 4\n" \
"s_cmp_le_u32 s53, 40\n" \
"s_cselect_b32 s44, s55, s44\n" \
"s_cselect_b32 s45, s52, s45\n" \
"v_mov_b32 v248, s44\n" \
"v_mov_b32 v249, s45\n" \
"v_mov_b32 v250, s46\n" \
"s_mov_b64 exec, 1\n" \
"ds_write_b96 v252, v[248:250]\n" \
"s_mov_b64 exec, -1\n" \
"s_waitcnt lgkmcnt(0)\n" \
"s_waitcnt vmcnt(0)\n" \
"s_barrier\n" \
"s_add_u32 s43, s30, 9\n" \
"s_mov_b32 s50, 0\n" \
".LX0_w63l:\n" \
"buffer_load_dword v240, v215, s[4:7], 0 offen sc1\n" \
"s_waitcnt vmcnt(0)\n" \
".LX0_w63c:\n" \
"v_subrev_u32 v241, s43, v240\n" \
"v_cmp_gt_u32 vcc, 0x100, v241\n" \
"s_cmp_eq_u64 vcc, -1\n" \
"s_cbranch_scc1 .LX0_w63d\n" \
"s_sleep 2\n" \
"s_add_u32 s50, s50, 1\n" \
"s_cmp_lt_u32 s50, 0x20000\n" \
"s_cbranch_scc1 .LX0_w63l\n" \
"s_branch .LX0_end\n" \
".LX0_w63d:\n" \
".LX0_pre:\n" \
".LX0_loop:\n" \
"s_add_u32 s43, s40, 2\n" \
"s_and_b32 s43, s43, 3\n" \
"s_bitcmp1_b32 s43, 0\n" \
"s_cselect_b32 s49, s32, s31\n" \
"s_lshl_b32 s43, s43, 19\n" \
"s_add_u32 s49, s49, s43\n" \
"ds_read_b128 v[0:3], v216 offset:0\n" \
"ds_read_b128 v[4:7], v216 offset:1024\n" \
"ds_read_b128 v[8:11], v216 offset:2048\n" \
"ds_read_b128 v[12:15], v216 offset:3072\n" \
"ds_read_b128 v[16:19], v216 offset:4096\n" \
"ds_read_b128 v[20:23], v216 offset:5120\n" \
"ds_read_b128 v[24:27], v216 offset:6144\n" \
"v_mov_b32 v60, 0\n" \
"v_mov_b32 v61, 0\n" \
"v_mov_b32 v62, 0\n" \
"v_mov_b32 v63, 0\n" \
"v_mov_b32 v64, 0\n" \
"v_mov_b32 v65, 0\n" \
"v_mov_b32 v66, 0\n" \
"v_mov_b32 v67, 0\n" \
"v_mov_b32 v68, 0\n" \
"v_mov_b32 v69, 0\n" \
"v_mov_b32 v70, 0\n" \
"v_mov_b32 v71, 0\n" \
"v_mov_b32 v72, 0\n" \
"v_mov_b32 v73, 0\n" \
"v_mov_b32 v74, 0\n" \
"v_mov_b32 v75, 0\n" \
"s_nop 1\n" \
"s_waitcnt lgkmcnt(6)\n" \
"v_smfmac_f32_16x16x64_bf16 v[60:63], v[0:3], a[0:7], v200\n" \
"v_smfmac_f32_16x16x64_bf16 v[64:67], v[0:3], a[56:63], v200\n" \
"v_smfmac_f32_16x16x64_bf16 v[68:71], v[0:3], a[112:119], v200\n" \
"v_smfmac_f32_16x16x64_bf16 v[72:75], v[0:3], a[168:175], v200\n" \
"s_waitcnt lgkmcnt(5)\n" \
"v_smfmac_f32_16x16x64_bf16 v[60:63], v[4:7], a[8:15], v200\n" \
"v_smfmac_f32_16x16x64_bf16 v[64:67], v[4:7], a[64:71], v200\n" \
"v_smfmac_f32_16x16x64_bf16 v[68:71], v[4:7], a[120:127], v200\n" \
"v_smfmac_f32_16x16x64_bf16 v[72:75], v[4:7], a[176:183], v200\n" \
"s_waitcnt lgkmcnt(4)\n" \
"v_smfmac_f32_16x16x64_bf16 v[60:63], v[8:11], a[16:23], v200\n" \
"v_smfmac_f32_16x16x64_bf16 v[64:67], v[8:11], a[72:79], v200\n" \
"v_smfmac_f32_16x16x64_bf16 v[68:71], v[8:11], a[128:135], v200\n" \
"v_smfmac_f32_16x16x64_bf16 v[72:75], v[8:11], a[184:191], v200\n" \
"s_waitcnt lgkmcnt(3)\n" \
"v_smfmac_f32_16x16x64_bf16 v[60:63], v[12:15], a[24:31], v200\n" \
"v_smfmac_f32_16x16x64_bf16 v[64:67], v[12:15], a[80:87], v200\n" \
"v_smfmac_f32_16x16x64_bf16 v[68:71], v[12:15], a[136:143], v200\n" \
"v_smfmac_f32_16x16x64_bf16 v[72:75], v[12:15], a[192:199], v200\n" \
"s_waitcnt lgkmcnt(2)\n" \
"v_smfmac_f32_16x16x64_bf16 v[60:63], v[16:19], a[32:39], v200\n" \
"v_smfmac_f32_16x16x64_bf16 v[64:67], v[16:19], a[88:95], v200\n" \
"v_smfmac_f32_16x16x64_bf16 v[68:71], v[16:19], a[144:151], v200\n" \
"v_smfmac_f32_16x16x64_bf16 v[72:75], v[16:19], a[200:207], v200\n" \
"s_waitcnt lgkmcnt(1)\n" \
"v_smfmac_f32_16x16x64_bf16 v[60:63], v[20:23], a[40:47], v200\n" \
"v_smfmac_f32_16x16x64_bf16 v[64:67], v[20:23], a[96:103], v200\n" \
"v_smfmac_f32_16x16x64_bf16 v[68:71], v[20:23], a[152:159], v200\n" \
"v_smfmac_f32_16x16x64_bf16 v[72:75], v[20:23], a[208:215], v200\n" \
"s_waitcnt lgkmcnt(0)\n" \
"v_smfmac_f32_16x16x64_bf16 v[60:63], v[24:27], a[48:55], v200\n" \
"v_smfmac_f32_16x16x64_bf16 v[64:67], v[24:27], a[104:111], v200\n" \
"v_smfmac_f32_16x16x64_bf16 v[68:71], v[24:27], a[160:167], v200\n" \
"v_smfmac_f32_16x16x64_bf16 v[72:75], v[24:27], a[216:223], v200\n" \
"s_barrier\n" \
"s_add_u32 s50, s40, 1\n" \
"s_mul_i32 s50, s50, 28672\n" \
"s_add_u32 s50, s50, s34\n" \
"buffer_load_dwordx4 v[44:47], v201, s[12:15], s50 offen sc1\n" \
"buffer_load_dwordx4 v[48:51], v201, s[12:15], s50 offen offset:1024 sc1\n" \
"buffer_load_dwordx4 v[52:55], v201, s[12:15], s50 offen offset:2048 sc1\n" \
"buffer_load_dwordx4 v[56:59], v201, s[12:15], s50 offen offset:3072 sc1\n" \
"s_nop 7\n" \
"v_permlane32_swap_b32 v60, v64\n" \
"v_permlane32_swap_b32 v61, v65\n" \
"v_permlane32_swap_b32 v62, v66\n" \
"v_permlane32_swap_b32 v63, v67\n" \
"v_add_f32 v60, v60, v64\n" \
"v_add_f32 v61, v61, v65\n" \
"v_add_f32 v62, v62, v66\n" \
"v_add_f32 v63, v63, v67\n" \
"v_cvt_pk_bf16_f32 v184, v60, v61\n" \
"v_cvt_pk_bf16_f32 v185, v62, v63\n" \
"ds_write_b64 v217, v[184:185] offset:0\n" \
"v_permlane32_swap_b32 v68, v72\n" \
"v_permlane32_swap_b32 v69, v73\n" \
"v_permlane32_swap_b32 v70, v74\n" \
"v_permlane32_swap_b32 v71, v75\n" \
"v_add_f32 v68, v68, v72\n" \
"v_add_f32 v69, v69, v73\n" \
"v_add_f32 v70, v70, v74\n" \
"v_add_f32 v71, v71, v75\n" \
"v_cvt_pk_bf16_f32 v186, v68, v69\n" \
"v_cvt_pk_bf16_f32 v187, v70, v71\n" \
"ds_write_b64 v217, v[186:187] offset:512\n" \
"ds_read_b64_tr_b16 v[0:1], v218 offset:0\n" \
"ds_read_b64_tr_b16 v[2:3], v218 offset:64\n" \
"s_add_u32 s43, s40, 0\n" \
"s_mul_i32 s43, s43, 7168\n" \
"s_waitcnt lgkmcnt(0)\n" \
"buffer_store_dwordx4 v[0:3], v219, s[16:19], s43 offen sc1\n" \
"s_waitcnt vmcnt(5)\n" \
"s_barrier\n" \
"ds_read_b64_tr_b16 v[92:93], v213\n" \
"ds_read_b64_tr_b16 v[94:95], v213 offset:64\n" \
"ds_read_b32 v84, v252 offset:16\n" \
"v_mov_b32 v245, v245\n" \
"s_nop 2\n" \
"v_mov_b32 v245, v245\n" \
"s_nop 2\n" \
"v_mov_b32 v245, v245\n" \
"s_waitcnt lgkmcnt(1)\n" \
"v_mfma_f32_16x16x32_bf16 v[68:71], v[92:95], v[28:31], 0\n" \
"s_waitcnt lgkmcnt(0)\n" \
"v_xor_b32 v85, s29, v84\n" \
"v_mfma_f32_16x16x32_bf16 v[76:79], v[92:95], v[36:39], 0\n" \
"v_add_u32 v84, v84, v210\n" \
"v_add_u32 v85, v85, v210\n" \
"v_mfma_f32_16x16x32_bf16 v[72:75], v[92:95], v[32:35], 0\n" \
"s_nop 1\n" \
"v_mfma_f32_16x16x32_bf16 v[80:83], v[92:95], v[40:43], 0\n" \
"v_cvt_pk_bf16_f32 v96, v68, v69\n" \
"v_cvt_pk_bf16_f32 v97, v70, v71\n" \
"v_cvt_pk_bf16_f32 v98, v76, v77\n" \
"v_cvt_pk_bf16_f32 v99, v78, v79\n" \
"s_mov_b64 exec, s[2:3]\n" \
"v_cvt_pk_bf16_f32 v96, v72, v73\n" \
"v_cvt_pk_bf16_f32 v97, v74, v75\n" \
"s_nop 0\n" \
"v_cvt_pk_bf16_f32 v98, v80, v81\n" \
"v_cvt_pk_bf16_f32 v99, v82, v83\n" \
"s_mov_b64 exec, -1\n" \
"s_nop 1\n" \
"v_permlane16_swap_b32 v96, v98\n" \
"v_permlane16_swap_b32 v97, v99\n" \
"buffer_store_dwordx4 v[96:99], v84, s[4:7], 0 offen sc1\n" \
"s_barrier\n" \
"buffer_store_dwordx4 v[96:99], v85, s[4:7], 0 offen sc1\n" \
"ds_read_b96 v[248:250], v252\n" \
"s_waitcnt lgkmcnt(0)\n" \
"v_readfirstlane_b32 s44, v248\n" \
"v_readfirstlane_b32 s45, v249\n" \
"v_readfirstlane_b32 s46, v250\n" \
"s_mul_i32 s42, s25, s46\n" \
"s_lshr_b32 s42, s42, 12\n" \
".p2align 3\n" \
"s_memtime s[54:55]\n" \
"s_memrealtime s[56:57]\n" \
"s_memtime s[58:59]\n" \
"s_memrealtime s[60:61]\n" \
"s_nop 0\n" \
"s_nop 0\n" \
"s_memtime s[62:63]\n" \
"s_memrealtime s[64:65]\n" \
"s_memtime s[66:67]\n" \
"s_memrealtime s[68:69]\n" \
"s_memtime s[70:71]\n" \
"s_memrealtime s[72:73]\n" \
"s_memtime s[74:75]\n" \
"s_memrealtime s[76:77]\n" \
"s_nop 0\n" \
"s_nop 0\n" \
"s_memtime s[78:79]\n" \
"s_memrealtime s[80:81]\n" \
"s_memtime s[82:83]\n" \
"s_memrealtime s[84:85]\n" \
"s_memtime s[86:87]\n" \
"s_memrealtime s[88:89]\n" \
"s_memtime s[90:91]\n" \
"s_memrealtime s[92:93]\n" \
"s_nop 0\n" \
"s_nop 0\n" \
"s_memtime s[94:95]\n" \
"s_memrealtime s[96:97]\n" \
"s_memtime s[98:99]\n" \
"s_mov_b32 s52, 0\n" \
"s_mov_b32 s53, 4096\n" \
"s_waitcnt lgkmcnt(0)\n" \
"s_cmp_lg_u32 s96, s56\n" \
"s_cselect_b32 s52, s90, s52\n" \
"s_cselect_b32 s53, s98, s53\n" \
"s_cmp_lg_u32 s92, s56\n" \
"s_cselect_b32 s52, s86, s52\n" \
"s_cselect_b32 s53, s94, s53\n" \
"s_cmp_lg_u32 s88, s56\n" \
"s_cselect_b32 s52, s82, s52\n" \
"s_cselect_b32 s53, s90, s53\n" \
"s_cmp_lg_u32 s84, s56\n" \
"s_cselect_b32 s52, s78, s52\n" \
"s_cselect_b32 s53, s86, s53\n" \
"s_cmp_lg_u32 s80, s56\n" \
"s_cselect_b32 s52, s74, s52\n" \
"s_cselect_b32 s53, s82, s53\n" \
"s_cmp_lg_u32 s76, s56\n" \
"s_cselect_b32 s52, s70, s52\n" \
"s_cselect_b32 s53, s78, s53\n" \
"s_cmp_lg_u32 s72, s56\n" \
"s_cselect_b32 s52, s66, s52\n" \
"s_cselect_b32 s53, s74, s53\n" \
"s_cmp_lg_u32 s68, s56\n" \
"s_cselect_b32 s52, s62, s52\n" \
"s_cselect_b32 s53, s70, s53\n" \
"s_cmp_lg_u32 s64, s56\n" \
"s_cselect_b32 s52, s58, s52\n" \
"s_cselect_b32 s53, s66, s53\n" \
"s_cmp_lg_u32 s60, s56\n" \
"s_cselect_b32 s52, s54, s52\n" \
"s_cselect_b32 s53, s62, s53\n" \
"s_sub_u32 s53, s53, s52\n" \
"s_lshr_b32 s55, s53, 1\n" \
"s_add_u32 s52, s52, s55\n" \
"s_add_u32 s55, s56, 4\n" \
"s_lshl_b32 s55, s55, 4\n" \
"s_sub_u32 s59, s55, s44\n" \
"s_cmp_lt_u32 s59, 2600\n" \
"s_cselect_b32 s63, 1, 2\n" \
"s_mul_i32 s59, s59, s46\n" \
"s_ashr_i32 s59, s59, 12\n" \
"s_add_u32 s59, s59, s45\n" \
"s_sub_u32 s59, s52, s59\n" \
"s_max_i32 s59, s59, -96\n" \
"s_min_i32 s59, s59, 96\n" \
"s_ashr_i32 s63, s59, s63\n" \
"s_add_u32 s63, s63, s46\n" \
"s_max_i32 s63, s63, 0x800\n" \
"s_min_i32 s63, s63, 0x2000\n" \
"s_cmp_le_u32 s53, 40\n" \
"s_cselect_b32 s44, s55, s44\n" \
"s_cselect_b32 s45, s52, s45\n" \
"s_cselect_b32 s46, s63, s46\n" \
"v_mov_b32 v248, s44\n" \
"v_mov_b32 v249, s45\n" \
"v_mov_b32 v250, s46\n" \
"s_mov_b64 exec, 1\n" \
"ds_write_b96 v252, v[248:250]\n" \
"s_mov_b64 exec, -1\n" \
"buffer_store_dwordx4 v[100:103], v214, s[4:7], s49 offen sc1\n" \
"s_xor_b32 s43, s49, s29\n" \
"buffer_store_dwordx4 v[100:103], v214, s[4:7], s43 offen sc1\n" \
"s_waitcnt vmcnt(4)\n" \
"v_mov_b32 v28, v44\n" \
"v_mov_b32 v29, v45\n" \
"v_mov_b32 v30, v46\n" \
"v_mov_b32 v31, v47\n" \
"v_mov_b32 v32, v48\n" \
"v_mov_b32 v33, v49\n" \
"v_mov_b32 v34, v50\n" \
"v_mov_b32 v35, v51\n" \
"v_mov_b32 v36, v52\n" \
"v_mov_b32 v37, v53\n" \
"v_mov_b32 v38, v54\n" \
"v_mov_b32 v39, v55\n" \
"v_mov_b32 v40, v56\n" \
"v_mov_b32 v41, v57\n" \
"v_mov_b32 v42, v58\n" \
"v_mov_b32 v43, v59\n" \
"s_add_u32 s40, s40, 1\n" \
"s_cmp_eq_u32 s40, 0\n" \
"s_cbranch_scc1 .LX0_wet\n" \
"s_cmp_lt_u32 s40, 63\n" \
"s_cbranch_scc1 .LX0_loop\n" \
"ds_read_b128 v[0:3], v216 offset:0\n" \
"ds_read_b128 v[4:7], v216 offset:1024\n" \
"ds_read_b128 v[8:11], v216 offset:2048\n" \
"ds_read_b128 v[12:15], v216 offset:3072\n" \
"ds_read_b128 v[16:19], v216 offset:4096\n" \
"ds_read_b128 v[20:23], v216 offset:5120\n" \
"ds_read_b128 v[24:27], v216 offset:6144\n" \
"v_mov_b32 v60, 0\n" \
"v_mov_b32 v61, 0\n" \
"v_mov_b32 v62, 0\n" \
"v_mov_b32 v63, 0\n" \
"v_mov_b32 v64, 0\n" \
"v_mov_b32 v65, 0\n" \
"v_mov_b32 v66, 0\n" \
"v_mov_b32 v67, 0\n" \
"v_mov_b32 v68, 0\n" \
"v_mov_b32 v69, 0\n" \
"v_mov_b32 v70, 0\n" \
"v_mov_b32 v71, 0\n" \
"v_mov_b32 v72, 0\n" \
"v_mov_b32 v73, 0\n" \
"v_mov_b32 v74, 0\n" \
"v_mov_b32 v75, 0\n" \
"s_nop 1\n" \
"s_waitcnt lgkmcnt(6)\n" \
"v_smfmac_f32_16x16x64_bf16 v[60:63], v[0:3], a[0:7], v200\n" \
"v_smfmac_f32_16x16x64_bf16 v[64:67], v[0:3], a[56:63], v200\n" \
"v_smfmac_f32_16x16x64_bf16 v[68:71], v[0:3], a[112:119], v200\n" \
"v_smfmac_f32_16x16x64_bf16 v[72:75], v[0:3], a[168:175], v200\n" \
"s_waitcnt lgkmcnt(5)\n" \
"v_smfmac_f32_16x16x64_bf16 v[60:63], v[4:7], a[8:15], v200\n" \
"v_smfmac_f32_16x16x64_bf16 v[64:67], v[4:7], a[64:71], v200\n" \
"v_smfmac_f32_16x16x64_bf16 v[68:71], v[4:7], a[120:127], v200\n" \
"v_smfmac_f32_16x16x64_bf16 v[72:75], v[4:7], a[176:183], v200\n" \
"s_waitcnt lgkmcnt(4)\n" \
"v_smfmac_f32_16x16x64_bf16 v[60:63], v[8:11], a[16:23], v200\n" \
"v_smfmac_f32_16x16x64_bf16 v[64:67], v[8:11], a[72:79], v200\n" \
"v_smfmac_f32_16x16x64_bf16 v[68:71], v[8:11], a[128:135], v200\n" \
"v_smfmac_f32_16x16x64_bf16 v[72:75], v[8:11], a[184:191], v200\n" \
"s_waitcnt lgkmcnt(3)\n" \
"v_smfmac_f32_16x16x64_bf16 v[60:63], v[12:15], a[24:31], v200\n" \
"v_smfmac_f32_16x16x64_bf16 v[64:67], v[12:15], a[80:87], v200\n" \
"v_smfmac_f32_16x16x64_bf16 v[68:71], v[12:15], a[136:143], v200\n" \
"v_smfmac_f32_16x16x64_bf16 v[72:75], v[12:15], a[192:199], v200\n" \
"s_waitcnt lgkmcnt(2)\n" \
"v_smfmac_f32_16x16x64_bf16 v[60:63], v[16:19], a[32:39], v200\n" \
"v_smfmac_f32_16x16x64_bf16 v[64:67], v[16:19], a[88:95], v200\n" \
"v_smfmac_f32_16x16x64_bf16 v[68:71], v[16:19], a[144:151], v200\n" \
"v_smfmac_f32_16x16x64_bf16 v[72:75], v[16:19], a[200:207], v200\n" \
"s_waitcnt lgkmcnt(1)\n" \
"v_smfmac_f32_16x16x64_bf16 v[60:63], v[20:23], a[40:47], v200\n" \
"v_smfmac_f32_16x16x64_bf16 v[64:67], v[20:23], a[96:103], v200\n" \
"v_smfmac_f32_16x16x64_bf16 v[68:71], v[20:23], a[152:159], v200\n" \
"v_smfmac_f32_16x16x64_bf16 v[72:75], v[20:23], a[208:215], v200\n" \
"s_waitcnt lgkmcnt(0)\n" \
"v_smfmac_f32_16x16x64_bf16 v[60:63], v[24:27], a[48:55], v200\n" \
"v_smfmac_f32_16x16x64_bf16 v[64:67], v[24:27], a[104:111], v200\n" \
"v_smfmac_f32_16x16x64_bf16 v[68:71], v[24:27], a[160:167], v200\n" \
"v_smfmac_f32_16x16x64_bf16 v[72:75], v[24:27], a[216:223], v200\n" \
"s_nop 7\n" \
"v_permlane32_swap_b32 v60, v64\n" \
"v_permlane32_swap_b32 v61, v65\n" \
"v_permlane32_swap_b32 v62, v66\n" \
"v_permlane32_swap_b32 v63, v67\n" \
"v_add_f32 v60, v60, v64\n" \
"v_add_f32 v61, v61, v65\n" \
"v_add_f32 v62, v62, v66\n" \
"v_add_f32 v63, v63, v67\n" \
"v_cvt_pk_bf16_f32 v184, v60, v61\n" \
"v_cvt_pk_bf16_f32 v185, v62, v63\n" \
"ds_write_b64 v217, v[184:185] offset:0\n" \
"v_permlane32_swap_b32 v68, v72\n" \
"v_permlane32_swap_b32 v69, v73\n" \
"v_permlane32_swap_b32 v70, v74\n" \
"v_permlane32_swap_b32 v71, v75\n" \
"v_add_f32 v68, v68, v72\n" \
"v_add_f32 v69, v69, v73\n" \
"v_add_f32 v70, v70, v74\n" \
"v_add_f32 v71, v71, v75\n" \
"v_cvt_pk_bf16_f32 v186, v68, v69\n" \
"v_cvt_pk_bf16_f32 v187, v70, v71\n" \
"ds_write_b64 v217, v[186:187] offset:512\n" \
"ds_read_b64_tr_b16 v[0:1], v218 offset:0\n" \
"ds_read_b64_tr_b16 v[2:3], v218 offset:64\n" \
"s_add_u32 s43, s40, 0\n" \
"s_mul_i32 s43, s43, 7168\n" \
"s_waitcnt lgkmcnt(0)\n" \
"buffer_store_dwordx4 v[0:3], v219, s[16:19], s43 offen sc1\n" \
"s_waitcnt vmcnt(0)\n" \
"s_barrier\n" \
"ds_read_b128 v[0:3], v216 offset:7168\n" \
"ds_read_b128 v[4:7], v216 offset:8192\n" \
"ds_read_b128 v[8:11], v216 offset:9216\n" \
"ds_read_b128 v[12:15], v216 offset:10240\n" \
"ds_read_b128 v[16:19], v216 offset:11264\n" \
"ds_read_b128 v[20:23], v216 offset:12288\n" \
"ds_read_b128 v[24:27], v216 offset:13312\n" \
"v_mov_b32 v60, 0\n" \
"v_mov_b32 v61, 0\n" \
"v_mov_b32 v62, 0\n" \
"v_mov_b32 v63, 0\n" \
"v_mov_b32 v64, 0\n" \
"v_mov_b32 v65, 0\n" \
"v_mov_b32 v66, 0\n" \
"v_mov_b32 v67, 0\n" \
"v_mov_b32 v68, 0\n" \
"v_mov_b32 v69, 0\n" \
"v_mov_b32 v70, 0\n" \
"v_mov_b32 v71, 0\n" \
"v_mov_b32 v72, 0\n" \
"v_mov_b32 v73, 0\n" \
"v_mov_b32 v74, 0\n" \
"v_mov_b32 v75, 0\n" \
"s_nop 1\n" \
"s_waitcnt lgkmcnt(6)\n" \
"v_smfmac_f32_16x16x64_bf16 v[60:63], v[0:3], a[0:7], v200\n" \
"v_smfmac_f32_16x16x64_bf16 v[64:67], v[0:3], a[56:63], v200\n" \
"v_smfmac_f32_16x16x64_bf16 v[68:71], v[0:3], a[112:119], v200\n" \
"v_smfmac_f32_16x16x64_bf16 v[72:75], v[0:3], a[168:175], v200\n" \
"s_waitcnt lgkmcnt(5)\n" \
"v_smfmac_f32_16x16x64_bf16 v[60:63], v[4:7], a[8:15], v200\n" \
"v_smfmac_f32_16x16x64_bf16 v[64:67], v[4:7], a[64:71], v200\n" \
"v_smfmac_f32_16x16x64_bf16 v[68:71], v[4:7], a[120:127], v200\n" \
"v_smfmac_f32_16x16x64_bf16 v[72:75], v[4:7], a[176:183], v200\n" \
"s_waitcnt lgkmcnt(4)\n" \
"v_smfmac_f32_16x16x64_bf16 v[60:63], v[8:11], a[16:23], v200\n" \
"v_smfmac_f32_16x16x64_bf16 v[64:67], v[8:11], a[72:79], v200\n" \
"v_smfmac_f32_16x16x64_bf16 v[68:71], v[8:11], a[128:135], v200\n" \
"v_smfmac_f32_16x16x64_bf16 v[72:75], v[8:11], a[184:191], v200\n" \
"s_waitcnt lgkmcnt(3)\n" \
"v_smfmac_f32_16x16x64_bf16 v[60:63], v[12:15], a[24:31], v200\n" \
"v_smfmac_f32_16x16x64_bf16 v[64:67], v[12:15], a[80:87], v200\n" \
"v_smfmac_f32_16x16x64_bf16 v[68:71], v[12:15], a[136:143], v200\n" \
"v_smfmac_f32_16x16x64_bf16 v[72:75], v[12:15], a[192:199], v200\n" \
"s_waitcnt lgkmcnt(2)\n" \
"v_smfmac_f32_16x16x64_bf16 v[60:63], v[16:19], a[32:39], v200\n" \
"v_smfmac_f32_16x16x64_bf16 v[64:67], v[16:19], a[88:95], v200\n" \
"v_smfmac_f32_16x16x64_bf16 v[68:71], v[16:19], a[144:151], v200\n" \
"v_smfmac_f32_16x16x64_bf16 v[72:75], v[16:19], a[200:207], v200\n" \
"s_waitcnt lgkmcnt(1)\n" \
"v_smfmac_f32_16x16x64_bf16 v[60:63], v[20:23], a[40:47], v200\n" \
"v_smfmac_f32_16x16x64_bf16 v[64:67], v[20:23], a[96:103], v200\n" \
"v_smfmac_f32_16x16x64_bf16 v[68:71], v[20:23], a[152:159], v200\n" \
"v_smfmac_f32_16x16x64_bf16 v[72:75], v[20:23], a[208:215], v200\n" \
"s_waitcnt lgkmcnt(0)\n" \
"v_smfmac_f32_16x16x64_bf16 v[60:63], v[24:27], a[48:55], v200\n" \
"v_smfmac_f32_16x16x64_bf16 v[64:67], v[24:27], a[104:111], v200\n" \
"v_smfmac_f32_16x16x64_bf16 v[68:71], v[24:27], a[160:167], v200\n" \
"v_smfmac_f32_16x16x64_bf16 v[72:75], v[24:27], a[216:223], v200\n" \
"s_nop 7\n" \
"v_permlane32_swap_b32 v60, v64\n" \
"v_permlane32_swap_b32 v61, v65\n" \
"v_permlane32_swap_b32 v62, v66\n" \
"v_permlane32_swap_b32 v63, v67\n" \
"v_add_f32 v60, v60, v64\n" \
"v_add_f32 v61, v61, v65\n" \
"v_add_f32 v62, v62, v66\n" \
"v_add_f32 v63, v63, v67\n" \
"v_cvt_pk_bf16_f32 v184, v60, v61\n" \
"v_cvt_pk_bf16_f32 v185, v62, v63\n" \
"ds_write_b64 v217, v[184:185] offset:0\n" \
"v_permlane32_swap_b32 v68, v72\n" \
"v_permlane32_swap_b32 v69, v73\n" \
"v_permlane32_swap_b32 v70, v74\n" \
"v_permlane32_swap_b32 v71, v75\n" \
"v_add_f32 v68, v68, v72\n" \
"v_add_f32 v69, v69, v73\n" \
"v_add_f32 v70, v70, v74\n" \
"v_add_f32 v71, v71, v75\n" \
"v_cvt_pk_bf16_f32 v186, v68, v69\n" \
"v_cvt_pk_bf16_f32 v187, v70, v71\n" \
"ds_write_b64 v217, v[186:187] offset:512\n" \
"ds_read_b64_tr_b16 v[0:1], v218 offset:0\n" \
"ds_read_b64_tr_b16 v[2:3], v218 offset:64\n" \
"s_add_u32 s43, s40, 1\n" \
"s_mul_i32 s43, s43, 7168\n" \
"s_waitcnt lgkmcnt(0)\n" \
"buffer_store_dwordx4 v[0:3], v219, s[16:19], s43 offen sc1\n" \
"s_waitcnt vmcnt(0)\n" \
"s_barrier\n" \
".LX0_end:\n" \
"s_endpgm\n" \
".p2align 6\n" \
"fk_role_X1:\n" \
"ds_read_b32 v200, v255 offset:0\n" \
"ds_read_b32 v201, v255 offset:256\n" \
"ds_read_b32 v202, v255 offset:512\n" \
"ds_read_b32 v203, v255 offset:768\n" \
"ds_read_b32 v204, v255 offset:1024\n" \
"ds_read_b32 v205, v255 offset:1280\n" \
"ds_read_b32 v206, v255 offset:1536\n" \
"ds_read_b32 v207, v255 offset:1792\n" \
"ds_read_b32 v208, v255 offset:2048\n" \
"ds_read_b32 v209, v255 offset:2304\n" \
"ds_read_b32 v210, v255 offset:2560\n" \
"ds_read_b32 v211, v255 offset:2816\n" \
"ds_read_b32 v212, v255 offset:3072\n" \
"ds_read_b32 v213, v255 offset:3328\n" \
"ds_read_b32 v214, v255 offset:3584\n" \
"ds_read_b32 v215, v255 offset:3840\n" \
"ds_read_b32 v216, v255 offset:4096\n" \
"ds_read_b32 v217, v255 offset:4352\n" \
"ds_read_b32 v218, v255 offset:4608\n" \
"ds_read_b32 v219, v255 offset:4864\n" \
"ds_read_b32 v220, v255 offset:5120\n" \
"ds_read_b32 v221, v255 offset:5376\n" \
"ds_read_b32 v254, v255 offset:7936\n" \
"s_waitcnt lgkmcnt(0)\n" \
"v_readlane_b32 s4, v254, 0\n" \
"v_readlane_b32 s5, v254, 1\n" \
"v_readlane_b32 s6, v254, 2\n" \
"v_readlane_b32 s7, v254, 3\n" \
"v_readlane_b32 s8, v254, 4\n" \
"v_readlane_b32 s9, v254, 5\n" \
"v_readlane_b32 s10, v254, 6\n" \
"v_readlane_b32 s11, v254, 7\n" \
"v_readlane_b32 s12, v254, 8\n" \
"v_readlane_b32 s13, v254, 9\n" \
"v_readlane_b32 s14, v254, 10\n" \
"v_readlane_b32 s15, v254, 11\n" \
"v_readlane_b32 s16, v254, 12\n" \
"v_readlane_b32 s17, v254, 13\n" \
"v_readlane_b32 s18, v254, 14\n" \
"v_readlane_b32 s19, v254, 15\n" \
"v_readlane_b32 s20, v254, 16\n" \
"v_readlane_b32 s21, v254, 17\n" \
"v_readlane_b32 s22, v254, 18\n" \
"v_readlane_b32 s23, v254, 19\n" \
"v_readlane_b32 s24, v254, 20\n" \
"v_readlane_b32 s25, v254, 21\n" \
"v_readlane_b32 s26, v254, 22\n" \
"v_readlane_b32 s27, v254, 23\n" \
"v_readlane_b32 s28, v254, 24\n" \
"v_readlane_b32 s29, v254, 25\n" \
"v_readlane_b32 s30, v254, 26\n" \
"v_readlane_b32 s31, v254, 27\n" \
"v_readlane_b32 s32, v254, 28\n" \
"v_readlane_b32 s33, v254, 29\n" \
"v_readlane_b32 s34, v254, 30\n" \
"v_readlane_b32 s35, v254, 31\n" \
"v_readlane_b32 s36, v254, 32\n" \
"v_readlane_b32 s37, v254, 33\n" \
"v_readlane_b32 s38, v254, 34\n" \
"v_readlane_b32 s39, v254, 35\n" \
"s_mov_b32 s40, 0\n" \
"s_mov_b32 s51, 0\n" \
"s_nop 0\n" \
"v_mov_b32 v252, s38\n" \
"s_nop 4\n" \
"v_mov_b32 v100, -1\n" \
"v_mov_b32 v101, -1\n" \
"v_mov_b32 v102, -1\n" \
"v_mov_b32 v103, -1\n" \
"s_mov_b32 s35, -2\n" \
"s_mov_b32 s2, 0\n" \
"s_mov_b32 s3, -1\n" \
"v_writelane_b32 v251, s6, 0\n" \
"v_writelane_b32 v251, s10, 1\n" \
"v_writelane_b32 v251, s14, 2\n" \
"v_writelane_b32 v251, s18, 3\n" \
"s_mov_b32 s6, 0\n" \
"s_mov_b32 s10, 0\n" \
"s_mov_b32 s14, 0\n" \
"s_mov_b32 s18, 0\n" \
"s_mov_b32 s40, -1\n" \
"s_branch .LX1_pre\n" \
".LX1_wet:\n" \
"s_waitcnt vmcnt(0)\n" \
"v_readlane_b32 s6, v251, 0\n" \
"v_readlane_b32 s10, v251, 1\n" \
"v_readlane_b32 s14, v251, 2\n" \
"v_readlane_b32 s18, v251, 3\n" \
"s_nop 4\n" \
"s_add_u32 s43, s30, 1\n" \
"s_mov_b32 s50, 0\n" \
".LX1_w64l:\n" \
"buffer_load_dword v240, v204, s[4:7], 0 offen sc1\n" \
"s_waitcnt vmcnt(0)\n" \
".LX1_w64c:\n" \
"v_subrev_u32 v241, s43, v240\n" \
"v_cmp_gt_u32 vcc, 0x100, v241\n" \
"s_cmp_eq_u64 vcc, -1\n" \
"s_cbranch_scc1 .LX1_w64d\n" \
"s_sleep 2\n" \
"s_add_u32 s50, s50, 1\n" \
"s_cmp_lt_u32 s50, 0x20000\n" \
"s_cbranch_scc1 .LX1_w64l\n" \
"s_branch .LX1_end\n" \
".LX1_w64d:\n" \
"v_readlane_b32 s43, v202, 0\n" \
"s_nop 4\n" \
"buffer_load_dwordx4 a[0:3], v201, s[8:11], s43 offen sc1\n" \
"buffer_load_dwordx4 a[4:7], v201, s[8:11], s43 offen offset:1024 sc1\n" \
"v_readlane_b32 s43, v202, 7\n" \
"s_nop 4\n" \
"buffer_load_dwordx4 a[56:59], v201, s[8:11], s43 offen sc1\n" \
"buffer_load_dwordx4 a[60:63], v201, s[8:11], s43 offen offset:1024 sc1\n" \
"v_readlane_b32 s43, v202, 14\n" \
"s_nop 4\n" \
"buffer_load_dwordx4 a[112:115], v201, s[8:11], s43 offen sc1\n" \
"buffer_load_dwordx4 a[116:119], v201, s[8:11], s43 offen offset:1024 sc1\n" \
"v_readlane_b32 s43, v202, 21\n" \
"s_nop 4\n" \
"buffer_load_dwordx4 a[168:171], v201, s[8:11], s43 offen sc1\n" \
"buffer_load_dwordx4 a[172:175], v201, s[8:11], s43 offen offset:1024 sc1\n" \
"s_add_u32 s43, s30, 2\n" \
"s_mov_b32 s50, 0\n" \
"s_branch .LX1_w65c\n" \
".LX1_w65l:\n" \
"buffer_load_dword v240, v204, s[4:7], 0 offen sc1\n" \
"s_waitcnt vmcnt(0)\n" \
".LX1_w65c:\n" \
"v_subrev_u32 v241, s43, v240\n" \
"v_cmp_gt_u32 vcc, 0x100, v241\n" \
"s_cmp_eq_u64 vcc, -1\n" \
"s_cbranch_scc1 .LX1_w65d\n" \
"s_sleep 2\n" \
"s_add_u32 s50, s50, 1\n" \
"s_cmp_lt_u32 s50, 0x20000\n" \
"s_cbranch_scc1 .LX1_w65l\n" \
"s_branch .LX1_end\n" \
".LX1_w65d:\n" \
"v_readlane_b32 s43, v202, 4\n" \
"s_nop 4\n" \
"buffer_load_dwordx4 a[32:35], v201, s[8:11], s43 offen sc1\n" \
"buffer_load_dwordx4 a[36:39], v201, s[8:11], s43 offen offset:1024 sc1\n" \
"v_readlane_b32 s43, v202, 11\n" \
"s_nop 4\n" \
"buffer_load_dwordx4 a[88:91], v201, s[8:11], s43 offen sc1\n" \
"buffer_load_dwordx4 a[92:95], v201, s[8:11], s43 offen offset:1024 sc1\n" \
"v_readlane_b32 s43, v202, 18\n" \
"s_nop 4\n" \
"buffer_load_dwordx4 a[144:147], v201, s[8:11], s43 offen sc1\n" \
"buffer_load_dwordx4 a[148:151], v201, s[8:11], s43 offen offset:1024 sc1\n" \
"v_readlane_b32 s43, v202, 25\n" \
"s_nop 4\n" \
"buffer_load_dwordx4 a[200:203], v201, s[8:11], s43 offen sc1\n" \
"buffer_load_dwordx4 a[204:207], v201, s[8:11], s43 offen offset:1024 sc1\n" \
"s_add_u32 s43, s30, 3\n" \
"s_mov_b32 s50, 0\n" \
"s_branch .LX1_w66c\n" \
".LX1_w66l:\n" \
"buffer_load_dword v240, v204, s[4:7], 0 offen sc1\n" \
"s_waitcnt vmcnt(0)\n" \
".LX1_w66c:\n" \
"v_subrev_u32 v241, s43, v240\n" \
"v_cmp_gt_u32 vcc, 0x100, v241\n" \
"s_cmp_eq_u64 vcc, -1\n" \
"s_cbranch_scc1 .LX1_w66d\n" \
"s_sleep 2\n" \
"s_add_u32 s50, s50, 1\n" \
"s_cmp_lt_u32 s50, 0x20000\n" \
"s_cbranch_scc1 .LX1_w66l\n" \
"s_branch .LX1_end\n" \
".LX1_w66d:\n" \
"v_readlane_b32 s43, v202, 1\n" \
"s_nop 4\n" \
"buffer_load_dwordx4 a[8:11], v201, s[8:11], s43 offen sc1\n" \
"buffer_load_dwordx4 a[12:15], v201, s[8:11], s43 offen offset:1024 sc1\n" \
"v_readlane_b32 s43, v202, 8\n" \
"s_nop 4\n" \
"buffer_load_dwordx4 a[64:67], v201, s[8:11], s43 offen sc1\n" \
"buffer_load_dwordx4 a[68:71], v201, s[8:11], s43 offen offset:1024 sc1\n" \
"v_readlane_b32 s43, v202, 15\n" \
"s_nop 4\n" \
"buffer_load_dwordx4 a[120:123], v201, s[8:11], s43 offen sc1\n" \
"buffer_load_dwordx4 a[124:127], v201, s[8:11], s43 offen offset:1024 sc1\n" \
"v_readlane_b32 s43, v202, 22\n" \
"s_nop 4\n" \
"buffer_load_dwordx4 a[176:179], v201, s[8:11], s43 offen sc1\n" \
"buffer_load_dwordx4 a[180:183], v201, s[8:11], s43 offen offset:1024 sc1\n" \
"s_add_u32 s43, s30, 4\n" \
"s_mov_b32 s50, 0\n" \
"s_branch .LX1_w67c\n" \
".LX1_w67l:\n" \
"buffer_load_dword v240, v204, s[4:7], 0 offen sc1\n" \
"s_waitcnt vmcnt(0)\n" \
".LX1_w67c:\n" \
"v_subrev_u32 v241, s43, v240\n" \
"v_cmp_gt_u32 vcc, 0x100, v241\n" \
"s_cmp_eq_u64 vcc, -1\n" \
"s_cbranch_scc1 .LX1_w67d\n" \
"s_sleep 2\n" \
"s_add_u32 s50, s50, 1\n" \
"s_cmp_lt_u32 s50, 0x20000\n" \
"s_cbranch_scc1 .LX1_w67l\n" \
"s_branch .LX1_end\n" \
".LX1_w67d:\n" \
"v_readlane_b32 s43, v202, 5\n" \
"s_nop 4\n" \
"buffer_load_dwordx4 a[40:43], v201, s[8:11], s43 offen sc1\n" \
"buffer_load_dwordx4 a[44:47], v201, s[8:11], s43 offen offset:1024 sc1\n" \
"v_readlane_b32 s43, v202, 12\n" \
"s_nop 4\n" \
"buffer_load_dwordx4 a[96:99], v201, s[8:11], s43 offen sc1\n" \
"buffer_load_dwordx4 a[100:103], v201, s[8:11], s43 offen offset:1024 sc1\n" \
"v_readlane_b32 s43, v202, 19\n" \
"s_nop 4\n" \
"buffer_load_dwordx4 a[152:155], v201, s[8:11], s43 offen sc1\n" \
"buffer_load_dwordx4 a[156:159], v201, s[8:11], s43 offen offset:1024 sc1\n" \
"v_readlane_b32 s43, v202, 26\n" \
"s_nop 4\n" \
"buffer_load_dwordx4 a[208:211], v201, s[8:11], s43 offen sc1\n" \
"buffer_load_dwordx4 a[212:215], v201, s[8:11], s43 offen offset:1024 sc1\n" \
"s_add_u32 s43, s30, 5\n" \
"s_mov_b32 s50, 0\n" \
"s_branch .LX1_w68c\n" \
".LX1_w68l:\n" \
"buffer_load_dword v240, v204, s[4:7], 0 offen sc1\n" \
"s_waitcnt vmcnt(0)\n" \
".LX1_w68c:\n" \
"v_subrev_u32 v241, s43, v240\n" \
"v_cmp_gt_u32 vcc, 0x100, v241\n" \
"s_cmp_eq_u64 vcc, -1\n" \
"s_cbranch_scc1 .LX1_w68d\n" \
"s_sleep 2\n" \
"s_add_u32 s50, s50, 1\n" \
"s_cmp_lt_u32 s50, 0x20000\n" \
"s_cbranch_scc1 .LX1_w68l\n" \
"s_branch .LX1_end\n" \
".LX1_w68d:\n" \
"v_readlane_b32 s43, v202, 2\n" \
"s_nop 4\n" \
"buffer_load_dwordx4 a[16:19], v201, s[8:11], s43 offen sc1\n" \
"buffer_load_dwordx4 a[20:23], v201, s[8:11], s43 offen offset:1024 sc1\n" \
"v_readlane_b32 s43, v202, 9\n" \
"s_nop 4\n" \
"buffer_load_dwordx4 a[72:75], v201, s[8:11], s43 offen sc1\n" \
"buffer_load_dwordx4 a[76:79], v201, s[8:11], s43 offen offset:1024 sc1\n" \
"v_readlane_b32 s43, v202, 16\n" \
"s_nop 4\n" \
"buffer_load_dwordx4 a[128:131], v201, s[8:11], s43 offen sc1\n" \
"buffer_load_dwordx4 a[132:135], v201, s[8:11], s43 offen offset:1024 sc1\n" \
"v_readlane_b32 s43, v202, 23\n" \
"s_nop 4\n" \
"buffer_load_dwordx4 a[184:187], v201, s[8:11], s43 offen sc1\n" \
"buffer_load_dwordx4 a[188:191], v201, s[8:11], s43 offen offset:1024 sc1\n" \
"s_add_u32 s43, s30, 6\n" \
"s_mov_b32 s50, 0\n" \
"s_branch .LX1_w69c\n" \
".LX1_w69l:\n" \
"buffer_load_dword v240, v204, s[4:7], 0 offen sc1\n" \
"s_waitcnt vmcnt(0)\n" \
".LX1_w69c:\n" \
"v_subrev_u32 v241, s43, v240\n" \
"v_cmp_gt_u32 vcc, 0x100, v241\n" \
"s_cmp_eq_u64 vcc, -1\n" \
"s_cbranch_scc1 .LX1_w69d\n" \
"s_sleep 2\n" \
"s_add_u32 s50, s50, 1\n" \
"s_cmp_lt_u32 s50, 0x20000\n" \
"s_cbranch_scc1 .LX1_w69l\n" \
"s_branch .LX1_end\n" \
".LX1_w69d:\n" \
"v_readlane_b32 s43, v202, 6\n" \
"s_nop 4\n" \
"buffer_load_dwordx4 a[48:51], v201, s[8:11], s43 offen sc1\n" \
"buffer_load_dwordx4 a[52:55], v201, s[8:11], s43 offen offset:1024 sc1\n" \
"v_readlane_b32 s43, v202, 13\n" \
"s_nop 4\n" \
"buffer_load_dwordx4 a[104:107], v201, s[8:11], s43 offen sc1\n" \
"buffer_load_dwordx4 a[108:111], v201, s[8:11], s43 offen offset:1024 sc1\n" \
"v_readlane_b32 s43, v202, 20\n" \
"s_nop 4\n" \
"buffer_load_dwordx4 a[160:163], v201, s[8:11], s43 offen sc1\n" \
"buffer_load_dwordx4 a[164:167], v201, s[8:11], s43 offen offset:1024 sc1\n" \
"v_readlane_b32 s43, v202, 27\n" \
"s_nop 4\n" \
"buffer_load_dwordx4 a[216:219], v201, s[8:11], s43 offen sc1\n" \
"buffer_load_dwordx4 a[220:223], v201, s[8:11], s43 offen offset:1024 sc1\n" \
"s_add_u32 s43, s30, 7\n" \
"s_mov_b32 s50, 0\n" \
"s_branch .LX1_w70c\n" \
".LX1_w70l:\n" \
"buffer_load_dword v240, v204, s[4:7], 0 offen sc1\n" \
"s_waitcnt vmcnt(0)\n" \
".LX1_w70c:\n" \
"v_subrev_u32 v241, s43, v240\n" \
"v_cmp_gt_u32 vcc, 0x100, v241\n" \
"s_cmp_eq_u64 vcc, -1\n" \
"s_cbranch_scc1 .LX1_w70d\n" \
"s_sleep 2\n" \
"s_add_u32 s50, s50, 1\n" \
"s_cmp_lt_u32 s50, 0x20000\n" \
"s_cbranch_scc1 .LX1_w70l\n" \
"s_branch .LX1_end\n" \
".LX1_w70d:\n" \
"v_readlane_b32 s43, v202, 3\n" \
"s_nop 4\n" \
"buffer_load_dwordx4 a[24:27], v201, s[8:11], s43 offen sc1\n" \
"buffer_load_dwordx4 a[28:31], v201, s[8:11], s43 offen offset:1024 sc1\n" \
"v_readlane_b32 s43, v202, 10\n" \
"s_nop 4\n" \
"buffer_load_dwordx4 a[80:83], v201, s[8:11], s43 offen sc1\n" \
"buffer_load_dwordx4 a[84:87], v201, s[8:11], s43 offen offset:1024 sc1\n" \
"v_readlane_b32 s43, v202, 17\n" \
"s_nop 4\n" \
"buffer_load_dwordx4 a[136:139], v201, s[8:11], s43 offen sc1\n" \
"buffer_load_dwordx4 a[140:143], v201, s[8:11], s43 offen offset:1024 sc1\n" \
"v_readlane_b32 s43, v202, 24\n" \
"s_nop 4\n" \
"buffer_load_dwordx4 a[192:195], v201, s[8:11], s43 offen sc1\n" \
"buffer_load_dwordx4 a[196:199], v201, s[8:11], s43 offen offset:1024 sc1\n" \
"s_add_u32 s43, s30, 1\n" \
"s_mov_b32 s50, 0\n" \
".LX1_w71l:\n" \
"buffer_load_dword v240, v205, s[4:7], 0 offen sc1\n" \
"s_waitcnt vmcnt(0)\n" \
".LX1_w71c:\n" \
"v_subrev_u32 v241, s43, v240\n" \
"v_cmp_gt_u32 vcc, 0x100, v241\n" \
"s_cmp_eq_u64 vcc, -1\n" \
"s_cbranch_scc1 .LX1_w71d\n" \
"s_sleep 2\n" \
"s_add_u32 s50, s50, 1\n" \
"s_cmp_lt_u32 s50, 0x20000\n" \
"s_cbranch_scc1 .LX1_w71l\n" \
"s_branch .LX1_end\n" \
".LX1_w71d:\n" \
"v_readfirstlane_b32 s31, v241\n" \
"s_lshr_b32 s32, s31, 2\n" \
"s_xor_b32 s29, s31, s32\n" \
"s_and_b32 s29, s29, 3\n" \
"s_lshl_b32 s29, s29, 13\n" \
"s_bitset1_b32 s29, 21\n" \
"s_and_b32 s31, s31, 3\n" \
"s_lshl_b32 s31, s31, 13\n" \
"s_bitcmp1_b32 s39, 0\n" \
"s_cselect_b32 s43, s29, 0\n" \
"s_xor_b32 s31, s31, s43\n" \
"s_xor_b32 s32, s31, 0x4000\n" \
"s_mov_b32 s39, 0\n" \
"s_add_u32 s43, s30, 8\n" \
"s_mov_b32 s50, 0\n" \
".LX1_w72l:\n" \
"buffer_load_dword v240, v215, s[4:7], 0 offen sc1\n" \
"s_waitcnt vmcnt(0)\n" \
".LX1_w72c:\n" \
"v_subrev_u32 v241, s43, v240\n" \
"v_cmp_gt_u32 vcc, 0x100, v241\n" \
"s_cmp_eq_u64 vcc, -1\n" \
"s_cbranch_scc1 .LX1_w72d\n" \
"s_sleep 2\n" \
"s_add_u32 s50, s50, 1\n" \
"s_cmp_lt_u32 s50, 0x20000\n" \
"s_cbranch_scc1 .LX1_w72l\n" \
"s_branch .LX1_end\n" \
".LX1_w72d:\n" \
"s_add_u32 s50, s40, 0\n" \
"s_mul_i32 s50, s50, 28672\n" \
"s_add_u32 s50, s50, s34\n" \
"buffer_load_dwordx4 v[28:31], v201, s[12:15], s50 offen sc1\n" \
"buffer_load_dwordx4 v[32:35], v201, s[12:15], s50 offen offset:1024 sc1\n" \
"buffer_load_dwordx4 v[36:39], v201, s[12:15], s50 offen offset:2048 sc1\n" \
"buffer_load_dwordx4 v[40:43], v201, s[12:15], s50 offen offset:3072 sc1\n" \
"s_waitcnt vmcnt(0)\n" \
"s_barrier\n" \
"s_add_u32 s43, s30, 9\n" \
"s_mov_b32 s50, 0\n" \
".LX1_w73l:\n" \
"buffer_load_dword v240, v215, s[4:7], 0 offen sc1\n" \
"s_waitcnt vmcnt(0)\n" \
".LX1_w73c:\n" \
"v_subrev_u32 v241, s43, v240\n" \
"v_cmp_gt_u32 vcc, 0x100, v241\n" \
"s_cmp_eq_u64 vcc, -1\n" \
"s_cbranch_scc1 .LX1_w73d\n" \
"s_sleep 2\n" \
"s_add_u32 s50, s50, 1\n" \
"s_cmp_lt_u32 s50, 0x20000\n" \
"s_cbranch_scc1 .LX1_w73l\n" \
"s_branch .LX1_end\n" \
".LX1_w73d:\n" \
".LX1_pre:\n" \
".LX1_loop:\n" \
"s_add_u32 s43, s40, 2\n" \
"s_and_b32 s43, s43, 3\n" \
"s_bitcmp1_b32 s43, 0\n" \
"s_cselect_b32 s49, s32, s31\n" \
"s_lshl_b32 s43, s43, 19\n" \
"s_add_u32 s49, s49, s43\n" \
"ds_read_b128 v[0:3], v216 offset:0\n" \
"ds_read_b128 v[4:7], v216 offset:1024\n" \
"ds_read_b128 v[8:11], v216 offset:2048\n" \
"ds_read_b128 v[12:15], v216 offset:3072\n" \
"ds_read_b128 v[16:19], v216 offset:4096\n" \
"ds_read_b128 v[20:23], v216 offset:5120\n" \
"ds_read_b128 v[24:27], v216 offset:6144\n" \
"v_mov_b32 v60, 0\n" \
"v_mov_b32 v61, 0\n" \
"v_mov_b32 v62, 0\n" \
"v_mov_b32 v63, 0\n" \
"v_mov_b32 v64, 0\n" \
"v_mov_b32 v65, 0\n" \
"v_mov_b32 v66, 0\n" \
"v_mov_b32 v67, 0\n" \
"v_mov_b32 v68, 0\n" \
"v_mov_b32 v69, 0\n" \
"v_mov_b32 v70, 0\n" \
"v_mov_b32 v71, 0\n" \
"v_mov_b32 v72, 0\n" \
"v_mov_b32 v73, 0\n" \
"v_mov_b32 v74, 0\n" \
"v_mov_b32 v75, 0\n" \
"s_nop 1\n" \
"s_waitcnt lgkmcnt(6)\n" \
"v_smfmac_f32_16x16x64_bf16 v[60:63], v[0:3], a[0:7], v200\n" \
"v_smfmac_f32_16x16x64_bf16 v[64:67], v[0:3], a[56:63], v200\n" \
"v_smfmac_f32_16x16x64_bf16 v[68:71], v[0:3], a[112:119], v200\n" \
"v_smfmac_f32_16x16x64_bf16 v[72:75], v[0:3], a[168:175], v200\n" \
"s_waitcnt lgkmcnt(5)\n" \
"v_smfmac_f32_16x16x64_bf16 v[60:63], v[4:7], a[8:15], v200\n" \
"v_smfmac_f32_16x16x64_bf16 v[64:67], v[4:7], a[64:71], v200\n" \
"v_smfmac_f32_16x16x64_bf16 v[68:71], v[4:7], a[120:127], v200\n" \
"v_smfmac_f32_16x16x64_bf16 v[72:75], v[4:7], a[176:183], v200\n" \
"s_waitcnt lgkmcnt(4)\n" \
"v_smfmac_f32_16x16x64_bf16 v[60:63], v[8:11], a[16:23], v200\n" \
"v_smfmac_f32_16x16x64_bf16 v[64:67], v[8:11], a[72:79], v200\n" \
"v_smfmac_f32_16x16x64_bf16 v[68:71], v[8:11], a[128:135], v200\n" \
"v_smfmac_f32_16x16x64_bf16 v[72:75], v[8:11], a[184:191], v200\n" \
"s_waitcnt lgkmcnt(3)\n" \
"v_smfmac_f32_16x16x64_bf16 v[60:63], v[12:15], a[24:31], v200\n" \
"v_smfmac_f32_16x16x64_bf16 v[64:67], v[12:15], a[80:87], v200\n" \
"v_smfmac_f32_16x16x64_bf16 v[68:71], v[12:15], a[136:143], v200\n" \
"v_smfmac_f32_16x16x64_bf16 v[72:75], v[12:15], a[192:199], v200\n" \
"s_waitcnt lgkmcnt(2)\n" \
"v_smfmac_f32_16x16x64_bf16 v[60:63], v[16:19], a[32:39], v200\n" \
"v_smfmac_f32_16x16x64_bf16 v[64:67], v[16:19], a[88:95], v200\n" \
"v_smfmac_f32_16x16x64_bf16 v[68:71], v[16:19], a[144:151], v200\n" \
"v_smfmac_f32_16x16x64_bf16 v[72:75], v[16:19], a[200:207], v200\n" \
"s_waitcnt lgkmcnt(1)\n" \
"v_smfmac_f32_16x16x64_bf16 v[60:63], v[20:23], a[40:47], v200\n" \
"v_smfmac_f32_16x16x64_bf16 v[64:67], v[20:23], a[96:103], v200\n" \
"v_smfmac_f32_16x16x64_bf16 v[68:71], v[20:23], a[152:159], v200\n" \
"v_smfmac_f32_16x16x64_bf16 v[72:75], v[20:23], a[208:215], v200\n" \
"s_waitcnt lgkmcnt(0)\n" \
"v_smfmac_f32_16x16x64_bf16 v[60:63], v[24:27], a[48:55], v200\n" \
"v_smfmac_f32_16x16x64_bf16 v[64:67], v[24:27], a[104:111], v200\n" \
"v_smfmac_f32_16x16x64_bf16 v[68:71], v[24:27], a[160:167], v200\n" \
"v_smfmac_f32_16x16x64_bf16 v[72:75], v[24:27], a[216:223], v200\n" \
"s_barrier\n" \
"s_add_u32 s50, s40, 1\n" \
"s_mul_i32 s50, s50, 28672\n" \
"s_add_u32 s50, s50, s34\n" \
"buffer_load_dwordx4 v[44:47], v201, s[12:15], s50 offen sc1\n" \
"buffer_load_dwordx4 v[48:51], v201, s[12:15], s50 offen offset:1024 sc1\n" \
"buffer_load_dwordx4 v[52:55], v201, s[12:15], s50 offen offset:2048 sc1\n" \
"buffer_load_dwordx4 v[56:59], v201, s[12:15], s50 offen offset:3072 sc1\n" \
"s_add_u32 s43, s30, s40\n" \
"s_add_u32 s43, s43, -1\n" \
"v_mov_b32 v241, s43\n" \
"buffer_store_dword v241, v220, s[4:7], 0 offen sc1\n" \
"s_nop 7\n" \
"v_permlane32_swap_b32 v60, v64\n" \
"v_permlane32_swap_b32 v61, v65\n" \
"v_permlane32_swap_b32 v62, v66\n" \
"v_permlane32_swap_b32 v63, v67\n" \
"v_add_f32 v60, v60, v64\n" \
"v_add_f32 v61, v61, v65\n" \
"v_add_f32 v62, v62, v66\n" \
"v_add_f32 v63, v63, v67\n" \
"v_cvt_pk_bf16_f32 v184, v60, v61\n" \
"v_cvt_pk_bf16_f32 v185, v62, v63\n" \
"ds_write_b64 v217, v[184:185] offset:0\n" \
"v_permlane32_swap_b32 v68, v72\n" \
"v_permlane32_swap_b32 v69, v73\n" \
"v_permlane32_swap_b32 v70, v74\n" \
"v_permlane32_swap_b32 v71, v75\n" \
"v_add_f32 v68, v68, v72\n" \
"v_add_f32 v69, v69, v73\n" \
"v_add_f32 v70, v70, v74\n" \
"v_add_f32 v71, v71, v75\n" \
"v_cvt_pk_bf16_f32 v186, v68, v69\n" \
"v_cvt_pk_bf16_f32 v187, v70, v71\n" \
"ds_write_b64 v217, v[186:187] offset:512\n" \
"ds_read_b64_tr_b16 v[0:1], v218 offset:0\n" \
"ds_read_b64_tr_b16 v[2:3], v218 offset:64\n" \
"s_add_u32 s43, s40, 0\n" \
"s_mul_i32 s43, s43, 7168\n" \
"s_waitcnt lgkmcnt(0)\n" \
"buffer_store_dwordx4 v[0:3], v219, s[16:19], s43 offen sc1\n" \
"s_waitcnt vmcnt(6)\n" \
"s_barrier\n" \
"ds_read_b64_tr_b16 v[92:93], v213\n" \
"ds_read_b64_tr_b16 v[94:95], v213 offset:64\n" \
"ds_read_b32 v84, v252 offset:16\n" \
"v_mov_b32 v245, v245\n" \
"s_nop 2\n" \
"v_mov_b32 v245, v245\n" \
"s_nop 2\n" \
"v_mov_b32 v245, v245\n" \
"s_waitcnt lgkmcnt(1)\n" \
"v_mfma_f32_16x16x32_bf16 v[68:71], v[92:95], v[28:31], 0\n" \
"s_waitcnt lgkmcnt(0)\n" \
"v_xor_b32 v85, s29, v84\n" \
"v_mfma_f32_16x16x32_bf16 v[76:79], v[92:95], v[36:39], 0\n" \
"v_add_u32 v84, v84, v210\n" \
"v_add_u32 v85, v85, v210\n" \
"v_mfma_f32_16x16x32_bf16 v[72:75], v[92:95], v[32:35], 0\n" \
"s_nop 1\n" \
"v_mfma_f32_16x16x32_bf16 v[80:83], v[92:95], v[40:43], 0\n" \
"v_cvt_pk_bf16_f32 v96, v68, v69\n" \
"v_cvt_pk_bf16_f32 v97, v70, v71\n" \
"v_cvt_pk_bf16_f32 v98, v76, v77\n" \
"v_cvt_pk_bf16_f32 v99, v78, v79\n" \
"s_mov_b64 exec, s[2:3]\n" \
"v_cvt_pk_bf16_f32 v96, v72, v73\n" \
"v_cvt_pk_bf16_f32 v97, v74, v75\n" \
"s_nop 0\n" \
"v_cvt_pk_bf16_f32 v98, v80, v81\n" \
"v_cvt_pk_bf16_f32 v99, v82, v83\n" \
"s_mov_b64 exec, -1\n" \
"s_nop 1\n" \
"v_permlane16_swap_b32 v96, v98\n" \
"v_permlane16_swap_b32 v97, v99\n" \
"buffer_store_dwordx4 v[96:99], v84, s[4:7], 0 offen sc1\n" \
"s_barrier\n" \
"buffer_store_dwordx4 v[96:99], v85, s[4:7], 0 offen sc1\n" \
"buffer_store_dwordx4 v[100:103], v214, s[4:7], s49 offen sc1\n" \
"s_xor_b32 s43, s49, s29\n" \
"buffer_store_dwordx4 v[100:103], v214, s[4:7], s43 offen sc1\n" \
"s_waitcnt vmcnt(4)\n" \
"v_mov_b32 v28, v44\n" \
"v_mov_b32 v29, v45\n" \
"v_mov_b32 v30, v46\n" \
"v_mov_b32 v31, v47\n" \
"v_mov_b32 v32, v48\n" \
"v_mov_b32 v33, v49\n" \
"v_mov_b32 v34, v50\n" \
"v_mov_b32 v35, v51\n" \
"v_mov_b32 v36, v52\n" \
"v_mov_b32 v37, v53\n" \
"v_mov_b32 v38, v54\n" \
"v_mov_b32 v39, v55\n" \
"v_mov_b32 v40, v56\n" \
"v_mov_b32 v41, v57\n" \
"v_mov_b32 v42, v58\n" \
"v_mov_b32 v43, v59\n" \
"s_add_u32 s40, s40, 1\n" \
"s_cmp_eq_u32 s40, 0\n" \
"s_cbranch_scc1 .LX1_wet\n" \
"s_cmp_lt_u32 s40, 63\n" \
"s_cbranch_scc1 .LX1_loop\n" \
"ds_read_b128 v[0:3], v216 offset:0\n" \
"ds_read_b128 v[4:7], v216 offset:1024\n" \
"ds_read_b128 v[8:11], v216 offset:2048\n" \
"ds_read_b128 v[12:15], v216 offset:3072\n" \
"ds_read_b128 v[16:19], v216 offset:4096\n" \
"ds_read_b128 v[20:23], v216 offset:5120\n" \
"ds_read_b128 v[24:27], v216 offset:6144\n" \
"v_mov_b32 v60, 0\n" \
"v_mov_b32 v61, 0\n" \
"v_mov_b32 v62, 0\n" \
"v_mov_b32 v63, 0\n" \
"v_mov_b32 v64, 0\n" \
"v_mov_b32 v65, 0\n" \
"v_mov_b32 v66, 0\n" \
"v_mov_b32 v67, 0\n" \
"v_mov_b32 v68, 0\n" \
"v_mov_b32 v69, 0\n" \
"v_mov_b32 v70, 0\n" \
"v_mov_b32 v71, 0\n" \
"v_mov_b32 v72, 0\n" \
"v_mov_b32 v73, 0\n" \
"v_mov_b32 v74, 0\n" \
"v_mov_b32 v75, 0\n" \
"s_nop 1\n" \
"s_waitcnt lgkmcnt(6)\n" \
"v_smfmac_f32_16x16x64_bf16 v[60:63], v[0:3], a[0:7], v200\n" \
"v_smfmac_f32_16x16x64_bf16 v[64:67], v[0:3], a[56:63], v200\n" \
"v_smfmac_f32_16x16x64_bf16 v[68:71], v[0:3], a[112:119], v200\n" \
"v_smfmac_f32_16x16x64_bf16 v[72:75], v[0:3], a[168:175], v200\n" \
"s_waitcnt lgkmcnt(5)\n" \
"v_smfmac_f32_16x16x64_bf16 v[60:63], v[4:7], a[8:15], v200\n" \
"v_smfmac_f32_16x16x64_bf16 v[64:67], v[4:7], a[64:71], v200\n" \
"v_smfmac_f32_16x16x64_bf16 v[68:71], v[4:7], a[120:127], v200\n" \
"v_smfmac_f32_16x16x64_bf16 v[72:75], v[4:7], a[176:183], v200\n" \
"s_waitcnt lgkmcnt(4)\n" \
"v_smfmac_f32_16x16x64_bf16 v[60:63], v[8:11], a[16:23], v200\n" \
"v_smfmac_f32_16x16x64_bf16 v[64:67], v[8:11], a[72:79], v200\n" \
"v_smfmac_f32_16x16x64_bf16 v[68:71], v[8:11], a[128:135], v200\n" \
"v_smfmac_f32_16x16x64_bf16 v[72:75], v[8:11], a[184:191], v200\n" \
"s_waitcnt lgkmcnt(3)\n" \
"v_smfmac_f32_16x16x64_bf16 v[60:63], v[12:15], a[24:31], v200\n" \
"v_smfmac_f32_16x16x64_bf16 v[64:67], v[12:15], a[80:87], v200\n" \
"v_smfmac_f32_16x16x64_bf16 v[68:71], v[12:15], a[136:143], v200\n" \
"v_smfmac_f32_16x16x64_bf16 v[72:75], v[12:15], a[192:199], v200\n" \
"s_waitcnt lgkmcnt(2)\n" \
"v_smfmac_f32_16x16x64_bf16 v[60:63], v[16:19], a[32:39], v200\n" \
"v_smfmac_f32_16x16x64_bf16 v[64:67], v[16:19], a[88:95], v200\n" \
"v_smfmac_f32_16x16x64_bf16 v[68:71], v[16:19], a[144:151], v200\n" \
"v_smfmac_f32_16x16x64_bf16 v[72:75], v[16:19], a[200:207], v200\n" \
"s_waitcnt lgkmcnt(1)\n" \
"v_smfmac_f32_16x16x64_bf16 v[60:63], v[20:23], a[40:47], v200\n" \
"v_smfmac_f32_16x16x64_bf16 v[64:67], v[20:23], a[96:103], v200\n" \
"v_smfmac_f32_16x16x64_bf16 v[68:71], v[20:23], a[152:159], v200\n" \
"v_smfmac_f32_16x16x64_bf16 v[72:75], v[20:23], a[208:215], v200\n" \
"s_waitcnt lgkmcnt(0)\n" \
"v_smfmac_f32_16x16x64_bf16 v[60:63], v[24:27], a[48:55], v200\n" \
"v_smfmac_f32_16x16x64_bf16 v[64:67], v[24:27], a[104:111], v200\n" \
"v_smfmac_f32_16x16x64_bf16 v[68:71], v[24:27], a[160:167], v200\n" \
"v_smfmac_f32_16x16x64_bf16 v[72:75], v[24:27], a[216:223], v200\n" \
"s_nop 7\n" \
"v_permlane32_swap_b32 v60, v64\n" \
"v_permlane32_swap_b32 v61, v65\n" \
"v_permlane32_swap_b32 v62, v66\n" \
"v_permlane32_swap_b32 v63, v67\n" \
"v_add_f32 v60, v60, v64\n" \
"v_add_f32 v61, v61, v65\n" \
"v_add_f32 v62, v62, v66\n" \
"v_add_f32 v63, v63, v67\n" \
"v_cvt_pk_bf16_f32 v184, v60, v61\n" \
"v_cvt_pk_bf16_f32 v185, v62, v63\n" \
"ds_write_b64 v217, v[184:185] offset:0\n" \
"v_permlane32_swap_b32 v68, v72\n" \
"v_permlane32_swap_b32 v69, v73\n" \
"v_permlane32_swap_b32 v70, v74\n" \
"v_permlane32_swap_b32 v71, v75\n" \
"v_add_f32 v68, v68, v72\n" \
"v_add_f32 v69, v69, v73\n" \
"v_add_f32 v70, v70, v74\n" \
"v_add_f32 v71, v71, v75\n" \
"v_cvt_pk_bf16_f32 v186, v68, v69\n" \
"v_cvt_pk_bf16_f32 v187, v70, v71\n" \
"ds_write_b64 v217, v[186:187] offset:512\n" \
"ds_read_b64_tr_b16 v[0:1], v218 offset:0\n" \
"ds_read_b64_tr_b16 v[2:3], v218 offset:64\n" \
"s_add_u32 s43, s40, 0\n" \
"s_mul_i32 s43, s43, 7168\n" \
"s_waitcnt lgkmcnt(0)\n" \
"buffer_store_dwordx4 v[0:3], v219, s[16:19], s43 offen sc1\n" \
"s_waitcnt vmcnt(0)\n" \
"s_barrier\n" \
"s_add_u32 s43, s30, s40\n" \
"v_mov_b32 v241, s43\n" \
"buffer_store_dword v241, v220, s[4:7], 0 offen sc1\n" \
"ds_read_b128 v[0:3], v216 offset:7168\n" \
"ds_read_b128 v[4:7], v216 offset:8192\n" \
"ds_read_b128 v[8:11], v216 offset:9216\n" \
"ds_read_b128 v[12:15], v216 offset:10240\n" \
"ds_read_b128 v[16:19], v216 offset:11264\n" \
"ds_read_b128 v[20:23], v216 offset:12288\n" \
"ds_read_b128 v[24:27], v216 offset:13312\n" \
"v_mov_b32 v60, 0\n" \
"v_mov_b32 v61, 0\n" \
"v_mov_b32 v62, 0\n" \
"v_mov_b32 v63, 0\n" \
"v_mov_b32 v64, 0\n" \
"v_mov_b32 v65, 0\n" \
"v_mov_b32 v66, 0\n" \
"v_mov_b32 v67, 0\n" \
"v_mov_b32 v68, 0\n" \
"v_mov_b32 v69, 0\n" \
"v_mov_b32 v70, 0\n" \
"v_mov_b32 v71, 0\n" \
"v_mov_b32 v72, 0\n" \
"v_mov_b32 v73, 0\n" \
"v_mov_b32 v74, 0\n" \
"v_mov_b32 v75, 0\n" \
"s_nop 1\n" \
"s_waitcnt lgkmcnt(6)\n" \
"v_smfmac_f32_16x16x64_bf16 v[60:63], v[0:3], a[0:7], v200\n" \
"v_smfmac_f32_16x16x64_bf16 v[64:67], v[0:3], a[56:63], v200\n" \
"v_smfmac_f32_16x16x64_bf16 v[68:71], v[0:3], a[112:119], v200\n" \
"v_smfmac_f32_16x16x64_bf16 v[72:75], v[0:3], a[168:175], v200\n" \
"s_waitcnt lgkmcnt(5)\n" \
"v_smfmac_f32_16x16x64_bf16 v[60:63], v[4:7], a[8:15], v200\n" \
"v_smfmac_f32_16x16x64_bf16 v[64:67], v[4:7], a[64:71], v200\n" \
"v_smfmac_f32_16x16x64_bf16 v[68:71], v[4:7], a[120:127], v200\n" \
"v_smfmac_f32_16x16x64_bf16 v[72:75], v[4:7], a[176:183], v200\n" \
"s_waitcnt lgkmcnt(4)\n" \
"v_smfmac_f32_16x16x64_bf16 v[60:63], v[8:11], a[16:23], v200\n" \
"v_smfmac_f32_16x16x64_bf16 v[64:67], v[8:11], a[72:79], v200\n" \
"v_smfmac_f32_16x16x64_bf16 v[68:71], v[8:11], a[128:135], v200\n" \
"v_smfmac_f32_16x16x64_bf16 v[72:75], v[8:11], a[184:191], v200\n" \
"s_waitcnt lgkmcnt(3)\n" \
"v_smfmac_f32_16x16x64_bf16 v[60:63], v[12:15], a[24:31], v200\n" \
"v_smfmac_f32_16x16x64_bf16 v[64:67], v[12:15], a[80:87], v200\n" \
"v_smfmac_f32_16x16x64_bf16 v[68:71], v[12:15], a[136:143], v200\n" \
"v_smfmac_f32_16x16x64_bf16 v[72:75], v[12:15], a[192:199], v200\n" \
"s_waitcnt lgkmcnt(2)\n" \
"v_smfmac_f32_16x16x64_bf16 v[60:63], v[16:19], a[32:39], v200\n" \
"v_smfmac_f32_16x16x64_bf16 v[64:67], v[16:19], a[88:95], v200\n" \
"v_smfmac_f32_16x16x64_bf16 v[68:71], v[16:19], a[144:151], v200\n" \
"v_smfmac_f32_16x16x64_bf16 v[72:75], v[16:19], a[200:207], v200\n" \
"s_waitcnt lgkmcnt(1)\n" \
"v_smfmac_f32_16x16x64_bf16 v[60:63], v[20:23], a[40:47], v200\n" \
"v_smfmac_f32_16x16x64_bf16 v[64:67], v[20:23], a[96:103], v200\n" \
"v_smfmac_f32_16x16x64_bf16 v[68:71], v[20:23], a[152:159], v200\n" \
"v_smfmac_f32_16x16x64_bf16 v[72:75], v[20:23], a[208:215], v200\n" \
"s_waitcnt lgkmcnt(0)\n" \
"v_smfmac_f32_16x16x64_bf16 v[60:63], v[24:27], a[48:55], v200\n" \
"v_smfmac_f32_16x16x64_bf16 v[64:67], v[24:27], a[104:111], v200\n" \
"v_smfmac_f32_16x16x64_bf16 v[68:71], v[24:27], a[160:167], v200\n" \
"v_smfmac_f32_16x16x64_bf16 v[72:75], v[24:27], a[216:223], v200\n" \
"s_nop 7\n" \
"v_permlane32_swap_b32 v60, v64\n" \
"v_permlane32_swap_b32 v61, v65\n" \
"v_permlane32_swap_b32 v62, v66\n" \
"v_permlane32_swap_b32 v63, v67\n" \
"v_add_f32 v60, v60, v64\n" \
"v_add_f32 v61, v61, v65\n" \
"v_add_f32 v62, v62, v66\n" \
"v_add_f32 v63, v63, v67\n" \
"v_cvt_pk_bf16_f32 v184, v60, v61\n" \
"v_cvt_pk_bf16_f32 v185, v62, v63\n" \
"ds_write_b64 v217, v[184:185] offset:0\n" \
"v_permlane32_swap_b32 v68, v72\n" \
"v_permlane32_swap_b32 v69, v73\n" \
"v_permlane32_swap_b32 v70, v74\n" \
"v_permlane32_swap_b32 v71, v75\n" \
"v_add_f32 v68, v68, v72\n" \
"v_add_f32 v69, v69, v73\n" \
"v_add_f32 v70, v70, v74\n" \
"v_add_f32 v71, v71, v75\n" \
"v_cvt_pk_bf16_f32 v186, v68, v69\n" \
"v_cvt_pk_bf16_f32 v187, v70, v71\n" \
"ds_write_b64 v217, v[186:187] offset:512\n" \
"ds_read_b64_tr_b16 v[0:1], v218 offset:0\n" \
"ds_read_b64_tr_b16 v[2:3], v218 offset:64\n" \
"s_add_u32 s43, s40, 1\n" \
"s_mul_i32 s43, s43, 7168\n" \
"s_waitcnt lgkmcnt(0)\n" \
"buffer_store_dwordx4 v[0:3], v219, s[16:19], s43 offen sc1\n" \
"s_waitcnt vmcnt(0)\n" \
"s_barrier\n" \
"s_add_u32 s43, s30, s40\n" \
"s_add_u32 s43, s43, 1\n" \
"v_mov_b32 v241, s43\n" \
"buffer_store_dword v241, v220, s[4:7], 0 offen sc1\n" \
".LX1_end:\n" \
"s_endpgm\n" \
".p2align 6\n" \
"fk_role_X2:\n" \
"ds_read_b32 v200, v255 offset:0\n" \
"ds_read_b32 v201, v255 offset:256\n" \
"ds_read_b32 v202, v255 offset:512\n" \
"ds_read_b32 v203, v255 offset:768\n" \
"ds_read_b32 v204, v255 offset:1024\n" \
"ds_read_b32 v205, v255 offset:1280\n" \
"ds_read_b32 v206, v255 offset:1536\n" \
"ds_read_b32 v207, v255 offset:1792\n" \
"ds_read_b32 v208, v255 offset:2048\n" \
"ds_read_b32 v209, v255 offset:2304\n" \
"ds_read_b32 v210, v255 offset:2560\n" \
"ds_read_b32 v211, v255 offset:2816\n" \
"ds_read_b32 v212, v255 offset:3072\n" \
"ds_read_b32 v213, v255 offset:3328\n" \
"ds_read_b32 v214, v255 offset:3584\n" \
"ds_read_b32 v215, v255 offset:3840\n" \
"ds_read_b32 v216, v255 offset:4096\n" \
"ds_read_b32 v217, v255 offset:4352\n" \
"ds_read_b32 v218, v255 offset:4608\n" \
"ds_read_b32 v219, v255 offset:4864\n" \
"ds_read_b32 v220, v255 offset:5120\n" \
"ds_read_b32 v221, v255 offset:5376\n" \
"ds_read_b32 v254, v255 offset:7936\n" \
"s_waitcnt lgkmcnt(0)\n" \
"v_readlane_b32 s4, v254, 0\n" \
"v_readlane_b32 s5, v254, 1\n" \
"v_readlane_b32 s6, v254, 2\n" \
"v_readlane_b32 s7, v254, 3\n" \
"v_readlane_b32 s8, v254, 4\n" \
"v_readlane_b32 s9, v254, 5\n" \
"v_readlane_b32 s10, v254, 6\n" \
"v_readlane_b32 s11, v254, 7\n" \
"v_readlane_b32 s12, v254, 8\n" \
"v_readlane_b32 s13, v254, 9\n" \
"v_readlane_b32 s14, v254, 10\n" \
"v_readlane_b32 s15, v254, 11\n" \
"v_readlane_b32 s16, v254, 12\n" \
"v_readlane_b32 s17, v254, 13\n" \
"v_readlane_b32 s18, v254, 14\n" \
"v_readlane_b32 s19, v254, 15\n" \
"v_readlane_b32 s20, v254, 16\n" \
"v_readlane_b32 s21, v254, 17\n" \
"v_readlane_b32 s22, v254, 18\n" \
"v_readlane_b32 s23, v254, 19\n" \
"v_readlane_b32 s24, v254, 20\n" \
"v_readlane_b32 s25, v254, 21\n" \
"v_readlane_b32 s26, v254, 22\n" \
"v_readlane_b32 s27, v254, 23\n" \
"v_readlane_b32 s28, v254, 24\n" \
"v_readlane_b32 s29, v254, 25\n" \
"v_readlane_b32 s30, v254, 26\n" \
"v_readlane_b32 s31, v254, 27\n" \
"v_readlane_b32 s32, v254, 28\n" \
"v_readlane_b32 s33, v254, 29\n" \
"v_readlane_b32 s34, v254, 30\n" \
"v_readlane_b32 s35, v254, 31\n" \
"v_readlane_b32 s36, v254, 32\n" \
"v_readlane_b32 s37, v254, 33\n" \
"v_readlane_b32 s38, v254, 34\n" \
"v_readlane_b32 s39, v254, 35\n" \
"s_mov_b32 s40, 0\n" \
"s_mov_b32 s51, 0\n" \
"s_nop 0\n" \
"v_mov_b32 v252, s38\n" \
"s_nop 4\n" \
"v_mov_b32 v100, -1\n" \
"v_mov_b32 v101, -1\n" \
"v_mov_b32 v102, -1\n" \
"v_mov_b32 v103, -1\n" \
"s_mov_b32 s35, -2\n" \
"s_mov_b32 s2, 0\n" \
"s_mov_b32 s3, -1\n" \
"v_writelane_b32 v251, s6, 0\n" \
"v_writelane_b32 v251, s10, 1\n" \
"v_writelane_b32 v251, s14, 2\n" \
"v_writelane_b32 v251, s18, 3\n" \
"s_mov_b32 s6, 0\n" \
"s_mov_b32 s10, 0\n" \
"s_mov_b32 s14, 0\n" \
"s_mov_b32 s18, 0\n" \
"s_mov_b32 s40, -1\n" \
"s_branch .LX2_pre\n" \
".LX2_wet:\n" \
"s_waitcnt vmcnt(0)\n" \
"v_readlane_b32 s6, v251, 0\n" \
"v_readlane_b32 s10, v251, 1\n" \
"v_readlane_b32 s14, v251, 2\n" \
"v_readlane_b32 s18, v251, 3\n" \
"s_nop 4\n" \
"s_add_u32 s43, s30, 1\n" \
"s_mov_b32 s50, 0\n" \
".LX2_w74l:\n" \
"buffer_load_dword v240, v204, s[4:7], 0 offen sc1\n" \
"s_waitcnt vmcnt(0)\n" \
".LX2_w74c:\n" \
"v_subrev_u32 v241, s43, v240\n" \
"v_cmp_gt_u32 vcc, 0x100, v241\n" \
"s_cmp_eq_u64 vcc, -1\n" \
"s_cbranch_scc1 .LX2_w74d\n" \
"s_sleep 2\n" \
"s_add_u32 s50, s50, 1\n" \
"s_cmp_lt_u32 s50, 0x20000\n" \
"s_cbranch_scc1 .LX2_w74l\n" \
"s_branch .LX2_end\n" \
".LX2_w74d:\n" \
"v_readlane_b32 s43, v202, 0\n" \
"s_nop 4\n" \
"buffer_load_dwordx4 a[0:3], v201, s[8:11], s43 offen sc1\n" \
"buffer_load_dwordx4 a[4:7], v201, s[8:11], s43 offen offset:1024 sc1\n" \
"v_readlane_b32 s43, v202, 7\n" \
"s_nop 4\n" \
"buffer_load_dwordx4 a[56:59], v201, s[8:11], s43 offen sc1\n" \
"buffer_load_dwordx4 a[60:63], v201, s[8:11], s43 offen offset:1024 sc1\n" \
"v_readlane_b32 s43, v202, 14\n" \
"s_nop 4\n" \
"buffer_load_dwordx4 a[112:115], v201, s[8:11], s43 offen sc1\n" \
"buffer_load_dwordx4 a[116:119], v201, s[8:11], s43 offen offset:1024 sc1\n" \
"v_readlane_b32 s43, v202, 21\n" \
"s_nop 4\n" \
"buffer_load_dwordx4 a[168:171], v201, s[8:11], s43 offen sc1\n" \
"buffer_load_dwordx4 a[172:175], v201, s[8:11], s43 offen offset:1024 sc1\n" \
"v_readlane_b32 s43, v202, 28\n" \
"s_nop 4\n" \
"buffer_load_dwordx4 a[224:227], v201, s[8:11], s43 offen sc1\n" \
"buffer_load_dwordx4 a[228:231], v201, s[8:11], s43 offen offset:1024 sc1\n" \
"v_readlane_b32 s43, v202, 35\n" \
"s_nop 4\n" \
"buffer_load_dwordx4 v[128:131], v201, s[8:11], s43 offen sc1\n" \
"buffer_load_dwordx4 v[132:135], v201, s[8:11], s43 offen offset:1024 sc1\n" \
"s_add_u32 s43, s30, 2\n" \
"s_mov_b32 s50, 0\n" \
"s_branch .LX2_w75c\n" \
".LX2_w75l:\n" \
"buffer_load_dword v240, v204, s[4:7], 0 offen sc1\n" \
"s_waitcnt vmcnt(0)\n" \
".LX2_w75c:\n" \
"v_subrev_u32 v241, s43, v240\n" \
"v_cmp_gt_u32 vcc, 0x100, v241\n" \
"s_cmp_eq_u64 vcc, -1\n" \
"s_cbranch_scc1 .LX2_w75d\n" \
"s_sleep 2\n" \
"s_add_u32 s50, s50, 1\n" \
"s_cmp_lt_u32 s50, 0x20000\n" \
"s_cbranch_scc1 .LX2_w75l\n" \
"s_branch .LX2_end\n" \
".LX2_w75d:\n" \
"v_readlane_b32 s43, v202, 4\n" \
"s_nop 4\n" \
"buffer_load_dwordx4 a[32:35], v201, s[8:11], s43 offen sc1\n" \
"buffer_load_dwordx4 a[36:39], v201, s[8:11], s43 offen offset:1024 sc1\n" \
"v_readlane_b32 s43, v202, 11\n" \
"s_nop 4\n" \
"buffer_load_dwordx4 a[88:91], v201, s[8:11], s43 offen sc1\n" \
"buffer_load_dwordx4 a[92:95], v201, s[8:11], s43 offen offset:1024 sc1\n" \
"v_readlane_b32 s43, v202, 18\n" \
"s_nop 4\n" \
"buffer_load_dwordx4 a[144:147], v201, s[8:11], s43 offen sc1\n" \
"buffer_load_dwordx4 a[148:151], v201, s[8:11], s43 offen offset:1024 sc1\n" \
"v_readlane_b32 s43, v202, 25\n" \
"s_nop 4\n" \
"buffer_load_dwordx4 a[200:203], v201, s[8:11], s43 offen sc1\n" \
"buffer_load_dwordx4 a[204:207], v201, s[8:11], s43 offen offset:1024 sc1\n" \
"v_readlane_b32 s43, v202, 32\n" \
"s_nop 4\n" \
"buffer_load_dwordx4 v[104:107], v201, s[8:11], s43 offen sc1\n" \
"buffer_load_dwordx4 v[108:111], v201, s[8:11], s43 offen offset:1024 sc1\n" \
"v_readlane_b32 s43, v202, 39\n" \
"s_nop 4\n" \
"buffer_load_dwordx4 v[160:163], v201, s[8:11], s43 offen sc1\n" \
"buffer_load_dwordx4 v[164:167], v201, s[8:11], s43 offen offset:1024 sc1\n" \
"s_add_u32 s43, s30, 3\n" \
"s_mov_b32 s50, 0\n" \
"s_branch .LX2_w76c\n" \
".LX2_w76l:\n" \
"buffer_load_dword v240, v204, s[4:7], 0 offen sc1\n" \
"s_waitcnt vmcnt(0)\n" \
".LX2_w76c:\n" \
"v_subrev_u32 v241, s43, v240\n" \
"v_cmp_gt_u32 vcc, 0x100, v241\n" \
"s_cmp_eq_u64 vcc, -1\n" \
"s_cbranch_scc1 .LX2_w76d\n" \
"s_sleep 2\n" \
"s_add_u32 s50, s50, 1\n" \
"s_cmp_lt_u32 s50, 0x20000\n" \
"s_cbranch_scc1 .LX2_w76l\n" \
"s_branch .LX2_end\n" \
".LX2_w76d:\n" \
"v_readlane_b32 s43, v202, 1\n" \
"s_nop 4\n" \
"buffer_load_dwordx4 a[8:11], v201, s[8:11], s43 offen sc1\n" \
"buffer_load_dwordx4 a[12:15], v201, s[8:11], s43 offen offset:1024 sc1\n" \
"v_readlane_b32 s43, v202, 8\n" \
"s_nop 4\n" \
"buffer_load_dwordx4 a[64:67], v201, s[8:11], s43 offen sc1\n" \
"buffer_load_dwordx4 a[68:71], v201, s[8:11], s43 offen offset:1024 sc1\n" \
"v_readlane_b32 s43, v202, 15\n" \
"s_nop 4\n" \
"buffer_load_dwordx4 a[120:123], v201, s[8:11], s43 offen sc1\n" \
"buffer_load_dwordx4 a[124:127], v201, s[8:11], s43 offen offset:1024 sc1\n" \
"v_readlane_b32 s43, v202, 22\n" \
"s_nop 4\n" \
"buffer_load_dwordx4 a[176:179], v201, s[8:11], s43 offen sc1\n" \
"buffer_load_dwordx4 a[180:183], v201, s[8:11], s43 offen offset:1024 sc1\n" \
"v_readlane_b32 s43, v202, 29\n" \
"s_nop 4\n" \
"buffer_load_dwordx4 a[232:235], v201, s[8:11], s43 offen sc1\n" \
"buffer_load_dwordx4 a[236:239], v201, s[8:11], s43 offen offset:1024 sc1\n" \
"v_readlane_b32 s43, v202, 36\n" \
"s_nop 4\n" \
"buffer_load_dwordx4 v[136:139], v201, s[8:11], s43 offen sc1\n" \
"buffer_load_dwordx4 v[140:143], v201, s[8:11], s43 offen offset:1024 sc1\n" \
"s_add_u32 s43, s30, 4\n" \
"s_mov_b32 s50, 0\n" \
"s_branch .LX2_w77c\n" \
".LX2_w77l:\n" \
"buffer_load_dword v240, v204, s[4:7], 0 offen sc1\n" \
"s_waitcnt vmcnt(0)\n" \
".LX2_w77c:\n" \
"v_subrev_u32 v241, s43, v240\n" \
"v_cmp_gt_u32 vcc, 0x100, v241\n" \
"s_cmp_eq_u64 vcc, -1\n" \
"s_cbranch_scc1 .LX2_w77d\n" \
"s_sleep 2\n" \
"s_add_u32 s50, s50, 1\n" \
"s_cmp_lt_u32 s50, 0x20000\n" \
"s_cbranch_scc1 .LX2_w77l\n" \
"s_branch .LX2_end\n" \
".LX2_w77d:\n" \
"v_readlane_b32 s43, v202, 5\n" \
"s_nop 4\n" \
"buffer_load_dwordx4 a[40:43], v201, s[8:11], s43 offen sc1\n" \
"buffer_load_dwordx4 a[44:47], v201, s[8:11], s43 offen offset:1024 sc1\n" \
"v_readlane_b32 s43, v202, 12\n" \
"s_nop 4\n" \
"buffer_load_dwordx4 a[96:99], v201, s[8:11], s43 offen sc1\n" \
"buffer_load_dwordx4 a[100:103], v201, s[8:11], s43 offen offset:1024 sc1\n" \
"v_readlane_b32 s43, v202, 19\n" \
"s_nop 4\n" \
"buffer_load_dwordx4 a[152:155], v201, s[8:11], s43 offen sc1\n" \
"buffer_load_dwordx4 a[156:159], v201, s[8:11], s43 offen offset:1024 sc1\n" \
"v_readlane_b32 s43, v202, 26\n" \
"s_nop 4\n" \
"buffer_load_dwordx4 a[208:211], v201, s[8:11], s43 offen sc1\n" \
"buffer_load_dwordx4 a[212:215], v201, s[8:11], s43 offen offset:1024 sc1\n" \
"v_readlane_b32 s43, v202, 33\n" \
"s_nop 4\n" \
"buffer_load_dwordx4 v[112:115], v201, s[8:11], s43 offen sc1\n" \
"buffer_load_dwordx4 v[116:119], v201, s[8:11], s43 offen offset:1024 sc1\n" \
"v_readlane_b32 s43, v202, 40\n" \
"s_nop 4\n" \
"buffer_load_dwordx4 v[168:171], v201, s[8:11], s43 offen sc1\n" \
"buffer_load_dwordx4 v[172:175], v201, s[8:11], s43 offen offset:1024 sc1\n" \
"s_add_u32 s43, s30, 5\n" \
"s_mov_b32 s50, 0\n" \
"s_branch .LX2_w78c\n" \
".LX2_w78l:\n" \
"buffer_load_dword v240, v204, s[4:7], 0 offen sc1\n" \
"s_waitcnt vmcnt(0)\n" \
".LX2_w78c:\n" \
"v_subrev_u32 v241, s43, v240\n" \
"v_cmp_gt_u32 vcc, 0x100, v241\n" \
"s_cmp_eq_u64 vcc, -1\n" \
"s_cbranch_scc1 .LX2_w78d\n" \
"s_sleep 2\n" \
"s_add_u32 s50, s50, 1\n" \
"s_cmp_lt_u32 s50, 0x20000\n" \
"s_cbranch_scc1 .LX2_w78l\n" \
"s_branch .LX2_end\n" \
".LX2_w78d:\n" \
"v_readlane_b32 s43, v202, 2\n" \
"s_nop 4\n" \
"buffer_load_dwordx4 a[16:19], v201, s[8:11], s43 offen sc1\n" \
"buffer_load_dwordx4 a[20:23], v201, s[8:11], s43 offen offset:1024 sc1\n" \
"v_readlane_b32 s43, v202, 9\n" \
"s_nop 4\n" \
"buffer_load_dwordx4 a[72:75], v201, s[8:11], s43 offen sc1\n" \
"buffer_load_dwordx4 a[76:79], v201, s[8:11], s43 offen offset:1024 sc1\n" \
"v_readlane_b32 s43, v202, 16\n" \
"s_nop 4\n" \
"buffer_load_dwordx4 a[128:131], v201, s[8:11], s43 offen sc1\n" \
"buffer_load_dwordx4 a[132:135], v201, s[8:11], s43 offen offset:1024 sc1\n" \
"v_readlane_b32 s43, v202, 23\n" \
"s_nop 4\n" \
"buffer_load_dwordx4 a[184:187], v201, s[8:11], s43 offen sc1\n" \
"buffer_load_dwordx4 a[188:191], v201, s[8:11], s43 offen offset:1024 sc1\n" \
"v_readlane_b32 s43, v202, 30\n" \
"s_nop 4\n" \
"buffer_load_dwordx4 a[240:243], v201, s[8:11], s43 offen sc1\n" \
"buffer_load_dwordx4 a[244:247], v201, s[8:11], s43 offen offset:1024 sc1\n" \
"v_readlane_b32 s43, v202, 37\n" \
"s_nop 4\n" \
"buffer_load_dwordx4 v[144:147], v201, s[8:11], s43 offen sc1\n" \
"buffer_load_dwordx4 v[148:151], v201, s[8:11], s43 offen offset:1024 sc1\n" \
"s_add_u32 s43, s30, 6\n" \
"s_mov_b32 s50, 0\n" \
"s_branch .LX2_w79c\n" \
".LX2_w79l:\n" \
"buffer_load_dword v240, v204, s[4:7], 0 offen sc1\n" \
"s_waitcnt vmcnt(0)\n" \
".LX2_w79c:\n" \
"v_subrev_u32 v241, s43, v240\n" \
"v_cmp_gt_u32 vcc, 0x100, v241\n" \
"s_cmp_eq_u64 vcc, -1\n" \
"s_cbranch_scc1 .LX2_w79d\n" \
"s_sleep 2\n" \
"s_add_u32 s50, s50, 1\n" \
"s_cmp_lt_u32 s50, 0x20000\n" \
"s_cbranch_scc1 .LX2_w79l\n" \
"s_branch .LX2_end\n" \
".LX2_w79d:\n" \
"v_readlane_b32 s43, v202, 6\n" \
"s_nop 4\n" \
"buffer_load_dwordx4 a[48:51], v201, s[8:11], s43 offen sc1\n" \
"buffer_load_dwordx4 a[52:55], v201, s[8:11], s43 offen offset:1024 sc1\n" \
"v_readlane_b32 s43, v202, 13\n" \
"s_nop 4\n" \
"buffer_load_dwordx4 a[104:107], v201, s[8:11], s43 offen sc1\n" \
"buffer_load_dwordx4 a[108:111], v201, s[8:11], s43 offen offset:1024 sc1\n" \
"v_readlane_b32 s43, v202, 20\n" \
"s_nop 4\n" \
"buffer_load_dwordx4 a[160:163], v201, s[8:11], s43 offen sc1\n" \
"buffer_load_dwordx4 a[164:167], v201, s[8:11], s43 offen offset:1024 sc1\n" \
"v_readlane_b32 s43, v202, 27\n" \
"s_nop 4\n" \
"buffer_load_dwordx4 a[216:219], v201, s[8:11], s43 offen sc1\n" \
"buffer_load_dwordx4 a[220:223], v201, s[8:11], s43 offen offset:1024 sc1\n" \
"v_readlane_b32 s43, v202, 34\n" \
"s_nop 4\n" \
"buffer_load_dwordx4 v[120:123], v201, s[8:11], s43 offen sc1\n" \
"buffer_load_dwordx4 v[124:127], v201, s[8:11], s43 offen offset:1024 sc1\n" \
"v_readlane_b32 s43, v202, 41\n" \
"s_nop 4\n" \
"buffer_load_dwordx4 v[176:179], v201, s[8:11], s43 offen sc1\n" \
"buffer_load_dwordx4 v[180:183], v201, s[8:11], s43 offen offset:1024 sc1\n" \
"s_add_u32 s43, s30, 7\n" \
"s_mov_b32 s50, 0\n" \
"s_branch .LX2_w80c\n" \
".LX2_w80l:\n" \
"buffer_load_dword v240, v204, s[4:7], 0 offen sc1\n" \
"s_waitcnt vmcnt(0)\n" \
".LX2_w80c:\n" \
"v_subrev_u32 v241, s43, v240\n" \
"v_cmp_gt_u32 vcc, 0x100, v241\n" \
"s_cmp_eq_u64 vcc, -1\n" \
"s_cbranch_scc1 .LX2_w80d\n" \
"s_sleep 2\n" \
"s_add_u32 s50, s50, 1\n" \
"s_cmp_lt_u32 s50, 0x20000\n" \
"s_cbranch_scc1 .LX2_w80l\n" \
"s_branch .LX2_end\n" \
".LX2_w80d:\n" \
"v_readlane_b32 s43, v202, 3\n" \
"s_nop 4\n" \
"buffer_load_dwordx4 a[24:27], v201, s[8:11], s43 offen sc1\n" \
"buffer_load_dwordx4 a[28:31], v201, s[8:11], s43 offen offset:1024 sc1\n" \
"v_readlane_b32 s43, v202, 10\n" \
"s_nop 4\n" \
"buffer_load_dwordx4 a[80:83], v201, s[8:11], s43 offen sc1\n" \
"buffer_load_dwordx4 a[84:87], v201, s[8:11], s43 offen offset:1024 sc1\n" \
"v_readlane_b32 s43, v202, 17\n" \
"s_nop 4\n" \
"buffer_load_dwordx4 a[136:139], v201, s[8:11], s43 offen sc1\n" \
"buffer_load_dwordx4 a[140:143], v201, s[8:11], s43 offen offset:1024 sc1\n" \
"v_readlane_b32 s43, v202, 24\n" \
"s_nop 4\n" \
"buffer_load_dwordx4 a[192:195], v201, s[8:11], s43 offen sc1\n" \
"buffer_load_dwordx4 a[196:199], v201, s[8:11], s43 offen offset:1024 sc1\n" \
"v_readlane_b32 s43, v202, 31\n" \
"s_nop 4\n" \
"buffer_load_dwordx4 a[248:251], v201, s[8:11], s43 offen sc1\n" \
"buffer_load_dwordx4 a[252:255], v201, s[8:11], s43 offen offset:1024 sc1\n" \
"v_readlane_b32 s43, v202, 38\n" \
"s_nop 4\n" \
"buffer_load_dwordx4 v[152:155], v201, s[8:11], s43 offen sc1\n" \
"buffer_load_dwordx4 v[156:159], v201, s[8:11], s43 offen offset:1024 sc1\n" \
"s_add_u32 s43, s30, 1\n" \
"s_mov_b32 s50, 0\n" \
".LX2_w81l:\n" \
"buffer_load_dword v240, v205, s[4:7], 0 offen sc1\n" \
"s_waitcnt vmcnt(0)\n" \
".LX2_w81c:\n" \
"v_subrev_u32 v241, s43, v240\n" \
"v_cmp_gt_u32 vcc, 0x100, v241\n" \
"s_cmp_eq_u64 vcc, -1\n" \
"s_cbranch_scc1 .LX2_w81d\n" \
"s_sleep 2\n" \
"s_add_u32 s50, s50, 1\n" \
"s_cmp_lt_u32 s50, 0x20000\n" \
"s_cbranch_scc1 .LX2_w81l\n" \
"s_branch .LX2_end\n" \
".LX2_w81d:\n" \
"v_readfirstlane_b32 s31, v241\n" \
"s_lshr_b32 s32, s31, 2\n" \
"s_xor_b32 s29, s31, s32\n" \
"s_and_b32 s29, s29, 3\n" \
"s_lshl_b32 s29, s29, 13\n" \
"s_bitset1_b32 s29, 21\n" \
"s_and_b32 s31, s31, 3\n" \
"s_lshl_b32 s31, s31, 13\n" \
"s_bitcmp1_b32 s39, 0\n" \
"s_cselect_b32 s43, s29, 0\n" \
"s_xor_b32 s31, s31, s43\n" \
"s_xor_b32 s32, s31, 0x4000\n" \
"s_mov_b32 s39, 0\n" \
"s_add_u32 s43, s30, 8\n" \
"s_mov_b32 s50, 0\n" \
".LX2_w82l:\n" \
"buffer_load_dword v240, v215, s[4:7], 0 offen sc1\n" \
"s_waitcnt vmcnt(0)\n" \
".LX2_w82c:\n" \
"v_subrev_u32 v241, s43, v240\n" \
"v_cmp_gt_u32 vcc, 0x100, v241\n" \
"s_cmp_eq_u64 vcc, -1\n" \
"s_cbranch_scc1 .LX2_w82d\n" \
"s_sleep 2\n" \
"s_add_u32 s50, s50, 1\n" \
"s_cmp_lt_u32 s50, 0x20000\n" \
"s_cbranch_scc1 .LX2_w82l\n" \
"s_branch .LX2_end\n" \
".LX2_w82d:\n" \
"s_add_u32 s50, s40, 0\n" \
"s_mul_i32 s50, s50, 28672\n" \
"s_add_u32 s50, s50, s34\n" \
"buffer_load_dwordx4 v[28:31], v201, s[12:15], s50 offen sc1\n" \
"buffer_load_dwordx4 v[32:35], v201, s[12:15], s50 offen offset:1024 sc1\n" \
"buffer_load_dwordx4 v[36:39], v201, s[12:15], s50 offen offset:2048 sc1\n" \
"buffer_load_dwordx4 v[40:43], v201, s[12:15], s50 offen offset:3072 sc1\n" \
"s_waitcnt vmcnt(0)\n" \
"s_barrier\n" \
"s_add_u32 s43, s30, 9\n" \
"s_mov_b32 s50, 0\n" \
".LX2_w83l:\n" \
"buffer_load_dword v240, v215, s[4:7], 0 offen sc1\n" \
"s_waitcnt vmcnt(0)\n" \
".LX2_w83c:\n" \
"v_subrev_u32 v241, s43, v240\n" \
"v_cmp_gt_u32 vcc, 0x100, v241\n" \
"s_cmp_eq_u64 vcc, -1\n" \
"s_cbranch_scc1 .LX2_w83d\n" \
"s_sleep 2\n" \
"s_add_u32 s50, s50, 1\n" \
"s_cmp_lt_u32 s50, 0x20000\n" \
"s_cbranch_scc1 .LX2_w83l\n" \
"s_branch .LX2_end\n" \
".LX2_w83d:\n" \
".LX2_pre:\n" \
".LX2_loop:\n" \
"s_add_u32 s43, s40, 2\n" \
"s_and_b32 s43, s43, 3\n" \
"s_bitcmp1_b32 s43, 0\n" \
"s_cselect_b32 s49, s32, s31\n" \
"s_lshl_b32 s43, s43, 19\n" \
"s_add_u32 s49, s49, s43\n" \
"ds_read_b128 v[0:3], v216 offset:0\n" \
"ds_read_b128 v[4:7], v216 offset:1024\n" \
"ds_read_b128 v[8:11], v216 offset:2048\n" \
"ds_read_b128 v[12:15], v216 offset:3072\n" \
"ds_read_b128 v[16:19], v216 offset:4096\n" \
"ds_read_b128 v[20:23], v216 offset:5120\n" \
"ds_read_b128 v[24:27], v216 offset:6144\n" \
"v_mov_b32 v60, 0\n" \
"v_mov_b32 v61, 0\n" \
"v_mov_b32 v62, 0\n" \
"v_mov_b32 v63, 0\n" \
"v_mov_b32 v64, 0\n" \
"v_mov_b32 v65, 0\n" \
"v_mov_b32 v66, 0\n" \
"v_mov_b32 v67, 0\n" \
"v_mov_b32 v68, 0\n" \
"v_mov_b32 v69, 0\n" \
"v_mov_b32 v70, 0\n" \
"v_mov_b32 v71, 0\n" \
"v_mov_b32 v72, 0\n" \
"v_mov_b32 v73, 0\n" \
"v_mov_b32 v74, 0\n" \
"v_mov_b32 v75, 0\n" \
"v_mov_b32 v76, 0\n" \
"v_mov_b32 v77, 0\n" \
"v_mov_b32 v78, 0\n" \
"v_mov_b32 v79, 0\n" \
"v_mov_b32 v80, 0\n" \
"v_mov_b32 v81, 0\n" \
"v_mov_b32 v82, 0\n" \
"v_mov_b32 v83, 0\n" \
"s_nop 1\n" \
"s_waitcnt lgkmcnt(6)\n" \
"v_smfmac_f32_16x16x64_bf16 v[60:63], v[0:3], a[0:7], v200\n" \
"v_smfmac_f32_16x16x64_bf16 v[64:67], v[0:3], a[56:63], v200\n" \
"v_smfmac_f32_16x16x64_bf16 v[68:71], v[0:3], a[112:119], v200\n" \
"v_smfmac_f32_16x16x64_bf16 v[72:75], v[0:3], a[168:175], v200\n" \
"v_smfmac_f32_16x16x64_bf16 v[76:79], v[0:3], a[224:231], v200\n" \
"v_smfmac_f32_16x16x64_bf16 v[80:83], v[0:3], v[128:135], v200\n" \
"s_waitcnt lgkmcnt(5)\n" \
"v_smfmac_f32_16x16x64_bf16 v[60:63], v[4:7], a[8:15], v200\n" \
"v_smfmac_f32_16x16x64_bf16 v[64:67], v[4:7], a[64:71], v200\n" \
"v_smfmac_f32_16x16x64_bf16 v[68:71], v[4:7], a[120:127], v200\n" \
"v_smfmac_f32_16x16x64_bf16 v[72:75], v[4:7], a[176:183], v200\n" \
"v_smfmac_f32_16x16x64_bf16 v[76:79], v[4:7], a[232:239], v200\n" \
"v_smfmac_f32_16x16x64_bf16 v[80:83], v[4:7], v[136:143], v200\n" \
"s_waitcnt lgkmcnt(4)\n" \
"v_smfmac_f32_16x16x64_bf16 v[60:63], v[8:11], a[16:23], v200\n" \
"v_smfmac_f32_16x16x64_bf16 v[64:67], v[8:11], a[72:79], v200\n" \
"v_smfmac_f32_16x16x64_bf16 v[68:71], v[8:11], a[128:135], v200\n" \
"v_smfmac_f32_16x16x64_bf16 v[72:75], v[8:11], a[184:191], v200\n" \
"v_smfmac_f32_16x16x64_bf16 v[76:79], v[8:11], a[240:247], v200\n" \
"v_smfmac_f32_16x16x64_bf16 v[80:83], v[8:11], v[144:151], v200\n" \
"s_waitcnt lgkmcnt(3)\n" \
"v_smfmac_f32_16x16x64_bf16 v[60:63], v[12:15], a[24:31], v200\n" \
"v_smfmac_f32_16x16x64_bf16 v[64:67], v[12:15], a[80:87], v200\n" \
"v_smfmac_f32_16x16x64_bf16 v[68:71], v[12:15], a[136:143], v200\n" \
"v_smfmac_f32_16x16x64_bf16 v[72:75], v[12:15], a[192:199], v200\n" \
"v_smfmac_f32_16x16x64_bf16 v[76:79], v[12:15], a[248:255], v200\n" \
"v_smfmac_f32_16x16x64_bf16 v[80:83], v[12:15], v[152:159], v200\n" \
"s_waitcnt lgkmcnt(2)\n" \
"v_smfmac_f32_16x16x64_bf16 v[60:63], v[16:19], a[32:39], v200\n" \
"v_smfmac_f32_16x16x64_bf16 v[64:67], v[16:19], a[88:95], v200\n" \
"v_smfmac_f32_16x16x64_bf16 v[68:71], v[16:19], a[144:151], v200\n" \
"v_smfmac_f32_16x16x64_bf16 v[72:75], v[16:19], a[200:207], v200\n" \
"s_barrier\n" \
"s_add_u32 s50, s40, 1\n" \
"s_mul_i32 s50, s50, 28672\n" \
"s_add_u32 s50, s50, s34\n" \
"buffer_load_dwordx4 v[44:47], v201, s[12:15], s50 offen sc1\n" \
"buffer_load_dwordx4 v[48:51], v201, s[12:15], s50 offen offset:1024 sc1\n" \
"buffer_load_dwordx4 v[52:55], v201, s[12:15], s50 offen offset:2048 sc1\n" \
"buffer_load_dwordx4 v[56:59], v201, s[12:15], s50 offen offset:3072 sc1\n" \
"buffer_load_dword v240, v215, s[4:7], 0 offen sc1\n" \
"v_smfmac_f32_16x16x64_bf16 v[76:79], v[16:19], v[104:111], v200\n" \
"v_smfmac_f32_16x16x64_bf16 v[80:83], v[16:19], v[160:167], v200\n" \
"s_waitcnt lgkmcnt(1)\n" \
"v_smfmac_f32_16x16x64_bf16 v[60:63], v[20:23], a[40:47], v200\n" \
"v_smfmac_f32_16x16x64_bf16 v[64:67], v[20:23], a[96:103], v200\n" \
"v_smfmac_f32_16x16x64_bf16 v[68:71], v[20:23], a[152:159], v200\n" \
"v_smfmac_f32_16x16x64_bf16 v[72:75], v[20:23], a[208:215], v200\n" \
"v_smfmac_f32_16x16x64_bf16 v[76:79], v[20:23], v[112:119], v200\n" \
"v_smfmac_f32_16x16x64_bf16 v[80:83], v[20:23], v[168:175], v200\n" \
"s_waitcnt lgkmcnt(0)\n" \
"v_smfmac_f32_16x16x64_bf16 v[60:63], v[24:27], a[48:55], v200\n" \
"v_smfmac_f32_16x16x64_bf16 v[64:67], v[24:27], a[104:111], v200\n" \
"v_smfmac_f32_16x16x64_bf16 v[68:71], v[24:27], a[160:167], v200\n" \
"v_smfmac_f32_16x16x64_bf16 v[72:75], v[24:27], a[216:223], v200\n" \
"v_smfmac_f32_16x16x64_bf16 v[76:79], v[24:27], v[120:127], v200\n" \
"v_smfmac_f32_16x16x64_bf16 v[80:83], v[24:27], v[176:183], v200\n" \
"s_nop 7\n" \
"v_permlane32_swap_b32 v60, v64\n" \
"v_permlane32_swap_b32 v61, v65\n" \
"v_permlane32_swap_b32 v62, v66\n" \
"v_permlane32_swap_b32 v63, v67\n" \
"v_add_f32 v60, v60, v64\n" \
"v_add_f32 v61, v61, v65\n" \
"v_add_f32 v62, v62, v66\n" \
"v_add_f32 v63, v63, v67\n" \
"v_cvt_pk_bf16_f32 v184, v60, v61\n" \
"v_cvt_pk_bf16_f32 v185, v62, v63\n" \
"ds_write_b64 v217, v[184:185] offset:0\n" \
"v_permlane32_swap_b32 v68, v72\n" \
"v_permlane32_swap_b32 v69, v73\n" \
"v_permlane32_swap_b32 v70, v74\n" \
"v_permlane32_swap_b32 v71, v75\n" \
"v_add_f32 v68, v68, v72\n" \
"v_add_f32 v69, v69, v73\n" \
"v_add_f32 v70, v70, v74\n" \
"v_add_f32 v71, v71, v75\n" \
"v_cvt_pk_bf16_f32 v186, v68, v69\n" \
"v_cvt_pk_bf16_f32 v187, v70, v71\n" \
"ds_write_b64 v217, v[186:187] offset:512\n" \
"v_permlane32_swap_b32 v76, v80\n" \
"v_permlane32_swap_b32 v77, v81\n" \
"v_permlane32_swap_b32 v78, v82\n" \
"v_permlane32_swap_b32 v79, v83\n" \
"v_add_f32 v76, v76, v80\n" \
"v_add_f32 v77, v77, v81\n" \
"v_add_f32 v78, v78, v82\n" \
"v_add_f32 v79, v79, v83\n" \
"v_cvt_pk_bf16_f32 v188, v76, v77\n" \
"v_cvt_pk_bf16_f32 v189, v78, v79\n" \
"ds_write_b64 v217, v[188:189] offset:1024\n" \
"ds_read_b64_tr_b16 v[0:1], v218 offset:0\n" \
"ds_read_b64_tr_b16 v[2:3], v218 offset:64\n" \
"ds_read_b64_tr_b16 v[4:5], v218 offset:1024\n" \
"ds_read_b64_tr_b16 v[6:7], v218 offset:1088\n" \
"s_add_u32 s43, s40, 0\n" \
"s_mul_i32 s43, s43, 7168\n" \
"s_waitcnt lgkmcnt(0)\n" \
"buffer_store_dwordx4 v[0:3], v219, s[16:19], s43 offen sc1\n" \
"buffer_store_dwordx4 v[4:7], v221, s[16:19], s43 offen offset:1024 sc1\n" \
"s_waitcnt vmcnt(7)\n" \
"s_barrier\n" \
"ds_read_b64_tr_b16 v[92:93], v213\n" \
"ds_read_b64_tr_b16 v[94:95], v213 offset:64\n" \
"ds_read_b32 v84, v252 offset:16\n" \
"v_mov_b32 v245, v245\n" \
"s_nop 2\n" \
"v_mov_b32 v245, v245\n" \
"s_nop 2\n" \
"v_mov_b32 v245, v245\n" \
"s_waitcnt lgkmcnt(1)\n" \
"v_mfma_f32_16x16x32_bf16 v[68:71], v[92:95], v[28:31], 0\n" \
"s_waitcnt lgkmcnt(0)\n" \
"v_xor_b32 v85, s29, v84\n" \
"v_mfma_f32_16x16x32_bf16 v[76:79], v[92:95], v[36:39], 0\n" \
"v_add_u32 v84, v84, v210\n" \
"v_add_u32 v85, v85, v210\n" \
"v_mfma_f32_16x16x32_bf16 v[72:75], v[92:95], v[32:35], 0\n" \
"s_nop 1\n" \
"v_mfma_f32_16x16x32_bf16 v[80:83], v[92:95], v[40:43], 0\n" \
"v_cvt_pk_bf16_f32 v96, v68, v69\n" \
"v_cvt_pk_bf16_f32 v97, v70, v71\n" \
"v_cvt_pk_bf16_f32 v98, v76, v77\n" \
"v_cvt_pk_bf16_f32 v99, v78, v79\n" \
"s_mov_b64 exec, s[2:3]\n" \
"v_cvt_pk_bf16_f32 v96, v72, v73\n" \
"v_cvt_pk_bf16_f32 v97, v74, v75\n" \
"s_nop 0\n" \
"v_cvt_pk_bf16_f32 v98, v80, v81\n" \
"v_cvt_pk_bf16_f32 v99, v82, v83\n" \
"s_mov_b64 exec, -1\n" \
"s_nop 1\n" \
"v_permlane16_swap_b32 v96, v98\n" \
"v_permlane16_swap_b32 v97, v99\n" \
"buffer_store_dwordx4 v[96:99], v84, s[4:7], 0 offen sc1\n" \
"s_barrier\n" \
"buffer_store_dwordx4 v[96:99], v85, s[4:7], 0 offen sc1\n" \
"buffer_store_dwordx4 v[100:103], v214, s[4:7], s49 offen sc1\n" \
"s_xor_b32 s43, s49, s29\n" \
"buffer_store_dwordx4 v[100:103], v214, s[4:7], s43 offen sc1\n" \
"s_waitcnt vmcnt(4)\n" \
"v_mov_b32 v28, v44\n" \
"v_mov_b32 v29, v45\n" \
"v_mov_b32 v30, v46\n" \
"v_mov_b32 v31, v47\n" \
"v_mov_b32 v32, v48\n" \
"v_mov_b32 v33, v49\n" \
"v_mov_b32 v34, v50\n" \
"v_mov_b32 v35, v51\n" \
"v_mov_b32 v36, v52\n" \
"v_mov_b32 v37, v53\n" \
"v_mov_b32 v38, v54\n" \
"v_mov_b32 v39, v55\n" \
"v_mov_b32 v40, v56\n" \
"v_mov_b32 v41, v57\n" \
"v_mov_b32 v42, v58\n" \
"v_mov_b32 v43, v59\n" \
"s_cmp_eq_u32 s40, -1\n" \
"s_cbranch_scc1 .LX2_nogu\n" \
"s_add_u32 s36, s40, 10\n" \
"s_min_u32 s36, s36, 71\n" \
"s_add_u32 s43, s30, s36\n" \
"s_mov_b32 s50, 0\n" \
"s_branch .LX2_w84c\n" \
".LX2_w84l:\n" \
"buffer_load_dword v240, v215, s[4:7], 0 offen sc1\n" \
"s_waitcnt vmcnt(0)\n" \
".LX2_w84c:\n" \
"v_subrev_u32 v241, s43, v240\n" \
"v_cmp_gt_u32 vcc, 0x100, v241\n" \
"s_cmp_eq_u64 vcc, -1\n" \
"s_cbranch_scc1 .LX2_w84d\n" \
"s_sleep 2\n" \
"s_add_u32 s50, s50, 1\n" \
"s_cmp_lt_u32 s50, 0x20000\n" \
"s_cbranch_scc1 .LX2_w84l\n" \
"s_branch .LX2_end\n" \
".LX2_w84d:\n" \
".LX2_nogu:\n" \
"s_add_u32 s40, s40, 1\n" \
"s_cmp_eq_u32 s40, 0\n" \
"s_cbranch_scc1 .LX2_wet\n" \
"s_cmp_lt_u32 s40, 63\n" \
"s_cbranch_scc1 .LX2_loop\n" \
"ds_read_b128 v[0:3], v216 offset:0\n" \
"ds_read_b128 v[4:7], v216 offset:1024\n" \
"ds_read_b128 v[8:11], v216 offset:2048\n" \
"ds_read_b128 v[12:15], v216 offset:3072\n" \
"ds_read_b128 v[16:19], v216 offset:4096\n" \
"ds_read_b128 v[20:23], v216 offset:5120\n" \
"ds_read_b128 v[24:27], v216 offset:6144\n" \
"v_mov_b32 v60, 0\n" \
"v_mov_b32 v61, 0\n" \
"v_mov_b32 v62, 0\n" \
"v_mov_b32 v63, 0\n" \
"v_mov_b32 v64, 0\n" \
"v_mov_b32 v65, 0\n" \
"v_mov_b32 v66, 0\n" \
"v_mov_b32 v67, 0\n" \
"v_mov_b32 v68, 0\n" \
"v_mov_b32 v69, 0\n" \
"v_mov_b32 v70, 0\n" \
"v_mov_b32 v71, 0\n" \
"v_mov_b32 v72, 0\n" \
"v_mov_b32 v73, 0\n" \
"v_mov_b32 v74, 0\n" \
"v_mov_b32 v75, 0\n" \
"v_mov_b32 v76, 0\n" \
"v_mov_b32 v77, 0\n" \
"v_mov_b32 v78, 0\n" \
"v_mov_b32 v79, 0\n" \
"v_mov_b32 v80, 0\n" \
"v_mov_b32 v81, 0\n" \
"v_mov_b32 v82, 0\n" \
"v_mov_b32 v83, 0\n" \
"s_nop 1\n" \
"s_waitcnt lgkmcnt(6)\n" \
"v_smfmac_f32_16x16x64_bf16 v[60:63], v[0:3], a[0:7], v200\n" \
"v_smfmac_f32_16x16x64_bf16 v[64:67], v[0:3], a[56:63], v200\n" \
"v_smfmac_f32_16x16x64_bf16 v[68:71], v[0:3], a[112:119], v200\n" \
"v_smfmac_f32_16x16x64_bf16 v[72:75], v[0:3], a[168:175], v200\n" \
"v_smfmac_f32_16x16x64_bf16 v[76:79], v[0:3], a[224:231], v200\n" \
"v_smfmac_f32_16x16x64_bf16 v[80:83], v[0:3], v[128:135], v200\n" \
"s_waitcnt lgkmcnt(5)\n" \
"v_smfmac_f32_16x16x64_bf16 v[60:63], v[4:7], a[8:15], v200\n" \
"v_smfmac_f32_16x16x64_bf16 v[64:67], v[4:7], a[64:71], v200\n" \
"v_smfmac_f32_16x16x64_bf16 v[68:71], v[4:7], a[120:127], v200\n" \
"v_smfmac_f32_16x16x64_bf16 v[72:75], v[4:7], a[176:183], v200\n" \
"v_smfmac_f32_16x16x64_bf16 v[76:79], v[4:7], a[232:239], v200\n" \
"v_smfmac_f32_16x16x64_bf16 v[80:83], v[4:7], v[136:143], v200\n" \
"s_waitcnt lgkmcnt(4)\n" \
"v_smfmac_f32_16x16x64_bf16 v[60:63], v[8:11], a[16:23], v200\n" \
"v_smfmac_f32_16x16x64_bf16 v[64:67], v[8:11], a[72:79], v200\n" \
"v_smfmac_f32_16x16x64_bf16 v[68:71], v[8:11], a[128:135], v200\n" \
"v_smfmac_f32_16x16x64_bf16 v[72:75], v[8:11], a[184:191], v200\n" \
"v_smfmac_f32_16x16x64_bf16 v[76:79], v[8:11], a[240:247], v200\n" \
"v_smfmac_f32_16x16x64_bf16 v[80:83], v[8:11], v[144:151], v200\n" \
"s_waitcnt lgkmcnt(3)\n" \
"v_smfmac_f32_16x16x64_bf16 v[60:63], v[12:15], a[24:31], v200\n" \
"v_smfmac_f32_16x16x64_bf16 v[64:67], v[12:15], a[80:87], v200\n" \
"v_smfmac_f32_16x16x64_bf16 v[68:71], v[12:15], a[136:143], v200\n" \
"v_smfmac_f32_16x16x64_bf16 v[72:75], v[12:15], a[192:199], v200\n" \
"v_smfmac_f32_16x16x64_bf16 v[76:79], v[12:15], a[248:255], v200\n" \
"v_smfmac_f32_16x16x64_bf16 v[80:83], v[12:15], v[152:159], v200\n" \
"s_waitcnt lgkmcnt(2)\n" \
"v_smfmac_f32_16x16x64_bf16 v[60:63], v[16:19], a[32:39], v200\n" \
"v_smfmac_f32_16x16x64_bf16 v[64:67], v[16:19], a[88:95], v200\n" \
"v_smfmac_f32_16x16x64_bf16 v[68:71], v[16:19], a[144:151], v200\n" \
"v_smfmac_f32_16x16x64_bf16 v[72:75], v[16:19], a[200:207], v200\n" \
"v_smfmac_f32_16x16x64_bf16 v[76:79], v[16:19], v[104:111], v200\n" \
"v_smfmac_f32_16x16x64_bf16 v[80:83], v[16:19], v[160:167], v200\n" \
"s_waitcnt lgkmcnt(1)\n" \
"v_smfmac_f32_16x16x64_bf16 v[60:63], v[20:23], a[40:47], v200\n" \
"v_smfmac_f32_16x16x64_bf16 v[64:67], v[20:23], a[96:103], v200\n" \
"v_smfmac_f32_16x16x64_bf16 v[68:71], v[20:23], a[152:159], v200\n" \
"v_smfmac_f32_16x16x64_bf16 v[72:75], v[20:23], a[208:215], v200\n" \
"v_smfmac_f32_16x16x64_bf16 v[76:79], v[20:23], v[112:119], v200\n" \
"v_smfmac_f32_16x16x64_bf16 v[80:83], v[20:23], v[168:175], v200\n" \
"s_waitcnt lgkmcnt(0)\n" \
"v_smfmac_f32_16x16x64_bf16 v[60:63], v[24:27], a[48:55], v200\n" \
"v_smfmac_f32_16x16x64_bf16 v[64:67], v[24:27], a[104:111], v200\n" \
"v_smfmac_f32_16x16x64_bf16 v[68:71], v[24:27], a[160:167], v200\n" \
"v_smfmac_f32_16x16x64_bf16 v[72:75], v[24:27], a[216:223], v200\n" \
"v_smfmac_f32_16x16x64_bf16 v[76:79], v[24:27], v[120:127], v200\n" \
"v_smfmac_f32_16x16x64_bf16 v[80:83], v[24:27], v[176:183], v200\n" \
"s_nop 7\n" \
"v_permlane32_swap_b32 v60, v64\n" \
"v_permlane32_swap_b32 v61, v65\n" \
"v_permlane32_swap_b32 v62, v66\n" \
"v_permlane32_swap_b32 v63, v67\n" \
"v_add_f32 v60, v60, v64\n" \
"v_add_f32 v61, v61, v65\n" \
"v_add_f32 v62, v62, v66\n" \
"v_add_f32 v63, v63, v67\n" \
"v_cvt_pk_bf16_f32 v184, v60, v61\n" \
"v_cvt_pk_bf16_f32 v185, v62, v63\n" \
"ds_write_b64 v217, v[184:185] offset:0\n" \
"v_permlane32_swap_b32 v68, v72\n" \
"v_permlane32_swap_b32 v69, v73\n" \
"v_permlane32_swap_b32 v70, v74\n" \
"v_permlane32_swap_b32 v71, v75\n" \
"v_add_f32 v68, v68, v72\n" \
"v_add_f32 v69, v69, v73\n" \
"v_add_f32 v70, v70, v74\n" \
"v_add_f32 v71, v71, v75\n" \
"v_cvt_pk_bf16_f32 v186, v68, v69\n" \
"v_cvt_pk_bf16_f32 v187, v70, v71\n" \
"ds_write_b64 v217, v[186:187] offset:512\n" \
"v_permlane32_swap_b32 v76, v80\n" \
"v_permlane32_swap_b32 v77, v81\n" \
"v_permlane32_swap_b32 v78, v82\n" \
"v_permlane32_swap_b32 v79, v83\n" \
"v_add_f32 v76, v76, v80\n" \
"v_add_f32 v77, v77, v81\n" \
"v_add_f32 v78, v78, v82\n" \
"v_add_f32 v79, v79, v83\n" \
"v_cvt_pk_bf16_f32 v188, v76, v77\n" \
"v_cvt_pk_bf16_f32 v189, v78, v79\n" \
"ds_write_b64 v217, v[188:189] offset:1024\n" \
"ds_read_b64_tr_b16 v[0:1], v218 offset:0\n" \
"ds_read_b64_tr_b16 v[2:3], v218 offset:64\n" \
"ds_read_b64_tr_b16 v[4:5], v218 offset:1024\n" \
"ds_read_b64_tr_b16 v[6:7], v218 offset:1088\n" \
"s_add_u32 s43, s40, 0\n" \
"s_mul_i32 s43, s43, 7168\n" \
"s_waitcnt lgkmcnt(0)\n" \
"buffer_store_dwordx4 v[0:3], v219, s[16:19], s43 offen sc1\n" \
"buffer_store_dwordx4 v[4:7], v221, s[16:19], s43 offen offset:1024 sc1\n" \
"s_waitcnt vmcnt(0)\n" \
"s_barrier\n" \
"ds_read_b128 v[0:3], v216 offset:7168\n" \
"ds_read_b128 v[4:7], v216 offset:8192\n" \
"ds_read_b128 v[8:11], v216 offset:9216\n" \
"ds_read_b128 v[12:15], v216 offset:10240\n" \
"ds_read_b128 v[16:19], v216 offset:11264\n" \
"ds_read_b128 v[20:23], v216 offset:12288\n" \
"ds_read_b128 v[24:27], v216 offset:13312\n" \
"v_mov_b32 v60, 0\n" \
"v_mov_b32 v61, 0\n" \
"v_mov_b32 v62, 0\n" \
"v_mov_b32 v63, 0\n" \
"v_mov_b32 v64, 0\n" \
"v_mov_b32 v65, 0\n" \
"v_mov_b32 v66, 0\n" \
"v_mov_b32 v67, 0\n" \
"v_mov_b32 v68, 0\n" \
"v_mov_b32 v69, 0\n" \
"v_mov_b32 v70, 0\n" \
"v_mov_b32 v71, 0\n" \
"v_mov_b32 v72, 0\n" \
"v_mov_b32 v73, 0\n" \
"v_mov_b32 v74, 0\n" \
"v_mov_b32 v75, 0\n" \
"v_mov_b32 v76, 0\n" \
"v_mov_b32 v77, 0\n" \
"v_mov_b32 v78, 0\n" \
"v_mov_b32 v79, 0\n" \
"v_mov_b32 v80, 0\n" \
"v_mov_b32 v81, 0\n" \
"v_mov_b32 v82, 0\n" \
"v_mov_b32 v83, 0\n" \
"s_nop 1\n" \
"s_waitcnt lgkmcnt(6)\n" \
"v_smfmac_f32_16x16x64_bf16 v[60:63], v[0:3], a[0:7], v200\n" \
"v_smfmac_f32_16x16x64_bf16 v[64:67], v[0:3], a[56:63], v200\n" \
"v_smfmac_f32_16x16x64_bf16 v[68:71], v[0:3], a[112:119], v200\n" \
"v_smfmac_f32_16x16x64_bf16 v[72:75], v[0:3], a[168:175], v200\n" \
"v_smfmac_f32_16x16x64_bf16 v[76:79], v[0:3], a[224:231], v200\n" \
"v_smfmac_f32_16x16x64_bf16 v[80:83], v[0:3], v[128:135], v200\n" \
"s_waitcnt lgkmcnt(5)\n" \
"v_smfmac_f32_16x16x64_bf16 v[60:63], v[4:7], a[8:15], v200\n" \
"v_smfmac_f32_16x16x64_bf16 v[64:67], v[4:7], a[64:71], v200\n" \
"v_smfmac_f32_16x16x64_bf16 v[68:71], v[4:7], a[120:127], v200\n" \
"v_smfmac_f32_16x16x64_bf16 v[72:75], v[4:7], a[176:183], v200\n" \
"v_smfmac_f32_16x16x64_bf16 v[76:79], v[4:7], a[232:239], v200\n" \
"v_smfmac_f32_16x16x64_bf16 v[80:83], v[4:7], v[136:143], v200\n" \
"s_waitcnt lgkmcnt(4)\n" \
"v_smfmac_f32_16x16x64_bf16 v[60:63], v[8:11], a[16:23], v200\n" \
"v_smfmac_f32_16x16x64_bf16 v[64:67], v[8:11], a[72:79], v200\n" \
"v_smfmac_f32_16x16x64_bf16 v[68:71], v[8:11], a[128:135], v200\n" \
"v_smfmac_f32_16x16x64_bf16 v[72:75], v[8:11], a[184:191], v200\n" \
"v_smfmac_f32_16x16x64_bf16 v[76:79], v[8:11], a[240:247], v200\n" \
"v_smfmac_f32_16x16x64_bf16 v[80:83], v[8:11], v[144:151], v200\n" \
"s_waitcnt lgkmcnt(3)\n" \
"v_smfmac_f32_16x16x64_bf16 v[60:63], v[12:15], a[24:31], v200\n" \
"v_smfmac_f32_16x16x64_bf16 v[64:67], v[12:15], a[80:87], v200\n" \
"v_smfmac_f32_16x16x64_bf16 v[68:71], v[12:15], a[136:143], v200\n" \
"v_smfmac_f32_16x16x64_bf16 v[72:75], v[12:15], a[192:199], v200\n" \
"v_smfmac_f32_16x16x64_bf16 v[76:79], v[12:15], a[248:255], v200\n" \
"v_smfmac_f32_16x16x64_bf16 v[80:83], v[12:15], v[152:159], v200\n" \
"s_waitcnt lgkmcnt(2)\n" \
"v_smfmac_f32_16x16x64_bf16 v[60:63], v[16:19], a[32:39], v200\n" \
"v_smfmac_f32_16x16x64_bf16 v[64:67], v[16:19], a[88:95], v200\n" \
"v_smfmac_f32_16x16x64_bf16 v[68:71], v[16:19], a[144:151], v200\n" \
"v_smfmac_f32_16x16x64_bf16 v[72:75], v[16:19], a[200:207], v200\n" \
"v_smfmac_f32_16x16x64_bf16 v[76:79], v[16:19], v[104:111], v200\n" \
"v_smfmac_f32_16x16x64_bf16 v[80:83], v[16:19], v[160:167], v200\n" \
"s_waitcnt lgkmcnt(1)\n" \
"v_smfmac_f32_16x16x64_bf16 v[60:63], v[20:23], a[40:47], v200\n" \
"v_smfmac_f32_16x16x64_bf16 v[64:67], v[20:23], a[96:103], v200\n" \
"v_smfmac_f32_16x16x64_bf16 v[68:71], v[20:23], a[152:159], v200\n" \
"v_smfmac_f32_16x16x64_bf16 v[72:75], v[20:23], a[208:215], v200\n" \
"v_smfmac_f32_16x16x64_bf16 v[76:79], v[20:23], v[112:119], v200\n" \
"v_smfmac_f32_16x16x64_bf16 v[80:83], v[20:23], v[168:175], v200\n" \
"s_waitcnt lgkmcnt(0)\n" \
"v_smfmac_f32_16x16x64_bf16 v[60:63], v[24:27], a[48:55], v200\n" \
"v_smfmac_f32_16x16x64_bf16 v[64:67], v[24:27], a[104:111], v200\n" \
"v_smfmac_f32_16x16x64_bf16 v[68:71], v[24:27], a[160:167], v200\n" \
"v_smfmac_f32_16x16x64_bf16 v[72:75], v[24:27], a[216:223], v200\n" \
"v_smfmac_f32_16x16x64_bf16 v[76:79], v[24:27], v[120:127], v200\n" \
"v_smfmac_f32_16x16x64_bf16 v[80:83], v[24:27], v[176:183], v200\n" \
"s_nop 7\n" \
"v_permlane32_swap_b32 v60, v64\n" \
"v_permlane32_swap_b32 v61, v65\n" \
"v_permlane32_swap_b32 v62, v66\n" \
"v_permlane32_swap_b32 v63, v67\n" \
"v_add_f32 v60, v60, v64\n" \
"v_add_f32 v61, v61, v65\n" \
"v_add_f32 v62, v62, v66\n" \
"v_add_f32 v63, v63, v67\n" \
"v_cvt_pk_bf16_f32 v184, v60, v61\n" \
"v_cvt_pk_bf16_f32 v185, v62, v63\n" \
"ds_write_b64 v217, v[184:185] offset:0\n" \
"v_permlane32_swap_b32 v68, v72\n" \
"v_permlane32_swap_b32 v69, v73\n" \
"v_permlane32_swap_b32 v70, v74\n" \
"v_permlane32_swap_b32 v71, v75\n" \
"v_add_f32 v68, v68, v72\n" \
"v_add_f32 v69, v69, v73\n" \
"v_add_f32 v70, v70, v74\n" \
"v_add_f32 v71, v71, v75\n" \
"v_cvt_pk_bf16_f32 v186, v68, v69\n" \
"v_cvt_pk_bf16_f32 v187, v70, v71\n" \
"ds_write_b64 v217, v[186:187] offset:512\n" \
"v_permlane32_swap_b32 v76, v80\n" \
"v_permlane32_swap_b32 v77, v81\n" \
"v_permlane32_swap_b32 v78, v82\n" \
"v_permlane32_swap_b32 v79, v83\n" \
"v_add_f32 v76, v76, v80\n" \
"v_add_f32 v77, v77, v81\n" \
"v_add_f32 v78, v78, v82\n" \
"v_add_f32 v79, v79, v83\n" \
"v_cvt_pk_bf16_f32 v188, v76, v77\n" \
"v_cvt_pk_bf16_f32 v189, v78, v79\n" \
"ds_write_b64 v217, v[188:189] offset:1024\n" \
"ds_read_b64_tr_b16 v[0:1], v218 offset:0\n" \
"ds_read_b64_tr_b16 v[2:3], v218 offset:64\n" \
"ds_read_b64_tr_b16 v[4:5], v218 offset:1024\n" \
"ds_read_b64_tr_b16 v[6:7], v218 offset:1088\n" \
"s_add_u32 s43, s40, 1\n" \
"s_mul_i32 s43, s43, 7168\n" \
"s_waitcnt lgkmcnt(0)\n" \
"buffer_store_dwordx4 v[0:3], v219, s[16:19], s43 offen sc1\n" \
"buffer_store_dwordx4 v[4:7], v221, s[16:19], s43 offen offset:1024 sc1\n" \
"s_waitcnt vmcnt(0)\n" \
"s_barrier\n" \
".LX2_end:\n" \
"s_endpgm\n" \
".p2align 6\n" \
"fk_role_X3:\n" \
"ds_read_b32 v200, v255 offset:0\n" \
"ds_read_b32 v201, v255 offset:256\n" \
"ds_read_b32 v202, v255 offset:512\n" \
"ds_read_b32 v203, v255 offset:768\n" \
"ds_read_b32 v204, v255 offset:1024\n" \
"ds_read_b32 v205, v255 offset:1280\n" \
"ds_read_b32 v206, v255 offset:1536\n" \
"ds_read_b32 v207, v255 offset:1792\n" \
"ds_read_b32 v208, v255 offset:2048\n" \
"ds_read_b32 v209, v255 offset:2304\n" \
"ds_read_b32 v210, v255 offset:2560\n" \
"ds_read_b32 v211, v255 offset:2816\n" \
"ds_read_b32 v212, v255 offset:3072\n" \
"ds_read_b32 v213, v255 offset:3328\n" \
"ds_read_b32 v214, v255 offset:3584\n" \
"ds_read_b32 v215, v255 offset:3840\n" \
"ds_read_b32 v216, v255 offset:4096\n" \
"ds_read_b32 v217, v255 offset:4352\n" \
"ds_read_b32 v218, v255 offset:4608\n" \
"ds_read_b32 v219, v255 offset:4864\n" \
"ds_read_b32 v220, v255 offset:5120\n" \
"ds_read_b32 v221, v255 offset:5376\n" \
"ds_read_b32 v254, v255 offset:7936\n" \
"s_waitcnt lgkmcnt(0)\n" \
"v_readlane_b32 s4, v254, 0\n" \
"v_readlane_b32 s5, v254, 1\n" \
"v_readlane_b32 s6, v254, 2\n" \
"v_readlane_b32 s7, v254, 3\n" \
"v_readlane_b32 s8, v254, 4\n" \
"v_readlane_b32 s9, v254, 5\n" \
"v_readlane_b32 s10, v254, 6\n" \
"v_readlane_b32 s11, v254, 7\n" \
"v_readlane_b32 s12, v254, 8\n" \
"v_readlane_b32 s13, v254, 9\n" \
"v_readlane_b32 s14, v254, 10\n" \
"v_readlane_b32 s15, v254, 11\n" \
"v_readlane_b32 s16, v254, 12\n" \
"v_readlane_b32 s17, v254, 13\n" \
"v_readlane_b32 s18, v254, 14\n" \
"v_readlane_b32 s19, v254, 15\n" \
"v_readlane_b32 s20, v254, 16\n" \
"v_readlane_b32 s21, v254, 17\n" \
"v_readlane_b32 s22, v254, 18\n" \
"v_readlane_b32 s23, v254, 19\n" \
"v_readlane_b32 s24, v254, 20\n" \
"v_readlane_b32 s25, v254, 21\n" \
"v_readlane_b32 s26, v254, 22\n" \
"v_readlane_b32 s27, v254, 23\n" \
"v_readlane_b32 s28, v254, 24\n" \
"v_readlane_b32 s29, v254, 25\n" \
"v_readlane_b32 s30, v254, 26\n" \
"v_readlane_b32 s31, v254, 27\n" \
"v_readlane_b32 s32, v254, 28\n" \
"v_readlane_b32 s33, v254, 29\n" \
"v_readlane_b32 s34, v254, 30\n" \
"v_readlane_b32 s35, v254, 31\n" \
"v_readlane_b32 s36, v254, 32\n" \
"v_readlane_b32 s37, v254, 33\n" \
"v_readlane_b32 s38, v254, 34\n" \
"v_readlane_b32 s39, v254, 35\n" \
"s_mov_b32 s40, 0\n" \
"s_mov_b32 s51, 0\n" \
"s_nop 0\n" \
"v_mov_b32 v252, s38\n" \
"s_nop 4\n" \
"v_mov_b32 v100, -1\n" \
"v_mov_b32 v101, -1\n" \
"v_mov_b32 v102, -1\n" \
"v_mov_b32 v103, -1\n" \
"s_mov_b32 s35, -2\n" \
"s_mov_b32 s2, 0\n" \
"s_mov_b32 s3, -1\n" \
"v_writelane_b32 v251, s6, 0\n" \
"v_writelane_b32 v251, s10, 1\n" \
"v_writelane_b32 v251, s14, 2\n" \
"v_writelane_b32 v251, s18, 3\n" \
"s_mov_b32 s6, 0\n" \
"s_mov_b32 s10, 0\n" \
"s_mov_b32 s14, 0\n" \
"s_mov_b32 s18, 0\n" \
"s_mov_b32 s40, -1\n" \
"s_branch .LX3_pre\n" \
".LX3_wet:\n" \
"s_waitcnt vmcnt(0)\n" \
"v_readlane_b32 s6, v251, 0\n" \
"v_readlane_b32 s10, v251, 1\n" \
"v_readlane_b32 s14, v251, 2\n" \
"v_readlane_b32 s18, v251, 3\n" \
"s_nop 4\n" \
"s_add_u32 s43, s30, 1\n" \
"s_mov_b32 s50, 0\n" \
".LX3_w85l:\n" \
"buffer_load_dword v240, v206, s[4:7], 0 offen sc1\n" \
"s_waitcnt vmcnt(0)\n" \
".LX3_w85c:\n" \
"v_subrev_u32 v241, s43, v240\n" \
"v_cmp_gt_u32 vcc, 0x100, v241\n" \
"s_cmp_eq_u64 vcc, -1\n" \
"s_cbranch_scc1 .LX3_w85d\n" \
"s_sleep 2\n" \
"s_add_u32 s50, s50, 1\n" \
"s_cmp_lt_u32 s50, 0x20000\n" \
"s_cbranch_scc1 .LX3_w85l\n" \
"s_branch .LX3_end\n" \
".LX3_w85d:\n" \
"v_readfirstlane_b32 s31, v241\n" \
"s_lshr_b32 s32, s31, 2\n" \
"s_xor_b32 s29, s31, s32\n" \
"s_and_b32 s29, s29, 3\n" \
"s_lshl_b32 s29, s29, 13\n" \
"s_bitset1_b32 s29, 21\n" \
"s_and_b32 s31, s31, 3\n" \
"s_lshl_b32 s31, s31, 13\n" \
"s_bitcmp1_b32 s39, 0\n" \
"s_cselect_b32 s43, s29, 0\n" \
"s_xor_b32 s31, s31, s43\n" \
"s_xor_b32 s32, s31, 0x4000\n" \
"s_mov_b32 s39, 0\n" \
"s_add_u32 s47, s31, 0\n" \
"buffer_store_dwordx4 v[100:103], v214, s[4:7], s47 offen sc1\n" \
"buffer_store_dwordx4 v[100:103], v217, s[4:7], s47 offen sc1\n" \
"buffer_store_dwordx4 v[100:103], v218, s[4:7], s47 offen sc1\n" \
"buffer_store_dwordx4 v[100:103], v219, s[4:7], s47 offen sc1\n" \
"s_xor_b32 s47, s47, s29\n" \
"buffer_store_dwordx4 v[100:103], v214, s[4:7], s47 offen sc1\n" \
"buffer_store_dwordx4 v[100:103], v217, s[4:7], s47 offen sc1\n" \
"buffer_store_dwordx4 v[100:103], v218, s[4:7], s47 offen sc1\n" \
"buffer_store_dwordx4 v[100:103], v219, s[4:7], s47 offen sc1\n" \
"s_add_u32 s47, s32, 524288\n" \
"buffer_store_dwordx4 v[100:103], v214, s[4:7], s47 offen sc1\n" \
"buffer_store_dwordx4 v[100:103], v217, s[4:7], s47 offen sc1\n" \
"buffer_store_dwordx4 v[100:103], v218, s[4:7], s47 offen sc1\n" \
"buffer_store_dwordx4 v[100:103], v219, s[4:7], s47 offen sc1\n" \
"s_xor_b32 s47, s47, s29\n" \
"buffer_store_dwordx4 v[100:103], v214, s[4:7], s47 offen sc1\n" \
"buffer_store_dwordx4 v[100:103], v217, s[4:7], s47 offen sc1\n" \
"buffer_store_dwordx4 v[100:103], v218, s[4:7], s47 offen sc1\n" \
"buffer_store_dwordx4 v[100:103], v219, s[4:7], s47 offen sc1\n" \
"s_add_u32 s47, s31, 1048576\n" \
"buffer_store_dwordx4 v[100:103], v214, s[4:7], s47 offen sc1\n" \
"buffer_store_dwordx4 v[100:103], v217, s[4:7], s47 offen sc1\n" \
"buffer_store_dwordx4 v[100:103], v218, s[4:7], s47 offen sc1\n" \
"buffer_store_dwordx4 v[100:103], v219, s[4:7], s47 offen sc1\n" \
"s_xor_b32 s47, s47, s29\n" \
"buffer_store_dwordx4 v[100:103], v214, s[4:7], s47 offen sc1\n" \
"buffer_store_dwordx4 v[100:103], v217, s[4:7], s47 offen sc1\n" \
"buffer_store_dwordx4 v[100:103], v218, s[4:7], s47 offen sc1\n" \
"buffer_store_dwordx4 v[100:103], v219, s[4:7], s47 offen sc1\n" \
"s_add_u32 s47, s32, 1572864\n" \
"buffer_store_dwordx4 v[100:103], v214, s[4:7], s47 offen sc1\n" \
"buffer_store_dwordx4 v[100:103], v217, s[4:7], s47 offen sc1\n" \
"buffer_store_dwordx4 v[100:103], v218, s[4:7], s47 offen sc1\n" \
"buffer_store_dwordx4 v[100:103], v219, s[4:7], s47 offen sc1\n" \
"s_xor_b32 s47, s47, s29\n" \
"buffer_store_dwordx4 v[100:103], v214, s[4:7], s47 offen sc1\n" \
"buffer_store_dwordx4 v[100:103], v217, s[4:7], s47 offen sc1\n" \
"buffer_store_dwordx4 v[100:103], v218, s[4:7], s47 offen sc1\n" \
"buffer_store_dwordx4 v[100:103], v219, s[4:7], s47 offen sc1\n" \
"s_waitcnt vmcnt(0)\n" \
"s_add_u32 s43, s30, s40\n" \
"s_add_u32 s43, s43, 1\n" \
"v_mov_b32 v241, s43\n" \
"buffer_store_dword v241, v220, s[4:7], 0 offen sc1\n" \
"s_add_u32 s43, s30, 1\n" \
"s_mov_b32 s50, 0\n" \
".LX3_w86l:\n" \
"buffer_load_dword v240, v205, s[4:7], 0 offen sc1\n" \
"s_waitcnt vmcnt(0)\n" \
".LX3_w86c:\n" \
"v_subrev_u32 v241, s43, v240\n" \
"v_cmp_gt_u32 vcc, 0x100, v241\n" \
"s_cmp_eq_u64 vcc, -1\n" \
"s_cbranch_scc1 .LX3_w86d\n" \
"s_sleep 2\n" \
"s_add_u32 s50, s50, 1\n" \
"s_cmp_lt_u32 s50, 0x20000\n" \
"s_cbranch_scc1 .LX3_w86l\n" \
"s_branch .LX3_end\n" \
".LX3_w86d:\n" \
"s_add_u32 s43, s30, 8\n" \
"s_mov_b32 s50, 0\n" \
".LX3_w87l:\n" \
"buffer_load_dword v240, v215, s[4:7], 0 offen sc1\n" \
"s_waitcnt vmcnt(0)\n" \
".LX3_w87c:\n" \
"v_subrev_u32 v241, s43, v240\n" \
"v_cmp_gt_u32 vcc, 0x100, v241\n" \
"s_cmp_eq_u64 vcc, -1\n" \
"s_cbranch_scc1 .LX3_w87d\n" \
"s_sleep 2\n" \
"s_add_u32 s50, s50, 1\n" \
"s_cmp_lt_u32 s50, 0x20000\n" \
"s_cbranch_scc1 .LX3_w87l\n" \
"s_branch .LX3_end\n" \
".LX3_w87d:\n" \
"s_add_u32 s43, s40, 0\n" \
"s_mul_i32 s43, s43, 28672\n" \
"s_add_u32 s43, s43, s33\n" \
"buffer_load_dwordx4 a[0:3], v201, s[8:11], s43 offen sc1\n" \
"buffer_load_dwordx4 a[4:7], v201, s[8:11], s43 offen offset:1024 sc1\n" \
"buffer_load_dwordx4 a[8:11], v201, s[8:11], s43 offen offset:2048 sc1\n" \
"buffer_load_dwordx4 a[12:15], v201, s[8:11], s43 offen offset:3072 sc1\n" \
"s_add_u32 s43, s43, 4096\n" \
"buffer_load_dwordx4 a[16:19], v201, s[8:11], s43 offen sc1\n" \
"buffer_load_dwordx4 a[20:23], v201, s[8:11], s43 offen offset:1024 sc1\n" \
"buffer_load_dwordx4 a[24:27], v201, s[8:11], s43 offen offset:2048 sc1\n" \
"buffer_load_dwordx4 a[28:31], v201, s[8:11], s43 offen offset:3072 sc1\n" \
"s_add_u32 s43, s43, 4096\n" \
"buffer_load_dwordx4 a[32:35], v201, s[8:11], s43 offen sc1\n" \
"buffer_load_dwordx4 a[36:39], v201, s[8:11], s43 offen offset:1024 sc1\n" \
"buffer_load_dwordx4 a[40:43], v201, s[8:11], s43 offen offset:2048 sc1\n" \
"buffer_load_dwordx4 a[44:47], v201, s[8:11], s43 offen offset:3072 sc1\n" \
"s_add_u32 s43, s43, 4096\n" \
"buffer_load_dwordx4 a[48:51], v201, s[8:11], s43 offen sc1\n" \
"buffer_load_dwordx4 a[52:55], v201, s[8:11], s43 offen offset:1024 sc1\n" \
"buffer_load_dwordx4 a[56:59], v201, s[8:11], s43 offen offset:2048 sc1\n" \
"buffer_load_dwordx4 a[60:63], v201, s[8:11], s43 offen offset:3072 sc1\n" \
"s_add_u32 s43, s43, 4096\n" \
"buffer_load_dwordx4 a[64:67], v201, s[8:11], s43 offen sc1\n" \
"buffer_load_dwordx4 a[68:71], v201, s[8:11], s43 offen offset:1024 sc1\n" \
"buffer_load_dwordx4 a[72:75], v201, s[8:11], s43 offen offset:2048 sc1\n" \
"buffer_load_dwordx4 a[76:79], v201, s[8:11], s43 offen offset:3072 sc1\n" \
"s_add_u32 s43, s43, 4096\n" \
"buffer_load_dwordx4 a[80:83], v201, s[8:11], s43 offen sc1\n" \
"buffer_load_dwordx4 a[84:87], v201, s[8:11], s43 offen offset:1024 sc1\n" \
"buffer_load_dwordx4 a[88:91], v201, s[8:11], s43 offen offset:2048 sc1\n" \
"buffer_load_dwordx4 a[92:95], v201, s[8:11], s43 offen offset:3072 sc1\n" \
"s_add_u32 s43, s43, 4096\n" \
"buffer_load_dwordx4 a[96:99], v201, s[8:11], s43 offen sc1\n" \
"buffer_load_dwordx4 a[100:103], v201, s[8:11], s43 offen offset:1024 sc1\n" \
"buffer_load_dwordx4 a[104:107], v201, s[8:11], s43 offen offset:2048 sc1\n" \
"buffer_load_dwordx4 a[108:111], v201, s[8:11], s43 offen offset:3072 sc1\n" \
"s_add_u32 s50, s40, 0\n" \
"s_mul_i32 s50, s50, 28672\n" \
"s_add_u32 s50, s50, s34\n" \
"buffer_load_dwordx4 v[28:31], v201, s[12:15], s50 offen sc1\n" \
"buffer_load_dwordx4 v[32:35], v201, s[12:15], s50 offen offset:1024 sc1\n" \
"s_waitcnt vmcnt(0)\n" \
"s_barrier\n" \
".LX3_pre:\n" \
"ds_read_b96 v[248:250], v252\n" \
"s_waitcnt lgkmcnt(0)\n" \
"v_readfirstlane_b32 s44, v248\n" \
"v_readfirstlane_b32 s45, v249\n" \
"v_readfirstlane_b32 s46, v250\n" \
"s_mul_i32 s42, s25, s46\n" \
"s_lshr_b32 s42, s42, 12\n" \
".LX3_cu:\n" \
"s_sub_u32 s43, s24, s44\n" \
"s_cmp_lt_i32 s43, 0\n" \
"s_cbranch_scc0 .LX3_cud\n" \
"s_add_u32 s24, s24, s25\n" \
"s_xor_b32 s31, s31, s29\n" \
"s_xor_b32 s32, s32, s29\n" \
"s_xor_b32 s47, s47, s29\n" \
"s_add_u32 s39, s39, 1\n" \
"s_branch .LX3_cu\n" \
".LX3_cud:\n" \
".LX3_loop:\n" \
"s_add_u32 s43, s40, 2\n" \
"s_and_b32 s43, s43, 3\n" \
"s_bitcmp1_b32 s43, 0\n" \
"s_cselect_b32 s49, s32, s31\n" \
"s_lshl_b32 s43, s43, 19\n" \
"s_add_u32 s49, s49, s43\n" \
"ds_read_b96 v[248:250], v252\n" \
"s_waitcnt lgkmcnt(0)\n" \
"v_readfirstlane_b32 s44, v248\n" \
"v_readfirstlane_b32 s45, v249\n" \
"v_readfirstlane_b32 s46, v250\n" \
"s_mul_i32 s42, s25, s46\n" \
"s_lshr_b32 s42, s42, 12\n" \
"s_add_u32 s43, s40, 0\n" \
"s_and_b32 s43, s43, 3\n" \
"s_bitcmp1_b32 s43, 0\n" \
"s_cselect_b32 s47, s32, s31\n" \
"s_lshl_b32 s43, s43, 19\n" \
"s_add_u32 s47, s47, s43\n" \
"s_sub_u32 s43, s24, s44\n" \
"s_add_u32 s43, s43, s28\n" \
"s_max_i32 s43, s43, 0xfffd0000\n" \
"s_min_i32 s43, s43, 0x4000\n" \
"s_mul_i32 s43, s43, s46\n" \
"s_ashr_i32 s43, s43, 12\n" \
"s_add_u32 s41, s45, s43\n" \
"s_branch .LX3_a1\n" \
".LX3_retrya:\n" \
"s_add_u32 s51, s51, 1\n" \
"s_cmp_gt_u32 s51, 0x20000\n" \
"s_cbranch_scc1 .LX3_end\n" \
".LX3_a0:\n" \
"s_add_u32 s41, s41, s42\n" \
"s_add_u32 s24, s24, s25\n" \
"s_xor_b32 s31, s31, s29\n" \
"s_xor_b32 s32, s32, s29\n" \
"s_xor_b32 s47, s47, s29\n" \
"s_xor_b32 s48, s48, s29\n" \
"s_add_u32 s39, s39, 1\n" \
"buffer_store_dwordx4 v[100:103], v214, s[4:7], s47 offen sc1\n" \
"buffer_store_dwordx4 v[100:103], v217, s[4:7], s47 offen sc1\n" \
"buffer_store_dwordx4 v[100:103], v218, s[4:7], s47 offen sc1\n" \
"buffer_store_dwordx4 v[100:103], v219, s[4:7], s47 offen sc1\n" \
".p2align 6\n" \
".LX3_a1:\n" \
"s_memtime vcc\n" \
"s_waitcnt lgkmcnt(0)\n" \
"s_sub_u32 s43, s41, vcc_lo\n" \
"s_cmp_gt_i32 s43, 128\n" \
"s_cbranch_scc1 .LX3_a1\n" \
"s_cmp_lt_i32 s43, s37\n" \
"s_cbranch_scc1 .LX3_a0\n" \
"s_max_i32 s43, s43, 0\n" \
"s_and_b32 s43, s43, -4\n" \
"s_getpc_b64 vcc\n" \
".LX3_a3:\n" \
"s_sub_u32 s43, .LX3_a4-.LX3_a3, s43\n" \
"s_add_u32 vcc_lo, vcc_lo, s43\n" \
"s_addc_u32 vcc_hi, vcc_hi, 0\n" \
"s_setpc_b64 vcc\n" \
".rept 32\n" \
"s_nop 0\n" \
".endr\n" \
".LX3_a4:\n" \
"buffer_load_dwordx4 v[0:3], v208, s[4:7], s47 offen sc1\n" \
"buffer_load_dwordx4 v[4:7], v208, s[4:7], s47 offen offset:1024 sc1\n" \
"buffer_load_dwordx4 v[8:11], v208, s[4:7], s47 offen offset:2048 sc1\n" \
"buffer_load_dwordx4 v[12:15], v208, s[4:7], s47 offen offset:3072 sc1\n" \
"buffer_load_dwordx4 v[16:19], v209, s[4:7], s47 offen sc1\n" \
"buffer_load_dwordx4 v[20:23], v209, s[4:7], s47 offen offset:1024 sc1\n" \
"buffer_load_dwordx4 v[24:27], v209, s[4:7], s47 offen offset:2048 sc1\n" \
"s_cmp_eq_u32 s35, s40\n" \
"s_cbranch_scc1 .LX3_nb\n" \
"s_barrier\n" \
".LX3_nb:\n" \
"s_mov_b32 s35, s40\n" \
"v_mov_b32 v96, s47\n" \
"ds_write_b32 v252, v96 offset:16\n" \
"v_mov_b32 v60, 0\n" \
"v_mov_b32 v61, 0\n" \
"v_mov_b32 v62, 0\n" \
"v_mov_b32 v63, 0\n" \
"v_mov_b32 v64, 0\n" \
"v_mov_b32 v65, 0\n" \
"v_mov_b32 v66, 0\n" \
"v_mov_b32 v67, 0\n" \
"s_lshl_b32 s43, s40, 7\n" \
"buffer_load_dwordx4 v[236:239], v207, s[4:7], s43 offen sc1\n" \
"s_add_u32 s50, s40, 1\n" \
"s_mul_i32 s50, s50, 28672\n" \
"s_add_u32 s50, s50, s34\n" \
"buffer_load_dwordx4 v[44:47], v201, s[12:15], s50 offen sc1\n" \
"buffer_load_dwordx4 v[48:51], v201, s[12:15], s50 offen offset:1024 sc1\n" \
"s_waitcnt vmcnt(9)\n" \
"v_smfmac_f32_16x16x64_bf16 v[60:63], v[0:3], a[0:7], v200\n" \
"v_smfmac_f32_16x16x64_bf16 v[64:67], v[0:3], a[56:63], v200\n" \
"s_waitcnt vmcnt(8)\n" \
"v_smfmac_f32_16x16x64_bf16 v[60:63], v[4:7], a[8:15], v200\n" \
"v_smfmac_f32_16x16x64_bf16 v[64:67], v[4:7], a[64:71], v200\n" \
"s_waitcnt vmcnt(7)\n" \
"v_smfmac_f32_16x16x64_bf16 v[60:63], v[8:11], a[16:23], v200\n" \
"v_smfmac_f32_16x16x64_bf16 v[64:67], v[8:11], a[72:79], v200\n" \
"s_waitcnt vmcnt(6)\n" \
"v_smfmac_f32_16x16x64_bf16 v[60:63], v[12:15], a[24:31], v200\n" \
"v_smfmac_f32_16x16x64_bf16 v[64:67], v[12:15], a[80:87], v200\n" \
"s_waitcnt vmcnt(5)\n" \
"v_smfmac_f32_16x16x64_bf16 v[60:63], v[16:19], a[32:39], v200\n" \
"v_smfmac_f32_16x16x64_bf16 v[64:67], v[16:19], a[88:95], v200\n" \
"s_waitcnt vmcnt(4)\n" \
"v_smfmac_f32_16x16x64_bf16 v[60:63], v[20:23], a[40:47], v200\n" \
"v_smfmac_f32_16x16x64_bf16 v[64:67], v[20:23], a[96:103], v200\n" \
"s_waitcnt vmcnt(3)\n" \
"v_smfmac_f32_16x16x64_bf16 v[60:63], v[24:27], a[48:55], v200\n" \
"v_smfmac_f32_16x16x64_bf16 v[64:67], v[24:27], a[104:111], v200\n" \
"s_nop 2\n" \
"v_max3_u32 v84, v0, v0, v1\n" \
"v_max3_u32 v84, v84, v2, v3\n" \
"v_max3_u32 v84, v84, v4, v5\n" \
"v_max3_u32 v84, v84, v6, v7\n" \
"v_max3_u32 v84, v84, v8, v9\n" \
"v_permlane32_swap_b32 v60, v64\n" \
"v_permlane32_swap_b32 v61, v65\n" \
"v_permlane32_swap_b32 v62, v66\n" \
"v_permlane32_swap_b32 v63, v67\n" \
"v_pk_add_f32 v[60:61], v[60:61], v[64:65]\n" \
"v_pk_add_f32 v[62:63], v[62:63], v[66:67]\n" \
"v_max3_u32 v84, v84, v10, v11\n" \
"v_max_f32_dpp v88, v60, v60 row_ror:8 row_mask:0xf bank_mask:0x3\n" \
"v_max_f32_dpp v89, v61, v61 row_ror:8 row_mask:0xf bank_mask:0x3\n" \
"v_max_f32_dpp v88, v62, v62 row_ror:8 row_mask:0xf bank_mask:0xc\n" \
"v_max_f32_dpp v89, v63, v63 row_ror:8 row_mask:0xf bank_mask:0xc\n" \
"v_max3_u32 v84, v84, v12, v13\n" \
"v_max_f32_dpp v90, v88, v88 row_shl:4 row_mask:0xf bank_mask:0x5\n" \
"v_max_f32_dpp v90, v89, v89 row_shr:4 row_mask:0xf bank_mask:0xa\n" \
"v_max3_u32 v84, v84, v14, v15\n" \
"v_max3_u32 v84, v84, v16, v17\n" \
"v_max_f32_dpp v88, v90, v90 quad_perm:[1,0,3,2] row_mask:0xf bank_mask:0xf\n" \
"v_max3_u32 v84, v84, v18, v19\n" \
"v_max3_u32 v84, v84, v20, v21\n" \
"v_max_f32_dpp v86, v88, v88 quad_perm:[2,3,0,1] row_mask:0xf bank_mask:0xf\n" \
"v_max_f32_dpp v89, v88, v88 quad_perm:[2,3,0,1] row_mask:0xf bank_mask:0xf\n" \
"v_max3_u32 v84, v84, v22, v23\n" \
"v_max3_u32 v84, v84, v24, v25\n" \
"v_permlane32_swap_b32 v86, v89\n" \
"v_max_f32 v86, v86, v89\n" \
"v_max3_u32 v84, v84, v26, v27\n" \
"v_cmp_eq_u32 vcc, -1, v84\n" \
"v_subrev_f32_dpp v60, v86, v60 row_newbcast:0 row_mask:0xf bank_mask:0xf\n" \
"v_subrev_f32_dpp v61, v86, v61 row_newbcast:4 row_mask:0xf bank_mask:0xf\n" \
"v_subrev_f32_dpp v62, v86, v62 row_newbcast:8 row_mask:0xf bank_mask:0xf\n" \
"v_subrev_f32_dpp v63, v86, v63 row_newbcast:12 row_mask:0xf bank_mask:0xf\n" \
"v_exp_f32 v60, v60\n" \
"v_exp_f32 v61, v61\n" \
"v_exp_f32 v62, v62\n" \
"v_exp_f32 v63, v63\n" \
"v_cvt_pk_bf16_f32 v92, v60, v61\n" \
"v_cvt_pk_bf16_f32 v93, v62, v63\n" \
"ds_write_b64 v212, v[92:93]\n" \
"s_cbranch_vccnz .LX3_retrya\n" \
"v_add_f32_dpp v88, v60, v60 row_ror:8 row_mask:0xf bank_mask:0x3\n" \
"v_add_f32_dpp v89, v61, v61 row_ror:8 row_mask:0xf bank_mask:0x3\n" \
"v_add_f32_dpp v88, v62, v62 row_ror:8 row_mask:0xf bank_mask:0xc\n" \
"v_add_f32_dpp v89, v63, v63 row_ror:8 row_mask:0xf bank_mask:0xc\n" \
"v_mov_b32 v245, v245\n" \
"v_add_f32_dpp v90, v88, v88 row_shl:4 row_mask:0xf bank_mask:0x5\n" \
"v_add_f32_dpp v90, v89, v89 row_shr:4 row_mask:0xf bank_mask:0xa\n" \
"v_mov_b32 v245, v245\n" \
"v_mov_b32 v245, v245\n" \
"v_add_f32_dpp v88, v90, v90 quad_perm:[1,0,3,2] row_mask:0xf bank_mask:0xf\n" \
"v_mov_b32 v245, v245\n" \
"v_mov_b32 v245, v245\n" \
"v_add_f32_dpp v87, v88, v88 quad_perm:[2,3,0,1] row_mask:0xf bank_mask:0xf\n" \
"buffer_store_dwordx2 v[86:87], v211, s[4:7], s47 offen sc1\n" \
"s_nop 0\n" \
"v_mov_b32 v245, v245\n" \
"s_waitcnt lgkmcnt(0)\n" \
"s_barrier\n" \
"ds_read_b64_tr_b16 v[92:93], v213\n" \
"ds_read_b64_tr_b16 v[94:95], v213 offset:64\n" \
"ds_read_b32 v84, v252 offset:16\n" \
"s_waitcnt lgkmcnt(1)\n" \
"v_mfma_f32_16x16x32_bf16 v[68:71], v[92:95], v[28:31], 0\n" \
"s_waitcnt lgkmcnt(0)\n" \
"v_xor_b32 v85, s29, v84\n" \
"v_mfma_f32_16x16x32_bf16 v[72:75], v[92:95], v[32:35], 0\n" \
"v_add_u32 v84, v84, v210\n" \
"v_add_u32 v85, v85, v210\n" \
"v_mov_b32 v245, v245\n" \
"v_mov_b32 v245, v245\n" \
"v_mov_b32 v245, v245\n" \
"v_cvt_pk_bf16_f32 v96, v68, v69\n" \
"v_cvt_pk_bf16_f32 v97, v70, v71\n" \
"s_mov_b64 exec, s[2:3]\n" \
"v_cvt_pk_bf16_f32 v96, v72, v73\n" \
"v_cvt_pk_bf16_f32 v97, v74, v75\n" \
"s_mov_b64 exec, -1\n" \
"v_mov_b32 v98, v96\n" \
"v_mov_b32 v99, v97\n" \
"s_nop 1\n" \
"v_permlane16_swap_b32 v96, v98\n" \
"v_permlane16_swap_b32 v97, v99\n" \
"buffer_store_dwordx4 v[96:99], v84, s[4:7], 0 offen sc1\n" \
"ds_write_b128 v216, v[0:3] offset:0\n" \
"ds_write_b128 v216, v[4:7] offset:1024\n" \
"ds_write_b128 v216, v[8:11] offset:2048\n" \
"ds_write_b128 v216, v[12:15] offset:3072\n" \
"ds_write_b128 v216, v[16:19] offset:4096\n" \
"ds_write_b128 v216, v[20:23] offset:5120\n" \
"ds_write_b128 v216, v[24:27] offset:6144\n" \
"s_waitcnt lgkmcnt(0)\n" \
"s_barrier\n" \
"buffer_store_dwordx4 v[96:99], v85, s[4:7], 0 offen sc1\n" \
"s_xor_b32 s43, s47, s29\n" \
"buffer_store_dwordx2 v[86:87], v211, s[4:7], s43 offen sc1\n" \
"s_add_u32 s43, s40, 1\n" \
"s_mul_i32 s43, s43, 28672\n" \
"s_add_u32 s43, s43, s33\n" \
"buffer_load_dwordx4 a[0:3], v201, s[8:11], s43 offen sc1\n" \
"buffer_load_dwordx4 a[4:7], v201, s[8:11], s43 offen offset:1024 sc1\n" \
"buffer_load_dwordx4 a[8:11], v201, s[8:11], s43 offen offset:2048 sc1\n" \
"buffer_load_dwordx4 a[12:15], v201, s[8:11], s43 offen offset:3072 sc1\n" \
"s_add_u32 s43, s43, 4096\n" \
"buffer_load_dwordx4 a[16:19], v201, s[8:11], s43 offen sc1\n" \
"buffer_load_dwordx4 a[20:23], v201, s[8:11], s43 offen offset:1024 sc1\n" \
"buffer_load_dwordx4 a[24:27], v201, s[8:11], s43 offen offset:2048 sc1\n" \
"buffer_load_dwordx4 a[28:31], v201, s[8:11], s43 offen offset:3072 sc1\n" \
"s_add_u32 s43, s43, 4096\n" \
"buffer_load_dwordx4 a[32:35], v201, s[8:11], s43 offen sc1\n" \
"buffer_load_dwordx4 a[36:39], v201, s[8:11], s43 offen offset:1024 sc1\n" \
"buffer_load_dwordx4 a[40:43], v201, s[8:11], s43 offen offset:2048 sc1\n" \
"buffer_load_dwordx4 a[44:47], v201, s[8:11], s43 offen offset:3072 sc1\n" \
"s_add_u32 s43, s43, 4096\n" \
"buffer_load_dwordx4 a[48:51], v201, s[8:11], s43 offen sc1\n" \
"buffer_load_dwordx4 a[52:55], v201, s[8:11], s43 offen offset:1024 sc1\n" \
"buffer_load_dwordx4 a[56:59], v201, s[8:11], s43 offen offset:2048 sc1\n" \
"buffer_load_dwordx4 a[60:63], v201, s[8:11], s43 offen offset:3072 sc1\n" \
"s_add_u32 s43, s43, 4096\n" \
"buffer_load_dwordx4 a[64:67], v201, s[8:11], s43 offen sc1\n" \
"buffer_load_dwordx4 a[68:71], v201, s[8:11], s43 offen offset:1024 sc1\n" \
"buffer_load_dwordx4 a[72:75], v201, s[8:11], s43 offen offset:2048 sc1\n" \
"buffer_load_dwordx4 a[76:79], v201, s[8:11], s43 offen offset:3072 sc1\n" \
"s_add_u32 s43, s43, 4096\n" \
"buffer_load_dwordx4 a[80:83], v201, s[8:11], s43 offen sc1\n" \
"buffer_load_dwordx4 a[84:87], v201, s[8:11], s43 offen offset:1024 sc1\n" \
"buffer_load_dwordx4 a[88:91], v201, s[8:11], s43 offen offset:2048 sc1\n" \
"buffer_load_dwordx4 a[92:95], v201, s[8:11], s43 offen offset:3072 sc1\n" \
"s_add_u32 s43, s43, 4096\n" \
"buffer_load_dwordx4 a[96:99], v201, s[8:11], s43 offen sc1\n" \
"buffer_load_dwordx4 a[100:103], v201, s[8:11], s43 offen offset:1024 sc1\n" \
"buffer_load_dwordx4 a[104:107], v201, s[8:11], s43 offen offset:2048 sc1\n" \
"buffer_load_dwordx4 a[108:111], v201, s[8:11], s43 offen offset:3072 sc1\n" \
"buffer_store_dwordx4 v[100:103], v214, s[4:7], s49 offen sc1\n" \
"s_xor_b32 s43, s49, s29\n" \
"buffer_store_dwordx4 v[100:103], v214, s[4:7], s43 offen sc1\n" \
"s_waitcnt vmcnt(34)\n" \
"v_mov_b32 v28, v44\n" \
"v_mov_b32 v29, v45\n" \
"v_mov_b32 v30, v46\n" \
"v_mov_b32 v31, v47\n" \
"v_mov_b32 v32, v48\n" \
"v_mov_b32 v33, v49\n" \
"v_mov_b32 v34, v50\n" \
"v_mov_b32 v35, v51\n" \
"v_max_u32 v236, v236, v238\n" \
"v_max_u32 v237, v237, v239\n" \
"v_and_b32 v236, 0xfff, v236\n" \
"v_and_b32 v237, 0xfff, v237\n" \
"s_nop 0\n" \
"v_readfirstlane_b32 s26, v236\n" \
"v_readfirstlane_b32 s27, v237\n" \
"s_add_u32 s24, s24, s26\n" \
"s_add_u32 s24, s24, s27\n" \
"s_add_u32 s40, s40, 1\n" \
"s_cmp_eq_u32 s40, 0\n" \
"s_cbranch_scc1 .LX3_wet\n" \
"s_cmp_lt_u32 s40, 63\n" \
"s_cbranch_scc1 .LX3_loop\n" \
"ds_read_b96 v[248:250], v252\n" \
"s_waitcnt lgkmcnt(0)\n" \
"v_readfirstlane_b32 s44, v248\n" \
"v_readfirstlane_b32 s45, v249\n" \
"v_readfirstlane_b32 s46, v250\n" \
"s_mul_i32 s42, s25, s46\n" \
"s_lshr_b32 s42, s42, 12\n" \
"s_add_u32 s43, s40, 0\n" \
"s_and_b32 s43, s43, 3\n" \
"s_bitcmp1_b32 s43, 0\n" \
"s_cselect_b32 s47, s32, s31\n" \
"s_lshl_b32 s43, s43, 19\n" \
"s_add_u32 s47, s47, s43\n" \
"s_sub_u32 s43, s24, s44\n" \
"s_add_u32 s43, s43, s28\n" \
"s_max_i32 s43, s43, 0xfffd0000\n" \
"s_min_i32 s43, s43, 0x4000\n" \
"s_mul_i32 s43, s43, s46\n" \
"s_ashr_i32 s43, s43, 12\n" \
"s_add_u32 s41, s45, s43\n" \
"s_branch .LX3_z1\n" \
".LX3_retryz:\n" \
"s_add_u32 s51, s51, 1\n" \
"s_cmp_gt_u32 s51, 0x20000\n" \
"s_cbranch_scc1 .LX3_end\n" \
".LX3_z0:\n" \
"s_add_u32 s41, s41, s42\n" \
"s_add_u32 s24, s24, s25\n" \
"s_xor_b32 s31, s31, s29\n" \
"s_xor_b32 s32, s32, s29\n" \
"s_xor_b32 s47, s47, s29\n" \
"s_xor_b32 s48, s48, s29\n" \
"s_add_u32 s39, s39, 1\n" \
".p2align 6\n" \
".LX3_z1:\n" \
"s_memtime vcc\n" \
"s_waitcnt lgkmcnt(0)\n" \
"s_sub_u32 s43, s41, vcc_lo\n" \
"s_cmp_gt_i32 s43, 128\n" \
"s_cbranch_scc1 .LX3_z1\n" \
"s_cmp_lt_i32 s43, s37\n" \
"s_cbranch_scc1 .LX3_z0\n" \
"s_max_i32 s43, s43, 0\n" \
"s_and_b32 s43, s43, -4\n" \
"s_getpc_b64 vcc\n" \
".LX3_z3:\n" \
"s_sub_u32 s43, .LX3_z4-.LX3_z3, s43\n" \
"s_add_u32 vcc_lo, vcc_lo, s43\n" \
"s_addc_u32 vcc_hi, vcc_hi, 0\n" \
"s_setpc_b64 vcc\n" \
".rept 32\n" \
"s_nop 0\n" \
".endr\n" \
".LX3_z4:\n" \
"buffer_load_dwordx4 v[0:3], v208, s[4:7], s47 offen sc1\n" \
"buffer_load_dwordx4 v[4:7], v208, s[4:7], s47 offen offset:1024 sc1\n" \
"buffer_load_dwordx4 v[8:11], v208, s[4:7], s47 offen offset:2048 sc1\n" \
"buffer_load_dwordx4 v[12:15], v208, s[4:7], s47 offen offset:3072 sc1\n" \
"buffer_load_dwordx4 v[16:19], v209, s[4:7], s47 offen sc1\n" \
"buffer_load_dwordx4 v[20:23], v209, s[4:7], s47 offen offset:1024 sc1\n" \
"buffer_load_dwordx4 v[24:27], v209, s[4:7], s47 offen offset:2048 sc1\n" \
"s_waitcnt vmcnt(0)\n" \
"ds_write_b128 v216, v[0:3] offset:7168\n" \
"ds_write_b128 v216, v[4:7] offset:8192\n" \
"ds_write_b128 v216, v[8:11] offset:9216\n" \
"ds_write_b128 v216, v[12:15] offset:10240\n" \
"ds_write_b128 v216, v[16:19] offset:11264\n" \
"ds_write_b128 v216, v[20:23] offset:12288\n" \
"ds_write_b128 v216, v[24:27] offset:13312\n" \
"v_max3_u32 v84, v0, v0, v1\n" \
"v_max3_u32 v84, v84, v2, v3\n" \
"v_max3_u32 v84, v84, v4, v5\n" \
"v_max3_u32 v84, v84, v6, v7\n" \
"v_max3_u32 v84, v84, v8, v9\n" \
"v_max3_u32 v84, v84, v10, v11\n" \
"v_max3_u32 v84, v84, v12, v13\n" \
"v_max3_u32 v84, v84, v14, v15\n" \
"v_max3_u32 v84, v84, v16, v17\n" \
"v_max3_u32 v84, v84, v18, v19\n" \
"v_max3_u32 v84, v84, v20, v21\n" \
"v_max3_u32 v84, v84, v22, v23\n" \
"v_max3_u32 v84, v84, v24, v25\n" \
"v_max3_u32 v84, v84, v26, v27\n" \
"v_cmp_eq_u32 vcc, -1, v84\n" \
"s_cbranch_vccnz .LX3_retryz\n" \
"s_waitcnt lgkmcnt(0)\n" \
"s_barrier\n" \
"s_barrier\n" \
".LX3_end:\n" \
"s_endpgm\n"
typedef __attribute__((ext_vector_type(16))) float f16v;
DI u4 pack8(const f16v& x, int s, float sc) { return (u4){pk2(x[8 * s] * sc, x[8 * s + 1] * sc), pk2(x[8 * s + 2] * sc, x[8 * s + 3] * sc), pk2(x[8 * s + 4] * sc, x[8 * s + 5] * sc), pk2(x[8 * s + 6] * sc, x[8 * s + 7] * sc)}; }
DI u4 pack8(const f16v& x, int s) { return (u4){pk2(x[8 * s], x[8 * s + 1]), pk2(x[8 * s + 2], x[8 * s + 3]), pk2(x[8 * s + 4], x[8 * s + 5]), pk2(x[8 * s + 6], x[8 * s + 7])}; }
DI void dma_pair(u4 srd, int v0, int v1, int soff, unsigned m) {
    asm volatile("s_mov_b32 m0, %4\n\ts_nop 0\n\tbuffer_load_dwordx4 %0, %2, %3 offen lds\n\tbuffer_load_dwordx4 %1, %2, %3 offen offset:1024 lds" :: "v"(v0), "v"(v1), "s"(srd), "s"(soff), "s"(m) : "memory");
}
DI void dma_one(u4 srd, int v0, int soff, unsigned m) {
    asm volatile("s_mov_b32 m0, %3\n\ts_nop 0\n\tbuffer_load_dwordx4 %0, %1, %2 offen lds" :: "v"(v0), "s"(srd), "s"(soff), "s"(m) : "memory");
}
DI void mV_va(f16v& c, u4 x, u4 y) { asm volatile("v_mfma_f32_32x32x16_bf16 %0, %1, %2, %0" : "+v"(c) : "v"(x), "a"(y)); }
DI void mV_va0(f16v& c, u4 x, u4 y) { asm volatile("v_mfma_f32_32x32x16_bf16 %0, %1, %2, 0" : "=&v"(c) : "v"(x), "a"(y)); }
DI void mV_av(f16v& c, u4 x, u4 y) { asm volatile("v_mfma_f32_32x32x16_bf16 %0, %1, %2, %0" : "+v"(c) : "a"(x), "v"(y)); }
DI void mV_av0(f16v& c, u4 x, u4 y) { asm volatile("v_mfma_f32_32x32x16_bf16 %0, %1, %2, 0" : "=&v"(c) : "a"(x), "v"(y)); }
DI void mV_vv(f16v& c, u4 x, u4 y) { asm volatile("v_mfma_f32_32x32x16_bf16 %0, %1, %2, %0" : "+v"(c) : "v"(x), "v"(y)); }
DI void mV_vv0(f16v& c, u4 x, u4 y) { asm volatile("v_mfma_f32_32x32x16_bf16 %0, %1, %2, 0" : "=&v"(c) : "v"(x), "v"(y)); }
template <int N, int PD, class RD, class MM, class EX> DI void pipe(RD rd, MM mm, EX ex) {
    u4 f[PD];
#pragma unroll
    for (int j = 0; j < PD; ++j) f[j] = rd(j);
    SB();
#pragma unroll
    for (int j = 0; j < N; ++j) { mm(j, f[j % PD]); if (j + PD < N) f[j % PD] = rd(j + PD); ex(j); SB(); }
}
DI u4 sld128(u4 srd, int voff, int soff) { u4 v; asm volatile("s_nop 4\n\tbuffer_load_dwordx4 %0, %1, %2, %3 offen sc1" : "=v"(v) : "v"(voff), "s"(srd), "s"(soff) : "memory"); return v; }
DI u2 sld64(u4 srd, int voff, int soff) { u2 v; asm volatile("s_nop 4\n\tbuffer_load_dwordx2 %0, %1, %2, %3 offen sc1" : "=v"(v) : "v"(voff), "s"(srd), "s"(soff) : "memory"); return v; }
DI void sst128(u4 v, u4 srd, int voff, int soff) { asm volatile("s_nop 4\n\tbuffer_store_dwordx4 %0, %1, %2, %3 offen sc1\n\ts_nop 1" :: "v"(v), "v"(voff), "s"(srd), "s"(soff) : "memory"); }
DI void sst64(u2 v, u4 srd, int voff, int soff) { asm volatile("s_nop 4\n\tbuffer_store_dwordx2 %0, %1, %2, %3 offen sc1" :: "v"(v), "v"(voff), "s"(srd), "s"(soff) : "memory"); }
DI void sst32(unsigned v, u4 srd, int voff, int soff) { asm volatile("s_nop 4\n\tbuffer_store_dword %0, %1, %2, %3 offen sc1" :: "v"(v), "v"(voff), "s"(srd), "s"(soff) : "memory"); }
DI unsigned sld32(u4 srd, int voff, int soff) { unsigned v; asm volatile("s_nop 4\n\tbuffer_load_dword %0, %1, %2, %3 offen sc1" : "=v"(v) : "v"(voff), "s"(srd), "s"(soff) : "memory"); return v; }
template <int N> DI void waitvm() { asm volatile("s_waitcnt vmcnt(%0)" :: "i"(N) : "memory"); }
DI int swzr(int row) { return ((row & 1) << 2) | (row & 8) | ((row >> 1) & 3); }
#define SLICE 32768
#define CSCALE (LOG2E * 0.0625f)

struct SideCtx {
    char LAS* sm; unsigned smb; int lane, wave;
    int vr0, vr1, vc0, vc1;
};
DI int fresh_lane() { int l; asm volatile("v_mbcnt_lo_u32_b32 %0, -1, 0\n\tv_mbcnt_hi_u32_b32 %0, -1, %0" : "=v"(l)); __builtin_assume(l >= 0 && l < 64); return l; }
DI SideCtx make_ctx(int wave) {
    SideCtx c; c.sm = (char LAS*)smem_; c.smb = (unsigned)(size_t)c.sm; c.lane = fresh_lane(); c.wave = wave;
    asm volatile("" : "+s"(c.wave), "+s"(c.smb));
    const int l = c.lane;
    { int row = 4 * c.wave + (l >> 5); c.vr0 = row * 8192 + (((l & 31) ^ swzr(row)) * 16); row += 2; c.vr1 = row * 8192 + (((l & 31) ^ swzr(row)) * 16) - 1024; }
    { int row = l >> 3; c.vc0 = row * 896 + (((l & 7) ^ (((row >> 1) & 1) << 2)) * 16); c.vc1 = c.vc0 + 8 * 896 - 1024; }
    return c;
}
DI void dma_rows_q(const SideCtx& c, u4 srd, int h, int r0, int slot, int g) { dma_pair(srd, c.vr0, c.vr1, (r0 + 16 * g) * 8192 + h * 512, c.smb + slot * SLICE + (8 * g + 2 * c.wave) * 1024); }
DI void dma_cols_q(const SideCtx& c, u4 srd, int h, int c0, int slot, int g) { const int k = 8 * c.wave + 2 * g; dma_pair(srd, c.vc0, c.vc1, (h * 256 + 8 * k) * 896 + c0 * 2, c.smb + slot * SLICE + k * 1024); }
DI void dma_rows(const SideCtx& c, u4 srd, int h, int r0, int slot) {
#pragma unroll
    for (int g = 0; g < 4; ++g) dma_rows_q(c, srd, h, r0, slot, g);
}
DI void dma_cols(const SideCtx& c, u4 srd, int h, int c0, int slot) {
#pragma unroll
    for (int g = 0; g < 4; ++g) dma_cols_q(c, srd, h, c0, slot, g);
}
DI u4 rd_rows(const SideCtx& c, int slot, int ks, int tau) {
    const int l = c.lane, hi = l >> 5, ro = l & 31;
    const int rowc = 32 * (ro >> 4) + 4 * ((ro >> 2) & 1) + 8 * ((ro >> 1) & 1) + 2 * ((ro >> 3) & 1) + (ro & 1);
    const int ch = (2 * (ks & 7) + hi) ^ swzr(rowc);
    return *(const u4 LAS*)(c.sm + (rowc * 512 + ch * 16) + (slot * SLICE + tau * 8192 + (ks >> 3) * 256));
}
DI u4 tr_cols(const SideCtx& c, int slot, int ks, int nt) {
    const int l = c.lane, hi = l >> 5, blk = (l >> 4) & 1, r = (l & 15) >> 2, p = l & 3;
    u2 o[2];
#pragma unroll
    for (int t = 0; t < 2; ++t) {
        const int ch = (4 * nt + 2 * blk + (p >> 1)) ^ (((r >> 1) & 1) << 2);
        o[t] = trread(c.sm + ((8 * hi + r) * 128 + ch * 16 + 8 * (p & 1)) + (slot * SLICE + ks * 2048 + t * 512));
    }
    return (u4){o[0][0], o[0][1], o[1][0], o[1][1]};
}
#define P_ESLP 0
#define MID_K 20
#define PF_AHEAD 12
#define NSEG1 10
DI void set_flag(const SideCtx& c, u4 rhb, int off, unsigned val) {
    waitvm<0>(); bar();
    if (c.wave == 0) sst32(val, rhb, c.lane == 0 ? off : OOB, 0);
}
DI void wait_prog(u4 rhb, int voff_or_oob, unsigned want) {
    unsigned sp = 0;
#pragma nounroll
    for (;;) {
        unsigned v = sld32(rhb, voff_or_oob, 0); WAITV(0, "+v"(v));
        if (__all((v - want < 256u) | (voff_or_oob >= OOB))) break;
        asm volatile("s_sleep 4" ::: "memory");
        if (++sp > SPIN_MAX) __builtin_amdgcn_endpgm();
    }
}
#define MFW32(x, y) asm volatile("s_nop 11" : "+v"(x), "+v"(y))
DI int perm16(int r) { return 4 * ((r >> 2) & 1) + 8 * ((r >> 1) & 1) + 2 * ((r >> 3) & 1) + (r & 1); }
#define CHUNK(u) ((u) == 6 ? 3 : ((u) >> 1) + 4 * ((u) & 1))
DI void w_first(int wave, int type, int h, int sub, int part) {
    CArgs& a = get_args();
    const SideCtx c = make_ctx(wave);
    h = __builtin_amdgcn_readfirstlane(h); sub = __builtin_amdgcn_readfirstlane(sub); asm volatile("" : "+s"(h), "+s"(sub));
    const u4 rw1 = mksrd(type ? a.Wv : a.Wk, 448 * 8192), rw2 = mksrd(type ? a.Wo : a.Wq, 448 * 8192);
    const int c1 = sub == 3 ? 384 : sub * 128 + 64;
    if (part) { dma_rows(c, rw1, h, 64 * CHUNK(0), 0); dma_rows(c, rw1, h, 64 * CHUNK(1), 1); }
    else if (type) { dma_cols(c, rw2, h, sub * 128, 2); dma_cols(c, rw2, h, c1, 3); }
    else { dma_rows(c, rw2, h, sub * 128, 2); dma_rows(c, rw2, h, c1, 3); }
}
template <int N> struct IntC { static constexpr int value = N; };
template <int TYPE>
DI void wgu_task(unsigned tag, int vb, int wave, int h, int sub) {
    CArgs& a = get_args();
    const SideCtx c = make_ctx(wave);
    const int lane = c.lane, hi = lane >> 5, l31 = lane & 31, w = c.wave;
    h = __builtin_amdgcn_readfirstlane(h); sub = __builtin_amdgcn_readfirstlane(sub); asm volatile("" : "+s"(h), "+s"(sub));
    const u4 rw1 = mksrd(TYPE ? a.Wv : a.Wk, 448 * 8192), rx = mksrd(a.X, 2048 * 896), rhb = mksrd(a.HB, HB_SZ);
    const int o_fl = (w == 0 && lane == 0) ? O_FW + ((h * 2 + TYPE) * 4 + sub) * 128 : OOB;
    int xo[7];
#pragma unroll
    for (int k = 0; k < 7; ++k) { const int n = 64 * (7 * (w & 1) + k) + lane, row = (n * 1171) >> 16, pos = n - 56 * row; xo[k] = (row + 16 * (w >> 1)) * 896 + ((pos ^ ((row >> 1) & 7)) * 16); }
    auto xdma = [&](int s, int reg, int k) { dma_one(rx, xo[k], s * 28672, c.smb + reg + (7 * w + k) * 1024); };
    const int m = sub * 4 + w;
    u4 wk[28];
    f16v d[2][2];
    {
        u4 kf[16];
        const int rowp = 16 * (l31 >> 4) + perm16(l31 & 15);
#pragma unroll
        for (int ks = 0; ks < 16; ++ks)
            kf[ks] = TYPE ? tr_cols(c, 2 + (w >> 1), ks, w & 1) : *(const u4 LAS*)(c.sm + 2 * SLICE + w * 16384 + (rowp * 512 + ((2 * (ks & 7) + hi) ^ swzr(rowp)) * 16) + (ks >> 3) * 256);
        bar();
        unsigned pf[3];
#pragma unroll
        for (int i = 0; i < 3; ++i) asm volatile("buffer_load_dword %0, %1, %2, %3 offen" : "=v"(pf[i]) : "v"(lane < 7 ? (vb >> 3) * 896 + lane * 128 : OOB), "s"(rx), "s"((4 * i + w) * 28672) : "memory");
        dma_rows(c, rw1, h, 64 * CHUNK(2), 2); dma_rows(c, rw1, h, 64 * CHUNK(3), 3); dma_rows(c, rw1, h, 64 * CHUNK(4), 4);
        const u4 rout = mksrd(TYPE ? a.WvoF : a.WqkF, 28 * 16 * 14336);
        const int t16 = 2 * m + (l31 >> 4);
        const int o_st = m > 13 ? OOB : (TYPE ? t16 * 16 + h : h * 28 + t16) * 14336 + (hi * 16 + (l31 & 15)) * 16;
        u4 p[4], pn; unsigned ts[7];
        auto epi = [&](int v, int e, int st) __attribute__((always_inline)) {
            const f16v& x = d[v & 1][e & 1]; const int o = 8 * (e >> 1) + 4 * st; const float sc = TYPE ? 1.f : CSCALE;
            if (st < 2) { p[e][2 * st] = pk2(x[o] * sc, x[o + 1] * sc); p[e][2 * st + 1] = pk2(x[o + 2] * sc, x[o + 3] * sc); asm volatile("" : "+v"(p[e][2 * st]), "+v"(p[e][2 * st + 1])); }
            else if (st == 2) st128(p[e], rout, o_st, (CHUNK(v) * 2 + (e >> 1)) * 1024 + (e & 1) * 512);
            else if (st == 3) { const auto s0 = __builtin_amdgcn_permlane32_swap(p[e][0], p[e][1], false, false), s1 = __builtin_amdgcn_permlane32_swap(p[e][2], p[e][3], false, false);
                                pn = (u4){s0[0], s1[0], s0[1], s1[1]}; PIN(pn); }
            else { wk[4 * CHUNK(v) + e] = pn; asm volatile("" : "+a"(wk[4 * CHUNK(v) + e])); }
        };
#pragma unroll
        for (int u = 0; u < 7; ++u) {
            if (u == 0) waitvm<35>(); else if (u == 1) waitvm<27>(); else if (u == 2) waitvm<28>(); else if (u == 3) waitvm<20>(); else if (u == 4) waitvm<13>(); else waitvm<7>();
            if (u == 2) asm volatile("" :: "v"(pf[0]), "v"(pf[1]), "v"(pf[2]));
            bar(); ts[u] = TNOW();
            if (u >= 3) st32(tag + (u < 5 ? u - 2 : u - 1), rhb, o_fl, 0);
            pipe<32, 4>([&](int j) { return rd_rows(c, u % 5, j >> 1, j & 1); },
                        [&](int j, u4 f) { if (j < 2) mV_vv0(d[u & 1][j & 1], f, kf[j >> 1]); else mV_vv(d[u & 1][j & 1], f, kf[j >> 1]); },
                        [&](int j) { if (u > 0 && j >= 2 && j < 14) { if (j == 2) asm volatile("s_nop 3" : "+v"(d[(u - 1) & 1][0]), "+v"(d[(u - 1) & 1][1])); epi(u - 1, (j - 2) / 3, (j - 2) % 3); }
                                     if (j < 14 || j >= 30) return;
                                     const int g = (j - 14) >> 1;
                                     if (j & 1) { if (u > 0) epi(u - 1, g >> 1, 3 + (g & 1)); }
                                     else if (u == 1 || u == 2) dma_one(rw1, (g & 1) ? c.vr1 + 1024 : c.vr0, (64 * CHUNK(u + 4) + 16 * (g >> 1)) * 8192 + h * 512, c.smb + (u - 1) * SLICE + (8 * (g >> 1) + 2 * w + (g & 1)) * 1024);
                                     else if (u >= 4 && g < 7) xdma(u - 4, (u - 2) * SLICE, g); });
        }
        MFW32(d[0][0], d[0][1]);
#pragma unroll
        for (int e = 0; e < 12; ++e) epi(6, e / 3, e % 3);
#pragma unroll
        for (int e = 0; e < 8; ++e) epi(6, e >> 1, 3 + (e & 1));
    }
    const u4 rout = mksrd(TYPE ? a.UF : a.GF, GU_SZ);
    auto o_out = [&](int mm) { return mm > 13 ? OOB : h * GU_H + (TYPE ? (2 * mm + (l31 >> 4)) * 1024 : mm * 1024 + (l31 >> 4) * 14336) + (hi * 16 + (l31 & 15)) * 16; };
    const int o_st = o_out(m);
    const char LAS* xb[4];
#pragma unroll
    for (int k = 0; k < 4; ++k) xb[k] = c.sm + l31 * 896 + ((2 * k + hi) ^ ((l31 >> 1) & 7)) * 16;
    const int o_pf = (w == 1 && lane < 56) ? (vb >> 3) * 896 + lane * 16 : OOB;
    unsigned ring = 0x10432;
    auto mm1 = [&](f16v& acc, const u4& wt, u4 f, bool first) __attribute__((always_inline)) {
        if (TYPE) { if (first) mV_va0(acc, f, wt); else mV_va(acc, f, wt); } else { if (first) mV_av0(acc, wt, f); else mV_av(acc, wt, f); } };
    u4 ph;
    auto half = [&](const f16v& x, int x8, int st, int vo, int so) __attribute__((always_inline)) {
        if (st == 0) { ph = pack8(x, x8); PIN(ph); } else st128(ph, rout, vo, so + x8 * 512); };
    auto seg = [&](auto first_, auto par_, int i) __attribute__((always_inline)) {
        constexpr int FI = decltype(first_)::value, PAR = decltype(par_)::value;
        if (FI == 0) waitvm<11>(); else waitvm<8>();
        bar();
        if (FI == 0) st32(tag + 6, rhb, o_fl, 0);
        dma_one(rx, o_pf, (i + PF_AHEAD) * 28672, c.smb + 4 * SLICE + 28672 + w * 1024);
        const int r0 = (ring & 7) * SLICE, r3 = ((ring >> 12) & 7) * SLICE;
        ring = (ring >> 4) | ((ring & 7) << 16);
        pipe<28, 4>([&](int k) { return *(const u4 LAS*)(xb[k & 3] + r0 + (k >> 2) * 128); },
                    [&](int k, u4 f) { mm1(d[PAR][0], wk[k], f, k == 0); },
                    [&](int k) { if (FI != 0 && k >= 1 && k < 5) { if (k == 1) asm volatile("s_nop 5" : "+v"(d[PAR ^ 1][0])); half(d[PAR ^ 1][0], (k - 1) >> 1, (k - 1) & 1, o_st, (i - 1) * 28672); }
                                 if (k == (FI ? MID_K : MID_K - 6)) { asm volatile("s_waitcnt vmcnt(%0)\n\ts_barrier" :: "i"(FI ? MID_K / 3 - 1 : MID_K / 3 - 1) : "memory"); st32(tag + 7 + i, rhb, o_fl, 0); }
                                 if (k >= 6 && k % 3 == 0 && k < 27) xdma(i + 3, r3, k / 3 - 2); });
    };
    seg(IntC<0>{}, IntC<0>{}, 0); seg(IntC<1>{}, IntC<1>{}, 1);
#pragma nounroll
    for (int i = 2; i < NSEG1; i += 2) { seg(IntC<2>{}, IntC<0>{}, i); seg(IntC<2>{}, IntC<1>{}, i + 1); }
    asm volatile("s_nop 11" : "+v"(d[1][0]));
    sst128(pack8(d[1][0], 0), rout, o_st, (NSEG1 - 1) * 28672); sst128(pack8(d[1][0], 1), rout, o_st, (NSEG1 - 1) * 28672 + 512);
    if (sub & 1) { set_flag(c, rhb, O_FW + ((h * 2 + TYPE) * 4 + sub) * 128, tag + 7 + NSEG); return; }
    wait_prog(rhb, lane < 2 ? O_FW + ((h * 2 + TYPE) * 4 + 1 + 2 * lane) * 128 : OOB, tag + 7);
    bar();
    if (w == 0) sst32(tag + 7 + NSEG1, rhb, o_fl, 0);
    const int pfo = lane < 56 ? ((h & 3) * 4 + TYPE * 2 + (sub >> 1)) * 1792 + lane * 16 : OOB;
    if (w == 3) {
        int xp[7];
#pragma unroll
        for (int k = 0; k < 7; ++k) { const int n = 64 * k + lane, row = (n * 1171) >> 16, pos = n - 56 * row; xp[k] = row * 896 + ((pos ^ ((row >> 1) & 7)) * 16) - (k & 1) * 1024; xo[k] -= 16 * 896 + (~k & 1) * 1024; }
        auto segl = [&](auto first_, auto par_, int i) __attribute__((always_inline)) {
            constexpr int FI = decltype(first_)::value, PAR = decltype(par_)::value;
            if (FI) { waitvm<62>(); bar(); }
            const int r0 = (ring & 7) * SLICE, r3 = ((ring >> 12) & 7) * SLICE;
            ring = (ring >> 4) | ((ring & 7) << 16);
            pipe<28, 4>([&](int k) { return *(const u4 LAS*)(xb[k & 3] + r0 + (k >> 2) * 128); },
                        [&](int k, u4 f) { mm1(d[PAR][0], wk[k], f, k == 0); },
                        [&](int k) { if (k == 0) { if (FI) { asm volatile("s_nop 9" : "+v"(d[PAR ^ 1][0])); st128(pack8(d[PAR ^ 1][0], 0), rout, o_st, (i - 1) * 28672); st128(pack8(d[PAR ^ 1][0], 1), rout, o_st, (i - 1) * 28672 + 512); }
                                                   dma_pair(rx, pfo, pfo - 128, (i + PF_AHEAD) * 28672, c.smb + 4 * SLICE + 28672); }
                                     if (k & 1) { const int k0 = (k - 1) % 14, k1 = k % 14; dma_pair(rx, k0 < 7 ? xp[k0] : xo[k0 - 7], k1 < 7 ? xp[k1] : xo[k1 - 7], (i + 3) * 28672 + (k / 14) * 14336, c.smb + r3 + (k - 1) * 1024); } });
        };
        segl(IntC<0>{}, IntC<0>{}, NSEG1); segl(IntC<1>{}, IntC<1>{}, NSEG1 + 1);
#pragma nounroll
        for (int i = NSEG1 + 2; i < NSEG; i += 2) { segl(IntC<1>{}, IntC<0>{}, i); segl(IntC<1>{}, IntC<1>{}, i + 1); }
        waitvm<30>(); bar();
        asm volatile("s_nop 11" : "+v"(d[1][0]));
    } else {
        const int m2 = sub == 0 ? 4 + w : w == 2 ? 7 : 12 + w, t16 = 2 * m2 + (l31 >> 4);
        const int o_s2 = o_out(m2);
        u4 w2[28];
        {
            const u4 rin = mksrd(TYPE ? a.WvoF : a.WqkF, 28 * 16 * 14336);
            const int o_in = (TYPE ? t16 * 16 + h : h * 28 + t16) * 14336 + (hi * 16 + (l31 & 15)) * 16;
#pragma unroll
            for (int k = 0; k < 28; ++k) w2[k] = sld128(rin, o_in, k * 512);
            waitvm<0>();
#pragma unroll
            for (int k = 0; k < 28; ++k) {
                PIN(w2[k]);
                const auto s0 = __builtin_amdgcn_permlane32_swap(w2[k][0], w2[k][1], false, false), s1 = __builtin_amdgcn_permlane32_swap(w2[k][2], w2[k][3], false, false);
                w2[k] = (u4){s0[0], s1[0], s0[1], s1[1]};
                asm volatile("" : "+a"(w2[k]));
            }
        }
        u4 fr[4];
#pragma unroll
        for (int k = 0; k < 4; ++k) fr[k] = *(const u4 LAS*)(xb[k] + (ring & 7) * SLICE);
        auto seg2 = [&](auto first_, auto par_, int i) __attribute__((always_inline)) {
            constexpr int FI = decltype(first_)::value, PAR = decltype(par_)::value;
            const int r0 = (ring & 7) * SLICE, r1 = ((ring >> 4) & 7) * SLICE;
            ring = (ring >> 4) | ((ring & 7) << 16);
#pragma unroll
            for (int k = 0; k < 28; ++k) {
                mm1(d[PAR][0], wk[k], fr[k & 3], k == 0);
                if (k == 21) { asm volatile("s_waitcnt vmcnt(5)\n\ts_barrier" ::: "memory"); st32(tag + (FI ? 6 + i : 7 + NSEG1), rhb, o_fl, 0); }
                if (FI && k >= 3 && k < 11) { if (k == 3) asm volatile("" : "+v"(d[PAR ^ 1][0]), "+v"(d[PAR ^ 1][1])); half(d[PAR ^ 1][k >= 7], ((k - 3) >> 1) & 1, (k - 3) & 1, k >= 7 ? o_s2 : o_st, (i - 1) * 28672); }
                SB();
                mm1(d[PAR][1], w2[k], fr[k & 3], k == 0);
                fr[k & 3] = *(const u4 LAS*)(xb[k & 3] + (k < 24 ? r0 + ((k + 4) >> 2) * 128 : r1));
                SB();
            }
        };
        seg2(IntC<0>{}, IntC<0>{}, NSEG1); seg2(IntC<1>{}, IntC<1>{}, NSEG1 + 1);
#pragma nounroll
        for (int i = NSEG1 + 2; i < NSEG; i += 2) { seg2(IntC<1>{}, IntC<0>{}, i); seg2(IntC<1>{}, IntC<1>{}, i + 1); }
        MFW32(d[1][0], d[1][1]);
        sst128(pack8(d[1][1], 0), rout, o_s2, 63 * 28672); sst128(pack8(d[1][1], 1), rout, o_s2, 63 * 28672 + 512);
    }
    sst128(pack8(d[1][0], 0), rout, o_st, 63 * 28672); sst128(pack8(d[1][0], 1), rout, o_st, 63 * 28672 + 512);
    set_flag(c, rhb, O_FW + ((h * 2 + TYPE) * 4 + sub) * 128, tag + 7 + NSEG);
}

DI void x_image(const SideCtx& c, u4 rx, int s) {
#pragma unroll
    for (int k = 0; k < 7; ++k) {
        const int n = 64 * (7 * (c.wave & 1) + k) + c.lane, row = (n * 1171) >> 16, pos = n - 56 * row;
        dma_one(rx, row * 896 + ((pos ^ ((row >> 1) & 7)) * 16), (s * 32 + 16 * (c.wave >> 1)) * 896, c.smb + 40960 + (7 * c.wave + k) * 1024);
    }
}
DI void e_task(unsigned tag, int wave, int s, int hg) {
    CArgs& a = get_args();
    const SideCtx c = make_ctx(wave);
    const int lane = c.lane, hi = lane >> 5, l31 = lane & 31;
    s = __builtin_amdgcn_readfirstlane(s); hg = __builtin_amdgcn_readfirstlane(hg); asm volatile("" : "+s"(s), "+s"(hg));
    const int sh = s * 16 + 4 * hg + c.wave;
    const u4 rhb = mksrd(a.HB, HB_SZ), rg = mksrd(a.GF, GU_SZ);
    bar();
    x_image(c, mksrd(a.X, 2048 * 896), s);
    wait_prog(rhb, lane < 4 ? O_FW + ((4 * hg + c.wave) * 8 + lane) * 128 : OOB, tag + 8 + s);
    u4 g[28];
    const int o_g = (l31 >> 4) * 14336 + (hi * 16 + (l31 & 15)) * 16;
#pragma unroll
    for (int k = 0; k < 28; ++k) {
        asm volatile("s_nop 4\n\tbuffer_load_dwordx4 %0, %1, %2, %3 offen sc1" : "=a"(g[k]) : "v"(o_g), "s"(rg), "s"((4 * hg + c.wave) * GU_H + s * 28672 + k * 512) : "memory");
        if (k % 7 == 6) asm volatile("s_sleep %0" :: "i"(P_ESLP));
    }
    bar();
    f16v sx;
    pipe<28, 4>([&](int k) { const char LAS* b = c.sm + 40960 + l31 * 896 + hi * 8 + (k >> 2) * 128;
                             const u2 c0 = *(const u2 LAS*)(b + ((2 * (k & 3)) ^ ((l31 >> 1) & 7)) * 16), c1 = *(const u2 LAS*)(b + ((2 * (k & 3) + 1) ^ ((l31 >> 1) & 7)) * 16);
                             return (u4){c0[0], c0[1], c1[0], c1[1]}; },
                [&](int k, u4 f) { if (k == 0) WAITV(21, "+a"(g[0]), "+a"(g[1]), "+a"(g[2]), "+a"(g[3]), "+a"(g[4]), "+a"(g[5]), "+a"(g[6]));
                                   if (k == 7) WAITV(14, "+a"(g[7]), "+a"(g[8]), "+a"(g[9]), "+a"(g[10]), "+a"(g[11]), "+a"(g[12]), "+a"(g[13]));
                                   if (k == 14) WAITV(7, "+a"(g[14]), "+a"(g[15]), "+a"(g[16]), "+a"(g[17]), "+a"(g[18]), "+a"(g[19]), "+a"(g[20]));
                                   if (k == 21) WAITV(0, "+a"(g[21]), "+a"(g[22]), "+a"(g[23]), "+a"(g[24]), "+a"(g[25]), "+a"(g[26]), "+a"(g[27]));
                                   u4 y = {f[0], f[2], f[1], f[3]}; asm volatile("s_nop 1" : "+v"(y));
                                   if (k == 0) mV_av0(sx, g[k], y); else mV_av(sx, g[k], y); },
                [&](int) {});
    asm volatile("s_nop 11" : "+v"(sx));
    float m = -3.0e38f;
#pragma unroll
    for (int j = 0; j < 16; ++j) { const int key = (j & 3) + 8 * (j >> 2) + 4 * hi; if (key > l31) sx[j] = -INFINITY; m = fmaxf(m, sx[j]); }
    m = x32max(m);
    float z = 0.f;
#pragma unroll
    for (int j = 0; j < 16; ++j) { sx[j] = __builtin_amdgcn_exp2f(sx[j] - m); z += sx[j]; }
    z = x32add(z);
    const u4 rex = mksrd(a.EX, 64 * 16 * 2048), rmz = mksrd(a.MZX, 64 * 16 * 256);
    sst128(pack8(sx, 0), rex, lane * 16, sh * 2048); sst128(pack8(sx, 1), rex, lane * 16, sh * 2048 + 1024);
    sst64((u2){__float_as_uint(m), __float_as_uint(z)}, rmz, lane < 32 ? lane * 8 : OOB, sh * 256);
    set_flag(c, rhb, O_FE + (s * 4 + hg) * 4, tag + 1);
}
#define P_XSLP 0
DI void slp(int n) {
#pragma nounroll
    for (int i = 0; i < n; i += 8) asm volatile("s_sleep 8");
}
DI void x_task(unsigned tag, int wave, int s, int cb) {
    CArgs& a = get_args();
    const SideCtx c = make_ctx(wave);
    const int lane = c.lane, hi = lane >> 5, l31 = lane & 31, w = c.wave;
    s = __builtin_amdgcn_readfirstlane(s); cb = __builtin_amdgcn_readfirstlane(cb); asm volatile("" : "+s"(s), "+s"(cb));
    const u4 rhb = mksrd(a.HB, HB_SZ);
    const u4 ru = mksrd(a.UF, GU_SZ), rex = mksrd(a.EX, 64 * 16 * 2048), rmz = mksrd(a.MZX, 64 * 16 * 256);
    const u4 rum = mksrd(a.UM, 28 * TU_P), rtf = mksrd(a.TF, 16 * TU_P), rx = mksrd(a.X, 2048 * 896);
    bar();
    x_image(c, rx, s);
    wait_prog(rhb, lane < 16 ? O_FW + (((4 * w + (lane >> 2)) * 2 + 1) * 4 + (lane & 3)) * 128 : lane == 16 ? O_FE + (s * 4 + w) * 4 : OOB, lane < 16 ? tag + 8 + s : tag + 1);
    u4 uf[4][2][2], ex[4][2]; u2 mz[4];
    const int o_u = (l31 >> 4) * 1024 + hi * 256 + (l31 & 15) * 16;
#pragma unroll
    for (int k = 0; k < 4; ++k) {
        const int sh = s * 16 + 4 * w + k;
#pragma unroll
        for (int sg = 0; sg < 2; ++sg) {
            ex[k][sg] = sld128(rex, lane * 16, sh * 2048 + sg * 1024);
#pragma unroll
            for (int nt = 0; nt < 2; ++nt) uf[k][sg][nt] = sld128(ru, o_u, (4 * w + k) * GU_H + (s * 28 + 4 * cb + 2 * nt) * 1024 + sg * 512);
        }
        mz[k] = sld64(rmz, l31 * 8, sh * 256);
        asm volatile("s_sleep %0" :: "i"(P_XSLP));
    }
    waitvm<0>();
    f16v rr[4][2];
#pragma unroll
    for (int k = 0; k < 4; ++k) {
        asm volatile("" : "+v"(mz[k]), "+v"(ex[k][0]), "+v"(ex[k][1]), "+v"(uf[k][0][0]), "+v"(uf[k][0][1]), "+v"(uf[k][1][0]), "+v"(uf[k][1][1]));
        mV_vv0(rr[k][0], uf[k][0][0], ex[k][0]); mV_vv0(rr[k][1], uf[k][0][1], ex[k][0]);
        mV_vv(rr[k][0], uf[k][1][0], ex[k][1]); mV_vv(rr[k][1], uf[k][1][1], ex[k][1]);
        MFW32(rr[k][0], rr[k][1]);
    }
    bar();
    wait_prog(rhb, lane < 8 ? O_FT + (8 * w + lane) * 128 : lane < 12 ? O_FT + (24 + 4 * cb + lane) * 128 : OOB, tag + s + 1);
    u4 tf[28]; u4 um[2][2];
    const int o_tf = (l31 >> 3) * TU_P + hi * 128 + (l31 & 7) * 16, o_um = (l31 >> 4) * TU_P + hi * 256 + (l31 & 15) * 16;
#pragma unroll
    for (int k = 0; k < 28; ++k) {
        asm volatile("s_nop 4\n\tbuffer_load_dwordx4 %0, %1, %2, %3 offen sc1" : "=a"(tf[k]) : "v"(o_tf), "s"(rtf), "s"(4 * w * TU_P + (s + 1) * 7168 + k * 256) : "memory");
        if (k % 7 == 6) slp(s < 58 ? P_XSLP : 0);
    }
#pragma unroll
    for (int k = 0; k < 4; ++k) um[k >> 1][k & 1] = sld128(rum, o_um, (4 * cb + 2 * (k & 1)) * TU_P + (s + 1) * 4096 + w * 1024 + (k >> 1) * 512);
    f16v sm_;
    pipe<28, 4>([&](int k) { return *(const u4 LAS*)(c.sm + 40960 + l31 * 896 + (((2 * (k & 3) + hi) ^ ((l31 >> 1) & 7)) * 16) + (k >> 2) * 128); },
                [&](int k, u4 f) { if (k == 0) WAITV(25, "+a"(tf[0]), "+a"(tf[1]), "+a"(tf[2]), "+a"(tf[3]), "+a"(tf[4]), "+a"(tf[5]), "+a"(tf[6]));
                                   if (k == 7) WAITV(18, "+a"(tf[7]), "+a"(tf[8]), "+a"(tf[9]), "+a"(tf[10]), "+a"(tf[11]), "+a"(tf[12]), "+a"(tf[13]));
                                   if (k == 14) WAITV(11, "+a"(tf[14]), "+a"(tf[15]), "+a"(tf[16]), "+a"(tf[17]), "+a"(tf[18]), "+a"(tf[19]), "+a"(tf[20]));
                                   if (k == 21) WAITV(4, "+a"(tf[21]), "+a"(tf[22]), "+a"(tf[23]), "+a"(tf[24]), "+a"(tf[25]), "+a"(tf[26]), "+a"(tf[27]));
                                   if (k == 0) mV_av0(sm_, tf[k], f); else mV_av(sm_, tf[k], f); },
                [&](int) {});
    waitvm<0>();
    asm volatile("s_nop 11" : "+v"(sm_));
#pragma unroll
    for (int k = 0; k < 4; ++k) PIN(um[k >> 1][k & 1]);
    u2 pm[4]; float scx[4];
#pragma unroll
    for (int k = 0; k < 4; ++k) {
        const f4 sc = {sm_[4 * k], sm_[4 * k + 1], sm_[4 * k + 2], sm_[4 * k + 3]};
        const float mx = __uint_as_float(mz[k][0]);
        float m = fmaxf(x32max(fmaxf(fmaxf(sc[0], sc[1]), fmaxf(sc[2], sc[3]))), mx);
        float e[4];
#pragma unroll
        for (int j = 0; j < 4; ++j) e[j] = __builtin_amdgcn_exp2f(sc[j] - m);
        const float cx = __builtin_amdgcn_exp2f(mx - m);
        const float z = x32add((e[0] + e[1]) + (e[2] + e[3])) + cx * __uint_as_float(mz[k][1]);
        const float inv = __builtin_amdgcn_rcpf(z);
        scx[k] = cx * inv;
        pm[k] = (u2){pk2(e[0] * inv, e[1] * inv), pk2(e[2] * inv, e[3] * inv)};
    }
    f16v d0, d1;
    {
        u4 p0 = {pm[0][0], pm[0][1], pm[2][0], pm[2][1]}, p1 = {pm[1][0], pm[1][1], pm[3][0], pm[3][1]};
        asm volatile("s_nop 1\n\tv_mfma_f32_32x32x16_bf16 %0, %2, %6, 0\n\tv_mfma_f32_32x32x16_bf16 %1, %3, %6, 0\n\tv_mfma_f32_32x32x16_bf16 %0, %4, %7, %0\n\tv_mfma_f32_32x32x16_bf16 %1, %5, %7, %1"
                     : "=&v"(d0), "=&v"(d1) : "v"(um[0][0]), "v"(um[0][1]), "v"(um[1][0]), "v"(um[1][1]), "v"(p0), "v"(p1));
    }
    f16v a0 = (rr[0][0] * scx[0] + rr[1][0] * scx[1]) + (rr[2][0] * scx[2] + rr[3][0] * scx[3]);
    f16v a1 = (rr[0][1] * scx[0] + rr[1][1] * scx[1]) + (rr[2][1] * scx[2] + rr[3][1] * scx[3]);
    asm volatile("s_nop 11" : "+v"(d0), "+v"(d1), "+v"(a0), "+v"(a1));
    a0 += d0; a1 += d1;
#pragma unroll
    for (int g = 0; g < 4; ++g) {
        *(f4 LAS*)(c.sm + w * 8704 + l31 * 272 + (8 * g + 4 * hi) * 4) = (f4){a0[4 * g], a0[4 * g + 1], a0[4 * g + 2], a0[4 * g + 3]};
        *(f4 LAS*)(c.sm + w * 8704 + l31 * 272 + 128 + (8 * g + 4 * hi) * 4) = (f4){a1[4 * g], a1[4 * g + 1], a1[4 * g + 2], a1[4 * g + 3]};
    }
    bar();
    {
        const int t = w * 8 + (lane >> 3), c8 = lane & 7;
        f4 s0 = {0.f, 0.f, 0.f, 0.f}, s1 = s0;
#pragma unroll
        for (int p = 0; p < 4; ++p) { s0 += *(const f4 LAS*)(c.sm + p * 8704 + t * 272 + c8 * 32); s1 += *(const f4 LAS*)(c.sm + p * 8704 + t * 272 + c8 * 32 + 16); }
        sst128((u4){pk2(s0[0], s0[1]), pk2(s0[2], s0[3]), pk2(s1[0], s1[1]), pk2(s1[2], s1[3])}, mksrd(a.out, 2048 * 1792), t * 1792 + c8 * 16, s * 32 * 1792 + 896 + cb * 128);
    }
}

#define P_ASLP 8
#define PA_UT 0
#define PA_TU 67584
#define PA_KH (PA_TU + 35456)
#define PA_KT (PA_KH + 7680)
#define PA_RED (PA_KT + 12288)
#define PA_SC (PA_RED + 36864)
typedef __attribute__((ext_vector_type(2))) _Float16 h2;
typedef __attribute__((ext_vector_type(8))) _Float16 h8;
DI unsigned pkh(f2 v) { return __builtin_bit_cast(unsigned, __builtin_convertvector(v, h2)); }
DI void mH(f16v& c, u4 x, u4 y) { asm volatile("v_mfma_f32_32x32x16_f16 %0, %1, %2, %0" : "+v"(c) : "v"(x), "v"(y)); }
DI void mH0(f16v& c, u4 x, u4 y) { asm volatile("v_mfma_f32_32x32x16_f16 %0, %1, %2, 0" : "=&v"(c) : "v"(x), "v"(y)); }
DI unsigned fold(unsigned m, unsigned x) { return max(max(m, x & 0x7f80u), (x >> 16) & 0x7f80u); }
DI int top_exp(unsigned m) {
#pragma unroll
    for (int sft = 32; sft >= 1; sft >>= 1) m = max(m, (unsigned)__shfl_xor(m, sft));
    return (int)(m >> 7) - 127;
}
DI float bf2f(u16 x) { return __uint_as_float((unsigned)x << 16); }
DI void parta_task(unsigned tag, int vb, int wave, int task, int nt) {
    CArgs& a = get_args();
    const SideCtx c = make_ctx(wave);
    char LAS* sm = c.sm;
    const int lane = c.lane, w = c.wave, tid = w * 64 + lane, hi = lane >> 5, l31 = lane & 31, l15 = lane & 15, kk = lane >> 4;
    task = __builtin_amdgcn_readfirstlane(task); asm volatile("" : "+s"(task));
    int d0 = task * 4;
    char LAS* ut = sm + PA_UT;
    const u4 rpa = mksrd(a.PA, 56 * 32768);
    bar();
    u2 pr[4][4], dsks[4]; unsigned ldts[4];
#pragma unroll
    for (int g = 0; g < 4; ++g) {
        const int d = d0 + (g < nt ? 4 * g : 0);
        ldts[g] = a.Alogdt[d + w]; dsks[g] = *(const u2*)(a.ADsk + d);
        const int n = (d + w) * 256 + 4 * lane;
        pr[g][0] = *(const u2*)(a.AlogA + n); pr[g][1] = *(const u2*)(a.Aim + n); pr[g][2] = *(const u2*)(a.ACr + n); pr[g][3] = *(const u2*)(a.ACi + n);
    }
    SB();
    for (int i = tid; i < 1024; i += 256) { const int ch = i >> 6, m = i & 63; *(u16 LAS*)(ut + ch * 4224 + (m == 0 ? 0 : (2048 + m) * 2)) = 0; }
    if (tid < 155) *(u4 LAS*)(sm + PA_KH + tid * 16) = (u4){0u, 0u, 0u, 0u};
    if (tid < 48) *(u4 LAS*)(sm + PA_TU + ((tid / 6) * 276 + (tid >= 6) + 256 + tid % 6) * 16) = (u4){0u, 0u, 0u, 0u};
    int toff[8];
#pragma unroll
    for (int j = 0; j < 8; ++j) {
        const int p = (int)bfr((float)(tid * 8 + j));
        int col, ar = (p & 255) >> 4;
        if (p < 256) col = p & 15; else if (p < 512) col = 16 + ((p & 15) >> 1); else if (p < 1024) col = 24 + ((p - 512) >> 8) * 4 + ((p & 15) >> 2);
        else if (p < 2048) col = 32 + ((p - 1024) >> 8) * 2 + ((p & 15) >> 3); else col = 40;
        toff[j] = (col * 16 + ar) * 4;
    }
    float qpos[3];
    qpos[0] = (float)l15;
    qpos[1] = l15 < 8 ? (float)(256 + 2 * l15) : (float)(512 + ((l15 - 8) >> 2) * 256 + (l15 & 3) * 4);
    qpos[2] = l15 < 8 ? (float)(1024 + (l15 >> 1) * 256 + (l15 & 1) * 8) : 2048.f;
    const float ppos = (float)(16 * l15);
#pragma nounroll
    for (int rnd = 0; rnd < nt; ++rnd) {
        const bool nld = rnd == 0; const u2 dsk4 = dsks[0];
        const float dt = bfr(expf(bf2f(ldts[0])));
        const int ec = top_exp(fold(fold(fold(fold(0u, pr[0][2][0]), pr[0][2][1]), pr[0][3][0]), pr[0][3][1]));
        {
            char LAS* pt = sm + PA_RED + w * 9216;
#pragma unroll
            for (int i = 0; i < 4; ++i) {
                auto el = [&](int k) { const unsigned x = pr[0][k][i >> 1]; return __uint_as_float((i & 1) ? x & 0xffff0000u : x << 16); };
                const float ar = -bfr(expf(el(0)));
                *(f4 LAS*)(pt + (lane + 64 * i) * 16) = (f4){ar * dt * LOG2E, el(1) * dt * 0.15915494309189535f, ldexpf(el(2), 3 - ec), ldexpf(el(3), 3 - ec)};
            }
            f4 acc[3] = {{0.f, 0.f, 0.f, 0.f}, {0.f, 0.f, 0.f, 0.f}, {0.f, 0.f, 0.f, 0.f}};
            auto cexp = [](f2 z, float p) __attribute__((always_inline)) {
                const f2 x = z * p + (f2){10.f, 0.f}; const float ex = __builtin_amdgcn_exp2f(x[0]), rv = __builtin_amdgcn_fractf(x[1]);
                return (f2){__builtin_amdgcn_cosf(rv), __builtin_amdgcn_sinf(rv)} * ex; };
            f4 zc[4];
#pragma unroll
            for (int j = 0; j < 4; ++j) zc[j] = *(const f4 LAS*)(pt + (4 * j + kk) * 16);
            const __amdgpu_buffer_rsrc_t ru = __builtin_amdgcn_make_buffer_rsrc((void*)a.Au, 0, 2048 * 896, 0x00020000);
#pragma nounroll
            for (int s2 = 0; s2 < 8; ++s2) {
                u4 uv[2];
                if (nld) { uv[0] = __builtin_amdgcn_raw_buffer_load_b128(ru, tid * 896, s2 * 256 * 896 + d0 * 2, 0); uv[1] = __builtin_amdgcn_raw_buffer_load_b128(ru, tid * 896 + 16, s2 * 256 * 896 + d0 * 2, 0); }
#pragma unroll
                for (int hf = 0; hf < 2; ++hf) {
                    u4 fa, fb; f2 zz[4];
#pragma unroll
                    for (int j = 0; j < 4; ++j) {
                        const f4 z = zc[j]; zz[j] = (f2){z[0], z[1]};
                        const f2 e = cexp(zz[j], ppos);
                        fa[j] = pkh((f2){z[2] * e[0] - z[3] * e[1], -(z[2] * e[1] + z[3] * e[0])});
                        zc[j] = *(const f4 LAS*)(pt + (32 * s2 + 16 * hf + 16 + 4 * j + kk) * 16);
                    }
#pragma unroll
                    for (int t = 0; t < 3; ++t) {
#pragma unroll
                        for (int j = 0; j < 4; ++j) fb[j] = pkh(cexp(zz[j], qpos[t]));
                        acc[t] = __builtin_amdgcn_mfma_f32_16x16x32_f16(__builtin_bit_cast(h8, fa), __builtin_bit_cast(h8, fb), acc[t], 0, 0, 0);
                        asm volatile("" : "+v"(acc[t]));
                    }
                }
                if (nld) {
#pragma unroll
                    for (int ch = 0; ch < 16; ++ch) *(u16 LAS*)(ut + ch * 4224 + (2048 - tid - 256 * s2) * 2) = (u16)(uv[ch >> 3][(ch >> 1) & 3] >> (16 * (ch & 1)));
                }
            }
#pragma unroll
            for (int t = 0; t < 3; ++t) *(f4 LAS*)(sm + PA_KT + w * 3072 + ((t * 16 + l15) * 16 + 4 * kk) * 4) = acc[t] * 0x1p-18f;
        }
        bar();
        {
            unsigned m = 0;
#pragma unroll
            for (int i = 0; i < 5; ++i) { const u4 x = *(const u4 LAS*)(ut + w * 4224 + min(lane + 64 * i, 256) * 16); m = fold(fold(fold(fold(m, x[0]), x[1]), x[2]), x[3]); }
            const int eu = top_exp(m);
            if (lane == 0) *(u4 LAS*)(sm + PA_SC + w * 16) = (u4){__float_as_uint(2.f * dt), (unsigned)(ec - 5 + eu - 14), (unsigned)(14 - eu), 0u};
        }
        bar();
        auto emit = [&](int ch) __attribute__((always_inline)) {
            f4 s0 = {0.f, 0.f, 0.f, 0.f}, s1 = s0;
#pragma unroll
            for (int p = 0; p < 4; ++p) { const char LAS* q_ = sm + PA_RED + p * 9216 + (tid >> 2) * 144 + (tid & 3) * 32; s0 += *(const f4 LAS*)q_; s1 += *(const f4 LAS*)(q_ + 16); }
            const u4 x0 = *(const u4 LAS*)(ut + ch * 4224 + (255 - tid) * 16); const unsigned x1 = *(const unsigned LAS*)(ut + ch * 4224 + (256 - tid) * 16);
            const float dsk = __uint_as_float((ch & 2 ? dsk4[1] : dsk4[0]) >> (16 * (ch & 1)) << 16);
            const u2 sc = *(const u2 LAS*)(sm + PA_SC + ch * 16);
            float yv[8];
#pragma unroll
            for (int j = 0; j < 8; ++j) { const unsigned uw = j == 0 ? x1 : x0[(8 - j) >> 1]; yv[j] = bfr(ldexpf((j < 4 ? s0[j & 3] : s1[j & 3]) * __uint_as_float(sc[0]), (int)sc[1])) + bfr(__uint_as_float((j & 1) ? uw & 0xffff0000u : uw << 16) * dsk); }
            sst128((u4){pk2(yv[0], yv[1]), pk2(yv[2], yv[3]), pk2(yv[4], yv[5]), pk2(yv[6], yv[7])}, rpa, tid * 16, (d0 + ch) * 4096);
        };
#pragma nounroll
        for (int cc = 0; cc < 4; ++cc) {
            const int ch = cc;
            {
                float kv[8];
#pragma unroll
                for (int j = 0; j < 8; ++j) kv[j] = *(const float LAS*)(sm + PA_KT + cc * 3072 + toff[j]);
                *(u4 LAS*)(sm + PA_KH + (31 + (tid >> 2)) * 80 + (tid & 3) * 16) = (u4){pkh((f2){kv[0], kv[1]}), pkh((f2){kv[2], kv[3]}), pkh((f2){kv[4], kv[5]}), pkh((f2){kv[6], kv[7]})};
            }
            {
                const int i = tid;
                const u4 x0 = *(const u4 LAS*)(ut + ch * 4224 + i * 16), x1 = *(const u4 LAS*)(ut + ch * 4224 + i * 16 + 16);
                const int su = *(const int LAS*)(sm + PA_SC + ch * 16 + 8);
                unsigned r[8] = {x0[0], x0[1], x0[2], x0[3], x1[0], x1[1], x1[2], x1[3]};
#pragma unroll
                for (int j = 0; j < 8; ++j) r[j] = pkh((f2){ldexpf(__uint_as_float(r[j] << 16), su), ldexpf(__uint_as_float(r[j] & 0xffff0000u), su)});
#pragma unroll
                for (int cp = 0; cp < 8; ++cp) {
                    u4 o;
#pragma unroll
                    for (int j = 0; j < 4; ++j) o[j] = (cp & 1) ? __builtin_amdgcn_alignbit(r[(cp >> 1) + j + 1], r[(cp >> 1) + j], 16) : r[(cp >> 1) + j];
                    *(u4 LAS*)(sm + PA_TU + (cp * 276 + (cp ? 1 : 0) + i) * 16) = o;
                }
                if (tid == 255) *(u4 LAS*)(sm + PA_TU + 256 * 16) = (u4){r[4], r[5], r[6], r[7]};
            }
            if (cc) emit(cc - 1);
            bar();
            {
                f16v y0, y1;
                const int m00 = 2048 - l31 + 8 * hi, cp = m00 & 7;
                const char LAS* ab = sm + PA_TU + (cp * 276 + (cp ? 1 : 0) + (m00 >> 3)) * 16 - 63 * 64;
                const char LAS* bb = sm + PA_KH + l31 * 80 + hi * 16;
                const char LAS* a1w = ab - w * 64; const char LAS* b1w = bb - w * 80;
                const char LAS* a2w = ab + w * 64; const char LAS* b2w = bb + w * 80;
                u4 fr[3][3];
                auto rdg = [&](int g, int sl) __attribute__((always_inline)) {
                    const int i = (g & 15) >> 1, ks = g & 1;
                    if (g < 16) { fr[sl][0] = *(const u4 LAS*)(b1w + (31 - 4 * i) * 80 + ks * 32); fr[sl][1] = *(const u4 LAS*)(a1w + (63 - 4 * i) * 64 + ks * 32); fr[sl][2] = *(const u4 LAS*)(a1w + (31 - 4 * i) * 64 + ks * 32); }
                    else { fr[sl][0] = *(const u4 LAS*)(b2w + (32 + 4 * i) * 80 + ks * 32); fr[sl][2] = *(const u4 LAS*)(a2w + (32 + 4 * i) * 64 + ks * 32); }
                };
                rdg(0, 0); rdg(1, 1); SB();
#pragma unroll
                for (int g = 0; g < 32; ++g) {
                    const int sl = g % 3;
                    if (g + 2 < 32) rdg(g + 2, (g + 2) % 3);
                    SB();
                    if (g == 0) { mH0(y0, fr[sl][1], fr[sl][0]); mH0(y1, fr[sl][2], fr[sl][0]); }
                    else { if (g < 16) mH(y0, fr[sl][1], fr[sl][0]); mH(y1, fr[sl][2], fr[sl][0]); }
                    SB();
                }
                MFW32(y0, y1);
#pragma unroll
                for (int g = 0; g < 4; ++g) {
                    *(f4 LAS*)(sm + PA_RED + w * 9216 + l31 * 144 + (8 * g + 4 * hi) * 4) = (f4){y0[4 * g], y0[4 * g + 1], y0[4 * g + 2], y0[4 * g + 3]};
                    *(f4 LAS*)(sm + PA_RED + w * 9216 + (32 + l31) * 144 + (8 * g + 4 * hi) * 4) = (f4){y1[4 * g], y1[4 * g + 1], y1[4 * g + 2], y1[4 * g + 3]};
                }
            }
            bar();
        }
        emit(3);
        bar();
        d0 += 4; ut += 16896;
#pragma unroll
        for (int g = 0; g < 3; ++g) {
            ldts[g] = ldts[g + 1]; dsks[g] = dsks[g + 1];
#pragma unroll
            for (int k = 0; k < 4; ++k) pr[g][k] = pr[g + 1][k];
        }
    }
    waitvm<0>(); bar();
    if (w == 0) sst32(tag + 1, mksrd(a.HB, HB_SZ), lane < nt ? O_FPA + (task + lane) * 4 : OOB, 0);
}
DI void pa_out(unsigned tag, int wave, int t) {
    CArgs& a = get_args();
    const SideCtx c = make_ctx(wave);
    const int lane = c.lane, tid = c.wave * 64 + lane;
    t = __builtin_amdgcn_readfirstlane(t); asm volatile("" : "+s"(t));
    const u4 rpa = mksrd(a.PA, 56 * 32768), ro = mksrd(a.out, 2048 * 1792);
    wait_prog(mksrd(a.HB, HB_SZ), lane < 16 ? O_FPA + (t & ~7) * 8 + lane * 4 : OOB, tag + 1);
    u4 v[8];
#pragma unroll
    for (int i = 0; i < 8; ++i) v[i] = sld128(rpa, (tid >> 5) * 4096 + (tid & 31) * 16, ((t >> 3) * 64 + i * 8) * 4096 + (t & 7) * 512);
    WAITV(0, "+v"(v[0]), "+v"(v[1]), "+v"(v[2]), "+v"(v[3]), "+v"(v[4]), "+v"(v[5]), "+v"(v[6]), "+v"(v[7]));
    bar();
#pragma unroll
    for (int i = 0; i < 8; ++i) *(u4 LAS*)(c.sm + (i * 8 + (tid >> 5)) * 528 + (tid & 31) * 16) = v[i];
    bar();
#pragma unroll
    for (int k = 0; k < 8; ++k) {
        const char LAS* p = c.sm + (tid & 7) * 8 * 528 + ((tid >> 3) + 32 * k) * 2;
        u4 o;
#pragma unroll
        for (int j = 0; j < 4; ++j) o[j] = (unsigned)*(const u16 LAS*)(p + (2 * j) * 528) | ((unsigned)*(const u16 LAS*)(p + (2 * j + 1) * 528) << 16);
        sst128(o, ro, (tid >> 3) * 1792 + (tid & 7) * 16, ((t & 7) * 256 + 32 * k) * 1792 + (t >> 3) * 128);
        asm volatile("s_sleep %0" :: "i"(P_ASLP));
    }
}
#define PROBE_N 3
#define L_TAB 0
#define L_CLK 32768
#define L_MEM 33792
#define L_WRK 49152
DI void edge(unsigned& ce, unsigned& ne) {
    const unsigned n0 = rt_now(); u64 t, c; int n = 0;
#pragma nounroll
    do { asm volatile("s_memrealtime %0\n\ts_memtime %1\n\ts_waitcnt lgkmcnt(0)" : "=&s"(t), "=&s"(c) :: "memory"); } while ((unsigned)t == n0 && ++n < 64);
    ce = (unsigned)c - 30u; ne = (unsigned)t;
}
DI unsigned srdw(const void* p, unsigned bytes, int i) { const u64 x = (u64)p; return i == 0 ? (unsigned)x : i == 1 ? (unsigned)(x >> 32) & 0xffffu : i == 2 ? bytes : 0x00020000u; }
#define RUN(r) asm volatile("v_mov_b32 v255, %0\n\ts_getpc_b64 vcc\n.Lfk_from_" #r ":\n\ts_add_u32 vcc_lo, vcc_lo, fk_role_" #r "-.Lfk_from_" #r "\n\ts_addc_u32 vcc_hi, vcc_hi, 0\n\ts_setpc_b64 vcc" :: "v"(tab) : "memory", "vcc", "scc", "s99", "v255", "a255")
DI unsigned probe(u4 r, int id, int xh) {
    const int lane = fresh_lane(), o = xh * (2 << 20) + O_PROBE + id * 128; unsigned best = 1016;
#pragma nounroll
    for (int rep = 0; rep < PROBE_N; ++rep) {
        sst32(rep, r, lane < 4 ? o + lane * 8192 : OOB, 0); waitvm<0>();
#pragma nounroll
        for (int k = 0; k < 4; ++k) {
            const int kk = (k + id) & 3, ol = lane == 0 ? o + kk * 8192 : OOB;
            const unsigned t0 = now32(); unsigned v = sld32(r, ol, 0); WAITV(0, "+v"(v));
            const unsigned d = now32() - t0;
            if (lane == kk) best = min(best, d);
        }
    }
    return best;
}
DI void ring_init(unsigned tag, int W, int b, unsigned best) {
    CArgs& a = get_args();
    const int lane = fresh_lane();
    const u4 r = mksrd(a.HB, HB_SZ);
    sst32(tag + 1 + (best >> 2), r, lane < 4 ? O_PAR + (b * 4 + W) * 16 + lane * 4 : OOB, 0);
    u4 m0[2];
#pragma unroll
    for (int k = 0; k < 2; ++k) {
        m0[k] = *(const u4*)(a.mem0 + (lane & 7) * 448 + min(2 * W + k, 6) * 64 + (lane >> 3) * 8);
        m0[k] = (b & 1) ? (u4){FF, FF, FF, FF} : (u4){san(m0[k][0]), san(m0[k][1]), san(m0[k][2]), san(m0[k][3])};
    }
    if (b == 0)
        for (int k = 0; k < 3; ++k) sst128((u4){REC_A, REC_B, REC_A, REC_B} + (W + k + lane == 0 ? REC_0 : 0u), r, O_REC + lane * 16, (W * 3 + k) * 1024);
    wait_prog(r, O_PAR + lane * 4, tag + 1);
    unsigned la = sld32(r, O_PAR + lane * 4, 0); WAITV(0, "+v"(la));
    la = ((la - tag) << 2) | (lane & 3);
#pragma unroll
    for (int sft = 16; sft >= 1; sft >>= 1) la = min(la, (unsigned)__shfl_xor(la, sft));
    const unsigned pa = __builtin_amdgcn_readlane(la, 0) & 3, pb = __builtin_amdgcn_readlane(la, 32) & 3;
    const int base = (b >> 1) * (2 << 20) + (b & 1) * 524288 + ((((b & 2) ? pb : pa) ^ ((b & 1) * 2)) << 13) + W * 2048;
    if (b == 0 && W == 0) sst32(tag + 1 + pa + 4 * pb, r, lane == 0 ? O_WIN : OOB, 0);
#pragma nounroll
    for (int n = 0; n < 6; ++n) {
        const int o = base + (n < 4 ? n * 32768 : 16384 + (n - 4) * 32768);
#pragma unroll
        for (int k = 0; k < 2; ++k) { sst128((n & 2) ? (u4){FF, FF, FF, FF} : m0[k], r, lane * 16, o + k * 1024); sst128((u4){FF, FF, FF, FF}, r, lane * 16, o + k * 1024 + (1 << 20)); }
    }
    waitvm<0>(); bar();
    if (W == 0) sst32(tag + 1 + pa + 4 * pb, r, lane == 0 ? O_RDY + b * 128 : OOB, 0);
}
DI void chain_role(unsigned tag, int vb, int W, int role, int id, int xh) {
    CArgs& a = get_args();
    char LAS* sm = (char LAS*)smem_;
    const unsigned smb = (unsigned)(size_t)sm;
    const int lane = fresh_lane(), q = lane >> 4, l15 = lane & 15, i8 = lane & 7, hb = (lane >> 3) & 1;
    unsigned LAS* tb = (unsigned LAS*)(sm + L_TAB + W * 8192) + lane;
    const int wave = W;
    unsigned c1, n1;
    if (role && W == 0) dma_one(mksrd(role == 1 ? a.UM : a.TF, 28 * TU_P), (role == 1 ? id : id >> 1) * TU_P, 0, smb + 140 * 1024);
    const int cp = xh * 32768;
    const void *p1, *p2 = a.UF; unsigned x1 = 0, x2 = 0, po = 0; int fw, wk;
    if (role == 0) {
        const int part = id & 1, h = id >> 1, rr = l15 >> 2, c = lane & 3;
        const int bq = q == 0 ? 0 : q == 1 ? 528 : q == 2 ? 1152 : 1680;
        tb[8 * 64] = O_MEM + cp + lane * 16 + part * 4096; tb[9 * 64] = O_MEM + cp + lane * 16 + (1 - part) * 4096; tb[10 * 64] = O_MEM + cp + lane * 16 + 3072;
        tb[11 * 64] = smb + L_WRK + bq + perm16(l15) * 8 + (W == 3 ? 3 * 2304 + part * 256 : 0); tb[14 * 64] = smb + L_WRK + bq + perm16(l15) * 8 + 3 * 2304 + (1 - part) * 256;
        tb[12 * 64] = smb + L_WRK + (c == 0 ? 0 : c == 2 ? 528 : c == 1 ? 1152 : 1680) + (16 * (q >> 1) + 4 * (q & 1) + 8 * (rr >> 1) + (rr & 1)) * 8;
        const int op = O_SMM + id * 256 + hb * 128 + (4 * (q & 1) + 2 * (q >> 1)) * 16 + i8 * 4;
        tb[13 * 64] = op + cp; tb[15 * 64] = op + (cp ^ 32768);
        int tile, chunk;
        if (W < 3) {
            const int i = lane < 28 ? lane : 0;
            const int pk = (int)((i < 12 ? SEQ_LO : i < 24 ? SEQ_MI : SEQ_HI) >> (5 * (i % 12))), p = pk & 7, k = (pk >> 3) & 3;
            const int n = 3 * (p & 3) + W;
            tile = 4 * (4 * (p < 4 ? part : 1 - part) + (n >> 2)) + (n & 3); chunk = k < 3 ? 4 * part + (W + k) % 3 : 3;
        } else {
            const int p = lane < 2 ? 2 + lane : (lane - 2) & 3, k = lane < 2 || lane >= 14 ? 3 : (lane - 2) >> 2;
            tile = 12 + (p < 2 ? 2 * part + p : 2 * (1 - part) + p - 2); chunk = k < 3 ? 4 * part + k : 3;
        }
        wk = ((h * 28 + tile) * 7 + chunk) * 2048; fw = h * 8 + (lane & 3);
        p1 = a.WqkF; x1 = part + 1;
    } else if (role == 1) {
        const int hc = 4 * W + ((q & 1) << 1) + (q >> 1), k = i8 - 4 * hb;
        const int ocp = W == 0 ? O_MEM : W == 3 ? O_MEM + 32768 : O_MEMX + (W - 1) * 32768;
        tb[8 * 64] = O_MEM + cp + lane * 16; tb[9 * 64] = O_MEM + cp + lane * 16 + 4096;
        tb[10 * 64] = O_SMM + cp + hc * 512 + l15 * 16; tb[11 * 64] = O_MZ + hc * 128 + l15 * 8;
        tb[12 * 64] = RXW(id >> 1) + (id & 1) * 4096 + hc * 256 + l15 * 16;
        tb[13 * 64] = ocp + id * 256 + lane * 4;
        tb[14 * 64] = k == 0 ? 0xffffu : k == 1 ? 0xffff0000u : 0u; tb[15 * 64] = k == 2 ? 0xffffu : k == 3 ? 0xffff0000u : 0u;
        tb[16 * 64] = smb + L_WRK + W * 512 + lane * 8;
        tb[17 * 64] = smb + L_WRK + ((lane & 32) + (lane & 2) * 8 + (lane & 1) * 8 + ((lane >> 2) & 7)) * 8;
        tb[18 * 64] = id * TU_P + W * 1024 + lane * 16;
        tb[20 * 64] = smb + L_MEM + lane * 16;
        tb[21 * 64] = lane == 0 ? O_FT + (32 + id) * 128 : OOB;
        tb[23 * 64] = (q == 0 ? O_MEM : q == 3 ? O_MEM + 32768 : O_MEMX + (q - 1) * 32768) + id * 256 + l15 * 16;
        const bool lead = id == 14 * xh;
        tb[22 * 64] = lane != 0 ? OOB : W == 2 ? (lead ? O_REC + xh * 8 : OOB) : id == 0 ? O_EPOCH : OOB;
        const int ord = ((W == 0 ? 0x6543210 : W == 1 ? 0x3106542 : W == 2 ? 0x4321065 : 0x6542103) >> (4 * (lane % 7))) & 7;
        wk = ((id * 16 + 4 * W + (lane / 7 & 3)) * 7 + ord) * 2048; fw = (4 * W + (lane & 3)) * 8 + 4 + (id >> 3);
        p1 = a.WvoF; p2 = a.UM; po = OFF_P;
    } else {
        const int h = id >> 1, half = id & 1, pt = 4 * W + q;
        tb[8 * 64] = O_MEMX + cp + lane * 16; tb[9 * 64] = O_MEMX + cp + lane * 16 + 4096;
        tb[10 * 64] = (W < 3 || (q & 1) == 0) ? RXW(half * 7 + 2 * W + (q & 1)) + (q >> 1) * 4096 + h * 256 + l15 * 16 : OOB;
        tb[11 * 64] = ((lane & 3) == 0 && half == 0) ? O_MZ + h * 128 + (lane >> 2) * 8 : OOB;
        tb[12 * 64] = smb + L_WRK + lane * 8;
        tb[13 * 64] = smb + L_WRK + ((q >> 1) * 32 + (lane & 1) * 16 + 4 * (q & 1) + (l15 >> 2)) * 8;
        tb[14 * 64] = pt < 14 ? RXW(half * 7 + (pt >> 1)) + (pt & 1) * 4096 + h * 256 + l15 * 16 : (pt == 14 && half == 0 && l15 < 8) ? O_MZ + h * 128 + l15 * 16 : OOB;
        tb[15 * 64] = O_FW + (h * 8 + i8) * 128;
        tb[16 * 64] = smb + L_MEM + lane * 16;
        tb[17 * 64] = smb + L_WRK + 1024 + W * 2048 + ((q >> 1) * 16 + perm16(l15)) * 16 + (q & 1) * 8;
        tb[18 * 64] = smb + L_WRK + 1024 + W * 2048 + (8 * q + (l15 >> 2) + 32 * ((lane >> 1) & 1)) * 16 + (lane & 1) * 8;
        const int otf = h * TU_P + half * 3584 + W * 1024 + (4 * hb + q) * 128 + i8 * 16;
        tb[19 * 64] = otf; tb[21 * 64] = hb ? OOB : otf;
        tb[20 * 64] = lane != 0 ? OOB : W == 3 ? O_RDY + (4 + id) * 128 : O_FT + id * 128;
        if (W == 3)
            for (int k = 0; k < 3; ++k) tb[(17 + k) * 64] = RXW(half * 7 + 2 * k + (q >> 1)) + (q & 1) * 4096 + h * 256 + l15 * 16;
        wk = ((h * 28 + half * 14 + 4 * W) * 7 + (lane < 42 ? lane : 0)) * 2048; fw = h * 8 + (lane & 3);
        p1 = W == 3 ? a.GF : a.WqkF; x1 = h * GU_H; x2 = h * GU_H + (half * 14 + 4 * W) * 1024; po = OFF_X;
    }
    tb[0 * 64] = (lane & 8) ? 0xEEEE : 0x4444; tb[1 * 64] = lane * 16; tb[2 * 64] = wk; tb[3 * 64] = ((vb >> 3) * 16 + (vb & 3) * 4 + W) * 1280 + lane * 4;
    tb[4 * 64] = O_FW + fw * 128; tb[5 * 64] = O_RDY + (role == 1 && lane < 36 ? lane : lane & 3) * 128; tb[6 * 64] = role == 1 ? O_SIDE + (lane & 31) * 4 : O_WIN; tb[7 * 64] = O_REC;
    edge(c1, n1);
    const unsigned f = 5900;
    const unsigned k0 = n1 * 16u / LAT_R;
    const unsigned sz1 = (role == 2 && W == 3) ? GU_SZ : 28 * 16 * 14336, sz2 = role == 1 ? 28 * TU_P : GU_SZ;
    unsigned sv;
    if (lane < 4) sv = srdw(a.HB, HB_SZ, lane); else if (lane < 8) sv = srdw(p1, sz1, lane - 4); else if (lane < 12) sv = srdw(p2, sz2, lane - 8); else if (lane < 16) sv = srdw(a.TF, 16 * TU_P, lane - 12);
    else if (lane < 20) sv = srdw(a.TS, 512 * 1280, lane - 16);
    else sv = lane == 20 ? k0 * LAT_R : lane == 21 ? LAT_R : lane == 24 ? po : lane == 26 ? tag : lane == 35 ? k0 & 1 : lane == 29 ? x1 : lane == 30 ? x2
            : lane == 33 ? (unsigned)TOL_C : lane == 34 ? smb + L_CLK : 0u;
    tb[31 * 64] = sv;
    if (W == 0 && lane < 2) *(u4 LAS*)(sm + L_CLK + lane * 16) = lane == 0 ? (u4){n1 * 16u, c1, f, 0u} : (u4){0u, 0u, 0u, 0u};
    bar();
    const unsigned tab = smb + L_TAB + W * 8192 + lane * 4;
    if (role == 0) { if (W == 0) RUN(S0); else if (W == 1) RUN(S1); else if (W == 2) RUN(S2); else RUN(S3); }
    else if (role == 1) { if (W == 0) RUN(P0); else if (W == 1) RUN(P1); else if (W == 2) RUN(P2); else RUN(P3); }
    else { if (W == 0) RUN(X0); else if (W == 1) RUN(X1); else if (W == 2) RUN(X2); else RUN(X3); }
}

#define XCC_XOR 5
#define HD(i) ((i) & 3)
#define SUB(i) ((i) >> 3)
#define PF_MASK 5
#define PF_ROWS 864
#define PAO_AT 118
__global__ __launch_bounds__(256, 1) void fused_kernel(Args a_) {
    CArgs& a = get_args();
    const int wave = __builtin_amdgcn_readfirstlane(threadIdx.x >> 6);
    const u4 rhb = mksrd(a.HB, HB_SZ);
    unsigned ep = 0;
    if (wave == 0) ep = ld32(rhb, O_EPOCH, 0);
    else {
        u64 pc; unsigned len;
        asm volatile("s_getpc_b64 %0\nfk_code_lo:\n\ts_mov_b32 %1, fk_code_hi-fk_code_lo" : "=s"(pc), "=s"(len));
        const u4 rc = mksrd((const void*)pc, len & ~15u);
        const int vo = (((blockIdx.x >> 3) * 3 + wave - 1) * 2) * 1024 + (threadIdx.x & 63) * 16;
#pragma unroll
        for (int i = 0; i < 2; ++i) dma_one(rc, vo + i * 1024, 0, (unsigned)(size_t)(char LAS*)smem_ + 147456 + (wave * 3 + i) * 1024);
    }
    const int xcc = (__builtin_amdgcn_s_getreg((31 << 11) | 20) & 7) ^ XCC_XOR;
    int vb = xcc + (blockIdx.x & ~7);
    unsigned tag, best = 1016;
    if ((vb & 0xf5) == 0xf1 && wave) best = probe(rhb, ((vb >> 3) & 1) * 4 + wave, (vb >> 1) & 1);
    if (!(vb & PF_MASK) && vb < 240 && wave) {
        const int n = (PF_MASK == 4 ? (vb >> 3) * 4 + (vb & 3) : (vb >> 3) * 2 + ((vb >> 1) & 1)) * 3 + wave - 1;
#pragma nounroll
        for (int r = n; r < PF_ROWS; r += (PF_MASK == 4 ? 360 : 180)) {
            const int u = 2 + (r >> 7);
            const u4 rs = r < 640 ? mksrd((r & 64) ? a.Wv : a.Wk, 448 * 8192) : mksrd(r < 864 ? a.X : a.Au, 2048 * 896);
            asm volatile("s_mov_b32 m0, %3\n\ts_nop 4\n\tbuffer_load_dword %0, %1, %2 offen lds" :: "v"((threadIdx.x & 63) * 128), "s"(rs), "s"((r < 640 ? 64 * CHUNK(u) + (r & 63) : r < 864 ? r - 640 : r - 864) * 8192), "s"((unsigned)(size_t)(char LAS*)smem_ + 140 * 1024) : "memory");
        }
    }
#pragma nounroll
    for (int pass = 0;; ++pass) {
        const bool far = vb & 4;
        if (far) w_first(wave, (vb >> 5) & 1, 4 * (vb & 3) + HD(vb >> 3), SUB(vb >> 3), 0); else waitvm<0>();
        unsigned slot = (unsigned)xcc * 32u + (blockIdx.x >> 3), old = 0;
        if (wave == 0 && !pass) {
            WAITV(16, "+v"(ep));
            ep += 0x9E3779B4u; old = ep;
            asm volatile("s_nop 4\n\tbuffer_atomic_swap %0, %1, %2, 0 offen sc0 sc1" : "+v"(old) : "v"(threadIdx.x == 0 ? O_CNT + slot * 128u : (unsigned)OOB), "s"(rhb) : "memory");
        }
        if (far) { w_first(wave, (vb >> 5) & 1, 4 * (vb & 3) + HD(vb >> 3), SUB(vb >> 3), 1); } else waitvm<0>();
        WAITV(16, "+v"(old));
        if (pass) { bar(); break; }
        if (threadIdx.x == 0) {
            for (int k = 0; k < 256 && old == ep; ++k) {
                slot = (slot + 1u) & 255u;
                old = __hip_atomic_exchange((unsigned*)(a.HB + O_CNT) + slot * 32u, ep, __ATOMIC_RELAXED, __HIP_MEMORY_SCOPE_AGENT);
            }
            *(u2 LAS*)(smem_ + 147456) = (u2){(slot >> 5) + 8u * (slot & 31u), ep};
        }
        bar();
        const int got = __builtin_amdgcn_readfirstlane(*(const unsigned LAS*)(smem_ + 147456));
        tag = __builtin_amdgcn_readfirstlane(*(const unsigned LAS*)(smem_ + 147460));
        if (got == vb) break;
        vb = got; best = 1016; waitvm<0>(); bar();
    }
    const int res = vb & 7, idx = vb >> 3;
    const int xq = res & 3;
    unsigned* const tick = (unsigned*)(a.HB + O_TICK) + xq * 32;
    if (res < 4) {
        const int xh = res >> 1;
        if (idx < 16 || ((res & 1) && idx < 30)) { chain_role(tag, vb, wave, (res & 1) ? idx >> 4 : 2, idx < 16 ? xh * 16 + idx : xh * 14 + idx - 16, xh); return; }
        if (res & 1) { ring_init(tag, wave, xh * 2 + (idx & 1), best); return; }
        if (wave == 0) sst32(tag + 1, rhb, fresh_lane() == 0 ? O_SIDE + (xh * 16 + idx - 16) * 4 : OOB, 0);
        const int sid = xh * 16 + idx - 16;
        parta_task(tag, vb, wave, (sid >> 1) * 7 + (sid & 1) * 4, 4 - (sid & 1));
        return;
    } else {
        if (idx == 0 && wave == 0) sst32(0u, rhb, fresh_lane() == 0 ? O_TICK + xq * 128 : OOB, 0);
        if (idx & 4) wgu_task<1>(tag, vb, wave, 4 * xq + HD(idx), SUB(idx)); else wgu_task<0>(tag, vb, wave, 4 * xq + HD(idx), SUB(idx));
        wait_prog(rhb, fresh_lane() == 0 ? O_FW + xq * 32 * 128 : OOB, tag + 1);
    }
    int round = 0;
#pragma nounroll
    for (; round < 256; ++round) {
        if (wave == 0 && fresh_lane() == 0) *(unsigned LAS*)(smem_ + 160 * 1024 - 16) = __hip_atomic_fetch_add(tick, 1u, __ATOMIC_RELAXED, __HIP_MEMORY_SCOPE_AGENT);
        bar();
        int k = __builtin_amdgcn_readfirstlane(*(const unsigned LAS*)(smem_ + 160 * 1024 - 16));
        if ((unsigned)k >= 190u) break;
        if (k >= PAO_AT && k < PAO_AT + 14) { pa_out(tag, wave, (k - PAO_AT) * 4 + xq); bar(); continue; }
        if (k >= PAO_AT) k -= 14;
        const int g = k < 162 ? (k + 14) / 11 : 16 + (k - 162) / 7, o = k < 162 ? (k + 14) % 11 : (k - 162) % 7;
        if (k < 8 || o >= 7) e_task(tag, wave, 4 * (k < 8 ? k >> 2 : g) + xq, k < 8 ? k & 3 : o - 7);
        else { x_task(tag, wave, 4 * (g - 2) + xq, o); bar(); }
    }
}
__device__ __attribute__((used, noinline)) void fused_kernel_roles() { asm volatile("s_endpgm\n" ASM_ROLES "fk_code_hi:"); }

#define WS_SKIP 0
extern "C" void kernel_launch(void* const* d_in, const int* in_sizes, int n_in, void* d_out, int out_size, void* d_ws, size_t ws_size, hipStream_t stream) {
    Args a;
    a.Au = (const u16*)d_in[0]; a.AlogA = (const u16*)d_in[1]; a.Aim = (const u16*)d_in[2]; a.ACr = (const u16*)d_in[3]; a.ACi = (const u16*)d_in[4];
    a.Alogdt = (const u16*)d_in[5]; a.ADsk = (const u16*)d_in[6]; a.X = (const u16*)d_in[7]; a.mem0 = (const u16*)d_in[8];
    a.Wq = (const u16*)d_in[9]; a.Wk = (const u16*)d_in[10]; a.Wv = (const u16*)d_in[11]; a.Wo = (const u16*)d_in[12];
    a.out = (u16*)d_out;
    char* base = (char*)(((size_t)d_ws + WS_SKIP + (2u << 20) - 1) & ~(size_t)((2u << 20) - 1));
    a.HB = base;
    size_t off = 6u << 20;
    auto alloc = [&](size_t n) { char* p = base + off; off += (n + 4095) & ~(size_t)4095; return p; };
    a.GF = (u16*)alloc(GU_SZ); a.UF = (u16*)alloc(GU_SZ); a.TF = (u16*)alloc(16 * TU_P); a.UM = (u16*)alloc(28 * TU_P);
    a.WvoF = (u16*)alloc(28 * 16 * 14336); a.WqkF = (u16*)alloc(28 * 16 * 14336); a.EX = (u16*)alloc(64 * 16 * 2048); a.MZX = (float*)alloc(64 * 16 * 256); a.PA = (u16*)alloc(56 * 32768);
    a.TS = (unsigned*)alloc(768 * 1280);
    (void)hipFuncSetAttribute((const void*)fused_kernel, hipFuncAttributeMaxDynamicSharedMemorySize, 160 * 1024);
    fused_kernel<<<256, 256, 160 * 1024, stream>>>(a);
}
```

```cpp
#include <hip/hip_runtime.h>
#include <stdint.h>
typedef unsigned short u16;
typedef unsigned long long u64;
typedef __attribute__((ext_vector_type(8))) __bf16 bf8;
typedef __attribute__((ext_vector_type(16))) __bf16 bf16v;
typedef __attribute__((ext_vector_type(2))) __bf16 bf2;
typedef __attribute__((ext_vector_type(2))) float f2;
typedef __attribute__((ext_vector_type(4))) float f4;
typedef __attribute__((ext_vector_type(4))) unsigned u4;
typedef __attribute__((ext_vector_type(2))) unsigned u2;
typedef __attribute__((ext_vector_type(8))) unsigned u8v;
typedef __attribute__((ext_vector_type(4))) short s4;
#define LAS __attribute__((address_space(3)))
#define DI __device__ __forceinline__
#define SB() __builtin_amdgcn_sched_barrier(0)

DI unsigned pk2(float a, float b) { f2 v = {a, b}; return __builtin_bit_cast(unsigned, __builtin_convertvector(v, bf2)); }
DI float bfr(float a) { return __uint_as_float(pk2(a, a) & 0xffff0000u); }
DI unsigned san(unsigned x) { return x < 0xFFFFFFFEu ? x : 0xFFFFFFFEu; }
DI void bar() { asm volatile("s_waitcnt lgkmcnt(0)\n\ts_barrier" ::: "memory"); SB(); }
DI float x32max(float a) { auto r = __builtin_amdgcn_permlane32_swap(__float_as_uint(a), __float_as_uint(a), false, false); return fmaxf(__uint_as_float(r[0]), __uint_as_float(r[1])); }
DI float x32add(float a) { auto r = __builtin_amdgcn_permlane32_swap(__float_as_uint(a), __float_as_uint(a), false, false); return __uint_as_float(r[0]) + __uint_as_float(r[1]); }
DI u2 trread(const char LAS* p) { return __builtin_bit_cast(u2, __builtin_amdgcn_ds_read_tr16_b64_v4i16((LAS s4*)p)); }
DI u4 mksrd(const void* p, unsigned bytes) { u64 x = (u64)p; return (u4){(unsigned)x, (unsigned)(x >> 32) & 0xffffu, bytes, 0x00020000u}; }
DI u4 ld128(u4 srd, int voff, int soff) { u4 v; asm volatile("buffer_load_dwordx4 %0, %1, %2, %3 offen sc1" : "=v"(v) : "v"(voff), "s"(srd), "s"(soff) : "memory"); return v; }
DI unsigned ld32(u4 srd, int voff, int soff) { unsigned v; asm volatile("buffer_load_dword %0, %1, %2, %3 offen sc1" : "=v"(v) : "v"(voff), "s"(srd), "s"(soff) : "memory"); return v; }
DI void st128(u4 v, u4 srd, int voff, int soff) { asm volatile("buffer_store_dwordx4 %0, %1, %2, %3 offen sc1\n\ts_nop 1" :: "v"(v), "v"(voff), "s"(srd), "s"(soff) : "memory"); }
DI void st32(unsigned v, u4 srd, int voff, int soff) { asm volatile("buffer_store_dword %0, %1, %2, %3 offen sc1" :: "v"(v), "v"(voff), "s"(srd), "s"(soff) : "memory"); }
#define PIN(x) asm volatile("" : "+v"(x))
#define WAITV(n, ...) asm volatile("s_waitcnt vmcnt(" #n ")" : __VA_ARGS__ :: "memory")

#define NSEG 64
#define O_MEM 0
#define O_SMM (2 * 32768)
#define O_MZ (4 * 32768)
#define O_MEMX 16384
#define RXW(n) ((n) < 7 ? (5 + (n)) * 32768 : 16384 + ((n) - 5) * 32768)
#define O_PROBE (15 * 32768)
#define O_FL (4 << 20)
#define O_PAR O_FL
#define O_SIDE (O_FL + 256)
#define O_WIN (O_FL + 384)
#define O_FPA (O_FL + 1024)
#define O_FE (O_FL + 2048)
#define O_FW (O_FL + 4096)
#define O_FT (O_FW + 16384)
#define O_REC (O_FT + 16384)
#define O_CNT (O_REC + 16384)
#define O_EPOCH (O_CNT + 32768)
#define O_RDY (O_EPOCH + 1024)
#define O_TICK (O_EPOCH + 128)
#define HB_SZ (5 << 20)
#define GU_H ((2 << 20) + 15360)
#define GU_SZ (32 << 20)
#define TU_P 524288
#define TU_S (65 * 7168)
#define SPIN_MAX (1u << 17)
#define LOG2E 1.4426950408889634f
#define FF 0xFFFFFFFFu
#define OOB 0x7f000000

struct Args {
    const u16 *Au, *AlogA, *Aim, *ACr, *ACi, *Alogdt, *ADsk, *X, *mem0, *Wq, *Wk, *Wv, *Wo;
    u16* out;
    char* HB;
    u16 *WvoF, *WqkF, *GF, *UF, *EX, *UM, *TF, *PA; float *MZX;
    unsigned* TS;
};
typedef const Args __attribute__((address_space(4))) CArgs;
DI CArgs& get_args() { CArgs* p = (CArgs*)__builtin_amdgcn_kernarg_segment_ptr(); asm volatile("" : "+s"(p)); return *p; }
extern __shared__ __attribute__((aligned(16))) char smem_[];
DI unsigned now32() { u64 t; asm volatile("s_memtime %0\n\ts_waitcnt lgkmcnt(0)" : "=s"(t) :: "memory"); return (unsigned)t; }
DI unsigned rt_now() { u64 t; asm volatile("s_memrealtime %0\n\ts_waitcnt lgkmcnt(0)" : "=s"(t) :: "memory"); return (unsigned)t; }
#define REC_A 1140
#define REC_B 890
#define REC_0 150
#define LAT_R 1200u
#define OFF_P 60
#define OFF_X 20
#define TOL_C -100
#define SEQ_LO 0x5a928398a418820ull
#define SEQ_MI 0xb3bb56f28c9ca30ull
#define SEQ_HI 0xfddfeull
#define ASM_ROLES \
".p2align 6\n" \
"fk_role_S0:\n" \
"ds_read_b32 v200, v255 offset:0\n" \
"ds_read_b32 v201, v255 offset:256\n" \
"ds_read_b32 v202, v255 offset:512\n" \
"ds_read_b32 v203, v255 offset:768\n" \
"ds_read_b32 v204, v255 offset:1024\n" \
"ds_read_b32 v205, v255 offset:1280\n" \
"ds_read_b32 v206, v255 offset:1536\n" \
"ds_read_b32 v207, v255 offset:1792\n" \
"ds_read_b32 v208, v255 offset:2048\n" \
"ds_read_b32 v209, v255 offset:2304\n" \
"ds_read_b32 v210, v255 offset:2560\n" \
"ds_read_b32 v211, v255 offset:2816\n" \
"ds_read_b32 v212, v255 offset:3072\n" \
"ds_read_b32 v213, v255 offset:3328\n" \
"ds_read_b32 v214, v255 offset:3584\n" \
"ds_read_b32 v215, v255 offset:3840\n" \
"ds_read_b32 v254, v255 offset:7936\n" \
"s_waitcnt lgkmcnt(0)\n" \
"v_readlane_b32 s4, v254, 0\n" \
"v_readlane_b32 s5, v254, 1\n" \
"v_readlane_b32 s6, v254, 2\n" \
"v_readlane_b32 s7, v254, 3\n" \
"v_readlane_b32 s8, v254, 4\n" \
"v_readlane_b32 s9, v254, 5\n" \
"v_readlane_b32 s10, v254, 6\n" \
"v_readlane_b32 s11, v254, 7\n" \
"v_readlane_b32 s12, v254, 8\n" \
"v_readlane_b32 s13, v254, 9\n" \
"v_readlane_b32 s14, v254, 10\n" \
"v_readlane_b32 s15, v254, 11\n" \
"v_readlane_b32 s16, v254, 12\n" \
"v_readlane_b32 s17, v254, 13\n" \
"v_readlane_b32 s18, v254, 14\n" \
"v_readlane_b32 s19, v254, 15\n" \
"v_readlane_b32 s20, v254, 16\n" \
"v_readlane_b32 s21, v254, 17\n" \
"v_readlane_b32 s22, v254, 18\n" \
"v_readlane_b32 s23, v254, 19\n" \
"v_readlane_b32 s24, v254, 20\n" \
"v_readlane_b32 s25, v254, 21\n" \
"v_readlane_b32 s26, v254, 22\n" \
"v_readlane_b32 s27, v254, 23\n" \
"v_readlane_b32 s28, v254, 24\n" \
"v_readlane_b32 s29, v254, 25\n" \
"v_readlane_b32 s30, v254, 26\n" \
"v_readlane_b32 s31, v254, 27\n" \
"v_readlane_b32 s32, v254, 28\n" \
"v_readlane_b32 s33, v254, 29\n" \
"v_readlane_b32 s34, v254, 30\n" \
"v_readlane_b32 s35, v254, 31\n" \
"v_readlane_b32 s36, v254, 32\n" \
"v_readlane_b32 s37, v254, 33\n" \
"v_readlane_b32 s38, v254, 34\n" \
"v_readlane_b32 s39, v254, 35\n" \
"s_mov_b32 s40, 0\n" \
"s_mov_b32 s51, 0\n" \
"s_nop 0\n" \
"v_mov_b32 v252, s38\n" \
"s_nop 4\n" \
"v_mov_b32 v188, -1\n" \
"s_mov_b32 s28, s33\n" \
"s_mov_b32 s33, -1\n" \
"v_writelane_b32 v251, s6, 0\n" \
"v_writelane_b32 v251, s10, 1\n" \
"v_writelane_b32 v251, s14, 2\n" \
"v_writelane_b32 v251, s18, 3\n" \
"s_mov_b32 s6, 0\n" \
"s_mov_b32 s10, 0\n" \
"s_mov_b32 s14, 0\n" \
"s_mov_b32 s18, 0\n" \
"s_mov_b32 s40, -1\n" \
"s_branch .LS0_pre\n" \
".LS0_wet:\n" \
"s_waitcnt vmcnt(0)\n" \
"v_readlane_b32 s6, v251, 0\n" \
"v_readlane_b32 s10, v251, 1\n" \
"v_readlane_b32 s14, v251, 2\n" \
"v_readlane_b32 s18, v251, 3\n" \
"s_nop 4\n" \
"ds_read_b32 v230, v255 offset:6912\n" \
"v_readlane_b32 s60, v254, 12\n" \
"v_readlane_b32 s61, v254, 13\n" \
"v_readlane_b32 s62, v254, 14\n" \
"v_readlane_b32 s63, v254, 15\n" \
"s_waitcnt lgkmcnt(0)\n" \
"s_nop 3\n" \
"buffer_load_dword v228, v230, s[60:63], 0 offen\n" \
"s_mov_b32 s33, s28\n" \
"s_add_u32 s34, s33, 2\n" \
"s_add_u32 s36, s33, 4\n" \
"s_add_u32 s43, s30, s33\n" \
"s_mov_b32 s50, 0\n" \
".LS0_w1l:\n" \
"buffer_load_dword v240, v204, s[4:7], 0 offen sc1\n" \
"s_waitcnt vmcnt(0)\n" \
".LS0_w1c:\n" \
"v_subrev_u32 v241, s43, v240\n" \
"v_cmp_gt_u32 vcc, 0x100, v241\n" \
"s_cmp_eq_u64 vcc, -1\n" \
"s_cbranch_scc1 .LS0_w1d\n" \
"s_sleep 2\n" \
"s_add_u32 s50, s50, 1\n" \
"s_cmp_lt_u32 s50, 0x20000\n" \
"s_cbranch_scc1 .LS0_w1l\n" \
"s_branch .LS0_end\n" \
".LS0_w1d:\n" \
"v_readlane_b32 s43, v202, 0\n" \
"s_nop 4\n" \
"buffer_load_dwordx4 a[0:3], v201, s[8:11], s43 offen sc1\n" \
"buffer_load_dwordx4 a[4:7], v201, s[8:11], s43 offen offset:1024 sc1\n" \
"v_readlane_b32 s43, v202, 1\n" \
"s_nop 4\n" \
"buffer_load_dwordx4 a[8:11], v201, s[8:11], s43 offen sc1\n" \
"buffer_load_dwordx4 a[12:15], v201, s[8:11], s43 offen offset:1024 sc1\n" \
"v_readlane_b32 s43, v202, 2\n" \
"s_nop 4\n" \
"buffer_load_dwordx4 a[16:19], v201, s[8:11], s43 offen sc1\n" \
"buffer_load_dwordx4 a[20:23], v201, s[8:11], s43 offen offset:1024 sc1\n" \
"v_readlane_b32 s43, v202, 3\n" \
"s_nop 4\n" \
"buffer_load_dwordx4 a[24:27], v201, s[8:11], s43 offen sc1\n" \
"buffer_load_dwordx4 a[28:31], v201, s[8:11], s43 offen offset:1024 sc1\n" \
"v_readlane_b32 s43, v202, 4\n" \
"s_nop 4\n" \
"buffer_load_dwordx4 a[32:35], v201, s[8:11], s43 offen sc1\n" \
"buffer_load_dwordx4 a[36:39], v201, s[8:11], s43 offen offset:1024 sc1\n" \
"v_readlane_b32 s43, v202, 5\n" \
"s_nop 4\n" \
"buffer_load_dwordx4 a[40:43], v201, s[8:11], s43 offen sc1\n" \
"buffer_load_dwordx4 a[44:47], v201, s[8:11], s43 offen offset:1024 sc1\n" \
"v_readlane_b32 s43, v202, 6\n" \
"s_nop 4\n" \
"buffer_load_dwordx4 a[48:51], v201, s[8:11], s43 offen sc1\n" \
"buffer_load_dwordx4 a[52:55], v201, s[8:11], s43 offen offset:1024 sc1\n" \
"v_readlane_b32 s43, v202, 7\n" \
"s_nop 4\n" \
"buffer_load_dwordx4 a[56:59], v201, s[8:11], s43 offen sc1\n" \
"buffer_load_dwordx4 a[60:63], v201, s[8:11], s43 offen offset:1024 sc1\n" \
"s_add_u32 s43, s30, s34\n" \
"s_mov_b32 s50, 0\n" \
"s_branch .LS0_w2c\n" \
".LS0_w2l:\n" \
"buffer_load_dword v240, v204, s[4:7], 0 offen sc1\n" \
"s_waitcnt vmcnt(0)\n" \
".LS0_w2c:\n" \
"v_subrev_u32 v241, s43, v240\n" \
"v_cmp_gt_u32 vcc, 0x100, v241\n" \
"s_cmp_eq_u64 vcc, -1\n" \
"s_cbranch_scc1 .LS0_w2d\n" \
"s_sleep 2\n" \
"s_add_u32 s50, s50, 1\n" \
"s_cmp_lt_u32 s50, 0x20000\n" \
"s_cbranch_scc1 .LS0_w2l\n" \
"s_branch .LS0_end\n" \
".LS0_w2d:\n" \
"v_readlane_b32 s43, v202, 8\n" \
"s_nop 4\n" \
"buffer_load_dwordx4 a[64:67], v201, s[8:11], s43 offen sc1\n" \
"buffer_load_dwordx4 a[68:71], v201, s[8:11], s43 offen offset:1024 sc1\n" \
"v_readlane_b32 s43, v202, 9\n" \
"s_nop 4\n" \
"buffer_load_dwordx4 a[72:75], v201, s[8:11], s43 offen sc1\n" \
"buffer_load_dwordx4 a[76:79], v201, s[8:11], s43 offen offset:1024 sc1\n" \
"v_readlane_b32 s43, v202, 10\n" \
"s_nop 4\n" \
"buffer_load_dwordx4 a[80:83], v201, s[8:11], s43 offen sc1\n" \
"buffer_load_dwordx4 a[84:87], v201, s[8:11], s43 offen offset:1024 sc1\n" \
"v_readlane_b32 s43, v202, 11\n" \
"s_nop 4\n" \
"buffer_load_dwordx4 a[88:91], v201, s[8:11], s43 offen sc1\n" \
"buffer_load_dwordx4 a[92:95], v201, s[8:11], s43 offen offset:1024 sc1\n" \
"v_readlane_b32 s43, v202, 16\n" \
"s_nop 4\n" \
"buffer_load_dwordx4 a[128:131], v201, s[8:11], s43 offen sc1\n" \
"buffer_load_dwordx4 a[132:135], v201, s[8:11], s43 offen offset:1024 sc1\n" \
"v_readlane_b32 s43, v202, 19\n" \
"s_nop 4\n" \
"buffer_load_dwordx4 a[152:155], v201, s[8:11], s43 offen sc1\n" \
"buffer_load_dwordx4 a[156:159], v201, s[8:11], s43 offen offset:1024 sc1\n" \
"v_readlane_b32 s43, v202, 22\n" \
"s_nop 4\n" \
"buffer_load_dwordx4 a[176:179], v201, s[8:11], s43 offen sc1\n" \
"buffer_load_dwordx4 a[180:183], v201, s[8:11], s43 offen offset:1024 sc1\n" \
"v_readlane_b32 s43, v202, 25\n" \
"s_nop 4\n" \
"buffer_load_dwordx4 a[200:203], v201, s[8:11], s43 offen sc1\n" \
"buffer_load_dwordx4 a[204:207], v201, s[8:11], s43 offen offset:1024 sc1\n" \
"s_add_u32 s43, s30, s36\n" \
"s_mov_b32 s50, 0\n" \
"s_branch .LS0_w3c\n" \
".LS0_w3l:\n" \
"buffer_load_dword v240, v204, s[4:7], 0 offen sc1\n" \
"s_waitcnt vmcnt(0)\n" \
".LS0_w3c:\n" \
"v_subrev_u32 v241, s43, v240\n" \
"v_cmp_gt_u32 vcc, 0x100, v241\n" \
"s_cmp_eq_u64 vcc, -1\n" \
"s_cbranch_scc1 .LS0_w3d\n" \
"s_sleep 2\n" \
"s_add_u32 s50, s50, 1\n" \
"s_cmp_lt_u32 s50, 0x20000\n" \
"s_cbranch_scc1 .LS0_w3l\n" \
"s_branch .LS0_end\n" \
".LS0_w3d:\n" \
"v_readlane_b32 s43, v202, 12\n" \
"s_nop 4\n" \
"buffer_load_dwordx4 a[96:99], v201, s[8:11], s43 offen sc1\n" \
"buffer_load_dwordx4 a[100:103], v201, s[8:11], s43 offen offset:1024 sc1\n" \
"v_readlane_b32 s43, v202, 13\n" \
"s_nop 4\n" \
"buffer_load_dwordx4 a[104:107], v201, s[8:11], s43 offen sc1\n" \
"buffer_load_dwordx4 a[108:111], v201, s[8:11], s43 offen offset:1024 sc1\n" \
"v_readlane_b32 s43, v202, 14\n" \
"s_nop 4\n" \
"buffer_load_dwordx4 a[112:115], v201, s[8:11], s43 offen sc1\n" \
"buffer_load_dwordx4 a[116:119], v201, s[8:11], s43 offen offset:1024 sc1\n" \
"v_readlane_b32 s43, v202, 15\n" \
"s_nop 4\n" \
"buffer_load_dwordx4 a[120:123], v201, s[8:11], s43 offen sc1\n" \
"buffer_load_dwordx4 a[124:127], v201, s[8:11], s43 offen offset:1024 sc1\n" \
"v_readlane_b32 s43, v202, 17\n" \
"s_nop 4\n" \
"buffer_load_dwordx4 a[136:139], v201, s[8:11], s43 offen sc1\n" \
"buffer_load_dwordx4 a[140:143], v201, s[8:11], s43 offen offset:1024 sc1\n" \
"v_readlane_b32 s43, v202, 20\n" \
"s_nop 4\n" \
"buffer_load_dwordx4 a[160:163], v201, s[8:11], s43 offen sc1\n" \
"buffer_load_dwordx4 a[164:167], v201, s[8:11], s43 offen offset:1024 sc1\n" \
"v_readlane_b32 s43, v202, 23\n" \
"s_nop 4\n" \
"buffer_load_dwordx4 a[184:187], v201, s[8:11], s43 offen sc1\n" \
"buffer_load_dwordx4 a[188:191], v201, s[8:11], s43 offen offset:1024 sc1\n" \
"v_readlane_b32 s43, v202, 26\n" \
"s_nop 4\n" \
"buffer_load_dwordx4 a[208:211], v201, s[8:11], s43 offen sc1\n" \
"buffer_load_dwordx4 a[212:215], v201, s[8:11], s43 offen offset:1024 sc1\n" \
"s_add_u32 s43, s30, 7\n" \
"s_mov_b32 s50, 0\n" \
"s_branch .LS0_w4c\n" \
".LS0_w4l:\n" \
"buffer_load_dword v240, v204, s[4:7], 0 offen sc1\n" \
"s_waitcnt vmcnt(0)\n" \
".LS0_w4c:\n" \
"v_subrev_u32 v241, s43, v240\n" \
"v_cmp_gt_u32 vcc, 0x100, v241\n" \
"s_cmp_eq_u64 vcc, -1\n" \
"s_cbranch_scc1 .LS0_w4d\n" \
"s_sleep 2\n" \
"s_add_u32 s50, s50, 1\n" \
"s_cmp_lt_u32 s50, 0x20000\n" \
"s_cbranch_scc1 .LS0_w4l\n" \
"s_branch .LS0_end\n" \
".LS0_w4d:\n" \
"v_readlane_b32 s43, v202, 18\n" \
"s_nop 4\n" \
"buffer_load_dwordx4 a[144:147], v201, s[8:11], s43 offen sc1\n" \
"buffer_load_dwordx4 a[148:151], v201, s[8:11], s43 offen offset:1024 sc1\n" \
"v_readlane_b32 s43, v202, 21\n" \
"s_nop 4\n" \
"buffer_load_dwordx4 a[168:171], v201, s[8:11], s43 offen sc1\n" \
"buffer_load_dwordx4 a[172:175], v201, s[8:11], s43 offen offset:1024 sc1\n" \
"v_readlane_b32 s43, v202, 24\n" \
"s_nop 4\n" \
"buffer_load_dwordx4 a[192:195], v201, s[8:11], s43 offen sc1\n" \
"buffer_load_dwordx4 a[196:199], v201, s[8:11], s43 offen offset:1024 sc1\n" \
"v_readlane_b32 s43, v202, 27\n" \
"s_nop 4\n" \
"buffer_load_dwordx4 a[216:219], v201, s[8:11], s43 offen sc1\n" \
"buffer_load_dwordx4 a[220:223], v201, s[8:11], s43 offen offset:1024 sc1\n" \
"s_mov_b32 s33, -1\n" \
"s_add_u32 s43, s30, 1\n" \
"s_mov_b32 s50, 0\n" \
".LS0_w5l:\n" \
"buffer_load_dword v240, v205, s[4:7], 0 offen sc1\n" \
"s_waitcnt vmcnt(0)\n" \
".LS0_w5c:\n" \
"v_subrev_u32 v241, s43, v240\n" \
"v_cmp_gt_u32 vcc, 0x100, v241\n" \
"s_cmp_eq_u64 vcc, -1\n" \
"s_cbranch_scc1 .LS0_w5d\n" \
"s_sleep 2\n" \
"s_add_u32 s50, s50, 1\n" \
"s_cmp_lt_u32 s50, 0x20000\n" \
"s_cbranch_scc1 .LS0_w5l\n" \
"s_branch .LS0_end\n" \
".LS0_w5d:\n" \
"v_readfirstlane_b32 s31, v241\n" \
"s_lshr_b32 s32, s31, 2\n" \
"s_xor_b32 s29, s31, s32\n" \
"s_and_b32 s29, s29, 3\n" \
"s_lshl_b32 s29, s29, 13\n" \
"s_bitset1_b32 s29, 21\n" \
"s_and_b32 s31, s31, 3\n" \
"s_lshl_b32 s31, s31, 13\n" \
"s_bitcmp1_b32 s39, 0\n" \
"s_cselect_b32 s43, s29, 0\n" \
"s_xor_b32 s31, s31, s43\n" \
"s_xor_b32 s32, s31, 0x4000\n" \
"s_mov_b32 s39, 0\n" \
".LS0_pre:\n" \
"s_barrier\n" \
"ds_read_b96 v[248:250], v252\n" \
"s_waitcnt lgkmcnt(0)\n" \
"v_readfirstlane_b32 s44, v248\n" \
"v_readfirstlane_b32 s45, v249\n" \
"v_readfirstlane_b32 s46, v250\n" \
"s_mul_i32 s42, s25, s46\n" \
"s_lshr_b32 s42, s42, 12\n" \
".LS0_cu:\n" \
"s_sub_u32 s43, s24, s44\n" \
"s_cmp_lt_i32 s43, 0\n" \
"s_cbranch_scc0 .LS0_cud\n" \
"s_add_u32 s24, s24, s25\n" \
"s_xor_b32 s31, s31, s29\n" \
"s_xor_b32 s32, s32, s29\n" \
"s_xor_b32 s47, s47, s29\n" \
"s_add_u32 s39, s39, 1\n" \
"s_branch .LS0_cu\n" \
".LS0_cud:\n" \
".LS0_loop:\n" \
"s_add_u32 s43, s40, 0\n" \
"s_and_b32 s43, s43, 3\n" \
"s_bitcmp1_b32 s43, 0\n" \
"s_cselect_b32 s47, s32, s31\n" \
"s_lshl_b32 s43, s43, 19\n" \
"s_add_u32 s47, s47, s43\n" \
"s_sub_u32 s43, s24, s44\n" \
"s_max_i32 s43, s43, 0xfffd0000\n" \
"s_min_i32 s43, s43, 0x4000\n" \
"s_mul_i32 s43, s43, s46\n" \
"s_ashr_i32 s43, s43, 12\n" \
"s_add_u32 s41, s45, s43\n" \
"s_branch .LS0_a1\n" \
".LS0_retry:\n" \
"s_add_u32 s51, s51, 1\n" \
"s_cmp_gt_u32 s51, 0x20000\n" \
"s_cbranch_scc1 .LS0_end\n" \
".LS0_a0:\n" \
"s_add_u32 s41, s41, s42\n" \
"s_add_u32 s24, s24, s25\n" \
"s_xor_b32 s31, s31, s29\n" \
"s_xor_b32 s32, s32, s29\n" \
"s_xor_b32 s47, s47, s29\n" \
"s_xor_b32 s48, s48, s29\n" \
"s_add_u32 s39, s39, 1\n" \
".p2align 6\n" \
".LS0_a1:\n" \
"s_memtime vcc\n" \
"s_waitcnt lgkmcnt(0)\n" \
"s_sub_u32 s43, s41, vcc_lo\n" \
"s_cmp_gt_i32 s43, 128\n" \
"s_cbranch_scc1 .LS0_a1\n" \
"s_cmp_lt_i32 s43, s37\n" \
"s_cbranch_scc1 .LS0_a0\n" \
"s_max_i32 s43, s43, 0\n" \
"s_and_b32 s43, s43, -4\n" \
"s_getpc_b64 vcc\n" \
".LS0_a3:\n" \
"s_sub_u32 s43, .LS0_a4-.LS0_a3, s43\n" \
"s_add_u32 vcc_lo, vcc_lo, s43\n" \
"s_addc_u32 vcc_hi, vcc_hi, 0\n" \
"s_setpc_b64 vcc\n" \
".rept 32\n" \
"s_nop 0\n" \
".endr\n" \
".LS0_a4:\n" \
"buffer_load_dwordx4 v[0:3], v208, s[4:7], s47 offen sc1\n" \
"s_nop 9\n" \
"buffer_load_dwordx4 v[4:7], v208, s[4:7], s47 offen offset:1024 sc1\n" \
"s_nop 9\n" \
"buffer_load_dwordx4 v[8:11], v208, s[4:7], s47 offen offset:2048 sc1\n" \
"s_nop 9\n" \
"buffer_load_dwordx4 v[12:15], v210, s[4:7], s47 offen sc1\n" \
"s_add_u32 s35, s35, 1\n" \
"v_mov_b32 v28, 0\n" \
"v_mov_b32 v29, 0\n" \
"v_mov_b32 v30, 0\n" \
"v_mov_b32 v31, 0\n" \
"v_mov_b32 v32, 0\n" \
"v_mov_b32 v33, 0\n" \
"v_mov_b32 v34, 0\n" \
"v_mov_b32 v35, 0\n" \
"v_mov_b32 v36, 0\n" \
"v_mov_b32 v37, 0\n" \
"v_mov_b32 v38, 0\n" \
"v_mov_b32 v39, 0\n" \
"v_mov_b32 v40, 0\n" \
"v_mov_b32 v41, 0\n" \
"v_mov_b32 v42, 0\n" \
"v_mov_b32 v43, 0\n" \
"v_mov_b32 v44, 0\n" \
"v_mov_b32 v45, 0\n" \
"v_mov_b32 v46, 0\n" \
"v_mov_b32 v47, 0\n" \
"v_mov_b32 v48, 0\n" \
"v_mov_b32 v49, 0\n" \
"v_mov_b32 v50, 0\n" \
"v_mov_b32 v51, 0\n" \
"v_mov_b32 v52, 0\n" \
"v_mov_b32 v53, 0\n" \
"v_mov_b32 v54, 0\n" \
"v_mov_b32 v55, 0\n" \
"v_mov_b32 v56, 0\n" \
"v_mov_b32 v57, 0\n" \
"v_mov_b32 v58, 0\n" \
"v_mov_b32 v59, 0\n" \
"s_waitcnt vmcnt(3)\n" \
"v_smfmac_f32_16x16x64_bf16 v[28:31], v[0:3], a[0:7], v200\n" \
"v_smfmac_f32_16x16x64_bf16 v[32:35], v[0:3], a[8:15], v200\n" \
"v_smfmac_f32_16x16x64_bf16 v[36:39], v[0:3], a[16:23], v200\n" \
"v_smfmac_f32_16x16x64_bf16 v[40:43], v[0:3], a[24:31], v200\n" \
"v_smfmac_f32_16x16x64_bf16 v[44:47], v[0:3], a[32:39], v200\n" \
"v_smfmac_f32_16x16x64_bf16 v[48:51], v[0:3], a[40:47], v200\n" \
"v_smfmac_f32_16x16x64_bf16 v[52:55], v[0:3], a[48:55], v200\n" \
"v_smfmac_f32_16x16x64_bf16 v[56:59], v[0:3], a[56:63], v200\n" \
"s_waitcnt vmcnt(2)\n" \
"v_smfmac_f32_16x16x64_bf16 v[28:31], v[4:7], a[64:71], v200\n" \
"v_smfmac_f32_16x16x64_bf16 v[32:35], v[4:7], a[72:79], v200\n" \
"v_smfmac_f32_16x16x64_bf16 v[36:39], v[4:7], a[80:87], v200\n" \
"v_smfmac_f32_16x16x64_bf16 v[40:43], v[4:7], a[88:95], v200\n" \
"s_waitcnt vmcnt(1)\n" \
"v_smfmac_f32_16x16x64_bf16 v[28:31], v[8:11], a[96:103], v200\n" \
"s_nop 2\n" \
"v_smfmac_f32_16x16x64_bf16 v[32:35], v[8:11], a[104:111], v200\n" \
"s_nop 2\n" \
"v_smfmac_f32_16x16x64_bf16 v[36:39], v[8:11], a[112:119], v200\n" \
"s_nop 0\n" \
"v_cvt_pk_bf16_f32 v128, v28, v29\n" \
"v_cvt_pk_bf16_f32 v129, v30, v31\n" \
"v_smfmac_f32_16x16x64_bf16 v[40:43], v[8:11], a[120:127], v200\n" \
"ds_write_b64 v211, v[128:129] offset:0\n" \
"v_cvt_pk_bf16_f32 v130, v32, v33\n" \
"v_cvt_pk_bf16_f32 v131, v34, v35\n" \
"v_smfmac_f32_16x16x64_bf16 v[44:47], v[4:7], a[128:135], v200\n" \
"ds_write_b64 v211, v[130:131] offset:384\n" \
"v_cvt_pk_bf16_f32 v132, v36, v37\n" \
"v_cvt_pk_bf16_f32 v133, v38, v39\n" \
"v_smfmac_f32_16x16x64_bf16 v[44:47], v[8:11], a[136:143], v200\n" \
"s_waitcnt vmcnt(0)\n" \
"ds_write_b64 v211, v[132:133] offset:2560\n" \
"v_cvt_pk_bf16_f32 v134, v40, v41\n" \
"v_cvt_pk_bf16_f32 v135, v42, v43\n" \
"v_smfmac_f32_16x16x64_bf16 v[44:47], v[12:15], a[144:151], v200\n" \
"ds_write_b64 v211, v[134:135] offset:4736\n" \
"s_nop 1\n" \
"v_smfmac_f32_16x16x64_bf16 v[48:51], v[4:7], a[152:159], v200\n" \
"s_nop 2\n" \
"v_smfmac_f32_16x16x64_bf16 v[48:51], v[8:11], a[160:167], v200\n" \
"s_nop 0\n" \
"v_cvt_pk_bf16_f32 v136, v44, v45\n" \
"v_cvt_pk_bf16_f32 v137, v46, v47\n" \
"v_smfmac_f32_16x16x64_bf16 v[48:51], v[12:15], a[168:175], v200\n" \
"ds_write_b64 v211, v[136:137] offset:9216\n" \
"s_nop 1\n" \
"v_smfmac_f32_16x16x64_bf16 v[52:55], v[4:7], a[176:183], v200\n" \
"s_waitcnt lgkmcnt(1)\n" \
"s_barrier\n" \
"s_nop 0\n" \
"v_smfmac_f32_16x16x64_bf16 v[52:55], v[8:11], a[184:191], v200\n" \
"s_nop 0\n" \
"v_cvt_pk_bf16_f32 v138, v48, v49\n" \
"v_cvt_pk_bf16_f32 v139, v50, v51\n" \
"v_smfmac_f32_16x16x64_bf16 v[52:55], v[12:15], a[192:199], v200\n" \
"ds_write_b64 v211, v[138:139] offset:9600\n" \
"s_nop 1\n" \
"v_smfmac_f32_16x16x64_bf16 v[56:59], v[4:7], a[200:207], v200\n" \
"s_nop 2\n" \
"v_smfmac_f32_16x16x64_bf16 v[56:59], v[8:11], a[208:215], v200\n" \
"s_nop 2\n" \
"v_smfmac_f32_16x16x64_bf16 v[56:59], v[12:15], a[216:223], v200\n" \
"v_cvt_pk_bf16_f32 v140, v52, v53\n" \
"v_cvt_pk_bf16_f32 v141, v54, v55\n" \
"s_nop 1\n" \
"ds_write_b64 v211, v[140:141] offset:11776\n" \
"s_nop 2\n" \
"v_cvt_pk_bf16_f32 v142, v56, v57\n" \
"v_cvt_pk_bf16_f32 v143, v58, v59\n" \
"ds_write_b64 v211, v[142:143] offset:13952\n" \
"s_waitcnt lgkmcnt(1)\n" \
"s_barrier\n" \
"s_waitcnt lgkmcnt(0)\n" \
"s_barrier\n" \
"s_nop 3\n" \
"ds_read_b32 v64, v252 offset:20\n" \
"s_waitcnt lgkmcnt(0)\n" \
"v_readfirstlane_b32 s43, v64\n" \
"s_cmp_eq_u32 s43, s35\n" \
"s_cbranch_scc1 .LS0_retry\n" \
"s_nop 15\n" \
"v_max3_u32 v64, v0, v0, v1\n" \
"v_max3_u32 v64, v64, v2, v3\n" \
"v_max3_u32 v64, v64, v4, v5\n" \
"v_max3_u32 v64, v64, v6, v7\n" \
"v_max3_u32 v64, v64, v8, v9\n" \
"v_max3_u32 v64, v64, v10, v11\n" \
"v_max3_u32 v64, v64, v12, v13\n" \
"v_max3_u32 v64, v64, v14, v15\n" \
"v_cmp_eq_u32 vcc, -1, v64\n" \
"s_cbranch_vccnz .LS0_veto\n" \
".LS0_back:\n" \
"s_waitcnt lgkmcnt(0)\n" \
"s_barrier\n" \
"ds_read_b32 v64, v252 offset:16\n" \
"ds_read_b64 v[246:247], v252 offset:24\n" \
"ds_read_b96 v[248:250], v252\n" \
"s_waitcnt lgkmcnt(0)\n" \
"v_readfirstlane_b32 s44, v248\n" \
"v_readfirstlane_b32 s45, v249\n" \
"v_readfirstlane_b32 s46, v250\n" \
"v_readfirstlane_b32 s26, v246\n" \
"v_readfirstlane_b32 s27, v247\n" \
"s_mul_i32 s42, s25, s46\n" \
"s_lshr_b32 s42, s42, 12\n" \
"v_readfirstlane_b32 s43, v64\n" \
"s_cmp_eq_u32 s43, s35\n" \
"s_cbranch_scc1 .LS0_retry\n" \
"s_add_u32 s40, s40, 1\n" \
"s_add_u32 s24, s24, s26\n" \
"s_add_u32 s24, s24, s27\n" \
"s_cmp_eq_u32 s40, 0\n" \
"s_cbranch_scc1 .LS0_wet\n" \
"s_cmp_lt_u32 s40, 63\n" \
"s_cbranch_scc1 .LS0_loop\n" \
".LS0_end:\n" \
"s_endpgm\n" \
".LS0_veto:\n" \
"v_mov_b32 v64, s35\n" \
"ds_write_b32 v252, v64 offset:16\n" \
"s_branch .LS0_back\n" \
".p2align 6\n" \
"fk_role_S1:\n" \
"ds_read_b32 v200, v255 offset:0\n" \
"ds_read_b32 v201, v255 offset:256\n" \
"ds_read_b32 v202, v255 offset:512\n" \
"ds_read_b32 v203, v255 offset:768\n" \
"ds_read_b32 v204, v255 offset:1024\n" \
"ds_read_b32 v205, v255 offset:1280\n" \
"ds_read_b32 v206, v255 offset:1536\n" \
"ds_read_b32 v207, v255 offset:1792\n" \
"ds_read_b32 v208, v255 offset:2048\n" \
"ds_read_b32 v209, v255 offset:2304\n" \
"ds_read_b32 v210, v255 offset:2560\n" \
"ds_read_b32 v211, v255 offset:2816\n" \
"ds_read_b32 v212, v255 offset:3072\n" \
"ds_read_b32 v213, v255 offset:3328\n" \
"ds_read_b32 v214, v255 offset:3584\n" \
"ds_read_b32 v215, v255 offset:3840\n" \
"ds_read_b32 v254, v255 offset:7936\n" \
"s_waitcnt lgkmcnt(0)\n" \
"v_readlane_b32 s4, v254, 0\n" \
"v_readlane_b32 s5, v254, 1\n" \
"v_readlane_b32 s6, v254, 2\n" \
"v_readlane_b32 s7, v254, 3\n" \
"v_readlane_b32 s8, v254, 4\n" \
"v_readlane_b32 s9, v254, 5\n" \
"v_readlane_b32 s10, v254, 6\n" \
"v_readlane_b32 s11, v254, 7\n" \
"v_readlane_b32 s12, v254, 8\n" \
"v_readlane_b32 s13, v254, 9\n" \
"v_readlane_b32 s14, v254, 10\n" \
"v_readlane_b32 s15, v254, 11\n" \
"v_readlane_b32 s16, v254, 12\n" \
"v_readlane_b32 s17, v254, 13\n" \
"v_readlane_b32 s18, v254, 14\n" \
"v_readlane_b32 s19, v254, 15\n" \
"v_readlane_b32 s20, v254, 16\n" \
"v_readlane_b32 s21, v254, 17\n" \
"v_readlane_b32 s22, v254, 18\n" \
"v_readlane_b32 s23, v254, 19\n" \
"v_readlane_b32 s24, v254, 20\n" \
"v_readlane_b32 s25, v254, 21\n" \
"v_readlane_b32 s26, v254, 22\n" \
"v_readlane_b32 s27, v254, 23\n" \
"v_readlane_b32 s28, v254, 24\n" \
"v_readlane_b32 s29, v254, 25\n" \
"v_readlane_b32 s30, v254, 26\n" \
"v_readlane_b32 s31, v254, 27\n" \
"v_readlane_b32 s32, v254, 28\n" \
"v_readlane_b32 s33, v254, 29\n" \
"v_readlane_b32 s34, v254, 30\n" \
"v_readlane_b32 s35, v254, 31\n" \
"v_readlane_b32 s36, v254, 32\n" \
"v_readlane_b32 s37, v254, 33\n" \
"v_readlane_b32 s38, v254, 34\n" \
"v_readlane_b32 s39, v254, 35\n" \
"s_mov_b32 s40, 0\n" \
"s_mov_b32 s51, 0\n" \
"s_nop 0\n" \
"v_mov_b32 v252, s38\n" \
"s_nop 4\n" \
"v_mov_b32 v188, -1\n" \
"s_mov_b32 s28, s33\n" \
"s_mov_b32 s33, -1\n" \
"v_writelane_b32 v251, s6, 0\n" \
"v_writelane_b32 v251, s10, 1\n" \
"v_writelane_b32 v251, s14, 2\n" \
"v_writelane_b32 v251, s18, 3\n" \
"s_mov_b32 s6, 0\n" \
"s_mov_b32 s10, 0\n" \
"s_mov_b32 s14, 0\n" \
"s_mov_b32 s18, 0\n" \
"s_mov_b32 s40, -1\n" \
"s_branch .LS1_pre\n" \
".LS1_wet:\n" \
"s_waitcnt vmcnt(0)\n" \
"v_readlane_b32 s6, v251, 0\n" \
"v_readlane_b32 s10, v251, 1\n" \
"v_readlane_b32 s14, v251, 2\n" \
"v_readlane_b32 s18, v251, 3\n" \
"s_nop 4\n" \
"ds_read_b32 v230, v255 offset:6912\n" \
"v_readlane_b32 s60, v254, 12\n" \
"v_readlane_b32 s61, v254, 13\n" \
"v_readlane_b32 s62, v254, 14\n" \
"v_readlane_b32 s63, v254, 15\n" \
"s_waitcnt lgkmcnt(0)\n" \
"s_nop 3\n" \
"buffer_load_dword v228, v230, s[60:63], 0 offen\n" \
"s_mov_b32 s33, s28\n" \
"s_add_u32 s34, s33, 2\n" \
"s_add_u32 s36, s33, 4\n" \
"s_add_u32 s43, s30, s33\n" \
"s_mov_b32 s50, 0\n" \
".LS1_w6l:\n" \
"buffer_load_dword v240, v204, s[4:7], 0 offen sc1\n" \
"s_waitcnt vmcnt(0)\n" \
".LS1_w6c:\n" \
"v_subrev_u32 v241, s43, v240\n" \
"v_cmp_gt_u32 vcc, 0x100, v241\n" \
"s_cmp_eq_u64 vcc, -1\n" \
"s_cbranch_scc1 .LS1_w6d\n" \
"s_sleep 2\n" \
"s_add_u32 s50, s50, 1\n" \
"s_cmp_lt_u32 s50, 0x20000\n" \
"s_cbranch_scc1 .LS1_w6l\n" \
"s_branch .LS1_end\n" \
".LS1_w6d:\n" \
"v_readlane_b32 s43, v202, 12\n" \
"s_nop 4\n" \
"buffer_load_dwordx4 a[96:99], v201, s[8:11], s43 offen sc1\n" \
"buffer_load_dwordx4 a[100:103], v201, s[8:11], s43 offen offset:1024 sc1\n" \
"v_readlane_b32 s43, v202, 13\n" \
"s_nop 4\n" \
"buffer_load_dwordx4 a[104:107], v201, s[8:11], s43 offen sc1\n" \
"buffer_load_dwordx4 a[108:111], v201, s[8:11], s43 offen offset:1024 sc1\n" \
"v_readlane_b32 s43, v202, 14\n" \
"s_nop 4\n" \
"buffer_load_dwordx4 a[112:115], v201, s[8:11], s43 offen sc1\n" \
"buffer_load_dwordx4 a[116:119], v201, s[8:11], s43 offen offset:1024 sc1\n" \
"v_readlane_b32 s43, v202, 15\n" \
"s_nop 4\n" \
"buffer_load_dwordx4 a[120:123], v201, s[8:11], s43 offen sc1\n" \
"buffer_load_dwordx4 a[124:127], v201, s[8:11], s43 offen offset:1024 sc1\n" \
"v_readlane_b32 s43, v202, 17\n" \
"s_nop 4\n" \
"buffer_load_dwordx4 a[136:139], v201, s[8:11], s43 offen sc1\n" \
"buffer_load_dwordx4 a[140:143], v201, s[8:11], s43 offen offset:1024 sc1\n" \
"v_readlane_b32 s43, v202, 20\n" \
"s_nop 4\n" \
"buffer_load_dwordx4 a[160:163], v201, s[8:11], s43 offen sc1\n" \
"buffer_load_dwordx4 a[164:167], v201, s[8:11], s43 offen offset:1024 sc1\n" \
"v_readlane_b32 s43, v202, 23\n" \
"s_nop 4\n" \
"buffer_load_dwordx4 a[184:187], v201, s[8:11], s43 offen sc1\n" \
"buffer_load_dwordx4 a[188:191], v201, s[8:11], s43 offen offset:1024 sc1\n" \
"v_readlane_b32 s43, v202, 26\n" \
"s_nop 4\n" \
"buffer_load_dwordx4 a[208:211], v201, s[8:11], s43 offen sc1\n" \
"buffer_load_dwordx4 a[212:215], v201, s[8:11], s43 offen offset:1024 sc1\n" \
"s_add_u32 s43, s30, s34\n" \
"s_mov_b32 s50, 0\n" \
"s_branch .LS1_w7c\n" \
".LS1_w7l:\n" \
"buffer_load_dword v240, v204, s[4:7], 0 offen sc1\n" \
"s_waitcnt vmcnt(0)\n" \
".LS1_w7c:\n" \
"v_subrev_u32 v241, s43, v240\n" \
"v_cmp_gt_u32 vcc, 0x100, v241\n" \
"s_cmp_eq_u64 vcc, -1\n" \
"s_cbranch_scc1 .LS1_w7d\n" \
"s_sleep 2\n" \
"s_add_u32 s50, s50, 1\n" \
"s_cmp_lt_u32 s50, 0x20000\n" \
"s_cbranch_scc1 .LS1_w7l\n" \
"s_branch .LS1_end\n" \
".LS1_w7d:\n" \
"v_readlane_b32 s43, v202, 0\n" \
"s_nop 4\n" \
"buffer_load_dwordx4 a[0:3], v201, s[8:11], s43 offen sc1\n" \
"buffer_load_dwordx4 a[4:7], v201, s[8:11], s43 offen offset:1024 sc1\n" \
"v_readlane_b32 s43, v202, 1\n" \
"s_nop 4\n" \
"buffer_load_dwordx4 a[8:11], v201, s[8:11], s43 offen sc1\n" \
"buffer_load_dwordx4 a[12:15], v201, s[8:11], s43 offen offset:1024 sc1\n" \
"v_readlane_b32 s43, v202, 2\n" \
"s_nop 4\n" \
"buffer_load_dwordx4 a[16:19], v201, s[8:11], s43 offen sc1\n" \
"buffer_load_dwordx4 a[20:23], v201, s[8:11], s43 offen offset:1024 sc1\n" \
"v_readlane_b32 s43, v202, 3\n" \
"s_nop 4\n" \
"buffer_load_dwordx4 a[24:27], v201, s[8:11], s43 offen sc1\n" \
"buffer_load_dwordx4 a[28:31], v201, s[8:11], s43 offen offset:1024 sc1\n" \
"v_readlane_b32 s43, v202, 4\n" \
"s_nop 4\n" \
"buffer_load_dwordx4 a[32:35], v201, s[8:11], s43 offen sc1\n" \
"buffer_load_dwordx4 a[36:39], v201, s[8:11], s43 offen offset:1024 sc1\n" \
"v_readlane_b32 s43, v202, 5\n" \
"s_nop 4\n" \
"buffer_load_dwordx4 a[40:43], v201, s[8:11], s43 offen sc1\n" \
"buffer_load_dwordx4 a[44:47], v201, s[8:11], s43 offen offset:1024 sc1\n" \
"v_readlane_b32 s43, v202, 6\n" \
"s_nop 4\n" \
"buffer_load_dwordx4 a[48:51], v201, s[8:11], s43 offen sc1\n" \
"buffer_load_dwordx4 a[52:55], v201, s[8:11], s43 offen offset:1024 sc1\n" \
"v_readlane_b32 s43, v202, 7\n" \
"s_nop 4\n" \
"buffer_load_dwordx4 a[56:59], v201, s[8:11], s43 offen sc1\n" \
"buffer_load_dwordx4 a[60:63], v201, s[8:11], s43 offen offset:1024 sc1\n" \
"s_add_u32 s43, s30, s36\n" \
"s_mov_b32 s50, 0\n" \
"s_branch .LS1_w8c\n" \
".LS1_w8l:\n" \
"buffer_load_dword v240, v204, s[4:7], 0 offen sc1\n" \
"s_waitcnt vmcnt(0)\n" \
".LS1_w8c:\n" \
"v_subrev_u32 v241, s43, v240\n" \
"v_cmp_gt_u32 vcc, 0x100, v241\n" \
"s_cmp_eq_u64 vcc, -1\n" \
"s_cbranch_scc1 .LS1_w8d\n" \
"s_sleep 2\n" \
"s_add_u32 s50, s50, 1\n" \
"s_cmp_lt_u32 s50, 0x20000\n" \
"s_cbranch_scc1 .LS1_w8l\n" \
"s_branch .LS1_end\n" \
".LS1_w8d:\n" \
"v_readlane_b32 s43, v202, 8\n" \
"s_nop 4\n" \
"buffer_load_dwordx4 a[64:67], v201, s[8:11], s43 offen sc1\n" \
"buffer_load_dwordx4 a[68:71], v201, s[8:11], s43 offen offset:1024 sc1\n" \
"v_readlane_b32 s43, v202, 9\n" \
"s_nop 4\n" \
"buffer_load_dwordx4 a[72:75], v201, s[8:11], s43 offen sc1\n" \
"buffer_load_dwordx4 a[76:79], v201, s[8:11], s43 offen offset:1024 sc1\n" \
"v_readlane_b32 s43, v202, 10\n" \
"s_nop 4\n" \
"buffer_load_dwordx4 a[80:83], v201, s[8:11], s43 offen sc1\n" \
"buffer_load_dwordx4 a[84:87], v201, s[8:11], s43 offen offset:1024 sc1\n" \
"v_readlane_b32 s43, v202, 11\n" \
"s_nop 4\n" \
"buffer_load_dwordx4 a[88:91], v201, s[8:11], s43 offen sc1\n" \
"buffer_load_dwordx4 a[92:95], v201, s[8:11], s43 offen offset:1024 sc1\n" \
"v_readlane_b32 s43, v202, 16\n" \
"s_nop 4\n" \
"buffer_load_dwordx4 a[128:131], v201, s[8:11], s43 offen sc1\n" \
"buffer_load_dwordx4 a[132:135], v201, s[8:11], s43 offen offset:1024 sc1\n" \
"v_readlane_b32 s43, v202, 19\n" \
"s_nop 4\n" \
"buffer_load_dwordx4 a[152:155], v201, s[8:11], s43 offen sc1\n" \
"buffer_load_dwordx4 a[156:159], v201, s[8:11], s43 offen offset:1024 sc1\n" \
"v_readlane_b32 s43, v202, 22\n" \
"s_nop 4\n" \
"buffer_load_dwordx4 a[176:179], v201, s[8:11], s43 offen sc1\n" \
"buffer_load_dwordx4 a[180:183], v201, s[8:11], s43 offen offset:1024 sc1\n" \
"v_readlane_b32 s43, v202, 25\n" \
"s_nop 4\n" \
"buffer_load_dwordx4 a[200:203], v201, s[8:11], s43 offen sc1\n" \
"buffer_load_dwordx4 a[204:207], v201, s[8:11], s43 offen offset:1024 sc1\n" \
"s_add_u32 s43, s30, 7\n" \
"s_mov_b32 s50, 0\n" \
"s_branch .LS1_w9c\n" \
".LS1_w9l:\n" \
"buffer_load_dword v240, v204, s[4:7], 0 offen sc1\n" \
"s_waitcnt vmcnt(0)\n" \
".LS1_w9c:\n" \
"v_subrev_u32 v241, s43, v240\n" \
"v_cmp_gt_u32 vcc, 0x100, v241\n" \
"s_cmp_eq_u64 vcc, -1\n" \
"s_cbranch_scc1 .LS1_w9d\n" \
"s_sleep 2\n" \
"s_add_u32 s50, s50, 1\n" \
"s_cmp_lt_u32 s50, 0x20000\n" \
"s_cbranch_scc1 .LS1_w9l\n" \
"s_branch .LS1_end\n" \
".LS1_w9d:\n" \
"v_readlane_b32 s43, v202, 18\n" \
"s_nop 4\n" \
"buffer_load_dwordx4 a[144:147], v201, s[8:11], s43 offen sc1\n" \
"buffer_load_dwordx4 a[148:151], v201, s[8:11], s43 offen offset:1024 sc1\n" \
"v_readlane_b32 s43, v202, 21\n" \
"s_nop 4\n" \
"buffer_load_dwordx4 a[168:171], v201, s[8:11], s43 offen sc1\n" \
"buffer_load_dwordx4 a[172:175], v201, s[8:11], s43 offen offset:1024 sc1\n" \
"v_readlane_b32 s43, v202, 24\n" \
"s_nop 4\n" \
"buffer_load_dwordx4 a[192:195], v201, s[8:11], s43 offen sc1\n" \
"buffer_load_dwordx4 a[196:199], v201, s[8:11], s43 offen offset:1024 sc1\n" \
"v_readlane_b32 s43, v202, 27\n" \
"s_nop 4\n" \
"buffer_load_dwordx4 a[216:219], v201, s[8:11], s43 offen sc1\n" \
"buffer_load_dwordx4 a[220:223], v201, s[8:11], s43 offen offset:1024 sc1\n" \
"s_mov_b32 s33, -1\n" \
"s_add_u32 s43, s30, 1\n" \
"s_mov_b32 s50, 0\n" \
".LS1_w10l:\n" \
"buffer_load_dword v240, v205, s[4:7], 0 offen sc1\n" \
"s_waitcnt vmcnt(0)\n" \
".LS1_w10c:\n" \
"v_subrev_u32 v241, s43, v240\n" \
"v_cmp_gt_u32 vcc, 0x100, v241\n" \
"s_cmp_eq_u64 vcc, -1\n" \
"s_cbranch_scc1 .LS1_w10d\n" \
"s_sleep 2\n" \
"s_add_u32 s50, s50, 1\n" \
"s_cmp_lt_u32 s50, 0x20000\n" \
"s_cbranch_scc1 .LS1_w10l\n" \
"s_branch .LS1_end\n" \
".LS1_w10d:\n" \
"v_readfirstlane_b32 s31, v241\n" \
"s_lshr_b32 s32, s31, 2\n" \
"s_xor_b32 s29, s31, s32\n" \
"s_and_b32 s29, s29, 3\n" \
"s_lshl_b32 s29, s29, 13\n" \
"s_bitset1_b32 s29, 21\n" \
"s_and_b32 s31, s31, 3\n" \
"s_lshl_b32 s31, s31, 13\n" \
"s_bitcmp1_b32 s39, 0\n" \
"s_cselect_b32 s43, s29, 0\n" \
"s_xor_b32 s31, s31, s43\n" \
"s_xor_b32 s32, s31, 0x4000\n" \
"s_mov_b32 s39, 0\n" \
".LS1_pre:\n" \
"s_barrier\n" \
"ds_read_b96 v[248:250], v252\n" \
"s_waitcnt lgkmcnt(0)\n" \
"v_readfirstlane_b32 s44, v248\n" \
"v_readfirstlane_b32 s45, v249\n" \
"v_readfirstlane_b32 s46, v250\n" \
"s_mul_i32 s42, s25, s46\n" \
"s_lshr_b32 s42, s42, 12\n" \
".LS1_cu:\n" \
"s_sub_u32 s43, s24, s44\n" \
"s_cmp_lt_i32 s43, 0\n" \
"s_cbranch_scc0 .LS1_cud\n" \
"s_add_u32 s24, s24, s25\n" \
"s_xor_b32 s31, s31, s29\n" \
"s_xor_b32 s32, s32, s29\n" \
"s_xor_b32 s47, s47, s29\n" \
"s_add_u32 s39, s39, 1\n" \
"s_branch .LS1_cu\n" \
".LS1_cud:\n" \
".LS1_loop:\n" \
"s_add_u32 s43, s40, 0\n" \
"s_and_b32 s43, s43, 3\n" \
"s_bitcmp1_b32 s43, 0\n" \
"s_cselect_b32 s47, s32, s31\n" \
"s_lshl_b32 s43, s43, 19\n" \
"s_add_u32 s47, s47, s43\n" \
"s_sub_u32 s43, s24, s44\n" \
"s_max_i32 s43, s43, 0xfffd0000\n" \
"s_min_i32 s43, s43, 0x4000\n" \
"s_mul_i32 s43, s43, s46\n" \
"s_ashr_i32 s43, s43, 12\n" \
"s_add_u32 s41, s45, s43\n" \
"s_add_u32 s41, s41, 4\n" \
"s_branch .LS1_a1\n" \
".LS1_retry:\n" \
"s_add_u32 s51, s51, 1\n" \
"s_cmp_gt_u32 s51, 0x20000\n" \
"s_cbranch_scc1 .LS1_end\n" \
".LS1_a0:\n" \
"s_add_u32 s41, s41, s42\n" \
"s_add_u32 s24, s24, s25\n" \
"s_xor_b32 s31, s31, s29\n" \
"s_xor_b32 s32, s32, s29\n" \
"s_xor_b32 s47, s47, s29\n" \
"s_xor_b32 s48, s48, s29\n" \
"s_add_u32 s39, s39, 1\n" \
".p2align 6\n" \
".LS1_a1:\n" \
"s_memtime vcc\n" \
"s_waitcnt lgkmcnt(0)\n" \
"s_sub_u32 s43, s41, vcc_lo\n" \
"s_cmp_gt_i32 s43, 128\n" \
"s_cbranch_scc1 .LS1_a1\n" \
"s_cmp_lt_i32 s43, s37\n" \
"s_cbranch_scc1 .LS1_a0\n" \
"s_max_i32 s43, s43, 0\n" \
"s_and_b32 s43, s43, -4\n" \
"s_getpc_b64 vcc\n" \
".LS1_a3:\n" \
"s_sub_u32 s43, .LS1_a4-.LS1_a3, s43\n" \
"s_add_u32 vcc_lo, vcc_lo, s43\n" \
"s_addc_u32 vcc_hi, vcc_hi, 0\n" \
"s_setpc_b64 vcc\n" \
".rept 32\n" \
"s_nop 0\n" \
".endr\n" \
".LS1_a4:\n" \
"buffer_load_dwordx4 v[0:3], v208, s[4:7], s47 offen offset:1024 sc1\n" \
"s_nop 9\n" \
"buffer_load_dwordx4 v[4:7], v208, s[4:7], s47 offen offset:2048 sc1\n" \
"s_nop 9\n" \
"buffer_load_dwordx4 v[8:11], v208, s[4:7], s47 offen sc1\n" \
"s_nop 9\n" \
"buffer_load_dwordx4 v[12:15], v210, s[4:7], s47 offen sc1\n" \
"s_add_u32 s35, s35, 1\n" \
"v_mov_b32 v28, 0\n" \
"v_mov_b32 v29, 0\n" \
"v_mov_b32 v30, 0\n" \
"v_mov_b32 v31, 0\n" \
"v_mov_b32 v32, 0\n" \
"v_mov_b32 v33, 0\n" \
"v_mov_b32 v34, 0\n" \
"v_mov_b32 v35, 0\n" \
"v_mov_b32 v36, 0\n" \
"v_mov_b32 v37, 0\n" \
"v_mov_b32 v38, 0\n" \
"v_mov_b32 v39, 0\n" \
"v_mov_b32 v40, 0\n" \
"v_mov_b32 v41, 0\n" \
"v_mov_b32 v42, 0\n" \
"v_mov_b32 v43, 0\n" \
"v_mov_b32 v44, 0\n" \
"v_mov_b32 v45, 0\n" \
"v_mov_b32 v46, 0\n" \
"v_mov_b32 v47, 0\n" \
"v_mov_b32 v48, 0\n" \
"v_mov_b32 v49, 0\n" \
"v_mov_b32 v50, 0\n" \
"v_mov_b32 v51, 0\n" \
"v_mov_b32 v52, 0\n" \
"v_mov_b32 v53, 0\n" \
"v_mov_b32 v54, 0\n" \
"v_mov_b32 v55, 0\n" \
"v_mov_b32 v56, 0\n" \
"v_mov_b32 v57, 0\n" \
"v_mov_b32 v58, 0\n" \
"v_mov_b32 v59, 0\n" \
"s_waitcnt vmcnt(3)\n" \
"v_smfmac_f32_16x16x64_bf16 v[28:31], v[0:3], a[0:7], v200\n" \
"v_smfmac_f32_16x16x64_bf16 v[32:35], v[0:3], a[8:15], v200\n" \
"v_smfmac_f32_16x16x64_bf16 v[36:39], v[0:3], a[16:23], v200\n" \
"v_smfmac_f32_16x16x64_bf16 v[40:43], v[0:3], a[24:31], v200\n" \
"v_smfmac_f32_16x16x64_bf16 v[44:47], v[0:3], a[32:39], v200\n" \
"v_smfmac_f32_16x16x64_bf16 v[48:51], v[0:3], a[40:47], v200\n" \
"v_smfmac_f32_16x16x64_bf16 v[52:55], v[0:3], a[48:55], v200\n" \
"v_smfmac_f32_16x16x64_bf16 v[56:59], v[0:3], a[56:63], v200\n" \
"s_waitcnt vmcnt(2)\n" \
"v_smfmac_f32_16x16x64_bf16 v[28:31], v[4:7], a[64:71], v200\n" \
"v_smfmac_f32_16x16x64_bf16 v[32:35], v[4:7], a[72:79], v200\n" \
"v_smfmac_f32_16x16x64_bf16 v[36:39], v[4:7], a[80:87], v200\n" \
"v_smfmac_f32_16x16x64_bf16 v[40:43], v[4:7], a[88:95], v200\n" \
"s_waitcnt vmcnt(1)\n" \
"v_smfmac_f32_16x16x64_bf16 v[28:31], v[8:11], a[96:103], v200\n" \
"s_nop 2\n" \
"v_smfmac_f32_16x16x64_bf16 v[32:35], v[8:11], a[104:111], v200\n" \
"s_nop 2\n" \
"v_smfmac_f32_16x16x64_bf16 v[36:39], v[8:11], a[112:119], v200\n" \
"s_nop 0\n" \
"v_cvt_pk_bf16_f32 v128, v28, v29\n" \
"v_cvt_pk_bf16_f32 v129, v30, v31\n" \
"v_smfmac_f32_16x16x64_bf16 v[40:43], v[8:11], a[120:127], v200\n" \
"ds_write_b64 v211, v[128:129] offset:128\n" \
"v_cvt_pk_bf16_f32 v130, v32, v33\n" \
"v_cvt_pk_bf16_f32 v131, v34, v35\n" \
"v_smfmac_f32_16x16x64_bf16 v[44:47], v[4:7], a[128:135], v200\n" \
"ds_write_b64 v211, v[130:131] offset:2304\n" \
"v_cvt_pk_bf16_f32 v132, v36, v37\n" \
"v_cvt_pk_bf16_f32 v133, v38, v39\n" \
"v_smfmac_f32_16x16x64_bf16 v[44:47], v[8:11], a[136:143], v200\n" \
"s_waitcnt vmcnt(0)\n" \
"ds_write_b64 v211, v[132:133] offset:2688\n" \
"v_cvt_pk_bf16_f32 v134, v40, v41\n" \
"v_cvt_pk_bf16_f32 v135, v42, v43\n" \
"v_smfmac_f32_16x16x64_bf16 v[44:47], v[12:15], a[144:151], v200\n" \
"ds_write_b64 v211, v[134:135] offset:4864\n" \
"s_nop 1\n" \
"v_smfmac_f32_16x16x64_bf16 v[48:51], v[4:7], a[152:159], v200\n" \
"s_nop 2\n" \
"v_smfmac_f32_16x16x64_bf16 v[48:51], v[8:11], a[160:167], v200\n" \
"s_nop 0\n" \
"v_cvt_pk_bf16_f32 v136, v44, v45\n" \
"v_cvt_pk_bf16_f32 v137, v46, v47\n" \
"v_smfmac_f32_16x16x64_bf16 v[48:51], v[12:15], a[168:175], v200\n" \
"ds_write_b64 v211, v[136:137] offset:9344\n" \
"s_nop 1\n" \
"v_smfmac_f32_16x16x64_bf16 v[52:55], v[4:7], a[176:183], v200\n" \
"s_waitcnt lgkmcnt(1)\n" \
"s_barrier\n" \
"s_nop 0\n" \
"v_smfmac_f32_16x16x64_bf16 v[52:55], v[8:11], a[184:191], v200\n" \
"s_nop 0\n" \
"v_cvt_pk_bf16_f32 v138, v48, v49\n" \
"v_cvt_pk_bf16_f32 v139, v50, v51\n" \
"v_smfmac_f32_16x16x64_bf16 v[52:55], v[12:15], a[192:199], v200\n" \
"ds_write_b64 v211, v[138:139] offset:11520\n" \
"s_nop 1\n" \
"v_smfmac_f32_16x16x64_bf16 v[56:59], v[4:7], a[200:207], v200\n" \
"s_nop 2\n" \
"v_smfmac_f32_16x16x64_bf16 v[56:59], v[8:11], a[208:215], v200\n" \
"s_nop 2\n" \
"v_smfmac_f32_16x16x64_bf16 v[56:59], v[12:15], a[216:223], v200\n" \
"v_cvt_pk_bf16_f32 v140, v52, v53\n" \
"v_cvt_pk_bf16_f32 v141, v54, v55\n" \
"s_nop 1\n" \
"ds_write_b64 v211, v[140:141] offset:11904\n" \
"s_nop 2\n" \
"v_cvt_pk_bf16_f32 v142, v56, v57\n" \
"v_cvt_pk_bf16_f32 v143, v58, v59\n" \
"ds_write_b64 v211, v[142:143] offset:14080\n" \
"s_waitcnt lgkmcnt(1)\n" \
"s_barrier\n" \
"s_waitcnt lgkmcnt(0)\n" \
"s_barrier\n" \
"s_nop 3\n" \
"ds_read_b32 v64, v252 offset:20\n" \
"s_waitcnt lgkmcnt(0)\n" \
"v_readfirstlane_b32 s43, v64\n" \
"s_cmp_eq_u32 s43, s35\n" \
"s_cbranch_scc1 .LS1_retry\n" \
"s_nop 15\n" \
"v_max3_u32 v64, v0, v0, v1\n" \
"v_max3_u32 v64, v64, v2, v3\n" \
"v_max3_u32 v64, v64, v4, v5\n" \
"v_max3_u32 v64, v64, v6, v7\n" \
"v_max3_u32 v64, v64, v8, v9\n" \
"v_max3_u32 v64, v64, v10, v11\n" \
"v_max3_u32 v64, v64, v12, v13\n" \
"v_max3_u32 v64, v64, v14, v15\n" \
"v_cmp_eq_u32 vcc, -1, v64\n" \
"s_cbranch_vccnz .LS1_veto\n" \
".LS1_back:\n" \
"s_waitcnt lgkmcnt(0)\n" \
"s_barrier\n" \
"ds_read_b32 v64, v252 offset:16\n" \
"ds_read_b64 v[246:247], v252 offset:24\n" \
"ds_read_b96 v[248:250], v252\n" \
"s_waitcnt lgkmcnt(0)\n" \
"v_readfirstlane_b32 s44, v248\n" \
"v_readfirstlane_b32 s45, v249\n" \
"v_readfirstlane_b32 s46, v250\n" \
"v_readfirstlane_b32 s26, v246\n" \
"v_readfirstlane_b32 s27, v247\n" \
"s_mul_i32 s42, s25, s46\n" \
"s_lshr_b32 s42, s42, 12\n" \
"v_readfirstlane_b32 s43, v64\n" \
"s_cmp_eq_u32 s43, s35\n" \
"s_cbranch_scc1 .LS1_retry\n" \
"s_add_u32 s40, s40, 1\n" \
"s_add_u32 s24, s24, s26\n" \
"s_add_u32 s24, s24, s27\n" \
"s_cmp_eq_u32 s40, 0\n" \
"s_cbranch_scc1 .LS1_wet\n" \
"s_cmp_lt_u32 s40, 63\n" \
"s_cbranch_scc1 .LS1_loop\n" \
".LS1_end:\n" \
"s_endpgm\n" \
".LS1_veto:\n" \
"v_mov_b32 v64, s35\n" \
"ds_write_b32 v252, v64 offset:16\n" \
"s_branch .LS1_back\n" \
".p2align 6\n" \
"fk_role_S2:\n" \
"ds_read_b32 v200, v255 offset:0\n" \
"ds_read_b32 v201, v255 offset:256\n" \
"ds_read_b32 v202, v255 offset:512\n" \
"ds_read_b32 v203, v255 offset:768\n" \
"ds_read_b32 v204, v255 offset:1024\n" \
"ds_read_b32 v205, v255 offset:1280\n" \
"ds_read_b32 v206, v255 offset:1536\n" \
"ds_read_b32 v207, v255 offset:1792\n" \
"ds_read_b32 v208, v255 offset:2048\n" \
"ds_read_b32 v209, v255 offset:2304\n" \
"ds_read_b32 v210, v255 offset:2560\n" \
"ds_read_b32 v211, v255 offset:2816\n" \
"ds_read_b32 v212, v255 offset:3072\n" \
"ds_read_b32 v213, v255 offset:3328\n" \
"ds_read_b32 v214, v255 offset:3584\n" \
"ds_read_b32 v215, v255 offset:3840\n" \
"ds_read_b32 v254, v255 offset:7936\n" \
"s_waitcnt lgkmcnt(0)\n" \
"v_readlane_b32 s4, v254, 0\n" \
"v_readlane_b32 s5, v254, 1\n" \
"v_readlane_b32 s6, v254, 2\n" \
"v_readlane_b32 s7, v254, 3\n" \
"v_readlane_b32 s8, v254, 4\n" \
"v_readlane_b32 s9, v254, 5\n" \
"v_readlane_b32 s10, v254, 6\n" \
"v_readlane_b32 s11, v254, 7\n" \
"v_readlane_b32 s12, v254, 8\n" \
"v_readlane_b32 s13, v254, 9\n" \
"v_readlane_b32 s14, v254, 10\n" \
"v_readlane_b32 s15, v254, 11\n" \
"v_readlane_b32 s16, v254, 12\n" \
"v_readlane_b32 s17, v254, 13\n" \
"v_readlane_b32 s18, v254, 14\n" \
"v_readlane_b32 s19, v254, 15\n" \
"v_readlane_b32 s20, v254, 16\n" \
"v_readlane_b32 s21, v254, 17\n" \
"v_readlane_b32 s22, v254, 18\n" \
"v_readlane_b32 s23, v254, 19\n" \
"v_readlane_b32 s24, v254, 20\n" \
"v_readlane_b32 s25, v254, 21\n" \
"v_readlane_b32 s26, v254, 22\n" \
"v_readlane_b32 s27, v254, 23\n" \
"v_readlane_b32 s28, v254, 24\n" \
"v_readlane_b32 s29, v254, 25\n" \
"v_readlane_b32 s30, v254, 26\n" \
"v_readlane_b32 s31, v254, 27\n" \
"v_readlane_b32 s32, v254, 28\n" \
"v_readlane_b32 s33, v254, 29\n" \
"v_readlane_b32 s34, v254, 30\n" \
"v_readlane_b32 s35, v254, 31\n" \
"v_readlane_b32 s36, v254, 32\n" \
"v_readlane_b32 s37, v254, 33\n" \
"v_readlane_b32 s38, v254, 34\n" \
"v_readlane_b32 s39, v254, 35\n" \
"s_mov_b32 s40, 0\n" \
"s_mov_b32 s51, 0\n" \
"s_nop 0\n" \
"v_mov_b32 v252, s38\n" \
"s_nop 4\n" \
"v_mov_b32 v188, -1\n" \
"s_mov_b32 s28, s33\n" \
"s_mov_b32 s33, -1\n" \
"v_writelane_b32 v251, s6, 0\n" \
"v_writelane_b32 v251, s10, 1\n" \
"v_writelane_b32 v251, s14, 2\n" \
"v_writelane_b32 v251, s18, 3\n" \
"s_mov_b32 s6, 0\n" \
"s_mov_b32 s10, 0\n" \
"s_mov_b32 s14, 0\n" \
"s_mov_b32 s18, 0\n" \
"s_mov_b32 s40, -1\n" \
"s_branch .LS2_pre\n" \
".LS2_wet:\n" \
"s_waitcnt vmcnt(0)\n" \
"v_readlane_b32 s6, v251, 0\n" \
"v_readlane_b32 s10, v251, 1\n" \
"v_readlane_b32 s14, v251, 2\n" \
"v_readlane_b32 s18, v251, 3\n" \
"s_nop 4\n" \
"ds_read_b32 v230, v255 offset:6912\n" \
"v_readlane_b32 s60, v254, 12\n" \
"v_readlane_b32 s61, v254, 13\n" \
"v_readlane_b32 s62, v254, 14\n" \
"v_readlane_b32 s63, v254, 15\n" \
"s_waitcnt lgkmcnt(0)\n" \
"s_nop 3\n" \
"buffer_load_dword v228, v230, s[60:63], 0 offen\n" \
"s_mov_b32 s33, s28\n" \
"s_add_u32 s34, s33, 2\n" \
"s_add_u32 s36, s33, 4\n" \
"s_add_u32 s43, s30, s33\n" \
"s_mov_b32 s50, 0\n" \
".LS2_w11l:\n" \
"buffer_load_dword v240, v204, s[4:7], 0 offen sc1\n" \
"s_waitcnt vmcnt(0)\n" \
".LS2_w11c:\n" \
"v_subrev_u32 v241, s43, v240\n" \
"v_cmp_gt_u32 vcc, 0x100, v241\n" \
"s_cmp_eq_u64 vcc, -1\n" \
"s_cbranch_scc1 .LS2_w11d\n" \
"s_sleep 2\n" \
"s_add_u32 s50, s50, 1\n" \
"s_cmp_lt_u32 s50, 0x20000\n" \
"s_cbranch_scc1 .LS2_w11l\n" \
"s_branch .LS2_end\n" \
".LS2_w11d:\n" \
"v_readlane_b32 s43, v202, 8\n" \
"s_nop 4\n" \
"buffer_load_dwordx4 a[64:67], v201, s[8:11], s43 offen sc1\n" \
"buffer_load_dwordx4 a[68:71], v201, s[8:11], s43 offen offset:1024 sc1\n" \
"v_readlane_b32 s43, v202, 9\n" \
"s_nop 4\n" \
"buffer_load_dwordx4 a[72:75], v201, s[8:11], s43 offen sc1\n" \
"buffer_load_dwordx4 a[76:79], v201, s[8:11], s43 offen offset:1024 sc1\n" \
"v_readlane_b32 s43, v202, 10\n" \
"s_nop 4\n" \
"buffer_load_dwordx4 a[80:83], v201, s[8:11], s43 offen sc1\n" \
"buffer_load_dwordx4 a[84:87], v201, s[8:11], s43 offen offset:1024 sc1\n" \
"v_readlane_b32 s43, v202, 11\n" \
"s_nop 4\n" \
"buffer_load_dwordx4 a[88:91], v201, s[8:11], s43 offen sc1\n" \
"buffer_load_dwordx4 a[92:95], v201, s[8:11], s43 offen offset:1024 sc1\n" \
"v_readlane_b32 s43, v202, 16\n" \
"s_nop 4\n" \
"buffer_load_dwordx4 a[128:131], v201, s[8:11], s43 offen sc1\n" \
"buffer_load_dwordx4 a[132:135], v201, s[8:11], s43 offen offset:1024 sc1\n" \
"v_readlane_b32 s43, v202, 19\n" \
"s_nop 4\n" \
"buffer_load_dwordx4 a[152:155], v201, s[8:11], s43 offen sc1\n" \
"buffer_load_dwordx4 a[156:159], v201, s[8:11], s43 offen offset:1024 sc1\n" \
"v_readlane_b32 s43, v202, 22\n" \
"s_nop 4\n" \
"buffer_load_dwordx4 a[176:179], v201, s[8:11], s43 offen sc1\n" \
"buffer_load_dwordx4 a[180:183], v201, s[8:11], s43 offen offset:1024 sc1\n" \
"v_readlane_b32 s43, v202, 25\n" \
"s_nop 4\n" \
"buffer_load_dwordx4 a[200:203], v201, s[8:11], s43 offen sc1\n" \
"buffer_load_dwordx4 a[204:207], v201, s[8:11], s43 offen offset:1024 sc1\n" \
"s_add_u32 s43, s30, s34\n" \
"s_mov_b32 s50, 0\n" \
"s_branch .LS2_w12c\n" \
".LS2_w12l:\n" \
"buffer_load_dword v240, v204, s[4:7], 0 offen sc1\n" \
"s_waitcnt vmcnt(0)\n" \
".LS2_w12c:\n" \
"v_subrev_u32 v241, s43, v240\n" \
"v_cmp_gt_u32 vcc, 0x100, v241\n" \
"s_cmp_eq_u64 vcc, -1\n" \
"s_cbranch_scc1 .LS2_w12d\n" \
"s_sleep 2\n" \
"s_add_u32 s50, s50, 1\n" \
"s_cmp_lt_u32 s50, 0x20000\n" \
"s_cbranch_scc1 .LS2_w12l\n" \
"s_branch .LS2_end\n" \
".LS2_w12d:\n" \
"v_readlane_b32 s43, v202, 12\n" \
"s_nop 4\n" \
"buffer_load_dwordx4 a[96:99], v201, s[8:11], s43 offen sc1\n" \
"buffer_load_dwordx4 a[100:103], v201, s[8:11], s43 offen offset:1024 sc1\n" \
"v_readlane_b32 s43, v202, 13\n" \
"s_nop 4\n" \
"buffer_load_dwordx4 a[104:107], v201, s[8:11], s43 offen sc1\n" \
"buffer_load_dwordx4 a[108:111], v201, s[8:11], s43 offen offset:1024 sc1\n" \
"v_readlane_b32 s43, v202, 14\n" \
"s_nop 4\n" \
"buffer_load_dwordx4 a[112:115], v201, s[8:11], s43 offen sc1\n" \
"buffer_load_dwordx4 a[116:119], v201, s[8:11], s43 offen offset:1024 sc1\n" \
"v_readlane_b32 s43, v202, 15\n" \
"s_nop 4\n" \
"buffer_load_dwordx4 a[120:123], v201, s[8:11], s43 offen sc1\n" \
"buffer_load_dwordx4 a[124:127], v201, s[8:11], s43 offen offset:1024 sc1\n" \
"v_readlane_b32 s43, v202, 17\n" \
"s_nop 4\n" \
"buffer_load_dwordx4 a[136:139], v201, s[8:11], s43 offen sc1\n" \
"buffer_load_dwordx4 a[140:143], v201, s[8:11], s43 offen offset:1024 sc1\n" \
"v_readlane_b32 s43, v202, 20\n" \
"s_nop 4\n" \
"buffer_load_dwordx4 a[160:163], v201, s[8:11], s43 offen sc1\n" \
"buffer_load_dwordx4 a[164:167], v201, s[8:11], s43 offen offset:1024 sc1\n" \
"v_readlane_b32 s43, v202, 23\n" \
"s_nop 4\n" \
"buffer_load_dwordx4 a[184:187], v201, s[8:11], s43 offen sc1\n" \
"buffer_load_dwordx4 a[188:191], v201, s[8:11], s43 offen offset:1024 sc1\n" \
"v_readlane_b32 s43, v202, 26\n" \
"s_nop 4\n" \
"buffer_load_dwordx4 a[208:211], v201, s[8:11], s43 offen sc1\n" \
"buffer_load_dwordx4 a[212:215], v201, s[8:11], s43 offen offset:1024 sc1\n" \
"s_add_u32 s43, s30, s36\n" \
"s_mov_b32 s50, 0\n" \
"s_branch .LS2_w13c\n" \
".LS2_w13l:\n" \
"buffer_load_dword v240, v204, s[4:7], 0 offen sc1\n" \
"s_waitcnt vmcnt(0)\n" \
".LS2_w13c:\n" \
"v_subrev_u32 v241, s43, v240\n" \
"v_cmp_gt_u32 vcc, 0x100, v241\n" \
"s_cmp_eq_u64 vcc, -1\n" \
"s_cbranch_scc1 .LS2_w13d\n" \
"s_sleep 2\n" \
"s_add_u32 s50, s50, 1\n" \
"s_cmp_lt_u32 s50, 0x20000\n" \
"s_cbranch_scc1 .LS2_w13l\n" \
"s_branch .LS2_end\n" \
".LS2_w13d:\n" \
"v_readlane_b32 s43, v202, 0\n" \
"s_nop 4\n" \
"buffer_load_dwordx4 a[0:3], v201, s[8:11], s43 offen sc1\n" \
"buffer_load_dwordx4 a[4:7], v201, s[8:11], s43 offen offset:1024 sc1\n" \
"v_readlane_b32 s43, v202, 1\n" \
"s_nop 4\n" \
"buffer_load_dwordx4 a[8:11], v201, s[8:11], s43 offen sc1\n" \
"buffer_load_dwordx4 a[12:15], v201, s[8:11], s43 offen offset:1024 sc1\n" \
"v_readlane_b32 s43, v202, 2\n" \
"s_nop 4\n" \
"buffer_load_dwordx4 a[16:19], v201, s[8:11], s43 offen sc1\n" \
"buffer_load_dwordx4 a[20:23], v201, s[8:11], s43 offen offset:1024 sc1\n" \
"v_readlane_b32 s43, v202, 3\n" \
"s_nop 4\n" \
"buffer_load_dwordx4 a[24:27], v201, s[8:11], s43 offen sc1\n" \
"buffer_load_dwordx4 a[28:31], v201, s[8:11], s43 offen offset:1024 sc1\n" \
"v_readlane_b32 s43, v202, 4\n" \
"s_nop 4\n" \
"buffer_load_dwordx4 a[32:35], v201, s[8:11], s43 offen sc1\n" \
"buffer_load_dwordx4 a[36:39], v201, s[8:11], s43 offen offset:1024 sc1\n" \
"v_readlane_b32 s43, v202, 5\n" \
"s_nop 4\n" \
"buffer_load_dwordx4 a[40:43], v201, s[8:11], s43 offen sc1\n" \
"buffer_load_dwordx4 a[44:47], v201, s[8:11], s43 offen offset:1024 sc1\n" \
"v_readlane_b32 s43, v202, 6\n" \
"s_nop 4\n" \
"buffer_load_dwordx4 a[48:51], v201, s[8:11], s43 offen sc1\n" \
"buffer_load_dwordx4 a[52:55], v201, s[8:11], s43 offen offset:1024 sc1\n" \
"v_readlane_b32 s43, v202, 7\n" \
"s_nop 4\n" \
"buffer_load_dwordx4 a[56:59], v201, s[8:11], s43 offen sc1\n" \
"buffer_load_dwordx4 a[60:63], v201, s[8:11], s43 offen offset:1024 sc1\n" \
"s_add_u32 s43, s30, 7\n" \
"s_mov_b32 s50, 0\n" \
"s_branch .LS2_w14c\n" \
".LS2_w14l:\n" \
"buffer_load_dword v240, v204, s[4:7], 0 offen sc1\n" \
"s_waitcnt vmcnt(0)\n" \
".LS2_w14c:\n" \
"v_subrev_u32 v241, s43, v240\n" \
"v_cmp_gt_u32 vcc, 0x100, v241\n" \
"s_cmp_eq_u64 vcc, -1\n" \
"s_cbranch_scc1 .LS2_w14d\n" \
"s_sleep 2\n" \
"s_add_u32 s50, s50, 1\n" \
"s_cmp_lt_u32 s50, 0x20000\n" \
"s_cbranch_scc1 .LS2_w14l\n" \
"s_branch .LS2_end\n" \
".LS2_w14d:\n" \
"v_readlane_b32 s43, v202, 18\n" \
"s_nop 4\n" \
"buffer_load_dwordx4 a[144:147], v201, s[8:11], s43 offen sc1\n" \
"buffer_load_dwordx4 a[148:151], v201, s[8:11], s43 offen offset:1024 sc1\n" \
"v_readlane_b32 s43, v202, 21\n" \
"s_nop 4\n" \
"buffer_load_dwordx4 a[168:171], v201, s[8:11], s43 offen sc1\n" \
"buffer_load_dwordx4 a[172:175], v201, s[8:11], s43 offen offset:1024 sc1\n" \
"v_readlane_b32 s43, v202, 24\n" \
"s_nop 4\n" \
"buffer_load_dwordx4 a[192:195], v201, s[8:11], s43 offen sc1\n" \
"buffer_load_dwordx4 a[196:199], v201, s[8:11], s43 offen offset:1024 sc1\n" \
"v_readlane_b32 s43, v202, 27\n" \
"s_nop 4\n" \
"buffer_load_dwordx4 a[216:219], v201, s[8:11], s43 offen sc1\n" \
"buffer_load_dwordx4 a[220:223], v201, s[8:11], s43 offen offset:1024 sc1\n" \
"s_mov_b32 s33, -1\n" \
"ds_read_b96 v[248:250], v252\n" \
"s_waitcnt lgkmcnt(0)\n" \
"v_readfirstlane_b32 s44, v248\n" \
"v_readfirstlane_b32 s45, v249\n" \
"v_readfirstlane_b32 s46, v250\n" \
"s_mul_i32 s42, s25, s46\n" \
"s_lshr_b32 s42, s42, 12\n" \
"s_mov_b32 s26, s44\n" \
"s_mov_b32 s27, s45\n" \
".p2align 3\n" \
"s_memtime s[54:55]\n" \
"s_memrealtime s[56:57]\n" \
"s_memtime s[58:59]\n" \
"s_memrealtime s[60:61]\n" \
"s_nop 0\n" \
"s_nop 0\n" \
"s_memtime s[62:63]\n" \
"s_memrealtime s[64:65]\n" \
"s_memtime s[66:67]\n" \
"s_memrealtime s[68:69]\n" \
"s_memtime s[70:71]\n" \
"s_memrealtime s[72:73]\n" \
"s_memtime s[74:75]\n" \
"s_memrealtime s[76:77]\n" \
"s_nop 0\n" \
"s_nop 0\n" \
"s_memtime s[78:79]\n" \
"s_memrealtime s[80:81]\n" \
"s_memtime s[82:83]\n" \
"s_memrealtime s[84:85]\n" \
"s_memtime s[86:87]\n" \
"s_memrealtime s[88:89]\n" \
"s_memtime s[90:91]\n" \
"s_memrealtime s[92:93]\n" \
"s_nop 0\n" \
"s_nop 0\n" \
"s_memtime s[94:95]\n" \
"s_memrealtime s[96:97]\n" \
"s_memtime s[98:99]\n" \
"s_mov_b32 s52, 0\n" \
"s_mov_b32 s53, 4096\n" \
"s_waitcnt lgkmcnt(0)\n" \
"s_cmp_lg_u32 s96, s56\n" \
"s_cselect_b32 s52, s90, s52\n" \
"s_cselect_b32 s53, s98, s53\n" \
"s_cmp_lg_u32 s92, s56\n" \
"s_cselect_b32 s52, s86, s52\n" \
"s_cselect_b32 s53, s94, s53\n" \
"s_cmp_lg_u32 s88, s56\n" \
"s_cselect_b32 s52, s82, s52\n" \
"s_cselect_b32 s53, s90, s53\n" \
"s_cmp_lg_u32 s84, s56\n" \
"s_cselect_b32 s52, s78, s52\n" \
"s_cselect_b32 s53, s86, s53\n" \
"s_cmp_lg_u32 s80, s56\n" \
"s_cselect_b32 s52, s74, s52\n" \
"s_cselect_b32 s53, s82, s53\n" \
"s_cmp_lg_u32 s76, s56\n" \
"s_cselect_b32 s52, s70, s52\n" \
"s_cselect_b32 s53, s78, s53\n" \
"s_cmp_lg_u32 s72, s56\n" \
"s_cselect_b32 s52, s66, s52\n" \
"s_cselect_b32 s53, s74, s53\n" \
"s_cmp_lg_u32 s68, s56\n" \
"s_cselect_b32 s52, s62, s52\n" \
"s_cselect_b32 s53, s70, s53\n" \
"s_cmp_lg_u32 s64, s56\n" \
"s_cselect_b32 s52, s58, s52\n" \
"s_cselect_b32 s53, s66, s53\n" \
"s_cmp_lg_u32 s60, s56\n" \
"s_cselect_b32 s52, s54, s52\n" \
"s_cselect_b32 s53, s62, s53\n" \
"s_sub_u32 s53, s53, s52\n" \
"s_lshr_b32 s55, s53, 1\n" \
"s_add_u32 s52, s52, s55\n" \
"s_add_u32 s55, s56, 4\n" \
"s_lshl_b32 s55, s55, 4\n" \
"s_cmp_le_u32 s53, 40\n" \
"s_cselect_b32 s44, s55, s44\n" \
"s_cselect_b32 s45, s52, s45\n" \
"v_mov_b32 v248, s44\n" \
"v_mov_b32 v249, s45\n" \
"v_mov_b32 v250, s46\n" \
"s_mov_b64 exec, 1\n" \
"ds_write_b96 v252, v[248:250]\n" \
"s_mov_b64 exec, -1\n" \
"s_sub_u32 s26, s44, s26\n" \
"s_sub_u32 s27, s45, s27\n" \
"v_cvt_f32_u32 v248, s26\n" \
"v_cvt_f32_u32 v249, s27\n" \
"v_rcp_f32 v248, v248\n" \
"v_mul_f32 v249, 0x457ae148, v249\n" \
"v_mul_f32 v248, v248, v249\n" \
"v_cvt_u32_f32 v248, v248\n" \
"s_nop 0\n" \
"v_readfirstlane_b32 s27, v248\n" \
"s_max_u32 s27, s27, 0x800\n" \
"s_min_u32 s27, s27, 0x2000\n" \
"s_cmp_gt_u32 s26, 1600\n" \
"s_cselect_b32 s46, s27, s46\n" \
".p2align 3\n" \
"s_memtime s[54:55]\n" \
"s_memrealtime s[56:57]\n" \
"s_memtime s[58:59]\n" \
"s_memrealtime s[60:61]\n" \
"s_nop 0\n" \
"s_nop 0\n" \
"s_memtime s[62:63]\n" \
"s_memrealtime s[64:65]\n" \
"s_memtime s[66:67]\n" \
"s_memrealtime s[68:69]\n" \
"s_memtime s[70:71]\n" \
"s_memrealtime s[72:73]\n" \
"s_memtime s[74:75]\n" \
"s_memrealtime s[76:77]\n" \
"s_nop 0\n" \
"s_nop 0\n" \
"s_memtime s[78:79]\n" \
"s_memrealtime s[80:81]\n" \
"s_memtime s[82:83]\n" \
"s_memrealtime s[84:85]\n" \
"s_memtime s[86:87]\n" \
"s_memrealtime s[88:89]\n" \
"s_memtime s[90:91]\n" \
"s_memrealtime s[92:93]\n" \
"s_nop 0\n" \
"s_nop 0\n" \
"s_memtime s[94:95]\n" \
"s_memrealtime s[96:97]\n" \
"s_memtime s[98:99]\n" \
"s_mov_b32 s52, 0\n" \
"s_mov_b32 s53, 4096\n" \
"s_waitcnt lgkmcnt(0)\n" \
"s_cmp_lg_u32 s96, s56\n" \
"s_cselect_b32 s52, s90, s52\n" \
"s_cselect_b32 s53, s98, s53\n" \
"s_cmp_lg_u32 s92, s56\n" \
"s_cselect_b32 s52, s86, s52\n" \
"s_cselect_b32 s53, s94, s53\n" \
"s_cmp_lg_u32 s88, s56\n" \
"s_cselect_b32 s52, s82, s52\n" \
"s_cselect_b32 s53, s90, s53\n" \
"s_cmp_lg_u32 s84, s56\n" \
"s_cselect_b32 s52, s78, s52\n" \
"s_cselect_b32 s53, s86, s53\n" \
"s_cmp_lg_u32 s80, s56\n" \
"s_cselect_b32 s52, s74, s52\n" \
"s_cselect_b32 s53, s82, s53\n" \
"s_cmp_lg_u32 s76, s56\n" \
"s_cselect_b32 s52, s70, s52\n" \
"s_cselect_b32 s53, s78, s53\n" \
"s_cmp_lg_u32 s72, s56\n" \
"s_cselect_b32 s52, s66, s52\n" \
"s_cselect_b32 s53, s74, s53\n" \
"s_cmp_lg_u32 s68, s56\n" \
"s_cselect_b32 s52, s62, s52\n" \
"s_cselect_b32 s53, s70, s53\n" \
"s_cmp_lg_u32 s64, s56\n" \
"s_cselect_b32 s52, s58, s52\n" \
"s_cselect_b32 s53, s66, s53\n" \
"s_cmp_lg_u32 s60, s56\n" \
"s_cselect_b32 s52, s54, s52\n" \
"s_cselect_b32 s53, s62, s53\n" \
"s_sub_u32 s53, s53, s52\n" \
"s_lshr_b32 s55, s53, 1\n" \
"s_add_u32 s52, s52, s55\n" \
"s_add_u32 s55, s56, 4\n" \
"s_lshl_b32 s55, s55, 4\n" \
"s_cmp_le_u32 s53, 40\n" \
"s_cselect_b32 s44, s55, s44\n" \
"s_cselect_b32 s45, s52, s45\n" \
"v_mov_b32 v248, s44\n" \
"v_mov_b32 v249, s45\n" \
"v_mov_b32 v250, s46\n" \
"s_mov_b64 exec, 1\n" \
"ds_write_b96 v252, v[248:250]\n" \
"s_mov_b64 exec, -1\n" \
"s_waitcnt lgkmcnt(0)\n" \
"s_add_u32 s43, s30, 1\n" \
"s_mov_b32 s50, 0\n" \
".LS2_w15l:\n" \
"buffer_load_dword v240, v205, s[4:7], 0 offen sc1\n" \
"s_waitcnt vmcnt(0)\n" \
".LS2_w15c:\n" \
"v_subrev_u32 v241, s43, v240\n" \
"v_cmp_gt_u32 vcc, 0x100, v241\n" \
"s_cmp_eq_u64 vcc, -1\n" \
"s_cbranch_scc1 .LS2_w15d\n" \
"s_sleep 2\n" \
"s_add_u32 s50, s50, 1\n" \
"s_cmp_lt_u32 s50, 0x20000\n" \
"s_cbranch_scc1 .LS2_w15l\n" \
"s_branch .LS2_end\n" \
".LS2_w15d:\n" \
"v_readfirstlane_b32 s31, v241\n" \
"s_lshr_b32 s32, s31, 2\n" \
"s_xor_b32 s29, s31, s32\n" \
"s_and_b32 s29, s29, 3\n" \
"s_lshl_b32 s29, s29, 13\n" \
"s_bitset1_b32 s29, 21\n" \
"s_and_b32 s31, s31, 3\n" \
"s_lshl_b32 s31, s31, 13\n" \
"s_bitcmp1_b32 s39, 0\n" \
"s_cselect_b32 s43, s29, 0\n" \
"s_xor_b32 s31, s31, s43\n" \
"s_xor_b32 s32, s31, 0x4000\n" \
"s_mov_b32 s39, 0\n" \
".LS2_pre:\n" \
"s_barrier\n" \
"ds_read_b96 v[248:250], v252\n" \
"s_waitcnt lgkmcnt(0)\n" \
"v_readfirstlane_b32 s44, v248\n" \
"v_readfirstlane_b32 s45, v249\n" \
"v_readfirstlane_b32 s46, v250\n" \
"s_mul_i32 s42, s25, s46\n" \
"s_lshr_b32 s42, s42, 12\n" \
".LS2_cu:\n" \
"s_sub_u32 s43, s24, s44\n" \
"s_cmp_lt_i32 s43, 0\n" \
"s_cbranch_scc0 .LS2_cud\n" \
"s_add_u32 s24, s24, s25\n" \
"s_xor_b32 s31, s31, s29\n" \
"s_xor_b32 s32, s32, s29\n" \
"s_xor_b32 s47, s47, s29\n" \
"s_add_u32 s39, s39, 1\n" \
"s_branch .LS2_cu\n" \
".LS2_cud:\n" \
".LS2_loop:\n" \
"s_add_u32 s43, s40, 0\n" \
"s_and_b32 s43, s43, 3\n" \
"s_bitcmp1_b32 s43, 0\n" \
"s_cselect_b32 s47, s32, s31\n" \
"s_lshl_b32 s43, s43, 19\n" \
"s_add_u32 s47, s47, s43\n" \
"s_sub_u32 s43, s24, s44\n" \
"s_max_i32 s43, s43, 0xfffd0000\n" \
"s_min_i32 s43, s43, 0x4000\n" \
"s_mul_i32 s43, s43, s46\n" \
"s_ashr_i32 s43, s43, 12\n" \
"s_add_u32 s41, s45, s43\n" \
"s_add_u32 s41, s41, 8\n" \
"s_branch .LS2_a1\n" \
".LS2_retry:\n" \
"s_add_u32 s51, s51, 1\n" \
"s_cmp_gt_u32 s51, 0x20000\n" \
"s_cbranch_scc1 .LS2_end\n" \
".LS2_a0:\n" \
"s_add_u32 s41, s41, s42\n" \
"s_add_u32 s24, s24, s25\n" \
"s_xor_b32 s31, s31, s29\n" \
"s_xor_b32 s32, s32, s29\n" \
"s_xor_b32 s47, s47, s29\n" \
"s_xor_b32 s48, s48, s29\n" \
"s_add_u32 s39, s39, 1\n" \
".p2align 6\n" \
".LS2_a1:\n" \
"s_memtime vcc\n" \
"s_waitcnt lgkmcnt(0)\n" \
"s_sub_u32 s43, s41, vcc_lo\n" \
"s_cmp_gt_i32 s43, 128\n" \
"s_cbranch_scc1 .LS2_a1\n" \
"s_cmp_lt_i32 s43, s37\n" \
"s_cbranch_scc1 .LS2_a0\n" \
"s_max_i32 s43, s43, 0\n" \
"s_and_b32 s43, s43, -4\n" \
"s_getpc_b64 vcc\n" \
".LS2_a3:\n" \
"s_sub_u32 s43, .LS2_a4-.LS2_a3, s43\n" \
"s_add_u32 vcc_lo, vcc_lo, s43\n" \
"s_addc_u32 vcc_hi, vcc_hi, 0\n" \
"s_setpc_b64 vcc\n" \
".rept 32\n" \
"s_nop 0\n" \
".endr\n" \
".LS2_a4:\n" \
"buffer_load_dwordx4 v[0:3], v208, s[4:7], s47 offen offset:2048 sc1\n" \
"s_nop 9\n" \
"buffer_load_dwordx4 v[4:7], v208, s[4:7], s47 offen sc1\n" \
"s_nop 9\n" \
"buffer_load_dwordx4 v[8:11], v208, s[4:7], s47 offen offset:1024 sc1\n" \
"s_nop 9\n" \
"buffer_load_dwordx4 v[12:15], v210, s[4:7], s47 offen sc1\n" \
"s_add_u32 s35, s35, 1\n" \
"v_mov_b32 v28, 0\n" \
"v_mov_b32 v29, 0\n" \
"v_mov_b32 v30, 0\n" \
"v_mov_b32 v31, 0\n" \
"v_mov_b32 v32, 0\n" \
"v_mov_b32 v33, 0\n" \
"v_mov_b32 v34, 0\n" \
"v_mov_b32 v35, 0\n" \
"v_mov_b32 v36, 0\n" \
"v_mov_b32 v37, 0\n" \
"v_mov_b32 v38, 0\n" \
"v_mov_b32 v39, 0\n" \
"v_mov_b32 v40, 0\n" \
"v_mov_b32 v41, 0\n" \
"v_mov_b32 v42, 0\n" \
"v_mov_b32 v43, 0\n" \
"v_mov_b32 v44, 0\n" \
"v_mov_b32 v45, 0\n" \
"v_mov_b32 v46, 0\n" \
"v_mov_b32 v47, 0\n" \
"v_mov_b32 v48, 0\n" \
"v_mov_b32 v49, 0\n" \
"v_mov_b32 v50, 0\n" \
"v_mov_b32 v51, 0\n" \
"v_mov_b32 v52, 0\n" \
"v_mov_b32 v53, 0\n" \
"v_mov_b32 v54, 0\n" \
"v_mov_b32 v55, 0\n" \
"v_mov_b32 v56, 0\n" \
"v_mov_b32 v57, 0\n" \
"v_mov_b32 v58, 0\n" \
"v_mov_b32 v59, 0\n" \
"s_waitcnt vmcnt(3)\n" \
"v_smfmac_f32_16x16x64_bf16 v[28:31], v[0:3], a[0:7], v200\n" \
"v_smfmac_f32_16x16x64_bf16 v[32:35], v[0:3], a[8:15], v200\n" \
"v_smfmac_f32_16x16x64_bf16 v[36:39], v[0:3], a[16:23], v200\n" \
"v_smfmac_f32_16x16x64_bf16 v[40:43], v[0:3], a[24:31], v200\n" \
"v_smfmac_f32_16x16x64_bf16 v[44:47], v[0:3], a[32:39], v200\n" \
"v_smfmac_f32_16x16x64_bf16 v[48:51], v[0:3], a[40:47], v200\n" \
"v_smfmac_f32_16x16x64_bf16 v[52:55], v[0:3], a[48:55], v200\n" \
"v_smfmac_f32_16x16x64_bf16 v[56:59], v[0:3], a[56:63], v200\n" \
"s_waitcnt vmcnt(2)\n" \
"v_smfmac_f32_16x16x64_bf16 v[28:31], v[4:7], a[64:71], v200\n" \
"v_smfmac_f32_16x16x64_bf16 v[32:35], v[4:7], a[72:79], v200\n" \
"v_smfmac_f32_16x16x64_bf16 v[36:39], v[4:7], a[80:87], v200\n" \
"v_smfmac_f32_16x16x64_bf16 v[40:43], v[4:7], a[88:95], v200\n" \
"s_waitcnt vmcnt(1)\n" \
"v_smfmac_f32_16x16x64_bf16 v[28:31], v[8:11], a[96:103], v200\n" \
"s_nop 2\n" \
"v_smfmac_f32_16x16x64_bf16 v[32:35], v[8:11], a[104:111], v200\n" \
"s_nop 2\n" \
"v_smfmac_f32_16x16x64_bf16 v[36:39], v[8:11], a[112:119], v200\n" \
"s_nop 0\n" \
"v_cvt_pk_bf16_f32 v128, v28, v29\n" \
"v_cvt_pk_bf16_f32 v129, v30, v31\n" \
"v_smfmac_f32_16x16x64_bf16 v[40:43], v[8:11], a[120:127], v200\n" \
"ds_write_b64 v211, v[128:129] offset:256\n" \
"v_cvt_pk_bf16_f32 v130, v32, v33\n" \
"v_cvt_pk_bf16_f32 v131, v34, v35\n" \
"v_smfmac_f32_16x16x64_bf16 v[44:47], v[4:7], a[128:135], v200\n" \
"ds_write_b64 v211, v[130:131] offset:2432\n" \
"v_cvt_pk_bf16_f32 v132, v36, v37\n" \
"v_cvt_pk_bf16_f32 v133, v38, v39\n" \
"v_smfmac_f32_16x16x64_bf16 v[44:47], v[8:11], a[136:143], v200\n" \
"s_waitcnt vmcnt(0)\n" \
"ds_write_b64 v211, v[132:133] offset:4608\n" \
"v_cvt_pk_bf16_f32 v134, v40, v41\n" \
"v_cvt_pk_bf16_f32 v135, v42, v43\n" \
"v_smfmac_f32_16x16x64_bf16 v[44:47], v[12:15], a[144:151], v200\n" \
"ds_write_b64 v211, v[134:135] offset:4992\n" \
"s_nop 1\n" \
"v_smfmac_f32_16x16x64_bf16 v[48:51], v[4:7], a[152:159], v200\n" \
"s_nop 2\n" \
"v_smfmac_f32_16x16x64_bf16 v[48:51], v[8:11], a[160:167], v200\n" \
"s_nop 0\n" \
"v_cvt_pk_bf16_f32 v136, v44, v45\n" \
"v_cvt_pk_bf16_f32 v137, v46, v47\n" \
"v_smfmac_f32_16x16x64_bf16 v[48:51], v[12:15], a[168:175], v200\n" \
"ds_write_b64 v211, v[136:137] offset:9472\n" \
"s_nop 1\n" \
"v_smfmac_f32_16x16x64_bf16 v[52:55], v[4:7], a[176:183], v200\n" \
"s_waitcnt lgkmcnt(1)\n" \
"s_barrier\n" \
"s_nop 0\n" \
"v_smfmac_f32_16x16x64_bf16 v[52:55], v[8:11], a[184:191], v200\n" \
"s_nop 0\n" \
"v_cvt_pk_bf16_f32 v138, v48, v49\n" \
"v_cvt_pk_bf16_f32 v139, v50, v51\n" \
"v_smfmac_f32_16x16x64_bf16 v[52:55], v[12:15], a[192:199], v200\n" \
"ds_write_b64 v211, v[138:139] offset:11648\n" \
"s_nop 1\n" \
"v_smfmac_f32_16x16x64_bf16 v[56:59], v[4:7], a[200:207], v200\n" \
"s_nop 2\n" \
"v_smfmac_f32_16x16x64_bf16 v[56:59], v[8:11], a[208:215], v200\n" \
"s_nop 2\n" \
"v_smfmac_f32_16x16x64_bf16 v[56:59], v[12:15], a[216:223], v200\n" \
"v_cvt_pk_bf16_f32 v140, v52, v53\n" \
"v_cvt_pk_bf16_f32 v141, v54, v55\n" \
"s_nop 1\n" \
"ds_write_b64 v211, v[140:141] offset:13824\n" \
"s_nop 2\n" \
"v_cvt_pk_bf16_f32 v142, v56, v57\n" \
"v_cvt_pk_bf16_f32 v143, v58, v59\n" \
"ds_write_b64 v211, v[142:143] offset:14208\n" \
"s_waitcnt lgkmcnt(1)\n" \
"s_barrier\n" \
"s_waitcnt lgkmcnt(0)\n" \
"s_barrier\n" \
"s_nop 3\n" \
"ds_read_b32 v64, v252 offset:20\n" \
"s_waitcnt lgkmcnt(0)\n" \
"v_readfirstlane_b32 s43, v64\n" \
"s_cmp_eq_u32 s43, s35\n" \
"s_cbranch_scc1 .LS2_retry\n" \
".p2align 3\n" \
"s_memtime s[54:55]\n" \
"s_memrealtime s[56:57]\n" \
"s_memtime s[58:59]\n" \
"s_memrealtime s[60:61]\n" \
"s_nop 0\n" \
"s_nop 0\n" \
"s_memtime s[62:63]\n" \
"s_memrealtime s[64:65]\n" \
"s_memtime s[66:67]\n" \
"s_memrealtime s[68:69]\n" \
"s_memtime s[70:71]\n" \
"s_memrealtime s[72:73]\n" \
"s_memtime s[74:75]\n" \
"s_memrealtime s[76:77]\n" \
"s_nop 0\n" \
"s_nop 0\n" \
"s_memtime s[78:79]\n" \
"s_memrealtime s[80:81]\n" \
"s_memtime s[82:83]\n" \
"s_memrealtime s[84:85]\n" \
"s_memtime s[86:87]\n" \
"s_memrealtime s[88:89]\n" \
"s_memtime s[90:91]\n" \
"s_memrealtime s[92:93]\n" \
"s_nop 0\n" \
"s_nop 0\n" \
"s_memtime s[94:95]\n" \
"s_memrealtime s[96:97]\n" \
"s_memtime s[98:99]\n" \
"s_mov_b32 s52, 0\n" \
"s_mov_b32 s53, 4096\n" \
"s_waitcnt lgkmcnt(0)\n" \
"s_cmp_lg_u32 s96, s56\n" \
"s_cselect_b32 s52, s90, s52\n" \
"s_cselect_b32 s53, s98, s53\n" \
"s_cmp_lg_u32 s92, s56\n" \
"s_cselect_b32 s52, s86, s52\n" \
"s_cselect_b32 s53, s94, s53\n" \
"s_cmp_lg_u32 s88, s56\n" \
"s_cselect_b32 s52, s82, s52\n" \
"s_cselect_b32 s53, s90, s53\n" \
"s_cmp_lg_u32 s84, s56\n" \
"s_cselect_b32 s52, s78, s52\n" \
"s_cselect_b32 s53, s86, s53\n" \
"s_cmp_lg_u32 s80, s56\n" \
"s_cselect_b32 s52, s74, s52\n" \
"s_cselect_b32 s53, s82, s53\n" \
"s_cmp_lg_u32 s76, s56\n" \
"s_cselect_b32 s52, s70, s52\n" \
"s_cselect_b32 s53, s78, s53\n" \
"s_cmp_lg_u32 s72, s56\n" \
"s_cselect_b32 s52, s66, s52\n" \
"s_cselect_b32 s53, s74, s53\n" \
"s_cmp_lg_u32 s68, s56\n" \
"s_cselect_b32 s52, s62, s52\n" \
"s_cselect_b32 s53, s70, s53\n" \
"s_cmp_lg_u32 s64, s56\n" \
"s_cselect_b32 s52, s58, s52\n" \
"s_cselect_b32 s53, s66, s53\n" \
"s_cmp_lg_u32 s60, s56\n" \
"s_cselect_b32 s52, s54, s52\n" \
"s_cselect_b32 s53, s62, s53\n" \
"s_sub_u32 s53, s53, s52\n" \
"s_lshr_b32 s55, s53, 1\n" \
"s_add_u32 s52, s52, s55\n" \
"s_add_u32 s55, s56, 4\n" \
"s_lshl_b32 s55, s55, 4\n" \
"s_sub_u32 s59, s55, s44\n" \
"s_cmp_lt_u32 s59, 2600\n" \
"s_cselect_b32 s63, 1, 2\n" \
"s_mul_i32 s59, s59, s46\n" \
"s_ashr_i32 s59, s59, 12\n" \
"s_add_u32 s59, s59, s45\n" \
"s_sub_u32 s59, s52, s59\n" \
"s_max_i32 s59, s59, -96\n" \
"s_min_i32 s59, s59, 96\n" \
"s_ashr_i32 s63, s59, s63\n" \
"s_add_u32 s63, s63, s46\n" \
"s_max_i32 s63, s63, 0x800\n" \
"s_min_i32 s63, s63, 0x2000\n" \
"s_cmp_le_u32 s53, 40\n" \
"s_cselect_b32 s44, s55, s44\n" \
"s_cselect_b32 s45, s52, s45\n" \
"s_cselect_b32 s46, s63, s46\n" \
"v_mov_b32 v248, s44\n" \
"v_mov_b32 v249, s45\n" \
"v_mov_b32 v250, s46\n" \
"s_mov_b64 exec, 1\n" \
"ds_write_b96 v252, v[248:250]\n" \
"s_mov_b64 exec, -1\n" \
"v_max3_u32 v64, v0, v0, v1\n" \
"v_max3_u32 v64, v64, v2, v3\n" \
"v_max3_u32 v64, v64, v4, v5\n" \
"v_max3_u32 v64, v64, v6, v7\n" \
"v_max3_u32 v64, v64, v8, v9\n" \
"v_max3_u32 v64, v64, v10, v11\n" \
"v_max3_u32 v64, v64, v12, v13\n" \
"v_max3_u32 v64, v64, v14, v15\n" \
"v_cmp_eq_u32 vcc, -1, v64\n" \
"s_cbranch_vccnz .LS2_veto\n" \
".LS2_back:\n" \
"s_waitcnt lgkmcnt(0)\n" \
"s_barrier\n" \
"ds_read_b32 v64, v252 offset:16\n" \
"ds_read_b64 v[246:247], v252 offset:24\n" \
"ds_read_b96 v[248:250], v252\n" \
"s_waitcnt lgkmcnt(0)\n" \
"v_readfirstlane_b32 s44, v248\n" \
"v_readfirstlane_b32 s45, v249\n" \
"v_readfirstlane_b32 s46, v250\n" \
"v_readfirstlane_b32 s26, v246\n" \
"v_readfirstlane_b32 s27, v247\n" \
"s_mul_i32 s42, s25, s46\n" \
"s_lshr_b32 s42, s42, 12\n" \
"v_readfirstlane_b32 s43, v64\n" \
"s_cmp_eq_u32 s43, s35\n" \
"s_cbranch_scc1 .LS2_retry\n" \
"s_add_u32 s40, s40, 1\n" \
"s_add_u32 s24, s24, s26\n" \
"s_add_u32 s24, s24, s27\n" \
"s_cmp_eq_u32 s40, 0\n" \
"s_cbranch_scc1 .LS2_wet\n" \
"s_cmp_lt_u32 s40, 63\n" \
"s_cbranch_scc1 .LS2_loop\n" \
".LS2_end:\n" \
"s_endpgm\n" \
".LS2_veto:\n" \
"v_mov_b32 v64, s35\n" \
"ds_write_b32 v252, v64 offset:16\n" \
"s_branch .LS2_back\n" \
".p2align 6\n" \
"fk_role_S3:\n" \
"ds_read_b32 v200, v255 offset:0\n" \
"ds_read_b32 v201, v255 offset:256\n" \
"ds_read_b32 v202, v255 offset:512\n" \
"ds_read_b32 v203, v255 offset:768\n" \
"ds_read_b32 v204, v255 offset:1024\n" \
"ds_read_b32 v205, v255 offset:1280\n" \
"ds_read_b32 v206, v255 offset:1536\n" \
"ds_read_b32 v207, v255 offset:1792\n" \
"ds_read_b32 v208, v255 offset:2048\n" \
"ds_read_b32 v209, v255 offset:2304\n" \
"ds_read_b32 v210, v255 offset:2560\n" \
"ds_read_b32 v211, v255 offset:2816\n" \
"ds_read_b32 v212, v255 offset:3072\n" \
"ds_read_b32 v213, v255 offset:3328\n" \
"ds_read_b32 v214, v255 offset:3584\n" \
"ds_read_b32 v215, v255 offset:3840\n" \
"ds_read_b32 v254, v255 offset:7936\n" \
"s_waitcnt lgkmcnt(0)\n" \
"v_readlane_b32 s4, v254, 0\n" \
"v_readlane_b32 s5, v254, 1\n" \
"v_readlane_b32 s6, v254, 2\n" \
"v_readlane_b32 s7, v254, 3\n" \
"v_readlane_b32 s8, v254, 4\n" \
"v_readlane_b32 s9, v254, 5\n" \
"v_readlane_b32 s10, v254, 6\n" \
"v_readlane_b32 s11, v254, 7\n" \
"v_readlane_b32 s12, v254, 8\n" \
"v_readlane_b32 s13, v254, 9\n" \
"v_readlane_b32 s14, v254, 10\n" \
"v_readlane_b32 s15, v254, 11\n" \
"v_readlane_b32 s16, v254, 12\n" \
"v_readlane_b32 s17, v254, 13\n" \
"v_readlane_b32 s18, v254, 14\n" \
"v_readlane_b32 s19, v254, 15\n" \
"v_readlane_b32 s20, v254, 16\n" \
"v_readlane_b32 s21, v254, 17\n" \
"v_readlane_b32 s22, v254, 18\n" \
"v_readlane_b32 s23, v254, 19\n" \
"v_readlane_b32 s24, v254, 20\n" \
"v_readlane_b32 s25, v254, 21\n" \
"v_readlane_b32 s26, v254, 22\n" \
"v_readlane_b32 s27, v254, 23\n" \
"v_readlane_b32 s28, v254, 24\n" \
"v_readlane_b32 s29, v254, 25\n" \
"v_readlane_b32 s30, v254, 26\n" \
"v_readlane_b32 s31, v254, 27\n" \
"v_readlane_b32 s32, v254, 28\n" \
"v_readlane_b32 s33, v254, 29\n" \
"v_readlane_b32 s34, v254, 30\n" \
"v_readlane_b32 s35, v254, 31\n" \
"v_readlane_b32 s36, v254, 32\n" \
"v_readlane_b32 s37, v254, 33\n" \
"v_readlane_b32 s38, v254, 34\n" \
"v_readlane_b32 s39, v254, 35\n" \
"s_mov_b32 s40, 0\n" \
"s_mov_b32 s51, 0\n" \
"s_nop 0\n" \
"v_mov_b32 v252, s38\n" \
"s_nop 4\n" \
"v_mov_b32 v188, -1\n" \
"s_mov_b32 s28, s33\n" \
"s_mov_b32 s33, -1\n" \
"v_writelane_b32 v251, s6, 0\n" \
"v_writelane_b32 v251, s10, 1\n" \
"v_writelane_b32 v251, s14, 2\n" \
"v_writelane_b32 v251, s18, 3\n" \
"s_mov_b32 s6, 0\n" \
"s_mov_b32 s10, 0\n" \
"s_mov_b32 s14, 0\n" \
"s_mov_b32 s18, 0\n" \
"s_mov_b32 s40, -1\n" \
"s_branch .LS3_pre\n" \
".LS3_wet:\n" \
"s_waitcnt vmcnt(0)\n" \
"v_readlane_b32 s6, v251, 0\n" \
"v_readlane_b32 s10, v251, 1\n" \
"v_readlane_b32 s14, v251, 2\n" \
"v_readlane_b32 s18, v251, 3\n" \
"s_nop 4\n" \
"ds_read_b32 v230, v255 offset:6912\n" \
"v_readlane_b32 s60, v254, 12\n" \
"v_readlane_b32 s61, v254, 13\n" \
"v_readlane_b32 s62, v254, 14\n" \
"v_readlane_b32 s63, v254, 15\n" \
"s_waitcnt lgkmcnt(0)\n" \
"s_nop 3\n" \
"buffer_load_dword v228, v230, s[60:63], 0 offen\n" \
"s_mov_b32 s33, s28\n" \
"s_add_u32 s34, s33, 2\n" \
"s_add_u32 s36, s33, 4\n" \
"s_add_u32 s43, s30, s33\n" \
"s_mov_b32 s50, 0\n" \
".LS3_w16l:\n" \
"buffer_load_dword v240, v204, s[4:7], 0 offen sc1\n" \
"s_waitcnt vmcnt(0)\n" \
".LS3_w16c:\n" \
"v_subrev_u32 v241, s43, v240\n" \
"v_cmp_gt_u32 vcc, 0x100, v241\n" \
"s_cmp_eq_u64 vcc, -1\n" \
"s_cbranch_scc1 .LS3_w16d\n" \
"s_sleep 2\n" \
"s_add_u32 s50, s50, 1\n" \
"s_cmp_lt_u32 s50, 0x20000\n" \
"s_cbranch_scc1 .LS3_w16l\n" \
"s_branch .LS3_end\n" \
".LS3_w16d:\n" \
"v_readlane_b32 s43, v202, 2\n" \
"s_nop 4\n" \
"buffer_load_dwordx4 a[16:19], v201, s[8:11], s43 offen sc1\n" \
"buffer_load_dwordx4 a[20:23], v201, s[8:11], s43 offen offset:1024 sc1\n" \
"v_readlane_b32 s43, v202, 3\n" \
"s_nop 4\n" \
"buffer_load_dwordx4 a[24:27], v201, s[8:11], s43 offen sc1\n" \
"buffer_load_dwordx4 a[28:31], v201, s[8:11], s43 offen offset:1024 sc1\n" \
"v_readlane_b32 s43, v202, 4\n" \
"s_nop 4\n" \
"buffer_load_dwordx4 a[32:35], v201, s[8:11], s43 offen sc1\n" \
"buffer_load_dwordx4 a[36:39], v201, s[8:11], s43 offen offset:1024 sc1\n" \
"v_readlane_b32 s43, v202, 5\n" \
"s_nop 4\n" \
"buffer_load_dwordx4 a[40:43], v201, s[8:11], s43 offen sc1\n" \
"buffer_load_dwordx4 a[44:47], v201, s[8:11], s43 offen offset:1024 sc1\n" \
"s_add_u32 s43, s30, s34\n" \
"s_mov_b32 s50, 0\n" \
"s_branch .LS3_w17c\n" \
".LS3_w17l:\n" \
"buffer_load_dword v240, v204, s[4:7], 0 offen sc1\n" \
"s_waitcnt vmcnt(0)\n" \
".LS3_w17c:\n" \
"v_subrev_u32 v241, s43, v240\n" \
"v_cmp_gt_u32 vcc, 0x100, v241\n" \
"s_cmp_eq_u64 vcc, -1\n" \
"s_cbranch_scc1 .LS3_w17d\n" \
"s_sleep 2\n" \
"s_add_u32 s50, s50, 1\n" \
"s_cmp_lt_u32 s50, 0x20000\n" \
"s_cbranch_scc1 .LS3_w17l\n" \
"s_branch .LS3_end\n" \
".LS3_w17d:\n" \
"v_readlane_b32 s43, v202, 6\n" \
"s_nop 4\n" \
"buffer_load_dwordx4 a[48:51], v201, s[8:11], s43 offen sc1\n" \
"buffer_load_dwordx4 a[52:55], v201, s[8:11], s43 offen offset:1024 sc1\n" \
"v_readlane_b32 s43, v202, 7\n" \
"s_nop 4\n" \
"buffer_load_dwordx4 a[56:59], v201, s[8:11], s43 offen sc1\n" \
"buffer_load_dwordx4 a[60:63], v201, s[8:11], s43 offen offset:1024 sc1\n" \
"v_readlane_b32 s43, v202, 8\n" \
"s_nop 4\n" \
"buffer_load_dwordx4 a[64:67], v201, s[8:11], s43 offen sc1\n" \
"buffer_load_dwordx4 a[68:71], v201, s[8:11], s43 offen offset:1024 sc1\n" \
"v_readlane_b32 s43, v202, 9\n" \
"s_nop 4\n" \
"buffer_load_dwordx4 a[72:75], v201, s[8:11], s43 offen sc1\n" \
"buffer_load_dwordx4 a[76:79], v201, s[8:11], s43 offen offset:1024 sc1\n" \
"s_add_u32 s43, s30, s36\n" \
"s_mov_b32 s50, 0\n" \
"s_branch .LS3_w18c\n" \
".LS3_w18l:\n" \
"buffer_load_dword v240, v204, s[4:7], 0 offen sc1\n" \
"s_waitcnt vmcnt(0)\n" \
".LS3_w18c:\n" \
"v_subrev_u32 v241, s43, v240\n" \
"v_cmp_gt_u32 vcc, 0x100, v241\n" \
"s_cmp_eq_u64 vcc, -1\n" \
"s_cbranch_scc1 .LS3_w18d\n" \
"s_sleep 2\n" \
"s_add_u32 s50, s50, 1\n" \
"s_cmp_lt_u32 s50, 0x20000\n" \
"s_cbranch_scc1 .LS3_w18l\n" \
"s_branch .LS3_end\n" \
".LS3_w18d:\n" \
"v_readlane_b32 s43, v202, 10\n" \
"s_nop 4\n" \
"buffer_load_dwordx4 a[80:83], v201, s[8:11], s43 offen sc1\n" \
"buffer_load_dwordx4 a[84:87], v201, s[8:11], s43 offen offset:1024 sc1\n" \
"v_readlane_b32 s43, v202, 11\n" \
"s_nop 4\n" \
"buffer_load_dwordx4 a[88:91], v201, s[8:11], s43 offen sc1\n" \
"buffer_load_dwordx4 a[92:95], v201, s[8:11], s43 offen offset:1024 sc1\n" \
"v_readlane_b32 s43, v202, 12\n" \
"s_nop 4\n" \
"buffer_load_dwordx4 a[96:99], v201, s[8:11], s43 offen sc1\n" \
"buffer_load_dwordx4 a[100:103], v201, s[8:11], s43 offen offset:1024 sc1\n" \
"v_readlane_b32 s43, v202, 13\n" \
"s_nop 4\n" \
"buffer_load_dwordx4 a[104:107], v201, s[8:11], s43 offen sc1\n" \
"buffer_load_dwordx4 a[108:111], v201, s[8:11], s43 offen offset:1024 sc1\n" \
"s_add_u32 s43, s30, 7\n" \
"s_mov_b32 s50, 0\n" \
"s_branch .LS3_w19c\n" \
".LS3_w19l:\n" \
"buffer_load_dword v240, v204, s[4:7], 0 offen sc1\n" \
"s_waitcnt vmcnt(0)\n" \
".LS3_w19c:\n" \
"v_subrev_u32 v241, s43, v240\n" \
"v_cmp_gt_u32 vcc, 0x100, v241\n" \
"s_cmp_eq_u64 vcc, -1\n" \
"s_cbranch_scc1 .LS3_w19d\n" \
"s_sleep 2\n" \
"s_add_u32 s50, s50, 1\n" \
"s_cmp_lt_u32 s50, 0x20000\n" \
"s_cbranch_scc1 .LS3_w19l\n" \
"s_branch .LS3_end\n" \
".LS3_w19d:\n" \
"v_readlane_b32 s43, v202, 0\n" \
"s_nop 4\n" \
"buffer_load_dwordx4 a[0:3], v201, s[8:11], s43 offen sc1\n" \
"buffer_load_dwordx4 a[4:7], v201, s[8:11], s43 offen offset:1024 sc1\n" \
"v_readlane_b32 s43, v202, 1\n" \
"s_nop 4\n" \
"buffer_load_dwordx4 a[8:11], v201, s[8:11], s43 offen sc1\n" \
"buffer_load_dwordx4 a[12:15], v201, s[8:11], s43 offen offset:1024 sc1\n" \
"s_mov_b32 s33, -1\n" \
"s_add_u32 s43, s30, 1\n" \
"s_mov_b32 s50, 0\n" \
".LS3_w20l:\n" \
"buffer_load_dword v240, v205, s[4:7], 0 offen sc1\n" \
"s_waitcnt vmcnt(0)\n" \
".LS3_w20c:\n" \
"v_subrev_u32 v241, s43, v240\n" \
"v_cmp_gt_u32 vcc, 0x100, v241\n" \
"s_cmp_eq_u64 vcc, -1\n" \
"s_cbranch_scc1 .LS3_w20d\n" \
"s_sleep 2\n" \
"s_add_u32 s50, s50, 1\n" \
"s_cmp_lt_u32 s50, 0x20000\n" \
"s_cbranch_scc1 .LS3_w20l\n" \
"s_branch .LS3_end\n" \
".LS3_w20d:\n" \
"v_readfirstlane_b32 s31, v241\n" \
"s_lshr_b32 s32, s31, 2\n" \
"s_xor_b32 s29, s31, s32\n" \
"s_and_b32 s29, s29, 3\n" \
"s_lshl_b32 s29, s29, 13\n" \
"s_bitset1_b32 s29, 21\n" \
"s_and_b32 s31, s31, 3\n" \
"s_lshl_b32 s31, s31, 13\n" \
"s_bitcmp1_b32 s39, 0\n" \
"s_cselect_b32 s43, s29, 0\n" \
"s_xor_b32 s31, s31, s43\n" \
"s_xor_b32 s32, s31, 0x4000\n" \
"s_mov_b32 s39, 0\n" \
".LS3_pre:\n" \
"s_barrier\n" \
"ds_read_b96 v[248:250], v252\n" \
"s_waitcnt lgkmcnt(0)\n" \
"v_readfirstlane_b32 s44, v248\n" \
"v_readfirstlane_b32 s45, v249\n" \
"v_readfirstlane_b32 s46, v250\n" \
"s_mul_i32 s42, s25, s46\n" \
"s_lshr_b32 s42, s42, 12\n" \
".LS3_cu:\n" \
"s_sub_u32 s43, s24, s44\n" \
"s_cmp_lt_i32 s43, 0\n" \
"s_cbranch_scc0 .LS3_cud\n" \
"s_add_u32 s24, s24, s25\n" \
"s_xor_b32 s31, s31, s29\n" \
"s_xor_b32 s32, s32, s29\n" \
"s_xor_b32 s47, s47, s29\n" \
"s_add_u32 s39, s39, 1\n" \
"s_branch .LS3_cu\n" \
".LS3_cud:\n" \
".LS3_loop:\n" \
"s_add_u32 s43, s40, 0\n" \
"s_and_b32 s43, s43, 3\n" \
"s_bitcmp1_b32 s43, 0\n" \
"s_cselect_b32 s47, s32, s31\n" \
"s_lshl_b32 s43, s43, 19\n" \
"s_add_u32 s47, s47, s43\n" \
"s_add_u32 s43, s40, 2\n" \
"s_and_b32 s43, s43, 3\n" \
"s_bitcmp1_b32 s43, 0\n" \
"s_cselect_b32 s49, s32, s31\n" \
"s_lshl_b32 s43, s43, 19\n" \
"s_add_u32 s49, s49, s43\n" \
"s_sub_u32 s43, s24, s44\n" \
"s_max_i32 s43, s43, 0xfffd0000\n" \
"s_min_i32 s43, s43, 0x4000\n" \
"s_mul_i32 s43, s43, s46\n" \
"s_ashr_i32 s43, s43, 12\n" \
"s_add_u32 s41, s45, s43\n" \
"s_add_u32 s41, s41, 12\n" \
"s_branch .LS3_a1\n" \
".LS3_retry:\n" \
"s_add_u32 s51, s51, 1\n" \
"s_cmp_gt_u32 s51, 0x20000\n" \
"s_cbranch_scc1 .LS3_end\n" \
".LS3_a0:\n" \
"s_add_u32 s41, s41, s42\n" \
"s_add_u32 s24, s24, s25\n" \
"s_xor_b32 s31, s31, s29\n" \
"s_xor_b32 s32, s32, s29\n" \
"s_xor_b32 s47, s47, s29\n" \
"s_xor_b32 s48, s48, s29\n" \
"s_add_u32 s39, s39, 1\n" \
"buffer_store_dword v188, v213, s[4:7], s47 offen sc1\n" \
"buffer_store_dword v188, v215, s[4:7], s47 offen sc1\n" \
".p2align 6\n" \
".LS3_a1:\n" \
"s_memtime vcc\n" \
"s_waitcnt lgkmcnt(0)\n" \
"s_sub_u32 s43, s41, vcc_lo\n" \
"s_cmp_gt_i32 s43, 128\n" \
"s_cbranch_scc1 .LS3_a1\n" \
"s_cmp_lt_i32 s43, s37\n" \
"s_cbranch_scc1 .LS3_a0\n" \
"s_max_i32 s43, s43, 0\n" \
"s_and_b32 s43, s43, -4\n" \
"s_getpc_b64 vcc\n" \
".LS3_a3:\n" \
"s_sub_u32 s43, .LS3_a4-.LS3_a3, s43\n" \
"s_add_u32 vcc_lo, vcc_lo, s43\n" \
"s_addc_u32 vcc_hi, vcc_hi, 0\n" \
"s_setpc_b64 vcc\n" \
".rept 32\n" \
"s_nop 0\n" \
".endr\n" \
".LS3_a4:\n" \
"buffer_load_dwordx4 v[12:15], v210, s[4:7], s47 offen sc1\n" \
"s_nop 3\n" \
"buffer_load_dwordx4 v[0:3], v208, s[4:7], s47 offen sc1\n" \
"s_nop 3\n" \
"buffer_load_dwordx4 v[4:7], v208, s[4:7], s47 offen offset:1024 sc1\n" \
"s_nop 3\n" \
"buffer_load_dwordx4 v[8:11], v208, s[4:7], s47 offen offset:2048 sc1\n" \
"s_nop 3\n" \
"buffer_load_dwordx4 v[16:19], v209, s[4:7], s47 offen sc1\n" \
"s_nop 3\n" \
"buffer_load_dwordx4 v[20:23], v209, s[4:7], s47 offen offset:1024 sc1\n" \
"s_nop 3\n" \
"buffer_load_dwordx4 v[24:27], v209, s[4:7], s47 offen offset:2048 sc1\n" \
"s_lshl_b32 s43, s40, 7\n" \
"buffer_load_dwordx4 v[236:239], v207, s[4:7], s43 offen sc1\n" \
"s_add_u32 s35, s35, 1\n" \
"v_mov_b32 v28, 0\n" \
"v_mov_b32 v29, 0\n" \
"v_mov_b32 v30, 0\n" \
"v_mov_b32 v31, 0\n" \
"v_mov_b32 v32, 0\n" \
"v_mov_b32 v33, 0\n" \
"v_mov_b32 v34, 0\n" \
"v_mov_b32 v35, 0\n" \
"v_mov_b32 v36, 0\n" \
"v_mov_b32 v37, 0\n" \
"v_mov_b32 v38, 0\n" \
"v_mov_b32 v39, 0\n" \
"v_mov_b32 v40, 0\n" \
"v_mov_b32 v41, 0\n" \
"v_mov_b32 v42, 0\n" \
"v_mov_b32 v43, 0\n" \
"v_mov_b32 v68, 0\n" \
"v_mov_b32 v69, 0\n" \
"v_mov_b32 v70, 0\n" \
"v_mov_b32 v71, 0\n" \
"s_waitcnt vmcnt(7)\n" \
"v_smfmac_f32_16x16x64_bf16 v[36:39], v[12:15], a[0:7], v200\n" \
"v_smfmac_f32_16x16x64_bf16 v[40:43], v[12:15], a[8:15], v200\n" \
"s_waitcnt vmcnt(6)\n" \
"v_smfmac_f32_16x16x64_bf16 v[28:31], v[0:3], a[16:23], v200\n" \
"v_smfmac_f32_16x16x64_bf16 v[32:35], v[0:3], a[24:31], v200\n" \
"v_smfmac_f32_16x16x64_bf16 v[36:39], v[0:3], a[32:39], v200\n" \
"v_smfmac_f32_16x16x64_bf16 v[40:43], v[0:3], a[40:47], v200\n" \
"s_waitcnt vmcnt(5)\n" \
"v_smfmac_f32_16x16x64_bf16 v[28:31], v[4:7], a[48:55], v200\n" \
"v_smfmac_f32_16x16x64_bf16 v[32:35], v[4:7], a[56:63], v200\n" \
"v_smfmac_f32_16x16x64_bf16 v[36:39], v[4:7], a[64:71], v200\n" \
"v_smfmac_f32_16x16x64_bf16 v[40:43], v[4:7], a[72:79], v200\n" \
"s_waitcnt vmcnt(4)\n" \
"v_smfmac_f32_16x16x64_bf16 v[28:31], v[8:11], a[80:87], v200\n" \
"v_smfmac_f32_16x16x64_bf16 v[32:35], v[8:11], a[88:95], v200\n" \
"v_smfmac_f32_16x16x64_bf16 v[36:39], v[8:11], a[96:103], v200\n" \
"v_smfmac_f32_16x16x64_bf16 v[40:43], v[8:11], a[104:111], v200\n" \
"s_nop 4\n" \
"v_cvt_pk_bf16_f32 v128, v28, v29\n" \
"v_cvt_pk_bf16_f32 v129, v30, v31\n" \
"v_cvt_pk_bf16_f32 v130, v32, v33\n" \
"v_cvt_pk_bf16_f32 v131, v34, v35\n" \
"v_cvt_pk_bf16_f32 v132, v36, v37\n" \
"v_cvt_pk_bf16_f32 v133, v38, v39\n" \
"ds_write2_b64 v211, v[128:129], v[130:131] offset0:0 offset1:16\n" \
"v_cvt_pk_bf16_f32 v134, v40, v41\n" \
"v_cvt_pk_bf16_f32 v135, v42, v43\n" \
"ds_write2_b64 v214, v[132:133], v[134:135] offset0:0 offset1:16\n" \
"ds_read_b64_tr_b16 v[96:97], v212 offset:6912\n" \
"ds_read_b64_tr_b16 v[98:99], v212 offset:6928\n" \
"ds_read_b64_tr_b16 v[100:101], v212 offset:7168\n" \
"ds_read_b64_tr_b16 v[102:103], v212 offset:7184\n" \
"s_waitcnt vmcnt(1)\n" \
"v_max3_u32 v64, v16, v16, v17\n" \
"v_max3_u32 v64, v64, v18, v19\n" \
"v_max3_u32 v64, v64, v20, v21\n" \
"v_max3_u32 v64, v64, v22, v23\n" \
"v_max3_u32 v64, v64, v24, v25\n" \
"v_max3_u32 v64, v64, v26, v27\n" \
"v_cmp_eq_u32 vcc, -1, v64\n" \
"s_barrier\n" \
"ds_read_b64_tr_b16 v[72:73], v212 offset:0\n" \
"ds_read_b64_tr_b16 v[74:75], v212 offset:16\n" \
"ds_read_b64_tr_b16 v[76:77], v212 offset:256\n" \
"ds_read_b64_tr_b16 v[78:79], v212 offset:272\n" \
"ds_read_b64_tr_b16 v[80:81], v212 offset:2304\n" \
"ds_read_b64_tr_b16 v[82:83], v212 offset:2320\n" \
"ds_read_b64_tr_b16 v[84:85], v212 offset:2560\n" \
"ds_read_b64_tr_b16 v[86:87], v212 offset:2576\n" \
"s_cbranch_vccnz .LS3_veto\n" \
"s_waitcnt lgkmcnt(8)\n" \
"v_smfmac_f32_16x16x64_bf16 v[68:71], v[12:15], v[96:103], v200\n" \
"ds_read_b64_tr_b16 v[88:89], v212 offset:4608\n" \
"ds_read_b64_tr_b16 v[90:91], v212 offset:4624\n" \
"ds_read_b64_tr_b16 v[92:93], v212 offset:4864\n" \
"ds_read_b64_tr_b16 v[94:95], v212 offset:4880\n" \
"s_waitcnt lgkmcnt(8)\n" \
"v_smfmac_f32_16x16x64_bf16 v[68:71], v[0:3], v[72:79], v200\n" \
"s_waitcnt lgkmcnt(4)\n" \
"v_smfmac_f32_16x16x64_bf16 v[68:71], v[4:7], v[80:87], v200\n" \
"s_waitcnt lgkmcnt(0)\n" \
"v_smfmac_f32_16x16x64_bf16 v[68:71], v[8:11], v[88:95], v200\n" \
"s_nop 7\n" \
"v_cmp_u_f32 vcc, v68, v68\n" \
"s_cbranch_vccnz .LS3_veto2\n" \
".LS3_veto2r:\n" \
"s_barrier\n" \
"ds_read_b64_tr_b16 v[104:105], v212 offset:9216\n" \
"ds_read_b64_tr_b16 v[106:107], v212 offset:9232\n" \
"ds_read_b64_tr_b16 v[108:109], v212 offset:9472\n" \
"ds_read_b64_tr_b16 v[110:111], v212 offset:9488\n" \
"ds_read_b64_tr_b16 v[112:113], v212 offset:11520\n" \
"ds_read_b64_tr_b16 v[114:115], v212 offset:11536\n" \
"ds_read_b64_tr_b16 v[116:117], v212 offset:11776\n" \
"ds_read_b64_tr_b16 v[118:119], v212 offset:11792\n" \
"s_barrier\n" \
"ds_read_b64_tr_b16 v[120:121], v212 offset:13824\n" \
"ds_read_b64_tr_b16 v[122:123], v212 offset:13840\n" \
"ds_read_b64_tr_b16 v[124:125], v212 offset:14080\n" \
"ds_read_b64_tr_b16 v[126:127], v212 offset:14096\n" \
"s_waitcnt lgkmcnt(8)\n" \
"v_smfmac_f32_16x16x64_bf16 v[68:71], v[16:19], v[104:111], v200\n" \
"s_waitcnt lgkmcnt(4)\n" \
"v_smfmac_f32_16x16x64_bf16 v[68:71], v[20:23], v[112:119], v200\n" \
"s_waitcnt lgkmcnt(0)\n" \
"v_smfmac_f32_16x16x64_bf16 v[68:71], v[24:27], v[120:127], v200\n" \
"s_nop 7\n" \
"v_add_f32_dpp v128, v68, v68 row_shl:4 row_mask:0xf bank_mask:0x5\n" \
"v_add_f32_dpp v129, v70, v70 row_shl:4 row_mask:0xf bank_mask:0x5\n" \
"v_add_f32_dpp v128, v69, v69 row_shr:4 row_mask:0xf bank_mask:0xa\n" \
"v_add_f32_dpp v129, v71, v71 row_shr:4 row_mask:0xf bank_mask:0xa\n" \
"s_nop 1\n" \
"v_permlane32_swap_b32 v128, v129\n" \
"v_add_f32 v128, v128, v129\n" \
"buffer_store_dword v128, v213, s[4:7], s47 offen sc1\n" \
"buffer_store_dword v128, v215, s[4:7], s47 offen sc1\n" \
"s_xor_b32 s43, s47, s29\n" \
"buffer_store_dword v128, v213, s[4:7], s43 offen sc1\n" \
"buffer_store_dword v128, v215, s[4:7], s43 offen sc1\n" \
"s_waitcnt vmcnt(4)\n" \
"v_max_u32 v236, v236, v238\n" \
"v_max_u32 v237, v237, v239\n" \
"v_and_b32 v236, 0xfff, v236\n" \
"v_and_b32 v237, 0xfff, v237\n" \
"s_mov_b64 exec, 1\n" \
"ds_write_b64 v252, v[236:237] offset:24\n" \
"s_mov_b64 exec, -1\n" \
"s_nop 15\n" \
"s_nop 13\n" \
"buffer_store_dword v188, v213, s[4:7], s49 offen sc1\n" \
"buffer_store_dword v188, v215, s[4:7], s49 offen sc1\n" \
"s_xor_b32 s43, s49, s29\n" \
"buffer_store_dword v188, v213, s[4:7], s43 offen sc1\n" \
"buffer_store_dword v188, v215, s[4:7], s43 offen sc1\n" \
"s_waitcnt lgkmcnt(0)\n" \
"s_barrier\n" \
"ds_read_b32 v64, v252 offset:16\n" \
"ds_read_b64 v[246:247], v252 offset:24\n" \
"ds_read_b96 v[248:250], v252\n" \
"s_waitcnt lgkmcnt(0)\n" \
"v_readfirstlane_b32 s44, v248\n" \
"v_readfirstlane_b32 s45, v249\n" \
"v_readfirstlane_b32 s46, v250\n" \
"v_readfirstlane_b32 s26, v246\n" \
"v_readfirstlane_b32 s27, v247\n" \
"s_mul_i32 s42, s25, s46\n" \
"s_lshr_b32 s42, s42, 12\n" \
"v_readfirstlane_b32 s43, v64\n" \
"s_cmp_eq_u32 s43, s35\n" \
"s_cbranch_scc1 .LS3_retry\n" \
"v_cmp_u_f32 vcc, v128, v128\n" \
"s_cbranch_vccnz .LS3_nan\n" \
".LS3_nanr:\n" \
"s_add_u32 s40, s40, 1\n" \
"s_add_u32 s24, s24, s26\n" \
"s_add_u32 s24, s24, s27\n" \
"s_cmp_eq_u32 s40, 0\n" \
"s_cbranch_scc1 .LS3_wet\n" \
"s_cmp_lt_u32 s40, 63\n" \
"s_cbranch_scc1 .LS3_loop\n" \
".LS3_end:\n" \
"s_endpgm\n" \
".LS3_nan:\n" \
"v_cmp_u_f32 vcc, v128, v128\n" \
"v_mov_b32 v129, 0x7fc00001\n" \
"s_nop 1\n" \
"v_cndmask_b32 v128, v128, v129, vcc\n" \
"buffer_store_dword v128, v213, s[4:7], s47 offen sc1\n" \
"buffer_store_dword v128, v215, s[4:7], s47 offen sc1\n" \
"s_xor_b32 s43, s47, s29\n" \
"buffer_store_dword v128, v213, s[4:7], s43 offen sc1\n" \
"buffer_store_dword v128, v215, s[4:7], s43 offen sc1\n" \
"s_branch .LS3_nanr\n" \
".LS3_veto:\n" \
"v_mov_b32 v64, s35\n" \
"ds_write_b32 v252, v64 offset:20\n" \
"s_barrier\n" \
"s_waitcnt lgkmcnt(0)\n" \
"s_barrier\n" \
"s_branch .LS3_retry\n" \
".LS3_veto2:\n" \
"v_max3_u32 v64, v0, v0, v1\n" \
"v_max3_u32 v64, v64, v2, v3\n" \
"v_max3_u32 v64, v64, v4, v5\n" \
"v_max3_u32 v64, v64, v6, v7\n" \
"v_max3_u32 v64, v64, v8, v9\n" \
"v_max3_u32 v64, v64, v10, v11\n" \
"v_max3_u32 v64, v64, v12, v13\n" \
"v_max3_u32 v64, v64, v14, v15\n" \
"v_cmp_eq_u32 vcc, -1, v64\n" \
"s_cbranch_vccnz .LS3_veto3\n" \
"s_cmp_eq_u32 s33, s40\n" \
"s_cbranch_scc1 .LS3_veto2r\n" \
"s_mov_b32 s33, s40\n" \
".LS3_veto3:\n" \
"v_mov_b32 v64, s35\n" \
"ds_write_b32 v252, v64 offset:20\n" \
"s_barrier\n" \
"s_waitcnt lgkmcnt(0)\n" \
"s_barrier\n" \
"s_branch .LS3_retry\n" \
".p2align 6\n" \
"fk_role_P0:\n" \
"ds_read_b32 v200, v255 offset:0\n" \
"ds_read_b32 v201, v255 offset:256\n" \
"ds_read_b32 v202, v255 offset:512\n" \
"ds_read_b32 v203, v255 offset:768\n" \
"ds_read_b32 v204, v255 offset:1024\n" \
"ds_read_b32 v205, v255 offset:1280\n" \
"ds_read_b32 v206, v255 offset:1536\n" \
"ds_read_b32 v207, v255 offset:1792\n" \
"ds_read_b32 v208, v255 offset:2048\n" \
"ds_read_b32 v209, v255 offset:2304\n" \
"ds_read_b32 v210, v255 offset:2560\n" \
"ds_read_b32 v211, v255 offset:2816\n" \
"ds_read_b32 v212, v255 offset:3072\n" \
"ds_read_b32 v213, v255 offset:3328\n" \
"ds_read_b32 v214, v255 offset:3584\n" \
"ds_read_b32 v215, v255 offset:3840\n" \
"ds_read_b32 v216, v255 offset:4096\n" \
"ds_read_b32 v217, v255 offset:4352\n" \
"ds_read_b32 v218, v255 offset:4608\n" \
"ds_read_b32 v219, v255 offset:4864\n" \
"ds_read_b32 v220, v255 offset:5120\n" \
"ds_read_b32 v221, v255 offset:5376\n" \
"ds_read_b32 v222, v255 offset:5632\n" \
"ds_read_b32 v223, v255 offset:5888\n" \
"ds_read_b32 v254, v255 offset:7936\n" \
"s_waitcnt lgkmcnt(0)\n" \
"v_readlane_b32 s4, v254, 0\n" \
"v_readlane_b32 s5, v254, 1\n" \
"v_readlane_b32 s6, v254, 2\n" \
"v_readlane_b32 s7, v254, 3\n" \
"v_readlane_b32 s8, v254, 4\n" \
"v_readlane_b32 s9, v254, 5\n" \
"v_readlane_b32 s10, v254, 6\n" \
"v_readlane_b32 s11, v254, 7\n" \
"v_readlane_b32 s12, v254, 8\n" \
"v_readlane_b32 s13, v254, 9\n" \
"v_readlane_b32 s14, v254, 10\n" \
"v_readlane_b32 s15, v254, 11\n" \
"v_readlane_b32 s16, v254, 12\n" \
"v_readlane_b32 s17, v254, 13\n" \
"v_readlane_b32 s18, v254, 14\n" \
"v_readlane_b32 s19, v254, 15\n" \
"v_readlane_b32 s20, v254, 16\n" \
"v_readlane_b32 s21, v254, 17\n" \
"v_readlane_b32 s22, v254, 18\n" \
"v_readlane_b32 s23, v254, 19\n" \
"v_readlane_b32 s24, v254, 20\n" \
"v_readlane_b32 s25, v254, 21\n" \
"v_readlane_b32 s26, v254, 22\n" \
"v_readlane_b32 s27, v254, 23\n" \
"v_readlane_b32 s28, v254, 24\n" \
"v_readlane_b32 s29, v254, 25\n" \
"v_readlane_b32 s30, v254, 26\n" \
"v_readlane_b32 s31, v254, 27\n" \
"v_readlane_b32 s32, v254, 28\n" \
"v_readlane_b32 s33, v254, 29\n" \
"v_readlane_b32 s34, v254, 30\n" \
"v_readlane_b32 s35, v254, 31\n" \
"v_readlane_b32 s36, v254, 32\n" \
"v_readlane_b32 s37, v254, 33\n" \
"v_readlane_b32 s38, v254, 34\n" \
"v_readlane_b32 s39, v254, 35\n" \
"s_mov_b32 s40, 0\n" \
"s_mov_b32 s51, 0\n" \
"s_nop 0\n" \
"v_mov_b32 v252, s38\n" \
"s_nop 4\n" \
"v_mov_b32 v112, -1\n" \
"v_mov_b32 v113, -1\n" \
"v_mov_b32 v114, -1\n" \
"v_mov_b32 v115, -1\n" \
"v_mov_b32 v109, 0x7fc00001\n" \
"s_mov_b32 s52, 0x8000\n" \
"s_mov_b32 s53, 0x4000\n" \
"s_mov_b32 s54, 0xc000\n" \
"v_writelane_b32 v251, s6, 0\n" \
"v_writelane_b32 v251, s10, 1\n" \
"v_writelane_b32 v251, s14, 2\n" \
"v_writelane_b32 v251, s18, 3\n" \
"s_mov_b32 s6, 0\n" \
"s_mov_b32 s10, 0\n" \
"s_mov_b32 s14, 0\n" \
"s_mov_b32 s18, 0\n" \
"s_mov_b32 s40, -1\n" \
"s_branch .LP0_pre\n" \
".LP0_wet:\n" \
"s_waitcnt vmcnt(0)\n" \
"v_readlane_b32 s6, v251, 0\n" \
"v_readlane_b32 s10, v251, 1\n" \
"v_readlane_b32 s14, v251, 2\n" \
"v_readlane_b32 s18, v251, 3\n" \
"s_nop 4\n" \
"ds_read_b32 v230, v255 offset:6912\n" \
"v_readlane_b32 s60, v254, 12\n" \
"v_readlane_b32 s61, v254, 13\n" \
"v_readlane_b32 s62, v254, 14\n" \
"v_readlane_b32 s63, v254, 15\n" \
"s_waitcnt lgkmcnt(0)\n" \
"s_nop 3\n" \
"buffer_load_dword v228, v230, s[60:63], 0 offen\n" \
"s_mov_b32 s33, 36\n" \
"s_mov_b32 s34, 0\n" \
"s_add_u32 s43, s30, 1\n" \
"s_mov_b32 s50, 0\n" \
".LP0_w21l:\n" \
"buffer_load_dword v240, v204, s[4:7], 0 offen sc1\n" \
"s_waitcnt vmcnt(0)\n" \
".LP0_w21c:\n" \
"v_subrev_u32 v241, s43, v240\n" \
"v_cmp_gt_u32 vcc, 0x100, v241\n" \
"s_cmp_eq_u64 vcc, -1\n" \
"s_cbranch_scc1 .LP0_w21d\n" \
"s_sleep 2\n" \
"s_add_u32 s50, s50, 1\n" \
"s_cmp_lt_u32 s50, 0x20000\n" \
"s_cbranch_scc1 .LP0_w21l\n" \
"s_branch .LP0_end\n" \
".LP0_w21d:\n" \
"v_readlane_b32 s43, v202, 0\n" \
"s_nop 4\n" \
"buffer_load_dwordx4 a[0:3], v201, s[8:11], s43 offen sc1\n" \
"buffer_load_dwordx4 a[4:7], v201, s[8:11], s43 offen offset:1024 sc1\n" \
"v_readlane_b32 s43, v202, 7\n" \
"s_nop 4\n" \
"buffer_load_dwordx4 a[56:59], v201, s[8:11], s43 offen sc1\n" \
"buffer_load_dwordx4 a[60:63], v201, s[8:11], s43 offen offset:1024 sc1\n" \
"v_readlane_b32 s43, v202, 14\n" \
"s_nop 4\n" \
"buffer_load_dwordx4 a[112:115], v201, s[8:11], s43 offen sc1\n" \
"buffer_load_dwordx4 a[116:119], v201, s[8:11], s43 offen offset:1024 sc1\n" \
"v_readlane_b32 s43, v202, 21\n" \
"s_nop 4\n" \
"buffer_load_dwordx4 a[168:171], v201, s[8:11], s43 offen sc1\n" \
"buffer_load_dwordx4 a[172:175], v201, s[8:11], s43 offen offset:1024 sc1\n" \
"s_add_u32 s43, s30, 2\n" \
"s_mov_b32 s50, 0\n" \
"s_branch .LP0_w22c\n" \
".LP0_w22l:\n" \
"buffer_load_dword v240, v204, s[4:7], 0 offen sc1\n" \
"s_waitcnt vmcnt(0)\n" \
".LP0_w22c:\n" \
"v_subrev_u32 v241, s43, v240\n" \
"v_cmp_gt_u32 vcc, 0x100, v241\n" \
"s_cmp_eq_u64 vcc, -1\n" \
"s_cbranch_scc1 .LP0_w22d\n" \
"s_sleep 2\n" \
"s_add_u32 s50, s50, 1\n" \
"s_cmp_lt_u32 s50, 0x20000\n" \
"s_cbranch_scc1 .LP0_w22l\n" \
"s_branch .LP0_end\n" \
".LP0_w22d:\n" \
"v_readlane_b32 s43, v202, 4\n" \
"s_nop 4\n" \
"buffer_load_dwordx4 a[32:35], v201, s[8:11], s43 offen sc1\n" \
"buffer_load_dwordx4 a[36:39], v201, s[8:11], s43 offen offset:1024 sc1\n" \
"v_readlane_b32 s43, v202, 11\n" \
"s_nop 4\n" \
"buffer_load_dwordx4 a[88:91], v201, s[8:11], s43 offen sc1\n" \
"buffer_load_dwordx4 a[92:95], v201, s[8:11], s43 offen offset:1024 sc1\n" \
"v_readlane_b32 s43, v202, 18\n" \
"s_nop 4\n" \
"buffer_load_dwordx4 a[144:147], v201, s[8:11], s43 offen sc1\n" \
"buffer_load_dwordx4 a[148:151], v201, s[8:11], s43 offen offset:1024 sc1\n" \
"v_readlane_b32 s43, v202, 25\n" \
"s_nop 4\n" \
"buffer_load_dwordx4 a[200:203], v201, s[8:11], s43 offen sc1\n" \
"buffer_load_dwordx4 a[204:207], v201, s[8:11], s43 offen offset:1024 sc1\n" \
"s_add_u32 s43, s30, 3\n" \
"s_mov_b32 s50, 0\n" \
"s_branch .LP0_w23c\n" \
".LP0_w23l:\n" \
"buffer_load_dword v240, v204, s[4:7], 0 offen sc1\n" \
"s_waitcnt vmcnt(0)\n" \
".LP0_w23c:\n" \
"v_subrev_u32 v241, s43, v240\n" \
"v_cmp_gt_u32 vcc, 0x100, v241\n" \
"s_cmp_eq_u64 vcc, -1\n" \
"s_cbranch_scc1 .LP0_w23d\n" \
"s_sleep 2\n" \
"s_add_u32 s50, s50, 1\n" \
"s_cmp_lt_u32 s50, 0x20000\n" \
"s_cbranch_scc1 .LP0_w23l\n" \
"s_branch .LP0_end\n" \
".LP0_w23d:\n" \
"v_readlane_b32 s43, v202, 1\n" \
"s_nop 4\n" \
"buffer_load_dwordx4 a[8:11], v201, s[8:11], s43 offen sc1\n" \
"buffer_load_dwordx4 a[12:15], v201, s[8:11], s43 offen offset:1024 sc1\n" \
"v_readlane_b32 s43, v202, 8\n" \
"s_nop 4\n" \
"buffer_load_dwordx4 a[64:67], v201, s[8:11], s43 offen sc1\n" \
"buffer_load_dwordx4 a[68:71], v201, s[8:11], s43 offen offset:1024 sc1\n" \
"v_readlane_b32 s43, v202, 15\n" \
"s_nop 4\n" \
"buffer_load_dwordx4 a[120:123], v201, s[8:11], s43 offen sc1\n" \
"buffer_load_dwordx4 a[124:127], v201, s[8:11], s43 offen offset:1024 sc1\n" \
"v_readlane_b32 s43, v202, 22\n" \
"s_nop 4\n" \
"buffer_load_dwordx4 a[176:179], v201, s[8:11], s43 offen sc1\n" \
"buffer_load_dwordx4 a[180:183], v201, s[8:11], s43 offen offset:1024 sc1\n" \
"s_add_u32 s43, s30, 4\n" \
"s_mov_b32 s50, 0\n" \
"s_branch .LP0_w24c\n" \
".LP0_w24l:\n" \
"buffer_load_dword v240, v204, s[4:7], 0 offen sc1\n" \
"s_waitcnt vmcnt(0)\n" \
".LP0_w24c:\n" \
"v_subrev_u32 v241, s43, v240\n" \
"v_cmp_gt_u32 vcc, 0x100, v241\n" \
"s_cmp_eq_u64 vcc, -1\n" \
"s_cbranch_scc1 .LP0_w24d\n" \
"s_sleep 2\n" \
"s_add_u32 s50, s50, 1\n" \
"s_cmp_lt_u32 s50, 0x20000\n" \
"s_cbranch_scc1 .LP0_w24l\n" \
"s_branch .LP0_end\n" \
".LP0_w24d:\n" \
"v_readlane_b32 s43, v202, 5\n" \
"s_nop 4\n" \
"buffer_load_dwordx4 a[40:43], v201, s[8:11], s43 offen sc1\n" \
"buffer_load_dwordx4 a[44:47], v201, s[8:11], s43 offen offset:1024 sc1\n" \
"v_readlane_b32 s43, v202, 12\n" \
"s_nop 4\n" \
"buffer_load_dwordx4 a[96:99], v201, s[8:11], s43 offen sc1\n" \
"buffer_load_dwordx4 a[100:103], v201, s[8:11], s43 offen offset:1024 sc1\n" \
"v_readlane_b32 s43, v202, 19\n" \
"s_nop 4\n" \
"buffer_load_dwordx4 a[152:155], v201, s[8:11], s43 offen sc1\n" \
"buffer_load_dwordx4 a[156:159], v201, s[8:11], s43 offen offset:1024 sc1\n" \
"v_readlane_b32 s43, v202, 26\n" \
"s_nop 4\n" \
"buffer_load_dwordx4 a[208:211], v201, s[8:11], s43 offen sc1\n" \
"buffer_load_dwordx4 a[212:215], v201, s[8:11], s43 offen offset:1024 sc1\n" \
"s_add_u32 s43, s30, 5\n" \
"s_mov_b32 s50, 0\n" \
"s_branch .LP0_w25c\n" \
".LP0_w25l:\n" \
"buffer_load_dword v240, v204, s[4:7], 0 offen sc1\n" \
"s_waitcnt vmcnt(0)\n" \
".LP0_w25c:\n" \
"v_subrev_u32 v241, s43, v240\n" \
"v_cmp_gt_u32 vcc, 0x100, v241\n" \
"s_cmp_eq_u64 vcc, -1\n" \
"s_cbranch_scc1 .LP0_w25d\n" \
"s_sleep 2\n" \
"s_add_u32 s50, s50, 1\n" \
"s_cmp_lt_u32 s50, 0x20000\n" \
"s_cbranch_scc1 .LP0_w25l\n" \
"s_branch .LP0_end\n" \
".LP0_w25d:\n" \
"v_readlane_b32 s43, v202, 2\n" \
"s_nop 4\n" \
"buffer_load_dwordx4 a[16:19], v201, s[8:11], s43 offen sc1\n" \
"buffer_load_dwordx4 a[20:23], v201, s[8:11], s43 offen offset:1024 sc1\n" \
"v_readlane_b32 s43, v202, 9\n" \
"s_nop 4\n" \
"buffer_load_dwordx4 a[72:75], v201, s[8:11], s43 offen sc1\n" \
"buffer_load_dwordx4 a[76:79], v201, s[8:11], s43 offen offset:1024 sc1\n" \
"v_readlane_b32 s43, v202, 16\n" \
"s_nop 4\n" \
"buffer_load_dwordx4 a[128:131], v201, s[8:11], s43 offen sc1\n" \
"buffer_load_dwordx4 a[132:135], v201, s[8:11], s43 offen offset:1024 sc1\n" \
"v_readlane_b32 s43, v202, 23\n" \
"s_nop 4\n" \
"buffer_load_dwordx4 a[184:187], v201, s[8:11], s43 offen sc1\n" \
"buffer_load_dwordx4 a[188:191], v201, s[8:11], s43 offen offset:1024 sc1\n" \
"s_add_u32 s43, s30, 6\n" \
"s_mov_b32 s50, 0\n" \
"s_branch .LP0_w26c\n" \
".LP0_w26l:\n" \
"buffer_load_dword v240, v204, s[4:7], 0 offen sc1\n" \
"s_waitcnt vmcnt(0)\n" \
".LP0_w26c:\n" \
"v_subrev_u32 v241, s43, v240\n" \
"v_cmp_gt_u32 vcc, 0x100, v241\n" \
"s_cmp_eq_u64 vcc, -1\n" \
"s_cbranch_scc1 .LP0_w26d\n" \
"s_sleep 2\n" \
"s_add_u32 s50, s50, 1\n" \
"s_cmp_lt_u32 s50, 0x20000\n" \
"s_cbranch_scc1 .LP0_w26l\n" \
"s_branch .LP0_end\n" \
".LP0_w26d:\n" \
"v_readlane_b32 s43, v202, 6\n" \
"s_nop 4\n" \
"buffer_load_dwordx4 a[48:51], v201, s[8:11], s43 offen sc1\n" \
"buffer_load_dwordx4 a[52:55], v201, s[8:11], s43 offen offset:1024 sc1\n" \
"v_readlane_b32 s43, v202, 13\n" \
"s_nop 4\n" \
"buffer_load_dwordx4 a[104:107], v201, s[8:11], s43 offen sc1\n" \
"buffer_load_dwordx4 a[108:111], v201, s[8:11], s43 offen offset:1024 sc1\n" \
"v_readlane_b32 s43, v202, 20\n" \
"s_nop 4\n" \
"buffer_load_dwordx4 a[160:163], v201, s[8:11], s43 offen sc1\n" \
"buffer_load_dwordx4 a[164:167], v201, s[8:11], s43 offen offset:1024 sc1\n" \
"v_readlane_b32 s43, v202, 27\n" \
"s_nop 4\n" \
"buffer_load_dwordx4 a[216:219], v201, s[8:11], s43 offen sc1\n" \
"buffer_load_dwordx4 a[220:223], v201, s[8:11], s43 offen offset:1024 sc1\n" \
"s_add_u32 s43, s30, 7\n" \
"s_mov_b32 s50, 0\n" \
"s_branch .LP0_w27c\n" \
".LP0_w27l:\n" \
"buffer_load_dword v240, v204, s[4:7], 0 offen sc1\n" \
"s_waitcnt vmcnt(0)\n" \
".LP0_w27c:\n" \
"v_subrev_u32 v241, s43, v240\n" \
"v_cmp_gt_u32 vcc, 0x100, v241\n" \
"s_cmp_eq_u64 vcc, -1\n" \
"s_cbranch_scc1 .LP0_w27d\n" \
"s_sleep 2\n" \
"s_add_u32 s50, s50, 1\n" \
"s_cmp_lt_u32 s50, 0x20000\n" \
"s_cbranch_scc1 .LP0_w27l\n" \
"s_branch .LP0_end\n" \
".LP0_w27d:\n" \
"v_readlane_b32 s43, v202, 3\n" \
"s_nop 4\n" \
"buffer_load_dwordx4 a[24:27], v201, s[8:11], s43 offen sc1\n" \
"buffer_load_dwordx4 a[28:31], v201, s[8:11], s43 offen offset:1024 sc1\n" \
"v_readlane_b32 s43, v202, 10\n" \
"s_nop 4\n" \
"buffer_load_dwordx4 a[80:83], v201, s[8:11], s43 offen sc1\n" \
"buffer_load_dwordx4 a[84:87], v201, s[8:11], s43 offen offset:1024 sc1\n" \
"v_readlane_b32 s43, v202, 17\n" \
"s_nop 4\n" \
"buffer_load_dwordx4 a[136:139], v201, s[8:11], s43 offen sc1\n" \
"buffer_load_dwordx4 a[140:143], v201, s[8:11], s43 offen offset:1024 sc1\n" \
"v_readlane_b32 s43, v202, 24\n" \
"s_nop 4\n" \
"buffer_load_dwordx4 a[192:195], v201, s[8:11], s43 offen sc1\n" \
"buffer_load_dwordx4 a[196:199], v201, s[8:11], s43 offen offset:1024 sc1\n" \
"s_add_u32 s43, s30, 1\n" \
"s_mov_b32 s50, 0\n" \
".LP0_w28l:\n" \
"buffer_load_dword v240, v205, s[4:7], 0 offen sc1\n" \
"s_waitcnt vmcnt(0)\n" \
".LP0_w28c:\n" \
"v_subrev_u32 v241, s43, v240\n" \
"v_cmp_gt_u32 vcc, 0x100, v241\n" \
"s_cmp_eq_u64 vcc, -1\n" \
"s_cbranch_scc1 .LP0_w28d\n" \
"s_sleep 2\n" \
"s_add_u32 s50, s50, 1\n" \
"s_cmp_lt_u32 s50, 0x20000\n" \
"s_cbranch_scc1 .LP0_w28l\n" \
"s_branch .LP0_end\n" \
".LP0_w28d:\n" \
"v_readfirstlane_b32 s31, v241\n" \
"s_lshr_b32 s32, s31, 2\n" \
"s_xor_b32 s29, s31, s32\n" \
"s_and_b32 s29, s29, 3\n" \
"s_lshl_b32 s29, s29, 13\n" \
"s_bitset1_b32 s29, 21\n" \
"s_and_b32 s31, s31, 3\n" \
"s_lshl_b32 s31, s31, 13\n" \
"s_bitcmp1_b32 s39, 0\n" \
"s_cselect_b32 s43, s29, 0\n" \
"s_xor_b32 s31, s31, s43\n" \
"s_xor_b32 s32, s31, 0x4000\n" \
"s_mov_b32 s39, 0\n" \
"s_add_u32 s43, s40, 0\n" \
"s_and_b32 s43, s43, 3\n" \
"s_bitcmp1_b32 s43, 0\n" \
"s_cselect_b32 s47, s32, s31\n" \
"s_lshl_b32 s43, s43, 19\n" \
"s_add_u32 s47, s47, s43\n" \
".LP0_pre:\n" \
"s_barrier\n" \
"ds_read_b96 v[248:250], v252\n" \
"s_waitcnt lgkmcnt(0)\n" \
"v_readfirstlane_b32 s44, v248\n" \
"v_readfirstlane_b32 s45, v249\n" \
"v_readfirstlane_b32 s46, v250\n" \
"s_mul_i32 s42, s25, s46\n" \
"s_lshr_b32 s42, s42, 12\n" \
".LP0_cu:\n" \
"s_sub_u32 s43, s24, s44\n" \
"s_cmp_lt_i32 s43, 0\n" \
"s_cbranch_scc0 .LP0_cud\n" \
"s_add_u32 s24, s24, s25\n" \
"s_xor_b32 s31, s31, s29\n" \
"s_xor_b32 s32, s32, s29\n" \
"s_xor_b32 s47, s47, s29\n" \
"s_add_u32 s39, s39, 1\n" \
"s_branch .LP0_cu\n" \
".LP0_cud:\n" \
"s_mov_b32 s3, s46\n" \
"s_mov_b32 s2, s46\n" \
"s_add_u32 s43, s40, 1\n" \
"s_and_b32 s43, s43, 3\n" \
"s_bitcmp1_b32 s43, 0\n" \
"s_cselect_b32 s48, s32, s31\n" \
"s_lshl_b32 s43, s43, 19\n" \
"s_add_u32 s48, s48, s43\n" \
"s_add_u32 s43, s40, 2\n" \
"s_and_b32 s43, s43, 3\n" \
"s_bitcmp1_b32 s43, 0\n" \
"s_cselect_b32 s49, s32, s31\n" \
"s_lshl_b32 s43, s43, 19\n" \
"s_add_u32 s49, s49, s43\n" \
"s_sub_u32 s43, s24, s44\n" \
"s_add_u32 s43, s43, s28\n" \
"s_max_i32 s43, s43, 0xfffd0000\n" \
"s_min_i32 s43, s43, 0x4000\n" \
"s_mul_i32 s43, s43, s46\n" \
"s_ashr_i32 s43, s43, 12\n" \
"s_add_u32 s41, s45, s43\n" \
"s_lshr_b32 s35, s42, 1\n" \
"s_sub_u32 s35, 0, s35\n" \
".LP0_loop:\n" \
"s_branch .LP0_m1\n" \
".LP0_retry1:\n" \
"s_add_u32 s51, s51, 1\n" \
"s_cmp_gt_u32 s51, 0x20000\n" \
"s_cbranch_scc1 .LP0_end\n" \
".LP0_m0:\n" \
"s_add_u32 s41, s41, s42\n" \
"s_add_u32 s24, s24, s25\n" \
"s_xor_b32 s31, s31, s29\n" \
"s_xor_b32 s32, s32, s29\n" \
"s_xor_b32 s47, s47, s29\n" \
"s_xor_b32 s48, s48, s29\n" \
"s_add_u32 s39, s39, 1\n" \
"s_bitcmp1_b32 s31, 21\n" \
"s_cselect_b32 s43, s29, 0\n" \
"v_mov_b32 v116, s43\n" \
"ds_write_b32 v252, v116 offset:16\n" \
".LP0_m1:\n" \
"s_memtime vcc\n" \
"s_waitcnt lgkmcnt(0)\n" \
"s_sub_u32 s43, s41, vcc_lo\n" \
"s_cmp_gt_i32 s43, 0\n" \
"s_cbranch_scc1 .LP0_m1\n" \
"s_cmp_lt_i32 s43, s35\n" \
"s_cbranch_scc1 .LP0_m0\n" \
"buffer_load_dwordx4 v[0:3], v208, s[4:7], s47 offen sc1\n" \
"buffer_load_dwordx4 v[4:7], v208, s[4:7], s47 offen offset:1024 sc1\n" \
"s_nop 15\n" \
"s_nop 13\n" \
"s_mul_i32 s50, s40, 4096\n" \
"buffer_store_dwordx4 v[68:71], v218, s[12:15], s50 offen sc1\n" \
"buffer_store_dword v112, v213, s[4:7], s49 offen sc1\n" \
"s_xor_b32 s43, s49, s29\n" \
"buffer_store_dword v112, v213, s[4:7], s43 offen sc1\n" \
"s_add_u32 s43, s30, s40\n" \
"s_add_u32 s43, s43, -1\n" \
"v_mov_b32 v241, s43\n" \
"buffer_store_dword v241, v221, s[4:7], 0 offen sc1\n" \
"s_bitcmp1_b32 s31, 21\n" \
"s_cselect_b32 s43, s29, 0\n" \
"v_mov_b32 v116, s43\n" \
"ds_write_b32 v252, v116 offset:16\n" \
"v_mov_b32 v28, 0\n" \
"v_mov_b32 v29, 0\n" \
"v_mov_b32 v30, 0\n" \
"v_mov_b32 v31, 0\n" \
"v_mov_b32 v32, 0\n" \
"v_mov_b32 v33, 0\n" \
"v_mov_b32 v34, 0\n" \
"v_mov_b32 v35, 0\n" \
"v_mov_b32 v36, 0\n" \
"v_mov_b32 v37, 0\n" \
"v_mov_b32 v38, 0\n" \
"v_mov_b32 v39, 0\n" \
"v_mov_b32 v40, 0\n" \
"v_mov_b32 v41, 0\n" \
"v_mov_b32 v42, 0\n" \
"v_mov_b32 v43, 0\n" \
"s_waitcnt vmcnt(5)\n" \
"ds_write_b128 v220, v[0:3] offset:0\n" \
"s_waitcnt vmcnt(4)\n" \
"ds_write_b128 v220, v[4:7] offset:1024\n" \
"v_max3_u32 v44, v0, v0, v1\n" \
"v_max3_u32 v44, v44, v2, v3\n" \
"v_max3_u32 v44, v44, v4, v5\n" \
"v_max3_u32 v44, v44, v6, v7\n" \
"v_cmp_eq_u32 vcc, -1, v44\n" \
"v_smfmac_f32_16x16x64_bf16 v[28:31], v[0:3], a[0:7], v200\n" \
"v_smfmac_f32_16x16x64_bf16 v[32:35], v[0:3], a[56:63], v200\n" \
"s_cbranch_vccnz .LP0_retry1\n" \
"s_waitcnt lgkmcnt(0)\n" \
"s_barrier\n" \
"ds_read_b128 v[8:11], v220 offset:2048\n" \
"ds_read_b128 v[12:15], v220 offset:3072\n" \
"ds_read_b128 v[16:19], v220 offset:4096\n" \
"ds_read_b128 v[20:23], v220 offset:5120\n" \
"ds_read_b128 v[24:27], v220 offset:6144\n" \
"ds_read_b64 v[246:247], v252 offset:24\n" \
"ds_read_b96 v[248:250], v252\n" \
"v_smfmac_f32_16x16x64_bf16 v[36:39], v[0:3], a[112:119], v200\n" \
"v_smfmac_f32_16x16x64_bf16 v[40:43], v[0:3], a[168:175], v200\n" \
"v_smfmac_f32_16x16x64_bf16 v[28:31], v[4:7], a[8:15], v200\n" \
"v_smfmac_f32_16x16x64_bf16 v[32:35], v[4:7], a[64:71], v200\n" \
"v_smfmac_f32_16x16x64_bf16 v[36:39], v[4:7], a[120:127], v200\n" \
"v_smfmac_f32_16x16x64_bf16 v[40:43], v[4:7], a[176:183], v200\n" \
"s_waitcnt lgkmcnt(6)\n" \
"v_smfmac_f32_16x16x64_bf16 v[28:31], v[8:11], a[16:23], v200\n" \
"v_smfmac_f32_16x16x64_bf16 v[32:35], v[8:11], a[72:79], v200\n" \
"v_smfmac_f32_16x16x64_bf16 v[36:39], v[8:11], a[128:135], v200\n" \
"v_smfmac_f32_16x16x64_bf16 v[40:43], v[8:11], a[184:191], v200\n" \
"s_waitcnt lgkmcnt(0)\n" \
"v_readfirstlane_b32 s44, v248\n" \
"v_readfirstlane_b32 s45, v249\n" \
"v_smfmac_f32_16x16x64_bf16 v[28:31], v[12:15], a[24:31], v200\n" \
"v_readfirstlane_b32 s46, v250\n" \
"v_readfirstlane_b32 s26, v246\n" \
"v_readfirstlane_b32 s27, v247\n" \
"v_smfmac_f32_16x16x64_bf16 v[32:35], v[12:15], a[80:87], v200\n" \
"s_mul_i32 s42, s25, s46\n" \
"s_lshr_b32 s42, s42, 12\n" \
"s_add_u32 s24, s24, s26\n" \
"v_smfmac_f32_16x16x64_bf16 v[36:39], v[12:15], a[136:143], v200\n" \
"s_sub_u32 s43, s24, s44\n" \
"s_max_i32 s43, s43, 0xfffd0000\n" \
"s_min_i32 s43, s43, 0x4000\n" \
"v_smfmac_f32_16x16x64_bf16 v[40:43], v[12:15], a[192:199], v200\n" \
"s_mul_i32 s43, s43, s46\n" \
"s_ashr_i32 s43, s43, 12\n" \
"s_add_u32 s41, s45, s43\n" \
"s_cmp_eq_u32 s40, 63\n" \
"s_cbranch_scc1 .LP0_last\n" \
"s_branch .LP0_z1\n" \
".LP0_z0:\n" \
"s_add_u32 s41, s41, s42\n" \
"s_add_u32 s24, s24, s25\n" \
"s_xor_b32 s31, s31, s29\n" \
"s_xor_b32 s32, s32, s29\n" \
"s_xor_b32 s47, s47, s29\n" \
"s_xor_b32 s48, s48, s29\n" \
"s_add_u32 s39, s39, 1\n" \
"s_bitcmp1_b32 s31, 21\n" \
"s_cselect_b32 s43, s29, 0\n" \
"v_mov_b32 v116, s43\n" \
"ds_write_b32 v252, v116 offset:16\n" \
".LP0_z1:\n" \
"s_memtime vcc\n" \
"s_waitcnt lgkmcnt(0)\n" \
"s_sub_u32 s43, s41, vcc_lo\n" \
"s_cmp_gt_i32 s43, 160\n" \
"s_cbranch_scc1 .LP0_z1\n" \
"s_cmp_lt_i32 s43, s37\n" \
"s_cbranch_scc1 .LP0_z0\n" \
"buffer_load_dwordx2 v[64:65], v211, s[4:7], s47 offen sc1\n" \
".p2align 6\n" \
".LP0_b1:\n" \
"s_memtime vcc\n" \
"s_waitcnt lgkmcnt(0)\n" \
"s_sub_u32 s43, s41, vcc_lo\n" \
"s_cmp_gt_i32 s43, 128\n" \
"s_cbranch_scc1 .LP0_b1\n" \
"s_cmp_lt_i32 s43, s37\n" \
"s_cbranch_scc1 .LP0_z0\n" \
"s_max_i32 s43, s43, 0\n" \
"s_and_b32 s43, s43, -4\n" \
"s_getpc_b64 vcc\n" \
".LP0_b3:\n" \
"s_sub_u32 s43, .LP0_b4-.LP0_b3, s43\n" \
"s_add_u32 vcc_lo, vcc_lo, s43\n" \
"s_addc_u32 vcc_hi, vcc_hi, 0\n" \
"s_setpc_b64 vcc\n" \
".rept 32\n" \
"s_nop 0\n" \
".endr\n" \
".LP0_b4:\n" \
"v_smfmac_f32_16x16x64_bf16 v[28:31], v[16:19], a[32:39], v200\n" \
"buffer_load_dwordx4 v[56:59], v210, s[4:7], s47 offen sc1\n" \
"buffer_load_dwordx4 v[60:63], v210, s[4:7], s47 offen offset:256 sc1\n" \
"v_smfmac_f32_16x16x64_bf16 v[32:35], v[16:19], a[88:95], v200\n" \
"v_smfmac_f32_16x16x64_bf16 v[36:39], v[16:19], a[144:151], v200\n" \
"v_smfmac_f32_16x16x64_bf16 v[40:43], v[16:19], a[200:207], v200\n" \
"v_smfmac_f32_16x16x64_bf16 v[28:31], v[20:23], a[40:47], v200\n" \
"buffer_load_dwordx4 v[50:53], v212, s[4:7], s47 offen sc1\n" \
"v_smfmac_f32_16x16x64_bf16 v[32:35], v[20:23], a[96:103], v200\n" \
"v_smfmac_f32_16x16x64_bf16 v[36:39], v[20:23], a[152:159], v200\n" \
"v_smfmac_f32_16x16x64_bf16 v[40:43], v[20:23], a[208:215], v200\n" \
"v_smfmac_f32_16x16x64_bf16 v[28:31], v[24:27], a[48:55], v200\n" \
"v_smfmac_f32_16x16x64_bf16 v[32:35], v[24:27], a[104:111], v200\n" \
"v_smfmac_f32_16x16x64_bf16 v[36:39], v[24:27], a[160:167], v200\n" \
"v_smfmac_f32_16x16x64_bf16 v[40:43], v[24:27], a[216:223], v200\n" \
"s_nop 2\n" \
"v_mov_b32 v245, v245\n" \
"v_mov_b32 v245, v245\n" \
"v_mov_b32 v245, v245\n" \
"v_permlane32_swap_b32 v28, v32\n" \
"v_permlane32_swap_b32 v29, v33\n" \
"v_permlane32_swap_b32 v30, v34\n" \
"v_permlane32_swap_b32 v31, v35\n" \
"v_add_f32 v28, v28, v32\n" \
"v_add_f32 v29, v29, v33\n" \
"v_add_f32 v30, v30, v34\n" \
"v_add_f32 v31, v31, v35\n" \
"v_cvt_pk_bf16_f32 v68, v28, v29\n" \
"v_cvt_pk_bf16_f32 v69, v30, v31\n" \
"v_permlane32_swap_b32 v36, v40\n" \
"v_permlane32_swap_b32 v37, v41\n" \
"v_permlane32_swap_b32 v38, v42\n" \
"v_permlane32_swap_b32 v39, v43\n" \
"v_add_f32 v36, v36, v40\n" \
"v_add_f32 v37, v37, v41\n" \
"v_add_f32 v38, v38, v42\n" \
"v_add_f32 v39, v39, v43\n" \
"v_cvt_pk_bf16_f32 v70, v36, v37\n" \
"v_cvt_pk_bf16_f32 v71, v38, v39\n" \
"v_mov_b32 v48, v68\n" \
"v_mov_b32 v49, v69\n" \
"v_mov_b32 v54, v70\n" \
"v_mov_b32 v55, v71\n" \
"s_nop 1\n" \
"v_permlane16_swap_b32 v48, v54\n" \
"v_permlane16_swap_b32 v49, v55\n" \
"s_nop 0\n" \
"v_mov_b32 v245, v245\n" \
"s_nop 2\n" \
"v_mov_b32 v245, v245\n" \
"s_nop 2\n" \
"v_mov_b32 v245, v245\n" \
"s_nop 2\n" \
"v_mov_b32 v245, v245\n" \
"s_nop 2\n" \
"v_mov_b32 v245, v245\n" \
"s_nop 2\n" \
"v_mov_b32 v245, v245\n" \
".LP0_wait:\n" \
"s_waitcnt vmcnt(1)\n" \
"v_pk_add_f32 v[72:73], v[56:57], v[60:61]\n" \
"v_pk_add_f32 v[74:75], v[58:59], v[62:63]\n" \
"v_max3_f32 v77, v72, v73, v64\n" \
"v_max3_f32 v77, v77, v74, v75\n" \
"s_nop 1\n" \
"v_max_f32_dpp v76, v77, v77 row_ror:8 row_mask:0xf bank_mask:0xf\n" \
"v_pk_add_f32 v[72:73], v[72:73], v[76:77] op_sel_hi:[1,0] neg_lo:[0,1] neg_hi:[0,1]\n" \
"v_pk_add_f32 v[74:75], v[74:75], v[76:77] op_sel_hi:[1,0] neg_lo:[0,1] neg_hi:[0,1]\n" \
"v_sub_f32 v78, v64, v76\n" \
"v_exp_f32 v72, v72\n" \
"v_exp_f32 v73, v73\n" \
"v_exp_f32 v74, v74\n" \
"v_exp_f32 v75, v75\n" \
"v_exp_f32 v78, v78\n" \
"v_pk_add_f32 v[80:81], v[72:73], v[74:75]\n" \
"v_fma_f32 v80, v78, v65, v80\n" \
"v_add_f32 v81, v80, v81\n" \
"s_nop 1\n" \
"v_add_f32_dpp v80, v81, v81 row_ror:8 row_mask:0xf bank_mask:0xf\n" \
"v_rcp_f32 v80, v80\n" \
"s_nop 0\n" \
"v_mul_f32 v78, v78, v80\n" \
"v_pk_mul_f32 v[72:73], v[72:73], v[80:81] op_sel_hi:[1,0]\n" \
"v_pk_mul_f32 v[74:75], v[74:75], v[80:81] op_sel_hi:[1,0]\n" \
"v_cvt_pk_bf16_f32 v78, v78, v78\n" \
"v_cvt_pk_bf16_f32 v82, v72, v73\n" \
"v_cvt_pk_bf16_f32 v83, v74, v75\n" \
"v_and_b32 v84, v78, v214\n" \
"v_and_b32 v85, v78, v215\n" \
"v_cvt_pk_bf16_f32 v86, v72, v73\n" \
"s_waitcnt vmcnt(0)\n" \
"v_mfma_f32_16x16x32_bf16 v[88:91], v[48:51], v[82:85], 0\n" \
"v_cvt_pk_bf16_f32 v87, v74, v75\n" \
"s_nop 1\n" \
"v_mfma_f32_16x16x32_bf16 v[92:95], v[52:55], v[84:87], 0\n" \
"s_bitcmp1_b32 s31, 21\n" \
"s_cselect_b32 s18, s29, 0\n" \
"s_xor_b32 s18, s18, s48\n" \
"v_mov_b32 v245, v245\n" \
"s_xor_b32 s19, s18, s29\n" \
"s_nop 0\n" \
"s_nop 0\n" \
"v_mov_b32 v245, v245\n" \
"v_add_f32_dpp v96, v92, v88 row_ror:8 row_mask:0xf bank_mask:0x3\n" \
"v_add_f32_dpp v97, v93, v89 row_ror:8 row_mask:0xf bank_mask:0x3\n" \
"v_add_f32_dpp v96, v90, v94 row_ror:8 row_mask:0xf bank_mask:0xc\n" \
"v_add_f32_dpp v97, v91, v95 row_ror:8 row_mask:0xf bank_mask:0xc\n" \
"ds_write_b64 v216, v[96:97]\n" \
"v_cmp_u_f32 vcc, v96, v97\n" \
"s_cbranch_vccnz .LP0_cls\n" \
".LP0_clsr:\n" \
"s_barrier\n" \
"ds_read_b64 v[100:101], v217 offset:0\n" \
"ds_read_b64 v[102:103], v217 offset:512\n" \
"ds_read_b64 v[104:105], v217 offset:1024\n" \
"ds_read_b64 v[106:107], v217 offset:1536\n" \
"ds_read_b32 v116, v252 offset:16\n" \
"s_add_u32 s40, s40, 1\n" \
"s_add_u32 s24, s24, s27\n" \
"s_mov_b32 s47, s48\n" \
"s_add_u32 s43, s40, 1\n" \
"s_and_b32 s43, s43, 3\n" \
"s_bitcmp1_b32 s43, 0\n" \
"s_cselect_b32 s48, s32, s31\n" \
"s_lshl_b32 s43, s43, 19\n" \
"s_waitcnt lgkmcnt(3)\n" \
"v_pk_add_f32 v[100:101], v[100:101], v[102:103]\n" \
"s_waitcnt lgkmcnt(0)\n" \
"v_pk_add_f32 v[104:105], v[104:105], v[106:107]\n" \
"v_pk_add_f32 v[100:101], v[100:101], v[104:105]\n" \
"v_xad_u32 v117, v116, s18, v213\n" \
"v_cvt_pk_bf16_f32 v96, v100, v101\n" \
"buffer_store_dword v96, v117, s[4:7], 0 offen sc1\n" \
"buffer_store_dword v96, v117, s[4:7], s52 offen sc1\n" \
"buffer_store_dword v96, v117, s[4:7], s53 offen sc1\n" \
"buffer_store_dword v96, v117, s[4:7], s54 offen sc1\n" \
"v_xad_u32 v116, v116, s19, v213\n" \
"s_add_u32 s48, s48, s43\n" \
"s_add_u32 s43, s40, 2\n" \
"s_and_b32 s43, s43, 3\n" \
"s_bitcmp1_b32 s43, 0\n" \
"s_cselect_b32 s49, s32, s31\n" \
"s_lshl_b32 s43, s43, 19\n" \
"s_add_u32 s49, s49, s43\n" \
"s_sub_u32 s43, s24, s44\n" \
"s_add_u32 s43, s43, s28\n" \
"s_max_i32 s43, s43, 0xfffd0000\n" \
"s_min_i32 s43, s43, 0x4000\n" \
"s_mul_i32 s43, s43, s46\n" \
"s_ashr_i32 s43, s43, 12\n" \
"s_add_u32 s41, s45, s43\n" \
"s_lshr_b32 s35, s42, 1\n" \
"s_sub_u32 s35, 0, s35\n" \
"buffer_store_dword v96, v116, s[4:7], 0 offen sc1\n" \
"buffer_store_dword v96, v116, s[4:7], s52 offen sc1\n" \
"buffer_store_dword v96, v116, s[4:7], s53 offen sc1\n" \
"buffer_store_dword v96, v116, s[4:7], s54 offen sc1\n" \
"s_cmp_eq_u32 s40, 0\n" \
"s_cbranch_scc1 .LP0_last\n" \
"s_branch .LP0_loop\n" \
"s_branch .LP0_y1\n" \
".LP0_retry2:\n" \
"s_add_u32 s51, s51, 1\n" \
"s_cmp_gt_u32 s51, 0x20000\n" \
"s_cbranch_scc1 .LP0_end\n" \
".LP0_y0:\n" \
"s_add_u32 s41, s41, s42\n" \
"s_add_u32 s24, s24, s25\n" \
"s_xor_b32 s31, s31, s29\n" \
"s_xor_b32 s32, s32, s29\n" \
"s_xor_b32 s47, s47, s29\n" \
"s_xor_b32 s48, s48, s29\n" \
"s_add_u32 s39, s39, 1\n" \
"buffer_store_dwordx4 v[112:115], v223, s[4:7], s48 offen sc1\n" \
"s_bitcmp1_b32 s31, 21\n" \
"s_cselect_b32 s43, s29, 0\n" \
"v_mov_b32 v116, s43\n" \
"ds_write_b32 v252, v116 offset:16\n" \
".LP0_y1:\n" \
"s_memtime vcc\n" \
"s_waitcnt lgkmcnt(0)\n" \
"s_sub_u32 s43, s41, vcc_lo\n" \
"s_cmp_gt_i32 s43, 160\n" \
"s_cbranch_scc1 .LP0_y1\n" \
"s_cmp_lt_i32 s43, s37\n" \
"s_cbranch_scc1 .LP0_y0\n" \
"buffer_load_dwordx2 v[64:65], v211, s[4:7], s47 offen sc1\n" \
".p2align 6\n" \
".LP0_c1:\n" \
"s_memtime vcc\n" \
"s_waitcnt lgkmcnt(0)\n" \
"s_sub_u32 s43, s41, vcc_lo\n" \
"s_cmp_gt_i32 s43, 128\n" \
"s_cbranch_scc1 .LP0_c1\n" \
"s_cmp_lt_i32 s43, s37\n" \
"s_cbranch_scc1 .LP0_y0\n" \
"s_max_i32 s43, s43, 0\n" \
"s_and_b32 s43, s43, -4\n" \
"s_getpc_b64 vcc\n" \
".LP0_c3:\n" \
"s_sub_u32 s43, .LP0_c4-.LP0_c3, s43\n" \
"s_add_u32 vcc_lo, vcc_lo, s43\n" \
"s_addc_u32 vcc_hi, vcc_hi, 0\n" \
"s_setpc_b64 vcc\n" \
".rept 32\n" \
"s_nop 0\n" \
".endr\n" \
".LP0_c4:\n" \
"s_nop 2\n" \
"v_mov_b32 v245, v245\n" \
"buffer_load_dwordx4 v[56:59], v210, s[4:7], s47 offen sc1\n" \
"buffer_load_dwordx4 v[60:63], v210, s[4:7], s47 offen offset:256 sc1\n" \
"s_nop 2\n" \
"v_mov_b32 v245, v245\n" \
"s_nop 2\n" \
"v_mov_b32 v245, v245\n" \
"s_nop 2\n" \
"v_mov_b32 v245, v245\n" \
"s_nop 2\n" \
"v_mov_b32 v245, v245\n" \
"buffer_load_dwordx4 v[50:53], v212, s[4:7], s47 offen sc1\n" \
"s_nop 2\n" \
"v_mov_b32 v245, v245\n" \
"s_nop 2\n" \
"v_mov_b32 v245, v245\n" \
"s_nop 2\n" \
"v_mov_b32 v245, v245\n" \
"s_nop 2\n" \
"v_mov_b32 v245, v245\n" \
"s_nop 2\n" \
"v_mov_b32 v245, v245\n" \
"s_nop 2\n" \
"v_mov_b32 v245, v245\n" \
"s_nop 2\n" \
"v_mov_b32 v245, v245\n" \
"s_nop 2\n" \
"v_mov_b32 v245, v245\n" \
"s_nop 2\n" \
"v_mov_b32 v245, v245\n" \
"s_nop 2\n" \
"v_mov_b32 v245, v245\n" \
"s_nop 2\n" \
"v_mov_b32 v245, v245\n" \
"s_nop 2\n" \
"v_mov_b32 v245, v245\n" \
"s_nop 2\n" \
"v_mov_b32 v245, v245\n" \
"s_nop 2\n" \
"v_mov_b32 v245, v245\n" \
"s_nop 2\n" \
"v_mov_b32 v245, v245\n" \
"s_nop 2\n" \
"v_mov_b32 v245, v245\n" \
"s_nop 2\n" \
"v_mov_b32 v245, v245\n" \
"s_nop 2\n" \
"v_mov_b32 v245, v245\n" \
"s_nop 0\n" \
"v_mov_b32 v245, v245\n" \
"s_nop 2\n" \
"v_mov_b32 v245, v245\n" \
"s_nop 2\n" \
"v_mov_b32 v245, v245\n" \
"s_nop 2\n" \
"v_mov_b32 v245, v245\n" \
"s_nop 2\n" \
"v_mov_b32 v245, v245\n" \
"s_nop 2\n" \
"v_mov_b32 v245, v245\n" \
"s_branch .LP0_wait\n" \
".LP0_cls:\n" \
"s_waitcnt vmcnt(0)\n" \
"v_max3_u32 v44, v64, v64, v65\n" \
"v_max3_u32 v44, v44, v50, v51\n" \
"v_max3_u32 v44, v44, v52, v53\n" \
"v_cmp_eq_u32 vcc, -1, v44\n" \
"s_cbranch_vccnz .LP0_retry2\n" \
"v_cmp_u_f32 vcc, v56, v56\n" \
"v_cmp_ne_u32 s[56:57], v56, v109\n" \
"s_and_b64 vcc, vcc, s[56:57]\n" \
"s_cbranch_vccnz .LP0_retry2\n" \
"v_cmp_u_f32 vcc, v57, v57\n" \
"v_cmp_ne_u32 s[56:57], v57, v109\n" \
"s_and_b64 vcc, vcc, s[56:57]\n" \
"s_cbranch_vccnz .LP0_retry2\n" \
"v_cmp_u_f32 vcc, v58, v58\n" \
"v_cmp_ne_u32 s[56:57], v58, v109\n" \
"s_and_b64 vcc, vcc, s[56:57]\n" \
"s_cbranch_vccnz .LP0_retry2\n" \
"v_cmp_u_f32 vcc, v59, v59\n" \
"v_cmp_ne_u32 s[56:57], v59, v109\n" \
"s_and_b64 vcc, vcc, s[56:57]\n" \
"s_cbranch_vccnz .LP0_retry2\n" \
"v_cmp_u_f32 vcc, v60, v60\n" \
"v_cmp_ne_u32 s[56:57], v60, v109\n" \
"s_and_b64 vcc, vcc, s[56:57]\n" \
"s_cbranch_vccnz .LP0_retry2\n" \
"v_cmp_u_f32 vcc, v61, v61\n" \
"v_cmp_ne_u32 s[56:57], v61, v109\n" \
"s_and_b64 vcc, vcc, s[56:57]\n" \
"s_cbranch_vccnz .LP0_retry2\n" \
"v_cmp_u_f32 vcc, v62, v62\n" \
"v_cmp_ne_u32 s[56:57], v62, v109\n" \
"s_and_b64 vcc, vcc, s[56:57]\n" \
"s_cbranch_vccnz .LP0_retry2\n" \
"v_cmp_u_f32 vcc, v63, v63\n" \
"v_cmp_ne_u32 s[56:57], v63, v109\n" \
"s_and_b64 vcc, vcc, s[56:57]\n" \
"s_cbranch_vccnz .LP0_retry2\n" \
"s_branch .LP0_clsr\n" \
".LP0_last:\n" \
"v_smfmac_f32_16x16x64_bf16 v[28:31], v[16:19], a[32:39], v200\n" \
"v_smfmac_f32_16x16x64_bf16 v[32:35], v[16:19], a[88:95], v200\n" \
"v_smfmac_f32_16x16x64_bf16 v[36:39], v[16:19], a[144:151], v200\n" \
"v_smfmac_f32_16x16x64_bf16 v[40:43], v[16:19], a[200:207], v200\n" \
"v_smfmac_f32_16x16x64_bf16 v[28:31], v[20:23], a[40:47], v200\n" \
"v_smfmac_f32_16x16x64_bf16 v[32:35], v[20:23], a[96:103], v200\n" \
"v_smfmac_f32_16x16x64_bf16 v[36:39], v[20:23], a[152:159], v200\n" \
"v_smfmac_f32_16x16x64_bf16 v[40:43], v[20:23], a[208:215], v200\n" \
"v_smfmac_f32_16x16x64_bf16 v[28:31], v[24:27], a[48:55], v200\n" \
"v_smfmac_f32_16x16x64_bf16 v[32:35], v[24:27], a[104:111], v200\n" \
"v_smfmac_f32_16x16x64_bf16 v[36:39], v[24:27], a[160:167], v200\n" \
"v_smfmac_f32_16x16x64_bf16 v[40:43], v[24:27], a[216:223], v200\n" \
"s_nop 7\n" \
"v_permlane32_swap_b32 v28, v32\n" \
"v_permlane32_swap_b32 v29, v33\n" \
"v_permlane32_swap_b32 v30, v34\n" \
"v_permlane32_swap_b32 v31, v35\n" \
"v_add_f32 v28, v28, v32\n" \
"v_add_f32 v29, v29, v33\n" \
"v_add_f32 v30, v30, v34\n" \
"v_add_f32 v31, v31, v35\n" \
"v_cvt_pk_bf16_f32 v68, v28, v29\n" \
"v_cvt_pk_bf16_f32 v69, v30, v31\n" \
"v_permlane32_swap_b32 v36, v40\n" \
"v_permlane32_swap_b32 v37, v41\n" \
"v_permlane32_swap_b32 v38, v42\n" \
"v_permlane32_swap_b32 v39, v43\n" \
"v_add_f32 v36, v36, v40\n" \
"v_add_f32 v37, v37, v41\n" \
"v_add_f32 v38, v38, v42\n" \
"v_add_f32 v39, v39, v43\n" \
"v_cvt_pk_bf16_f32 v70, v36, v37\n" \
"v_cvt_pk_bf16_f32 v71, v38, v39\n" \
"s_mov_b32 s50, 262144\n" \
"buffer_store_dwordx4 v[68:71], v218, s[12:15], s50 offen sc1\n" \
"s_cmp_eq_u32 s40, 0\n" \
"s_cbranch_scc1 .LP0_wet\n" \
".LP0_end:\n" \
"s_endpgm\n" \
".p2align 6\n" \
"fk_role_P1:\n" \
"ds_read_b32 v200, v255 offset:0\n" \
"ds_read_b32 v201, v255 offset:256\n" \
"ds_read_b32 v202, v255 offset:512\n" \
"ds_read_b32 v203, v255 offset:768\n" \
"ds_read_b32 v204, v255 offset:1024\n" \
"ds_read_b32 v205, v255 offset:1280\n" \
"ds_read_b32 v206, v255 offset:1536\n" \
"ds_read_b32 v207, v255 offset:1792\n" \
"ds_read_b32 v208, v255 offset:2048\n" \
"ds_read_b32 v209, v255 offset:2304\n" \
"ds_read_b32 v210, v255 offset:2560\n" \
"ds_read_b32 v211, v255 offset:2816\n" \
"ds_read_b32 v212, v255 offset:3072\n" \
"ds_read_b32 v213, v255 offset:3328\n" \
"ds_read_b32 v214, v255 offset:3584\n" \
"ds_read_b32 v215, v255 offset:3840\n" \
"ds_read_b32 v216, v255 offset:4096\n" \
"ds_read_b32 v217, v255 offset:4352\n" \
"ds_read_b32 v218, v255 offset:4608\n" \
"ds_read_b32 v219, v255 offset:4864\n" \
"ds_read_b32 v220, v255 offset:5120\n" \
"ds_read_b32 v221, v255 offset:5376\n" \
"ds_read_b32 v222, v255 offset:5632\n" \
"ds_read_b32 v223, v255 offset:5888\n" \
"ds_read_b32 v254, v255 offset:7936\n" \
"s_waitcnt lgkmcnt(0)\n" \
"v_readlane_b32 s4, v254, 0\n" \
"v_readlane_b32 s5, v254, 1\n" \
"v_readlane_b32 s6, v254, 2\n" \
"v_readlane_b32 s7, v254, 3\n" \
"v_readlane_b32 s8, v254, 4\n" \
"v_readlane_b32 s9, v254, 5\n" \
"v_readlane_b32 s10, v254, 6\n" \
"v_readlane_b32 s11, v254, 7\n" \
"v_readlane_b32 s12, v254, 8\n" \
"v_readlane_b32 s13, v254, 9\n" \
"v_readlane_b32 s14, v254, 10\n" \
"v_readlane_b32 s15, v254, 11\n" \
"v_readlane_b32 s16, v254, 12\n" \
"v_readlane_b32 s17, v254, 13\n" \
"v_readlane_b32 s18, v254, 14\n" \
"v_readlane_b32 s19, v254, 15\n" \
"v_readlane_b32 s20, v254, 16\n" \
"v_readlane_b32 s21, v254, 17\n" \
"v_readlane_b32 s22, v254, 18\n" \
"v_readlane_b32 s23, v254, 19\n" \
"v_readlane_b32 s24, v254, 20\n" \
"v_readlane_b32 s25, v254, 21\n" \
"v_readlane_b32 s26, v254, 22\n" \
"v_readlane_b32 s27, v254, 23\n" \
"v_readlane_b32 s28, v254, 24\n" \
"v_readlane_b32 s29, v254, 25\n" \
"v_readlane_b32 s30, v254, 26\n" \
"v_readlane_b32 s31, v254, 27\n" \
"v_readlane_b32 s32, v254, 28\n" \
"v_readlane_b32 s33, v254, 29\n" \
"v_readlane_b32 s34, v254, 30\n" \
"v_readlane_b32 s35, v254, 31\n" \
"v_readlane_b32 s36, v254, 32\n" \
"v_readlane_b32 s37, v254, 33\n" \
"v_readlane_b32 s38, v254, 34\n" \
"v_readlane_b32 s39, v254, 35\n" \
"s_mov_b32 s40, 0\n" \
"s_mov_b32 s51, 0\n" \
"s_nop 0\n" \
"v_mov_b32 v252, s38\n" \
"s_nop 4\n" \
"v_mov_b32 v112, -1\n" \
"v_mov_b32 v113, -1\n" \
"v_mov_b32 v114, -1\n" \
"v_mov_b32 v115, -1\n" \
"v_mov_b32 v109, 0x7fc00001\n" \
"s_mov_b32 s52, 0x8000\n" \
"s_mov_b32 s53, 0x4000\n" \
"s_mov_b32 s54, 0xc000\n" \
"v_writelane_b32 v251, s6, 0\n" \
"v_writelane_b32 v251, s10, 1\n" \
"v_writelane_b32 v251, s14, 2\n" \
"v_writelane_b32 v251, s18, 3\n" \
"s_mov_b32 s6, 0\n" \
"s_mov_b32 s10, 0\n" \
"s_mov_b32 s14, 0\n" \
"s_mov_b32 s18, 0\n" \
"s_mov_b32 s40, -1\n" \
"s_branch .LP1_pre\n" \
".LP1_wet:\n" \
"s_waitcnt vmcnt(0)\n" \
"v_readlane_b32 s6, v251, 0\n" \
"v_readlane_b32 s10, v251, 1\n" \
"v_readlane_b32 s14, v251, 2\n" \
"v_readlane_b32 s18, v251, 3\n" \
"s_nop 4\n" \
"ds_read_b32 v230, v255 offset:6912\n" \
"v_readlane_b32 s60, v254, 12\n" \
"v_readlane_b32 s61, v254, 13\n" \
"v_readlane_b32 s62, v254, 14\n" \
"v_readlane_b32 s63, v254, 15\n" \
"s_waitcnt lgkmcnt(0)\n" \
"s_nop 3\n" \
"buffer_load_dword v228, v230, s[60:63], 0 offen\n" \
"s_mov_b32 s33, 36\n" \
"s_mov_b32 s34, 0\n" \
"s_add_u32 s43, s30, 1\n" \
"s_mov_b32 s50, 0\n" \
".LP1_w29l:\n" \
"buffer_load_dword v240, v204, s[4:7], 0 offen sc1\n" \
"s_waitcnt vmcnt(0)\n" \
".LP1_w29c:\n" \
"v_subrev_u32 v241, s43, v240\n" \
"v_cmp_gt_u32 vcc, 0x100, v241\n" \
"s_cmp_eq_u64 vcc, -1\n" \
"s_cbranch_scc1 .LP1_w29d\n" \
"s_sleep 2\n" \
"s_add_u32 s50, s50, 1\n" \
"s_cmp_lt_u32 s50, 0x20000\n" \
"s_cbranch_scc1 .LP1_w29l\n" \
"s_branch .LP1_end\n" \
".LP1_w29d:\n" \
"v_readlane_b32 s43, v202, 4\n" \
"s_nop 4\n" \
"buffer_load_dwordx4 a[32:35], v201, s[8:11], s43 offen sc1\n" \
"buffer_load_dwordx4 a[36:39], v201, s[8:11], s43 offen offset:1024 sc1\n" \
"v_readlane_b32 s43, v202, 11\n" \
"s_nop 4\n" \
"buffer_load_dwordx4 a[88:91], v201, s[8:11], s43 offen sc1\n" \
"buffer_load_dwordx4 a[92:95], v201, s[8:11], s43 offen offset:1024 sc1\n" \
"v_readlane_b32 s43, v202, 18\n" \
"s_nop 4\n" \
"buffer_load_dwordx4 a[144:147], v201, s[8:11], s43 offen sc1\n" \
"buffer_load_dwordx4 a[148:151], v201, s[8:11], s43 offen offset:1024 sc1\n" \
"v_readlane_b32 s43, v202, 25\n" \
"s_nop 4\n" \
"buffer_load_dwordx4 a[200:203], v201, s[8:11], s43 offen sc1\n" \
"buffer_load_dwordx4 a[204:207], v201, s[8:11], s43 offen offset:1024 sc1\n" \
"s_add_u32 s43, s30, 2\n" \
"s_mov_b32 s50, 0\n" \
"s_branch .LP1_w30c\n" \
".LP1_w30l:\n" \
"buffer_load_dword v240, v204, s[4:7], 0 offen sc1\n" \
"s_waitcnt vmcnt(0)\n" \
".LP1_w30c:\n" \
"v_subrev_u32 v241, s43, v240\n" \
"v_cmp_gt_u32 vcc, 0x100, v241\n" \
"s_cmp_eq_u64 vcc, -1\n" \
"s_cbranch_scc1 .LP1_w30d\n" \
"s_sleep 2\n" \
"s_add_u32 s50, s50, 1\n" \
"s_cmp_lt_u32 s50, 0x20000\n" \
"s_cbranch_scc1 .LP1_w30l\n" \
"s_branch .LP1_end\n" \
".LP1_w30d:\n" \
"v_readlane_b32 s43, v202, 1\n" \
"s_nop 4\n" \
"buffer_load_dwordx4 a[8:11], v201, s[8:11], s43 offen sc1\n" \
"buffer_load_dwordx4 a[12:15], v201, s[8:11], s43 offen offset:1024 sc1\n" \
"v_readlane_b32 s43, v202, 8\n" \
"s_nop 4\n" \
"buffer_load_dwordx4 a[64:67], v201, s[8:11], s43 offen sc1\n" \
"buffer_load_dwordx4 a[68:71], v201, s[8:11], s43 offen offset:1024 sc1\n" \
"v_readlane_b32 s43, v202, 15\n" \
"s_nop 4\n" \
"buffer_load_dwordx4 a[120:123], v201, s[8:11], s43 offen sc1\n" \
"buffer_load_dwordx4 a[124:127], v201, s[8:11], s43 offen offset:1024 sc1\n" \
"v_readlane_b32 s43, v202, 22\n" \
"s_nop 4\n" \
"buffer_load_dwordx4 a[176:179], v201, s[8:11], s43 offen sc1\n" \
"buffer_load_dwordx4 a[180:183], v201, s[8:11], s43 offen offset:1024 sc1\n" \
"s_add_u32 s43, s30, 3\n" \
"s_mov_b32 s50, 0\n" \
"s_branch .LP1_w31c\n" \
".LP1_w31l:\n" \
"buffer_load_dword v240, v204, s[4:7], 0 offen sc1\n" \
"s_waitcnt vmcnt(0)\n" \
".LP1_w31c:\n" \
"v_subrev_u32 v241, s43, v240\n" \
"v_cmp_gt_u32 vcc, 0x100, v241\n" \
"s_cmp_eq_u64 vcc, -1\n" \
"s_cbranch_scc1 .LP1_w31d\n" \
"s_sleep 2\n" \
"s_add_u32 s50, s50, 1\n" \
"s_cmp_lt_u32 s50, 0x20000\n" \
"s_cbranch_scc1 .LP1_w31l\n" \
"s_branch .LP1_end\n" \
".LP1_w31d:\n" \
"v_readlane_b32 s43, v202, 5\n" \
"s_nop 4\n" \
"buffer_load_dwordx4 a[40:43], v201, s[8:11], s43 offen sc1\n" \
"buffer_load_dwordx4 a[44:47], v201, s[8:11], s43 offen offset:1024 sc1\n" \
"v_readlane_b32 s43, v202, 12\n" \
"s_nop 4\n" \
"buffer_load_dwordx4 a[96:99], v201, s[8:11], s43 offen sc1\n" \
"buffer_load_dwordx4 a[100:103], v201, s[8:11], s43 offen offset:1024 sc1\n" \
"v_readlane_b32 s43, v202, 19\n" \
"s_nop 4\n" \
"buffer_load_dwordx4 a[152:155], v201, s[8:11], s43 offen sc1\n" \
"buffer_load_dwordx4 a[156:159], v201, s[8:11], s43 offen offset:1024 sc1\n" \
"v_readlane_b32 s43, v202, 26\n" \
"s_nop 4\n" \
"buffer_load_dwordx4 a[208:211], v201, s[8:11], s43 offen sc1\n" \
"buffer_load_dwordx4 a[212:215], v201, s[8:11], s43 offen offset:1024 sc1\n" \
"s_add_u32 s43, s30, 4\n" \
"s_mov_b32 s50, 0\n" \
"s_branch .LP1_w32c\n" \
".LP1_w32l:\n" \
"buffer_load_dword v240, v204, s[4:7], 0 offen sc1\n" \
"s_waitcnt vmcnt(0)\n" \
".LP1_w32c:\n" \
"v_subrev_u32 v241, s43, v240\n" \
"v_cmp_gt_u32 vcc, 0x100, v241\n" \
"s_cmp_eq_u64 vcc, -1\n" \
"s_cbranch_scc1 .LP1_w32d\n" \
"s_sleep 2\n" \
"s_add_u32 s50, s50, 1\n" \
"s_cmp_lt_u32 s50, 0x20000\n" \
"s_cbranch_scc1 .LP1_w32l\n" \
"s_branch .LP1_end\n" \
".LP1_w32d:\n" \
"v_readlane_b32 s43, v202, 2\n" \
"s_nop 4\n" \
"buffer_load_dwordx4 a[16:19], v201, s[8:11], s43 offen sc1\n" \
"buffer_load_dwordx4 a[20:23], v201, s[8:11], s43 offen offset:1024 sc1\n" \
"v_readlane_b32 s43, v202, 9\n" \
"s_nop 4\n" \
"buffer_load_dwordx4 a[72:75], v201, s[8:11], s43 offen sc1\n" \
"buffer_load_dwordx4 a[76:79], v201, s[8:11], s43 offen offset:1024 sc1\n" \
"v_readlane_b32 s43, v202, 16\n" \
"s_nop 4\n" \
"buffer_load_dwordx4 a[128:131], v201, s[8:11], s43 offen sc1\n" \
"buffer_load_dwordx4 a[132:135], v201, s[8:11], s43 offen offset:1024 sc1\n" \
"v_readlane_b32 s43, v202, 23\n" \
"s_nop 4\n" \
"buffer_load_dwordx4 a[184:187], v201, s[8:11], s43 offen sc1\n" \
"buffer_load_dwordx4 a[188:191], v201, s[8:11], s43 offen offset:1024 sc1\n" \
"s_add_u32 s43, s30, 5\n" \
"s_mov_b32 s50, 0\n" \
"s_branch .LP1_w33c\n" \
".LP1_w33l:\n" \
"buffer_load_dword v240, v204, s[4:7], 0 offen sc1\n" \
"s_waitcnt vmcnt(0)\n" \
".LP1_w33c:\n" \
"v_subrev_u32 v241, s43, v240\n" \
"v_cmp_gt_u32 vcc, 0x100, v241\n" \
"s_cmp_eq_u64 vcc, -1\n" \
"s_cbranch_scc1 .LP1_w33d\n" \
"s_sleep 2\n" \
"s_add_u32 s50, s50, 1\n" \
"s_cmp_lt_u32 s50, 0x20000\n" \
"s_cbranch_scc1 .LP1_w33l\n" \
"s_branch .LP1_end\n" \
".LP1_w33d:\n" \
"v_readlane_b32 s43, v202, 0\n" \
"s_nop 4\n" \
"buffer_load_dwordx4 a[0:3], v201, s[8:11], s43 offen sc1\n" \
"buffer_load_dwordx4 a[4:7], v201, s[8:11], s43 offen offset:1024 sc1\n" \
"v_readlane_b32 s43, v202, 7\n" \
"s_nop 4\n" \
"buffer_load_dwordx4 a[56:59], v201, s[8:11], s43 offen sc1\n" \
"buffer_load_dwordx4 a[60:63], v201, s[8:11], s43 offen offset:1024 sc1\n" \
"v_readlane_b32 s43, v202, 14\n" \
"s_nop 4\n" \
"buffer_load_dwordx4 a[112:115], v201, s[8:11], s43 offen sc1\n" \
"buffer_load_dwordx4 a[116:119], v201, s[8:11], s43 offen offset:1024 sc1\n" \
"v_readlane_b32 s43, v202, 21\n" \
"s_nop 4\n" \
"buffer_load_dwordx4 a[168:171], v201, s[8:11], s43 offen sc1\n" \
"buffer_load_dwordx4 a[172:175], v201, s[8:11], s43 offen offset:1024 sc1\n" \
"s_add_u32 s43, s30, 6\n" \
"s_mov_b32 s50, 0\n" \
"s_branch .LP1_w34c\n" \
".LP1_w34l:\n" \
"buffer_load_dword v240, v204, s[4:7], 0 offen sc1\n" \
"s_waitcnt vmcnt(0)\n" \
".LP1_w34c:\n" \
"v_subrev_u32 v241, s43, v240\n" \
"v_cmp_gt_u32 vcc, 0x100, v241\n" \
"s_cmp_eq_u64 vcc, -1\n" \
"s_cbranch_scc1 .LP1_w34d\n" \
"s_sleep 2\n" \
"s_add_u32 s50, s50, 1\n" \
"s_cmp_lt_u32 s50, 0x20000\n" \
"s_cbranch_scc1 .LP1_w34l\n" \
"s_branch .LP1_end\n" \
".LP1_w34d:\n" \
"v_readlane_b32 s43, v202, 3\n" \
"s_nop 4\n" \
"buffer_load_dwordx4 a[24:27], v201, s[8:11], s43 offen sc1\n" \
"buffer_load_dwordx4 a[28:31], v201, s[8:11], s43 offen offset:1024 sc1\n" \
"v_readlane_b32 s43, v202, 10\n" \
"s_nop 4\n" \
"buffer_load_dwordx4 a[80:83], v201, s[8:11], s43 offen sc1\n" \
"buffer_load_dwordx4 a[84:87], v201, s[8:11], s43 offen offset:1024 sc1\n" \
"v_readlane_b32 s43, v202, 17\n" \
"s_nop 4\n" \
"buffer_load_dwordx4 a[136:139], v201, s[8:11], s43 offen sc1\n" \
"buffer_load_dwordx4 a[140:143], v201, s[8:11], s43 offen offset:1024 sc1\n" \
"v_readlane_b32 s43, v202, 24\n" \
"s_nop 4\n" \
"buffer_load_dwordx4 a[192:195], v201, s[8:11], s43 offen sc1\n" \
"buffer_load_dwordx4 a[196:199], v201, s[8:11], s43 offen offset:1024 sc1\n" \
"s_add_u32 s43, s30, 7\n" \
"s_mov_b32 s50, 0\n" \
"s_branch .LP1_w35c\n" \
".LP1_w35l:\n" \
"buffer_load_dword v240, v204, s[4:7], 0 offen sc1\n" \
"s_waitcnt vmcnt(0)\n" \
".LP1_w35c:\n" \
"v_subrev_u32 v241, s43, v240\n" \
"v_cmp_gt_u32 vcc, 0x100, v241\n" \
"s_cmp_eq_u64 vcc, -1\n" \
"s_cbranch_scc1 .LP1_w35d\n" \
"s_sleep 2\n" \
"s_add_u32 s50, s50, 1\n" \
"s_cmp_lt_u32 s50, 0x20000\n" \
"s_cbranch_scc1 .LP1_w35l\n" \
"s_branch .LP1_end\n" \
".LP1_w35d:\n" \
"v_readlane_b32 s43, v202, 6\n" \
"s_nop 4\n" \
"buffer_load_dwordx4 a[48:51], v201, s[8:11], s43 offen sc1\n" \
"buffer_load_dwordx4 a[52:55], v201, s[8:11], s43 offen offset:1024 sc1\n" \
"v_readlane_b32 s43, v202, 13\n" \
"s_nop 4\n" \
"buffer_load_dwordx4 a[104:107], v201, s[8:11], s43 offen sc1\n" \
"buffer_load_dwordx4 a[108:111], v201, s[8:11], s43 offen offset:1024 sc1\n" \
"v_readlane_b32 s43, v202, 20\n" \
"s_nop 4\n" \
"buffer_load_dwordx4 a[160:163], v201, s[8:11], s43 offen sc1\n" \
"buffer_load_dwordx4 a[164:167], v201, s[8:11], s43 offen offset:1024 sc1\n" \
"v_readlane_b32 s43, v202, 27\n" \
"s_nop 4\n" \
"buffer_load_dwordx4 a[216:219], v201, s[8:11], s43 offen sc1\n" \
"buffer_load_dwordx4 a[220:223], v201, s[8:11], s43 offen offset:1024 sc1\n" \
"s_add_u32 s43, s30, 1\n" \
"s_mov_b32 s50, 0\n" \
".LP1_w36l:\n" \
"buffer_load_dword v240, v205, s[4:7], 0 offen sc1\n" \
"s_waitcnt vmcnt(0)\n" \
".LP1_w36c:\n" \
"v_subrev_u32 v241, s43, v240\n" \
"v_cmp_gt_u32 vcc, 0x100, v241\n" \
"s_cmp_eq_u64 vcc, -1\n" \
"s_cbranch_scc1 .LP1_w36d\n" \
"s_sleep 2\n" \
"s_add_u32 s50, s50, 1\n" \
"s_cmp_lt_u32 s50, 0x20000\n" \
"s_cbranch_scc1 .LP1_w36l\n" \
"s_branch .LP1_end\n" \
".LP1_w36d:\n" \
"v_readfirstlane_b32 s31, v241\n" \
"s_lshr_b32 s32, s31, 2\n" \
"s_xor_b32 s29, s31, s32\n" \
"s_and_b32 s29, s29, 3\n" \
"s_lshl_b32 s29, s29, 13\n" \
"s_bitset1_b32 s29, 21\n" \
"s_and_b32 s31, s31, 3\n" \
"s_lshl_b32 s31, s31, 13\n" \
"s_bitcmp1_b32 s39, 0\n" \
"s_cselect_b32 s43, s29, 0\n" \
"s_xor_b32 s31, s31, s43\n" \
"s_xor_b32 s32, s31, 0x4000\n" \
"s_mov_b32 s39, 0\n" \
"s_add_u32 s43, s40, 0\n" \
"s_and_b32 s43, s43, 3\n" \
"s_bitcmp1_b32 s43, 0\n" \
"s_cselect_b32 s47, s32, s31\n" \
"s_lshl_b32 s43, s43, 19\n" \
"s_add_u32 s47, s47, s43\n" \
".LP1_pre:\n" \
"v_mov_b32 v236, 1290\n" \
"v_mov_b32 v237, 1040\n" \
"v_mov_b32 v238, 1290\n" \
"v_mov_b32 v239, 1040\n" \
"s_cmp_eq_u32 s40, 0\n" \
"s_cselect_b32 s43, -1, 0\n" \
"v_and_b32 v236, s43, v236\n" \
"v_and_b32 v237, s43, v237\n" \
"v_and_b32 v238, s43, v238\n" \
"v_and_b32 v239, s43, v239\n" \
"s_barrier\n" \
"ds_read_b96 v[248:250], v252\n" \
"s_waitcnt lgkmcnt(0)\n" \
"v_readfirstlane_b32 s44, v248\n" \
"v_readfirstlane_b32 s45, v249\n" \
"v_readfirstlane_b32 s46, v250\n" \
"s_mul_i32 s42, s25, s46\n" \
"s_lshr_b32 s42, s42, 12\n" \
".LP1_cu:\n" \
"s_sub_u32 s43, s24, s44\n" \
"s_cmp_lt_i32 s43, 0\n" \
"s_cbranch_scc0 .LP1_cud\n" \
"s_add_u32 s24, s24, s25\n" \
"s_xor_b32 s31, s31, s29\n" \
"s_xor_b32 s32, s32, s29\n" \
"s_xor_b32 s47, s47, s29\n" \
"s_add_u32 s39, s39, 1\n" \
"s_branch .LP1_cu\n" \
".LP1_cud:\n" \
"s_mov_b32 s3, s46\n" \
"s_mov_b32 s2, s46\n" \
"s_add_u32 s43, s40, 1\n" \
"s_and_b32 s43, s43, 3\n" \
"s_bitcmp1_b32 s43, 0\n" \
"s_cselect_b32 s48, s32, s31\n" \
"s_lshl_b32 s43, s43, 19\n" \
"s_add_u32 s48, s48, s43\n" \
"s_add_u32 s43, s40, 2\n" \
"s_and_b32 s43, s43, 3\n" \
"s_bitcmp1_b32 s43, 0\n" \
"s_cselect_b32 s49, s32, s31\n" \
"s_lshl_b32 s43, s43, 19\n" \
"s_add_u32 s49, s49, s43\n" \
"s_sub_u32 s43, s24, s44\n" \
"s_add_u32 s43, s43, s28\n" \
"s_max_i32 s43, s43, 0xfffd0000\n" \
"s_min_i32 s43, s43, 0x4000\n" \
"s_mul_i32 s43, s43, s46\n" \
"s_ashr_i32 s43, s43, 12\n" \
"s_add_u32 s41, s45, s43\n" \
"s_lshr_b32 s35, s42, 1\n" \
"s_sub_u32 s35, 0, s35\n" \
"v_max_u32 v236, v236, v238\n" \
"v_max_u32 v237, v237, v239\n" \
"v_and_b32 v236, 0xfff, v236\n" \
"v_and_b32 v237, 0xfff, v237\n" \
"s_mov_b64 exec, 1\n" \
"ds_write_b64 v252, v[236:237] offset:24\n" \
"s_mov_b64 exec, -1\n" \
".LP1_loop:\n" \
"s_branch .LP1_m1\n" \
".LP1_retry1:\n" \
"s_add_u32 s51, s51, 1\n" \
"s_cmp_gt_u32 s51, 0x20000\n" \
"s_cbranch_scc1 .LP1_end\n" \
".LP1_m0:\n" \
"s_add_u32 s41, s41, s42\n" \
"s_add_u32 s24, s24, s25\n" \
"s_xor_b32 s31, s31, s29\n" \
"s_xor_b32 s32, s32, s29\n" \
"s_xor_b32 s47, s47, s29\n" \
"s_xor_b32 s48, s48, s29\n" \
"s_add_u32 s39, s39, 1\n" \
"s_bitcmp1_b32 s31, 21\n" \
"s_cselect_b32 s43, s29, 0\n" \
"v_mov_b32 v116, s43\n" \
"ds_write_b32 v252, v116 offset:16\n" \
".LP1_m1:\n" \
"s_memtime vcc\n" \
"s_waitcnt lgkmcnt(0)\n" \
"s_sub_u32 s43, s41, vcc_lo\n" \
"s_cmp_gt_i32 s43, 0\n" \
"s_cbranch_scc1 .LP1_m1\n" \
"s_cmp_lt_i32 s43, s35\n" \
"s_cbranch_scc1 .LP1_m0\n" \
"buffer_load_dwordx4 v[0:3], v208, s[4:7], s47 offen offset:2048 sc1\n" \
"buffer_load_dwordx4 v[4:7], v209, s[4:7], s47 offen sc1\n" \
"s_nop 15\n" \
"s_nop 13\n" \
"s_mul_i32 s50, s40, 4096\n" \
"buffer_store_dwordx4 v[68:71], v218, s[12:15], s50 offen sc1\n" \
"buffer_store_dword v112, v213, s[4:7], s49 offen sc1\n" \
"s_xor_b32 s43, s49, s29\n" \
"buffer_store_dword v112, v213, s[4:7], s43 offen sc1\n" \
"s_lshl_b32 s43, s40, 7\n" \
"buffer_load_dwordx4 v[236:239], v207, s[4:7], s43 offen offset:128 sc1\n" \
"v_mov_b32 v28, 0\n" \
"v_mov_b32 v29, 0\n" \
"v_mov_b32 v30, 0\n" \
"v_mov_b32 v31, 0\n" \
"v_mov_b32 v32, 0\n" \
"v_mov_b32 v33, 0\n" \
"v_mov_b32 v34, 0\n" \
"v_mov_b32 v35, 0\n" \
"v_mov_b32 v36, 0\n" \
"v_mov_b32 v37, 0\n" \
"v_mov_b32 v38, 0\n" \
"v_mov_b32 v39, 0\n" \
"v_mov_b32 v40, 0\n" \
"v_mov_b32 v41, 0\n" \
"v_mov_b32 v42, 0\n" \
"v_mov_b32 v43, 0\n" \
"s_waitcnt vmcnt(5)\n" \
"ds_write_b128 v220, v[0:3] offset:2048\n" \
"s_waitcnt vmcnt(4)\n" \
"ds_write_b128 v220, v[4:7] offset:4096\n" \
"v_max3_u32 v44, v0, v0, v1\n" \
"v_max3_u32 v44, v44, v2, v3\n" \
"v_max3_u32 v44, v44, v4, v5\n" \
"v_max3_u32 v44, v44, v6, v7\n" \
"v_cmp_eq_u32 vcc, -1, v44\n" \
"v_smfmac_f32_16x16x64_bf16 v[28:31], v[0:3], a[0:7], v200\n" \
"v_smfmac_f32_16x16x64_bf16 v[32:35], v[0:3], a[56:63], v200\n" \
"s_cbranch_vccnz .LP1_retry1\n" \
"s_waitcnt lgkmcnt(0)\n" \
"s_barrier\n" \
"ds_read_b128 v[8:11], v220 offset:5120\n" \
"ds_read_b128 v[12:15], v220 offset:6144\n" \
"ds_read_b128 v[16:19], v220 offset:0\n" \
"ds_read_b128 v[20:23], v220 offset:1024\n" \
"ds_read_b128 v[24:27], v220 offset:3072\n" \
"ds_read_b64 v[246:247], v252 offset:24\n" \
"ds_read_b96 v[248:250], v252\n" \
"v_smfmac_f32_16x16x64_bf16 v[36:39], v[0:3], a[112:119], v200\n" \
"v_smfmac_f32_16x16x64_bf16 v[40:43], v[0:3], a[168:175], v200\n" \
"v_smfmac_f32_16x16x64_bf16 v[28:31], v[4:7], a[8:15], v200\n" \
"v_smfmac_f32_16x16x64_bf16 v[32:35], v[4:7], a[64:71], v200\n" \
"v_smfmac_f32_16x16x64_bf16 v[36:39], v[4:7], a[120:127], v200\n" \
"v_smfmac_f32_16x16x64_bf16 v[40:43], v[4:7], a[176:183], v200\n" \
"s_waitcnt lgkmcnt(6)\n" \
"v_smfmac_f32_16x16x64_bf16 v[28:31], v[8:11], a[16:23], v200\n" \
"v_smfmac_f32_16x16x64_bf16 v[32:35], v[8:11], a[72:79], v200\n" \
"v_smfmac_f32_16x16x64_bf16 v[36:39], v[8:11], a[128:135], v200\n" \
"v_smfmac_f32_16x16x64_bf16 v[40:43], v[8:11], a[184:191], v200\n" \
"s_waitcnt lgkmcnt(0)\n" \
"v_readfirstlane_b32 s44, v248\n" \
"v_readfirstlane_b32 s45, v249\n" \
"v_smfmac_f32_16x16x64_bf16 v[28:31], v[12:15], a[24:31], v200\n" \
"v_readfirstlane_b32 s46, v250\n" \
"v_readfirstlane_b32 s26, v246\n" \
"v_readfirstlane_b32 s27, v247\n" \
"v_smfmac_f32_16x16x64_bf16 v[32:35], v[12:15], a[80:87], v200\n" \
"s_mul_i32 s42, s25, s46\n" \
"s_lshr_b32 s42, s42, 12\n" \
"s_add_u32 s24, s24, s26\n" \
"v_smfmac_f32_16x16x64_bf16 v[36:39], v[12:15], a[136:143], v200\n" \
"s_sub_u32 s43, s24, s44\n" \
"s_max_i32 s43, s43, 0xfffd0000\n" \
"s_min_i32 s43, s43, 0x4000\n" \
"v_smfmac_f32_16x16x64_bf16 v[40:43], v[12:15], a[192:199], v200\n" \
"s_mul_i32 s43, s43, s46\n" \
"s_ashr_i32 s43, s43, 12\n" \
"s_add_u32 s41, s45, s43\n" \
"s_cmp_eq_u32 s40, 63\n" \
"s_cbranch_scc1 .LP1_last\n" \
"s_branch .LP1_z1\n" \
".LP1_z0:\n" \
"s_add_u32 s41, s41, s42\n" \
"s_add_u32 s24, s24, s25\n" \
"s_xor_b32 s31, s31, s29\n" \
"s_xor_b32 s32, s32, s29\n" \
"s_xor_b32 s47, s47, s29\n" \
"s_xor_b32 s48, s48, s29\n" \
"s_add_u32 s39, s39, 1\n" \
"s_bitcmp1_b32 s31, 21\n" \
"s_cselect_b32 s43, s29, 0\n" \
"v_mov_b32 v116, s43\n" \
"ds_write_b32 v252, v116 offset:16\n" \
".LP1_z1:\n" \
"s_memtime vcc\n" \
"s_waitcnt lgkmcnt(0)\n" \
"s_sub_u32 s43, s41, vcc_lo\n" \
"s_cmp_gt_i32 s43, 160\n" \
"s_cbranch_scc1 .LP1_z1\n" \
"s_cmp_lt_i32 s43, s37\n" \
"s_cbranch_scc1 .LP1_z0\n" \
"buffer_load_dwordx2 v[64:65], v211, s[4:7], s47 offen sc1\n" \
".p2align 6\n" \
".LP1_b1:\n" \
"s_memtime vcc\n" \
"s_waitcnt lgkmcnt(0)\n" \
"s_sub_u32 s43, s41, vcc_lo\n" \
"s_cmp_gt_i32 s43, 128\n" \
"s_cbranch_scc1 .LP1_b1\n" \
"s_cmp_lt_i32 s43, s37\n" \
"s_cbranch_scc1 .LP1_z0\n" \
"s_max_i32 s43, s43, 0\n" \
"s_and_b32 s43, s43, -4\n" \
"s_getpc_b64 vcc\n" \
".LP1_b3:\n" \
"s_sub_u32 s43, .LP1_b4-.LP1_b3, s43\n" \
"s_add_u32 vcc_lo, vcc_lo, s43\n" \
"s_addc_u32 vcc_hi, vcc_hi, 0\n" \
"s_setpc_b64 vcc\n" \
".rept 32\n" \
"s_nop 0\n" \
".endr\n" \
".LP1_b4:\n" \
"buffer_load_dwordx4 v[56:59], v210, s[4:7], s47 offen sc1\n" \
"buffer_load_dwordx4 v[60:63], v210, s[4:7], s47 offen offset:256 sc1\n" \
"v_smfmac_f32_16x16x64_bf16 v[28:31], v[16:19], a[32:39], v200\n" \
"v_smfmac_f32_16x16x64_bf16 v[32:35], v[16:19], a[88:95], v200\n" \
"v_smfmac_f32_16x16x64_bf16 v[36:39], v[16:19], a[144:151], v200\n" \
"v_smfmac_f32_16x16x64_bf16 v[40:43], v[16:19], a[200:207], v200\n" \
"buffer_load_dwordx4 v[50:53], v212, s[4:7], s47 offen sc1\n" \
"v_smfmac_f32_16x16x64_bf16 v[28:31], v[20:23], a[40:47], v200\n" \
"v_smfmac_f32_16x16x64_bf16 v[32:35], v[20:23], a[96:103], v200\n" \
"v_smfmac_f32_16x16x64_bf16 v[36:39], v[20:23], a[152:159], v200\n" \
"v_smfmac_f32_16x16x64_bf16 v[40:43], v[20:23], a[208:215], v200\n" \
"v_smfmac_f32_16x16x64_bf16 v[28:31], v[24:27], a[48:55], v200\n" \
"v_smfmac_f32_16x16x64_bf16 v[32:35], v[24:27], a[104:111], v200\n" \
"v_smfmac_f32_16x16x64_bf16 v[36:39], v[24:27], a[160:167], v200\n" \
"v_smfmac_f32_16x16x64_bf16 v[40:43], v[24:27], a[216:223], v200\n" \
"s_nop 2\n" \
"v_mov_b32 v245, v245\n" \
"v_mov_b32 v245, v245\n" \
"v_mov_b32 v245, v245\n" \
"v_permlane32_swap_b32 v28, v32\n" \
"v_permlane32_swap_b32 v29, v33\n" \
"v_permlane32_swap_b32 v30, v34\n" \
"v_permlane32_swap_b32 v31, v35\n" \
"v_add_f32 v28, v28, v32\n" \
"v_add_f32 v29, v29, v33\n" \
"v_add_f32 v30, v30, v34\n" \
"v_add_f32 v31, v31, v35\n" \
"v_cvt_pk_bf16_f32 v68, v28, v29\n" \
"v_cvt_pk_bf16_f32 v69, v30, v31\n" \
"v_permlane32_swap_b32 v36, v40\n" \
"v_permlane32_swap_b32 v37, v41\n" \
"v_permlane32_swap_b32 v38, v42\n" \
"v_permlane32_swap_b32 v39, v43\n" \
"v_add_f32 v36, v36, v40\n" \
"v_add_f32 v37, v37, v41\n" \
"v_add_f32 v38, v38, v42\n" \
"v_add_f32 v39, v39, v43\n" \
"v_cvt_pk_bf16_f32 v70, v36, v37\n" \
"v_cvt_pk_bf16_f32 v71, v38, v39\n" \
"v_mov_b32 v48, v68\n" \
"v_mov_b32 v49, v69\n" \
"v_mov_b32 v54, v70\n" \
"v_mov_b32 v55, v71\n" \
"s_nop 1\n" \
"v_permlane16_swap_b32 v48, v54\n" \
"v_permlane16_swap_b32 v49, v55\n" \
"s_nop 2\n" \
"v_mov_b32 v245, v245\n" \
".LP1_wait:\n" \
"s_waitcnt vmcnt(1)\n" \
"v_pk_add_f32 v[72:73], v[56:57], v[60:61]\n" \
"v_pk_add_f32 v[74:75], v[58:59], v[62:63]\n" \
"v_max3_f32 v77, v72, v73, v64\n" \
"v_max3_f32 v77, v77, v74, v75\n" \
"s_nop 1\n" \
"v_max_f32_dpp v76, v77, v77 row_ror:8 row_mask:0xf bank_mask:0xf\n" \
"v_pk_add_f32 v[72:73], v[72:73], v[76:77] op_sel_hi:[1,0] neg_lo:[0,1] neg_hi:[0,1]\n" \
"v_pk_add_f32 v[74:75], v[74:75], v[76:77] op_sel_hi:[1,0] neg_lo:[0,1] neg_hi:[0,1]\n" \
"v_sub_f32 v78, v64, v76\n" \
"v_exp_f32 v72, v72\n" \
"v_exp_f32 v73, v73\n" \
"v_exp_f32 v74, v74\n" \
"v_exp_f32 v75, v75\n" \
"v_exp_f32 v78, v78\n" \
"v_pk_add_f32 v[80:81], v[72:73], v[74:75]\n" \
"v_fma_f32 v80, v78, v65, v80\n" \
"v_add_f32 v81, v80, v81\n" \
"s_nop 1\n" \
"v_add_f32_dpp v80, v81, v81 row_ror:8 row_mask:0xf bank_mask:0xf\n" \
"v_rcp_f32 v80, v80\n" \
"s_nop 0\n" \
"v_mul_f32 v78, v78, v80\n" \
"v_pk_mul_f32 v[72:73], v[72:73], v[80:81] op_sel_hi:[1,0]\n" \
"v_pk_mul_f32 v[74:75], v[74:75], v[80:81] op_sel_hi:[1,0]\n" \
"v_cvt_pk_bf16_f32 v78, v78, v78\n" \
"v_cvt_pk_bf16_f32 v82, v72, v73\n" \
"v_cvt_pk_bf16_f32 v83, v74, v75\n" \
"v_and_b32 v84, v78, v214\n" \
"v_and_b32 v85, v78, v215\n" \
"v_cvt_pk_bf16_f32 v86, v72, v73\n" \
"s_waitcnt vmcnt(0)\n" \
"v_mfma_f32_16x16x32_bf16 v[88:91], v[48:51], v[82:85], 0\n" \
"v_cvt_pk_bf16_f32 v87, v74, v75\n" \
"s_nop 1\n" \
"v_mfma_f32_16x16x32_bf16 v[92:95], v[52:55], v[84:87], 0\n" \
"s_nop 0\n" \
"s_nop 0\n" \
"s_nop 0\n" \
"v_mov_b32 v245, v245\n" \
"s_nop 0\n" \
"s_nop 0\n" \
"s_nop 0\n" \
"v_mov_b32 v245, v245\n" \
"v_add_f32_dpp v96, v92, v88 row_ror:8 row_mask:0xf bank_mask:0x3\n" \
"v_add_f32_dpp v97, v93, v89 row_ror:8 row_mask:0xf bank_mask:0x3\n" \
"v_add_f32_dpp v96, v90, v94 row_ror:8 row_mask:0xf bank_mask:0xc\n" \
"v_add_f32_dpp v97, v91, v95 row_ror:8 row_mask:0xf bank_mask:0xc\n" \
"ds_write_b64 v216, v[96:97]\n" \
"v_cmp_u_f32 vcc, v96, v97\n" \
"s_nop 0\n" \
"v_mov_b32 v245, v245\n" \
"s_nop 2\n" \
"v_mov_b32 v245, v245\n" \
"s_cbranch_vccnz .LP1_cls\n" \
".LP1_clsr:\n" \
"s_waitcnt lgkmcnt(0)\n" \
"s_barrier\n" \
"s_add_u32 s40, s40, 1\n" \
"s_add_u32 s24, s24, s27\n" \
"s_mov_b32 s47, s48\n" \
"s_add_u32 s43, s40, 1\n" \
"s_and_b32 s43, s43, 3\n" \
"s_bitcmp1_b32 s43, 0\n" \
"s_cselect_b32 s48, s32, s31\n" \
"s_lshl_b32 s43, s43, 19\n" \
"s_add_u32 s48, s48, s43\n" \
"s_add_u32 s43, s40, 2\n" \
"s_and_b32 s43, s43, 3\n" \
"s_bitcmp1_b32 s43, 0\n" \
"s_cselect_b32 s49, s32, s31\n" \
"s_lshl_b32 s43, s43, 19\n" \
"s_add_u32 s49, s49, s43\n" \
"s_sub_u32 s43, s24, s44\n" \
"s_add_u32 s43, s43, s28\n" \
"s_max_i32 s43, s43, 0xfffd0000\n" \
"s_min_i32 s43, s43, 0x4000\n" \
"s_mul_i32 s43, s43, s46\n" \
"s_ashr_i32 s43, s43, 12\n" \
"s_add_u32 s41, s45, s43\n" \
"s_lshr_b32 s35, s42, 1\n" \
"s_sub_u32 s35, 0, s35\n" \
"s_nop 11\n" \
"v_max_u32 v236, v236, v238\n" \
"v_max_u32 v237, v237, v239\n" \
"v_and_b32 v236, 0xfff, v236\n" \
"v_and_b32 v237, 0xfff, v237\n" \
"s_mov_b64 exec, 1\n" \
"ds_write_b64 v252, v[236:237] offset:24\n" \
"s_mov_b64 exec, -1\n" \
"s_cmp_eq_u32 s40, 0\n" \
"s_cbranch_scc1 .LP1_last\n" \
"s_branch .LP1_loop\n" \
"s_branch .LP1_y1\n" \
".LP1_retry2:\n" \
"s_add_u32 s51, s51, 1\n" \
"s_cmp_gt_u32 s51, 0x20000\n" \
"s_cbranch_scc1 .LP1_end\n" \
".LP1_y0:\n" \
"s_add_u32 s41, s41, s42\n" \
"s_add_u32 s24, s24, s25\n" \
"s_xor_b32 s31, s31, s29\n" \
"s_xor_b32 s32, s32, s29\n" \
"s_xor_b32 s47, s47, s29\n" \
"s_xor_b32 s48, s48, s29\n" \
"s_add_u32 s39, s39, 1\n" \
"buffer_store_dwordx4 v[112:115], v223, s[4:7], s48 offen sc1\n" \
"s_bitcmp1_b32 s31, 21\n" \
"s_cselect_b32 s43, s29, 0\n" \
"v_mov_b32 v116, s43\n" \
"ds_write_b32 v252, v116 offset:16\n" \
".LP1_y1:\n" \
"s_memtime vcc\n" \
"s_waitcnt lgkmcnt(0)\n" \
"s_sub_u32 s43, s41, vcc_lo\n" \
"s_cmp_gt_i32 s43, 160\n" \
"s_cbranch_scc1 .LP1_y1\n" \
"s_cmp_lt_i32 s43, s37\n" \
"s_cbranch_scc1 .LP1_y0\n" \
"buffer_load_dwordx2 v[64:65], v211, s[4:7], s47 offen sc1\n" \
".p2align 6\n" \
".LP1_c1:\n" \
"s_memtime vcc\n" \
"s_waitcnt lgkmcnt(0)\n" \
"s_sub_u32 s43, s41, vcc_lo\n" \
"s_cmp_gt_i32 s43, 128\n" \
"s_cbranch_scc1 .LP1_c1\n" \
"s_cmp_lt_i32 s43, s37\n" \
"s_cbranch_scc1 .LP1_y0\n" \
"s_max_i32 s43, s43, 0\n" \
"s_and_b32 s43, s43, -4\n" \
"s_getpc_b64 vcc\n" \
".LP1_c3:\n" \
"s_sub_u32 s43, .LP1_c4-.LP1_c3, s43\n" \
"s_add_u32 vcc_lo, vcc_lo, s43\n" \
"s_addc_u32 vcc_hi, vcc_hi, 0\n" \
"s_setpc_b64 vcc\n" \
".rept 32\n" \
"s_nop 0\n" \
".endr\n" \
".LP1_c4:\n" \
"buffer_load_dwordx4 v[56:59], v210, s[4:7], s47 offen sc1\n" \
"buffer_load_dwordx4 v[60:63], v210, s[4:7], s47 offen offset:256 sc1\n" \
"s_nop 2\n" \
"v_mov_b32 v245, v245\n" \
"s_nop 2\n" \
"v_mov_b32 v245, v245\n" \
"s_nop 2\n" \
"v_mov_b32 v245, v245\n" \
"s_nop 2\n" \
"v_mov_b32 v245, v245\n" \
"buffer_load_dwordx4 v[50:53], v212, s[4:7], s47 offen sc1\n" \
"s_nop 2\n" \
"v_mov_b32 v245, v245\n" \
"s_nop 2\n" \
"v_mov_b32 v245, v245\n" \
"s_nop 2\n" \
"v_mov_b32 v245, v245\n" \
"s_nop 2\n" \
"v_mov_b32 v245, v245\n" \
"s_nop 2\n" \
"v_mov_b32 v245, v245\n" \
"s_nop 2\n" \
"v_mov_b32 v245, v245\n" \
"s_nop 2\n" \
"v_mov_b32 v245, v245\n" \
"s_nop 2\n" \
"v_mov_b32 v245, v245\n" \
"s_nop 2\n" \
"v_mov_b32 v245, v245\n" \
"s_nop 2\n" \
"v_mov_b32 v245, v245\n" \
"s_nop 2\n" \
"v_mov_b32 v245, v245\n" \
"s_nop 2\n" \
"v_mov_b32 v245, v245\n" \
"s_nop 2\n" \
"v_mov_b32 v245, v245\n" \
"s_nop 2\n" \
"v_mov_b32 v245, v245\n" \
"s_nop 2\n" \
"v_mov_b32 v245, v245\n" \
"s_nop 2\n" \
"v_mov_b32 v245, v245\n" \
"s_nop 2\n" \
"v_mov_b32 v245, v245\n" \
"s_nop 2\n" \
"v_mov_b32 v245, v245\n" \
"s_nop 2\n" \
"v_mov_b32 v245, v245\n" \
"s_nop 2\n" \
"v_mov_b32 v245, v245\n" \
"s_branch .LP1_wait\n" \
".LP1_cls:\n" \
"s_waitcnt vmcnt(0)\n" \
"v_max3_u32 v44, v64, v64, v65\n" \
"v_max3_u32 v44, v44, v50, v51\n" \
"v_max3_u32 v44, v44, v52, v53\n" \
"v_cmp_eq_u32 vcc, -1, v44\n" \
"s_cbranch_vccnz .LP1_retry2\n" \
"v_cmp_u_f32 vcc, v56, v56\n" \
"v_cmp_ne_u32 s[56:57], v56, v109\n" \
"s_and_b64 vcc, vcc, s[56:57]\n" \
"s_cbranch_vccnz .LP1_retry2\n" \
"v_cmp_u_f32 vcc, v57, v57\n" \
"v_cmp_ne_u32 s[56:57], v57, v109\n" \
"s_and_b64 vcc, vcc, s[56:57]\n" \
"s_cbranch_vccnz .LP1_retry2\n" \
"v_cmp_u_f32 vcc, v58, v58\n" \
"v_cmp_ne_u32 s[56:57], v58, v109\n" \
"s_and_b64 vcc, vcc, s[56:57]\n" \
"s_cbranch_vccnz .LP1_retry2\n" \
"v_cmp_u_f32 vcc, v59, v59\n" \
"v_cmp_ne_u32 s[56:57], v59, v109\n" \
"s_and_b64 vcc, vcc, s[56:57]\n" \
"s_cbranch_vccnz .LP1_retry2\n" \
"v_cmp_u_f32 vcc, v60, v60\n" \
"v_cmp_ne_u32 s[56:57], v60, v109\n" \
"s_and_b64 vcc, vcc, s[56:57]\n" \
"s_cbranch_vccnz .LP1_retry2\n" \
"v_cmp_u_f32 vcc, v61, v61\n" \
"v_cmp_ne_u32 s[56:57], v61, v109\n" \
"s_and_b64 vcc, vcc, s[56:57]\n" \
"s_cbranch_vccnz .LP1_retry2\n" \
"v_cmp_u_f32 vcc, v62, v62\n" \
"v_cmp_ne_u32 s[56:57], v62, v109\n" \
"s_and_b64 vcc, vcc, s[56:57]\n" \
"s_cbranch_vccnz .LP1_retry2\n" \
"v_cmp_u_f32 vcc, v63, v63\n" \
"v_cmp_ne_u32 s[56:57], v63, v109\n" \
"s_and_b64 vcc, vcc, s[56:57]\n" \
"s_cbranch_vccnz .LP1_retry2\n" \
"s_branch .LP1_clsr\n" \
".LP1_last:\n" \
"v_smfmac_f32_16x16x64_bf16 v[28:31], v[16:19], a[32:39], v200\n" \
"v_smfmac_f32_16x16x64_bf16 v[32:35], v[16:19], a[88:95], v200\n" \
"v_smfmac_f32_16x16x64_bf16 v[36:39], v[16:19], a[144:151], v200\n" \
"v_smfmac_f32_16x16x64_bf16 v[40:43], v[16:19], a[200:207], v200\n" \
"v_smfmac_f32_16x16x64_bf16 v[28:31], v[20:23], a[40:47], v200\n" \
"v_smfmac_f32_16x16x64_bf16 v[32:35], v[20:23], a[96:103], v200\n" \
"v_smfmac_f32_16x16x64_bf16 v[36:39], v[20:23], a[152:159], v200\n" \
"v_smfmac_f32_16x16x64_bf16 v[40:43], v[20:23], a[208:215], v200\n" \
"v_smfmac_f32_16x16x64_bf16 v[28:31], v[24:27], a[48:55], v200\n" \
"v_smfmac_f32_16x16x64_bf16 v[32:35], v[24:27], a[104:111], v200\n" \
"v_smfmac_f32_16x16x64_bf16 v[36:39], v[24:27], a[160:167], v200\n" \
"v_smfmac_f32_16x16x64_bf16 v[40:43], v[24:27], a[216:223], v200\n" \
"s_nop 7\n" \
"v_permlane32_swap_b32 v28, v32\n" \
"v_permlane32_swap_b32 v29, v33\n" \
"v_permlane32_swap_b32 v30, v34\n" \
"v_permlane32_swap_b32 v31, v35\n" \
"v_add_f32 v28, v28, v32\n" \
"v_add_f32 v29, v29, v33\n" \
"v_add_f32 v30, v30, v34\n" \
"v_add_f32 v31, v31, v35\n" \
"v_cvt_pk_bf16_f32 v68, v28, v29\n" \
"v_cvt_pk_bf16_f32 v69, v30, v31\n" \
"v_permlane32_swap_b32 v36, v40\n" \
"v_permlane32_swap_b32 v37, v41\n" \
"v_permlane32_swap_b32 v38, v42\n" \
"v_permlane32_swap_b32 v39, v43\n" \
"v_add_f32 v36, v36, v40\n" \
"v_add_f32 v37, v37, v41\n" \
"v_add_f32 v38, v38, v42\n" \
"v_add_f32 v39, v39, v43\n" \
"v_cvt_pk_bf16_f32 v70, v36, v37\n" \
"v_cvt_pk_bf16_f32 v71, v38, v39\n" \
"s_mov_b32 s50, 262144\n" \
"buffer_store_dwordx4 v[68:71], v218, s[12:15], s50 offen sc1\n" \
"s_cmp_eq_u32 s40, 0\n" \
"s_cbranch_scc1 .LP1_wet\n" \
"s_add_u32 s43, s30, 1\n" \
"s_mov_b32 s50, 0\n" \
".LP1_w37l:\n" \
"buffer_load_dword v240, v206, s[4:7], 0 offen sc1\n" \
"s_waitcnt vmcnt(0)\n" \
".LP1_w37c:\n" \
"v_subrev_u32 v241, s43, v240\n" \
"v_cmp_gt_u32 vcc, 0x100, v241\n" \
"s_cmp_eq_u64 vcc, -1\n" \
"s_cbranch_scc1 .LP1_w37d\n" \
"s_sleep 2\n" \
"s_add_u32 s50, s50, 1\n" \
"s_cmp_lt_u32 s50, 0x20000\n" \
"s_cbranch_scc1 .LP1_w37l\n" \
"s_branch .LP1_end\n" \
".LP1_w37d:\n" \
"v_mov_b32 v241, s30\n" \
"buffer_store_dword v241, v222, s[4:7], 0 offen sc1\n" \
".LP1_end:\n" \
"s_endpgm\n" \
".p2align 6\n" \
"fk_role_P2:\n" \
"ds_read_b32 v200, v255 offset:0\n" \
"ds_read_b32 v201, v255 offset:256\n" \
"ds_read_b32 v202, v255 offset:512\n" \
"ds_read_b32 v203, v255 offset:768\n" \
"ds_read_b32 v204, v255 offset:1024\n" \
"ds_read_b32 v205, v255 offset:1280\n" \
"ds_read_b32 v206, v255 offset:1536\n" \
"ds_read_b32 v207, v255 offset:1792\n" \
"ds_read_b32 v208, v255 offset:2048\n" \
"ds_read_b32 v209, v255 offset:2304\n" \
"ds_read_b32 v210, v255 offset:2560\n" \
"ds_read_b32 v211, v255 offset:2816\n" \
"ds_read_b32 v212, v255 offset:3072\n" \
"ds_read_b32 v213, v255 offset:3328\n" \
"ds_read_b32 v214, v255 offset:3584\n" \
"ds_read_b32 v215, v255 offset:3840\n" \
"ds_read_b32 v216, v255 offset:4096\n" \
"ds_read_b32 v217, v255 offset:4352\n" \
"ds_read_b32 v218, v255 offset:4608\n" \
"ds_read_b32 v219, v255 offset:4864\n" \
"ds_read_b32 v220, v255 offset:5120\n" \
"ds_read_b32 v221, v255 offset:5376\n" \
"ds_read_b32 v222, v255 offset:5632\n" \
"ds_read_b32 v223, v255 offset:5888\n" \
"ds_read_b32 v254, v255 offset:7936\n" \
"s_waitcnt lgkmcnt(0)\n" \
"v_readlane_b32 s4, v254, 0\n" \
"v_readlane_b32 s5, v254, 1\n" \
"v_readlane_b32 s6, v254, 2\n" \
"v_readlane_b32 s7, v254, 3\n" \
"v_readlane_b32 s8, v254, 4\n" \
"v_readlane_b32 s9, v254, 5\n" \
"v_readlane_b32 s10, v254, 6\n" \
"v_readlane_b32 s11, v254, 7\n" \
"v_readlane_b32 s12, v254, 8\n" \
"v_readlane_b32 s13, v254, 9\n" \
"v_readlane_b32 s14, v254, 10\n" \
"v_readlane_b32 s15, v254, 11\n" \
"v_readlane_b32 s16, v254, 12\n" \
"v_readlane_b32 s17, v254, 13\n" \
"v_readlane_b32 s18, v254, 14\n" \
"v_readlane_b32 s19, v254, 15\n" \
"v_readlane_b32 s20, v254, 16\n" \
"v_readlane_b32 s21, v254, 17\n" \
"v_readlane_b32 s22, v254, 18\n" \
"v_readlane_b32 s23, v254, 19\n" \
"v_readlane_b32 s24, v254, 20\n" \
"v_readlane_b32 s25, v254, 21\n" \
"v_readlane_b32 s26, v254, 22\n" \
"v_readlane_b32 s27, v254, 23\n" \
"v_readlane_b32 s28, v254, 24\n" \
"v_readlane_b32 s29, v254, 25\n" \
"v_readlane_b32 s30, v254, 26\n" \
"v_readlane_b32 s31, v254, 27\n" \
"v_readlane_b32 s32, v254, 28\n" \
"v_readlane_b32 s33, v254, 29\n" \
"v_readlane_b32 s34, v254, 30\n" \
"v_readlane_b32 s35, v254, 31\n" \
"v_readlane_b32 s36, v254, 32\n" \
"v_readlane_b32 s37, v254, 33\n" \
"v_readlane_b32 s38, v254, 34\n" \
"v_readlane_b32 s39, v254, 35\n" \
"s_mov_b32 s40, 0\n" \
"s_mov_b32 s51, 0\n" \
"s_nop 0\n" \
"v_mov_b32 v252, s38\n" \
"s_nop 4\n" \
"v_mov_b32 v112, -1\n" \
"v_mov_b32 v113, -1\n" \
"v_mov_b32 v114, -1\n" \
"v_mov_b32 v115, -1\n" \
"v_mov_b32 v109, 0x7fc00001\n" \
"s_mov_b32 s52, 0x8000\n" \
"s_mov_b32 s53, 0x4000\n" \
"s_mov_b32 s54, 0xc000\n" \
"v_writelane_b32 v251, s6, 0\n" \
"v_writelane_b32 v251, s10, 1\n" \
"v_writelane_b32 v251, s14, 2\n" \
"v_writelane_b32 v251, s18, 3\n" \
"s_mov_b32 s6, 0\n" \
"s_mov_b32 s10, 0\n" \
"s_mov_b32 s14, 0\n" \
"s_mov_b32 s18, 0\n" \
"s_mov_b32 s40, -1\n" \
"s_branch .LP2_pre\n" \
".LP2_wet:\n" \
"s_waitcnt vmcnt(0)\n" \
"v_readlane_b32 s6, v251, 0\n" \
"v_readlane_b32 s10, v251, 1\n" \
"v_readlane_b32 s14, v251, 2\n" \
"v_readlane_b32 s18, v251, 3\n" \
"s_nop 4\n" \
"ds_read_b32 v230, v255 offset:6912\n" \
"v_readlane_b32 s60, v254, 12\n" \
"v_readlane_b32 s61, v254, 13\n" \
"v_readlane_b32 s62, v254, 14\n" \
"v_readlane_b32 s63, v254, 15\n" \
"s_waitcnt lgkmcnt(0)\n" \
"s_nop 3\n" \
"buffer_load_dword v228, v230, s[60:63], 0 offen\n" \
"s_mov_b32 s33, 36\n" \
"s_mov_b32 s34, 0\n" \
"s_add_u32 s43, s30, 1\n" \
"s_mov_b32 s50, 0\n" \
".LP2_w38l:\n" \
"buffer_load_dword v240, v204, s[4:7], 0 offen sc1\n" \
"s_waitcnt vmcnt(0)\n" \
".LP2_w38c:\n" \
"v_subrev_u32 v241, s43, v240\n" \
"v_cmp_gt_u32 vcc, 0x100, v241\n" \
"s_cmp_eq_u64 vcc, -1\n" \
"s_cbranch_scc1 .LP2_w38d\n" \
"s_sleep 2\n" \
"s_add_u32 s50, s50, 1\n" \
"s_cmp_lt_u32 s50, 0x20000\n" \
"s_cbranch_scc1 .LP2_w38l\n" \
"s_branch .LP2_end\n" \
".LP2_w38d:\n" \
"v_readlane_b32 s43, v202, 2\n" \
"s_nop 4\n" \
"buffer_load_dwordx4 a[16:19], v201, s[8:11], s43 offen sc1\n" \
"buffer_load_dwordx4 a[20:23], v201, s[8:11], s43 offen offset:1024 sc1\n" \
"v_readlane_b32 s43, v202, 9\n" \
"s_nop 4\n" \
"buffer_load_dwordx4 a[72:75], v201, s[8:11], s43 offen sc1\n" \
"buffer_load_dwordx4 a[76:79], v201, s[8:11], s43 offen offset:1024 sc1\n" \
"v_readlane_b32 s43, v202, 16\n" \
"s_nop 4\n" \
"buffer_load_dwordx4 a[128:131], v201, s[8:11], s43 offen sc1\n" \
"buffer_load_dwordx4 a[132:135], v201, s[8:11], s43 offen offset:1024 sc1\n" \
"v_readlane_b32 s43, v202, 23\n" \
"s_nop 4\n" \
"buffer_load_dwordx4 a[184:187], v201, s[8:11], s43 offen sc1\n" \
"buffer_load_dwordx4 a[188:191], v201, s[8:11], s43 offen offset:1024 sc1\n" \
"s_add_u32 s43, s30, 2\n" \
"s_mov_b32 s50, 0\n" \
"s_branch .LP2_w39c\n" \
".LP2_w39l:\n" \
"buffer_load_dword v240, v204, s[4:7], 0 offen sc1\n" \
"s_waitcnt vmcnt(0)\n" \
".LP2_w39c:\n" \
"v_subrev_u32 v241, s43, v240\n" \
"v_cmp_gt_u32 vcc, 0x100, v241\n" \
"s_cmp_eq_u64 vcc, -1\n" \
"s_cbranch_scc1 .LP2_w39d\n" \
"s_sleep 2\n" \
"s_add_u32 s50, s50, 1\n" \
"s_cmp_lt_u32 s50, 0x20000\n" \
"s_cbranch_scc1 .LP2_w39l\n" \
"s_branch .LP2_end\n" \
".LP2_w39d:\n" \
"v_readlane_b32 s43, v202, 6\n" \
"s_nop 4\n" \
"buffer_load_dwordx4 a[48:51], v201, s[8:11], s43 offen sc1\n" \
"buffer_load_dwordx4 a[52:55], v201, s[8:11], s43 offen offset:1024 sc1\n" \
"v_readlane_b32 s43, v202, 13\n" \
"s_nop 4\n" \
"buffer_load_dwordx4 a[104:107], v201, s[8:11], s43 offen sc1\n" \
"buffer_load_dwordx4 a[108:111], v201, s[8:11], s43 offen offset:1024 sc1\n" \
"v_readlane_b32 s43, v202, 20\n" \
"s_nop 4\n" \
"buffer_load_dwordx4 a[160:163], v201, s[8:11], s43 offen sc1\n" \
"buffer_load_dwordx4 a[164:167], v201, s[8:11], s43 offen offset:1024 sc1\n" \
"v_readlane_b32 s43, v202, 27\n" \
"s_nop 4\n" \
"buffer_load_dwordx4 a[216:219], v201, s[8:11], s43 offen sc1\n" \
"buffer_load_dwordx4 a[220:223], v201, s[8:11], s43 offen offset:1024 sc1\n" \
"s_add_u32 s43, s30, 3\n" \
"s_mov_b32 s50, 0\n" \
"s_branch .LP2_w40c\n" \
".LP2_w40l:\n" \
"buffer_load_dword v240, v204, s[4:7], 0 offen sc1\n" \
"s_waitcnt vmcnt(0)\n" \
".LP2_w40c:\n" \
"v_subrev_u32 v241, s43, v240\n" \
"v_cmp_gt_u32 vcc, 0x100, v241\n" \
"s_cmp_eq_u64 vcc, -1\n" \
"s_cbranch_scc1 .LP2_w40d\n" \
"s_sleep 2\n" \
"s_add_u32 s50, s50, 1\n" \
"s_cmp_lt_u32 s50, 0x20000\n" \
"s_cbranch_scc1 .LP2_w40l\n" \
"s_branch .LP2_end\n" \
".LP2_w40d:\n" \
"v_readlane_b32 s43, v202, 3\n" \
"s_nop 4\n" \
"buffer_load_dwordx4 a[24:27], v201, s[8:11], s43 offen sc1\n" \
"buffer_load_dwordx4 a[28:31], v201, s[8:11], s43 offen offset:1024 sc1\n" \
"v_readlane_b32 s43, v202, 10\n" \
"s_nop 4\n" \
"buffer_load_dwordx4 a[80:83], v201, s[8:11], s43 offen sc1\n" \
"buffer_load_dwordx4 a[84:87], v201, s[8:11], s43 offen offset:1024 sc1\n" \
"v_readlane_b32 s43, v202, 17\n" \
"s_nop 4\n" \
"buffer_load_dwordx4 a[136:139], v201, s[8:11], s43 offen sc1\n" \
"buffer_load_dwordx4 a[140:143], v201, s[8:11], s43 offen offset:1024 sc1\n" \
"v_readlane_b32 s43, v202, 24\n" \
"s_nop 4\n" \
"buffer_load_dwordx4 a[192:195], v201, s[8:11], s43 offen sc1\n" \
"buffer_load_dwordx4 a[196:199], v201, s[8:11], s43 offen offset:1024 sc1\n" \
"s_add_u32 s43, s30, 4\n" \
"s_mov_b32 s50, 0\n" \
"s_branch .LP2_w41c\n" \
".LP2_w41l:\n" \
"buffer_load_dword v240, v204, s[4:7], 0 offen sc1\n" \
"s_waitcnt vmcnt(0)\n" \
".LP2_w41c:\n" \
"v_subrev_u32 v241, s43, v240\n" \
"v_cmp_gt_u32 vcc, 0x100, v241\n" \
"s_cmp_eq_u64 vcc, -1\n" \
"s_cbranch_scc1 .LP2_w41d\n" \
"s_sleep 2\n" \
"s_add_u32 s50, s50, 1\n" \
"s_cmp_lt_u32 s50, 0x20000\n" \
"s_cbranch_scc1 .LP2_w41l\n" \
"s_branch .LP2_end\n" \
".LP2_w41d:\n" \
"v_readlane_b32 s43, v202, 0\n" \
"s_nop 4\n" \
"buffer_load_dwordx4 a[0:3], v201, s[8:11], s43 offen sc1\n" \
"buffer_load_dwordx4 a[4:7], v201, s[8:11], s43 offen offset:1024 sc1\n" \
"v_readlane_b32 s43, v202, 7\n" \
"s_nop 4\n" \
"buffer_load_dwordx4 a[56:59], v201, s[8:11], s43 offen sc1\n" \
"buffer_load_dwordx4 a[60:63], v201, s[8:11], s43 offen offset:1024 sc1\n" \
"v_readlane_b32 s43, v202, 14\n" \
"s_nop 4\n" \
"buffer_load_dwordx4 a[112:115], v201, s[8:11], s43 offen sc1\n" \
"buffer_load_dwordx4 a[116:119], v201, s[8:11], s43 offen offset:1024 sc1\n" \
"v_readlane_b32 s43, v202, 21\n" \
"s_nop 4\n" \
"buffer_load_dwordx4 a[168:171], v201, s[8:11], s43 offen sc1\n" \
"buffer_load_dwordx4 a[172:175], v201, s[8:11], s43 offen offset:1024 sc1\n" \
"s_add_u32 s43, s30, 5\n" \
"s_mov_b32 s50, 0\n" \
"s_branch .LP2_w42c\n" \
".LP2_w42l:\n" \
"buffer_load_dword v240, v204, s[4:7], 0 offen sc1\n" \
"s_waitcnt vmcnt(0)\n" \
".LP2_w42c:\n" \
"v_subrev_u32 v241, s43, v240\n" \
"v_cmp_gt_u32 vcc, 0x100, v241\n" \
"s_cmp_eq_u64 vcc, -1\n" \
"s_cbranch_scc1 .LP2_w42d\n" \
"s_sleep 2\n" \
"s_add_u32 s50, s50, 1\n" \
"s_cmp_lt_u32 s50, 0x20000\n" \
"s_cbranch_scc1 .LP2_w42l\n" \
"s_branch .LP2_end\n" \
".LP2_w42d:\n" \
"v_readlane_b32 s43, v202, 4\n" \
"s_nop 4\n" \
"buffer_load_dwordx4 a[32:35], v201, s[8:11], s43 offen sc1\n" \
"buffer_load_dwordx4 a[36:39], v201, s[8:11], s43 offen offset:1024 sc1\n" \
"v_readlane_b32 s43, v202, 11\n" \
"s_nop 4\n" \
"buffer_load_dwordx4 a[88:91], v201, s[8:11], s43 offen sc1\n" \
"buffer_load_dwordx4 a[92:95], v201, s[8:11], s43 offen offset:1024 sc1\n" \
"v_readlane_b32 s43, v202, 18\n" \
"s_nop 4\n" \
"buffer_load_dwordx4 a[144:147], v201, s[8:11], s43 offen sc1\n" \
"buffer_load_dwordx4 a[148:151], v201, s[8:11], s43 offen offset:1024 sc1\n" \
"v_readlane_b32 s43, v202, 25\n" \
"s_nop 4\n" \
"buffer_load_dwordx4 a[200:203], v201, s[8:11], s43 offen sc1\n" \
"buffer_load_dwordx4 a[204:207], v201, s[8:11], s43 offen offset:1024 sc1\n" \
"s_add_u32 s43, s30, 6\n" \
"s_mov_b32 s50, 0\n" \
"s_branch .LP2_w43c\n" \
".LP2_w43l:\n" \
"buffer_load_dword v240, v204, s[4:7], 0 offen sc1\n" \
"s_waitcnt vmcnt(0)\n" \
".LP2_w43c:\n" \
"v_subrev_u32 v241, s43, v240\n" \
"v_cmp_gt_u32 vcc, 0x100, v241\n" \
"s_cmp_eq_u64 vcc, -1\n" \
"s_cbranch_scc1 .LP2_w43d\n" \
"s_sleep 2\n" \
"s_add_u32 s50, s50, 1\n" \
"s_cmp_lt_u32 s50, 0x20000\n" \
"s_cbranch_scc1 .LP2_w43l\n" \
"s_branch .LP2_end\n" \
".LP2_w43d:\n" \
"v_readlane_b32 s43, v202, 1\n" \
"s_nop 4\n" \
"buffer_load_dwordx4 a[8:11], v201, s[8:11], s43 offen sc1\n" \
"buffer_load_dwordx4 a[12:15], v201, s[8:11], s43 offen offset:1024 sc1\n" \
"v_readlane_b32 s43, v202, 8\n" \
"s_nop 4\n" \
"buffer_load_dwordx4 a[64:67], v201, s[8:11], s43 offen sc1\n" \
"buffer_load_dwordx4 a[68:71], v201, s[8:11], s43 offen offset:1024 sc1\n" \
"v_readlane_b32 s43, v202, 15\n" \
"s_nop 4\n" \
"buffer_load_dwordx4 a[120:123], v201, s[8:11], s43 offen sc1\n" \
"buffer_load_dwordx4 a[124:127], v201, s[8:11], s43 offen offset:1024 sc1\n" \
"v_readlane_b32 s43, v202, 22\n" \
"s_nop 4\n" \
"buffer_load_dwordx4 a[176:179], v201, s[8:11], s43 offen sc1\n" \
"buffer_load_dwordx4 a[180:183], v201, s[8:11], s43 offen offset:1024 sc1\n" \
"s_add_u32 s43, s30, 7\n" \
"s_mov_b32 s50, 0\n" \
"s_branch .LP2_w44c\n" \
".LP2_w44l:\n" \
"buffer_load_dword v240, v204, s[4:7], 0 offen sc1\n" \
"s_waitcnt vmcnt(0)\n" \
".LP2_w44c:\n" \
"v_subrev_u32 v241, s43, v240\n" \
"v_cmp_gt_u32 vcc, 0x100, v241\n" \
"s_cmp_eq_u64 vcc, -1\n" \
"s_cbranch_scc1 .LP2_w44d\n" \
"s_sleep 2\n" \
"s_add_u32 s50, s50, 1\n" \
"s_cmp_lt_u32 s50, 0x20000\n" \
"s_cbranch_scc1 .LP2_w44l\n" \
"s_branch .LP2_end\n" \
".LP2_w44d:\n" \
"v_readlane_b32 s43, v202, 5\n" \
"s_nop 4\n" \
"buffer_load_dwordx4 a[40:43], v201, s[8:11], s43 offen sc1\n" \
"buffer_load_dwordx4 a[44:47], v201, s[8:11], s43 offen offset:1024 sc1\n" \
"v_readlane_b32 s43, v202, 12\n" \
"s_nop 4\n" \
"buffer_load_dwordx4 a[96:99], v201, s[8:11], s43 offen sc1\n" \
"buffer_load_dwordx4 a[100:103], v201, s[8:11], s43 offen offset:1024 sc1\n" \
"v_readlane_b32 s43, v202, 19\n" \
"s_nop 4\n" \
"buffer_load_dwordx4 a[152:155], v201, s[8:11], s43 offen sc1\n" \
"buffer_load_dwordx4 a[156:159], v201, s[8:11], s43 offen offset:1024 sc1\n" \
"v_readlane_b32 s43, v202, 26\n" \
"s_nop 4\n" \
"buffer_load_dwordx4 a[208:211], v201, s[8:11], s43 offen sc1\n" \
"buffer_load_dwordx4 a[212:215], v201, s[8:11], s43 offen offset:1024 sc1\n" \
"s_add_u32 s43, s30, 1\n" \
"s_mov_b32 s50, 0\n" \
".LP2_w45l:\n" \
"buffer_load_dword v240, v205, s[4:7], 0 offen sc1\n" \
"s_waitcnt vmcnt(0)\n" \
".LP2_w45c:\n" \
"v_subrev_u32 v241, s43, v240\n" \
"v_cmp_gt_u32 vcc, 0x100, v241\n" \
"s_cmp_eq_u64 vcc, -1\n" \
"s_cbranch_scc1 .LP2_w45d\n" \
"s_sleep 2\n" \
"s_add_u32 s50, s50, 1\n" \
"s_cmp_lt_u32 s50, 0x20000\n" \
"s_cbranch_scc1 .LP2_w45l\n" \
"s_branch .LP2_end\n" \
".LP2_w45d:\n" \
"v_readfirstlane_b32 s31, v241\n" \
"s_lshr_b32 s32, s31, 2\n" \
"s_xor_b32 s29, s31, s32\n" \
"s_and_b32 s29, s29, 3\n" \
"s_lshl_b32 s29, s29, 13\n" \
"s_bitset1_b32 s29, 21\n" \
"s_and_b32 s31, s31, 3\n" \
"s_lshl_b32 s31, s31, 13\n" \
"s_bitcmp1_b32 s39, 0\n" \
"s_cselect_b32 s43, s29, 0\n" \
"s_xor_b32 s31, s31, s43\n" \
"s_xor_b32 s32, s31, 0x4000\n" \
"s_mov_b32 s39, 0\n" \
"s_add_u32 s43, s40, 0\n" \
"s_and_b32 s43, s43, 3\n" \
"s_bitcmp1_b32 s43, 0\n" \
"s_cselect_b32 s47, s32, s31\n" \
"s_lshl_b32 s43, s43, 19\n" \
"s_add_u32 s47, s47, s43\n" \
".LP2_pre:\n" \
"s_barrier\n" \
"ds_read_b96 v[248:250], v252\n" \
"s_waitcnt lgkmcnt(0)\n" \
"v_readfirstlane_b32 s44, v248\n" \
"v_readfirstlane_b32 s45, v249\n" \
"v_readfirstlane_b32 s46, v250\n" \
"s_mul_i32 s42, s25, s46\n" \
"s_lshr_b32 s42, s42, 12\n" \
".LP2_cu:\n" \
"s_sub_u32 s43, s24, s44\n" \
"s_cmp_lt_i32 s43, 0\n" \
"s_cbranch_scc0 .LP2_cud\n" \
"s_add_u32 s24, s24, s25\n" \
"s_xor_b32 s31, s31, s29\n" \
"s_xor_b32 s32, s32, s29\n" \
"s_xor_b32 s47, s47, s29\n" \
"s_add_u32 s39, s39, 1\n" \
"s_branch .LP2_cu\n" \
".LP2_cud:\n" \
"s_mov_b32 s3, s46\n" \
"s_mov_b32 s2, s46\n" \
"s_add_u32 s43, s40, 1\n" \
"s_and_b32 s43, s43, 3\n" \
"s_bitcmp1_b32 s43, 0\n" \
"s_cselect_b32 s48, s32, s31\n" \
"s_lshl_b32 s43, s43, 19\n" \
"s_add_u32 s48, s48, s43\n" \
"s_add_u32 s43, s40, 2\n" \
"s_and_b32 s43, s43, 3\n" \
"s_bitcmp1_b32 s43, 0\n" \
"s_cselect_b32 s49, s32, s31\n" \
"s_lshl_b32 s43, s43, 19\n" \
"s_add_u32 s49, s49, s43\n" \
"s_sub_u32 s43, s24, s44\n" \
"s_add_u32 s43, s43, s28\n" \
"s_max_i32 s43, s43, 0xfffd0000\n" \
"s_min_i32 s43, s43, 0x4000\n" \
"s_mul_i32 s43, s43, s46\n" \
"s_ashr_i32 s43, s43, 12\n" \
"s_add_u32 s41, s45, s43\n" \
"s_lshr_b32 s35, s42, 1\n" \
"s_sub_u32 s35, 0, s35\n" \
".LP2_loop:\n" \
"s_branch .LP2_m1\n" \
".LP2_retry1:\n" \
"s_add_u32 s51, s51, 1\n" \
"s_cmp_gt_u32 s51, 0x20000\n" \
"s_cbranch_scc1 .LP2_end\n" \
".LP2_m0:\n" \
"s_add_u32 s41, s41, s42\n" \
"s_add_u32 s24, s24, s25\n" \
"s_xor_b32 s31, s31, s29\n" \
"s_xor_b32 s32, s32, s29\n" \
"s_xor_b32 s47, s47, s29\n" \
"s_xor_b32 s48, s48, s29\n" \
"s_add_u32 s39, s39, 1\n" \
"s_bitcmp1_b32 s31, 21\n" \
"s_cselect_b32 s43, s29, 0\n" \
"v_mov_b32 v116, s43\n" \
"ds_write_b32 v252, v116 offset:16\n" \
".LP2_m1:\n" \
"s_memtime vcc\n" \
"s_waitcnt lgkmcnt(0)\n" \
"s_sub_u32 s43, s41, vcc_lo\n" \
"s_cmp_gt_i32 s43, 0\n" \
"s_cbranch_scc1 .LP2_m1\n" \
"s_cmp_lt_i32 s43, s35\n" \
"s_cbranch_scc1 .LP2_m0\n" \
"buffer_load_dwordx4 v[0:3], v209, s[4:7], s47 offen offset:1024 sc1\n" \
"buffer_load_dwordx4 v[4:7], v209, s[4:7], s47 offen offset:2048 sc1\n" \
"s_cmp_lt_u32 s40, 2\n" \
"s_cselect_b32 s34, s39, s34\n" \
"s_cmp_lg_u32 s39, s34\n" \
"s_cselect_b32 s43, 20, -4\n" \
"s_add_u32 s33, s33, s43\n" \
"s_max_i32 s33, s33, 0\n" \
"s_min_i32 s33, s33, 60\n" \
"s_mov_b32 s34, s39\n" \
"s_add_u32 s3, s3, 10\n" \
"s_min_u32 s3, s3, s46\n" \
"s_sub_u32 s43, 6042, s3\n" \
"s_mov_b32 s50, s33\n" \
"s_sub_u32 s36, s2, s46\n" \
"s_mov_b32 s2, s46\n" \
"s_max_i32 s36, s36, 0\n" \
"s_mul_i32 s36, s36, 3\n" \
"s_add_u32 s43, s43, s36\n" \
"s_mul_i32 s36, s43, 185\n" \
"s_ashr_i32 s36, s36, 10\n" \
"s_add_u32 s36, s36, s50\n" \
"s_add_u32 s36, s36, 1040\n" \
"v_mov_b32 v110, s36\n" \
"s_mul_i32 s36, s43, 138\n" \
"s_ashr_i32 s36, s36, 10\n" \
"s_add_u32 s36, s36, s50\n" \
"s_add_u32 s36, s36, 790\n" \
"v_mov_b32 v111, s36\n" \
"s_nop 15\n" \
"s_nop 13\n" \
"s_mul_i32 s50, s40, 4096\n" \
"buffer_store_dwordx4 v[68:71], v218, s[12:15], s50 offen sc1\n" \
"buffer_store_dword v112, v213, s[4:7], s49 offen sc1\n" \
"s_xor_b32 s43, s49, s29\n" \
"buffer_store_dword v112, v213, s[4:7], s43 offen sc1\n" \
"s_lshl_b32 s43, s40, 7\n" \
"buffer_store_dwordx2 v[110:111], v222, s[4:7], s43 offen offset:384 sc1\n" \
"v_mov_b32 v28, 0\n" \
"v_mov_b32 v29, 0\n" \
"v_mov_b32 v30, 0\n" \
"v_mov_b32 v31, 0\n" \
"v_mov_b32 v32, 0\n" \
"v_mov_b32 v33, 0\n" \
"v_mov_b32 v34, 0\n" \
"v_mov_b32 v35, 0\n" \
"v_mov_b32 v36, 0\n" \
"v_mov_b32 v37, 0\n" \
"v_mov_b32 v38, 0\n" \
"v_mov_b32 v39, 0\n" \
"v_mov_b32 v40, 0\n" \
"v_mov_b32 v41, 0\n" \
"v_mov_b32 v42, 0\n" \
"v_mov_b32 v43, 0\n" \
"s_waitcnt vmcnt(5)\n" \
"ds_write_b128 v220, v[0:3] offset:5120\n" \
"s_waitcnt vmcnt(4)\n" \
"ds_write_b128 v220, v[4:7] offset:6144\n" \
"v_max3_u32 v44, v0, v0, v1\n" \
"v_max3_u32 v44, v44, v2, v3\n" \
"v_max3_u32 v44, v44, v4, v5\n" \
"v_max3_u32 v44, v44, v6, v7\n" \
"v_cmp_eq_u32 vcc, -1, v44\n" \
"v_smfmac_f32_16x16x64_bf16 v[28:31], v[0:3], a[0:7], v200\n" \
"v_smfmac_f32_16x16x64_bf16 v[32:35], v[0:3], a[56:63], v200\n" \
"s_cbranch_vccnz .LP2_retry1\n" \
"s_waitcnt lgkmcnt(0)\n" \
"s_barrier\n" \
"ds_read_b128 v[8:11], v220 offset:0\n" \
"ds_read_b128 v[12:15], v220 offset:1024\n" \
"ds_read_b128 v[16:19], v220 offset:2048\n" \
"ds_read_b128 v[20:23], v220 offset:3072\n" \
"ds_read_b128 v[24:27], v220 offset:4096\n" \
"ds_read_b64 v[246:247], v252 offset:24\n" \
"ds_read_b96 v[248:250], v252\n" \
"v_smfmac_f32_16x16x64_bf16 v[36:39], v[0:3], a[112:119], v200\n" \
"v_smfmac_f32_16x16x64_bf16 v[40:43], v[0:3], a[168:175], v200\n" \
"v_smfmac_f32_16x16x64_bf16 v[28:31], v[4:7], a[8:15], v200\n" \
"v_smfmac_f32_16x16x64_bf16 v[32:35], v[4:7], a[64:71], v200\n" \
"v_smfmac_f32_16x16x64_bf16 v[36:39], v[4:7], a[120:127], v200\n" \
"v_smfmac_f32_16x16x64_bf16 v[40:43], v[4:7], a[176:183], v200\n" \
"s_waitcnt lgkmcnt(6)\n" \
"v_smfmac_f32_16x16x64_bf16 v[28:31], v[8:11], a[16:23], v200\n" \
"v_smfmac_f32_16x16x64_bf16 v[32:35], v[8:11], a[72:79], v200\n" \
"v_smfmac_f32_16x16x64_bf16 v[36:39], v[8:11], a[128:135], v200\n" \
"v_smfmac_f32_16x16x64_bf16 v[40:43], v[8:11], a[184:191], v200\n" \
"s_waitcnt lgkmcnt(0)\n" \
"v_readfirstlane_b32 s44, v248\n" \
"v_readfirstlane_b32 s45, v249\n" \
"v_smfmac_f32_16x16x64_bf16 v[28:31], v[12:15], a[24:31], v200\n" \
"v_readfirstlane_b32 s46, v250\n" \
"v_readfirstlane_b32 s26, v246\n" \
"v_readfirstlane_b32 s27, v247\n" \
"v_smfmac_f32_16x16x64_bf16 v[32:35], v[12:15], a[80:87], v200\n" \
"s_mul_i32 s42, s25, s46\n" \
"s_lshr_b32 s42, s42, 12\n" \
"s_add_u32 s24, s24, s26\n" \
"v_smfmac_f32_16x16x64_bf16 v[36:39], v[12:15], a[136:143], v200\n" \
"s_sub_u32 s43, s24, s44\n" \
"s_max_i32 s43, s43, 0xfffd0000\n" \
"s_min_i32 s43, s43, 0x4000\n" \
"v_smfmac_f32_16x16x64_bf16 v[40:43], v[12:15], a[192:199], v200\n" \
"s_mul_i32 s43, s43, s46\n" \
"s_ashr_i32 s43, s43, 12\n" \
"s_add_u32 s41, s45, s43\n" \
"s_cmp_eq_u32 s40, 63\n" \
"s_cbranch_scc1 .LP2_last\n" \
"s_branch .LP2_z1\n" \
".LP2_z0:\n" \
"s_add_u32 s41, s41, s42\n" \
"s_add_u32 s24, s24, s25\n" \
"s_xor_b32 s31, s31, s29\n" \
"s_xor_b32 s32, s32, s29\n" \
"s_xor_b32 s47, s47, s29\n" \
"s_xor_b32 s48, s48, s29\n" \
"s_add_u32 s39, s39, 1\n" \
"s_bitcmp1_b32 s31, 21\n" \
"s_cselect_b32 s43, s29, 0\n" \
"v_mov_b32 v116, s43\n" \
"ds_write_b32 v252, v116 offset:16\n" \
".LP2_z1:\n" \
"s_memtime vcc\n" \
"s_waitcnt lgkmcnt(0)\n" \
"s_sub_u32 s43, s41, vcc_lo\n" \
"s_cmp_gt_i32 s43, 160\n" \
"s_cbranch_scc1 .LP2_z1\n" \
"s_cmp_lt_i32 s43, s37\n" \
"s_cbranch_scc1 .LP2_z0\n" \
"buffer_load_dwordx2 v[64:65], v211, s[4:7], s47 offen sc1\n" \
".p2align 6\n" \
".LP2_b1:\n" \
"s_memtime vcc\n" \
"s_waitcnt lgkmcnt(0)\n" \
"s_sub_u32 s43, s41, vcc_lo\n" \
"s_cmp_gt_i32 s43, 128\n" \
"s_cbranch_scc1 .LP2_b1\n" \
"s_cmp_lt_i32 s43, s37\n" \
"s_cbranch_scc1 .LP2_z0\n" \
"s_max_i32 s43, s43, 0\n" \
"s_and_b32 s43, s43, -4\n" \
"s_getpc_b64 vcc\n" \
".LP2_b3:\n" \
"s_sub_u32 s43, .LP2_b4-.LP2_b3, s43\n" \
"s_add_u32 vcc_lo, vcc_lo, s43\n" \
"s_addc_u32 vcc_hi, vcc_hi, 0\n" \
"s_setpc_b64 vcc\n" \
".rept 32\n" \
"s_nop 0\n" \
".endr\n" \
".LP2_b4:\n" \
"buffer_load_dwordx4 v[56:59], v210, s[4:7], s47 offen sc1\n" \
"buffer_load_dwordx4 v[60:63], v210, s[4:7], s47 offen offset:256 sc1\n" \
"v_smfmac_f32_16x16x64_bf16 v[28:31], v[16:19], a[32:39], v200\n" \
"v_smfmac_f32_16x16x64_bf16 v[32:35], v[16:19], a[88:95], v200\n" \
"v_smfmac_f32_16x16x64_bf16 v[36:39], v[16:19], a[144:151], v200\n" \
"v_smfmac_f32_16x16x64_bf16 v[40:43], v[16:19], a[200:207], v200\n" \
"buffer_load_dwordx4 v[50:53], v212, s[4:7], s47 offen sc1\n" \
"v_smfmac_f32_16x16x64_bf16 v[28:31], v[20:23], a[40:47], v200\n" \
"v_smfmac_f32_16x16x64_bf16 v[32:35], v[20:23], a[96:103], v200\n" \
"v_smfmac_f32_16x16x64_bf16 v[36:39], v[20:23], a[152:159], v200\n" \
"v_smfmac_f32_16x16x64_bf16 v[40:43], v[20:23], a[208:215], v200\n" \
"v_smfmac_f32_16x16x64_bf16 v[28:31], v[24:27], a[48:55], v200\n" \
"v_smfmac_f32_16x16x64_bf16 v[32:35], v[24:27], a[104:111], v200\n" \
"v_smfmac_f32_16x16x64_bf16 v[36:39], v[24:27], a[160:167], v200\n" \
"v_smfmac_f32_16x16x64_bf16 v[40:43], v[24:27], a[216:223], v200\n" \
"s_nop 2\n" \
"v_mov_b32 v245, v245\n" \
"v_mov_b32 v245, v245\n" \
"v_mov_b32 v245, v245\n" \
"v_permlane32_swap_b32 v28, v32\n" \
"v_permlane32_swap_b32 v29, v33\n" \
"v_permlane32_swap_b32 v30, v34\n" \
"v_permlane32_swap_b32 v31, v35\n" \
"v_add_f32 v28, v28, v32\n" \
"v_add_f32 v29, v29, v33\n" \
"v_add_f32 v30, v30, v34\n" \
"v_add_f32 v31, v31, v35\n" \
"v_cvt_pk_bf16_f32 v68, v28, v29\n" \
"v_cvt_pk_bf16_f32 v69, v30, v31\n" \
"v_permlane32_swap_b32 v36, v40\n" \
"v_permlane32_swap_b32 v37, v41\n" \
"v_permlane32_swap_b32 v38, v42\n" \
"v_permlane32_swap_b32 v39, v43\n" \
"v_add_f32 v36, v36, v40\n" \
"v_add_f32 v37, v37, v41\n" \
"v_add_f32 v38, v38, v42\n" \
"v_add_f32 v39, v39, v43\n" \
"v_cvt_pk_bf16_f32 v70, v36, v37\n" \
"v_cvt_pk_bf16_f32 v71, v38, v39\n" \
"v_mov_b32 v48, v68\n" \
"v_mov_b32 v49, v69\n" \
"v_mov_b32 v54, v70\n" \
"v_mov_b32 v55, v71\n" \
"s_nop 1\n" \
"v_permlane16_swap_b32 v48, v54\n" \
"v_permlane16_swap_b32 v49, v55\n" \
"s_nop 2\n" \
"v_mov_b32 v245, v245\n" \
".LP2_wait:\n" \
"s_waitcnt vmcnt(1)\n" \
"v_pk_add_f32 v[72:73], v[56:57], v[60:61]\n" \
"v_pk_add_f32 v[74:75], v[58:59], v[62:63]\n" \
"v_max3_f32 v77, v72, v73, v64\n" \
"v_max3_f32 v77, v77, v74, v75\n" \
"s_nop 1\n" \
"v_max_f32_dpp v76, v77, v77 row_ror:8 row_mask:0xf bank_mask:0xf\n" \
"v_pk_add_f32 v[72:73], v[72:73], v[76:77] op_sel_hi:[1,0] neg_lo:[0,1] neg_hi:[0,1]\n" \
"v_pk_add_f32 v[74:75], v[74:75], v[76:77] op_sel_hi:[1,0] neg_lo:[0,1] neg_hi:[0,1]\n" \
"v_sub_f32 v78, v64, v76\n" \
"v_exp_f32 v72, v72\n" \
"v_exp_f32 v73, v73\n" \
"v_exp_f32 v74, v74\n" \
"v_exp_f32 v75, v75\n" \
"v_exp_f32 v78, v78\n" \
"v_pk_add_f32 v[80:81], v[72:73], v[74:75]\n" \
"v_fma_f32 v80, v78, v65, v80\n" \
"v_add_f32 v81, v80, v81\n" \
"s_nop 1\n" \
"v_add_f32_dpp v80, v81, v81 row_ror:8 row_mask:0xf bank_mask:0xf\n" \
"v_rcp_f32 v80, v80\n" \
"s_nop 0\n" \
"v_mul_f32 v78, v78, v80\n" \
"v_pk_mul_f32 v[72:73], v[72:73], v[80:81] op_sel_hi:[1,0]\n" \
"v_pk_mul_f32 v[74:75], v[74:75], v[80:81] op_sel_hi:[1,0]\n" \
"v_cvt_pk_bf16_f32 v78, v78, v78\n" \
"v_cvt_pk_bf16_f32 v82, v72, v73\n" \
"v_cvt_pk_bf16_f32 v83, v74, v75\n" \
"v_and_b32 v84, v78, v214\n" \
"v_and_b32 v85, v78, v215\n" \
"v_cvt_pk_bf16_f32 v86, v72, v73\n" \
"s_waitcnt vmcnt(0)\n" \
"v_mfma_f32_16x16x32_bf16 v[88:91], v[48:51], v[82:85], 0\n" \
"v_cvt_pk_bf16_f32 v87, v74, v75\n" \
"s_nop 1\n" \
"v_mfma_f32_16x16x32_bf16 v[92:95], v[52:55], v[84:87], 0\n" \
"s_nop 0\n" \
"s_nop 0\n" \
"s_nop 0\n" \
"v_mov_b32 v245, v245\n" \
"s_nop 0\n" \
"s_nop 0\n" \
"s_nop 0\n" \
"v_mov_b32 v245, v245\n" \
"v_add_f32_dpp v96, v92, v88 row_ror:8 row_mask:0xf bank_mask:0x3\n" \
"v_add_f32_dpp v97, v93, v89 row_ror:8 row_mask:0xf bank_mask:0x3\n" \
"v_add_f32_dpp v96, v90, v94 row_ror:8 row_mask:0xf bank_mask:0xc\n" \
"v_add_f32_dpp v97, v91, v95 row_ror:8 row_mask:0xf bank_mask:0xc\n" \
"ds_write_b64 v216, v[96:97]\n" \
"v_cmp_u_f32 vcc, v96, v97\n" \
"s_nop 0\n" \
"v_mov_b32 v245, v245\n" \
"s_nop 2\n" \
"v_mov_b32 v245, v245\n" \
"s_cbranch_vccnz .LP2_cls\n" \
".LP2_clsr:\n" \
"s_waitcnt lgkmcnt(0)\n" \
"s_barrier\n" \
"s_add_u32 s40, s40, 1\n" \
"s_add_u32 s24, s24, s27\n" \
"s_mov_b32 s47, s48\n" \
"s_add_u32 s43, s40, 1\n" \
"s_and_b32 s43, s43, 3\n" \
"s_bitcmp1_b32 s43, 0\n" \
"s_cselect_b32 s48, s32, s31\n" \
"s_lshl_b32 s43, s43, 19\n" \
"s_add_u32 s48, s48, s43\n" \
"s_add_u32 s43, s40, 2\n" \
"s_and_b32 s43, s43, 3\n" \
"s_bitcmp1_b32 s43, 0\n" \
"s_cselect_b32 s49, s32, s31\n" \
"s_lshl_b32 s43, s43, 19\n" \
"s_add_u32 s49, s49, s43\n" \
"s_sub_u32 s43, s24, s44\n" \
"s_add_u32 s43, s43, s28\n" \
"s_max_i32 s43, s43, 0xfffd0000\n" \
"s_min_i32 s43, s43, 0x4000\n" \
"s_mul_i32 s43, s43, s46\n" \
"s_ashr_i32 s43, s43, 12\n" \
"s_add_u32 s41, s45, s43\n" \
"s_lshr_b32 s35, s42, 1\n" \
"s_sub_u32 s35, 0, s35\n" \
"s_cmp_eq_u32 s40, 0\n" \
"s_cbranch_scc1 .LP2_last\n" \
"s_branch .LP2_loop\n" \
"s_branch .LP2_y1\n" \
".LP2_retry2:\n" \
"s_add_u32 s51, s51, 1\n" \
"s_cmp_gt_u32 s51, 0x20000\n" \
"s_cbranch_scc1 .LP2_end\n" \
".LP2_y0:\n" \
"s_add_u32 s41, s41, s42\n" \
"s_add_u32 s24, s24, s25\n" \
"s_xor_b32 s31, s31, s29\n" \
"s_xor_b32 s32, s32, s29\n" \
"s_xor_b32 s47, s47, s29\n" \
"s_xor_b32 s48, s48, s29\n" \
"s_add_u32 s39, s39, 1\n" \
"buffer_store_dwordx4 v[112:115], v223, s[4:7], s48 offen sc1\n" \
"s_bitcmp1_b32 s31, 21\n" \
"s_cselect_b32 s43, s29, 0\n" \
"v_mov_b32 v116, s43\n" \
"ds_write_b32 v252, v116 offset:16\n" \
".LP2_y1:\n" \
"s_memtime vcc\n" \
"s_waitcnt lgkmcnt(0)\n" \
"s_sub_u32 s43, s41, vcc_lo\n" \
"s_cmp_gt_i32 s43, 160\n" \
"s_cbranch_scc1 .LP2_y1\n" \
"s_cmp_lt_i32 s43, s37\n" \
"s_cbranch_scc1 .LP2_y0\n" \
"buffer_load_dwordx2 v[64:65], v211, s[4:7], s47 offen sc1\n" \
".p2align 6\n" \
".LP2_c1:\n" \
"s_memtime vcc\n" \
"s_waitcnt lgkmcnt(0)\n" \
"s_sub_u32 s43, s41, vcc_lo\n" \
"s_cmp_gt_i32 s43, 128\n" \
"s_cbranch_scc1 .LP2_c1\n" \
"s_cmp_lt_i32 s43, s37\n" \
"s_cbranch_scc1 .LP2_y0\n" \
"s_max_i32 s43, s43, 0\n" \
"s_and_b32 s43, s43, -4\n" \
"s_getpc_b64 vcc\n" \
".LP2_c3:\n" \
"s_sub_u32 s43, .LP2_c4-.LP2_c3, s43\n" \
"s_add_u32 vcc_lo, vcc_lo, s43\n" \
"s_addc_u32 vcc_hi, vcc_hi, 0\n" \
"s_setpc_b64 vcc\n" \
".rept 32\n" \
"s_nop 0\n" \
".endr\n" \
".LP2_c4:\n" \
"buffer_load_dwordx4 v[56:59], v210, s[4:7], s47 offen sc1\n" \
"buffer_load_dwordx4 v[60:63], v210, s[4:7], s47 offen offset:256 sc1\n" \
"s_nop 2\n" \
"v_mov_b32 v245, v245\n" \
"s_nop 2\n" \
"v_mov_b32 v245, v245\n" \
"s_nop 2\n" \
"v_mov_b32 v245, v245\n" \
"s_nop 2\n" \
"v_mov_b32 v245, v245\n" \
"buffer_load_dwordx4 v[50:53], v212, s[4:7], s47 offen sc1\n" \
"s_nop 2\n" \
"v_mov_b32 v245, v245\n" \
"s_nop 2\n" \
"v_mov_b32 v245, v245\n" \
"s_nop 2\n" \
"v_mov_b32 v245, v245\n" \
"s_nop 2\n" \
"v_mov_b32 v245, v245\n" \
"s_nop 2\n" \
"v_mov_b32 v245, v245\n" \
"s_nop 2\n" \
"v_mov_b32 v245, v245\n" \
"s_nop 2\n" \
"v_mov_b32 v245, v245\n" \
"s_nop 2\n" \
"v_mov_b32 v245, v245\n" \
"s_nop 2\n" \
"v_mov_b32 v245, v245\n" \
"s_nop 2\n" \
"v_mov_b32 v245, v245\n" \
"s_nop 2\n" \
"v_mov_b32 v245, v245\n" \
"s_nop 2\n" \
"v_mov_b32 v245, v245\n" \
"s_nop 2\n" \
"v_mov_b32 v245, v245\n" \
"s_nop 2\n" \
"v_mov_b32 v245, v245\n" \
"s_nop 2\n" \
"v_mov_b32 v245, v245\n" \
"s_nop 2\n" \
"v_mov_b32 v245, v245\n" \
"s_nop 2\n" \
"v_mov_b32 v245, v245\n" \
"s_nop 2\n" \
"v_mov_b32 v245, v245\n" \
"s_nop 2\n" \
"v_mov_b32 v245, v245\n" \
"s_nop 2\n" \
"v_mov_b32 v245, v245\n" \
"s_branch .LP2_wait\n" \
".LP2_cls:\n" \
"s_waitcnt vmcnt(0)\n" \
"v_max3_u32 v44, v64, v64, v65\n" \
"v_max3_u32 v44, v44, v50, v51\n" \
"v_max3_u32 v44, v44, v52, v53\n" \
"v_cmp_eq_u32 vcc, -1, v44\n" \
"s_cbranch_vccnz .LP2_retry2\n" \
"v_cmp_u_f32 vcc, v56, v56\n" \
"v_cmp_ne_u32 s[56:57], v56, v109\n" \
"s_and_b64 vcc, vcc, s[56:57]\n" \
"s_cbranch_vccnz .LP2_retry2\n" \
"v_cmp_u_f32 vcc, v57, v57\n" \
"v_cmp_ne_u32 s[56:57], v57, v109\n" \
"s_and_b64 vcc, vcc, s[56:57]\n" \
"s_cbranch_vccnz .LP2_retry2\n" \
"v_cmp_u_f32 vcc, v58, v58\n" \
"v_cmp_ne_u32 s[56:57], v58, v109\n" \
"s_and_b64 vcc, vcc, s[56:57]\n" \
"s_cbranch_vccnz .LP2_retry2\n" \
"v_cmp_u_f32 vcc, v59, v59\n" \
"v_cmp_ne_u32 s[56:57], v59, v109\n" \
"s_and_b64 vcc, vcc, s[56:57]\n" \
"s_cbranch_vccnz .LP2_retry2\n" \
"v_cmp_u_f32 vcc, v60, v60\n" \
"v_cmp_ne_u32 s[56:57], v60, v109\n" \
"s_and_b64 vcc, vcc, s[56:57]\n" \
"s_cbranch_vccnz .LP2_retry2\n" \
"v_cmp_u_f32 vcc, v61, v61\n" \
"v_cmp_ne_u32 s[56:57], v61, v109\n" \
"s_and_b64 vcc, vcc, s[56:57]\n" \
"s_cbranch_vccnz .LP2_retry2\n" \
"v_cmp_u_f32 vcc, v62, v62\n" \
"v_cmp_ne_u32 s[56:57], v62, v109\n" \
"s_and_b64 vcc, vcc, s[56:57]\n" \
"s_cbranch_vccnz .LP2_retry2\n" \
"v_cmp_u_f32 vcc, v63, v63\n" \
"v_cmp_ne_u32 s[56:57], v63, v109\n" \
"s_and_b64 vcc, vcc, s[56:57]\n" \
"s_cbranch_vccnz .LP2_retry2\n" \
"s_branch .LP2_clsr\n" \
".LP2_last:\n" \
"v_smfmac_f32_16x16x64_bf16 v[28:31], v[16:19], a[32:39], v200\n" \
"v_smfmac_f32_16x16x64_bf16 v[32:35], v[16:19], a[88:95], v200\n" \
"v_smfmac_f32_16x16x64_bf16 v[36:39], v[16:19], a[144:151], v200\n" \
"v_smfmac_f32_16x16x64_bf16 v[40:43], v[16:19], a[200:207], v200\n" \
"v_smfmac_f32_16x16x64_bf16 v[28:31], v[20:23], a[40:47], v200\n" \
"v_smfmac_f32_16x16x64_bf16 v[32:35], v[20:23], a[96:103], v200\n" \
"v_smfmac_f32_16x16x64_bf16 v[36:39], v[20:23], a[152:159], v200\n" \
"v_smfmac_f32_16x16x64_bf16 v[40:43], v[20:23], a[208:215], v200\n" \
"v_smfmac_f32_16x16x64_bf16 v[28:31], v[24:27], a[48:55], v200\n" \
"v_smfmac_f32_16x16x64_bf16 v[32:35], v[24:27], a[104:111], v200\n" \
"v_smfmac_f32_16x16x64_bf16 v[36:39], v[24:27], a[160:167], v200\n" \
"v_smfmac_f32_16x16x64_bf16 v[40:43], v[24:27], a[216:223], v200\n" \
"s_nop 7\n" \
"v_permlane32_swap_b32 v28, v32\n" \
"v_permlane32_swap_b32 v29, v33\n" \
"v_permlane32_swap_b32 v30, v34\n" \
"v_permlane32_swap_b32 v31, v35\n" \
"v_add_f32 v28, v28, v32\n" \
"v_add_f32 v29, v29, v33\n" \
"v_add_f32 v30, v30, v34\n" \
"v_add_f32 v31, v31, v35\n" \
"v_cvt_pk_bf16_f32 v68, v28, v29\n" \
"v_cvt_pk_bf16_f32 v69, v30, v31\n" \
"v_permlane32_swap_b32 v36, v40\n" \
"v_permlane32_swap_b32 v37, v41\n" \
"v_permlane32_swap_b32 v38, v42\n" \
"v_permlane32_swap_b32 v39, v43\n" \
"v_add_f32 v36, v36, v40\n" \
"v_add_f32 v37, v37, v41\n" \
"v_add_f32 v38, v38, v42\n" \
"v_add_f32 v39, v39, v43\n" \
"v_cvt_pk_bf16_f32 v70, v36, v37\n" \
"v_cvt_pk_bf16_f32 v71, v38, v39\n" \
"s_mov_b32 s50, 262144\n" \
"buffer_store_dwordx4 v[68:71], v218, s[12:15], s50 offen sc1\n" \
"s_cmp_eq_u32 s40, 0\n" \
"s_cbranch_scc1 .LP2_wet\n" \
".LP2_end:\n" \
"s_endpgm\n" \
".p2align 6\n" \
"fk_role_P3:\n" \
"ds_read_b32 v200, v255 offset:0\n" \
"ds_read_b32 v201, v255 offset:256\n" \
"ds_read_b32 v202, v255 offset:512\n" \
"ds_read_b32 v203, v255 offset:768\n" \
"ds_read_b32 v204, v255 offset:1024\n" \
"ds_read_b32 v205, v255 offset:1280\n" \
"ds_read_b32 v206, v255 offset:1536\n" \
"ds_read_b32 v207, v255 offset:1792\n" \
"ds_read_b32 v208, v255 offset:2048\n" \
"ds_read_b32 v209, v255 offset:2304\n" \
"ds_read_b32 v210, v255 offset:2560\n" \
"ds_read_b32 v211, v255 offset:2816\n" \
"ds_read_b32 v212, v255 offset:3072\n" \
"ds_read_b32 v213, v255 offset:3328\n" \
"ds_read_b32 v214, v255 offset:3584\n" \
"ds_read_b32 v215, v255 offset:3840\n" \
"ds_read_b32 v216, v255 offset:4096\n" \
"ds_read_b32 v217, v255 offset:4352\n" \
"ds_read_b32 v218, v255 offset:4608\n" \
"ds_read_b32 v219, v255 offset:4864\n" \
"ds_read_b32 v220, v255 offset:5120\n" \
"ds_read_b32 v221, v255 offset:5376\n" \
"ds_read_b32 v222, v255 offset:5632\n" \
"ds_read_b32 v223, v255 offset:5888\n" \
"ds_read_b32 v254, v255 offset:7936\n" \
"s_waitcnt lgkmcnt(0)\n" \
"v_readlane_b32 s4, v254, 0\n" \
"v_readlane_b32 s5, v254, 1\n" \
"v_readlane_b32 s6, v254, 2\n" \
"v_readlane_b32 s7, v254, 3\n" \
"v_readlane_b32 s8, v254, 4\n" \
"v_readlane_b32 s9, v254, 5\n" \
"v_readlane_b32 s10, v254, 6\n" \
"v_readlane_b32 s11, v254, 7\n" \
"v_readlane_b32 s12, v254, 8\n" \
"v_readlane_b32 s13, v254, 9\n" \
"v_readlane_b32 s14, v254, 10\n" \
"v_readlane_b32 s15, v254, 11\n" \
"v_readlane_b32 s16, v254, 12\n" \
"v_readlane_b32 s17, v254, 13\n" \
"v_readlane_b32 s18, v254, 14\n" \
"v_readlane_b32 s19, v254, 15\n" \
"v_readlane_b32 s20, v254, 16\n" \
"v_readlane_b32 s21, v254, 17\n" \
"v_readlane_b32 s22, v254, 18\n" \
"v_readlane_b32 s23, v254, 19\n" \
"v_readlane_b32 s24, v254, 20\n" \
"v_readlane_b32 s25, v254, 21\n" \
"v_readlane_b32 s26, v254, 22\n" \
"v_readlane_b32 s27, v254, 23\n" \
"v_readlane_b32 s28, v254, 24\n" \
"v_readlane_b32 s29, v254, 25\n" \
"v_readlane_b32 s30, v254, 26\n" \
"v_readlane_b32 s31, v254, 27\n" \
"v_readlane_b32 s32, v254, 28\n" \
"v_readlane_b32 s33, v254, 29\n" \
"v_readlane_b32 s34, v254, 30\n" \
"v_readlane_b32 s35, v254, 31\n" \
"v_readlane_b32 s36, v254, 32\n" \
"v_readlane_b32 s37, v254, 33\n" \
"v_readlane_b32 s38, v254, 34\n" \
"v_readlane_b32 s39, v254, 35\n" \
"s_mov_b32 s40, 0\n" \
"s_mov_b32 s51, 0\n" \
"s_nop 0\n" \
"v_mov_b32 v252, s38\n" \
"s_nop 4\n" \
"v_mov_b32 v112, -1\n" \
"v_mov_b32 v113, -1\n" \
"v_mov_b32 v114, -1\n" \
"v_mov_b32 v115, -1\n" \
"v_mov_b32 v109, 0x7fc00001\n" \
"s_mov_b32 s52, 0x8000\n" \
"s_mov_b32 s53, 0x4000\n" \
"s_mov_b32 s54, 0xc000\n" \
"v_writelane_b32 v251, s6, 0\n" \
"v_writelane_b32 v251, s10, 1\n" \
"v_writelane_b32 v251, s14, 2\n" \
"v_writelane_b32 v251, s18, 3\n" \
"s_mov_b32 s6, 0\n" \
"s_mov_b32 s10, 0\n" \
"s_mov_b32 s14, 0\n" \
"s_mov_b32 s18, 0\n" \
"s_mov_b32 s40, -1\n" \
"s_branch .LP3_pre\n" \
".LP3_wet:\n" \
"s_waitcnt vmcnt(0)\n" \
"v_readlane_b32 s6, v251, 0\n" \
"v_readlane_b32 s10, v251, 1\n" \
"v_readlane_b32 s14, v251, 2\n" \
"v_readlane_b32 s18, v251, 3\n" \
"s_nop 4\n" \
"ds_read_b32 v230, v255 offset:6912\n" \
"v_readlane_b32 s60, v254, 12\n" \
"v_readlane_b32 s61, v254, 13\n" \
"v_readlane_b32 s62, v254, 14\n" \
"v_readlane_b32 s63, v254, 15\n" \
"s_waitcnt lgkmcnt(0)\n" \
"s_nop 3\n" \
"buffer_load_dword v228, v230, s[60:63], 0 offen\n" \
"s_mov_b32 s33, 36\n" \
"s_mov_b32 s34, 0\n" \
"s_add_u32 s43, s30, 1\n" \
"s_mov_b32 s50, 0\n" \
".LP3_w46l:\n" \
"buffer_load_dword v240, v204, s[4:7], 0 offen sc1\n" \
"s_waitcnt vmcnt(0)\n" \
".LP3_w46c:\n" \
"v_subrev_u32 v241, s43, v240\n" \
"v_cmp_gt_u32 vcc, 0x100, v241\n" \
"s_cmp_eq_u64 vcc, -1\n" \
"s_cbranch_scc1 .LP3_w46d\n" \
"s_sleep 2\n" \
"s_add_u32 s50, s50, 1\n" \
"s_cmp_lt_u32 s50, 0x20000\n" \
"s_cbranch_scc1 .LP3_w46l\n" \
"s_branch .LP3_end\n" \
".LP3_w46d:\n" \
"v_readlane_b32 s43, v202, 1\n" \
"s_nop 4\n" \
"buffer_load_dwordx4 a[8:11], v201, s[8:11], s43 offen sc1\n" \
"buffer_load_dwordx4 a[12:15], v201, s[8:11], s43 offen offset:1024 sc1\n" \
"v_readlane_b32 s43, v202, 8\n" \
"s_nop 4\n" \
"buffer_load_dwordx4 a[64:67], v201, s[8:11], s43 offen sc1\n" \
"buffer_load_dwordx4 a[68:71], v201, s[8:11], s43 offen offset:1024 sc1\n" \
"v_readlane_b32 s43, v202, 15\n" \
"s_nop 4\n" \
"buffer_load_dwordx4 a[120:123], v201, s[8:11], s43 offen sc1\n" \
"buffer_load_dwordx4 a[124:127], v201, s[8:11], s43 offen offset:1024 sc1\n" \
"v_readlane_b32 s43, v202, 22\n" \
"s_nop 4\n" \
"buffer_load_dwordx4 a[176:179], v201, s[8:11], s43 offen sc1\n" \
"buffer_load_dwordx4 a[180:183], v201, s[8:11], s43 offen offset:1024 sc1\n" \
"s_add_u32 s43, s30, 2\n" \
"s_mov_b32 s50, 0\n" \
"s_branch .LP3_w47c\n" \
".LP3_w47l:\n" \
"buffer_load_dword v240, v204, s[4:7], 0 offen sc1\n" \
"s_waitcnt vmcnt(0)\n" \
".LP3_w47c:\n" \
"v_subrev_u32 v241, s43, v240\n" \
"v_cmp_gt_u32 vcc, 0x100, v241\n" \
"s_cmp_eq_u64 vcc, -1\n" \
"s_cbranch_scc1 .LP3_w47d\n" \
"s_sleep 2\n" \
"s_add_u32 s50, s50, 1\n" \
"s_cmp_lt_u32 s50, 0x20000\n" \
"s_cbranch_scc1 .LP3_w47l\n" \
"s_branch .LP3_end\n" \
".LP3_w47d:\n" \
"v_readlane_b32 s43, v202, 4\n" \
"s_nop 4\n" \
"buffer_load_dwordx4 a[32:35], v201, s[8:11], s43 offen sc1\n" \
"buffer_load_dwordx4 a[36:39], v201, s[8:11], s43 offen offset:1024 sc1\n" \
"v_readlane_b32 s43, v202, 11\n" \
"s_nop 4\n" \
"buffer_load_dwordx4 a[88:91], v201, s[8:11], s43 offen sc1\n" \
"buffer_load_dwordx4 a[92:95], v201, s[8:11], s43 offen offset:1024 sc1\n" \
"v_readlane_b32 s43, v202, 18\n" \
"s_nop 4\n" \
"buffer_load_dwordx4 a[144:147], v201, s[8:11], s43 offen sc1\n" \
"buffer_load_dwordx4 a[148:151], v201, s[8:11], s43 offen offset:1024 sc1\n" \
"v_readlane_b32 s43, v202, 25\n" \
"s_nop 4\n" \
"buffer_load_dwordx4 a[200:203], v201, s[8:11], s43 offen sc1\n" \
"buffer_load_dwordx4 a[204:207], v201, s[8:11], s43 offen offset:1024 sc1\n" \
"s_add_u32 s43, s30, 3\n" \
"s_mov_b32 s50, 0\n" \
"s_branch .LP3_w48c\n" \
".LP3_w48l:\n" \
"buffer_load_dword v240, v204, s[4:7], 0 offen sc1\n" \
"s_waitcnt vmcnt(0)\n" \
".LP3_w48c:\n" \
"v_subrev_u32 v241, s43, v240\n" \
"v_cmp_gt_u32 vcc, 0x100, v241\n" \
"s_cmp_eq_u64 vcc, -1\n" \
"s_cbranch_scc1 .LP3_w48d\n" \
"s_sleep 2\n" \
"s_add_u32 s50, s50, 1\n" \
"s_cmp_lt_u32 s50, 0x20000\n" \
"s_cbranch_scc1 .LP3_w48l\n" \
"s_branch .LP3_end\n" \
".LP3_w48d:\n" \
"v_readlane_b32 s43, v202, 2\n" \
"s_nop 4\n" \
"buffer_load_dwordx4 a[16:19], v201, s[8:11], s43 offen sc1\n" \
"buffer_load_dwordx4 a[20:23], v201, s[8:11], s43 offen offset:1024 sc1\n" \
"v_readlane_b32 s43, v202, 9\n" \
"s_nop 4\n" \
"buffer_load_dwordx4 a[72:75], v201, s[8:11], s43 offen sc1\n" \
"buffer_load_dwordx4 a[76:79], v201, s[8:11], s43 offen offset:1024 sc1\n" \
"v_readlane_b32 s43, v202, 16\n" \
"s_nop 4\n" \
"buffer_load_dwordx4 a[128:131], v201, s[8:11], s43 offen sc1\n" \
"buffer_load_dwordx4 a[132:135], v201, s[8:11], s43 offen offset:1024 sc1\n" \
"v_readlane_b32 s43, v202, 23\n" \
"s_nop 4\n" \
"buffer_load_dwordx4 a[184:187], v201, s[8:11], s43 offen sc1\n" \
"buffer_load_dwordx4 a[188:191], v201, s[8:11], s43 offen offset:1024 sc1\n" \
"s_add_u32 s43, s30, 4\n" \
"s_mov_b32 s50, 0\n" \
"s_branch .LP3_w49c\n" \
".LP3_w49l:\n" \
"buffer_load_dword v240, v204, s[4:7], 0 offen sc1\n" \
"s_waitcnt vmcnt(0)\n" \
".LP3_w49c:\n" \
"v_subrev_u32 v241, s43, v240\n" \
"v_cmp_gt_u32 vcc, 0x100, v241\n" \
"s_cmp_eq_u64 vcc, -1\n" \
"s_cbranch_scc1 .LP3_w49d\n" \
"s_sleep 2\n" \
"s_add_u32 s50, s50, 1\n" \
"s_cmp_lt_u32 s50, 0x20000\n" \
"s_cbranch_scc1 .LP3_w49l\n" \
"s_branch .LP3_end\n" \
".LP3_w49d:\n" \
"v_readlane_b32 s43, v202, 5\n" \
"s_nop 4\n" \
"buffer_load_dwordx4 a[40:43], v201, s[8:11], s43 offen sc1\n" \
"buffer_load_dwordx4 a[44:47], v201, s[8:11], s43 offen offset:1024 sc1\n" \
"v_readlane_b32 s43, v202, 12\n" \
"s_nop 4\n" \
"buffer_load_dwordx4 a[96:99], v201, s[8:11], s43 offen sc1\n" \
"buffer_load_dwordx4 a[100:103], v201, s[8:11], s43 offen offset:1024 sc1\n" \
"v_readlane_b32 s43, v202, 19\n" \
"s_nop 4\n" \
"buffer_load_dwordx4 a[152:155], v201, s[8:11], s43 offen sc1\n" \
"buffer_load_dwordx4 a[156:159], v201, s[8:11], s43 offen offset:1024 sc1\n" \
"v_readlane_b32 s43, v202, 26\n" \
"s_nop 4\n" \
"buffer_load_dwordx4 a[208:211], v201, s[8:11], s43 offen sc1\n" \
"buffer_load_dwordx4 a[212:215], v201, s[8:11], s43 offen offset:1024 sc1\n" \
"s_add_u32 s43, s30, 5\n" \
"s_mov_b32 s50, 0\n" \
"s_branch .LP3_w50c\n" \
".LP3_w50l:\n" \
"buffer_load_dword v240, v204, s[4:7], 0 offen sc1\n" \
"s_waitcnt vmcnt(0)\n" \
".LP3_w50c:\n" \
"v_subrev_u32 v241, s43, v240\n" \
"v_cmp_gt_u32 vcc, 0x100, v241\n" \
"s_cmp_eq_u64 vcc, -1\n" \
"s_cbranch_scc1 .LP3_w50d\n" \
"s_sleep 2\n" \
"s_add_u32 s50, s50, 1\n" \
"s_cmp_lt_u32 s50, 0x20000\n" \
"s_cbranch_scc1 .LP3_w50l\n" \
"s_branch .LP3_end\n" \
".LP3_w50d:\n" \
"v_readlane_b32 s43, v202, 3\n" \
"s_nop 4\n" \
"buffer_load_dwordx4 a[24:27], v201, s[8:11], s43 offen sc1\n" \
"buffer_load_dwordx4 a[28:31], v201, s[8:11], s43 offen offset:1024 sc1\n" \
"v_readlane_b32 s43, v202, 10\n" \
"s_nop 4\n" \
"buffer_load_dwordx4 a[80:83], v201, s[8:11], s43 offen sc1\n" \
"buffer_load_dwordx4 a[84:87], v201, s[8:11], s43 offen offset:1024 sc1\n" \
"v_readlane_b32 s43, v202, 17\n" \
"s_nop 4\n" \
"buffer_load_dwordx4 a[136:139], v201, s[8:11], s43 offen sc1\n" \
"buffer_load_dwordx4 a[140:143], v201, s[8:11], s43 offen offset:1024 sc1\n" \
"v_readlane_b32 s43, v202, 24\n" \
"s_nop 4\n" \
"buffer_load_dwordx4 a[192:195], v201, s[8:11], s43 offen sc1\n" \
"buffer_load_dwordx4 a[196:199], v201, s[8:11], s43 offen offset:1024 sc1\n" \
"s_add_u32 s43, s30, 6\n" \
"s_mov_b32 s50, 0\n" \
"s_branch .LP3_w51c\n" \
".LP3_w51l:\n" \
"buffer_load_dword v240, v204, s[4:7], 0 offen sc1\n" \
"s_waitcnt vmcnt(0)\n" \
".LP3_w51c:\n" \
"v_subrev_u32 v241, s43, v240\n" \
"v_cmp_gt_u32 vcc, 0x100, v241\n" \
"s_cmp_eq_u64 vcc, -1\n" \
"s_cbranch_scc1 .LP3_w51d\n" \
"s_sleep 2\n" \
"s_add_u32 s50, s50, 1\n" \
"s_cmp_lt_u32 s50, 0x20000\n" \
"s_cbranch_scc1 .LP3_w51l\n" \
"s_branch .LP3_end\n" \
".LP3_w51d:\n" \
"v_readlane_b32 s43, v202, 6\n" \
"s_nop 4\n" \
"buffer_load_dwordx4 a[48:51], v201, s[8:11], s43 offen sc1\n" \
"buffer_load_dwordx4 a[52:55], v201, s[8:11], s43 offen offset:1024 sc1\n" \
"v_readlane_b32 s43, v202, 13\n" \
"s_nop 4\n" \
"buffer_load_dwordx4 a[104:107], v201, s[8:11], s43 offen sc1\n" \
"buffer_load_dwordx4 a[108:111], v201, s[8:11], s43 offen offset:1024 sc1\n" \
"v_readlane_b32 s43, v202, 20\n" \
"s_nop 4\n" \
"buffer_load_dwordx4 a[160:163], v201, s[8:11], s43 offen sc1\n" \
"buffer_load_dwordx4 a[164:167], v201, s[8:11], s43 offen offset:1024 sc1\n" \
"v_readlane_b32 s43, v202, 27\n" \
"s_nop 4\n" \
"buffer_load_dwordx4 a[216:219], v201, s[8:11], s43 offen sc1\n" \
"buffer_load_dwordx4 a[220:223], v201, s[8:11], s43 offen offset:1024 sc1\n" \
"s_add_u32 s43, s30, 7\n" \
"s_mov_b32 s50, 0\n" \
"s_branch .LP3_w52c\n" \
".LP3_w52l:\n" \
"buffer_load_dword v240, v204, s[4:7], 0 offen sc1\n" \
"s_waitcnt vmcnt(0)\n" \
".LP3_w52c:\n" \
"v_subrev_u32 v241, s43, v240\n" \
"v_cmp_gt_u32 vcc, 0x100, v241\n" \
"s_cmp_eq_u64 vcc, -1\n" \
"s_cbranch_scc1 .LP3_w52d\n" \
"s_sleep 2\n" \
"s_add_u32 s50, s50, 1\n" \
"s_cmp_lt_u32 s50, 0x20000\n" \
"s_cbranch_scc1 .LP3_w52l\n" \
"s_branch .LP3_end\n" \
".LP3_w52d:\n" \
"v_readlane_b32 s43, v202, 0\n" \
"s_nop 4\n" \
"buffer_load_dwordx4 a[0:3], v201, s[8:11], s43 offen sc1\n" \
"buffer_load_dwordx4 a[4:7], v201, s[8:11], s43 offen offset:1024 sc1\n" \
"v_readlane_b32 s43, v202, 7\n" \
"s_nop 4\n" \
"buffer_load_dwordx4 a[56:59], v201, s[8:11], s43 offen sc1\n" \
"buffer_load_dwordx4 a[60:63], v201, s[8:11], s43 offen offset:1024 sc1\n" \
"v_readlane_b32 s43, v202, 14\n" \
"s_nop 4\n" \
"buffer_load_dwordx4 a[112:115], v201, s[8:11], s43 offen sc1\n" \
"buffer_load_dwordx4 a[116:119], v201, s[8:11], s43 offen offset:1024 sc1\n" \
"v_readlane_b32 s43, v202, 21\n" \
"s_nop 4\n" \
"buffer_load_dwordx4 a[168:171], v201, s[8:11], s43 offen sc1\n" \
"buffer_load_dwordx4 a[172:175], v201, s[8:11], s43 offen offset:1024 sc1\n" \
"ds_read_b96 v[248:250], v252\n" \
"s_waitcnt lgkmcnt(0)\n" \
"v_readfirstlane_b32 s44, v248\n" \
"v_readfirstlane_b32 s45, v249\n" \
"v_readfirstlane_b32 s46, v250\n" \
"s_mul_i32 s42, s25, s46\n" \
"s_lshr_b32 s42, s42, 12\n" \
"s_mov_b32 s26, s44\n" \
"s_mov_b32 s27, s45\n" \
".p2align 3\n" \
"s_memtime s[54:55]\n" \
"s_memrealtime s[56:57]\n" \
"s_memtime s[58:59]\n" \
"s_memrealtime s[60:61]\n" \
"s_nop 0\n" \
"s_nop 0\n" \
"s_memtime s[62:63]\n" \
"s_memrealtime s[64:65]\n" \
"s_memtime s[66:67]\n" \
"s_memrealtime s[68:69]\n" \
"s_memtime s[70:71]\n" \
"s_memrealtime s[72:73]\n" \
"s_memtime s[74:75]\n" \
"s_memrealtime s[76:77]\n" \
"s_nop 0\n" \
"s_nop 0\n" \
"s_memtime s[78:79]\n" \
"s_memrealtime s[80:81]\n" \
"s_memtime s[82:83]\n" \
"s_memrealtime s[84:85]\n" \
"s_memtime s[86:87]\n" \
"s_memrealtime s[88:89]\n" \
"s_memtime s[90:91]\n" \
"s_memrealtime s[92:93]\n" \
"s_nop 0\n" \
"s_nop 0\n" \
"s_memtime s[94:95]\n" \
"s_memrealtime s[96:97]\n" \
"s_memtime s[98:99]\n" \
"s_mov_b32 s52, 0\n" \
"s_mov_b32 s53, 4096\n" \
"s_waitcnt lgkmcnt(0)\n" \
"s_cmp_lg_u32 s96, s56\n" \
"s_cselect_b32 s52, s90, s52\n" \
"s_cselect_b32 s53, s98, s53\n" \
"s_cmp_lg_u32 s92, s56\n" \
"s_cselect_b32 s52, s86, s52\n" \
"s_cselect_b32 s53, s94, s53\n" \
"s_cmp_lg_u32 s88, s56\n" \
"s_cselect_b32 s52, s82, s52\n" \
"s_cselect_b32 s53, s90, s53\n" \
"s_cmp_lg_u32 s84, s56\n" \
"s_cselect_b32 s52, s78, s52\n" \
"s_cselect_b32 s53, s86, s53\n" \
"s_cmp_lg_u32 s80, s56\n" \
"s_cselect_b32 s52, s74, s52\n" \
"s_cselect_b32 s53, s82, s53\n" \
"s_cmp_lg_u32 s76, s56\n" \
"s_cselect_b32 s52, s70, s52\n" \
"s_cselect_b32 s53, s78, s53\n" \
"s_cmp_lg_u32 s72, s56\n" \
"s_cselect_b32 s52, s66, s52\n" \
"s_cselect_b32 s53, s74, s53\n" \
"s_cmp_lg_u32 s68, s56\n" \
"s_cselect_b32 s52, s62, s52\n" \
"s_cselect_b32 s53, s70, s53\n" \
"s_cmp_lg_u32 s64, s56\n" \
"s_cselect_b32 s52, s58, s52\n" \
"s_cselect_b32 s53, s66, s53\n" \
"s_cmp_lg_u32 s60, s56\n" \
"s_cselect_b32 s52, s54, s52\n" \
"s_cselect_b32 s53, s62, s53\n" \
"s_sub_u32 s53, s53, s52\n" \
"s_lshr_b32 s55, s53, 1\n" \
"s_add_u32 s52, s52, s55\n" \
"s_add_u32 s55, s56, 4\n" \
"s_lshl_b32 s55, s55, 4\n" \
"s_cmp_le_u32 s53, 40\n" \
"s_cselect_b32 s44, s55, s44\n" \
"s_cselect_b32 s45, s52, s45\n" \
"v_mov_b32 v248, s44\n" \
"v_mov_b32 v249, s45\n" \
"v_mov_b32 v250, s46\n" \
"s_mov_b64 exec, 1\n" \
"ds_write_b96 v252, v[248:250]\n" \
"s_mov_b64 exec, -1\n" \
"s_sub_u32 s26, s44, s26\n" \
"s_sub_u32 s27, s45, s27\n" \
"v_cvt_f32_u32 v248, s26\n" \
"v_cvt_f32_u32 v249, s27\n" \
"v_rcp_f32 v248, v248\n" \
"v_mul_f32 v249, 0x457ae148, v249\n" \
"v_mul_f32 v248, v248, v249\n" \
"v_cvt_u32_f32 v248, v248\n" \
"s_nop 0\n" \
"v_readfirstlane_b32 s27, v248\n" \
"s_max_u32 s27, s27, 0x800\n" \
"s_min_u32 s27, s27, 0x2000\n" \
"s_cmp_gt_u32 s26, 1600\n" \
"s_cselect_b32 s46, s27, s46\n" \
".p2align 3\n" \
"s_memtime s[54:55]\n" \
"s_memrealtime s[56:57]\n" \
"s_memtime s[58:59]\n" \
"s_memrealtime s[60:61]\n" \
"s_nop 0\n" \
"s_nop 0\n" \
"s_memtime s[62:63]\n" \
"s_memrealtime s[64:65]\n" \
"s_memtime s[66:67]\n" \
"s_memrealtime s[68:69]\n" \
"s_memtime s[70:71]\n" \
"s_memrealtime s[72:73]\n" \
"s_memtime s[74:75]\n" \
"s_memrealtime s[76:77]\n" \
"s_nop 0\n" \
"s_nop 0\n" \
"s_memtime s[78:79]\n" \
"s_memrealtime s[80:81]\n" \
"s_memtime s[82:83]\n" \
"s_memrealtime s[84:85]\n" \
"s_memtime s[86:87]\n" \
"s_memrealtime s[88:89]\n" \
"s_memtime s[90:91]\n" \
"s_memrealtime s[92:93]\n" \
"s_nop 0\n" \
"s_nop 0\n" \
"s_memtime s[94:95]\n" \
"s_memrealtime s[96:97]\n" \
"s_memtime s[98:99]\n" \
"s_mov_b32 s52, 0\n" \
"s_mov_b32 s53, 4096\n" \
"s_waitcnt lgkmcnt(0)\n" \
"s_cmp_lg_u32 s96, s56\n" \
"s_cselect_b32 s52, s90, s52\n" \
"s_cselect_b32 s53, s98, s53\n" \
"s_cmp_lg_u32 s92, s56\n" \
"s_cselect_b32 s52, s86, s52\n" \
"s_cselect_b32 s53, s94, s53\n" \
"s_cmp_lg_u32 s88, s56\n" \
"s_cselect_b32 s52, s82, s52\n" \
"s_cselect_b32 s53, s90, s53\n" \
"s_cmp_lg_u32 s84, s56\n" \
"s_cselect_b32 s52, s78, s52\n" \
"s_cselect_b32 s53, s86, s53\n" \
"s_cmp_lg_u32 s80, s56\n" \
"s_cselect_b32 s52, s74, s52\n" \
"s_cselect_b32 s53, s82, s53\n" \
"s_cmp_lg_u32 s76, s56\n" \
"s_cselect_b32 s52, s70, s52\n" \
"s_cselect_b32 s53, s78, s53\n" \
"s_cmp_lg_u32 s72, s56\n" \
"s_cselect_b32 s52, s66, s52\n" \
"s_cselect_b32 s53, s74, s53\n" \
"s_cmp_lg_u32 s68, s56\n" \
"s_cselect_b32 s52, s62, s52\n" \
"s_cselect_b32 s53, s70, s53\n" \
"s_cmp_lg_u32 s64, s56\n" \
"s_cselect_b32 s52, s58, s52\n" \
"s_cselect_b32 s53, s66, s53\n" \
"s_cmp_lg_u32 s60, s56\n" \
"s_cselect_b32 s52, s54, s52\n" \
"s_cselect_b32 s53, s62, s53\n" \
"s_sub_u32 s53, s53, s52\n" \
"s_lshr_b32 s55, s53, 1\n" \
"s_add_u32 s52, s52, s55\n" \
"s_add_u32 s55, s56, 4\n" \
"s_lshl_b32 s55, s55, 4\n" \
"s_cmp_le_u32 s53, 40\n" \
"s_cselect_b32 s44, s55, s44\n" \
"s_cselect_b32 s45, s52, s45\n" \
"v_mov_b32 v248, s44\n" \
"v_mov_b32 v249, s45\n" \
"v_mov_b32 v250, s46\n" \
"s_mov_b64 exec, 1\n" \
"ds_write_b96 v252, v[248:250]\n" \
"s_mov_b64 exec, -1\n" \
"s_waitcnt lgkmcnt(0)\n" \
"s_add_u32 s43, s30, 1\n" \
"s_mov_b32 s50, 0\n" \
".LP3_w53l:\n" \
"buffer_load_dword v240, v205, s[4:7], 0 offen sc1\n" \
"s_waitcnt vmcnt(0)\n" \
".LP3_w53c:\n" \
"v_subrev_u32 v241, s43, v240\n" \
"v_cmp_gt_u32 vcc, 0x100, v241\n" \
"s_cmp_eq_u64 vcc, -1\n" \
"s_cbranch_scc1 .LP3_w53d\n" \
"s_sleep 2\n" \
"s_add_u32 s50, s50, 1\n" \
"s_cmp_lt_u32 s50, 0x20000\n" \
"s_cbranch_scc1 .LP3_w53l\n" \
"s_branch .LP3_end\n" \
".LP3_w53d:\n" \
"v_readfirstlane_b32 s31, v241\n" \
"s_lshr_b32 s32, s31, 2\n" \
"s_xor_b32 s29, s31, s32\n" \
"s_and_b32 s29, s29, 3\n" \
"s_lshl_b32 s29, s29, 13\n" \
"s_bitset1_b32 s29, 21\n" \
"s_and_b32 s31, s31, 3\n" \
"s_lshl_b32 s31, s31, 13\n" \
"s_bitcmp1_b32 s39, 0\n" \
"s_cselect_b32 s43, s29, 0\n" \
"s_xor_b32 s31, s31, s43\n" \
"s_xor_b32 s32, s31, 0x4000\n" \
"s_mov_b32 s39, 0\n" \
"s_add_u32 s43, s40, 0\n" \
"s_and_b32 s43, s43, 3\n" \
"s_bitcmp1_b32 s43, 0\n" \
"s_cselect_b32 s47, s32, s31\n" \
"s_lshl_b32 s43, s43, 19\n" \
"s_add_u32 s47, s47, s43\n" \
".LP3_pre:\n" \
"s_barrier\n" \
"ds_read_b96 v[248:250], v252\n" \
"s_waitcnt lgkmcnt(0)\n" \
"v_readfirstlane_b32 s44, v248\n" \
"v_readfirstlane_b32 s45, v249\n" \
"v_readfirstlane_b32 s46, v250\n" \
"s_mul_i32 s42, s25, s46\n" \
"s_lshr_b32 s42, s42, 12\n" \
".LP3_cu:\n" \
"s_sub_u32 s43, s24, s44\n" \
"s_cmp_lt_i32 s43, 0\n" \
"s_cbranch_scc0 .LP3_cud\n" \
"s_add_u32 s24, s24, s25\n" \
"s_xor_b32 s31, s31, s29\n" \
"s_xor_b32 s32, s32, s29\n" \
"s_xor_b32 s47, s47, s29\n" \
"s_add_u32 s39, s39, 1\n" \
"s_branch .LP3_cu\n" \
".LP3_cud:\n" \
"s_mov_b32 s3, s46\n" \
"s_mov_b32 s2, s46\n" \
"s_add_u32 s43, s40, 1\n" \
"s_and_b32 s43, s43, 3\n" \
"s_bitcmp1_b32 s43, 0\n" \
"s_cselect_b32 s48, s32, s31\n" \
"s_lshl_b32 s43, s43, 19\n" \
"s_add_u32 s48, s48, s43\n" \
"s_add_u32 s43, s40, 2\n" \
"s_and_b32 s43, s43, 3\n" \
"s_bitcmp1_b32 s43, 0\n" \
"s_cselect_b32 s49, s32, s31\n" \
"s_lshl_b32 s43, s43, 19\n" \
"s_add_u32 s49, s49, s43\n" \
"s_sub_u32 s43, s24, s44\n" \
"s_add_u32 s43, s43, s28\n" \
"s_max_i32 s43, s43, 0xfffd0000\n" \
"s_min_i32 s43, s43, 0x4000\n" \
"s_mul_i32 s43, s43, s46\n" \
"s_ashr_i32 s43, s43, 12\n" \
"s_add_u32 s41, s45, s43\n" \
"s_lshr_b32 s35, s42, 1\n" \
"s_sub_u32 s35, 0, s35\n" \
".LP3_loop:\n" \
"s_branch .LP3_m1\n" \
".LP3_retry1:\n" \
"s_add_u32 s51, s51, 1\n" \
"s_cmp_gt_u32 s51, 0x20000\n" \
"s_cbranch_scc1 .LP3_end\n" \
".LP3_m0:\n" \
"s_add_u32 s41, s41, s42\n" \
"s_add_u32 s24, s24, s25\n" \
"s_xor_b32 s31, s31, s29\n" \
"s_xor_b32 s32, s32, s29\n" \
"s_xor_b32 s47, s47, s29\n" \
"s_xor_b32 s48, s48, s29\n" \
"s_add_u32 s39, s39, 1\n" \
"s_bitcmp1_b32 s31, 21\n" \
"s_cselect_b32 s43, s29, 0\n" \
"v_mov_b32 v116, s43\n" \
"ds_write_b32 v252, v116 offset:16\n" \
".LP3_m1:\n" \
"s_memtime vcc\n" \
"s_waitcnt lgkmcnt(0)\n" \
"s_sub_u32 s43, s41, vcc_lo\n" \
"s_cmp_gt_i32 s43, 0\n" \
"s_cbranch_scc1 .LP3_m1\n" \
"s_cmp_lt_i32 s43, s35\n" \
"s_cbranch_scc1 .LP3_m0\n" \
"buffer_load_dwordx4 v[0:3], v208, s[4:7], s47 offen offset:3072 sc1\n" \
".p2align 3\n" \
"s_memtime s[54:55]\n" \
"s_memrealtime s[56:57]\n" \
"s_memtime s[58:59]\n" \
"s_memrealtime s[60:61]\n" \
"s_nop 0\n" \
"s_nop 0\n" \
"s_memtime s[62:63]\n" \
"s_memrealtime s[64:65]\n" \
"s_memtime s[66:67]\n" \
"s_memrealtime s[68:69]\n" \
"s_memtime s[70:71]\n" \
"s_memrealtime s[72:73]\n" \
"s_memtime s[74:75]\n" \
"s_memrealtime s[76:77]\n" \
"s_nop 0\n" \
"s_nop 0\n" \
"s_memtime s[78:79]\n" \
"s_memrealtime s[80:81]\n" \
"s_memtime s[82:83]\n" \
"s_memrealtime s[84:85]\n" \
"s_memtime s[86:87]\n" \
"s_memrealtime s[88:89]\n" \
"s_memtime s[90:91]\n" \
"s_memrealtime s[92:93]\n" \
"s_nop 0\n" \
"s_nop 0\n" \
"s_memtime s[94:95]\n" \
"s_memrealtime s[96:97]\n" \
"s_memtime s[98:99]\n" \
"s_mov_b32 s52, 0\n" \
"s_mov_b32 s53, 4096\n" \
"s_waitcnt lgkmcnt(0)\n" \
"s_cmp_lg_u32 s96, s56\n" \
"s_cselect_b32 s52, s90, s52\n" \
"s_cselect_b32 s53, s98, s53\n" \
"s_cmp_lg_u32 s92, s56\n" \
"s_cselect_b32 s52, s86, s52\n" \
"s_cselect_b32 s53, s94, s53\n" \
"s_cmp_lg_u32 s88, s56\n" \
"s_cselect_b32 s52, s82, s52\n" \
"s_cselect_b32 s53, s90, s53\n" \
"s_cmp_lg_u32 s84, s56\n" \
"s_cselect_b32 s52, s78, s52\n" \
"s_cselect_b32 s53, s86, s53\n" \
"s_cmp_lg_u32 s80, s56\n" \
"s_cselect_b32 s52, s74, s52\n" \
"s_cselect_b32 s53, s82, s53\n" \
"s_cmp_lg_u32 s76, s56\n" \
"s_cselect_b32 s52, s70, s52\n" \
"s_cselect_b32 s53, s78, s53\n" \
"s_cmp_lg_u32 s72, s56\n" \
"s_cselect_b32 s52, s66, s52\n" \
"s_cselect_b32 s53, s74, s53\n" \
"s_cmp_lg_u32 s68, s56\n" \
"s_cselect_b32 s52, s62, s52\n" \
"s_cselect_b32 s53, s70, s53\n" \
"s_cmp_lg_u32 s64, s56\n" \
"s_cselect_b32 s52, s58, s52\n" \
"s_cselect_b32 s53, s66, s53\n" \
"s_cmp_lg_u32 s60, s56\n" \
"s_cselect_b32 s52, s54, s52\n" \
"s_cselect_b32 s53, s62, s53\n" \
"s_sub_u32 s53, s53, s52\n" \
"s_lshr_b32 s55, s53, 1\n" \
"s_add_u32 s52, s52, s55\n" \
"s_add_u32 s55, s56, 4\n" \
"s_lshl_b32 s55, s55, 4\n" \
"s_sub_u32 s59, s55, s44\n" \
"s_cmp_lt_u32 s59, 2600\n" \
"s_cselect_b32 s63, 1, 2\n" \
"s_mul_i32 s59, s59, s46\n" \
"s_ashr_i32 s59, s59, 12\n" \
"s_add_u32 s59, s59, s45\n" \
"s_sub_u32 s59, s52, s59\n" \
"s_max_i32 s59, s59, -96\n" \
"s_min_i32 s59, s59, 96\n" \
"s_ashr_i32 s63, s59, s63\n" \
"s_add_u32 s63, s63, s46\n" \
"s_max_i32 s63, s63, 0x800\n" \
"s_min_i32 s63, s63, 0x2000\n" \
"s_cmp_le_u32 s53, 40\n" \
"s_cselect_b32 s44, s55, s44\n" \
"s_cselect_b32 s45, s52, s45\n" \
"s_cselect_b32 s46, s63, s46\n" \
"v_mov_b32 v248, s44\n" \
"v_mov_b32 v249, s45\n" \
"v_mov_b32 v250, s46\n" \
"s_mov_b64 exec, 1\n" \
"ds_write_b96 v252, v[248:250]\n" \
"s_mov_b64 exec, -1\n" \
"v_mov_b32 v28, 0\n" \
"v_mov_b32 v29, 0\n" \
"v_mov_b32 v30, 0\n" \
"v_mov_b32 v31, 0\n" \
"v_mov_b32 v32, 0\n" \
"v_mov_b32 v33, 0\n" \
"v_mov_b32 v34, 0\n" \
"v_mov_b32 v35, 0\n" \
"v_mov_b32 v36, 0\n" \
"v_mov_b32 v37, 0\n" \
"v_mov_b32 v38, 0\n" \
"v_mov_b32 v39, 0\n" \
"v_mov_b32 v40, 0\n" \
"v_mov_b32 v41, 0\n" \
"v_mov_b32 v42, 0\n" \
"v_mov_b32 v43, 0\n" \
"s_waitcnt vmcnt(0)\n" \
"ds_write_b128 v220, v[0:3] offset:3072\n" \
"v_max3_u32 v44, v0, v0, v1\n" \
"v_max3_u32 v44, v44, v2, v3\n" \
"v_cmp_eq_u32 vcc, -1, v44\n" \
"v_smfmac_f32_16x16x64_bf16 v[28:31], v[0:3], a[0:7], v200\n" \
"v_smfmac_f32_16x16x64_bf16 v[32:35], v[0:3], a[56:63], v200\n" \
"s_cbranch_vccnz .LP3_retry1\n" \
"s_waitcnt lgkmcnt(0)\n" \
"s_barrier\n" \
"ds_read_b128 v[4:7], v220 offset:0\n" \
"ds_read_b128 v[8:11], v220 offset:1024\n" \
"ds_read_b128 v[12:15], v220 offset:2048\n" \
"ds_read_b128 v[16:19], v220 offset:4096\n" \
"ds_read_b128 v[20:23], v220 offset:5120\n" \
"ds_read_b128 v[24:27], v220 offset:6144\n" \
"ds_read_b64 v[246:247], v252 offset:24\n" \
"ds_read_b96 v[248:250], v252\n" \
"s_mul_i32 s50, s40, 4096\n" \
"buffer_store_dwordx4 v[68:71], v218, s[12:15], s50 offen sc1\n" \
"buffer_store_dword v112, v213, s[4:7], s49 offen sc1\n" \
"s_xor_b32 s43, s49, s29\n" \
"buffer_store_dword v112, v213, s[4:7], s43 offen sc1\n" \
"v_smfmac_f32_16x16x64_bf16 v[36:39], v[0:3], a[112:119], v200\n" \
"v_smfmac_f32_16x16x64_bf16 v[40:43], v[0:3], a[168:175], v200\n" \
"s_waitcnt lgkmcnt(7)\n" \
"v_smfmac_f32_16x16x64_bf16 v[28:31], v[4:7], a[8:15], v200\n" \
"v_smfmac_f32_16x16x64_bf16 v[32:35], v[4:7], a[64:71], v200\n" \
"v_smfmac_f32_16x16x64_bf16 v[36:39], v[4:7], a[120:127], v200\n" \
"v_smfmac_f32_16x16x64_bf16 v[40:43], v[4:7], a[176:183], v200\n" \
"s_waitcnt lgkmcnt(6)\n" \
"v_smfmac_f32_16x16x64_bf16 v[28:31], v[8:11], a[16:23], v200\n" \
"v_smfmac_f32_16x16x64_bf16 v[32:35], v[8:11], a[72:79], v200\n" \
"v_smfmac_f32_16x16x64_bf16 v[36:39], v[8:11], a[128:135], v200\n" \
"v_smfmac_f32_16x16x64_bf16 v[40:43], v[8:11], a[184:191], v200\n" \
"s_waitcnt lgkmcnt(0)\n" \
"v_readfirstlane_b32 s44, v248\n" \
"v_readfirstlane_b32 s45, v249\n" \
"v_smfmac_f32_16x16x64_bf16 v[28:31], v[12:15], a[24:31], v200\n" \
"v_readfirstlane_b32 s46, v250\n" \
"v_readfirstlane_b32 s26, v246\n" \
"v_readfirstlane_b32 s27, v247\n" \
"v_smfmac_f32_16x16x64_bf16 v[32:35], v[12:15], a[80:87], v200\n" \
"s_mul_i32 s42, s25, s46\n" \
"s_lshr_b32 s42, s42, 12\n" \
"s_add_u32 s24, s24, s26\n" \
"v_smfmac_f32_16x16x64_bf16 v[36:39], v[12:15], a[136:143], v200\n" \
"s_sub_u32 s43, s24, s44\n" \
"s_max_i32 s43, s43, 0xfffd0000\n" \
"s_min_i32 s43, s43, 0x4000\n" \
"v_smfmac_f32_16x16x64_bf16 v[40:43], v[12:15], a[192:199], v200\n" \
"s_mul_i32 s43, s43, s46\n" \
"s_ashr_i32 s43, s43, 12\n" \
"s_add_u32 s41, s45, s43\n" \
"s_cmp_eq_u32 s40, 63\n" \
"s_cbranch_scc1 .LP3_last\n" \
"s_branch .LP3_z1\n" \
".LP3_z0:\n" \
"s_add_u32 s41, s41, s42\n" \
"s_add_u32 s24, s24, s25\n" \
"s_xor_b32 s31, s31, s29\n" \
"s_xor_b32 s32, s32, s29\n" \
"s_xor_b32 s47, s47, s29\n" \
"s_xor_b32 s48, s48, s29\n" \
"s_add_u32 s39, s39, 1\n" \
"s_bitcmp1_b32 s31, 21\n" \
"s_cselect_b32 s43, s29, 0\n" \
"v_mov_b32 v116, s43\n" \
"ds_write_b32 v252, v116 offset:16\n" \
".LP3_z1:\n" \
"s_memtime vcc\n" \
"s_waitcnt lgkmcnt(0)\n" \
"s_sub_u32 s43, s41, vcc_lo\n" \
"s_cmp_gt_i32 s43, 160\n" \
"s_cbranch_scc1 .LP3_z1\n" \
"s_cmp_lt_i32 s43, s37\n" \
"s_cbranch_scc1 .LP3_z0\n" \
"buffer_load_dwordx2 v[64:65], v211, s[4:7], s47 offen sc1\n" \
".p2align 6\n" \
".LP3_b1:\n" \
"s_memtime vcc\n" \
"s_waitcnt lgkmcnt(0)\n" \
"s_sub_u32 s43, s41, vcc_lo\n" \
"s_cmp_gt_i32 s43, 128\n" \
"s_cbranch_scc1 .LP3_b1\n" \
"s_cmp_lt_i32 s43, s37\n" \
"s_cbranch_scc1 .LP3_z0\n" \
"s_max_i32 s43, s43, 0\n" \
"s_and_b32 s43, s43, -4\n" \
"s_getpc_b64 vcc\n" \
".LP3_b3:\n" \
"s_sub_u32 s43, .LP3_b4-.LP3_b3, s43\n" \
"s_add_u32 vcc_lo, vcc_lo, s43\n" \
"s_addc_u32 vcc_hi, vcc_hi, 0\n" \
"s_setpc_b64 vcc\n" \
".rept 32\n" \
"s_nop 0\n" \
".endr\n" \
".LP3_b4:\n" \
"buffer_load_dwordx4 v[56:59], v210, s[4:7], s47 offen sc1\n" \
"buffer_load_dwordx4 v[60:63], v210, s[4:7], s47 offen offset:256 sc1\n" \
"v_smfmac_f32_16x16x64_bf16 v[28:31], v[16:19], a[32:39], v200\n" \
"v_smfmac_f32_16x16x64_bf16 v[32:35], v[16:19], a[88:95], v200\n" \
"v_smfmac_f32_16x16x64_bf16 v[36:39], v[16:19], a[144:151], v200\n" \
"v_smfmac_f32_16x16x64_bf16 v[40:43], v[16:19], a[200:207], v200\n" \
"buffer_load_dwordx4 v[50:53], v212, s[4:7], s47 offen sc1\n" \
"v_smfmac_f32_16x16x64_bf16 v[28:31], v[20:23], a[40:47], v200\n" \
"v_smfmac_f32_16x16x64_bf16 v[32:35], v[20:23], a[96:103], v200\n" \
"v_smfmac_f32_16x16x64_bf16 v[36:39], v[20:23], a[152:159], v200\n" \
"v_smfmac_f32_16x16x64_bf16 v[40:43], v[20:23], a[208:215], v200\n" \
"v_smfmac_f32_16x16x64_bf16 v[28:31], v[24:27], a[48:55], v200\n" \
"v_smfmac_f32_16x16x64_bf16 v[32:35], v[24:27], a[104:111], v200\n" \
"v_smfmac_f32_16x16x64_bf16 v[36:39], v[24:27], a[160:167], v200\n" \
"v_smfmac_f32_16x16x64_bf16 v[40:43], v[24:27], a[216:223], v200\n" \
"s_nop 2\n" \
"v_mov_b32 v245, v245\n" \
"v_mov_b32 v245, v245\n" \
"v_mov_b32 v245, v245\n" \
"v_permlane32_swap_b32 v28, v32\n" \
"v_permlane32_swap_b32 v29, v33\n" \
"v_permlane32_swap_b32 v30, v34\n" \
"v_permlane32_swap_b32 v31, v35\n" \
"v_add_f32 v28, v28, v32\n" \
"v_add_f32 v29, v29, v33\n" \
"v_add_f32 v30, v30, v34\n" \
"v_add_f32 v31, v31, v35\n" \
"v_cvt_pk_bf16_f32 v68, v28, v29\n" \
"v_cvt_pk_bf16_f32 v69, v30, v31\n" \
"v_permlane32_swap_b32 v36, v40\n" \
"v_permlane32_swap_b32 v37, v41\n" \
"v_permlane32_swap_b32 v38, v42\n" \
"v_permlane32_swap_b32 v39, v43\n" \
"v_add_f32 v36, v36, v40\n" \
"v_add_f32 v37, v37, v41\n" \
"v_add_f32 v38, v38, v42\n" \
"v_add_f32 v39, v39, v43\n" \
"v_cvt_pk_bf16_f32 v70, v36, v37\n" \
"v_cvt_pk_bf16_f32 v71, v38, v39\n" \
"v_mov_b32 v48, v68\n" \
"v_mov_b32 v49, v69\n" \
"v_mov_b32 v54, v70\n" \
"v_mov_b32 v55, v71\n" \
"s_nop 1\n" \
"v_permlane16_swap_b32 v48, v54\n" \
"v_permlane16_swap_b32 v49, v55\n" \
"s_nop 2\n" \
"v_mov_b32 v245, v245\n" \
".LP3_wait:\n" \
"s_waitcnt vmcnt(1)\n" \
"v_pk_add_f32 v[72:73], v[56:57], v[60:61]\n" \
"v_pk_add_f32 v[74:75], v[58:59], v[62:63]\n" \
"v_max3_f32 v77, v72, v73, v64\n" \
"v_max3_f32 v77, v77, v74, v75\n" \
"s_nop 1\n" \
"v_max_f32_dpp v76, v77, v77 row_ror:8 row_mask:0xf bank_mask:0xf\n" \
"v_pk_add_f32 v[72:73], v[72:73], v[76:77] op_sel_hi:[1,0] neg_lo:[0,1] neg_hi:[0,1]\n" \
"v_pk_add_f32 v[74:75], v[74:75], v[76:77] op_sel_hi:[1,0] neg_lo:[0,1] neg_hi:[0,1]\n" \
"v_sub_f32 v78, v64, v76\n" \
"v_exp_f32 v72, v72\n" \
"v_exp_f32 v73, v73\n" \
"v_exp_f32 v74, v74\n" \
"v_exp_f32 v75, v75\n" \
"v_exp_f32 v78, v78\n" \
"v_pk_add_f32 v[80:81], v[72:73], v[74:75]\n" \
"v_fma_f32 v80, v78, v65, v80\n" \
"v_add_f32 v81, v80, v81\n" \
"s_nop 1\n" \
"v_add_f32_dpp v80, v81, v81 row_ror:8 row_mask:0xf bank_mask:0xf\n" \
"v_rcp_f32 v80, v80\n" \
"s_nop 0\n" \
"v_mul_f32 v78, v78, v80\n" \
"v_pk_mul_f32 v[72:73], v[72:73], v[80:81] op_sel_hi:[1,0]\n" \
"v_pk_mul_f32 v[74:75], v[74:75], v[80:81] op_sel_hi:[1,0]\n" \
"v_cvt_pk_bf16_f32 v78, v78, v78\n" \
"v_cvt_pk_bf16_f32 v82, v72, v73\n" \
"v_cvt_pk_bf16_f32 v83, v74, v75\n" \
"v_and_b32 v84, v78, v214\n" \
"v_and_b32 v85, v78, v215\n" \
"v_cvt_pk_bf16_f32 v86, v72, v73\n" \
"s_waitcnt vmcnt(0)\n" \
"v_mfma_f32_16x16x32_bf16 v[88:91], v[48:51], v[82:85], 0\n" \
"v_cvt_pk_bf16_f32 v87, v74, v75\n" \
"s_nop 1\n" \
"v_mfma_f32_16x16x32_bf16 v[92:95], v[52:55], v[84:87], 0\n" \
"s_nop 0\n" \
"s_nop 0\n" \
"s_nop 0\n" \
"v_mov_b32 v245, v245\n" \
"s_nop 0\n" \
"s_nop 0\n" \
"s_nop 0\n" \
"v_mov_b32 v245, v245\n" \
"v_add_f32_dpp v96, v92, v88 row_ror:8 row_mask:0xf bank_mask:0x3\n" \
"v_add_f32_dpp v97, v93, v89 row_ror:8 row_mask:0xf bank_mask:0x3\n" \
"v_add_f32_dpp v96, v90, v94 row_ror:8 row_mask:0xf bank_mask:0xc\n" \
"v_add_f32_dpp v97, v91, v95 row_ror:8 row_mask:0xf bank_mask:0xc\n" \
"ds_write_b64 v216, v[96:97]\n" \
"v_cmp_u_f32 vcc, v96, v97\n" \
"s_nop 0\n" \
"v_mov_b32 v245, v245\n" \
"s_nop 2\n" \
"v_mov_b32 v245, v245\n" \
"s_cbranch_vccnz .LP3_cls\n" \
".LP3_clsr:\n" \
"s_waitcnt lgkmcnt(0)\n" \
"s_barrier\n" \
"s_add_u32 s40, s40, 1\n" \
"s_add_u32 s24, s24, s27\n" \
"s_mov_b32 s47, s48\n" \
"s_add_u32 s43, s40, 1\n" \
"s_and_b32 s43, s43, 3\n" \
"s_bitcmp1_b32 s43, 0\n" \
"s_cselect_b32 s48, s32, s31\n" \
"s_lshl_b32 s43, s43, 19\n" \
"s_add_u32 s48, s48, s43\n" \
"s_add_u32 s43, s40, 2\n" \
"s_and_b32 s43, s43, 3\n" \
"s_bitcmp1_b32 s43, 0\n" \
"s_cselect_b32 s49, s32, s31\n" \
"s_lshl_b32 s43, s43, 19\n" \
"s_add_u32 s49, s49, s43\n" \
"s_sub_u32 s43, s24, s44\n" \
"s_add_u32 s43, s43, s28\n" \
"s_max_i32 s43, s43, 0xfffd0000\n" \
"s_min_i32 s43, s43, 0x4000\n" \
"s_mul_i32 s43, s43, s46\n" \
"s_ashr_i32 s43, s43, 12\n" \
"s_add_u32 s41, s45, s43\n" \
"s_lshr_b32 s35, s42, 1\n" \
"s_sub_u32 s35, 0, s35\n" \
"s_cmp_eq_u32 s40, 0\n" \
"s_cbranch_scc1 .LP3_last\n" \
"s_branch .LP3_loop\n" \
"s_branch .LP3_y1\n" \
".LP3_retry2:\n" \
"s_add_u32 s51, s51, 1\n" \
"s_cmp_gt_u32 s51, 0x20000\n" \
"s_cbranch_scc1 .LP3_end\n" \
".LP3_y0:\n" \
"s_add_u32 s41, s41, s42\n" \
"s_add_u32 s24, s24, s25\n" \
"s_xor_b32 s31, s31, s29\n" \
"s_xor_b32 s32, s32, s29\n" \
"s_xor_b32 s47, s47, s29\n" \
"s_xor_b32 s48, s48, s29\n" \
"s_add_u32 s39, s39, 1\n" \
"buffer_store_dwordx4 v[112:115], v223, s[4:7], s48 offen sc1\n" \
"s_bitcmp1_b32 s31, 21\n" \
"s_cselect_b32 s43, s29, 0\n" \
"v_mov_b32 v116, s43\n" \
"ds_write_b32 v252, v116 offset:16\n" \
".LP3_y1:\n" \
"s_memtime vcc\n" \
"s_waitcnt lgkmcnt(0)\n" \
"s_sub_u32 s43, s41, vcc_lo\n" \
"s_cmp_gt_i32 s43, 160\n" \
"s_cbranch_scc1 .LP3_y1\n" \
"s_cmp_lt_i32 s43, s37\n" \
"s_cbranch_scc1 .LP3_y0\n" \
"buffer_load_dwordx2 v[64:65], v211, s[4:7], s47 offen sc1\n" \
".p2align 6\n" \
".LP3_c1:\n" \
"s_memtime vcc\n" \
"s_waitcnt lgkmcnt(0)\n" \
"s_sub_u32 s43, s41, vcc_lo\n" \
"s_cmp_gt_i32 s43, 128\n" \
"s_cbranch_scc1 .LP3_c1\n" \
"s_cmp_lt_i32 s43, s37\n" \
"s_cbranch_scc1 .LP3_y0\n" \
"s_max_i32 s43, s43, 0\n" \
"s_and_b32 s43, s43, -4\n" \
"s_getpc_b64 vcc\n" \
".LP3_c3:\n" \
"s_sub_u32 s43, .LP3_c4-.LP3_c3, s43\n" \
"s_add_u32 vcc_lo, vcc_lo, s43\n" \
"s_addc_u32 vcc_hi, vcc_hi, 0\n" \
"s_setpc_b64 vcc\n" \
".rept 32\n" \
"s_nop 0\n" \
".endr\n" \
".LP3_c4:\n" \
"buffer_load_dwordx4 v[56:59], v210, s[4:7], s47 offen sc1\n" \
"buffer_load_dwordx4 v[60:63], v210, s[4:7], s47 offen offset:256 sc1\n" \
"s_nop 2\n" \
"v_mov_b32 v245, v245\n" \
"s_nop 2\n" \
"v_mov_b32 v245, v245\n" \
"s_nop 2\n" \
"v_mov_b32 v245, v245\n" \
"s_nop 2\n" \
"v_mov_b32 v245, v245\n" \
"buffer_load_dwordx4 v[50:53], v212, s[4:7], s47 offen sc1\n" \
"s_nop 2\n" \
"v_mov_b32 v245, v245\n" \
"s_nop 2\n" \
"v_mov_b32 v245, v245\n" \
"s_nop 2\n" \
"v_mov_b32 v245, v245\n" \
"s_nop 2\n" \
"v_mov_b32 v245, v245\n" \
"s_nop 2\n" \
"v_mov_b32 v245, v245\n" \
"s_nop 2\n" \
"v_mov_b32 v245, v245\n" \
"s_nop 2\n" \
"v_mov_b32 v245, v245\n" \
"s_nop 2\n" \
"v_mov_b32 v245, v245\n" \
"s_nop 2\n" \
"v_mov_b32 v245, v245\n" \
"s_nop 2\n" \
"v_mov_b32 v245, v245\n" \
"s_nop 2\n" \
"v_mov_b32 v245, v245\n" \
"s_nop 2\n" \
"v_mov_b32 v245, v245\n" \
"s_nop 2\n" \
"v_mov_b32 v245, v245\n" \
"s_nop 2\n" \
"v_mov_b32 v245, v245\n" \
"s_nop 2\n" \
"v_mov_b32 v245, v245\n" \
"s_nop 2\n" \
"v_mov_b32 v245, v245\n" \
"s_nop 2\n" \
"v_mov_b32 v245, v245\n" \
"s_nop 2\n" \
"v_mov_b32 v245, v245\n" \
"s_nop 2\n" \
"v_mov_b32 v245, v245\n" \
"s_nop 2\n" \
"v_mov_b32 v245, v245\n" \
"s_branch .LP3_wait\n" \
".LP3_cls:\n" \
"s_waitcnt vmcnt(0)\n" \
"v_max3_u32 v44, v64, v64, v65\n" \
"v_max3_u32 v44, v44, v50, v51\n" \
"v_max3_u32 v44, v44, v52, v53\n" \
"v_cmp_eq_u32 vcc, -1, v44\n" \
"s_cbranch_vccnz .LP3_retry2\n" \
"v_cmp_u_f32 vcc, v56, v56\n" \
"v_cmp_ne_u32 s[56:57], v56, v109\n" \
"s_and_b64 vcc, vcc, s[56:57]\n" \
"s_cbranch_vccnz .LP3_retry2\n" \
"v_cmp_u_f32 vcc, v57, v57\n" \
"v_cmp_ne_u32 s[56:57], v57, v109\n" \
"s_and_b64 vcc, vcc, s[56:57]\n" \
"s_cbranch_vccnz .LP3_retry2\n" \
"v_cmp_u_f32 vcc, v58, v58\n" \
"v_cmp_ne_u32 s[56:57], v58, v109\n" \
"s_and_b64 vcc, vcc, s[56:57]\n" \
"s_cbranch_vccnz .LP3_retry2\n" \
"v_cmp_u_f32 vcc, v59, v59\n" \
"v_cmp_ne_u32 s[56:57], v59, v109\n" \
"s_and_b64 vcc, vcc, s[56:57]\n" \
"s_cbranch_vccnz .LP3_retry2\n" \
"v_cmp_u_f32 vcc, v60, v60\n" \
"v_cmp_ne_u32 s[56:57], v60, v109\n" \
"s_and_b64 vcc, vcc, s[56:57]\n" \
"s_cbranch_vccnz .LP3_retry2\n" \
"v_cmp_u_f32 vcc, v61, v61\n" \
"v_cmp_ne_u32 s[56:57], v61, v109\n" \
"s_and_b64 vcc, vcc, s[56:57]\n" \
"s_cbranch_vccnz .LP3_retry2\n" \
"v_cmp_u_f32 vcc, v62, v62\n" \
"v_cmp_ne_u32 s[56:57], v62, v109\n" \
"s_and_b64 vcc, vcc, s[56:57]\n" \
"s_cbranch_vccnz .LP3_retry2\n" \
"v_cmp_u_f32 vcc, v63, v63\n" \
"v_cmp_ne_u32 s[56:57], v63, v109\n" \
"s_and_b64 vcc, vcc, s[56:57]\n" \
"s_cbranch_vccnz .LP3_retry2\n" \
"s_branch .LP3_clsr\n" \
".LP3_last:\n" \
"v_smfmac_f32_16x16x64_bf16 v[28:31], v[16:19], a[32:39], v200\n" \
"v_smfmac_f32_16x16x64_bf16 v[32:35], v[16:19], a[88:95], v200\n" \
"v_smfmac_f32_16x16x64_bf16 v[36:39], v[16:19], a[144:151], v200\n" \
"v_smfmac_f32_16x16x64_bf16 v[40:43], v[16:19], a[200:207], v200\n" \
"v_smfmac_f32_16x16x64_bf16 v[28:31], v[20:23], a[40:47], v200\n" \
"v_smfmac_f32_16x16x64_bf16 v[32:35], v[20:23], a[96:103], v200\n" \
"v_smfmac_f32_16x16x64_bf16 v[36:39], v[20:23], a[152:159], v200\n" \
"v_smfmac_f32_16x16x64_bf16 v[40:43], v[20:23], a[208:215], v200\n" \
"v_smfmac_f32_16x16x64_bf16 v[28:31], v[24:27], a[48:55], v200\n" \
"v_smfmac_f32_16x16x64_bf16 v[32:35], v[24:27], a[104:111], v200\n" \
"v_smfmac_f32_16x16x64_bf16 v[36:39], v[24:27], a[160:167], v200\n" \
"v_smfmac_f32_16x16x64_bf16 v[40:43], v[24:27], a[216:223], v200\n" \
"s_nop 7\n" \
"v_permlane32_swap_b32 v28, v32\n" \
"v_permlane32_swap_b32 v29, v33\n" \
"v_permlane32_swap_b32 v30, v34\n" \
"v_permlane32_swap_b32 v31, v35\n" \
"v_add_f32 v28, v28, v32\n" \
"v_add_f32 v29, v29, v33\n" \
"v_add_f32 v30, v30, v34\n" \
"v_add_f32 v31, v31, v35\n" \
"v_cvt_pk_bf16_f32 v68, v28, v29\n" \
"v_cvt_pk_bf16_f32 v69, v30, v31\n" \
"v_permlane32_swap_b32 v36, v40\n" \
"v_permlane32_swap_b32 v37, v41\n" \
"v_permlane32_swap_b32 v38, v42\n" \
"v_permlane32_swap_b32 v39, v43\n" \
"v_add_f32 v36, v36, v40\n" \
"v_add_f32 v37, v37, v41\n" \
"v_add_f32 v38, v38, v42\n" \
"v_add_f32 v39, v39, v43\n" \
"v_cvt_pk_bf16_f32 v70, v36, v37\n" \
"v_cvt_pk_bf16_f32 v71, v38, v39\n" \
"s_mov_b32 s50, 262144\n" \
"buffer_store_dwordx4 v[68:71], v218, s[12:15], s50 offen sc1\n" \
"s_cmp_eq_u32 s40, 0\n" \
"s_cbranch_scc1 .LP3_wet\n" \
".LP3_end:\n" \
"s_endpgm\n" \
".p2align 6\n" \
"fk_role_X0:\n" \
"ds_read_b32 v200, v255 offset:0\n" \
"ds_read_b32 v201, v255 offset:256\n" \
"ds_read_b32 v202, v255 offset:512\n" \
"ds_read_b32 v203, v255 offset:768\n" \
"ds_read_b32 v204, v255 offset:1024\n" \
"ds_read_b32 v205, v255 offset:1280\n" \
"ds_read_b32 v206, v255 offset:1536\n" \
"ds_read_b32 v207, v255 offset:1792\n" \
"ds_read_b32 v208, v255 offset:2048\n" \
"ds_read_b32 v209, v255 offset:2304\n" \
"ds_read_b32 v210, v255 offset:2560\n" \
"ds_read_b32 v211, v255 offset:2816\n" \
"ds_read_b32 v212, v255 offset:3072\n" \
"ds_read_b32 v213, v255 offset:3328\n" \
"ds_read_b32 v214, v255 offset:3584\n" \
"ds_read_b32 v215, v255 offset:3840\n" \
"ds_read_b32 v216, v255 offset:4096\n" \
"ds_read_b32 v217, v255 offset:4352\n" \
"ds_read_b32 v218, v255 offset:4608\n" \
"ds_read_b32 v219, v255 offset:4864\n" \
"ds_read_b32 v220, v255 offset:5120\n" \
"ds_read_b32 v221, v255 offset:5376\n" \
"ds_read_b32 v222, v255 offset:5632\n" \
"ds_read_b32 v223, v255 offset:5888\n" \
"ds_read_b32 v224, v255 offset:6144\n" \
"ds_read_b32 v254, v255 offset:7936\n" \
"s_waitcnt lgkmcnt(0)\n" \
"v_readlane_b32 s4, v254, 0\n" \
"v_readlane_b32 s5, v254, 1\n" \
"v_readlane_b32 s6, v254, 2\n" \
"v_readlane_b32 s7, v254, 3\n" \
"v_readlane_b32 s8, v254, 4\n" \
"v_readlane_b32 s9, v254, 5\n" \
"v_readlane_b32 s10, v254, 6\n" \
"v_readlane_b32 s11, v254, 7\n" \
"v_readlane_b32 s12, v254, 8\n" \
"v_readlane_b32 s13, v254, 9\n" \
"v_readlane_b32 s14, v254, 10\n" \
"v_readlane_b32 s15, v254, 11\n" \
"v_readlane_b32 s16, v254, 12\n" \
"v_readlane_b32 s17, v254, 13\n" \
"v_readlane_b32 s18, v254, 14\n" \
"v_readlane_b32 s19, v254, 15\n" \
"v_readlane_b32 s20, v254, 16\n" \
"v_readlane_b32 s21, v254, 17\n" \
"v_readlane_b32 s22, v254, 18\n" \
"v_readlane_b32 s23, v254, 19\n" \
"v_readlane_b32 s24, v254, 20\n" \
"v_readlane_b32 s25, v254, 21\n" \
"v_readlane_b32 s26, v254, 22\n" \
"v_readlane_b32 s27, v254, 23\n" \
"v_readlane_b32 s28, v254, 24\n" \
"v_readlane_b32 s29, v254, 25\n" \
"v_readlane_b32 s30, v254, 26\n" \
"v_readlane_b32 s31, v254, 27\n" \
"v_readlane_b32 s32, v254, 28\n" \
"v_readlane_b32 s33, v254, 29\n" \
"v_readlane_b32 s34, v254, 30\n" \
"v_readlane_b32 s35, v254, 31\n" \
"v_readlane_b32 s36, v254, 32\n" \
"v_readlane_b32 s37, v254, 33\n" \
"v_readlane_b32 s38, v254, 34\n" \
"v_readlane_b32 s39, v254, 35\n" \
"s_mov_b32 s40, 0\n" \
"s_mov_b32 s51, 0\n" \
"s_nop 0\n" \
"v_mov_b32 v252, s38\n" \
"s_nop 4\n" \
"v_mov_b32 v100, -1\n" \
"v_mov_b32 v101, -1\n" \
"v_mov_b32 v102, -1\n" \
"v_mov_b32 v103, -1\n" \
"s_mov_b32 s35, -2\n" \
"s_mov_b32 s2, 0\n" \
"s_mov_b32 s3, -1\n" \
"ds_read_b32 v199, v255 offset:6656\n" \
"s_waitcnt lgkmcnt(0)\n" \
"v_writelane_b32 v251, s6, 0\n" \
"v_writelane_b32 v251, s10, 1\n" \
"v_writelane_b32 v251, s14, 2\n" \
"v_writelane_b32 v251, s18, 3\n" \
"s_mov_b32 s6, 0\n" \
"s_mov_b32 s10, 0\n" \
"s_mov_b32 s14, 0\n" \
"s_mov_b32 s18, 0\n" \
"s_mov_b32 s40, -1\n" \
"s_branch .LX0_pre\n" \
".LX0_wet:\n" \
"s_waitcnt vmcnt(0)\n" \
"v_readlane_b32 s6, v251, 0\n" \
"v_readlane_b32 s10, v251, 1\n" \
"v_readlane_b32 s14, v251, 2\n" \
"v_readlane_b32 s18, v251, 3\n" \
"s_nop 4\n" \
"s_add_u32 s43, s30, 1\n" \
"s_mov_b32 s50, 0\n" \
".LX0_w54l:\n" \
"buffer_load_dword v240, v204, s[4:7], 0 offen sc1\n" \
"s_waitcnt vmcnt(0)\n" \
".LX0_w54c:\n" \
"v_subrev_u32 v241, s43, v240\n" \
"v_cmp_gt_u32 vcc, 0x100, v241\n" \
"s_cmp_eq_u64 vcc, -1\n" \
"s_cbranch_scc1 .LX0_w54d\n" \
"s_sleep 2\n" \
"s_add_u32 s50, s50, 1\n" \
"s_cmp_lt_u32 s50, 0x20000\n" \
"s_cbranch_scc1 .LX0_w54l\n" \
"s_branch .LX0_end\n" \
".LX0_w54d:\n" \
"v_readlane_b32 s43, v202, 0\n" \
"s_nop 4\n" \
"buffer_load_dwordx4 a[0:3], v201, s[8:11], s43 offen sc1\n" \
"buffer_load_dwordx4 a[4:7], v201, s[8:11], s43 offen offset:1024 sc1\n" \
"v_readlane_b32 s43, v202, 7\n" \
"s_nop 4\n" \
"buffer_load_dwordx4 a[56:59], v201, s[8:11], s43 offen sc1\n" \
"buffer_load_dwordx4 a[60:63], v201, s[8:11], s43 offen offset:1024 sc1\n" \
"v_readlane_b32 s43, v202, 14\n" \
"s_nop 4\n" \
"buffer_load_dwordx4 a[112:115], v201, s[8:11], s43 offen sc1\n" \
"buffer_load_dwordx4 a[116:119], v201, s[8:11], s43 offen offset:1024 sc1\n" \
"v_readlane_b32 s43, v202, 21\n" \
"s_nop 4\n" \
"buffer_load_dwordx4 a[168:171], v201, s[8:11], s43 offen sc1\n" \
"buffer_load_dwordx4 a[172:175], v201, s[8:11], s43 offen offset:1024 sc1\n" \
"s_add_u32 s43, s30, 2\n" \
"s_mov_b32 s50, 0\n" \
"s_branch .LX0_w55c\n" \
".LX0_w55l:\n" \
"buffer_load_dword v240, v204, s[4:7], 0 offen sc1\n" \
"s_waitcnt vmcnt(0)\n" \
".LX0_w55c:\n" \
"v_subrev_u32 v241, s43, v240\n" \
"v_cmp_gt_u32 vcc, 0x100, v241\n" \
"s_cmp_eq_u64 vcc, -1\n" \
"s_cbranch_scc1 .LX0_w55d\n" \
"s_sleep 2\n" \
"s_add_u32 s50, s50, 1\n" \
"s_cmp_lt_u32 s50, 0x20000\n" \
"s_cbranch_scc1 .LX0_w55l\n" \
"s_branch .LX0_end\n" \
".LX0_w55d:\n" \
"v_readlane_b32 s43, v202, 4\n" \
"s_nop 4\n" \
"buffer_load_dwordx4 a[32:35], v201, s[8:11], s43 offen sc1\n" \
"buffer_load_dwordx4 a[36:39], v201, s[8:11], s43 offen offset:1024 sc1\n" \
"v_readlane_b32 s43, v202, 11\n" \
"s_nop 4\n" \
"buffer_load_dwordx4 a[88:91], v201, s[8:11], s43 offen sc1\n" \
"buffer_load_dwordx4 a[92:95], v201, s[8:11], s43 offen offset:1024 sc1\n" \
"v_readlane_b32 s43, v202, 18\n" \
"s_nop 4\n" \
"buffer_load_dwordx4 a[144:147], v201, s[8:11], s43 offen sc1\n" \
"buffer_load_dwordx4 a[148:151], v201, s[8:11], s43 offen offset:1024 sc1\n" \
"v_readlane_b32 s43, v202, 25\n" \
"s_nop 4\n" \
"buffer_load_dwordx4 a[200:203], v201, s[8:11], s43 offen sc1\n" \
"buffer_load_dwordx4 a[204:207], v201, s[8:11], s43 offen offset:1024 sc1\n" \
"s_add_u32 s43, s30, 3\n" \
"s_mov_b32 s50, 0\n" \
"s_branch .LX0_w56c\n" \
".LX0_w56l:\n" \
"buffer_load_dword v240, v204, s[4:7], 0 offen sc1\n" \
"s_waitcnt vmcnt(0)\n" \
".LX0_w56c:\n" \
"v_subrev_u32 v241, s43, v240\n" \
"v_cmp_gt_u32 vcc, 0x100, v241\n" \
"s_cmp_eq_u64 vcc, -1\n" \
"s_cbranch_scc1 .LX0_w56d\n" \
"s_sleep 2\n" \
"s_add_u32 s50, s50, 1\n" \
"s_cmp_lt_u32 s50, 0x20000\n" \
"s_cbranch_scc1 .LX0_w56l\n" \
"s_branch .LX0_end\n" \
".LX0_w56d:\n" \
"v_readlane_b32 s43, v202, 1\n" \
"s_nop 4\n" \
"buffer_load_dwordx4 a[8:11], v201, s[8:11], s43 offen sc1\n" \
"buffer_load_dwordx4 a[12:15], v201, s[8:11], s43 offen offset:1024 sc1\n" \
"v_readlane_b32 s43, v202, 8\n" \
"s_nop 4\n" \
"buffer_load_dwordx4 a[64:67], v201, s[8:11], s43 offen sc1\n" \
"buffer_load_dwordx4 a[68:71], v201, s[8:11], s43 offen offset:1024 sc1\n" \
"v_readlane_b32 s43, v202, 15\n" \
"s_nop 4\n" \
"buffer_load_dwordx4 a[120:123], v201, s[8:11], s43 offen sc1\n" \
"buffer_load_dwordx4 a[124:127], v201, s[8:11], s43 offen offset:1024 sc1\n" \
"v_readlane_b32 s43, v202, 22\n" \
"s_nop 4\n" \
"buffer_load_dwordx4 a[176:179], v201, s[8:11], s43 offen sc1\n" \
"buffer_load_dwordx4 a[180:183], v201, s[8:11], s43 offen offset:1024 sc1\n" \
"s_add_u32 s43, s30, 4\n" \
"s_mov_b32 s50, 0\n" \
"s_branch .LX0_w57c\n" \
".LX0_w57l:\n" \
"buffer_load_dword v240, v204, s[4:7], 0 offen sc1\n" \
"s_waitcnt vmcnt(0)\n" \
".LX0_w57c:\n" \
"v_subrev_u32 v241, s43, v240\n" \
"v_cmp_gt_u32 vcc, 0x100, v241\n" \
"s_cmp_eq_u64 vcc, -1\n" \
"s_cbranch_scc1 .LX0_w57d\n" \
"s_sleep 2\n" \
"s_add_u32 s50, s50, 1\n" \
"s_cmp_lt_u32 s50, 0x20000\n" \
"s_cbranch_scc1 .LX0_w57l\n" \
"s_branch .LX0_end\n" \
".LX0_w57d:\n" \
"v_readlane_b32 s43, v202, 5\n" \
"s_nop 4\n" \
"buffer_load_dwordx4 a[40:43], v201, s[8:11], s43 offen sc1\n" \
"buffer_load_dwordx4 a[44:47], v201, s[8:11], s43 offen offset:1024 sc1\n" \
"v_readlane_b32 s43, v202, 12\n" \
"s_nop 4\n" \
"buffer_load_dwordx4 a[96:99], v201, s[8:11], s43 offen sc1\n" \
"buffer_load_dwordx4 a[100:103], v201, s[8:11], s43 offen offset:1024 sc1\n" \
"v_readlane_b32 s43, v202, 19\n" \
"s_nop 4\n" \
"buffer_load_dwordx4 a[152:155], v201, s[8:11], s43 offen sc1\n" \
"buffer_load_dwordx4 a[156:159], v201, s[8:11], s43 offen offset:1024 sc1\n" \
"v_readlane_b32 s43, v202, 26\n" \
"s_nop 4\n" \
"buffer_load_dwordx4 a[208:211], v201, s[8:11], s43 offen sc1\n" \
"buffer_load_dwordx4 a[212:215], v201, s[8:11], s43 offen offset:1024 sc1\n" \
"s_add_u32 s43, s30, 5\n" \
"s_mov_b32 s50, 0\n" \
"s_branch .LX0_w58c\n" \
".LX0_w58l:\n" \
"buffer_load_dword v240, v204, s[4:7], 0 offen sc1\n" \
"s_waitcnt vmcnt(0)\n" \
".LX0_w58c:\n" \
"v_subrev_u32 v241, s43, v240\n" \
"v_cmp_gt_u32 vcc, 0x100, v241\n" \
"s_cmp_eq_u64 vcc, -1\n" \
"s_cbranch_scc1 .LX0_w58d\n" \
"s_sleep 2\n" \
"s_add_u32 s50, s50, 1\n" \
"s_cmp_lt_u32 s50, 0x20000\n" \
"s_cbranch_scc1 .LX0_w58l\n" \
"s_branch .LX0_end\n" \
".LX0_w58d:\n" \
"v_readlane_b32 s43, v202, 2\n" \
"s_nop 4\n" \
"buffer_load_dwordx4 a[16:19], v201, s[8:11], s43 offen sc1\n" \
"buffer_load_dwordx4 a[20:23], v201, s[8:11], s43 offen offset:1024 sc1\n" \
"v_readlane_b32 s43, v202, 9\n" \
"s_nop 4\n" \
"buffer_load_dwordx4 a[72:75], v201, s[8:11], s43 offen sc1\n" \
"buffer_load_dwordx4 a[76:79], v201, s[8:11], s43 offen offset:1024 sc1\n" \
"v_readlane_b32 s43, v202, 16\n" \
"s_nop 4\n" \
"buffer_load_dwordx4 a[128:131], v201, s[8:11], s43 offen sc1\n" \
"buffer_load_dwordx4 a[132:135], v201, s[8:11], s43 offen offset:1024 sc1\n" \
"v_readlane_b32 s43, v202, 23\n" \
"s_nop 4\n" \
"buffer_load_dwordx4 a[184:187], v201, s[8:11], s43 offen sc1\n" \
"buffer_load_dwordx4 a[188:191], v201, s[8:11], s43 offen offset:1024 sc1\n" \
"s_add_u32 s43, s30, 6\n" \
"s_mov_b32 s50, 0\n" \
"s_branch .LX0_w59c\n" \
".LX0_w59l:\n" \
"buffer_load_dword v240, v204, s[4:7], 0 offen sc1\n" \
"s_waitcnt vmcnt(0)\n" \
".LX0_w59c:\n" \
"v_subrev_u32 v241, s43, v240\n" \
"v_cmp_gt_u32 vcc, 0x100, v241\n" \
"s_cmp_eq_u64 vcc, -1\n" \
"s_cbranch_scc1 .LX0_w59d\n" \
"s_sleep 2\n" \
"s_add_u32 s50, s50, 1\n" \
"s_cmp_lt_u32 s50, 0x20000\n" \
"s_cbranch_scc1 .LX0_w59l\n" \
"s_branch .LX0_end\n" \
".LX0_w59d:\n" \
"v_readlane_b32 s43, v202, 6\n" \
"s_nop 4\n" \
"buffer_load_dwordx4 a[48:51], v201, s[8:11], s43 offen sc1\n" \
"buffer_load_dwordx4 a[52:55], v201, s[8:11], s43 offen offset:1024 sc1\n" \
"v_readlane_b32 s43, v202, 13\n" \
"s_nop 4\n" \
"buffer_load_dwordx4 a[104:107], v201, s[8:11], s43 offen sc1\n" \
"buffer_load_dwordx4 a[108:111], v201, s[8:11], s43 offen offset:1024 sc1\n" \
"v_readlane_b32 s43, v202, 20\n" \
"s_nop 4\n" \
"buffer_load_dwordx4 a[160:163], v201, s[8:11], s43 offen sc1\n" \
"buffer_load_dwordx4 a[164:167], v201, s[8:11], s43 offen offset:1024 sc1\n" \
"v_readlane_b32 s43, v202, 27\n" \
"s_nop 4\n" \
"buffer_load_dwordx4 a[216:219], v201, s[8:11], s43 offen sc1\n" \
"buffer_load_dwordx4 a[220:223], v201, s[8:11], s43 offen offset:1024 sc1\n" \
"s_add_u32 s43, s30, 7\n" \
"s_mov_b32 s50, 0\n" \
"s_branch .LX0_w60c\n" \
".LX0_w60l:\n" \
"buffer_load_dword v240, v204, s[4:7], 0 offen sc1\n" \
"s_waitcnt vmcnt(0)\n" \
".LX0_w60c:\n" \
"v_subrev_u32 v241, s43, v240\n" \
"v_cmp_gt_u32 vcc, 0x100, v241\n" \
"s_cmp_eq_u64 vcc, -1\n" \
"s_cbranch_scc1 .LX0_w60d\n" \
"s_sleep 2\n" \
"s_add_u32 s50, s50, 1\n" \
"s_cmp_lt_u32 s50, 0x20000\n" \
"s_cbranch_scc1 .LX0_w60l\n" \
"s_branch .LX0_end\n" \
".LX0_w60d:\n" \
"v_readlane_b32 s43, v202, 3\n" \
"s_nop 4\n" \
"buffer_load_dwordx4 a[24:27], v201, s[8:11], s43 offen sc1\n" \
"buffer_load_dwordx4 a[28:31], v201, s[8:11], s43 offen offset:1024 sc1\n" \
"v_readlane_b32 s43, v202, 10\n" \
"s_nop 4\n" \
"buffer_load_dwordx4 a[80:83], v201, s[8:11], s43 offen sc1\n" \
"buffer_load_dwordx4 a[84:87], v201, s[8:11], s43 offen offset:1024 sc1\n" \
"v_readlane_b32 s43, v202, 17\n" \
"s_nop 4\n" \
"buffer_load_dwordx4 a[136:139], v201, s[8:11], s43 offen sc1\n" \
"buffer_load_dwordx4 a[140:143], v201, s[8:11], s43 offen offset:1024 sc1\n" \
"v_readlane_b32 s43, v202, 24\n" \
"s_nop 4\n" \
"buffer_load_dwordx4 a[192:195], v201, s[8:11], s43 offen sc1\n" \
"buffer_load_dwordx4 a[196:199], v201, s[8:11], s43 offen offset:1024 sc1\n" \
"s_add_u32 s43, s30, 1\n" \
"s_mov_b32 s50, 0\n" \
".LX0_w61l:\n" \
"buffer_load_dword v240, v205, s[4:7], 0 offen sc1\n" \
"s_waitcnt vmcnt(0)\n" \
".LX0_w61c:\n" \
"v_subrev_u32 v241, s43, v240\n" \
"v_cmp_gt_u32 vcc, 0x100, v241\n" \
"s_cmp_eq_u64 vcc, -1\n" \
"s_cbranch_scc1 .LX0_w61d\n" \
"s_sleep 2\n" \
"s_add_u32 s50, s50, 1\n" \
"s_cmp_lt_u32 s50, 0x20000\n" \
"s_cbranch_scc1 .LX0_w61l\n" \
"s_branch .LX0_end\n" \
".LX0_w61d:\n" \
"v_readfirstlane_b32 s31, v241\n" \
"s_lshr_b32 s32, s31, 2\n" \
"s_xor_b32 s29, s31, s32\n" \
"s_and_b32 s29, s29, 3\n" \
"s_lshl_b32 s29, s29, 13\n" \
"s_bitset1_b32 s29, 21\n" \
"s_and_b32 s31, s31, 3\n" \
"s_lshl_b32 s31, s31, 13\n" \
"s_bitcmp1_b32 s39, 0\n" \
"s_cselect_b32 s43, s29, 0\n" \
"s_xor_b32 s31, s31, s43\n" \
"s_xor_b32 s32, s31, 0x4000\n" \
"s_mov_b32 s39, 0\n" \
"s_add_u32 s43, s30, 8\n" \
"s_mov_b32 s50, 0\n" \
".LX0_w62l:\n" \
"buffer_load_dword v240, v215, s[4:7], 0 offen sc1\n" \
"s_waitcnt vmcnt(0)\n" \
".LX0_w62c:\n" \
"v_subrev_u32 v241, s43, v240\n" \
"v_cmp_gt_u32 vcc, 0x100, v241\n" \
"s_cmp_eq_u64 vcc, -1\n" \
"s_cbranch_scc1 .LX0_w62d\n" \
"s_sleep 2\n" \
"s_add_u32 s50, s50, 1\n" \
"s_cmp_lt_u32 s50, 0x20000\n" \
"s_cbranch_scc1 .LX0_w62l\n" \
"s_branch .LX0_end\n" \
".LX0_w62d:\n" \
"s_add_u32 s50, s40, 0\n" \
"s_mul_i32 s50, s50, 28672\n" \
"s_add_u32 s50, s50, s34\n" \
"buffer_load_dwordx4 v[28:31], v201, s[12:15], s50 offen sc1\n" \
"buffer_load_dwordx4 v[32:35], v201, s[12:15], s50 offen offset:1024 sc1\n" \
"buffer_load_dwordx4 v[36:39], v201, s[12:15], s50 offen offset:2048 sc1\n" \
"buffer_load_dwordx4 v[40:43], v201, s[12:15], s50 offen offset:3072 sc1\n" \
"ds_read_b96 v[248:250], v252\n" \
"s_waitcnt lgkmcnt(0)\n" \
"v_readfirstlane_b32 s44, v248\n" \
"v_readfirstlane_b32 s45, v249\n" \
"v_readfirstlane_b32 s46, v250\n" \
"s_mul_i32 s42, s25, s46\n" \
"s_lshr_b32 s42, s42, 12\n" \
"s_mov_b32 s26, s44\n" \
"s_mov_b32 s27, s45\n" \
".p2align 3\n" \
"s_memtime s[54:55]\n" \
"s_memrealtime s[56:57]\n" \
"s_memtime s[58:59]\n" \
"s_memrealtime s[60:61]\n" \
"s_nop 0\n" \
"s_nop 0\n" \
"s_memtime s[62:63]\n" \
"s_memrealtime s[64:65]\n" \
"s_memtime s[66:67]\n" \
"s_memrealtime s[68:69]\n" \
"s_memtime s[70:71]\n" \
"s_memrealtime s[72:73]\n" \
"s_memtime s[74:75]\n" \
"s_memrealtime s[76:77]\n" \
"s_nop 0\n" \
"s_nop 0\n" \
"s_memtime s[78:79]\n" \
"s_memrealtime s[80:81]\n" \
"s_memtime s[82:83]\n" \
"s_memrealtime s[84:85]\n" \
"s_memtime s[86:87]\n" \
"s_memrealtime s[88:89]\n" \
"s_memtime s[90:91]\n" \
"s_memrealtime s[92:93]\n" \
"s_nop 0\n" \
"s_nop 0\n" \
"s_memtime s[94:95]\n" \
"s_memrealtime s[96:97]\n" \
"s_memtime s[98:99]\n" \
"s_mov_b32 s52, 0\n" \
"s_mov_b32 s53, 4096\n" \
"s_waitcnt lgkmcnt(0)\n" \
"s_cmp_lg_u32 s96, s56\n" \
"s_cselect_b32 s52, s90, s52\n" \
"s_cselect_b32 s53, s98, s53\n" \
"s_cmp_lg_u32 s92, s56\n" \
"s_cselect_b32 s52, s86, s52\n" \
"s_cselect_b32 s53, s94, s53\n" \
"s_cmp_lg_u32 s88, s56\n" \
"s_cselect_b32 s52, s82, s52\n" \
"s_cselect_b32 s53, s90, s53\n" \
"s_cmp_lg_u32 s84, s56\n" \
"s_cselect_b32 s52, s78, s52\n" \
"s_cselect_b32 s53, s86, s53\n" \
"s_cmp_lg_u32 s80, s56\n" \
"s_cselect_b32 s52, s74, s52\n" \
"s_cselect_b32 s53, s82, s53\n" \
"s_cmp_lg_u32 s76, s56\n" \
"s_cselect_b32 s52, s70, s52\n" \
"s_cselect_b32 s53, s78, s53\n" \
"s_cmp_lg_u32 s72, s56\n" \
"s_cselect_b32 s52, s66, s52\n" \
"s_cselect_b32 s53, s74, s53\n" \
"s_cmp_lg_u32 s68, s56\n" \
"s_cselect_b32 s52, s62, s52\n" \
"s_cselect_b32 s53, s70, s53\n" \
"s_cmp_lg_u32 s64, s56\n" \
"s_cselect_b32 s52, s58, s52\n" \
"s_cselect_b32 s53, s66, s53\n" \
"s_cmp_lg_u32 s60, s56\n" \
"s_cselect_b32 s52, s54, s52\n" \
"s_cselect_b32 s53, s62, s53\n" \
"s_sub_u32 s53, s53, s52\n" \
"s_lshr_b32 s55, s53, 1\n" \
"s_add_u32 s52, s52, s55\n" \
"s_add_u32 s55, s56, 4\n" \
"s_lshl_b32 s55, s55, 4\n" \
"s_cmp_le_u32 s53, 40\n" \
"s_cselect_b32 s44, s55, s44\n" \
"s_cselect_b32 s45, s52, s45\n" \
"v_mov_b32 v248, s44\n" \
"v_mov_b32 v249, s45\n" \
"v_mov_b32 v250, s46\n" \
"s_mov_b64 exec, 1\n" \
"ds_write_b96 v252, v[248:250]\n" \
"s_mov_b64 exec, -1\n" \
"s_sub_u32 s26, s44, s26\n" \
"s_sub_u32 s27, s45, s27\n" \
"v_cvt_f32_u32 v248, s26\n" \
"v_cvt_f32_u32 v249, s27\n" \
"v_rcp_f32 v248, v248\n" \
"v_mul_f32 v249, 0x457ae148, v249\n" \
"v_mul_f32 v248, v248, v249\n" \
"v_cvt_u32_f32 v248, v248\n" \
"s_nop 0\n" \
"v_readfirstlane_b32 s27, v248\n" \
"s_max_u32 s27, s27, 0x800\n" \
"s_min_u32 s27, s27, 0x2000\n" \
"s_cmp_gt_u32 s26, 1600\n" \
"s_cselect_b32 s46, s27, s46\n" \
".p2align 3\n" \
"s_memtime s[54:55]\n" \
"s_memrealtime s[56:57]\n" \
"s_memtime s[58:59]\n" \
"s_memrealtime s[60:61]\n" \
"s_nop 0\n" \
"s_nop 0\n" \
"s_memtime s[62:63]\n" \
"s_memrealtime s[64:65]\n" \
"s_memtime s[66:67]\n" \
"s_memrealtime s[68:69]\n" \
"s_memtime s[70:71]\n" \
"s_memrealtime s[72:73]\n" \
"s_memtime s[74:75]\n" \
"s_memrealtime s[76:77]\n" \
"s_nop 0\n" \
"s_nop 0\n" \
"s_memtime s[78:79]\n" \
"s_memrealtime s[80:81]\n" \
"s_memtime s[82:83]\n" \
"s_memrealtime s[84:85]\n" \
"s_memtime s[86:87]\n" \
"s_memrealtime s[88:89]\n" \
"s_memtime s[90:91]\n" \
"s_memrealtime s[92:93]\n" \
"s_nop 0\n" \
"s_nop 0\n" \
"s_memtime s[94:95]\n" \
"s_memrealtime s[96:97]\n" \
"s_memtime s[98:99]\n" \
"s_mov_b32 s52, 0\n" \
"s_mov_b32 s53, 4096\n" \
"s_waitcnt lgkmcnt(0)\n" \
"s_cmp_lg_u32 s96, s56\n" \
"s_cselect_b32 s52, s90, s52\n" \
"s_cselect_b32 s53, s98, s53\n" \
"s_cmp_lg_u32 s92, s56\n" \
"s_cselect_b32 s52, s86, s52\n" \
"s_cselect_b32 s53, s94, s53\n" \
"s_cmp_lg_u32 s88, s56\n" \
"s_cselect_b32 s52, s82, s52\n" \
"s_cselect_b32 s53, s90, s53\n" \
"s_cmp_lg_u32 s84, s56\n" \
"s_cselect_b32 s52, s78, s52\n" \
"s_cselect_b32 s53, s86, s53\n" \
"s_cmp_lg_u32 s80, s56\n" \
"s_cselect_b32 s52, s74, s52\n" \
"s_cselect_b32 s53, s82, s53\n" \
"s_cmp_lg_u32 s76, s56\n" \
"s_cselect_b32 s52, s70, s52\n" \
"s_cselect_b32 s53, s78, s53\n" \
"s_cmp_lg_u32 s72, s56\n" \
"s_cselect_b32 s52, s66, s52\n" \
"s_cselect_b32 s53, s74, s53\n" \
"s_cmp_lg_u32 s68, s56\n" \
"s_cselect_b32 s52, s62, s52\n" \
"s_cselect_b32 s53, s70, s53\n" \
"s_cmp_lg_u32 s64, s56\n" \
"s_cselect_b32 s52, s58, s52\n" \
"s_cselect_b32 s53, s66, s53\n" \
"s_cmp_lg_u32 s60, s56\n" \
"s_cselect_b32 s52, s54, s52\n" \
"s_cselect_b32 s53, s62, s53\n" \
"s_sub_u32 s53, s53, s52\n" \
"s_lshr_b32 s55, s53, 1\n" \
"s_add_u32 s52, s52, s55\n" \
"s_add_u32 s55, s56, 4\n" \
"s_lshl_b32 s55, s55, 4\n" \
"s_cmp_le_u32 s53, 40\n" \
"s_cselect_b32 s44, s55, s44\n" \
"s_cselect_b32 s45, s52, s45\n" \
"v_mov_b32 v248, s44\n" \
"v_mov_b32 v249, s45\n" \
"v_mov_b32 v250, s46\n" \
"s_mov_b64 exec, 1\n" \
"ds_write_b96 v252, v[248:250]\n" \
"s_mov_b64 exec, -1\n" \
"s_waitcnt lgkmcnt(0)\n" \
"s_waitcnt vmcnt(0)\n" \
"s_barrier\n" \
"s_add_u32 s43, s30, 9\n" \
"s_mov_b32 s50, 0\n" \
".LX0_w63l:\n" \
"buffer_load_dword v240, v215, s[4:7], 0 offen sc1\n" \
"s_waitcnt vmcnt(0)\n" \
".LX0_w63c:\n" \
"v_subrev_u32 v241, s43, v240\n" \
"v_cmp_gt_u32 vcc, 0x100, v241\n" \
"s_cmp_eq_u64 vcc, -1\n" \
"s_cbranch_scc1 .LX0_w63d\n" \
"s_sleep 2\n" \
"s_add_u32 s50, s50, 1\n" \
"s_cmp_lt_u32 s50, 0x20000\n" \
"s_cbranch_scc1 .LX0_w63l\n" \
"s_branch .LX0_end\n" \
".LX0_w63d:\n" \
".LX0_pre:\n" \
".LX0_loop:\n" \
"s_add_u32 s43, s40, 2\n" \
"s_and_b32 s43, s43, 3\n" \
"s_bitcmp1_b32 s43, 0\n" \
"s_cselect_b32 s49, s32, s31\n" \
"s_lshl_b32 s43, s43, 19\n" \
"s_add_u32 s49, s49, s43\n" \
"ds_read_b128 v[0:3], v216 offset:0\n" \
"ds_read_b128 v[4:7], v216 offset:1024\n" \
"ds_read_b128 v[8:11], v216 offset:2048\n" \
"ds_read_b128 v[12:15], v216 offset:3072\n" \
"ds_read_b128 v[16:19], v216 offset:4096\n" \
"ds_read_b128 v[20:23], v216 offset:5120\n" \
"ds_read_b128 v[24:27], v216 offset:6144\n" \
"v_mov_b32 v60, 0\n" \
"v_mov_b32 v61, 0\n" \
"v_mov_b32 v62, 0\n" \
"v_mov_b32 v63, 0\n" \
"v_mov_b32 v64, 0\n" \
"v_mov_b32 v65, 0\n" \
"v_mov_b32 v66, 0\n" \
"v_mov_b32 v67, 0\n" \
"v_mov_b32 v68, 0\n" \
"v_mov_b32 v69, 0\n" \
"v_mov_b32 v70, 0\n" \
"v_mov_b32 v71, 0\n" \
"v_mov_b32 v72, 0\n" \
"v_mov_b32 v73, 0\n" \
"v_mov_b32 v74, 0\n" \
"v_mov_b32 v75, 0\n" \
"s_nop 1\n" \
"s_waitcnt lgkmcnt(6)\n" \
"v_smfmac_f32_16x16x64_bf16 v[60:63], v[0:3], a[0:7], v200\n" \
"v_smfmac_f32_16x16x64_bf16 v[64:67], v[0:3], a[56:63], v200\n" \
"v_smfmac_f32_16x16x64_bf16 v[68:71], v[0:3], a[112:119], v200\n" \
"v_smfmac_f32_16x16x64_bf16 v[72:75], v[0:3], a[168:175], v200\n" \
"s_waitcnt lgkmcnt(5)\n" \
"v_smfmac_f32_16x16x64_bf16 v[60:63], v[4:7], a[8:15], v200\n" \
"v_smfmac_f32_16x16x64_bf16 v[64:67], v[4:7], a[64:71], v200\n" \
"v_smfmac_f32_16x16x64_bf16 v[68:71], v[4:7], a[120:127], v200\n" \
"v_smfmac_f32_16x16x64_bf16 v[72:75], v[4:7], a[176:183], v200\n" \
"s_waitcnt lgkmcnt(4)\n" \
"v_smfmac_f32_16x16x64_bf16 v[60:63], v[8:11], a[16:23], v200\n" \
"v_smfmac_f32_16x16x64_bf16 v[64:67], v[8:11], a[72:79], v200\n" \
"v_smfmac_f32_16x16x64_bf16 v[68:71], v[8:11], a[128:135], v200\n" \
"v_smfmac_f32_16x16x64_bf16 v[72:75], v[8:11], a[184:191], v200\n" \
"s_waitcnt lgkmcnt(3)\n" \
"v_smfmac_f32_16x16x64_bf16 v[60:63], v[12:15], a[24:31], v200\n" \
"v_smfmac_f32_16x16x64_bf16 v[64:67], v[12:15], a[80:87], v200\n" \
"v_smfmac_f32_16x16x64_bf16 v[68:71], v[12:15], a[136:143], v200\n" \
"v_smfmac_f32_16x16x64_bf16 v[72:75], v[12:15], a[192:199], v200\n" \
"s_waitcnt lgkmcnt(2)\n" \
"v_smfmac_f32_16x16x64_bf16 v[60:63], v[16:19], a[32:39], v200\n" \
"v_smfmac_f32_16x16x64_bf16 v[64:67], v[16:19], a[88:95], v200\n" \
"v_smfmac_f32_16x16x64_bf16 v[68:71], v[16:19], a[144:151], v200\n" \
"v_smfmac_f32_16x16x64_bf16 v[72:75], v[16:19], a[200:207], v200\n" \
"s_waitcnt lgkmcnt(1)\n" \
"v_smfmac_f32_16x16x64_bf16 v[60:63], v[20:23], a[40:47], v200\n" \
"v_smfmac_f32_16x16x64_bf16 v[64:67], v[20:23], a[96:103], v200\n" \
"v_smfmac_f32_16x16x64_bf16 v[68:71], v[20:23], a[152:159], v200\n" \
"v_smfmac_f32_16x16x64_bf16 v[72:75], v[20:23], a[208:215], v200\n" \
"s_waitcnt lgkmcnt(0)\n" \
"v_smfmac_f32_16x16x64_bf16 v[60:63], v[24:27], a[48:55], v200\n" \
"v_smfmac_f32_16x16x64_bf16 v[64:67], v[24:27], a[104:111], v200\n" \
"v_smfmac_f32_16x16x64_bf16 v[68:71], v[24:27], a[160:167], v200\n" \
"v_smfmac_f32_16x16x64_bf16 v[72:75], v[24:27], a[216:223], v200\n" \
"s_barrier\n" \
"s_add_u32 s50, s40, 1\n" \
"s_mul_i32 s50, s50, 28672\n" \
"s_add_u32 s50, s50, s34\n" \
"buffer_load_dwordx4 v[44:47], v201, s[12:15], s50 offen sc1\n" \
"buffer_load_dwordx4 v[48:51], v201, s[12:15], s50 offen offset:1024 sc1\n" \
"buffer_load_dwordx4 v[52:55], v201, s[12:15], s50 offen offset:2048 sc1\n" \
"buffer_load_dwordx4 v[56:59], v201, s[12:15], s50 offen offset:3072 sc1\n" \
"s_nop 7\n" \
"v_permlane32_swap_b32 v60, v64\n" \
"v_permlane32_swap_b32 v61, v65\n" \
"v_permlane32_swap_b32 v62, v66\n" \
"v_permlane32_swap_b32 v63, v67\n" \
"v_add_f32 v60, v60, v64\n" \
"v_add_f32 v61, v61, v65\n" \
"v_add_f32 v62, v62, v66\n" \
"v_add_f32 v63, v63, v67\n" \
"v_cvt_pk_bf16_f32 v184, v60, v61\n" \
"v_cvt_pk_bf16_f32 v185, v62, v63\n" \
"ds_write_b64 v217, v[184:185] offset:0\n" \
"v_permlane32_swap_b32 v68, v72\n" \
"v_permlane32_swap_b32 v69, v73\n" \
"v_permlane32_swap_b32 v70, v74\n" \
"v_permlane32_swap_b32 v71, v75\n" \
"v_add_f32 v68, v68, v72\n" \
"v_add_f32 v69, v69, v73\n" \
"v_add_f32 v70, v70, v74\n" \
"v_add_f32 v71, v71, v75\n" \
"v_cvt_pk_bf16_f32 v186, v68, v69\n" \
"v_cvt_pk_bf16_f32 v187, v70, v71\n" \
"ds_write_b64 v217, v[186:187] offset:512\n" \
"ds_read_b64_tr_b16 v[0:1], v218 offset:0\n" \
"ds_read_b64_tr_b16 v[2:3], v218 offset:64\n" \
"s_add_u32 s43, s40, 0\n" \
"s_mul_i32 s43, s43, 7168\n" \
"s_waitcnt lgkmcnt(0)\n" \
"buffer_store_dwordx4 v[0:3], v219, s[16:19], s43 offen sc1\n" \
"s_waitcnt vmcnt(5)\n" \
"s_barrier\n" \
"ds_read_b64_tr_b16 v[92:93], v213\n" \
"ds_read_b64_tr_b16 v[94:95], v213 offset:64\n" \
"ds_read_b32 v84, v252 offset:16\n" \
"v_mov_b32 v245, v245\n" \
"s_nop 2\n" \
"v_mov_b32 v245, v245\n" \
"s_nop 2\n" \
"v_mov_b32 v245, v245\n" \
"s_waitcnt lgkmcnt(1)\n" \
"v_mfma_f32_16x16x32_bf16 v[68:71], v[92:95], v[28:31], 0\n" \
"s_waitcnt lgkmcnt(0)\n" \
"v_xor_b32 v85, s29, v84\n" \
"v_mfma_f32_16x16x32_bf16 v[76:79], v[92:95], v[36:39], 0\n" \
"v_add_u32 v84, v84, v210\n" \
"v_add_u32 v85, v85, v210\n" \
"v_mfma_f32_16x16x32_bf16 v[72:75], v[92:95], v[32:35], 0\n" \
"s_nop 1\n" \
"v_mfma_f32_16x16x32_bf16 v[80:83], v[92:95], v[40:43], 0\n" \
"v_cvt_pk_bf16_f32 v96, v68, v69\n" \
"v_cvt_pk_bf16_f32 v97, v70, v71\n" \
"v_cvt_pk_bf16_f32 v98, v76, v77\n" \
"v_cvt_pk_bf16_f32 v99, v78, v79\n" \
"s_mov_b64 exec, s[2:3]\n" \
"v_cvt_pk_bf16_f32 v96, v72, v73\n" \
"v_cvt_pk_bf16_f32 v97, v74, v75\n" \
"s_nop 0\n" \
"v_cvt_pk_bf16_f32 v98, v80, v81\n" \
"v_cvt_pk_bf16_f32 v99, v82, v83\n" \
"s_mov_b64 exec, -1\n" \
"s_nop 1\n" \
"v_permlane16_swap_b32 v96, v98\n" \
"v_permlane16_swap_b32 v97, v99\n" \
"buffer_store_dwordx4 v[96:99], v84, s[4:7], 0 offen sc1\n" \
"s_barrier\n" \
"buffer_store_dwordx4 v[96:99], v85, s[4:7], 0 offen sc1\n" \
"ds_read_b96 v[248:250], v252\n" \
"s_waitcnt lgkmcnt(0)\n" \
"v_readfirstlane_b32 s44, v248\n" \
"v_readfirstlane_b32 s45, v249\n" \
"v_readfirstlane_b32 s46, v250\n" \
"s_mul_i32 s42, s25, s46\n" \
"s_lshr_b32 s42, s42, 12\n" \
".p2align 3\n" \
"s_memtime s[54:55]\n" \
"s_memrealtime s[56:57]\n" \
"s_memtime s[58:59]\n" \
"s_memrealtime s[60:61]\n" \
"s_nop 0\n" \
"s_nop 0\n" \
"s_memtime s[62:63]\n" \
"s_memrealtime s[64:65]\n" \
"s_memtime s[66:67]\n" \
"s_memrealtime s[68:69]\n" \
"s_memtime s[70:71]\n" \
"s_memrealtime s[72:73]\n" \
"s_memtime s[74:75]\n" \
"s_memrealtime s[76:77]\n" \
"s_nop 0\n" \
"s_nop 0\n" \
"s_memtime s[78:79]\n" \
"s_memrealtime s[80:81]\n" \
"s_memtime s[82:83]\n" \
"s_memrealtime s[84:85]\n" \
"s_memtime s[86:87]\n" \
"s_memrealtime s[88:89]\n" \
"s_memtime s[90:91]\n" \
"s_memrealtime s[92:93]\n" \
"s_nop 0\n" \
"s_nop 0\n" \
"s_memtime s[94:95]\n" \
"s_memrealtime s[96:97]\n" \
"s_memtime s[98:99]\n" \
"s_mov_b32 s52, 0\n" \
"s_mov_b32 s53, 4096\n" \
"s_waitcnt lgkmcnt(0)\n" \
"s_cmp_lg_u32 s96, s56\n" \
"s_cselect_b32 s52, s90, s52\n" \
"s_cselect_b32 s53, s98, s53\n" \
"s_cmp_lg_u32 s92, s56\n" \
"s_cselect_b32 s52, s86, s52\n" \
"s_cselect_b32 s53, s94, s53\n" \
"s_cmp_lg_u32 s88, s56\n" \
"s_cselect_b32 s52, s82, s52\n" \
"s_cselect_b32 s53, s90, s53\n" \
"s_cmp_lg_u32 s84, s56\n" \
"s_cselect_b32 s52, s78, s52\n" \
"s_cselect_b32 s53, s86, s53\n" \
"s_cmp_lg_u32 s80, s56\n" \
"s_cselect_b32 s52, s74, s52\n" \
"s_cselect_b32 s53, s82, s53\n" \
"s_cmp_lg_u32 s76, s56\n" \
"s_cselect_b32 s52, s70, s52\n" \
"s_cselect_b32 s53, s78, s53\n" \
"s_cmp_lg_u32 s72, s56\n" \
"s_cselect_b32 s52, s66, s52\n" \
"s_cselect_b32 s53, s74, s53\n" \
"s_cmp_lg_u32 s68, s56\n" \
"s_cselect_b32 s52, s62, s52\n" \
"s_cselect_b32 s53, s70, s53\n" \
"s_cmp_lg_u32 s64, s56\n" \
"s_cselect_b32 s52, s58, s52\n" \
"s_cselect_b32 s53, s66, s53\n" \
"s_cmp_lg_u32 s60, s56\n" \
"s_cselect_b32 s52, s54, s52\n" \
"s_cselect_b32 s53, s62, s53\n" \
"s_sub_u32 s53, s53, s52\n" \
"s_lshr_b32 s55, s53, 1\n" \
"s_add_u32 s52, s52, s55\n" \
"s_add_u32 s55, s56, 4\n" \
"s_lshl_b32 s55, s55, 4\n" \
"s_sub_u32 s59, s55, s44\n" \
"s_cmp_lt_u32 s59, 2600\n" \
"s_cselect_b32 s63, 1, 2\n" \
"s_mul_i32 s59, s59, s46\n" \
"s_ashr_i32 s59, s59, 12\n" \
"s_add_u32 s59, s59, s45\n" \
"s_sub_u32 s59, s52, s59\n" \
"s_max_i32 s59, s59, -96\n" \
"s_min_i32 s59, s59, 96\n" \
"s_ashr_i32 s63, s59, s63\n" \
"s_add_u32 s63, s63, s46\n" \
"s_max_i32 s63, s63, 0x800\n" \
"s_min_i32 s63, s63, 0x2000\n" \
"s_cmp_le_u32 s53, 40\n" \
"s_cselect_b32 s44, s55, s44\n" \
"s_cselect_b32 s45, s52, s45\n" \
"s_cselect_b32 s46, s63, s46\n" \
"v_mov_b32 v248, s44\n" \
"v_mov_b32 v249, s45\n" \
"v_mov_b32 v250, s46\n" \
"s_mov_b64 exec, 1\n" \
"ds_write_b96 v252, v[248:250]\n" \
"s_mov_b64 exec, -1\n" \
"buffer_store_dwordx4 v[100:103], v214, s[4:7], s49 offen sc1\n" \
"s_xor_b32 s43, s49, s29\n" \
"buffer_store_dwordx4 v[100:103], v214, s[4:7], s43 offen sc1\n" \
"s_waitcnt vmcnt(4)\n" \
"v_mov_b32 v28, v44\n" \
"v_mov_b32 v29, v45\n" \
"v_mov_b32 v30, v46\n" \
"v_mov_b32 v31, v47\n" \
"v_mov_b32 v32, v48\n" \
"v_mov_b32 v33, v49\n" \
"v_mov_b32 v34, v50\n" \
"v_mov_b32 v35, v51\n" \
"v_mov_b32 v36, v52\n" \
"v_mov_b32 v37, v53\n" \
"v_mov_b32 v38, v54\n" \
"v_mov_b32 v39, v55\n" \
"v_mov_b32 v40, v56\n" \
"v_mov_b32 v41, v57\n" \
"v_mov_b32 v42, v58\n" \
"v_mov_b32 v43, v59\n" \
"s_add_u32 s40, s40, 1\n" \
"s_cmp_eq_u32 s40, 0\n" \
"s_cbranch_scc1 .LX0_fin\n" \
"s_cmp_lt_u32 s40, 63\n" \
"s_cbranch_scc1 .LX0_loop\n" \
".LX0_fin:\n" \
"s_barrier\n" \
"ds_read_b128 v[28:31], v223 offset:0\n" \
"ds_read_b128 v[32:35], v199 offset:0\n" \
"ds_read_b128 v[36:39], v223 offset:128\n" \
"ds_read_b128 v[40:43], v199 offset:128\n" \
"ds_read_b128 v[44:47], v223 offset:256\n" \
"ds_read_b128 v[48:51], v199 offset:256\n" \
"ds_read_b128 v[52:55], v223 offset:384\n" \
"ds_read_b128 v[0:3], v216 offset:0\n" \
"ds_read_b128 v[4:7], v216 offset:1024\n" \
"ds_read_b128 v[8:11], v216 offset:2048\n" \
"ds_read_b128 v[12:15], v216 offset:3072\n" \
"ds_read_b128 v[16:19], v216 offset:4096\n" \
"ds_read_b128 v[20:23], v216 offset:5120\n" \
"ds_read_b128 v[24:27], v216 offset:6144\n" \
"v_mov_b32 v60, 0\n" \
"v_mov_b32 v61, 0\n" \
"v_mov_b32 v62, 0\n" \
"v_mov_b32 v63, 0\n" \
"v_mov_b32 v64, 0\n" \
"v_mov_b32 v65, 0\n" \
"v_mov_b32 v66, 0\n" \
"v_mov_b32 v67, 0\n" \
"v_mov_b32 v68, 0\n" \
"v_mov_b32 v69, 0\n" \
"v_mov_b32 v70, 0\n" \
"v_mov_b32 v71, 0\n" \
"v_mov_b32 v72, 0\n" \
"v_mov_b32 v73, 0\n" \
"v_mov_b32 v74, 0\n" \
"v_mov_b32 v75, 0\n" \
"s_nop 1\n" \
"s_waitcnt lgkmcnt(6)\n" \
"v_smfmac_f32_16x16x64_bf16 v[60:63], v[0:3], a[0:7], v200\n" \
"v_smfmac_f32_16x16x64_bf16 v[64:67], v[0:3], a[56:63], v200\n" \
"v_smfmac_f32_16x16x64_bf16 v[68:71], v[0:3], a[112:119], v200\n" \
"v_smfmac_f32_16x16x64_bf16 v[72:75], v[0:3], a[168:175], v200\n" \
"s_waitcnt lgkmcnt(5)\n" \
"v_smfmac_f32_16x16x64_bf16 v[60:63], v[4:7], a[8:15], v200\n" \
"v_smfmac_f32_16x16x64_bf16 v[64:67], v[4:7], a[64:71], v200\n" \
"v_smfmac_f32_16x16x64_bf16 v[68:71], v[4:7], a[120:127], v200\n" \
"v_smfmac_f32_16x16x64_bf16 v[72:75], v[4:7], a[176:183], v200\n" \
"s_waitcnt lgkmcnt(4)\n" \
"v_smfmac_f32_16x16x64_bf16 v[60:63], v[8:11], a[16:23], v200\n" \
"v_smfmac_f32_16x16x64_bf16 v[64:67], v[8:11], a[72:79], v200\n" \
"v_smfmac_f32_16x16x64_bf16 v[68:71], v[8:11], a[128:135], v200\n" \
"v_smfmac_f32_16x16x64_bf16 v[72:75], v[8:11], a[184:191], v200\n" \
"s_waitcnt lgkmcnt(3)\n" \
"v_smfmac_f32_16x16x64_bf16 v[60:63], v[12:15], a[24:31], v200\n" \
"v_smfmac_f32_16x16x64_bf16 v[64:67], v[12:15], a[80:87], v200\n" \
"v_smfmac_f32_16x16x64_bf16 v[68:71], v[12:15], a[136:143], v200\n" \
"v_smfmac_f32_16x16x64_bf16 v[72:75], v[12:15], a[192:199], v200\n" \
"s_waitcnt lgkmcnt(2)\n" \
"v_smfmac_f32_16x16x64_bf16 v[60:63], v[16:19], a[32:39], v200\n" \
"v_smfmac_f32_16x16x64_bf16 v[64:67], v[16:19], a[88:95], v200\n" \
"v_smfmac_f32_16x16x64_bf16 v[68:71], v[16:19], a[144:151], v200\n" \
"v_smfmac_f32_16x16x64_bf16 v[72:75], v[16:19], a[200:207], v200\n" \
"s_waitcnt lgkmcnt(1)\n" \
"v_smfmac_f32_16x16x64_bf16 v[60:63], v[20:23], a[40:47], v200\n" \
"v_smfmac_f32_16x16x64_bf16 v[64:67], v[20:23], a[96:103], v200\n" \
"v_smfmac_f32_16x16x64_bf16 v[68:71], v[20:23], a[152:159], v200\n" \
"v_smfmac_f32_16x16x64_bf16 v[72:75], v[20:23], a[208:215], v200\n" \
"s_waitcnt lgkmcnt(0)\n" \
"v_smfmac_f32_16x16x64_bf16 v[60:63], v[24:27], a[48:55], v200\n" \
"v_smfmac_f32_16x16x64_bf16 v[64:67], v[24:27], a[104:111], v200\n" \
"v_smfmac_f32_16x16x64_bf16 v[68:71], v[24:27], a[160:167], v200\n" \
"v_smfmac_f32_16x16x64_bf16 v[72:75], v[24:27], a[216:223], v200\n" \
"s_nop 7\n" \
"v_permlane32_swap_b32 v60, v64\n" \
"v_permlane32_swap_b32 v61, v65\n" \
"v_permlane32_swap_b32 v62, v66\n" \
"v_permlane32_swap_b32 v63, v67\n" \
"v_add_f32 v60, v60, v64\n" \
"v_add_f32 v61, v61, v65\n" \
"v_add_f32 v62, v62, v66\n" \
"v_add_f32 v63, v63, v67\n" \
"v_cvt_pk_bf16_f32 v184, v60, v61\n" \
"v_cvt_pk_bf16_f32 v185, v62, v63\n" \
"ds_write_b64 v217, v[184:185] offset:0\n" \
"v_permlane32_swap_b32 v68, v72\n" \
"v_permlane32_swap_b32 v69, v73\n" \
"v_permlane32_swap_b32 v70, v74\n" \
"v_permlane32_swap_b32 v71, v75\n" \
"v_add_f32 v68, v68, v72\n" \
"v_add_f32 v69, v69, v73\n" \
"v_add_f32 v70, v70, v74\n" \
"v_add_f32 v71, v71, v75\n" \
"v_cvt_pk_bf16_f32 v186, v68, v69\n" \
"v_cvt_pk_bf16_f32 v187, v70, v71\n" \
"ds_write_b64 v217, v[186:187] offset:512\n" \
"s_waitcnt lgkmcnt(0)\n" \
"s_barrier\n" \
"ds_read_b64_tr_b16 v[0:1], v222 offset:0\n" \
"ds_read_b64_tr_b16 v[2:3], v222 offset:64\n" \
"ds_read_b64_tr_b16 v[4:5], v222 offset:512\n" \
"ds_read_b64_tr_b16 v[6:7], v222 offset:576\n" \
"ds_read_b64_tr_b16 v[8:9], v222 offset:2048\n" \
"ds_read_b64_tr_b16 v[10:11], v222 offset:2112\n" \
"ds_read_b64_tr_b16 v[12:13], v222 offset:2560\n" \
"ds_read_b64_tr_b16 v[14:15], v222 offset:2624\n" \
"ds_read_b64_tr_b16 v[16:17], v222 offset:4096\n" \
"ds_read_b64_tr_b16 v[18:19], v222 offset:4160\n" \
"ds_read_b64_tr_b16 v[20:21], v222 offset:4608\n" \
"ds_read_b64_tr_b16 v[22:23], v222 offset:4672\n" \
"ds_read_b64_tr_b16 v[24:25], v222 offset:5120\n" \
"ds_read_b64_tr_b16 v[26:27], v222 offset:5184\n" \
"s_waitcnt lgkmcnt(12)\n" \
"v_mfma_f32_16x16x32_bf16 v[96:99], v[0:3], v[28:31], 0\n" \
"s_waitcnt lgkmcnt(10)\n" \
"v_mfma_f32_16x16x32_bf16 v[96:99], v[4:7], v[32:35], v[96:99]\n" \
"s_waitcnt lgkmcnt(8)\n" \
"v_mfma_f32_16x16x32_bf16 v[96:99], v[8:11], v[36:39], v[96:99]\n" \
"s_waitcnt lgkmcnt(6)\n" \
"v_mfma_f32_16x16x32_bf16 v[96:99], v[12:15], v[40:43], v[96:99]\n" \
"s_waitcnt lgkmcnt(4)\n" \
"v_mfma_f32_16x16x32_bf16 v[96:99], v[16:19], v[44:47], v[96:99]\n" \
"s_waitcnt lgkmcnt(2)\n" \
"v_mfma_f32_16x16x32_bf16 v[96:99], v[20:23], v[48:51], v[96:99]\n" \
"s_waitcnt lgkmcnt(0)\n" \
"v_mfma_f32_16x16x32_bf16 v[96:99], v[24:27], v[52:55], v[96:99]\n" \
"s_nop 7\n" \
"buffer_store_dwordx4 v[96:99], v224, s[16:19], 0 offen sc1\n" \
"ds_read_b128 v[28:31], v223 offset:28672\n" \
"ds_read_b128 v[32:35], v199 offset:28672\n" \
"ds_read_b128 v[36:39], v223 offset:28800\n" \
"ds_read_b128 v[40:43], v199 offset:28800\n" \
"ds_read_b128 v[44:47], v223 offset:28928\n" \
"ds_read_b128 v[48:51], v199 offset:28928\n" \
"ds_read_b128 v[52:55], v223 offset:29056\n" \
"s_waitcnt lgkmcnt(0)\n" \
"s_barrier\n" \
"ds_read_b128 v[0:3], v216 offset:7168\n" \
"ds_read_b128 v[4:7], v216 offset:8192\n" \
"ds_read_b128 v[8:11], v216 offset:9216\n" \
"ds_read_b128 v[12:15], v216 offset:10240\n" \
"ds_read_b128 v[16:19], v216 offset:11264\n" \
"ds_read_b128 v[20:23], v216 offset:12288\n" \
"ds_read_b128 v[24:27], v216 offset:13312\n" \
"v_mov_b32 v60, 0\n" \
"v_mov_b32 v61, 0\n" \
"v_mov_b32 v62, 0\n" \
"v_mov_b32 v63, 0\n" \
"v_mov_b32 v64, 0\n" \
"v_mov_b32 v65, 0\n" \
"v_mov_b32 v66, 0\n" \
"v_mov_b32 v67, 0\n" \
"v_mov_b32 v68, 0\n" \
"v_mov_b32 v69, 0\n" \
"v_mov_b32 v70, 0\n" \
"v_mov_b32 v71, 0\n" \
"v_mov_b32 v72, 0\n" \
"v_mov_b32 v73, 0\n" \
"v_mov_b32 v74, 0\n" \
"v_mov_b32 v75, 0\n" \
"s_nop 1\n" \
"s_waitcnt lgkmcnt(6)\n" \
"v_smfmac_f32_16x16x64_bf16 v[60:63], v[0:3], a[0:7], v200\n" \
"v_smfmac_f32_16x16x64_bf16 v[64:67], v[0:3], a[56:63], v200\n" \
"v_smfmac_f32_16x16x64_bf16 v[68:71], v[0:3], a[112:119], v200\n" \
"v_smfmac_f32_16x16x64_bf16 v[72:75], v[0:3], a[168:175], v200\n" \
"s_waitcnt lgkmcnt(5)\n" \
"v_smfmac_f32_16x16x64_bf16 v[60:63], v[4:7], a[8:15], v200\n" \
"v_smfmac_f32_16x16x64_bf16 v[64:67], v[4:7], a[64:71], v200\n" \
"v_smfmac_f32_16x16x64_bf16 v[68:71], v[4:7], a[120:127], v200\n" \
"v_smfmac_f32_16x16x64_bf16 v[72:75], v[4:7], a[176:183], v200\n" \
"s_waitcnt lgkmcnt(4)\n" \
"v_smfmac_f32_16x16x64_bf16 v[60:63], v[8:11], a[16:23], v200\n" \
"v_smfmac_f32_16x16x64_bf16 v[64:67], v[8:11], a[72:79], v200\n" \
"v_smfmac_f32_16x16x64_bf16 v[68:71], v[8:11], a[128:135], v200\n" \
"v_smfmac_f32_16x16x64_bf16 v[72:75], v[8:11], a[184:191], v200\n" \
"s_waitcnt lgkmcnt(3)\n" \
"v_smfmac_f32_16x16x64_bf16 v[60:63], v[12:15], a[24:31], v200\n" \
"v_smfmac_f32_16x16x64_bf16 v[64:67], v[12:15], a[80:87], v200\n" \
"v_smfmac_f32_16x16x64_bf16 v[68:71], v[12:15], a[136:143], v200\n" \
"v_smfmac_f32_16x16x64_bf16 v[72:75], v[12:15], a[192:199], v200\n" \
"s_waitcnt lgkmcnt(2)\n" \
"v_smfmac_f32_16x16x64_bf16 v[60:63], v[16:19], a[32:39], v200\n" \
"v_smfmac_f32_16x16x64_bf16 v[64:67], v[16:19], a[88:95], v200\n" \
"v_smfmac_f32_16x16x64_bf16 v[68:71], v[16:19], a[144:151], v200\n" \
"v_smfmac_f32_16x16x64_bf16 v[72:75], v[16:19], a[200:207], v200\n" \
"s_waitcnt lgkmcnt(1)\n" \
"v_smfmac_f32_16x16x64_bf16 v[60:63], v[20:23], a[40:47], v200\n" \
"v_smfmac_f32_16x16x64_bf16 v[64:67], v[20:23], a[96:103], v200\n" \
"v_smfmac_f32_16x16x64_bf16 v[68:71], v[20:23], a[152:159], v200\n" \
"v_smfmac_f32_16x16x64_bf16 v[72:75], v[20:23], a[208:215], v200\n" \
"s_waitcnt lgkmcnt(0)\n" \
"v_smfmac_f32_16x16x64_bf16 v[60:63], v[24:27], a[48:55], v200\n" \
"v_smfmac_f32_16x16x64_bf16 v[64:67], v[24:27], a[104:111], v200\n" \
"v_smfmac_f32_16x16x64_bf16 v[68:71], v[24:27], a[160:167], v200\n" \
"v_smfmac_f32_16x16x64_bf16 v[72:75], v[24:27], a[216:223], v200\n" \
"s_nop 7\n" \
"v_permlane32_swap_b32 v60, v64\n" \
"v_permlane32_swap_b32 v61, v65\n" \
"v_permlane32_swap_b32 v62, v66\n" \
"v_permlane32_swap_b32 v63, v67\n" \
"v_add_f32 v60, v60, v64\n" \
"v_add_f32 v61, v61, v65\n" \
"v_add_f32 v62, v62, v66\n" \
"v_add_f32 v63, v63, v67\n" \
"v_cvt_pk_bf16_f32 v184, v60, v61\n" \
"v_cvt_pk_bf16_f32 v185, v62, v63\n" \
"ds_write_b64 v217, v[184:185] offset:0\n" \
"v_permlane32_swap_b32 v68, v72\n" \
"v_permlane32_swap_b32 v69, v73\n" \
"v_permlane32_swap_b32 v70, v74\n" \
"v_permlane32_swap_b32 v71, v75\n" \
"v_add_f32 v68, v68, v72\n" \
"v_add_f32 v69, v69, v73\n" \
"v_add_f32 v70, v70, v74\n" \
"v_add_f32 v71, v71, v75\n" \
"v_cvt_pk_bf16_f32 v186, v68, v69\n" \
"v_cvt_pk_bf16_f32 v187, v70, v71\n" \
"ds_write_b64 v217, v[186:187] offset:512\n" \
"s_waitcnt lgkmcnt(0)\n" \
"s_barrier\n" \
"ds_read_b64_tr_b16 v[0:1], v222 offset:0\n" \
"ds_read_b64_tr_b16 v[2:3], v222 offset:64\n" \
"ds_read_b64_tr_b16 v[4:5], v222 offset:512\n" \
"ds_read_b64_tr_b16 v[6:7], v222 offset:576\n" \
"ds_read_b64_tr_b16 v[8:9], v222 offset:2048\n" \
"ds_read_b64_tr_b16 v[10:11], v222 offset:2112\n" \
"ds_read_b64_tr_b16 v[12:13], v222 offset:2560\n" \
"ds_read_b64_tr_b16 v[14:15], v222 offset:2624\n" \
"ds_read_b64_tr_b16 v[16:17], v222 offset:4096\n" \
"ds_read_b64_tr_b16 v[18:19], v222 offset:4160\n" \
"ds_read_b64_tr_b16 v[20:21], v222 offset:4608\n" \
"ds_read_b64_tr_b16 v[22:23], v222 offset:4672\n" \
"ds_read_b64_tr_b16 v[24:25], v222 offset:5120\n" \
"ds_read_b64_tr_b16 v[26:27], v222 offset:5184\n" \
"s_waitcnt lgkmcnt(12)\n" \
"v_mfma_f32_16x16x32_bf16 v[96:99], v[0:3], v[28:31], 0\n" \
"s_waitcnt lgkmcnt(10)\n" \
"v_mfma_f32_16x16x32_bf16 v[96:99], v[4:7], v[32:35], v[96:99]\n" \
"s_waitcnt lgkmcnt(8)\n" \
"v_mfma_f32_16x16x32_bf16 v[96:99], v[8:11], v[36:39], v[96:99]\n" \
"s_waitcnt lgkmcnt(6)\n" \
"v_mfma_f32_16x16x32_bf16 v[96:99], v[12:15], v[40:43], v[96:99]\n" \
"s_waitcnt lgkmcnt(4)\n" \
"v_mfma_f32_16x16x32_bf16 v[96:99], v[16:19], v[44:47], v[96:99]\n" \
"s_waitcnt lgkmcnt(2)\n" \
"v_mfma_f32_16x16x32_bf16 v[96:99], v[20:23], v[48:51], v[96:99]\n" \
"s_waitcnt lgkmcnt(0)\n" \
"v_mfma_f32_16x16x32_bf16 v[96:99], v[24:27], v[52:55], v[96:99]\n" \
"s_nop 7\n" \
"buffer_store_dwordx4 v[96:99], v224, s[16:19], 0 offen offset:2048 sc1\n" \
"s_cmp_eq_u32 s40, 0\n" \
"s_cbranch_scc1 .LX0_wet\n" \
".LX0_end:\n" \
"s_endpgm\n" \
".p2align 6\n" \
"fk_role_X1:\n" \
"ds_read_b32 v200, v255 offset:0\n" \
"ds_read_b32 v201, v255 offset:256\n" \
"ds_read_b32 v202, v255 offset:512\n" \
"ds_read_b32 v203, v255 offset:768\n" \
"ds_read_b32 v204, v255 offset:1024\n" \
"ds_read_b32 v205, v255 offset:1280\n" \
"ds_read_b32 v206, v255 offset:1536\n" \
"ds_read_b32 v207, v255 offset:1792\n" \
"ds_read_b32 v208, v255 offset:2048\n" \
"ds_read_b32 v209, v255 offset:2304\n" \
"ds_read_b32 v210, v255 offset:2560\n" \
"ds_read_b32 v211, v255 offset:2816\n" \
"ds_read_b32 v212, v255 offset:3072\n" \
"ds_read_b32 v213, v255 offset:3328\n" \
"ds_read_b32 v214, v255 offset:3584\n" \
"ds_read_b32 v215, v255 offset:3840\n" \
"ds_read_b32 v216, v255 offset:4096\n" \
"ds_read_b32 v217, v255 offset:4352\n" \
"ds_read_b32 v218, v255 offset:4608\n" \
"ds_read_b32 v219, v255 offset:4864\n" \
"ds_read_b32 v220, v255 offset:5120\n" \
"ds_read_b32 v221, v255 offset:5376\n" \
"ds_read_b32 v222, v255 offset:5632\n" \
"ds_read_b32 v223, v255 offset:5888\n" \
"ds_read_b32 v224, v255 offset:6144\n" \
"ds_read_b32 v254, v255 offset:7936\n" \
"s_waitcnt lgkmcnt(0)\n" \
"v_readlane_b32 s4, v254, 0\n" \
"v_readlane_b32 s5, v254, 1\n" \
"v_readlane_b32 s6, v254, 2\n" \
"v_readlane_b32 s7, v254, 3\n" \
"v_readlane_b32 s8, v254, 4\n" \
"v_readlane_b32 s9, v254, 5\n" \
"v_readlane_b32 s10, v254, 6\n" \
"v_readlane_b32 s11, v254, 7\n" \
"v_readlane_b32 s12, v254, 8\n" \
"v_readlane_b32 s13, v254, 9\n" \
"v_readlane_b32 s14, v254, 10\n" \
"v_readlane_b32 s15, v254, 11\n" \
"v_readlane_b32 s16, v254, 12\n" \
"v_readlane_b32 s17, v254, 13\n" \
"v_readlane_b32 s18, v254, 14\n" \
"v_readlane_b32 s19, v254, 15\n" \
"v_readlane_b32 s20, v254, 16\n" \
"v_readlane_b32 s21, v254, 17\n" \
"v_readlane_b32 s22, v254, 18\n" \
"v_readlane_b32 s23, v254, 19\n" \
"v_readlane_b32 s24, v254, 20\n" \
"v_readlane_b32 s25, v254, 21\n" \
"v_readlane_b32 s26, v254, 22\n" \
"v_readlane_b32 s27, v254, 23\n" \
"v_readlane_b32 s28, v254, 24\n" \
"v_readlane_b32 s29, v254, 25\n" \
"v_readlane_b32 s30, v254, 26\n" \
"v_readlane_b32 s31, v254, 27\n" \
"v_readlane_b32 s32, v254, 28\n" \
"v_readlane_b32 s33, v254, 29\n" \
"v_readlane_b32 s34, v254, 30\n" \
"v_readlane_b32 s35, v254, 31\n" \
"v_readlane_b32 s36, v254, 32\n" \
"v_readlane_b32 s37, v254, 33\n" \
"v_readlane_b32 s38, v254, 34\n" \
"v_readlane_b32 s39, v254, 35\n" \
"s_mov_b32 s40, 0\n" \
"s_mov_b32 s51, 0\n" \
"s_nop 0\n" \
"v_mov_b32 v252, s38\n" \
"s_nop 4\n" \
"v_mov_b32 v100, -1\n" \
"v_mov_b32 v101, -1\n" \
"v_mov_b32 v102, -1\n" \
"v_mov_b32 v103, -1\n" \
"s_mov_b32 s35, -2\n" \
"s_mov_b32 s2, 0\n" \
"s_mov_b32 s3, -1\n" \
"ds_read_b32 v199, v255 offset:6656\n" \
"s_waitcnt lgkmcnt(0)\n" \
"v_writelane_b32 v251, s6, 0\n" \
"v_writelane_b32 v251, s10, 1\n" \
"v_writelane_b32 v251, s14, 2\n" \
"v_writelane_b32 v251, s18, 3\n" \
"s_mov_b32 s6, 0\n" \
"s_mov_b32 s10, 0\n" \
"s_mov_b32 s14, 0\n" \
"s_mov_b32 s18, 0\n" \
"s_mov_b32 s40, -1\n" \
"s_branch .LX1_pre\n" \
".LX1_wet:\n" \
"s_waitcnt vmcnt(0)\n" \
"v_readlane_b32 s6, v251, 0\n" \
"v_readlane_b32 s10, v251, 1\n" \
"v_readlane_b32 s14, v251, 2\n" \
"v_readlane_b32 s18, v251, 3\n" \
"s_nop 4\n" \
"s_add_u32 s43, s30, 1\n" \
"s_mov_b32 s50, 0\n" \
".LX1_w64l:\n" \
"buffer_load_dword v240, v204, s[4:7], 0 offen sc1\n" \
"s_waitcnt vmcnt(0)\n" \
".LX1_w64c:\n" \
"v_subrev_u32 v241, s43, v240\n" \
"v_cmp_gt_u32 vcc, 0x100, v241\n" \
"s_cmp_eq_u64 vcc, -1\n" \
"s_cbranch_scc1 .LX1_w64d\n" \
"s_sleep 2\n" \
"s_add_u32 s50, s50, 1\n" \
"s_cmp_lt_u32 s50, 0x20000\n" \
"s_cbranch_scc1 .LX1_w64l\n" \
"s_branch .LX1_end\n" \
".LX1_w64d:\n" \
"v_readlane_b32 s43, v202, 0\n" \
"s_nop 4\n" \
"buffer_load_dwordx4 a[0:3], v201, s[8:11], s43 offen sc1\n" \
"buffer_load_dwordx4 a[4:7], v201, s[8:11], s43 offen offset:1024 sc1\n" \
"v_readlane_b32 s43, v202, 7\n" \
"s_nop 4\n" \
"buffer_load_dwordx4 a[56:59], v201, s[8:11], s43 offen sc1\n" \
"buffer_load_dwordx4 a[60:63], v201, s[8:11], s43 offen offset:1024 sc1\n" \
"v_readlane_b32 s43, v202, 14\n" \
"s_nop 4\n" \
"buffer_load_dwordx4 a[112:115], v201, s[8:11], s43 offen sc1\n" \
"buffer_load_dwordx4 a[116:119], v201, s[8:11], s43 offen offset:1024 sc1\n" \
"v_readlane_b32 s43, v202, 21\n" \
"s_nop 4\n" \
"buffer_load_dwordx4 a[168:171], v201, s[8:11], s43 offen sc1\n" \
"buffer_load_dwordx4 a[172:175], v201, s[8:11], s43 offen offset:1024 sc1\n" \
"s_add_u32 s43, s30, 2\n" \
"s_mov_b32 s50, 0\n" \
"s_branch .LX1_w65c\n" \
".LX1_w65l:\n" \
"buffer_load_dword v240, v204, s[4:7], 0 offen sc1\n" \
"s_waitcnt vmcnt(0)\n" \
".LX1_w65c:\n" \
"v_subrev_u32 v241, s43, v240\n" \
"v_cmp_gt_u32 vcc, 0x100, v241\n" \
"s_cmp_eq_u64 vcc, -1\n" \
"s_cbranch_scc1 .LX1_w65d\n" \
"s_sleep 2\n" \
"s_add_u32 s50, s50, 1\n" \
"s_cmp_lt_u32 s50, 0x20000\n" \
"s_cbranch_scc1 .LX1_w65l\n" \
"s_branch .LX1_end\n" \
".LX1_w65d:\n" \
"v_readlane_b32 s43, v202, 4\n" \
"s_nop 4\n" \
"buffer_load_dwordx4 a[32:35], v201, s[8:11], s43 offen sc1\n" \
"buffer_load_dwordx4 a[36:39], v201, s[8:11], s43 offen offset:1024 sc1\n" \
"v_readlane_b32 s43, v202, 11\n" \
"s_nop 4\n" \
"buffer_load_dwordx4 a[88:91], v201, s[8:11], s43 offen sc1\n" \
"buffer_load_dwordx4 a[92:95], v201, s[8:11], s43 offen offset:1024 sc1\n" \
"v_readlane_b32 s43, v202, 18\n" \
"s_nop 4\n" \
"buffer_load_dwordx4 a[144:147], v201, s[8:11], s43 offen sc1\n" \
"buffer_load_dwordx4 a[148:151], v201, s[8:11], s43 offen offset:1024 sc1\n" \
"v_readlane_b32 s43, v202, 25\n" \
"s_nop 4\n" \
"buffer_load_dwordx4 a[200:203], v201, s[8:11], s43 offen sc1\n" \
"buffer_load_dwordx4 a[204:207], v201, s[8:11], s43 offen offset:1024 sc1\n" \
"s_add_u32 s43, s30, 3\n" \
"s_mov_b32 s50, 0\n" \
"s_branch .LX1_w66c\n" \
".LX1_w66l:\n" \
"buffer_load_dword v240, v204, s[4:7], 0 offen sc1\n" \
"s_waitcnt vmcnt(0)\n" \
".LX1_w66c:\n" \
"v_subrev_u32 v241, s43, v240\n" \
"v_cmp_gt_u32 vcc, 0x100, v241\n" \
"s_cmp_eq_u64 vcc, -1\n" \
"s_cbranch_scc1 .LX1_w66d\n" \
"s_sleep 2\n" \
"s_add_u32 s50, s50, 1\n" \
"s_cmp_lt_u32 s50, 0x20000\n" \
"s_cbranch_scc1 .LX1_w66l\n" \
"s_branch .LX1_end\n" \
".LX1_w66d:\n" \
"v_readlane_b32 s43, v202, 1\n" \
"s_nop 4\n" \
"buffer_load_dwordx4 a[8:11], v201, s[8:11], s43 offen sc1\n" \
"buffer_load_dwordx4 a[12:15], v201, s[8:11], s43 offen offset:1024 sc1\n" \
"v_readlane_b32 s43, v202, 8\n" \
"s_nop 4\n" \
"buffer_load_dwordx4 a[64:67], v201, s[8:11], s43 offen sc1\n" \
"buffer_load_dwordx4 a[68:71], v201, s[8:11], s43 offen offset:1024 sc1\n" \
"v_readlane_b32 s43, v202, 15\n" \
"s_nop 4\n" \
"buffer_load_dwordx4 a[120:123], v201, s[8:11], s43 offen sc1\n" \
"buffer_load_dwordx4 a[124:127], v201, s[8:11], s43 offen offset:1024 sc1\n" \
"v_readlane_b32 s43, v202, 22\n" \
"s_nop 4\n" \
"buffer_load_dwordx4 a[176:179], v201, s[8:11], s43 offen sc1\n" \
"buffer_load_dwordx4 a[180:183], v201, s[8:11], s43 offen offset:1024 sc1\n" \
"s_add_u32 s43, s30, 4\n" \
"s_mov_b32 s50, 0\n" \
"s_branch .LX1_w67c\n" \
".LX1_w67l:\n" \
"buffer_load_dword v240, v204, s[4:7], 0 offen sc1\n" \
"s_waitcnt vmcnt(0)\n" \
".LX1_w67c:\n" \
"v_subrev_u32 v241, s43, v240\n" \
"v_cmp_gt_u32 vcc, 0x100, v241\n" \
"s_cmp_eq_u64 vcc, -1\n" \
"s_cbranch_scc1 .LX1_w67d\n" \
"s_sleep 2\n" \
"s_add_u32 s50, s50, 1\n" \
"s_cmp_lt_u32 s50, 0x20000\n" \
"s_cbranch_scc1 .LX1_w67l\n" \
"s_branch .LX1_end\n" \
".LX1_w67d:\n" \
"v_readlane_b32 s43, v202, 5\n" \
"s_nop 4\n" \
"buffer_load_dwordx4 a[40:43], v201, s[8:11], s43 offen sc1\n" \
"buffer_load_dwordx4 a[44:47], v201, s[8:11], s43 offen offset:1024 sc1\n" \
"v_readlane_b32 s43, v202, 12\n" \
"s_nop 4\n" \
"buffer_load_dwordx4 a[96:99], v201, s[8:11], s43 offen sc1\n" \
"buffer_load_dwordx4 a[100:103], v201, s[8:11], s43 offen offset:1024 sc1\n" \
"v_readlane_b32 s43, v202, 19\n" \
"s_nop 4\n" \
"buffer_load_dwordx4 a[152:155], v201, s[8:11], s43 offen sc1\n" \
"buffer_load_dwordx4 a[156:159], v201, s[8:11], s43 offen offset:1024 sc1\n" \
"v_readlane_b32 s43, v202, 26\n" \
"s_nop 4\n" \
"buffer_load_dwordx4 a[208:211], v201, s[8:11], s43 offen sc1\n" \
"buffer_load_dwordx4 a[212:215], v201, s[8:11], s43 offen offset:1024 sc1\n" \
"s_add_u32 s43, s30, 5\n" \
"s_mov_b32 s50, 0\n" \
"s_branch .LX1_w68c\n" \
".LX1_w68l:\n" \
"buffer_load_dword v240, v204, s[4:7], 0 offen sc1\n" \
"s_waitcnt vmcnt(0)\n" \
".LX1_w68c:\n" \
"v_subrev_u32 v241, s43, v240\n" \
"v_cmp_gt_u32 vcc, 0x100, v241\n" \
"s_cmp_eq_u64 vcc, -1\n" \
"s_cbranch_scc1 .LX1_w68d\n" \
"s_sleep 2\n" \
"s_add_u32 s50, s50, 1\n" \
"s_cmp_lt_u32 s50, 0x20000\n" \
"s_cbranch_scc1 .LX1_w68l\n" \
"s_branch .LX1_end\n" \
".LX1_w68d:\n" \
"v_readlane_b32 s43, v202, 2\n" \
"s_nop 4\n" \
"buffer_load_dwordx4 a[16:19], v201, s[8:11], s43 offen sc1\n" \
"buffer_load_dwordx4 a[20:23], v201, s[8:11], s43 offen offset:1024 sc1\n" \
"v_readlane_b32 s43, v202, 9\n" \
"s_nop 4\n" \
"buffer_load_dwordx4 a[72:75], v201, s[8:11], s43 offen sc1\n" \
"buffer_load_dwordx4 a[76:79], v201, s[8:11], s43 offen offset:1024 sc1\n" \
"v_readlane_b32 s43, v202, 16\n" \
"s_nop 4\n" \
"buffer_load_dwordx4 a[128:131], v201, s[8:11], s43 offen sc1\n" \
"buffer_load_dwordx4 a[132:135], v201, s[8:11], s43 offen offset:1024 sc1\n" \
"v_readlane_b32 s43, v202, 23\n" \
"s_nop 4\n" \
"buffer_load_dwordx4 a[184:187], v201, s[8:11], s43 offen sc1\n" \
"buffer_load_dwordx4 a[188:191], v201, s[8:11], s43 offen offset:1024 sc1\n" \
"s_add_u32 s43, s30, 6\n" \
"s_mov_b32 s50, 0\n" \
"s_branch .LX1_w69c\n" \
".LX1_w69l:\n" \
"buffer_load_dword v240, v204, s[4:7], 0 offen sc1\n" \
"s_waitcnt vmcnt(0)\n" \
".LX1_w69c:\n" \
"v_subrev_u32 v241, s43, v240\n" \
"v_cmp_gt_u32 vcc, 0x100, v241\n" \
"s_cmp_eq_u64 vcc, -1\n" \
"s_cbranch_scc1 .LX1_w69d\n" \
"s_sleep 2\n" \
"s_add_u32 s50, s50, 1\n" \
"s_cmp_lt_u32 s50, 0x20000\n" \
"s_cbranch_scc1 .LX1_w69l\n" \
"s_branch .LX1_end\n" \
".LX1_w69d:\n" \
"v_readlane_b32 s43, v202, 6\n" \
"s_nop 4\n" \
"buffer_load_dwordx4 a[48:51], v201, s[8:11], s43 offen sc1\n" \
"buffer_load_dwordx4 a[52:55], v201, s[8:11], s43 offen offset:1024 sc1\n" \
"v_readlane_b32 s43, v202, 13\n" \
"s_nop 4\n" \
"buffer_load_dwordx4 a[104:107], v201, s[8:11], s43 offen sc1\n" \
"buffer_load_dwordx4 a[108:111], v201, s[8:11], s43 offen offset:1024 sc1\n" \
"v_readlane_b32 s43, v202, 20\n" \
"s_nop 4\n" \
"buffer_load_dwordx4 a[160:163], v201, s[8:11], s43 offen sc1\n" \
"buffer_load_dwordx4 a[164:167], v201, s[8:11], s43 offen offset:1024 sc1\n" \
"v_readlane_b32 s43, v202, 27\n" \
"s_nop 4\n" \
"buffer_load_dwordx4 a[216:219], v201, s[8:11], s43 offen sc1\n" \
"buffer_load_dwordx4 a[220:223], v201, s[8:11], s43 offen offset:1024 sc1\n" \
"s_add_u32 s43, s30, 7\n" \
"s_mov_b32 s50, 0\n" \
"s_branch .LX1_w70c\n" \
".LX1_w70l:\n" \
"buffer_load_dword v240, v204, s[4:7], 0 offen sc1\n" \
"s_waitcnt vmcnt(0)\n" \
".LX1_w70c:\n" \
"v_subrev_u32 v241, s43, v240\n" \
"v_cmp_gt_u32 vcc, 0x100, v241\n" \
"s_cmp_eq_u64 vcc, -1\n" \
"s_cbranch_scc1 .LX1_w70d\n" \
"s_sleep 2\n" \
"s_add_u32 s50, s50, 1\n" \
"s_cmp_lt_u32 s50, 0x20000\n" \
"s_cbranch_scc1 .LX1_w70l\n" \
"s_branch .LX1_end\n" \
".LX1_w70d:\n" \
"v_readlane_b32 s43, v202, 3\n" \
"s_nop 4\n" \
"buffer_load_dwordx4 a[24:27], v201, s[8:11], s43 offen sc1\n" \
"buffer_load_dwordx4 a[28:31], v201, s[8:11], s43 offen offset:1024 sc1\n" \
"v_readlane_b32 s43, v202, 10\n" \
"s_nop 4\n" \
"buffer_load_dwordx4 a[80:83], v201, s[8:11], s43 offen sc1\n" \
"buffer_load_dwordx4 a[84:87], v201, s[8:11], s43 offen offset:1024 sc1\n" \
"v_readlane_b32 s43, v202, 17\n" \
"s_nop 4\n" \
"buffer_load_dwordx4 a[136:139], v201, s[8:11], s43 offen sc1\n" \
"buffer_load_dwordx4 a[140:143], v201, s[8:11], s43 offen offset:1024 sc1\n" \
"v_readlane_b32 s43, v202, 24\n" \
"s_nop 4\n" \
"buffer_load_dwordx4 a[192:195], v201, s[8:11], s43 offen sc1\n" \
"buffer_load_dwordx4 a[196:199], v201, s[8:11], s43 offen offset:1024 sc1\n" \
"s_add_u32 s43, s30, 1\n" \
"s_mov_b32 s50, 0\n" \
".LX1_w71l:\n" \
"buffer_load_dword v240, v205, s[4:7], 0 offen sc1\n" \
"s_waitcnt vmcnt(0)\n" \
".LX1_w71c:\n" \
"v_subrev_u32 v241, s43, v240\n" \
"v_cmp_gt_u32 vcc, 0x100, v241\n" \
"s_cmp_eq_u64 vcc, -1\n" \
"s_cbranch_scc1 .LX1_w71d\n" \
"s_sleep 2\n" \
"s_add_u32 s50, s50, 1\n" \
"s_cmp_lt_u32 s50, 0x20000\n" \
"s_cbranch_scc1 .LX1_w71l\n" \
"s_branch .LX1_end\n" \
".LX1_w71d:\n" \
"v_readfirstlane_b32 s31, v241\n" \
"s_lshr_b32 s32, s31, 2\n" \
"s_xor_b32 s29, s31, s32\n" \
"s_and_b32 s29, s29, 3\n" \
"s_lshl_b32 s29, s29, 13\n" \
"s_bitset1_b32 s29, 21\n" \
"s_and_b32 s31, s31, 3\n" \
"s_lshl_b32 s31, s31, 13\n" \
"s_bitcmp1_b32 s39, 0\n" \
"s_cselect_b32 s43, s29, 0\n" \
"s_xor_b32 s31, s31, s43\n" \
"s_xor_b32 s32, s31, 0x4000\n" \
"s_mov_b32 s39, 0\n" \
"s_add_u32 s43, s30, 8\n" \
"s_mov_b32 s50, 0\n" \
".LX1_w72l:\n" \
"buffer_load_dword v240, v215, s[4:7], 0 offen sc1\n" \
"s_waitcnt vmcnt(0)\n" \
".LX1_w72c:\n" \
"v_subrev_u32 v241, s43, v240\n" \
"v_cmp_gt_u32 vcc, 0x100, v241\n" \
"s_cmp_eq_u64 vcc, -1\n" \
"s_cbranch_scc1 .LX1_w72d\n" \
"s_sleep 2\n" \
"s_add_u32 s50, s50, 1\n" \
"s_cmp_lt_u32 s50, 0x20000\n" \
"s_cbranch_scc1 .LX1_w72l\n" \
"s_branch .LX1_end\n" \
".LX1_w72d:\n" \
"s_add_u32 s50, s40, 0\n" \
"s_mul_i32 s50, s50, 28672\n" \
"s_add_u32 s50, s50, s34\n" \
"buffer_load_dwordx4 v[28:31], v201, s[12:15], s50 offen sc1\n" \
"buffer_load_dwordx4 v[32:35], v201, s[12:15], s50 offen offset:1024 sc1\n" \
"buffer_load_dwordx4 v[36:39], v201, s[12:15], s50 offen offset:2048 sc1\n" \
"buffer_load_dwordx4 v[40:43], v201, s[12:15], s50 offen offset:3072 sc1\n" \
"s_waitcnt vmcnt(0)\n" \
"s_barrier\n" \
"s_add_u32 s43, s30, 9\n" \
"s_mov_b32 s50, 0\n" \
".LX1_w73l:\n" \
"buffer_load_dword v240, v215, s[4:7], 0 offen sc1\n" \
"s_waitcnt vmcnt(0)\n" \
".LX1_w73c:\n" \
"v_subrev_u32 v241, s43, v240\n" \
"v_cmp_gt_u32 vcc, 0x100, v241\n" \
"s_cmp_eq_u64 vcc, -1\n" \
"s_cbranch_scc1 .LX1_w73d\n" \
"s_sleep 2\n" \
"s_add_u32 s50, s50, 1\n" \
"s_cmp_lt_u32 s50, 0x20000\n" \
"s_cbranch_scc1 .LX1_w73l\n" \
"s_branch .LX1_end\n" \
".LX1_w73d:\n" \
".LX1_pre:\n" \
".LX1_loop:\n" \
"s_add_u32 s43, s40, 2\n" \
"s_and_b32 s43, s43, 3\n" \
"s_bitcmp1_b32 s43, 0\n" \
"s_cselect_b32 s49, s32, s31\n" \
"s_lshl_b32 s43, s43, 19\n" \
"s_add_u32 s49, s49, s43\n" \
"ds_read_b128 v[0:3], v216 offset:0\n" \
"ds_read_b128 v[4:7], v216 offset:1024\n" \
"ds_read_b128 v[8:11], v216 offset:2048\n" \
"ds_read_b128 v[12:15], v216 offset:3072\n" \
"ds_read_b128 v[16:19], v216 offset:4096\n" \
"ds_read_b128 v[20:23], v216 offset:5120\n" \
"ds_read_b128 v[24:27], v216 offset:6144\n" \
"v_mov_b32 v60, 0\n" \
"v_mov_b32 v61, 0\n" \
"v_mov_b32 v62, 0\n" \
"v_mov_b32 v63, 0\n" \
"v_mov_b32 v64, 0\n" \
"v_mov_b32 v65, 0\n" \
"v_mov_b32 v66, 0\n" \
"v_mov_b32 v67, 0\n" \
"v_mov_b32 v68, 0\n" \
"v_mov_b32 v69, 0\n" \
"v_mov_b32 v70, 0\n" \
"v_mov_b32 v71, 0\n" \
"v_mov_b32 v72, 0\n" \
"v_mov_b32 v73, 0\n" \
"v_mov_b32 v74, 0\n" \
"v_mov_b32 v75, 0\n" \
"s_nop 1\n" \
"s_waitcnt lgkmcnt(6)\n" \
"v_smfmac_f32_16x16x64_bf16 v[60:63], v[0:3], a[0:7], v200\n" \
"v_smfmac_f32_16x16x64_bf16 v[64:67], v[0:3], a[56:63], v200\n" \
"v_smfmac_f32_16x16x64_bf16 v[68:71], v[0:3], a[112:119], v200\n" \
"v_smfmac_f32_16x16x64_bf16 v[72:75], v[0:3], a[168:175], v200\n" \
"s_waitcnt lgkmcnt(5)\n" \
"v_smfmac_f32_16x16x64_bf16 v[60:63], v[4:7], a[8:15], v200\n" \
"v_smfmac_f32_16x16x64_bf16 v[64:67], v[4:7], a[64:71], v200\n" \
"v_smfmac_f32_16x16x64_bf16 v[68:71], v[4:7], a[120:127], v200\n" \
"v_smfmac_f32_16x16x64_bf16 v[72:75], v[4:7], a[176:183], v200\n" \
"s_waitcnt lgkmcnt(4)\n" \
"v_smfmac_f32_16x16x64_bf16 v[60:63], v[8:11], a[16:23], v200\n" \
"v_smfmac_f32_16x16x64_bf16 v[64:67], v[8:11], a[72:79], v200\n" \
"v_smfmac_f32_16x16x64_bf16 v[68:71], v[8:11], a[128:135], v200\n" \
"v_smfmac_f32_16x16x64_bf16 v[72:75], v[8:11], a[184:191], v200\n" \
"s_waitcnt lgkmcnt(3)\n" \
"v_smfmac_f32_16x16x64_bf16 v[60:63], v[12:15], a[24:31], v200\n" \
"v_smfmac_f32_16x16x64_bf16 v[64:67], v[12:15], a[80:87], v200\n" \
"v_smfmac_f32_16x16x64_bf16 v[68:71], v[12:15], a[136:143], v200\n" \
"v_smfmac_f32_16x16x64_bf16 v[72:75], v[12:15], a[192:199], v200\n" \
"s_waitcnt lgkmcnt(2)\n" \
"v_smfmac_f32_16x16x64_bf16 v[60:63], v[16:19], a[32:39], v200\n" \
"v_smfmac_f32_16x16x64_bf16 v[64:67], v[16:19], a[88:95], v200\n" \
"v_smfmac_f32_16x16x64_bf16 v[68:71], v[16:19], a[144:151], v200\n" \
"v_smfmac_f32_16x16x64_bf16 v[72:75], v[16:19], a[200:207], v200\n" \
"s_waitcnt lgkmcnt(1)\n" \
"v_smfmac_f32_16x16x64_bf16 v[60:63], v[20:23], a[40:47], v200\n" \
"v_smfmac_f32_16x16x64_bf16 v[64:67], v[20:23], a[96:103], v200\n" \
"v_smfmac_f32_16x16x64_bf16 v[68:71], v[20:23], a[152:159], v200\n" \
"v_smfmac_f32_16x16x64_bf16 v[72:75], v[20:23], a[208:215], v200\n" \
"s_waitcnt lgkmcnt(0)\n" \
"v_smfmac_f32_16x16x64_bf16 v[60:63], v[24:27], a[48:55], v200\n" \
"v_smfmac_f32_16x16x64_bf16 v[64:67], v[24:27], a[104:111], v200\n" \
"v_smfmac_f32_16x16x64_bf16 v[68:71], v[24:27], a[160:167], v200\n" \
"v_smfmac_f32_16x16x64_bf16 v[72:75], v[24:27], a[216:223], v200\n" \
"s_barrier\n" \
"s_add_u32 s50, s40, 1\n" \
"s_mul_i32 s50, s50, 28672\n" \
"s_add_u32 s50, s50, s34\n" \
"buffer_load_dwordx4 v[44:47], v201, s[12:15], s50 offen sc1\n" \
"buffer_load_dwordx4 v[48:51], v201, s[12:15], s50 offen offset:1024 sc1\n" \
"buffer_load_dwordx4 v[52:55], v201, s[12:15], s50 offen offset:2048 sc1\n" \
"buffer_load_dwordx4 v[56:59], v201, s[12:15], s50 offen offset:3072 sc1\n" \
"s_add_u32 s43, s30, s40\n" \
"s_add_u32 s43, s43, -1\n" \
"v_mov_b32 v241, s43\n" \
"buffer_store_dword v241, v220, s[4:7], 0 offen sc1\n" \
"s_nop 7\n" \
"v_permlane32_swap_b32 v60, v64\n" \
"v_permlane32_swap_b32 v61, v65\n" \
"v_permlane32_swap_b32 v62, v66\n" \
"v_permlane32_swap_b32 v63, v67\n" \
"v_add_f32 v60, v60, v64\n" \
"v_add_f32 v61, v61, v65\n" \
"v_add_f32 v62, v62, v66\n" \
"v_add_f32 v63, v63, v67\n" \
"v_cvt_pk_bf16_f32 v184, v60, v61\n" \
"v_cvt_pk_bf16_f32 v185, v62, v63\n" \
"ds_write_b64 v217, v[184:185] offset:0\n" \
"v_permlane32_swap_b32 v68, v72\n" \
"v_permlane32_swap_b32 v69, v73\n" \
"v_permlane32_swap_b32 v70, v74\n" \
"v_permlane32_swap_b32 v71, v75\n" \
"v_add_f32 v68, v68, v72\n" \
"v_add_f32 v69, v69, v73\n" \
"v_add_f32 v70, v70, v74\n" \
"v_add_f32 v71, v71, v75\n" \
"v_cvt_pk_bf16_f32 v186, v68, v69\n" \
"v_cvt_pk_bf16_f32 v187, v70, v71\n" \
"ds_write_b64 v217, v[186:187] offset:512\n" \
"ds_read_b64_tr_b16 v[0:1], v218 offset:0\n" \
"ds_read_b64_tr_b16 v[2:3], v218 offset:64\n" \
"s_add_u32 s43, s40, 0\n" \
"s_mul_i32 s43, s43, 7168\n" \
"s_waitcnt lgkmcnt(0)\n" \
"buffer_store_dwordx4 v[0:3], v219, s[16:19], s43 offen sc1\n" \
"s_waitcnt vmcnt(6)\n" \
"s_barrier\n" \
"ds_read_b64_tr_b16 v[92:93], v213\n" \
"ds_read_b64_tr_b16 v[94:95], v213 offset:64\n" \
"ds_read_b32 v84, v252 offset:16\n" \
"v_mov_b32 v245, v245\n" \
"s_nop 2\n" \
"v_mov_b32 v245, v245\n" \
"s_nop 2\n" \
"v_mov_b32 v245, v245\n" \
"s_waitcnt lgkmcnt(1)\n" \
"v_mfma_f32_16x16x32_bf16 v[68:71], v[92:95], v[28:31], 0\n" \
"s_waitcnt lgkmcnt(0)\n" \
"v_xor_b32 v85, s29, v84\n" \
"v_mfma_f32_16x16x32_bf16 v[76:79], v[92:95], v[36:39], 0\n" \
"v_add_u32 v84, v84, v210\n" \
"v_add_u32 v85, v85, v210\n" \
"v_mfma_f32_16x16x32_bf16 v[72:75], v[92:95], v[32:35], 0\n" \
"s_nop 1\n" \
"v_mfma_f32_16x16x32_bf16 v[80:83], v[92:95], v[40:43], 0\n" \
"v_cvt_pk_bf16_f32 v96, v68, v69\n" \
"v_cvt_pk_bf16_f32 v97, v70, v71\n" \
"v_cvt_pk_bf16_f32 v98, v76, v77\n" \
"v_cvt_pk_bf16_f32 v99, v78, v79\n" \
"s_mov_b64 exec, s[2:3]\n" \
"v_cvt_pk_bf16_f32 v96, v72, v73\n" \
"v_cvt_pk_bf16_f32 v97, v74, v75\n" \
"s_nop 0\n" \
"v_cvt_pk_bf16_f32 v98, v80, v81\n" \
"v_cvt_pk_bf16_f32 v99, v82, v83\n" \
"s_mov_b64 exec, -1\n" \
"s_nop 1\n" \
"v_permlane16_swap_b32 v96, v98\n" \
"v_permlane16_swap_b32 v97, v99\n" \
"buffer_store_dwordx4 v[96:99], v84, s[4:7], 0 offen sc1\n" \
"s_barrier\n" \
"buffer_store_dwordx4 v[96:99], v85, s[4:7], 0 offen sc1\n" \
"buffer_store_dwordx4 v[100:103], v214, s[4:7], s49 offen sc1\n" \
"s_xor_b32 s43, s49, s29\n" \
"buffer_store_dwordx4 v[100:103], v214, s[4:7], s43 offen sc1\n" \
"s_waitcnt vmcnt(4)\n" \
"v_mov_b32 v28, v44\n" \
"v_mov_b32 v29, v45\n" \
"v_mov_b32 v30, v46\n" \
"v_mov_b32 v31, v47\n" \
"v_mov_b32 v32, v48\n" \
"v_mov_b32 v33, v49\n" \
"v_mov_b32 v34, v50\n" \
"v_mov_b32 v35, v51\n" \
"v_mov_b32 v36, v52\n" \
"v_mov_b32 v37, v53\n" \
"v_mov_b32 v38, v54\n" \
"v_mov_b32 v39, v55\n" \
"v_mov_b32 v40, v56\n" \
"v_mov_b32 v41, v57\n" \
"v_mov_b32 v42, v58\n" \
"v_mov_b32 v43, v59\n" \
"s_add_u32 s40, s40, 1\n" \
"s_cmp_eq_u32 s40, 0\n" \
"s_cbranch_scc1 .LX1_fin\n" \
"s_cmp_lt_u32 s40, 63\n" \
"s_cbranch_scc1 .LX1_loop\n" \
".LX1_fin:\n" \
"s_barrier\n" \
"s_add_u32 s43, s30, s40\n" \
"s_add_u32 s43, s43, -1\n" \
"v_mov_b32 v241, s43\n" \
"buffer_store_dword v241, v220, s[4:7], 0 offen sc1\n" \
"ds_read_b128 v[28:31], v223 offset:0\n" \
"ds_read_b128 v[32:35], v199 offset:0\n" \
"ds_read_b128 v[36:39], v223 offset:128\n" \
"ds_read_b128 v[40:43], v199 offset:128\n" \
"ds_read_b128 v[44:47], v223 offset:256\n" \
"ds_read_b128 v[48:51], v199 offset:256\n" \
"ds_read_b128 v[52:55], v223 offset:384\n" \
"ds_read_b128 v[0:3], v216 offset:0\n" \
"ds_read_b128 v[4:7], v216 offset:1024\n" \
"ds_read_b128 v[8:11], v216 offset:2048\n" \
"ds_read_b128 v[12:15], v216 offset:3072\n" \
"ds_read_b128 v[16:19], v216 offset:4096\n" \
"ds_read_b128 v[20:23], v216 offset:5120\n" \
"ds_read_b128 v[24:27], v216 offset:6144\n" \
"v_mov_b32 v60, 0\n" \
"v_mov_b32 v61, 0\n" \
"v_mov_b32 v62, 0\n" \
"v_mov_b32 v63, 0\n" \
"v_mov_b32 v64, 0\n" \
"v_mov_b32 v65, 0\n" \
"v_mov_b32 v66, 0\n" \
"v_mov_b32 v67, 0\n" \
"v_mov_b32 v68, 0\n" \
"v_mov_b32 v69, 0\n" \
"v_mov_b32 v70, 0\n" \
"v_mov_b32 v71, 0\n" \
"v_mov_b32 v72, 0\n" \
"v_mov_b32 v73, 0\n" \
"v_mov_b32 v74, 0\n" \
"v_mov_b32 v75, 0\n" \
"s_nop 1\n" \
"s_waitcnt lgkmcnt(6)\n" \
"v_smfmac_f32_16x16x64_bf16 v[60:63], v[0:3], a[0:7], v200\n" \
"v_smfmac_f32_16x16x64_bf16 v[64:67], v[0:3], a[56:63], v200\n" \
"v_smfmac_f32_16x16x64_bf16 v[68:71], v[0:3], a[112:119], v200\n" \
"v_smfmac_f32_16x16x64_bf16 v[72:75], v[0:3], a[168:175], v200\n" \
"s_waitcnt lgkmcnt(5)\n" \
"v_smfmac_f32_16x16x64_bf16 v[60:63], v[4:7], a[8:15], v200\n" \
"v_smfmac_f32_16x16x64_bf16 v[64:67], v[4:7], a[64:71], v200\n" \
"v_smfmac_f32_16x16x64_bf16 v[68:71], v[4:7], a[120:127], v200\n" \
"v_smfmac_f32_16x16x64_bf16 v[72:75], v[4:7], a[176:183], v200\n" \
"s_waitcnt lgkmcnt(4)\n" \
"v_smfmac_f32_16x16x64_bf16 v[60:63], v[8:11], a[16:23], v200\n" \
"v_smfmac_f32_16x16x64_bf16 v[64:67], v[8:11], a[72:79], v200\n" \
"v_smfmac_f32_16x16x64_bf16 v[68:71], v[8:11], a[128:135], v200\n" \
"v_smfmac_f32_16x16x64_bf16 v[72:75], v[8:11], a[184:191], v200\n" \
"s_waitcnt lgkmcnt(3)\n" \
"v_smfmac_f32_16x16x64_bf16 v[60:63], v[12:15], a[24:31], v200\n" \
"v_smfmac_f32_16x16x64_bf16 v[64:67], v[12:15], a[80:87], v200\n" \
"v_smfmac_f32_16x16x64_bf16 v[68:71], v[12:15], a[136:143], v200\n" \
"v_smfmac_f32_16x16x64_bf16 v[72:75], v[12:15], a[192:199], v200\n" \
"s_waitcnt lgkmcnt(2)\n" \
"v_smfmac_f32_16x16x64_bf16 v[60:63], v[16:19], a[32:39], v200\n" \
"v_smfmac_f32_16x16x64_bf16 v[64:67], v[16:19], a[88:95], v200\n" \
"v_smfmac_f32_16x16x64_bf16 v[68:71], v[16:19], a[144:151], v200\n" \
"v_smfmac_f32_16x16x64_bf16 v[72:75], v[16:19], a[200:207], v200\n" \
"s_waitcnt lgkmcnt(1)\n" \
"v_smfmac_f32_16x16x64_bf16 v[60:63], v[20:23], a[40:47], v200\n" \
"v_smfmac_f32_16x16x64_bf16 v[64:67], v[20:23], a[96:103], v200\n" \
"v_smfmac_f32_16x16x64_bf16 v[68:71], v[20:23], a[152:159], v200\n" \
"v_smfmac_f32_16x16x64_bf16 v[72:75], v[20:23], a[208:215], v200\n" \
"s_waitcnt lgkmcnt(0)\n" \
"v_smfmac_f32_16x16x64_bf16 v[60:63], v[24:27], a[48:55], v200\n" \
"v_smfmac_f32_16x16x64_bf16 v[64:67], v[24:27], a[104:111], v200\n" \
"v_smfmac_f32_16x16x64_bf16 v[68:71], v[24:27], a[160:167], v200\n" \
"v_smfmac_f32_16x16x64_bf16 v[72:75], v[24:27], a[216:223], v200\n" \
"s_nop 7\n" \
"v_permlane32_swap_b32 v60, v64\n" \
"v_permlane32_swap_b32 v61, v65\n" \
"v_permlane32_swap_b32 v62, v66\n" \
"v_permlane32_swap_b32 v63, v67\n" \
"v_add_f32 v60, v60, v64\n" \
"v_add_f32 v61, v61, v65\n" \
"v_add_f32 v62, v62, v66\n" \
"v_add_f32 v63, v63, v67\n" \
"v_cvt_pk_bf16_f32 v184, v60, v61\n" \
"v_cvt_pk_bf16_f32 v185, v62, v63\n" \
"ds_write_b64 v217, v[184:185] offset:0\n" \
"v_permlane32_swap_b32 v68, v72\n" \
"v_permlane32_swap_b32 v69, v73\n" \
"v_permlane32_swap_b32 v70, v74\n" \
"v_permlane32_swap_b32 v71, v75\n" \
"v_add_f32 v68, v68, v72\n" \
"v_add_f32 v69, v69, v73\n" \
"v_add_f32 v70, v70, v74\n" \
"v_add_f32 v71, v71, v75\n" \
"v_cvt_pk_bf16_f32 v186, v68, v69\n" \
"v_cvt_pk_bf16_f32 v187, v70, v71\n" \
"ds_write_b64 v217, v[186:187] offset:512\n" \
"s_waitcnt lgkmcnt(0)\n" \
"s_barrier\n" \
"ds_read_b64_tr_b16 v[0:1], v222 offset:0\n" \
"ds_read_b64_tr_b16 v[2:3], v222 offset:64\n" \
"ds_read_b64_tr_b16 v[4:5], v222 offset:512\n" \
"ds_read_b64_tr_b16 v[6:7], v222 offset:576\n" \
"ds_read_b64_tr_b16 v[8:9], v222 offset:2048\n" \
"ds_read_b64_tr_b16 v[10:11], v222 offset:2112\n" \
"ds_read_b64_tr_b16 v[12:13], v222 offset:2560\n" \
"ds_read_b64_tr_b16 v[14:15], v222 offset:2624\n" \
"ds_read_b64_tr_b16 v[16:17], v222 offset:4096\n" \
"ds_read_b64_tr_b16 v[18:19], v222 offset:4160\n" \
"ds_read_b64_tr_b16 v[20:21], v222 offset:4608\n" \
"ds_read_b64_tr_b16 v[22:23], v222 offset:4672\n" \
"ds_read_b64_tr_b16 v[24:25], v222 offset:5120\n" \
"ds_read_b64_tr_b16 v[26:27], v222 offset:5184\n" \
"s_waitcnt lgkmcnt(12)\n" \
"v_mfma_f32_16x16x32_bf16 v[96:99], v[0:3], v[28:31], 0\n" \
"s_waitcnt lgkmcnt(10)\n" \
"v_mfma_f32_16x16x32_bf16 v[96:99], v[4:7], v[32:35], v[96:99]\n" \
"s_waitcnt lgkmcnt(8)\n" \
"v_mfma_f32_16x16x32_bf16 v[96:99], v[8:11], v[36:39], v[96:99]\n" \
"s_waitcnt lgkmcnt(6)\n" \
"v_mfma_f32_16x16x32_bf16 v[96:99], v[12:15], v[40:43], v[96:99]\n" \
"s_waitcnt lgkmcnt(4)\n" \
"v_mfma_f32_16x16x32_bf16 v[96:99], v[16:19], v[44:47], v[96:99]\n" \
"s_waitcnt lgkmcnt(2)\n" \
"v_mfma_f32_16x16x32_bf16 v[96:99], v[20:23], v[48:51], v[96:99]\n" \
"s_waitcnt lgkmcnt(0)\n" \
"v_mfma_f32_16x16x32_bf16 v[96:99], v[24:27], v[52:55], v[96:99]\n" \
"s_nop 7\n" \
"buffer_store_dwordx4 v[96:99], v224, s[16:19], 0 offen sc1\n" \
"ds_read_b128 v[28:31], v223 offset:28672\n" \
"ds_read_b128 v[32:35], v199 offset:28672\n" \
"ds_read_b128 v[36:39], v223 offset:28800\n" \
"ds_read_b128 v[40:43], v199 offset:28800\n" \
"ds_read_b128 v[44:47], v223 offset:28928\n" \
"ds_read_b128 v[48:51], v199 offset:28928\n" \
"ds_read_b128 v[52:55], v223 offset:29056\n" \
"s_waitcnt lgkmcnt(0)\n" \
"s_barrier\n" \
"ds_read_b128 v[0:3], v216 offset:7168\n" \
"ds_read_b128 v[4:7], v216 offset:8192\n" \
"ds_read_b128 v[8:11], v216 offset:9216\n" \
"ds_read_b128 v[12:15], v216 offset:10240\n" \
"ds_read_b128 v[16:19], v216 offset:11264\n" \
"ds_read_b128 v[20:23], v216 offset:12288\n" \
"ds_read_b128 v[24:27], v216 offset:13312\n" \
"v_mov_b32 v60, 0\n" \
"v_mov_b32 v61, 0\n" \
"v_mov_b32 v62, 0\n" \
"v_mov_b32 v63, 0\n" \
"v_mov_b32 v64, 0\n" \
"v_mov_b32 v65, 0\n" \
"v_mov_b32 v66, 0\n" \
"v_mov_b32 v67, 0\n" \
"v_mov_b32 v68, 0\n" \
"v_mov_b32 v69, 0\n" \
"v_mov_b32 v70, 0\n" \
"v_mov_b32 v71, 0\n" \
"v_mov_b32 v72, 0\n" \
"v_mov_b32 v73, 0\n" \
"v_mov_b32 v74, 0\n" \
"v_mov_b32 v75, 0\n" \
"s_nop 1\n" \
"s_waitcnt lgkmcnt(6)\n" \
"v_smfmac_f32_16x16x64_bf16 v[60:63], v[0:3], a[0:7], v200\n" \
"v_smfmac_f32_16x16x64_bf16 v[64:67], v[0:3], a[56:63], v200\n" \
"v_smfmac_f32_16x16x64_bf16 v[68:71], v[0:3], a[112:119], v200\n" \
"v_smfmac_f32_16x16x64_bf16 v[72:75], v[0:3], a[168:175], v200\n" \
"s_waitcnt lgkmcnt(5)\n" \
"v_smfmac_f32_16x16x64_bf16 v[60:63], v[4:7], a[8:15], v200\n" \
"v_smfmac_f32_16x16x64_bf16 v[64:67], v[4:7], a[64:71], v200\n" \
"v_smfmac_f32_16x16x64_bf16 v[68:71], v[4:7], a[120:127], v200\n" \
"v_smfmac_f32_16x16x64_bf16 v[72:75], v[4:7], a[176:183], v200\n" \
"s_waitcnt lgkmcnt(4)\n" \
"v_smfmac_f32_16x16x64_bf16 v[60:63], v[8:11], a[16:23], v200\n" \
"v_smfmac_f32_16x16x64_bf16 v[64:67], v[8:11], a[72:79], v200\n" \
"v_smfmac_f32_16x16x64_bf16 v[68:71], v[8:11], a[128:135], v200\n" \
"v_smfmac_f32_16x16x64_bf16 v[72:75], v[8:11], a[184:191], v200\n" \
"s_waitcnt lgkmcnt(3)\n" \
"v_smfmac_f32_16x16x64_bf16 v[60:63], v[12:15], a[24:31], v200\n" \
"v_smfmac_f32_16x16x64_bf16 v[64:67], v[12:15], a[80:87], v200\n" \
"v_smfmac_f32_16x16x64_bf16 v[68:71], v[12:15], a[136:143], v200\n" \
"v_smfmac_f32_16x16x64_bf16 v[72:75], v[12:15], a[192:199], v200\n" \
"s_waitcnt lgkmcnt(2)\n" \
"v_smfmac_f32_16x16x64_bf16 v[60:63], v[16:19], a[32:39], v200\n" \
"v_smfmac_f32_16x16x64_bf16 v[64:67], v[16:19], a[88:95], v200\n" \
"v_smfmac_f32_16x16x64_bf16 v[68:71], v[16:19], a[144:151], v200\n" \
"v_smfmac_f32_16x16x64_bf16 v[72:75], v[16:19], a[200:207], v200\n" \
"s_waitcnt lgkmcnt(1)\n" \
"v_smfmac_f32_16x16x64_bf16 v[60:63], v[20:23], a[40:47], v200\n" \
"v_smfmac_f32_16x16x64_bf16 v[64:67], v[20:23], a[96:103], v200\n" \
"v_smfmac_f32_16x16x64_bf16 v[68:71], v[20:23], a[152:159], v200\n" \
"v_smfmac_f32_16x16x64_bf16 v[72:75], v[20:23], a[208:215], v200\n" \
"s_waitcnt lgkmcnt(0)\n" \
"v_smfmac_f32_16x16x64_bf16 v[60:63], v[24:27], a[48:55], v200\n" \
"v_smfmac_f32_16x16x64_bf16 v[64:67], v[24:27], a[104:111], v200\n" \
"v_smfmac_f32_16x16x64_bf16 v[68:71], v[24:27], a[160:167], v200\n" \
"v_smfmac_f32_16x16x64_bf16 v[72:75], v[24:27], a[216:223], v200\n" \
"s_nop 7\n" \
"v_permlane32_swap_b32 v60, v64\n" \
"v_permlane32_swap_b32 v61, v65\n" \
"v_permlane32_swap_b32 v62, v66\n" \
"v_permlane32_swap_b32 v63, v67\n" \
"v_add_f32 v60, v60, v64\n" \
"v_add_f32 v61, v61, v65\n" \
"v_add_f32 v62, v62, v66\n" \
"v_add_f32 v63, v63, v67\n" \
"v_cvt_pk_bf16_f32 v184, v60, v61\n" \
"v_cvt_pk_bf16_f32 v185, v62, v63\n" \
"ds_write_b64 v217, v[184:185] offset:0\n" \
"v_permlane32_swap_b32 v68, v72\n" \
"v_permlane32_swap_b32 v69, v73\n" \
"v_permlane32_swap_b32 v70, v74\n" \
"v_permlane32_swap_b32 v71, v75\n" \
"v_add_f32 v68, v68, v72\n" \
"v_add_f32 v69, v69, v73\n" \
"v_add_f32 v70, v70, v74\n" \
"v_add_f32 v71, v71, v75\n" \
"v_cvt_pk_bf16_f32 v186, v68, v69\n" \
"v_cvt_pk_bf16_f32 v187, v70, v71\n" \
"ds_write_b64 v217, v[186:187] offset:512\n" \
"s_waitcnt lgkmcnt(0)\n" \
"s_barrier\n" \
"ds_read_b64_tr_b16 v[0:1], v222 offset:0\n" \
"ds_read_b64_tr_b16 v[2:3], v222 offset:64\n" \
"ds_read_b64_tr_b16 v[4:5], v222 offset:512\n" \
"ds_read_b64_tr_b16 v[6:7], v222 offset:576\n" \
"ds_read_b64_tr_b16 v[8:9], v222 offset:2048\n" \
"ds_read_b64_tr_b16 v[10:11], v222 offset:2112\n" \
"ds_read_b64_tr_b16 v[12:13], v222 offset:2560\n" \
"ds_read_b64_tr_b16 v[14:15], v222 offset:2624\n" \
"ds_read_b64_tr_b16 v[16:17], v222 offset:4096\n" \
"ds_read_b64_tr_b16 v[18:19], v222 offset:4160\n" \
"ds_read_b64_tr_b16 v[20:21], v222 offset:4608\n" \
"ds_read_b64_tr_b16 v[22:23], v222 offset:4672\n" \
"ds_read_b64_tr_b16 v[24:25], v222 offset:5120\n" \
"ds_read_b64_tr_b16 v[26:27], v222 offset:5184\n" \
"s_waitcnt lgkmcnt(12)\n" \
"v_mfma_f32_16x16x32_bf16 v[96:99], v[0:3], v[28:31], 0\n" \
"s_waitcnt lgkmcnt(10)\n" \
"v_mfma_f32_16x16x32_bf16 v[96:99], v[4:7], v[32:35], v[96:99]\n" \
"s_waitcnt lgkmcnt(8)\n" \
"v_mfma_f32_16x16x32_bf16 v[96:99], v[8:11], v[36:39], v[96:99]\n" \
"s_waitcnt lgkmcnt(6)\n" \
"v_mfma_f32_16x16x32_bf16 v[96:99], v[12:15], v[40:43], v[96:99]\n" \
"s_waitcnt lgkmcnt(4)\n" \
"v_mfma_f32_16x16x32_bf16 v[96:99], v[16:19], v[44:47], v[96:99]\n" \
"s_waitcnt lgkmcnt(2)\n" \
"v_mfma_f32_16x16x32_bf16 v[96:99], v[20:23], v[48:51], v[96:99]\n" \
"s_waitcnt lgkmcnt(0)\n" \
"v_mfma_f32_16x16x32_bf16 v[96:99], v[24:27], v[52:55], v[96:99]\n" \
"s_nop 7\n" \
"buffer_store_dwordx4 v[96:99], v224, s[16:19], 0 offen offset:2048 sc1\n" \
"s_cmp_eq_u32 s40, 0\n" \
"s_cbranch_scc1 .LX1_wet\n" \
".LX1_end:\n" \
"s_endpgm\n" \
".p2align 6\n" \
"fk_role_X2:\n" \
"ds_read_b32 v200, v255 offset:0\n" \
"ds_read_b32 v201, v255 offset:256\n" \
"ds_read_b32 v202, v255 offset:512\n" \
"ds_read_b32 v203, v255 offset:768\n" \
"ds_read_b32 v204, v255 offset:1024\n" \
"ds_read_b32 v205, v255 offset:1280\n" \
"ds_read_b32 v206, v255 offset:1536\n" \
"ds_read_b32 v207, v255 offset:1792\n" \
"ds_read_b32 v208, v255 offset:2048\n" \
"ds_read_b32 v209, v255 offset:2304\n" \
"ds_read_b32 v210, v255 offset:2560\n" \
"ds_read_b32 v211, v255 offset:2816\n" \
"ds_read_b32 v212, v255 offset:3072\n" \
"ds_read_b32 v213, v255 offset:3328\n" \
"ds_read_b32 v214, v255 offset:3584\n" \
"ds_read_b32 v215, v255 offset:3840\n" \
"ds_read_b32 v216, v255 offset:4096\n" \
"ds_read_b32 v217, v255 offset:4352\n" \
"ds_read_b32 v218, v255 offset:4608\n" \
"ds_read_b32 v219, v255 offset:4864\n" \
"ds_read_b32 v220, v255 offset:5120\n" \
"ds_read_b32 v221, v255 offset:5376\n" \
"ds_read_b32 v222, v255 offset:5632\n" \
"ds_read_b32 v223, v255 offset:5888\n" \
"ds_read_b32 v224, v255 offset:6144\n" \
"ds_read_b32 v254, v255 offset:7936\n" \
"s_waitcnt lgkmcnt(0)\n" \
"v_readlane_b32 s4, v254, 0\n" \
"v_readlane_b32 s5, v254, 1\n" \
"v_readlane_b32 s6, v254, 2\n" \
"v_readlane_b32 s7, v254, 3\n" \
"v_readlane_b32 s8, v254, 4\n" \
"v_readlane_b32 s9, v254, 5\n" \
"v_readlane_b32 s10, v254, 6\n" \
"v_readlane_b32 s11, v254, 7\n" \
"v_readlane_b32 s12, v254, 8\n" \
"v_readlane_b32 s13, v254, 9\n" \
"v_readlane_b32 s14, v254, 10\n" \
"v_readlane_b32 s15, v254, 11\n" \
"v_readlane_b32 s16, v254, 12\n" \
"v_readlane_b32 s17, v254, 13\n" \
"v_readlane_b32 s18, v254, 14\n" \
"v_readlane_b32 s19, v254, 15\n" \
"v_readlane_b32 s20, v254, 16\n" \
"v_readlane_b32 s21, v254, 17\n" \
"v_readlane_b32 s22, v254, 18\n" \
"v_readlane_b32 s23, v254, 19\n" \
"v_readlane_b32 s24, v254, 20\n" \
"v_readlane_b32 s25, v254, 21\n" \
"v_readlane_b32 s26, v254, 22\n" \
"v_readlane_b32 s27, v254, 23\n" \
"v_readlane_b32 s28, v254, 24\n" \
"v_readlane_b32 s29, v254, 25\n" \
"v_readlane_b32 s30, v254, 26\n" \
"v_readlane_b32 s31, v254, 27\n" \
"v_readlane_b32 s32, v254, 28\n" \
"v_readlane_b32 s33, v254, 29\n" \
"v_readlane_b32 s34, v254, 30\n" \
"v_readlane_b32 s35, v254, 31\n" \
"v_readlane_b32 s36, v254, 32\n" \
"v_readlane_b32 s37, v254, 33\n" \
"v_readlane_b32 s38, v254, 34\n" \
"v_readlane_b32 s39, v254, 35\n" \
"s_mov_b32 s40, 0\n" \
"s_mov_b32 s51, 0\n" \
"s_nop 0\n" \
"v_mov_b32 v252, s38\n" \
"s_nop 4\n" \
"v_mov_b32 v100, -1\n" \
"v_mov_b32 v101, -1\n" \
"v_mov_b32 v102, -1\n" \
"v_mov_b32 v103, -1\n" \
"s_mov_b32 s35, -2\n" \
"s_mov_b32 s2, 0\n" \
"s_mov_b32 s3, -1\n" \
"ds_read_b32 v199, v255 offset:6656\n" \
"s_waitcnt lgkmcnt(0)\n" \
"v_writelane_b32 v251, s6, 0\n" \
"v_writelane_b32 v251, s10, 1\n" \
"v_writelane_b32 v251, s14, 2\n" \
"v_writelane_b32 v251, s18, 3\n" \
"s_mov_b32 s6, 0\n" \
"s_mov_b32 s10, 0\n" \
"s_mov_b32 s14, 0\n" \
"s_mov_b32 s18, 0\n" \
"s_mov_b32 s40, -1\n" \
"s_branch .LX2_pre\n" \
".LX2_wet:\n" \
"s_waitcnt vmcnt(0)\n" \
"v_readlane_b32 s6, v251, 0\n" \
"v_readlane_b32 s10, v251, 1\n" \
"v_readlane_b32 s14, v251, 2\n" \
"v_readlane_b32 s18, v251, 3\n" \
"s_nop 4\n" \
"s_add_u32 s43, s30, 1\n" \
"s_mov_b32 s50, 0\n" \
".LX2_w74l:\n" \
"buffer_load_dword v240, v204, s[4:7], 0 offen sc1\n" \
"s_waitcnt vmcnt(0)\n" \
".LX2_w74c:\n" \
"v_subrev_u32 v241, s43, v240\n" \
"v_cmp_gt_u32 vcc, 0x100, v241\n" \
"s_cmp_eq_u64 vcc, -1\n" \
"s_cbranch_scc1 .LX2_w74d\n" \
"s_sleep 2\n" \
"s_add_u32 s50, s50, 1\n" \
"s_cmp_lt_u32 s50, 0x20000\n" \
"s_cbranch_scc1 .LX2_w74l\n" \
"s_branch .LX2_end\n" \
".LX2_w74d:\n" \
"v_readlane_b32 s43, v202, 0\n" \
"s_nop 4\n" \
"buffer_load_dwordx4 a[0:3], v201, s[8:11], s43 offen sc1\n" \
"buffer_load_dwordx4 a[4:7], v201, s[8:11], s43 offen offset:1024 sc1\n" \
"v_readlane_b32 s43, v202, 7\n" \
"s_nop 4\n" \
"buffer_load_dwordx4 a[56:59], v201, s[8:11], s43 offen sc1\n" \
"buffer_load_dwordx4 a[60:63], v201, s[8:11], s43 offen offset:1024 sc1\n" \
"v_readlane_b32 s43, v202, 14\n" \
"s_nop 4\n" \
"buffer_load_dwordx4 a[112:115], v201, s[8:11], s43 offen sc1\n" \
"buffer_load_dwordx4 a[116:119], v201, s[8:11], s43 offen offset:1024 sc1\n" \
"v_readlane_b32 s43, v202, 21\n" \
"s_nop 4\n" \
"buffer_load_dwordx4 a[168:171], v201, s[8:11], s43 offen sc1\n" \
"buffer_load_dwordx4 a[172:175], v201, s[8:11], s43 offen offset:1024 sc1\n" \
"v_readlane_b32 s43, v202, 28\n" \
"s_nop 4\n" \
"buffer_load_dwordx4 a[224:227], v201, s[8:11], s43 offen sc1\n" \
"buffer_load_dwordx4 a[228:231], v201, s[8:11], s43 offen offset:1024 sc1\n" \
"v_readlane_b32 s43, v202, 35\n" \
"s_nop 4\n" \
"buffer_load_dwordx4 v[128:131], v201, s[8:11], s43 offen sc1\n" \
"buffer_load_dwordx4 v[132:135], v201, s[8:11], s43 offen offset:1024 sc1\n" \
"s_add_u32 s43, s30, 2\n" \
"s_mov_b32 s50, 0\n" \
"s_branch .LX2_w75c\n" \
".LX2_w75l:\n" \
"buffer_load_dword v240, v204, s[4:7], 0 offen sc1\n" \
"s_waitcnt vmcnt(0)\n" \
".LX2_w75c:\n" \
"v_subrev_u32 v241, s43, v240\n" \
"v_cmp_gt_u32 vcc, 0x100, v241\n" \
"s_cmp_eq_u64 vcc, -1\n" \
"s_cbranch_scc1 .LX2_w75d\n" \
"s_sleep 2\n" \
"s_add_u32 s50, s50, 1\n" \
"s_cmp_lt_u32 s50, 0x20000\n" \
"s_cbranch_scc1 .LX2_w75l\n" \
"s_branch .LX2_end\n" \
".LX2_w75d:\n" \
"v_readlane_b32 s43, v202, 4\n" \
"s_nop 4\n" \
"buffer_load_dwordx4 a[32:35], v201, s[8:11], s43 offen sc1\n" \
"buffer_load_dwordx4 a[36:39], v201, s[8:11], s43 offen offset:1024 sc1\n" \
"v_readlane_b32 s43, v202, 11\n" \
"s_nop 4\n" \
"buffer_load_dwordx4 a[88:91], v201, s[8:11], s43 offen sc1\n" \
"buffer_load_dwordx4 a[92:95], v201, s[8:11], s43 offen offset:1024 sc1\n" \
"v_readlane_b32 s43, v202, 18\n" \
"s_nop 4\n" \
"buffer_load_dwordx4 a[144:147], v201, s[8:11], s43 offen sc1\n" \
"buffer_load_dwordx4 a[148:151], v201, s[8:11], s43 offen offset:1024 sc1\n" \
"v_readlane_b32 s43, v202, 25\n" \
"s_nop 4\n" \
"buffer_load_dwordx4 a[200:203], v201, s[8:11], s43 offen sc1\n" \
"buffer_load_dwordx4 a[204:207], v201, s[8:11], s43 offen offset:1024 sc1\n" \
"v_readlane_b32 s43, v202, 32\n" \
"s_nop 4\n" \
"buffer_load_dwordx4 v[104:107], v201, s[8:11], s43 offen sc1\n" \
"buffer_load_dwordx4 v[108:111], v201, s[8:11], s43 offen offset:1024 sc1\n" \
"v_readlane_b32 s43, v202, 39\n" \
"s_nop 4\n" \
"buffer_load_dwordx4 v[160:163], v201, s[8:11], s43 offen sc1\n" \
"buffer_load_dwordx4 v[164:167], v201, s[8:11], s43 offen offset:1024 sc1\n" \
"s_add_u32 s43, s30, 3\n" \
"s_mov_b32 s50, 0\n" \
"s_branch .LX2_w76c\n" \
".LX2_w76l:\n" \
"buffer_load_dword v240, v204, s[4:7], 0 offen sc1\n" \
"s_waitcnt vmcnt(0)\n" \
".LX2_w76c:\n" \
"v_subrev_u32 v241, s43, v240\n" \
"v_cmp_gt_u32 vcc, 0x100, v241\n" \
"s_cmp_eq_u64 vcc, -1\n" \
"s_cbranch_scc1 .LX2_w76d\n" \
"s_sleep 2\n" \
"s_add_u32 s50, s50, 1\n" \
"s_cmp_lt_u32 s50, 0x20000\n" \
"s_cbranch_scc1 .LX2_w76l\n" \
"s_branch .LX2_end\n" \
".LX2_w76d:\n" \
"v_readlane_b32 s43, v202, 1\n" \
"s_nop 4\n" \
"buffer_load_dwordx4 a[8:11], v201, s[8:11], s43 offen sc1\n" \
"buffer_load_dwordx4 a[12:15], v201, s[8:11], s43 offen offset:1024 sc1\n" \
"v_readlane_b32 s43, v202, 8\n" \
"s_nop 4\n" \
"buffer_load_dwordx4 a[64:67], v201, s[8:11], s43 offen sc1\n" \
"buffer_load_dwordx4 a[68:71], v201, s[8:11], s43 offen offset:1024 sc1\n" \
"v_readlane_b32 s43, v202, 15\n" \
"s_nop 4\n" \
"buffer_load_dwordx4 a[120:123], v201, s[8:11], s43 offen sc1\n" \
"buffer_load_dwordx4 a[124:127], v201, s[8:11], s43 offen offset:1024 sc1\n" \
"v_readlane_b32 s43, v202, 22\n" \
"s_nop 4\n" \
"buffer_load_dwordx4 a[176:179], v201, s[8:11], s43 offen sc1\n" \
"buffer_load_dwordx4 a[180:183], v201, s[8:11], s43 offen offset:1024 sc1\n" \
"v_readlane_b32 s43, v202, 29\n" \
"s_nop 4\n" \
"buffer_load_dwordx4 a[232:235], v201, s[8:11], s43 offen sc1\n" \
"buffer_load_dwordx4 a[236:239], v201, s[8:11], s43 offen offset:1024 sc1\n" \
"v_readlane_b32 s43, v202, 36\n" \
"s_nop 4\n" \
"buffer_load_dwordx4 v[136:139], v201, s[8:11], s43 offen sc1\n" \
"buffer_load_dwordx4 v[140:143], v201, s[8:11], s43 offen offset:1024 sc1\n" \
"s_add_u32 s43, s30, 4\n" \
"s_mov_b32 s50, 0\n" \
"s_branch .LX2_w77c\n" \
".LX2_w77l:\n" \
"buffer_load_dword v240, v204, s[4:7], 0 offen sc1\n" \
"s_waitcnt vmcnt(0)\n" \
".LX2_w77c:\n" \
"v_subrev_u32 v241, s43, v240\n" \
"v_cmp_gt_u32 vcc, 0x100, v241\n" \
"s_cmp_eq_u64 vcc, -1\n" \
"s_cbranch_scc1 .LX2_w77d\n" \
"s_sleep 2\n" \
"s_add_u32 s50, s50, 1\n" \
"s_cmp_lt_u32 s50, 0x20000\n" \
"s_cbranch_scc1 .LX2_w77l\n" \
"s_branch .LX2_end\n" \
".LX2_w77d:\n" \
"v_readlane_b32 s43, v202, 5\n" \
"s_nop 4\n" \
"buffer_load_dwordx4 a[40:43], v201, s[8:11], s43 offen sc1\n" \
"buffer_load_dwordx4 a[44:47], v201, s[8:11], s43 offen offset:1024 sc1\n" \
"v_readlane_b32 s43, v202, 12\n" \
"s_nop 4\n" \
"buffer_load_dwordx4 a[96:99], v201, s[8:11], s43 offen sc1\n" \
"buffer_load_dwordx4 a[100:103], v201, s[8:11], s43 offen offset:1024 sc1\n" \
"v_readlane_b32 s43, v202, 19\n" \
"s_nop 4\n" \
"buffer_load_dwordx4 a[152:155], v201, s[8:11], s43 offen sc1\n" \
"buffer_load_dwordx4 a[156:159], v201, s[8:11], s43 offen offset:1024 sc1\n" \
"v_readlane_b32 s43, v202, 26\n" \
"s_nop 4\n" \
"buffer_load_dwordx4 a[208:211], v201, s[8:11], s43 offen sc1\n" \
"buffer_load_dwordx4 a[212:215], v201, s[8:11], s43 offen offset:1024 sc1\n" \
"v_readlane_b32 s43, v202, 33\n" \
"s_nop 4\n" \
"buffer_load_dwordx4 v[112:115], v201, s[8:11], s43 offen sc1\n" \
"buffer_load_dwordx4 v[116:119], v201, s[8:11], s43 offen offset:1024 sc1\n" \
"v_readlane_b32 s43, v202, 40\n" \
"s_nop 4\n" \
"buffer_load_dwordx4 v[168:171], v201, s[8:11], s43 offen sc1\n" \
"buffer_load_dwordx4 v[172:175], v201, s[8:11], s43 offen offset:1024 sc1\n" \
"s_add_u32 s43, s30, 5\n" \
"s_mov_b32 s50, 0\n" \
"s_branch .LX2_w78c\n" \
".LX2_w78l:\n" \
"buffer_load_dword v240, v204, s[4:7], 0 offen sc1\n" \
"s_waitcnt vmcnt(0)\n" \
".LX2_w78c:\n" \
"v_subrev_u32 v241, s43, v240\n" \
"v_cmp_gt_u32 vcc, 0x100, v241\n" \
"s_cmp_eq_u64 vcc, -1\n" \
"s_cbranch_scc1 .LX2_w78d\n" \
"s_sleep 2\n" \
"s_add_u32 s50, s50, 1\n" \
"s_cmp_lt_u32 s50, 0x20000\n" \
"s_cbranch_scc1 .LX2_w78l\n" \
"s_branch .LX2_end\n" \
".LX2_w78d:\n" \
"v_readlane_b32 s43, v202, 2\n" \
"s_nop 4\n" \
"buffer_load_dwordx4 a[16:19], v201, s[8:11], s43 offen sc1\n" \
"buffer_load_dwordx4 a[20:23], v201, s[8:11], s43 offen offset:1024 sc1\n" \
"v_readlane_b32 s43, v202, 9\n" \
"s_nop 4\n" \
"buffer_load_dwordx4 a[72:75], v201, s[8:11], s43 offen sc1\n" \
"buffer_load_dwordx4 a[76:79], v201, s[8:11], s43 offen offset:1024 sc1\n" \
"v_readlane_b32 s43, v202, 16\n" \
"s_nop 4\n" \
"buffer_load_dwordx4 a[128:131], v201, s[8:11], s43 offen sc1\n" \
"buffer_load_dwordx4 a[132:135], v201, s[8:11], s43 offen offset:1024 sc1\n" \
"v_readlane_b32 s43, v202, 23\n" \
"s_nop 4\n" \
"buffer_load_dwordx4 a[184:187], v201, s[8:11], s43 offen sc1\n" \
"buffer_load_dwordx4 a[188:191], v201, s[8:11], s43 offen offset:1024 sc1\n" \
"v_readlane_b32 s43, v202, 30\n" \
"s_nop 4\n" \
"buffer_load_dwordx4 a[240:243], v201, s[8:11], s43 offen sc1\n" \
"buffer_load_dwordx4 a[244:247], v201, s[8:11], s43 offen offset:1024 sc1\n" \
"v_readlane_b32 s43, v202, 37\n" \
"s_nop 4\n" \
"buffer_load_dwordx4 v[144:147], v201, s[8:11], s43 offen sc1\n" \
"buffer_load_dwordx4 v[148:151], v201, s[8:11], s43 offen offset:1024 sc1\n" \
"s_add_u32 s43, s30, 6\n" \
"s_mov_b32 s50, 0\n" \
"s_branch .LX2_w79c\n" \
".LX2_w79l:\n" \
"buffer_load_dword v240, v204, s[4:7], 0 offen sc1\n" \
"s_waitcnt vmcnt(0)\n" \
".LX2_w79c:\n" \
"v_subrev_u32 v241, s43, v240\n" \
"v_cmp_gt_u32 vcc, 0x100, v241\n" \
"s_cmp_eq_u64 vcc, -1\n" \
"s_cbranch_scc1 .LX2_w79d\n" \
"s_sleep 2\n" \
"s_add_u32 s50, s50, 1\n" \
"s_cmp_lt_u32 s50, 0x20000\n" \
"s_cbranch_scc1 .LX2_w79l\n" \
"s_branch .LX2_end\n" \
".LX2_w79d:\n" \
"v_readlane_b32 s43, v202, 6\n" \
"s_nop 4\n" \
"buffer_load_dwordx4 a[48:51], v201, s[8:11], s43 offen sc1\n" \
"buffer_load_dwordx4 a[52:55], v201, s[8:11], s43 offen offset:1024 sc1\n" \
"v_readlane_b32 s43, v202, 13\n" \
"s_nop 4\n" \
"buffer_load_dwordx4 a[104:107], v201, s[8:11], s43 offen sc1\n" \
"buffer_load_dwordx4 a[108:111], v201, s[8:11], s43 offen offset:1024 sc1\n" \
"v_readlane_b32 s43, v202, 20\n" \
"s_nop 4\n" \
"buffer_load_dwordx4 a[160:163], v201, s[8:11], s43 offen sc1\n" \
"buffer_load_dwordx4 a[164:167], v201, s[8:11], s43 offen offset:1024 sc1\n" \
"v_readlane_b32 s43, v202, 27\n" \
"s_nop 4\n" \
"buffer_load_dwordx4 a[216:219], v201, s[8:11], s43 offen sc1\n" \
"buffer_load_dwordx4 a[220:223], v201, s[8:11], s43 offen offset:1024 sc1\n" \
"v_readlane_b32 s43, v202, 34\n" \
"s_nop 4\n" \
"buffer_load_dwordx4 v[120:123], v201, s[8:11], s43 offen sc1\n" \
"buffer_load_dwordx4 v[124:127], v201, s[8:11], s43 offen offset:1024 sc1\n" \
"v_readlane_b32 s43, v202, 41\n" \
"s_nop 4\n" \
"buffer_load_dwordx4 v[176:179], v201, s[8:11], s43 offen sc1\n" \
"buffer_load_dwordx4 v[180:183], v201, s[8:11], s43 offen offset:1024 sc1\n" \
"s_add_u32 s43, s30, 7\n" \
"s_mov_b32 s50, 0\n" \
"s_branch .LX2_w80c\n" \
".LX2_w80l:\n" \
"buffer_load_dword v240, v204, s[4:7], 0 offen sc1\n" \
"s_waitcnt vmcnt(0)\n" \
".LX2_w80c:\n" \
"v_subrev_u32 v241, s43, v240\n" \
"v_cmp_gt_u32 vcc, 0x100, v241\n" \
"s_cmp_eq_u64 vcc, -1\n" \
"s_cbranch_scc1 .LX2_w80d\n" \
"s_sleep 2\n" \
"s_add_u32 s50, s50, 1\n" \
"s_cmp_lt_u32 s50, 0x20000\n" \
"s_cbranch_scc1 .LX2_w80l\n" \
"s_branch .LX2_end\n" \
".LX2_w80d:\n" \
"v_readlane_b32 s43, v202, 3\n" \
"s_nop 4\n" \
"buffer_load_dwordx4 a[24:27], v201, s[8:11], s43 offen sc1\n" \
"buffer_load_dwordx4 a[28:31], v201, s[8:11], s43 offen offset:1024 sc1\n" \
"v_readlane_b32 s43, v202, 10\n" \
"s_nop 4\n" \
"buffer_load_dwordx4 a[80:83], v201, s[8:11], s43 offen sc1\n" \
"buffer_load_dwordx4 a[84:87], v201, s[8:11], s43 offen offset:1024 sc1\n" \
"v_readlane_b32 s43, v202, 17\n" \
"s_nop 4\n" \
"buffer_load_dwordx4 a[136:139], v201, s[8:11], s43 offen sc1\n" \
"buffer_load_dwordx4 a[140:143], v201, s[8:11], s43 offen offset:1024 sc1\n" \
"v_readlane_b32 s43, v202, 24\n" \
"s_nop 4\n" \
"buffer_load_dwordx4 a[192:195], v201, s[8:11], s43 offen sc1\n" \
"buffer_load_dwordx4 a[196:199], v201, s[8:11], s43 offen offset:1024 sc1\n" \
"v_readlane_b32 s43, v202, 31\n" \
"s_nop 4\n" \
"buffer_load_dwordx4 a[248:251], v201, s[8:11], s43 offen sc1\n" \
"buffer_load_dwordx4 a[252:255], v201, s[8:11], s43 offen offset:1024 sc1\n" \
"v_readlane_b32 s43, v202, 38\n" \
"s_nop 4\n" \
"buffer_load_dwordx4 v[152:155], v201, s[8:11], s43 offen sc1\n" \
"buffer_load_dwordx4 v[156:159], v201, s[8:11], s43 offen offset:1024 sc1\n" \
"s_add_u32 s43, s30, 1\n" \
"s_mov_b32 s50, 0\n" \
".LX2_w81l:\n" \
"buffer_load_dword v240, v205, s[4:7], 0 offen sc1\n" \
"s_waitcnt vmcnt(0)\n" \
".LX2_w81c:\n" \
"v_subrev_u32 v241, s43, v240\n" \
"v_cmp_gt_u32 vcc, 0x100, v241\n" \
"s_cmp_eq_u64 vcc, -1\n" \
"s_cbranch_scc1 .LX2_w81d\n" \
"s_sleep 2\n" \
"s_add_u32 s50, s50, 1\n" \
"s_cmp_lt_u32 s50, 0x20000\n" \
"s_cbranch_scc1 .LX2_w81l\n" \
"s_branch .LX2_end\n" \
".LX2_w81d:\n" \
"v_readfirstlane_b32 s31, v241\n" \
"s_lshr_b32 s32, s31, 2\n" \
"s_xor_b32 s29, s31, s32\n" \
"s_and_b32 s29, s29, 3\n" \
"s_lshl_b32 s29, s29, 13\n" \
"s_bitset1_b32 s29, 21\n" \
"s_and_b32 s31, s31, 3\n" \
"s_lshl_b32 s31, s31, 13\n" \
"s_bitcmp1_b32 s39, 0\n" \
"s_cselect_b32 s43, s29, 0\n" \
"s_xor_b32 s31, s31, s43\n" \
"s_xor_b32 s32, s31, 0x4000\n" \
"s_mov_b32 s39, 0\n" \
"s_add_u32 s43, s30, 8\n" \
"s_mov_b32 s50, 0\n" \
".LX2_w82l:\n" \
"buffer_load_dword v240, v215, s[4:7], 0 offen sc1\n" \
"s_waitcnt vmcnt(0)\n" \
".LX2_w82c:\n" \
"v_subrev_u32 v241, s43, v240\n" \
"v_cmp_gt_u32 vcc, 0x100, v241\n" \
"s_cmp_eq_u64 vcc, -1\n" \
"s_cbranch_scc1 .LX2_w82d\n" \
"s_sleep 2\n" \
"s_add_u32 s50, s50, 1\n" \
"s_cmp_lt_u32 s50, 0x20000\n" \
"s_cbranch_scc1 .LX2_w82l\n" \
"s_branch .LX2_end\n" \
".LX2_w82d:\n" \
"s_add_u32 s50, s40, 0\n" \
"s_mul_i32 s50, s50, 28672\n" \
"s_add_u32 s50, s50, s34\n" \
"buffer_load_dwordx4 v[28:31], v201, s[12:15], s50 offen sc1\n" \
"buffer_load_dwordx4 v[32:35], v201, s[12:15], s50 offen offset:1024 sc1\n" \
"buffer_load_dwordx4 v[36:39], v201, s[12:15], s50 offen offset:2048 sc1\n" \
"buffer_load_dwordx4 v[40:43], v201, s[12:15], s50 offen offset:3072 sc1\n" \
"s_waitcnt vmcnt(0)\n" \
"s_barrier\n" \
"s_add_u32 s43, s30, 9\n" \
"s_mov_b32 s50, 0\n" \
".LX2_w83l:\n" \
"buffer_load_dword v240, v215, s[4:7], 0 offen sc1\n" \
"s_waitcnt vmcnt(0)\n" \
".LX2_w83c:\n" \
"v_subrev_u32 v241, s43, v240\n" \
"v_cmp_gt_u32 vcc, 0x100, v241\n" \
"s_cmp_eq_u64 vcc, -1\n" \
"s_cbranch_scc1 .LX2_w83d\n" \
"s_sleep 2\n" \
"s_add_u32 s50, s50, 1\n" \
"s_cmp_lt_u32 s50, 0x20000\n" \
"s_cbranch_scc1 .LX2_w83l\n" \
"s_branch .LX2_end\n" \
".LX2_w83d:\n" \
".LX2_pre:\n" \
".LX2_loop:\n" \
"s_add_u32 s43, s40, 2\n" \
"s_and_b32 s43, s43, 3\n" \
"s_bitcmp1_b32 s43, 0\n" \
"s_cselect_b32 s49, s32, s31\n" \
"s_lshl_b32 s43, s43, 19\n" \
"s_add_u32 s49, s49, s43\n" \
"ds_read_b128 v[0:3], v216 offset:0\n" \
"ds_read_b128 v[4:7], v216 offset:1024\n" \
"ds_read_b128 v[8:11], v216 offset:2048\n" \
"ds_read_b128 v[12:15], v216 offset:3072\n" \
"ds_read_b128 v[16:19], v216 offset:4096\n" \
"ds_read_b128 v[20:23], v216 offset:5120\n" \
"ds_read_b128 v[24:27], v216 offset:6144\n" \
"v_mov_b32 v60, 0\n" \
"v_mov_b32 v61, 0\n" \
"v_mov_b32 v62, 0\n" \
"v_mov_b32 v63, 0\n" \
"v_mov_b32 v64, 0\n" \
"v_mov_b32 v65, 0\n" \
"v_mov_b32 v66, 0\n" \
"v_mov_b32 v67, 0\n" \
"v_mov_b32 v68, 0\n" \
"v_mov_b32 v69, 0\n" \
"v_mov_b32 v70, 0\n" \
"v_mov_b32 v71, 0\n" \
"v_mov_b32 v72, 0\n" \
"v_mov_b32 v73, 0\n" \
"v_mov_b32 v74, 0\n" \
"v_mov_b32 v75, 0\n" \
"v_mov_b32 v76, 0\n" \
"v_mov_b32 v77, 0\n" \
"v_mov_b32 v78, 0\n" \
"v_mov_b32 v79, 0\n" \
"v_mov_b32 v80, 0\n" \
"v_mov_b32 v81, 0\n" \
"v_mov_b32 v82, 0\n" \
"v_mov_b32 v83, 0\n" \
"s_nop 1\n" \
"s_waitcnt lgkmcnt(6)\n" \
"v_smfmac_f32_16x16x64_bf16 v[60:63], v[0:3], a[0:7], v200\n" \
"v_smfmac_f32_16x16x64_bf16 v[64:67], v[0:3], a[56:63], v200\n" \
"v_smfmac_f32_16x16x64_bf16 v[68:71], v[0:3], a[112:119], v200\n" \
"v_smfmac_f32_16x16x64_bf16 v[72:75], v[0:3], a[168:175], v200\n" \
"v_smfmac_f32_16x16x64_bf16 v[76:79], v[0:3], a[224:231], v200\n" \
"v_smfmac_f32_16x16x64_bf16 v[80:83], v[0:3], v[128:135], v200\n" \
"s_waitcnt lgkmcnt(5)\n" \
"v_smfmac_f32_16x16x64_bf16 v[60:63], v[4:7], a[8:15], v200\n" \
"v_smfmac_f32_16x16x64_bf16 v[64:67], v[4:7], a[64:71], v200\n" \
"v_smfmac_f32_16x16x64_bf16 v[68:71], v[4:7], a[120:127], v200\n" \
"v_smfmac_f32_16x16x64_bf16 v[72:75], v[4:7], a[176:183], v200\n" \
"v_smfmac_f32_16x16x64_bf16 v[76:79], v[4:7], a[232:239], v200\n" \
"v_smfmac_f32_16x16x64_bf16 v[80:83], v[4:7], v[136:143], v200\n" \
"s_waitcnt lgkmcnt(4)\n" \
"v_smfmac_f32_16x16x64_bf16 v[60:63], v[8:11], a[16:23], v200\n" \
"v_smfmac_f32_16x16x64_bf16 v[64:67], v[8:11], a[72:79], v200\n" \
"v_smfmac_f32_16x16x64_bf16 v[68:71], v[8:11], a[128:135], v200\n" \
"v_smfmac_f32_16x16x64_bf16 v[72:75], v[8:11], a[184:191], v200\n" \
"v_smfmac_f32_16x16x64_bf16 v[76:79], v[8:11], a[240:247], v200\n" \
"v_smfmac_f32_16x16x64_bf16 v[80:83], v[8:11], v[144:151], v200\n" \
"s_waitcnt lgkmcnt(3)\n" \
"v_smfmac_f32_16x16x64_bf16 v[60:63], v[12:15], a[24:31], v200\n" \
"v_smfmac_f32_16x16x64_bf16 v[64:67], v[12:15], a[80:87], v200\n" \
"v_smfmac_f32_16x16x64_bf16 v[68:71], v[12:15], a[136:143], v200\n" \
"v_smfmac_f32_16x16x64_bf16 v[72:75], v[12:15], a[192:199], v200\n" \
"v_smfmac_f32_16x16x64_bf16 v[76:79], v[12:15], a[248:255], v200\n" \
"v_smfmac_f32_16x16x64_bf16 v[80:83], v[12:15], v[152:159], v200\n" \
"s_waitcnt lgkmcnt(2)\n" \
"v_smfmac_f32_16x16x64_bf16 v[60:63], v[16:19], a[32:39], v200\n" \
"v_smfmac_f32_16x16x64_bf16 v[64:67], v[16:19], a[88:95], v200\n" \
"v_smfmac_f32_16x16x64_bf16 v[68:71], v[16:19], a[144:151], v200\n" \
"v_smfmac_f32_16x16x64_bf16 v[72:75], v[16:19], a[200:207], v200\n" \
"s_barrier\n" \
"s_add_u32 s50, s40, 1\n" \
"s_mul_i32 s50, s50, 28672\n" \
"s_add_u32 s50, s50, s34\n" \
"buffer_load_dwordx4 v[44:47], v201, s[12:15], s50 offen sc1\n" \
"buffer_load_dwordx4 v[48:51], v201, s[12:15], s50 offen offset:1024 sc1\n" \
"buffer_load_dwordx4 v[52:55], v201, s[12:15], s50 offen offset:2048 sc1\n" \
"buffer_load_dwordx4 v[56:59], v201, s[12:15], s50 offen offset:3072 sc1\n" \
"buffer_load_dword v240, v215, s[4:7], 0 offen sc1\n" \
"v_smfmac_f32_16x16x64_bf16 v[76:79], v[16:19], v[104:111], v200\n" \
"v_smfmac_f32_16x16x64_bf16 v[80:83], v[16:19], v[160:167], v200\n" \
"s_waitcnt lgkmcnt(1)\n" \
"v_smfmac_f32_16x16x64_bf16 v[60:63], v[20:23], a[40:47], v200\n" \
"v_smfmac_f32_16x16x64_bf16 v[64:67], v[20:23], a[96:103], v200\n" \
"v_smfmac_f32_16x16x64_bf16 v[68:71], v[20:23], a[152:159], v200\n" \
"v_smfmac_f32_16x16x64_bf16 v[72:75], v[20:23], a[208:215], v200\n" \
"v_smfmac_f32_16x16x64_bf16 v[76:79], v[20:23], v[112:119], v200\n" \
"v_smfmac_f32_16x16x64_bf16 v[80:83], v[20:23], v[168:175], v200\n" \
"s_waitcnt lgkmcnt(0)\n" \
"v_smfmac_f32_16x16x64_bf16 v[60:63], v[24:27], a[48:55], v200\n" \
"v_smfmac_f32_16x16x64_bf16 v[64:67], v[24:27], a[104:111], v200\n" \
"v_smfmac_f32_16x16x64_bf16 v[68:71], v[24:27], a[160:167], v200\n" \
"v_smfmac_f32_16x16x64_bf16 v[72:75], v[24:27], a[216:223], v200\n" \
"v_smfmac_f32_16x16x64_bf16 v[76:79], v[24:27], v[120:127], v200\n" \
"v_smfmac_f32_16x16x64_bf16 v[80:83], v[24:27], v[176:183], v200\n" \
"s_nop 7\n" \
"v_permlane32_swap_b32 v60, v64\n" \
"v_permlane32_swap_b32 v61, v65\n" \
"v_permlane32_swap_b32 v62, v66\n" \
"v_permlane32_swap_b32 v63, v67\n" \
"v_add_f32 v60, v60, v64\n" \
"v_add_f32 v61, v61, v65\n" \
"v_add_f32 v62, v62, v66\n" \
"v_add_f32 v63, v63, v67\n" \
"v_cvt_pk_bf16_f32 v184, v60, v61\n" \
"v_cvt_pk_bf16_f32 v185, v62, v63\n" \
"ds_write_b64 v217, v[184:185] offset:0\n" \
"v_permlane32_swap_b32 v68, v72\n" \
"v_permlane32_swap_b32 v69, v73\n" \
"v_permlane32_swap_b32 v70, v74\n" \
"v_permlane32_swap_b32 v71, v75\n" \
"v_add_f32 v68, v68, v72\n" \
"v_add_f32 v69, v69, v73\n" \
"v_add_f32 v70, v70, v74\n" \
"v_add_f32 v71, v71, v75\n" \
"v_cvt_pk_bf16_f32 v186, v68, v69\n" \
"v_cvt_pk_bf16_f32 v187, v70, v71\n" \
"ds_write_b64 v217, v[186:187] offset:512\n" \
"v_permlane32_swap_b32 v76, v80\n" \
"v_permlane32_swap_b32 v77, v81\n" \
"v_permlane32_swap_b32 v78, v82\n" \
"v_permlane32_swap_b32 v79, v83\n" \
"v_add_f32 v76, v76, v80\n" \
"v_add_f32 v77, v77, v81\n" \
"v_add_f32 v78, v78, v82\n" \
"v_add_f32 v79, v79, v83\n" \
"v_cvt_pk_bf16_f32 v188, v76, v77\n" \
"v_cvt_pk_bf16_f32 v189, v78, v79\n" \
"ds_write_b64 v217, v[188:189] offset:1024\n" \
"ds_read_b64_tr_b16 v[0:1], v218 offset:0\n" \
"ds_read_b64_tr_b16 v[2:3], v218 offset:64\n" \
"ds_read_b64_tr_b16 v[4:5], v218 offset:1024\n" \
"ds_read_b64_tr_b16 v[6:7], v218 offset:1088\n" \
"s_add_u32 s43, s40, 0\n" \
"s_mul_i32 s43, s43, 7168\n" \
"s_waitcnt lgkmcnt(0)\n" \
"buffer_store_dwordx4 v[0:3], v219, s[16:19], s43 offen sc1\n" \
"buffer_store_dwordx4 v[4:7], v221, s[16:19], s43 offen offset:1024 sc1\n" \
"s_waitcnt vmcnt(7)\n" \
"s_barrier\n" \
"ds_read_b64_tr_b16 v[92:93], v213\n" \
"ds_read_b64_tr_b16 v[94:95], v213 offset:64\n" \
"ds_read_b32 v84, v252 offset:16\n" \
"v_mov_b32 v245, v245\n" \
"s_nop 2\n" \
"v_mov_b32 v245, v245\n" \
"s_nop 2\n" \
"v_mov_b32 v245, v245\n" \
"s_waitcnt lgkmcnt(1)\n" \
"v_mfma_f32_16x16x32_bf16 v[68:71], v[92:95], v[28:31], 0\n" \
"s_waitcnt lgkmcnt(0)\n" \
"v_xor_b32 v85, s29, v84\n" \
"v_mfma_f32_16x16x32_bf16 v[76:79], v[92:95], v[36:39], 0\n" \
"v_add_u32 v84, v84, v210\n" \
"v_add_u32 v85, v85, v210\n" \
"v_mfma_f32_16x16x32_bf16 v[72:75], v[92:95], v[32:35], 0\n" \
"s_nop 1\n" \
"v_mfma_f32_16x16x32_bf16 v[80:83], v[92:95], v[40:43], 0\n" \
"v_cvt_pk_bf16_f32 v96, v68, v69\n" \
"v_cvt_pk_bf16_f32 v97, v70, v71\n" \
"v_cvt_pk_bf16_f32 v98, v76, v77\n" \
"v_cvt_pk_bf16_f32 v99, v78, v79\n" \
"s_mov_b64 exec, s[2:3]\n" \
"v_cvt_pk_bf16_f32 v96, v72, v73\n" \
"v_cvt_pk_bf16_f32 v97, v74, v75\n" \
"s_nop 0\n" \
"v_cvt_pk_bf16_f32 v98, v80, v81\n" \
"v_cvt_pk_bf16_f32 v99, v82, v83\n" \
"s_mov_b64 exec, -1\n" \
"s_nop 1\n" \
"v_permlane16_swap_b32 v96, v98\n" \
"v_permlane16_swap_b32 v97, v99\n" \
"buffer_store_dwordx4 v[96:99], v84, s[4:7], 0 offen sc1\n" \
"s_barrier\n" \
"buffer_store_dwordx4 v[96:99], v85, s[4:7], 0 offen sc1\n" \
"buffer_store_dwordx4 v[100:103], v214, s[4:7], s49 offen sc1\n" \
"s_xor_b32 s43, s49, s29\n" \
"buffer_store_dwordx4 v[100:103], v214, s[4:7], s43 offen sc1\n" \
"s_waitcnt vmcnt(4)\n" \
"v_mov_b32 v28, v44\n" \
"v_mov_b32 v29, v45\n" \
"v_mov_b32 v30, v46\n" \
"v_mov_b32 v31, v47\n" \
"v_mov_b32 v32, v48\n" \
"v_mov_b32 v33, v49\n" \
"v_mov_b32 v34, v50\n" \
"v_mov_b32 v35, v51\n" \
"v_mov_b32 v36, v52\n" \
"v_mov_b32 v37, v53\n" \
"v_mov_b32 v38, v54\n" \
"v_mov_b32 v39, v55\n" \
"v_mov_b32 v40, v56\n" \
"v_mov_b32 v41, v57\n" \
"v_mov_b32 v42, v58\n" \
"v_mov_b32 v43, v59\n" \
"s_cmp_eq_u32 s40, -1\n" \
"s_cbranch_scc1 .LX2_nogu\n" \
"s_add_u32 s36, s40, 10\n" \
"s_min_u32 s36, s36, 71\n" \
"s_add_u32 s43, s30, s36\n" \
"s_mov_b32 s50, 0\n" \
"s_branch .LX2_w84c\n" \
".LX2_w84l:\n" \
"buffer_load_dword v240, v215, s[4:7], 0 offen sc1\n" \
"s_waitcnt vmcnt(0)\n" \
".LX2_w84c:\n" \
"v_subrev_u32 v241, s43, v240\n" \
"v_cmp_gt_u32 vcc, 0x100, v241\n" \
"s_cmp_eq_u64 vcc, -1\n" \
"s_cbranch_scc1 .LX2_w84d\n" \
"s_sleep 2\n" \
"s_add_u32 s50, s50, 1\n" \
"s_cmp_lt_u32 s50, 0x20000\n" \
"s_cbranch_scc1 .LX2_w84l\n" \
"s_branch .LX2_end\n" \
".LX2_w84d:\n" \
".LX2_nogu:\n" \
"s_add_u32 s40, s40, 1\n" \
"s_cmp_eq_u32 s40, 0\n" \
"s_cbranch_scc1 .LX2_fin\n" \
"s_cmp_lt_u32 s40, 63\n" \
"s_cbranch_scc1 .LX2_loop\n" \
".LX2_fin:\n" \
"s_barrier\n" \
"ds_read_b128 v[0:3], v216 offset:0\n" \
"ds_read_b128 v[4:7], v216 offset:1024\n" \
"ds_read_b128 v[8:11], v216 offset:2048\n" \
"ds_read_b128 v[12:15], v216 offset:3072\n" \
"ds_read_b128 v[16:19], v216 offset:4096\n" \
"ds_read_b128 v[20:23], v216 offset:5120\n" \
"ds_read_b128 v[24:27], v216 offset:6144\n" \
"v_mov_b32 v60, 0\n" \
"v_mov_b32 v61, 0\n" \
"v_mov_b32 v62, 0\n" \
"v_mov_b32 v63, 0\n" \
"v_mov_b32 v64, 0\n" \
"v_mov_b32 v65, 0\n" \
"v_mov_b32 v66, 0\n" \
"v_mov_b32 v67, 0\n" \
"v_mov_b32 v68, 0\n" \
"v_mov_b32 v69, 0\n" \
"v_mov_b32 v70, 0\n" \
"v_mov_b32 v71, 0\n" \
"v_mov_b32 v72, 0\n" \
"v_mov_b32 v73, 0\n" \
"v_mov_b32 v74, 0\n" \
"v_mov_b32 v75, 0\n" \
"v_mov_b32 v76, 0\n" \
"v_mov_b32 v77, 0\n" \
"v_mov_b32 v78, 0\n" \
"v_mov_b32 v79, 0\n" \
"v_mov_b32 v80, 0\n" \
"v_mov_b32 v81, 0\n" \
"v_mov_b32 v82, 0\n" \
"v_mov_b32 v83, 0\n" \
"s_nop 1\n" \
"s_waitcnt lgkmcnt(6)\n" \
"v_smfmac_f32_16x16x64_bf16 v[60:63], v[0:3], a[0:7], v200\n" \
"v_smfmac_f32_16x16x64_bf16 v[64:67], v[0:3], a[56:63], v200\n" \
"v_smfmac_f32_16x16x64_bf16 v[68:71], v[0:3], a[112:119], v200\n" \
"v_smfmac_f32_16x16x64_bf16 v[72:75], v[0:3], a[168:175], v200\n" \
"v_smfmac_f32_16x16x64_bf16 v[76:79], v[0:3], a[224:231], v200\n" \
"v_smfmac_f32_16x16x64_bf16 v[80:83], v[0:3], v[128:135], v200\n" \
"s_waitcnt lgkmcnt(5)\n" \
"v_smfmac_f32_16x16x64_bf16 v[60:63], v[4:7], a[8:15], v200\n" \
"v_smfmac_f32_16x16x64_bf16 v[64:67], v[4:7], a[64:71], v200\n" \
"v_smfmac_f32_16x16x64_bf16 v[68:71], v[4:7], a[120:127], v200\n" \
"v_smfmac_f32_16x16x64_bf16 v[72:75], v[4:7], a[176:183], v200\n" \
"v_smfmac_f32_16x16x64_bf16 v[76:79], v[4:7], a[232:239], v200\n" \
"v_smfmac_f32_16x16x64_bf16 v[80:83], v[4:7], v[136:143], v200\n" \
"s_waitcnt lgkmcnt(4)\n" \
"v_smfmac_f32_16x16x64_bf16 v[60:63], v[8:11], a[16:23], v200\n" \
"v_smfmac_f32_16x16x64_bf16 v[64:67], v[8:11], a[72:79], v200\n" \
"v_smfmac_f32_16x16x64_bf16 v[68:71], v[8:11], a[128:135], v200\n" \
"v_smfmac_f32_16x16x64_bf16 v[72:75], v[8:11], a[184:191], v200\n" \
"v_smfmac_f32_16x16x64_bf16 v[76:79], v[8:11], a[240:247], v200\n" \
"v_smfmac_f32_16x16x64_bf16 v[80:83], v[8:11], v[144:151], v200\n" \
"s_waitcnt lgkmcnt(3)\n" \
"v_smfmac_f32_16x16x64_bf16 v[60:63], v[12:15], a[24:31], v200\n" \
"v_smfmac_f32_16x16x64_bf16 v[64:67], v[12:15], a[80:87], v200\n" \
"v_smfmac_f32_16x16x64_bf16 v[68:71], v[12:15], a[136:143], v200\n" \
"v_smfmac_f32_16x16x64_bf16 v[72:75], v[12:15], a[192:199], v200\n" \
"v_smfmac_f32_16x16x64_bf16 v[76:79], v[12:15], a[248:255], v200\n" \
"v_smfmac_f32_16x16x64_bf16 v[80:83], v[12:15], v[152:159], v200\n" \
"s_waitcnt lgkmcnt(2)\n" \
"v_smfmac_f32_16x16x64_bf16 v[60:63], v[16:19], a[32:39], v200\n" \
"v_smfmac_f32_16x16x64_bf16 v[64:67], v[16:19], a[88:95], v200\n" \
"v_smfmac_f32_16x16x64_bf16 v[68:71], v[16:19], a[144:151], v200\n" \
"v_smfmac_f32_16x16x64_bf16 v[72:75], v[16:19], a[200:207], v200\n" \
"v_smfmac_f32_16x16x64_bf16 v[76:79], v[16:19], v[104:111], v200\n" \
"v_smfmac_f32_16x16x64_bf16 v[80:83], v[16:19], v[160:167], v200\n" \
"s_waitcnt lgkmcnt(1)\n" \
"v_smfmac_f32_16x16x64_bf16 v[60:63], v[20:23], a[40:47], v200\n" \
"v_smfmac_f32_16x16x64_bf16 v[64:67], v[20:23], a[96:103], v200\n" \
"v_smfmac_f32_16x16x64_bf16 v[68:71], v[20:23], a[152:159], v200\n" \
"v_smfmac_f32_16x16x64_bf16 v[72:75], v[20:23], a[208:215], v200\n" \
"v_smfmac_f32_16x16x64_bf16 v[76:79], v[20:23], v[112:119], v200\n" \
"v_smfmac_f32_16x16x64_bf16 v[80:83], v[20:23], v[168:175], v200\n" \
"s_waitcnt lgkmcnt(0)\n" \
"v_smfmac_f32_16x16x64_bf16 v[60:63], v[24:27], a[48:55], v200\n" \
"v_smfmac_f32_16x16x64_bf16 v[64:67], v[24:27], a[104:111], v200\n" \
"v_smfmac_f32_16x16x64_bf16 v[68:71], v[24:27], a[160:167], v200\n" \
"v_smfmac_f32_16x16x64_bf16 v[72:75], v[24:27], a[216:223], v200\n" \
"v_smfmac_f32_16x16x64_bf16 v[76:79], v[24:27], v[120:127], v200\n" \
"v_smfmac_f32_16x16x64_bf16 v[80:83], v[24:27], v[176:183], v200\n" \
"s_nop 7\n" \
"v_permlane32_swap_b32 v60, v64\n" \
"v_permlane32_swap_b32 v61, v65\n" \
"v_permlane32_swap_b32 v62, v66\n" \
"v_permlane32_swap_b32 v63, v67\n" \
"v_add_f32 v60, v60, v64\n" \
"v_add_f32 v61, v61, v65\n" \
"v_add_f32 v62, v62, v66\n" \
"v_add_f32 v63, v63, v67\n" \
"v_cvt_pk_bf16_f32 v184, v60, v61\n" \
"v_cvt_pk_bf16_f32 v185, v62, v63\n" \
"ds_write_b64 v217, v[184:185] offset:0\n" \
"v_permlane32_swap_b32 v68, v72\n" \
"v_permlane32_swap_b32 v69, v73\n" \
"v_permlane32_swap_b32 v70, v74\n" \
"v_permlane32_swap_b32 v71, v75\n" \
"v_add_f32 v68, v68, v72\n" \
"v_add_f32 v69, v69, v73\n" \
"v_add_f32 v70, v70, v74\n" \
"v_add_f32 v71, v71, v75\n" \
"v_cvt_pk_bf16_f32 v186, v68, v69\n" \
"v_cvt_pk_bf16_f32 v187, v70, v71\n" \
"ds_write_b64 v217, v[186:187] offset:512\n" \
"v_permlane32_swap_b32 v76, v80\n" \
"v_permlane32_swap_b32 v77, v81\n" \
"v_permlane32_swap_b32 v78, v82\n" \
"v_permlane32_swap_b32 v79, v83\n" \
"v_add_f32 v76, v76, v80\n" \
"v_add_f32 v77, v77, v81\n" \
"v_add_f32 v78, v78, v82\n" \
"v_add_f32 v79, v79, v83\n" \
"v_cvt_pk_bf16_f32 v188, v76, v77\n" \
"v_cvt_pk_bf16_f32 v189, v78, v79\n" \
"ds_write_b64 v217, v[188:189] offset:1024\n" \
"s_waitcnt lgkmcnt(0)\n" \
"s_barrier\n" \
"s_waitcnt lgkmcnt(0)\n" \
"s_barrier\n" \
"ds_read_b128 v[0:3], v216 offset:7168\n" \
"ds_read_b128 v[4:7], v216 offset:8192\n" \
"ds_read_b128 v[8:11], v216 offset:9216\n" \
"ds_read_b128 v[12:15], v216 offset:10240\n" \
"ds_read_b128 v[16:19], v216 offset:11264\n" \
"ds_read_b128 v[20:23], v216 offset:12288\n" \
"ds_read_b128 v[24:27], v216 offset:13312\n" \
"v_mov_b32 v60, 0\n" \
"v_mov_b32 v61, 0\n" \
"v_mov_b32 v62, 0\n" \
"v_mov_b32 v63, 0\n" \
"v_mov_b32 v64, 0\n" \
"v_mov_b32 v65, 0\n" \
"v_mov_b32 v66, 0\n" \
"v_mov_b32 v67, 0\n" \
"v_mov_b32 v68, 0\n" \
"v_mov_b32 v69, 0\n" \
"v_mov_b32 v70, 0\n" \
"v_mov_b32 v71, 0\n" \
"v_mov_b32 v72, 0\n" \
"v_mov_b32 v73, 0\n" \
"v_mov_b32 v74, 0\n" \
"v_mov_b32 v75, 0\n" \
"v_mov_b32 v76, 0\n" \
"v_mov_b32 v77, 0\n" \
"v_mov_b32 v78, 0\n" \
"v_mov_b32 v79, 0\n" \
"v_mov_b32 v80, 0\n" \
"v_mov_b32 v81, 0\n" \
"v_mov_b32 v82, 0\n" \
"v_mov_b32 v83, 0\n" \
"s_nop 1\n" \
"s_waitcnt lgkmcnt(6)\n" \
"v_smfmac_f32_16x16x64_bf16 v[60:63], v[0:3], a[0:7], v200\n" \
"v_smfmac_f32_16x16x64_bf16 v[64:67], v[0:3], a[56:63], v200\n" \
"v_smfmac_f32_16x16x64_bf16 v[68:71], v[0:3], a[112:119], v200\n" \
"v_smfmac_f32_16x16x64_bf16 v[72:75], v[0:3], a[168:175], v200\n" \
"v_smfmac_f32_16x16x64_bf16 v[76:79], v[0:3], a[224:231], v200\n" \
"v_smfmac_f32_16x16x64_bf16 v[80:83], v[0:3], v[128:135], v200\n" \
"s_waitcnt lgkmcnt(5)\n" \
"v_smfmac_f32_16x16x64_bf16 v[60:63], v[4:7], a[8:15], v200\n" \
"v_smfmac_f32_16x16x64_bf16 v[64:67], v[4:7], a[64:71], v200\n" \
"v_smfmac_f32_16x16x64_bf16 v[68:71], v[4:7], a[120:127], v200\n" \
"v_smfmac_f32_16x16x64_bf16 v[72:75], v[4:7], a[176:183], v200\n" \
"v_smfmac_f32_16x16x64_bf16 v[76:79], v[4:7], a[232:239], v200\n" \
"v_smfmac_f32_16x16x64_bf16 v[80:83], v[4:7], v[136:143], v200\n" \
"s_waitcnt lgkmcnt(4)\n" \
"v_smfmac_f32_16x16x64_bf16 v[60:63], v[8:11], a[16:23], v200\n" \
"v_smfmac_f32_16x16x64_bf16 v[64:67], v[8:11], a[72:79], v200\n" \
"v_smfmac_f32_16x16x64_bf16 v[68:71], v[8:11], a[128:135], v200\n" \
"v_smfmac_f32_16x16x64_bf16 v[72:75], v[8:11], a[184:191], v200\n" \
"v_smfmac_f32_16x16x64_bf16 v[76:79], v[8:11], a[240:247], v200\n" \
"v_smfmac_f32_16x16x64_bf16 v[80:83], v[8:11], v[144:151], v200\n" \
"s_waitcnt lgkmcnt(3)\n" \
"v_smfmac_f32_16x16x64_bf16 v[60:63], v[12:15], a[24:31], v200\n" \
"v_smfmac_f32_16x16x64_bf16 v[64:67], v[12:15], a[80:87], v200\n" \
"v_smfmac_f32_16x16x64_bf16 v[68:71], v[12:15], a[136:143], v200\n" \
"v_smfmac_f32_16x16x64_bf16 v[72:75], v[12:15], a[192:199], v200\n" \
"v_smfmac_f32_16x16x64_bf16 v[76:79], v[12:15], a[248:255], v200\n" \
"v_smfmac_f32_16x16x64_bf16 v[80:83], v[12:15], v[152:159], v200\n" \
"s_waitcnt lgkmcnt(2)\n" \
"v_smfmac_f32_16x16x64_bf16 v[60:63], v[16:19], a[32:39], v200\n" \
"v_smfmac_f32_16x16x64_bf16 v[64:67], v[16:19], a[88:95], v200\n" \
"v_smfmac_f32_16x16x64_bf16 v[68:71], v[16:19], a[144:151], v200\n" \
"v_smfmac_f32_16x16x64_bf16 v[72:75], v[16:19], a[200:207], v200\n" \
"v_smfmac_f32_16x16x64_bf16 v[76:79], v[16:19], v[104:111], v200\n" \
"v_smfmac_f32_16x16x64_bf16 v[80:83], v[16:19], v[160:167], v200\n" \
"s_waitcnt lgkmcnt(1)\n" \
"v_smfmac_f32_16x16x64_bf16 v[60:63], v[20:23], a[40:47], v200\n" \
"v_smfmac_f32_16x16x64_bf16 v[64:67], v[20:23], a[96:103], v200\n" \
"v_smfmac_f32_16x16x64_bf16 v[68:71], v[20:23], a[152:159], v200\n" \
"v_smfmac_f32_16x16x64_bf16 v[72:75], v[20:23], a[208:215], v200\n" \
"v_smfmac_f32_16x16x64_bf16 v[76:79], v[20:23], v[112:119], v200\n" \
"v_smfmac_f32_16x16x64_bf16 v[80:83], v[20:23], v[168:175], v200\n" \
"s_waitcnt lgkmcnt(0)\n" \
"v_smfmac_f32_16x16x64_bf16 v[60:63], v[24:27], a[48:55], v200\n" \
"v_smfmac_f32_16x16x64_bf16 v[64:67], v[24:27], a[104:111], v200\n" \
"v_smfmac_f32_16x16x64_bf16 v[68:71], v[24:27], a[160:167], v200\n" \
"v_smfmac_f32_16x16x64_bf16 v[72:75], v[24:27], a[216:223], v200\n" \
"v_smfmac_f32_16x16x64_bf16 v[76:79], v[24:27], v[120:127], v200\n" \
"v_smfmac_f32_16x16x64_bf16 v[80:83], v[24:27], v[176:183], v200\n" \
"s_nop 7\n" \
"v_permlane32_swap_b32 v60, v64\n" \
"v_permlane32_swap_b32 v61, v65\n" \
"v_permlane32_swap_b32 v62, v66\n" \
"v_permlane32_swap_b32 v63, v67\n" \
"v_add_f32 v60, v60, v64\n" \
"v_add_f32 v61, v61, v65\n" \
"v_add_f32 v62, v62, v66\n" \
"v_add_f32 v63, v63, v67\n" \
"v_cvt_pk_bf16_f32 v184, v60, v61\n" \
"v_cvt_pk_bf16_f32 v185, v62, v63\n" \
"ds_write_b64 v217, v[184:185] offset:0\n" \
"v_permlane32_swap_b32 v68, v72\n" \
"v_permlane32_swap_b32 v69, v73\n" \
"v_permlane32_swap_b32 v70, v74\n" \
"v_permlane32_swap_b32 v71, v75\n" \
"v_add_f32 v68, v68, v72\n" \
"v_add_f32 v69, v69, v73\n" \
"v_add_f32 v70, v70, v74\n" \
"v_add_f32 v71, v71, v75\n" \
"v_cvt_pk_bf16_f32 v186, v68, v69\n" \
"v_cvt_pk_bf16_f32 v187, v70, v71\n" \
"ds_write_b64 v217, v[186:187] offset:512\n" \
"v_permlane32_swap_b32 v76, v80\n" \
"v_permlane32_swap_b32 v77, v81\n" \
"v_permlane32_swap_b32 v78, v82\n" \
"v_permlane32_swap_b32 v79, v83\n" \
"v_add_f32 v76, v76, v80\n" \
"v_add_f32 v77, v77, v81\n" \
"v_add_f32 v78, v78, v82\n" \
"v_add_f32 v79, v79, v83\n" \
"v_cvt_pk_bf16_f32 v188, v76, v77\n" \
"v_cvt_pk_bf16_f32 v189, v78, v79\n" \
"ds_write_b64 v217, v[188:189] offset:1024\n" \
"s_waitcnt lgkmcnt(0)\n" \
"s_barrier\n" \
"s_cmp_eq_u32 s40, 0\n" \
"s_cbranch_scc1 .LX2_wet\n" \
".LX2_end:\n" \
"s_endpgm\n" \
".p2align 6\n" \
"fk_role_X3:\n" \
"ds_read_b32 v200, v255 offset:0\n" \
"ds_read_b32 v201, v255 offset:256\n" \
"ds_read_b32 v202, v255 offset:512\n" \
"ds_read_b32 v203, v255 offset:768\n" \
"ds_read_b32 v204, v255 offset:1024\n" \
"ds_read_b32 v205, v255 offset:1280\n" \
"ds_read_b32 v206, v255 offset:1536\n" \
"ds_read_b32 v207, v255 offset:1792\n" \
"ds_read_b32 v208, v255 offset:2048\n" \
"ds_read_b32 v209, v255 offset:2304\n" \
"ds_read_b32 v210, v255 offset:2560\n" \
"ds_read_b32 v211, v255 offset:2816\n" \
"ds_read_b32 v212, v255 offset:3072\n" \
"ds_read_b32 v213, v255 offset:3328\n" \
"ds_read_b32 v214, v255 offset:3584\n" \
"ds_read_b32 v215, v255 offset:3840\n" \
"ds_read_b32 v216, v255 offset:4096\n" \
"ds_read_b32 v217, v255 offset:4352\n" \
"ds_read_b32 v218, v255 offset:4608\n" \
"ds_read_b32 v219, v255 offset:4864\n" \
"ds_read_b32 v220, v255 offset:5120\n" \
"ds_read_b32 v221, v255 offset:5376\n" \
"ds_read_b32 v222, v255 offset:5632\n" \
"ds_read_b32 v223, v255 offset:5888\n" \
"ds_read_b32 v224, v255 offset:6144\n" \
"ds_read_b32 v254, v255 offset:7936\n" \
"s_waitcnt lgkmcnt(0)\n" \
"v_readlane_b32 s4, v254, 0\n" \
"v_readlane_b32 s5, v254, 1\n" \
"v_readlane_b32 s6, v254, 2\n" \
"v_readlane_b32 s7, v254, 3\n" \
"v_readlane_b32 s8, v254, 4\n" \
"v_readlane_b32 s9, v254, 5\n" \
"v_readlane_b32 s10, v254, 6\n" \
"v_readlane_b32 s11, v254, 7\n" \
"v_readlane_b32 s12, v254, 8\n" \
"v_readlane_b32 s13, v254, 9\n" \
"v_readlane_b32 s14, v254, 10\n" \
"v_readlane_b32 s15, v254, 11\n" \
"v_readlane_b32 s16, v254, 12\n" \
"v_readlane_b32 s17, v254, 13\n" \
"v_readlane_b32 s18, v254, 14\n" \
"v_readlane_b32 s19, v254, 15\n" \
"v_readlane_b32 s20, v254, 16\n" \
"v_readlane_b32 s21, v254, 17\n" \
"v_readlane_b32 s22, v254, 18\n" \
"v_readlane_b32 s23, v254, 19\n" \
"v_readlane_b32 s24, v254, 20\n" \
"v_readlane_b32 s25, v254, 21\n" \
"v_readlane_b32 s26, v254, 22\n" \
"v_readlane_b32 s27, v254, 23\n" \
"v_readlane_b32 s28, v254, 24\n" \
"v_readlane_b32 s29, v254, 25\n" \
"v_readlane_b32 s30, v254, 26\n" \
"v_readlane_b32 s31, v254, 27\n" \
"v_readlane_b32 s32, v254, 28\n" \
"v_readlane_b32 s33, v254, 29\n" \
"v_readlane_b32 s34, v254, 30\n" \
"v_readlane_b32 s35, v254, 31\n" \
"v_readlane_b32 s36, v254, 32\n" \
"v_readlane_b32 s37, v254, 33\n" \
"v_readlane_b32 s38, v254, 34\n" \
"v_readlane_b32 s39, v254, 35\n" \
"s_mov_b32 s40, 0\n" \
"s_mov_b32 s51, 0\n" \
"s_nop 0\n" \
"v_mov_b32 v252, s38\n" \
"s_nop 4\n" \
"v_mov_b32 v100, -1\n" \
"v_mov_b32 v101, -1\n" \
"v_mov_b32 v102, -1\n" \
"v_mov_b32 v103, -1\n" \
"s_mov_b32 s35, -2\n" \
"s_mov_b32 s2, 0\n" \
"s_mov_b32 s3, -1\n" \
"ds_read_b32 v199, v255 offset:6656\n" \
"s_waitcnt lgkmcnt(0)\n" \
"v_writelane_b32 v251, s6, 0\n" \
"v_writelane_b32 v251, s10, 1\n" \
"v_writelane_b32 v251, s14, 2\n" \
"v_writelane_b32 v251, s18, 3\n" \
"s_mov_b32 s6, 0\n" \
"s_mov_b32 s10, 0\n" \
"s_mov_b32 s14, 0\n" \
"s_mov_b32 s18, 0\n" \
"s_mov_b32 s40, -1\n" \
"s_branch .LX3_pre\n" \
".LX3_wet:\n" \
"s_waitcnt vmcnt(0)\n" \
"v_readlane_b32 s6, v251, 0\n" \
"v_readlane_b32 s10, v251, 1\n" \
"v_readlane_b32 s14, v251, 2\n" \
"v_readlane_b32 s18, v251, 3\n" \
"s_nop 4\n" \
"s_add_u32 s43, s30, 1\n" \
"s_mov_b32 s50, 0\n" \
".LX3_w85l:\n" \
"buffer_load_dword v240, v206, s[4:7], 0 offen sc1\n" \
"s_waitcnt vmcnt(0)\n" \
".LX3_w85c:\n" \
"v_subrev_u32 v241, s43, v240\n" \
"v_cmp_gt_u32 vcc, 0x100, v241\n" \
"s_cmp_eq_u64 vcc, -1\n" \
"s_cbranch_scc1 .LX3_w85d\n" \
"s_sleep 2\n" \
"s_add_u32 s50, s50, 1\n" \
"s_cmp_lt_u32 s50, 0x20000\n" \
"s_cbranch_scc1 .LX3_w85l\n" \
"s_branch .LX3_end\n" \
".LX3_w85d:\n" \
"v_readfirstlane_b32 s31, v241\n" \
"s_lshr_b32 s32, s31, 2\n" \
"s_xor_b32 s29, s31, s32\n" \
"s_and_b32 s29, s29, 3\n" \
"s_lshl_b32 s29, s29, 13\n" \
"s_bitset1_b32 s29, 21\n" \
"s_and_b32 s31, s31, 3\n" \
"s_lshl_b32 s31, s31, 13\n" \
"s_bitcmp1_b32 s39, 0\n" \
"s_cselect_b32 s43, s29, 0\n" \
"s_xor_b32 s31, s31, s43\n" \
"s_xor_b32 s32, s31, 0x4000\n" \
"s_mov_b32 s39, 0\n" \
"s_add_u32 s47, s31, 0\n" \
"buffer_store_dwordx4 v[100:103], v214, s[4:7], s47 offen sc1\n" \
"buffer_store_dwordx4 v[100:103], v217, s[4:7], s47 offen sc1\n" \
"buffer_store_dwordx4 v[100:103], v218, s[4:7], s47 offen sc1\n" \
"buffer_store_dwordx4 v[100:103], v219, s[4:7], s47 offen sc1\n" \
"s_xor_b32 s47, s47, s29\n" \
"buffer_store_dwordx4 v[100:103], v214, s[4:7], s47 offen sc1\n" \
"buffer_store_dwordx4 v[100:103], v217, s[4:7], s47 offen sc1\n" \
"buffer_store_dwordx4 v[100:103], v218, s[4:7], s47 offen sc1\n" \
"buffer_store_dwordx4 v[100:103], v219, s[4:7], s47 offen sc1\n" \
"s_add_u32 s47, s32, 524288\n" \
"buffer_store_dwordx4 v[100:103], v214, s[4:7], s47 offen sc1\n" \
"buffer_store_dwordx4 v[100:103], v217, s[4:7], s47 offen sc1\n" \
"buffer_store_dwordx4 v[100:103], v218, s[4:7], s47 offen sc1\n" \
"buffer_store_dwordx4 v[100:103], v219, s[4:7], s47 offen sc1\n" \
"s_xor_b32 s47, s47, s29\n" \
"buffer_store_dwordx4 v[100:103], v214, s[4:7], s47 offen sc1\n" \
"buffer_store_dwordx4 v[100:103], v217, s[4:7], s47 offen sc1\n" \
"buffer_store_dwordx4 v[100:103], v218, s[4:7], s47 offen sc1\n" \
"buffer_store_dwordx4 v[100:103], v219, s[4:7], s47 offen sc1\n" \
"s_add_u32 s47, s31, 1048576\n" \
"buffer_store_dwordx4 v[100:103], v214, s[4:7], s47 offen sc1\n" \
"buffer_store_dwordx4 v[100:103], v217, s[4:7], s47 offen sc1\n" \
"buffer_store_dwordx4 v[100:103], v218, s[4:7], s47 offen sc1\n" \
"buffer_store_dwordx4 v[100:103], v219, s[4:7], s47 offen sc1\n" \
"s_xor_b32 s47, s47, s29\n" \
"buffer_store_dwordx4 v[100:103], v214, s[4:7], s47 offen sc1\n" \
"buffer_store_dwordx4 v[100:103], v217, s[4:7], s47 offen sc1\n" \
"buffer_store_dwordx4 v[100:103], v218, s[4:7], s47 offen sc1\n" \
"buffer_store_dwordx4 v[100:103], v219, s[4:7], s47 offen sc1\n" \
"s_add_u32 s47, s32, 1572864\n" \
"buffer_store_dwordx4 v[100:103], v214, s[4:7], s47 offen sc1\n" \
"buffer_store_dwordx4 v[100:103], v217, s[4:7], s47 offen sc1\n" \
"buffer_store_dwordx4 v[100:103], v218, s[4:7], s47 offen sc1\n" \
"buffer_store_dwordx4 v[100:103], v219, s[4:7], s47 offen sc1\n" \
"s_xor_b32 s47, s47, s29\n" \
"buffer_store_dwordx4 v[100:103], v214, s[4:7], s47 offen sc1\n" \
"buffer_store_dwordx4 v[100:103], v217, s[4:7], s47 offen sc1\n" \
"buffer_store_dwordx4 v[100:103], v218, s[4:7], s47 offen sc1\n" \
"buffer_store_dwordx4 v[100:103], v219, s[4:7], s47 offen sc1\n" \
"s_waitcnt vmcnt(0)\n" \
"s_add_u32 s43, s30, s40\n" \
"s_add_u32 s43, s43, 1\n" \
"v_mov_b32 v241, s43\n" \
"buffer_store_dword v241, v220, s[4:7], 0 offen sc1\n" \
"s_add_u32 s43, s30, 1\n" \
"s_mov_b32 s50, 0\n" \
".LX3_w86l:\n" \
"buffer_load_dword v240, v205, s[4:7], 0 offen sc1\n" \
"s_waitcnt vmcnt(0)\n" \
".LX3_w86c:\n" \
"v_subrev_u32 v241, s43, v240\n" \
"v_cmp_gt_u32 vcc, 0x100, v241\n" \
"s_cmp_eq_u64 vcc, -1\n" \
"s_cbranch_scc1 .LX3_w86d\n" \
"s_sleep 2\n" \
"s_add_u32 s50, s50, 1\n" \
"s_cmp_lt_u32 s50, 0x20000\n" \
"s_cbranch_scc1 .LX3_w86l\n" \
"s_branch .LX3_end\n" \
".LX3_w86d:\n" \
"s_add_u32 s43, s30, 8\n" \
"s_mov_b32 s50, 0\n" \
".LX3_w87l:\n" \
"buffer_load_dword v240, v215, s[4:7], 0 offen sc1\n" \
"s_waitcnt vmcnt(0)\n" \
".LX3_w87c:\n" \
"v_subrev_u32 v241, s43, v240\n" \
"v_cmp_gt_u32 vcc, 0x100, v241\n" \
"s_cmp_eq_u64 vcc, -1\n" \
"s_cbranch_scc1 .LX3_w87d\n" \
"s_sleep 2\n" \
"s_add_u32 s50, s50, 1\n" \
"s_cmp_lt_u32 s50, 0x20000\n" \
"s_cbranch_scc1 .LX3_w87l\n" \
"s_branch .LX3_end\n" \
".LX3_w87d:\n" \
"s_add_u32 s43, s40, 0\n" \
"s_mul_i32 s43, s43, 28672\n" \
"s_add_u32 s43, s43, s33\n" \
"buffer_load_dwordx4 a[0:3], v201, s[8:11], s43 offen sc1\n" \
"buffer_load_dwordx4 a[4:7], v201, s[8:11], s43 offen offset:1024 sc1\n" \
"buffer_load_dwordx4 a[8:11], v201, s[8:11], s43 offen offset:2048 sc1\n" \
"buffer_load_dwordx4 a[12:15], v201, s[8:11], s43 offen offset:3072 sc1\n" \
"s_add_u32 s43, s43, 4096\n" \
"buffer_load_dwordx4 a[16:19], v201, s[8:11], s43 offen sc1\n" \
"buffer_load_dwordx4 a[20:23], v201, s[8:11], s43 offen offset:1024 sc1\n" \
"buffer_load_dwordx4 a[24:27], v201, s[8:11], s43 offen offset:2048 sc1\n" \
"buffer_load_dwordx4 a[28:31], v201, s[8:11], s43 offen offset:3072 sc1\n" \
"s_add_u32 s43, s43, 4096\n" \
"buffer_load_dwordx4 a[32:35], v201, s[8:11], s43 offen sc1\n" \
"buffer_load_dwordx4 a[36:39], v201, s[8:11], s43 offen offset:1024 sc1\n" \
"buffer_load_dwordx4 a[40:43], v201, s[8:11], s43 offen offset:2048 sc1\n" \
"buffer_load_dwordx4 a[44:47], v201, s[8:11], s43 offen offset:3072 sc1\n" \
"s_add_u32 s43, s43, 4096\n" \
"buffer_load_dwordx4 a[48:51], v201, s[8:11], s43 offen sc1\n" \
"buffer_load_dwordx4 a[52:55], v201, s[8:11], s43 offen offset:1024 sc1\n" \
"buffer_load_dwordx4 a[56:59], v201, s[8:11], s43 offen offset:2048 sc1\n" \
"buffer_load_dwordx4 a[60:63], v201, s[8:11], s43 offen offset:3072 sc1\n" \
"s_add_u32 s43, s43, 4096\n" \
"buffer_load_dwordx4 a[64:67], v201, s[8:11], s43 offen sc1\n" \
"buffer_load_dwordx4 a[68:71], v201, s[8:11], s43 offen offset:1024 sc1\n" \
"buffer_load_dwordx4 a[72:75], v201, s[8:11], s43 offen offset:2048 sc1\n" \
"buffer_load_dwordx4 a[76:79], v201, s[8:11], s43 offen offset:3072 sc1\n" \
"s_add_u32 s43, s43, 4096\n" \
"buffer_load_dwordx4 a[80:83], v201, s[8:11], s43 offen sc1\n" \
"buffer_load_dwordx4 a[84:87], v201, s[8:11], s43 offen offset:1024 sc1\n" \
"buffer_load_dwordx4 a[88:91], v201, s[8:11], s43 offen offset:2048 sc1\n" \
"buffer_load_dwordx4 a[92:95], v201, s[8:11], s43 offen offset:3072 sc1\n" \
"s_add_u32 s43, s43, 4096\n" \
"buffer_load_dwordx4 a[96:99], v201, s[8:11], s43 offen sc1\n" \
"buffer_load_dwordx4 a[100:103], v201, s[8:11], s43 offen offset:1024 sc1\n" \
"buffer_load_dwordx4 a[104:107], v201, s[8:11], s43 offen offset:2048 sc1\n" \
"buffer_load_dwordx4 a[108:111], v201, s[8:11], s43 offen offset:3072 sc1\n" \
"s_add_u32 s50, s40, 0\n" \
"s_mul_i32 s50, s50, 28672\n" \
"s_add_u32 s50, s50, s34\n" \
"buffer_load_dwordx4 v[28:31], v201, s[12:15], s50 offen sc1\n" \
"buffer_load_dwordx4 v[32:35], v201, s[12:15], s50 offen offset:1024 sc1\n" \
"s_waitcnt vmcnt(0)\n" \
"s_barrier\n" \
".LX3_pre:\n" \
"ds_read_b96 v[248:250], v252\n" \
"s_waitcnt lgkmcnt(0)\n" \
"v_readfirstlane_b32 s44, v248\n" \
"v_readfirstlane_b32 s45, v249\n" \
"v_readfirstlane_b32 s46, v250\n" \
"s_mul_i32 s42, s25, s46\n" \
"s_lshr_b32 s42, s42, 12\n" \
".LX3_cu:\n" \
"s_sub_u32 s43, s24, s44\n" \
"s_cmp_lt_i32 s43, 0\n" \
"s_cbranch_scc0 .LX3_cud\n" \
"s_add_u32 s24, s24, s25\n" \
"s_xor_b32 s31, s31, s29\n" \
"s_xor_b32 s32, s32, s29\n" \
"s_xor_b32 s47, s47, s29\n" \
"s_add_u32 s39, s39, 1\n" \
"s_branch .LX3_cu\n" \
".LX3_cud:\n" \
".LX3_loop:\n" \
"s_add_u32 s43, s40, 2\n" \
"s_and_b32 s43, s43, 3\n" \
"s_bitcmp1_b32 s43, 0\n" \
"s_cselect_b32 s49, s32, s31\n" \
"s_lshl_b32 s43, s43, 19\n" \
"s_add_u32 s49, s49, s43\n" \
"ds_read_b96 v[248:250], v252\n" \
"s_waitcnt lgkmcnt(0)\n" \
"v_readfirstlane_b32 s44, v248\n" \
"v_readfirstlane_b32 s45, v249\n" \
"v_readfirstlane_b32 s46, v250\n" \
"s_mul_i32 s42, s25, s46\n" \
"s_lshr_b32 s42, s42, 12\n" \
"s_add_u32 s43, s40, 0\n" \
"s_and_b32 s43, s43, 3\n" \
"s_bitcmp1_b32 s43, 0\n" \
"s_cselect_b32 s47, s32, s31\n" \
"s_lshl_b32 s43, s43, 19\n" \
"s_add_u32 s47, s47, s43\n" \
"s_sub_u32 s43, s24, s44\n" \
"s_add_u32 s43, s43, s28\n" \
"s_max_i32 s43, s43, 0xfffd0000\n" \
"s_min_i32 s43, s43, 0x4000\n" \
"s_mul_i32 s43, s43, s46\n" \
"s_ashr_i32 s43, s43, 12\n" \
"s_add_u32 s41, s45, s43\n" \
"s_branch .LX3_a1\n" \
".LX3_retrya:\n" \
"s_add_u32 s51, s51, 1\n" \
"s_cmp_gt_u32 s51, 0x20000\n" \
"s_cbranch_scc1 .LX3_end\n" \
".LX3_a0:\n" \
"s_add_u32 s41, s41, s42\n" \
"s_add_u32 s24, s24, s25\n" \
"s_xor_b32 s31, s31, s29\n" \
"s_xor_b32 s32, s32, s29\n" \
"s_xor_b32 s47, s47, s29\n" \
"s_xor_b32 s48, s48, s29\n" \
"s_add_u32 s39, s39, 1\n" \
"buffer_store_dwordx4 v[100:103], v214, s[4:7], s47 offen sc1\n" \
"buffer_store_dwordx4 v[100:103], v217, s[4:7], s47 offen sc1\n" \
"buffer_store_dwordx4 v[100:103], v218, s[4:7], s47 offen sc1\n" \
"buffer_store_dwordx4 v[100:103], v219, s[4:7], s47 offen sc1\n" \
".p2align 6\n" \
".LX3_a1:\n" \
"s_memtime vcc\n" \
"s_waitcnt lgkmcnt(0)\n" \
"s_sub_u32 s43, s41, vcc_lo\n" \
"s_cmp_gt_i32 s43, 128\n" \
"s_cbranch_scc1 .LX3_a1\n" \
"s_cmp_lt_i32 s43, s37\n" \
"s_cbranch_scc1 .LX3_a0\n" \
"s_max_i32 s43, s43, 0\n" \
"s_and_b32 s43, s43, -4\n" \
"s_getpc_b64 vcc\n" \
".LX3_a3:\n" \
"s_sub_u32 s43, .LX3_a4-.LX3_a3, s43\n" \
"s_add_u32 vcc_lo, vcc_lo, s43\n" \
"s_addc_u32 vcc_hi, vcc_hi, 0\n" \
"s_setpc_b64 vcc\n" \
".rept 32\n" \
"s_nop 0\n" \
".endr\n" \
".LX3_a4:\n" \
"buffer_load_dwordx4 v[0:3], v208, s[4:7], s47 offen sc1\n" \
"buffer_load_dwordx4 v[4:7], v208, s[4:7], s47 offen offset:1024 sc1\n" \
"buffer_load_dwordx4 v[8:11], v208, s[4:7], s47 offen offset:2048 sc1\n" \
"buffer_load_dwordx4 v[12:15], v208, s[4:7], s47 offen offset:3072 sc1\n" \
"buffer_load_dwordx4 v[16:19], v209, s[4:7], s47 offen sc1\n" \
"buffer_load_dwordx4 v[20:23], v209, s[4:7], s47 offen offset:1024 sc1\n" \
"buffer_load_dwordx4 v[24:27], v209, s[4:7], s47 offen offset:2048 sc1\n" \
"s_cmp_eq_u32 s35, s40\n" \
"s_cbranch_scc1 .LX3_nb\n" \
"s_barrier\n" \
".LX3_nb:\n" \
"s_mov_b32 s35, s40\n" \
"v_mov_b32 v96, s47\n" \
"ds_write_b32 v252, v96 offset:16\n" \
"v_mov_b32 v60, 0\n" \
"v_mov_b32 v61, 0\n" \
"v_mov_b32 v62, 0\n" \
"v_mov_b32 v63, 0\n" \
"v_mov_b32 v64, 0\n" \
"v_mov_b32 v65, 0\n" \
"v_mov_b32 v66, 0\n" \
"v_mov_b32 v67, 0\n" \
"s_lshl_b32 s43, s40, 7\n" \
"buffer_load_dwordx4 v[236:239], v207, s[4:7], s43 offen sc1\n" \
"s_add_u32 s50, s40, 1\n" \
"s_mul_i32 s50, s50, 28672\n" \
"s_add_u32 s50, s50, s34\n" \
"buffer_load_dwordx4 v[44:47], v201, s[12:15], s50 offen sc1\n" \
"buffer_load_dwordx4 v[48:51], v201, s[12:15], s50 offen offset:1024 sc1\n" \
"s_waitcnt vmcnt(9)\n" \
"v_smfmac_f32_16x16x64_bf16 v[60:63], v[0:3], a[0:7], v200\n" \
"v_smfmac_f32_16x16x64_bf16 v[64:67], v[0:3], a[56:63], v200\n" \
"s_waitcnt vmcnt(8)\n" \
"v_smfmac_f32_16x16x64_bf16 v[60:63], v[4:7], a[8:15], v200\n" \
"v_smfmac_f32_16x16x64_bf16 v[64:67], v[4:7], a[64:71], v200\n" \
"s_waitcnt vmcnt(7)\n" \
"v_smfmac_f32_16x16x64_bf16 v[60:63], v[8:11], a[16:23], v200\n" \
"v_smfmac_f32_16x16x64_bf16 v[64:67], v[8:11], a[72:79], v200\n" \
"s_waitcnt vmcnt(6)\n" \
"v_smfmac_f32_16x16x64_bf16 v[60:63], v[12:15], a[24:31], v200\n" \
"v_smfmac_f32_16x16x64_bf16 v[64:67], v[12:15], a[80:87], v200\n" \
"s_waitcnt vmcnt(5)\n" \
"v_smfmac_f32_16x16x64_bf16 v[60:63], v[16:19], a[32:39], v200\n" \
"v_smfmac_f32_16x16x64_bf16 v[64:67], v[16:19], a[88:95], v200\n" \
"s_waitcnt vmcnt(4)\n" \
"v_smfmac_f32_16x16x64_bf16 v[60:63], v[20:23], a[40:47], v200\n" \
"v_smfmac_f32_16x16x64_bf16 v[64:67], v[20:23], a[96:103], v200\n" \
"s_waitcnt vmcnt(3)\n" \
"v_smfmac_f32_16x16x64_bf16 v[60:63], v[24:27], a[48:55], v200\n" \
"v_smfmac_f32_16x16x64_bf16 v[64:67], v[24:27], a[104:111], v200\n" \
"s_nop 2\n" \
"v_max3_u32 v84, v0, v0, v1\n" \
"v_max3_u32 v84, v84, v2, v3\n" \
"v_max3_u32 v84, v84, v4, v5\n" \
"v_max3_u32 v84, v84, v6, v7\n" \
"v_max3_u32 v84, v84, v8, v9\n" \
"v_permlane32_swap_b32 v60, v64\n" \
"v_permlane32_swap_b32 v61, v65\n" \
"v_permlane32_swap_b32 v62, v66\n" \
"v_permlane32_swap_b32 v63, v67\n" \
"v_pk_add_f32 v[60:61], v[60:61], v[64:65]\n" \
"v_pk_add_f32 v[62:63], v[62:63], v[66:67]\n" \
"v_max3_u32 v84, v84, v10, v11\n" \
"v_max_f32_dpp v88, v60, v60 row_ror:8 row_mask:0xf bank_mask:0x3\n" \
"v_max_f32_dpp v89, v61, v61 row_ror:8 row_mask:0xf bank_mask:0x3\n" \
"v_max_f32_dpp v88, v62, v62 row_ror:8 row_mask:0xf bank_mask:0xc\n" \
"v_max_f32_dpp v89, v63, v63 row_ror:8 row_mask:0xf bank_mask:0xc\n" \
"v_max3_u32 v84, v84, v12, v13\n" \
"v_max_f32_dpp v90, v88, v88 row_shl:4 row_mask:0xf bank_mask:0x5\n" \
"v_max_f32_dpp v90, v89, v89 row_shr:4 row_mask:0xf bank_mask:0xa\n" \
"v_max3_u32 v84, v84, v14, v15\n" \
"v_max3_u32 v84, v84, v16, v17\n" \
"v_max_f32_dpp v88, v90, v90 quad_perm:[1,0,3,2] row_mask:0xf bank_mask:0xf\n" \
"v_max3_u32 v84, v84, v18, v19\n" \
"v_max3_u32 v84, v84, v20, v21\n" \
"v_max_f32_dpp v86, v88, v88 quad_perm:[2,3,0,1] row_mask:0xf bank_mask:0xf\n" \
"v_max_f32_dpp v89, v88, v88 quad_perm:[2,3,0,1] row_mask:0xf bank_mask:0xf\n" \
"v_max3_u32 v84, v84, v22, v23\n" \
"v_max3_u32 v84, v84, v24, v25\n" \
"v_permlane32_swap_b32 v86, v89\n" \
"v_max_f32 v86, v86, v89\n" \
"v_max3_u32 v84, v84, v26, v27\n" \
"v_cmp_eq_u32 vcc, -1, v84\n" \
"v_subrev_f32_dpp v60, v86, v60 row_newbcast:0 row_mask:0xf bank_mask:0xf\n" \
"v_subrev_f32_dpp v61, v86, v61 row_newbcast:4 row_mask:0xf bank_mask:0xf\n" \
"v_subrev_f32_dpp v62, v86, v62 row_newbcast:8 row_mask:0xf bank_mask:0xf\n" \
"v_subrev_f32_dpp v63, v86, v63 row_newbcast:12 row_mask:0xf bank_mask:0xf\n" \
"v_exp_f32 v60, v60\n" \
"v_exp_f32 v61, v61\n" \
"v_exp_f32 v62, v62\n" \
"v_exp_f32 v63, v63\n" \
"v_cvt_pk_bf16_f32 v92, v60, v61\n" \
"v_cvt_pk_bf16_f32 v93, v62, v63\n" \
"ds_write_b64 v212, v[92:93]\n" \
"s_cbranch_vccnz .LX3_retrya\n" \
"v_add_f32_dpp v88, v60, v60 row_ror:8 row_mask:0xf bank_mask:0x3\n" \
"v_add_f32_dpp v89, v61, v61 row_ror:8 row_mask:0xf bank_mask:0x3\n" \
"v_add_f32_dpp v88, v62, v62 row_ror:8 row_mask:0xf bank_mask:0xc\n" \
"v_add_f32_dpp v89, v63, v63 row_ror:8 row_mask:0xf bank_mask:0xc\n" \
"v_mov_b32 v245, v245\n" \
"v_add_f32_dpp v90, v88, v88 row_shl:4 row_mask:0xf bank_mask:0x5\n" \
"v_add_f32_dpp v90, v89, v89 row_shr:4 row_mask:0xf bank_mask:0xa\n" \
"v_mov_b32 v245, v245\n" \
"v_mov_b32 v245, v245\n" \
"v_add_f32_dpp v88, v90, v90 quad_perm:[1,0,3,2] row_mask:0xf bank_mask:0xf\n" \
"v_mov_b32 v245, v245\n" \
"v_mov_b32 v245, v245\n" \
"v_add_f32_dpp v87, v88, v88 quad_perm:[2,3,0,1] row_mask:0xf bank_mask:0xf\n" \
"buffer_store_dwordx2 v[86:87], v211, s[4:7], s47 offen sc1\n" \
"s_nop 0\n" \
"v_mov_b32 v245, v245\n" \
"s_waitcnt lgkmcnt(0)\n" \
"s_barrier\n" \
"ds_read_b64_tr_b16 v[92:93], v213\n" \
"ds_read_b64_tr_b16 v[94:95], v213 offset:64\n" \
"ds_read_b32 v84, v252 offset:16\n" \
"s_waitcnt lgkmcnt(1)\n" \
"v_mfma_f32_16x16x32_bf16 v[68:71], v[92:95], v[28:31], 0\n" \
"s_waitcnt lgkmcnt(0)\n" \
"v_xor_b32 v85, s29, v84\n" \
"v_mfma_f32_16x16x32_bf16 v[72:75], v[92:95], v[32:35], 0\n" \
"v_add_u32 v84, v84, v210\n" \
"v_add_u32 v85, v85, v210\n" \
"v_mov_b32 v245, v245\n" \
"v_mov_b32 v245, v245\n" \
"v_mov_b32 v245, v245\n" \
"v_cvt_pk_bf16_f32 v96, v68, v69\n" \
"v_cvt_pk_bf16_f32 v97, v70, v71\n" \
"s_mov_b64 exec, s[2:3]\n" \
"v_cvt_pk_bf16_f32 v96, v72, v73\n" \
"v_cvt_pk_bf16_f32 v97, v74, v75\n" \
"s_mov_b64 exec, -1\n" \
"v_mov_b32 v98, v96\n" \
"v_mov_b32 v99, v97\n" \
"s_nop 1\n" \
"v_permlane16_swap_b32 v96, v98\n" \
"v_permlane16_swap_b32 v97, v99\n" \
"buffer_store_dwordx4 v[96:99], v84, s[4:7], 0 offen sc1\n" \
"ds_write_b128 v216, v[0:3] offset:0\n" \
"ds_write_b128 v216, v[4:7] offset:1024\n" \
"ds_write_b128 v216, v[8:11] offset:2048\n" \
"ds_write_b128 v216, v[12:15] offset:3072\n" \
"ds_write_b128 v216, v[16:19] offset:4096\n" \
"ds_write_b128 v216, v[20:23] offset:5120\n" \
"ds_write_b128 v216, v[24:27] offset:6144\n" \
"s_waitcnt lgkmcnt(0)\n" \
"s_barrier\n" \
"buffer_store_dwordx4 v[96:99], v85, s[4:7], 0 offen sc1\n" \
"s_xor_b32 s43, s47, s29\n" \
"buffer_store_dwordx2 v[86:87], v211, s[4:7], s43 offen sc1\n" \
"s_cmp_eq_u32 s40, 62\n" \
"s_cbranch_scc1 .LX3_ng\n" \
"s_add_u32 s43, s40, 1\n" \
"s_mul_i32 s43, s43, 28672\n" \
"s_add_u32 s43, s43, s33\n" \
"buffer_load_dwordx4 a[0:3], v201, s[8:11], s43 offen sc1\n" \
"buffer_load_dwordx4 a[4:7], v201, s[8:11], s43 offen offset:1024 sc1\n" \
"buffer_load_dwordx4 a[8:11], v201, s[8:11], s43 offen offset:2048 sc1\n" \
"buffer_load_dwordx4 a[12:15], v201, s[8:11], s43 offen offset:3072 sc1\n" \
"s_add_u32 s43, s43, 4096\n" \
"buffer_load_dwordx4 a[16:19], v201, s[8:11], s43 offen sc1\n" \
"buffer_load_dwordx4 a[20:23], v201, s[8:11], s43 offen offset:1024 sc1\n" \
"buffer_load_dwordx4 a[24:27], v201, s[8:11], s43 offen offset:2048 sc1\n" \
"buffer_load_dwordx4 a[28:31], v201, s[8:11], s43 offen offset:3072 sc1\n" \
"s_add_u32 s43, s43, 4096\n" \
"buffer_load_dwordx4 a[32:35], v201, s[8:11], s43 offen sc1\n" \
"buffer_load_dwordx4 a[36:39], v201, s[8:11], s43 offen offset:1024 sc1\n" \
"buffer_load_dwordx4 a[40:43], v201, s[8:11], s43 offen offset:2048 sc1\n" \
"buffer_load_dwordx4 a[44:47], v201, s[8:11], s43 offen offset:3072 sc1\n" \
"s_add_u32 s43, s43, 4096\n" \
"buffer_load_dwordx4 a[48:51], v201, s[8:11], s43 offen sc1\n" \
"buffer_load_dwordx4 a[52:55], v201, s[8:11], s43 offen offset:1024 sc1\n" \
"buffer_load_dwordx4 a[56:59], v201, s[8:11], s43 offen offset:2048 sc1\n" \
"buffer_load_dwordx4 a[60:63], v201, s[8:11], s43 offen offset:3072 sc1\n" \
"s_add_u32 s43, s43, 4096\n" \
"buffer_load_dwordx4 a[64:67], v201, s[8:11], s43 offen sc1\n" \
"buffer_load_dwordx4 a[68:71], v201, s[8:11], s43 offen offset:1024 sc1\n" \
"buffer_load_dwordx4 a[72:75], v201, s[8:11], s43 offen offset:2048 sc1\n" \
"buffer_load_dwordx4 a[76:79], v201, s[8:11], s43 offen offset:3072 sc1\n" \
"s_add_u32 s43, s43, 4096\n" \
"buffer_load_dwordx4 a[80:83], v201, s[8:11], s43 offen sc1\n" \
"buffer_load_dwordx4 a[84:87], v201, s[8:11], s43 offen offset:1024 sc1\n" \
"buffer_load_dwordx4 a[88:91], v201, s[8:11], s43 offen offset:2048 sc1\n" \
"buffer_load_dwordx4 a[92:95], v201, s[8:11], s43 offen offset:3072 sc1\n" \
"s_add_u32 s43, s43, 4096\n" \
"buffer_load_dwordx4 a[96:99], v201, s[8:11], s43 offen sc1\n" \
"buffer_load_dwordx4 a[100:103], v201, s[8:11], s43 offen offset:1024 sc1\n" \
"buffer_load_dwordx4 a[104:107], v201, s[8:11], s43 offen offset:2048 sc1\n" \
"buffer_load_dwordx4 a[108:111], v201, s[8:11], s43 offen offset:3072 sc1\n" \
"buffer_store_dwordx4 v[100:103], v214, s[4:7], s49 offen sc1\n" \
"s_xor_b32 s43, s49, s29\n" \
"buffer_store_dwordx4 v[100:103], v214, s[4:7], s43 offen sc1\n" \
"s_waitcnt vmcnt(34)\n" \
"s_branch .LX3_ng2\n" \
".LX3_ng:\n" \
"s_waitcnt vmcnt(4)\n" \
".LX3_ng2:\n" \
"v_mov_b32 v28, v44\n" \
"v_mov_b32 v29, v45\n" \
"v_mov_b32 v30, v46\n" \
"v_mov_b32 v31, v47\n" \
"v_mov_b32 v32, v48\n" \
"v_mov_b32 v33, v49\n" \
"v_mov_b32 v34, v50\n" \
"v_mov_b32 v35, v51\n" \
"v_max_u32 v236, v236, v238\n" \
"v_max_u32 v237, v237, v239\n" \
"v_and_b32 v236, 0xfff, v236\n" \
"v_and_b32 v237, 0xfff, v237\n" \
"s_nop 0\n" \
"v_readfirstlane_b32 s26, v236\n" \
"v_readfirstlane_b32 s27, v237\n" \
"s_add_u32 s24, s24, s26\n" \
"s_add_u32 s24, s24, s27\n" \
"s_add_u32 s40, s40, 1\n" \
"s_cmp_eq_u32 s40, 0\n" \
"s_cbranch_scc1 .LX3_fin\n" \
"s_cmp_lt_u32 s40, 63\n" \
"s_cbranch_scc1 .LX3_loop\n" \
".LX3_fin:\n" \
"s_barrier\n" \
"ds_read_b96 v[248:250], v252\n" \
"s_waitcnt lgkmcnt(0)\n" \
"v_readfirstlane_b32 s44, v248\n" \
"v_readfirstlane_b32 s45, v249\n" \
"v_readfirstlane_b32 s46, v250\n" \
"s_mul_i32 s42, s25, s46\n" \
"s_lshr_b32 s42, s42, 12\n" \
"s_add_u32 s43, s40, 0\n" \
"s_and_b32 s43, s43, 3\n" \
"s_bitcmp1_b32 s43, 0\n" \
"s_cselect_b32 s47, s32, s31\n" \
"s_lshl_b32 s43, s43, 19\n" \
"s_add_u32 s47, s47, s43\n" \
"s_sub_u32 s43, s24, s44\n" \
"s_add_u32 s43, s43, s28\n" \
"s_max_i32 s43, s43, 0xfffd0000\n" \
"s_min_i32 s43, s43, 0x4000\n" \
"s_mul_i32 s43, s43, s46\n" \
"s_ashr_i32 s43, s43, 12\n" \
"s_add_u32 s41, s45, s43\n" \
"s_branch .LX3_z1\n" \
".LX3_retryz:\n" \
"s_add_u32 s51, s51, 1\n" \
"s_cmp_gt_u32 s51, 0x20000\n" \
"s_cbranch_scc1 .LX3_end\n" \
".LX3_z0:\n" \
"s_add_u32 s41, s41, s42\n" \
"s_add_u32 s24, s24, s25\n" \
"s_xor_b32 s31, s31, s29\n" \
"s_xor_b32 s32, s32, s29\n" \
"s_xor_b32 s47, s47, s29\n" \
"s_xor_b32 s48, s48, s29\n" \
"s_add_u32 s39, s39, 1\n" \
".p2align 6\n" \
".LX3_z1:\n" \
"s_memtime vcc\n" \
"s_waitcnt lgkmcnt(0)\n" \
"s_sub_u32 s43, s41, vcc_lo\n" \
"s_cmp_gt_i32 s43, 128\n" \
"s_cbranch_scc1 .LX3_z1\n" \
"s_cmp_lt_i32 s43, s37\n" \
"s_cbranch_scc1 .LX3_z0\n" \
"s_max_i32 s43, s43, 0\n" \
"s_and_b32 s43, s43, -4\n" \
"s_getpc_b64 vcc\n" \
".LX3_z3:\n" \
"s_sub_u32 s43, .LX3_z4-.LX3_z3, s43\n" \
"s_add_u32 vcc_lo, vcc_lo, s43\n" \
"s_addc_u32 vcc_hi, vcc_hi, 0\n" \
"s_setpc_b64 vcc\n" \
".rept 32\n" \
"s_nop 0\n" \
".endr\n" \
".LX3_z4:\n" \
"buffer_load_dwordx4 v[0:3], v208, s[4:7], s47 offen sc1\n" \
"buffer_load_dwordx4 v[4:7], v208, s[4:7], s47 offen offset:1024 sc1\n" \
"buffer_load_dwordx4 v[8:11], v208, s[4:7], s47 offen offset:2048 sc1\n" \
"buffer_load_dwordx4 v[12:15], v208, s[4:7], s47 offen offset:3072 sc1\n" \
"buffer_load_dwordx4 v[16:19], v209, s[4:7], s47 offen sc1\n" \
"buffer_load_dwordx4 v[20:23], v209, s[4:7], s47 offen offset:1024 sc1\n" \
"buffer_load_dwordx4 v[24:27], v209, s[4:7], s47 offen offset:2048 sc1\n" \
"s_cmp_eq_u32 s35, -3\n" \
"s_cbranch_scc1 .LX3_nc\n" \
"s_barrier\n" \
".LX3_nc:\n" \
"s_mov_b32 s35, -3\n" \
"s_waitcnt vmcnt(0)\n" \
"ds_write_b128 v216, v[0:3] offset:7168\n" \
"ds_write_b128 v216, v[4:7] offset:8192\n" \
"ds_write_b128 v216, v[8:11] offset:9216\n" \
"ds_write_b128 v216, v[12:15] offset:10240\n" \
"ds_write_b128 v216, v[16:19] offset:11264\n" \
"ds_write_b128 v216, v[20:23] offset:12288\n" \
"ds_write_b128 v216, v[24:27] offset:13312\n" \
"v_max3_u32 v84, v0, v0, v1\n" \
"v_max3_u32 v84, v84, v2, v3\n" \
"v_max3_u32 v84, v84, v4, v5\n" \
"v_max3_u32 v84, v84, v6, v7\n" \
"v_max3_u32 v84, v84, v8, v9\n" \
"v_max3_u32 v84, v84, v10, v11\n" \
"v_max3_u32 v84, v84, v12, v13\n" \
"v_max3_u32 v84, v84, v14, v15\n" \
"v_max3_u32 v84, v84, v16, v17\n" \
"v_max3_u32 v84, v84, v18, v19\n" \
"v_max3_u32 v84, v84, v20, v21\n" \
"v_max3_u32 v84, v84, v22, v23\n" \
"v_max3_u32 v84, v84, v24, v25\n" \
"v_max3_u32 v84, v84, v26, v27\n" \
"v_cmp_eq_u32 vcc, -1, v84\n" \
"s_cbranch_vccnz .LX3_retryz\n" \
"s_waitcnt lgkmcnt(0)\n" \
"s_barrier\n" \
"s_barrier\n" \
"s_cmp_eq_u32 s40, 0\n" \
"s_cbranch_scc1 .LX3_wet\n" \
".LX3_end:\n" \
"s_endpgm\n"
typedef __attribute__((ext_vector_type(16))) float f16v;
DI u4 pack8(const f16v& x, int s, float sc) { return (u4){pk2(x[8 * s] * sc, x[8 * s + 1] * sc), pk2(x[8 * s + 2] * sc, x[8 * s + 3] * sc), pk2(x[8 * s + 4] * sc, x[8 * s + 5] * sc), pk2(x[8 * s + 6] * sc, x[8 * s + 7] * sc)}; }
DI u4 pack8(const f16v& x, int s) { return (u4){pk2(x[8 * s], x[8 * s + 1]), pk2(x[8 * s + 2], x[8 * s + 3]), pk2(x[8 * s + 4], x[8 * s + 5]), pk2(x[8 * s + 6], x[8 * s + 7])}; }
DI void dma_pair(u4 srd, int v0, int v1, int soff, unsigned m) {
    asm volatile("s_mov_b32 m0, %4\n\ts_nop 0\n\tbuffer_load_dwordx4 %0, %2, %3 offen lds\n\tbuffer_load_dwordx4 %1, %2, %3 offen offset:1024 lds" :: "v"(v0), "v"(v1), "s"(srd), "s"(soff), "s"(m) : "memory");
}
DI void dma_one(u4 srd, int v0, int soff, unsigned m) {
    asm volatile("s_mov_b32 m0, %3\n\ts_nop 0\n\tbuffer_load_dwordx4 %0, %1, %2 offen lds" :: "v"(v0), "s"(srd), "s"(soff), "s"(m) : "memory");
}
DI void mV_va(f16v& c, u4 x, u4 y) { asm volatile("v_mfma_f32_32x32x16_bf16 %0, %1, %2, %0" : "+v"(c) : "v"(x), "a"(y)); }
DI void mV_va0(f16v& c, u4 x, u4 y) { asm volatile("v_mfma_f32_32x32x16_bf16 %0, %1, %2, 0" : "=&v"(c) : "v"(x), "a"(y)); }
DI void mV_av(f16v& c, u4 x, u4 y) { asm volatile("v_mfma_f32_32x32x16_bf16 %0, %1, %2, %0" : "+v"(c) : "a"(x), "v"(y)); }
DI void mV_av0(f16v& c, u4 x, u4 y) { asm volatile("v_mfma_f32_32x32x16_bf16 %0, %1, %2, 0" : "=&v"(c) : "a"(x), "v"(y)); }
DI void mV_vv(f16v& c, u4 x, u4 y) { asm volatile("v_mfma_f32_32x32x16_bf16 %0, %1, %2, %0" : "+v"(c) : "v"(x), "v"(y)); }
DI void mV_vv0(f16v& c, u4 x, u4 y) { asm volatile("v_mfma_f32_32x32x16_bf16 %0, %1, %2, 0" : "=&v"(c) : "v"(x), "v"(y)); }
template <int N, int PD, class RD, class MM, class EX> DI void pipe(RD rd, MM mm, EX ex) {
    u4 f[PD];
#pragma unroll
    for (int j = 0; j < PD; ++j) f[j] = rd(j);
    SB();
#pragma unroll
    for (int j = 0; j < N; ++j) { mm(j, f[j % PD]); if (j + PD < N) f[j % PD] = rd(j + PD); ex(j); SB(); }
}
DI u4 sld128(u4 srd, int voff, int soff) { u4 v; asm volatile("s_nop 4\n\tbuffer_load_dwordx4 %0, %1, %2, %3 offen sc1" : "=v"(v) : "v"(voff), "s"(srd), "s"(soff) : "memory"); return v; }
DI u2 sld64(u4 srd, int voff, int soff) { u2 v; asm volatile("s_nop 4\n\tbuffer_load_dwordx2 %0, %1, %2, %3 offen sc1" : "=v"(v) : "v"(voff), "s"(srd), "s"(soff) : "memory"); return v; }
DI void sst128(u4 v, u4 srd, int voff, int soff) { asm volatile("s_nop 4\n\tbuffer_store_dwordx4 %0, %1, %2, %3 offen sc1\n\ts_nop 1" :: "v"(v), "v"(voff), "s"(srd), "s"(soff) : "memory"); }
DI void sst64(u2 v, u4 srd, int voff, int soff) { asm volatile("s_nop 4\n\tbuffer_store_dwordx2 %0, %1, %2, %3 offen sc1" :: "v"(v), "v"(voff), "s"(srd), "s"(soff) : "memory"); }
DI void sst32(unsigned v, u4 srd, int voff, int soff) { asm volatile("s_nop 4\n\tbuffer_store_dword %0, %1, %2, %3 offen sc1" :: "v"(v), "v"(voff), "s"(srd), "s"(soff) : "memory"); }
DI unsigned sld32(u4 srd, int voff, int soff) { unsigned v; asm volatile("s_nop 4\n\tbuffer_load_dword %0, %1, %2, %3 offen sc1" : "=v"(v) : "v"(voff), "s"(srd), "s"(soff) : "memory"); return v; }
template <int N> DI void waitvm() { asm volatile("s_waitcnt vmcnt(%0)" :: "i"(N) : "memory"); }
DI int swzr(int row) { return ((row & 1) << 2) | (row & 8) | ((row >> 1) & 3); }
#define SLICE 32768
#define CSCALE (LOG2E * 0.0625f)

struct SideCtx {
    char LAS* sm; unsigned smb; int lane, wave;
    int vr0, vr1, vc0, vc1;
};
DI int fresh_lane() { int l; asm volatile("v_mbcnt_lo_u32_b32 %0, -1, 0\n\tv_mbcnt_hi_u32_b32 %0, -1, %0" : "=v"(l)); __builtin_assume(l >= 0 && l < 64); return l; }
DI SideCtx make_ctx(int wave) {
    SideCtx c; c.sm = (char LAS*)smem_; c.smb = (unsigned)(size_t)c.sm; c.lane = fresh_lane(); c.wave = wave;
    asm volatile("" : "+s"(c.wave), "+s"(c.smb));
    const int l = c.lane;
    { int row = 4 * c.wave + (l >> 5); c.vr0 = row * 8192 + (((l & 31) ^ swzr(row)) * 16); row += 2; c.vr1 = row * 8192 + (((l & 31) ^ swzr(row)) * 16) - 1024; }
    { int row = l >> 3; c.vc0 = row * 896 + (((l & 7) ^ (((row >> 1) & 1) << 2)) * 16); c.vc1 = c.vc0 + 8 * 896 - 1024; }
    return c;
}
DI void dma_rows_q(const SideCtx& c, u4 srd, int h, int r0, int slot, int g) { dma_pair(srd, c.vr0, c.vr1, (r0 + 16 * g) * 8192 + h * 512, c.smb + slot * SLICE + (8 * g + 2 * c.wave) * 1024); }
DI void dma_cols_q(const SideCtx& c, u4 srd, int h, int c0, int slot, int g) { const int k = 8 * c.wave + 2 * g; dma_pair(srd, c.vc0, c.vc1, (h * 256 + 8 * k) * 896 + c0 * 2, c.smb + slot * SLICE + k * 1024); }
DI void dma_rows(const SideCtx& c, u4 srd, int h, int r0, int slot) {
#pragma unroll
    for (int g = 0; g < 4; ++g) dma_rows_q(c, srd, h, r0, slot, g);
}
DI void dma_cols(const SideCtx& c, u4 srd, int h, int c0, int slot) {
#pragma unroll
    for (int g = 0; g < 4; ++g) dma_cols_q(c, srd, h, c0, slot, g);
}
DI u4 rd_rows(const SideCtx& c, int slot, int ks, int tau) {
    const int l = c.lane, hi = l >> 5, ro = l & 31;
    const int rowc = 32 * (ro >> 4) + 4 * ((ro >> 2) & 1) + 8 * ((ro >> 1) & 1) + 2 * ((ro >> 3) & 1) + (ro & 1);
    const int ch = (2 * (ks & 7) + hi) ^ swzr(rowc);
    return *(const u4 LAS*)(c.sm + (rowc * 512 + ch * 16) + (slot * SLICE + tau * 8192 + (ks >> 3) * 256));
}
DI u4 tr_cols(const SideCtx& c, int slot, int ks, int nt) {
    const int l = c.lane, hi = l >> 5, blk = (l >> 4) & 1, r = (l & 15) >> 2, p = l & 3;
    u2 o[2];
#pragma unroll
    for (int t = 0; t < 2; ++t) {
        const int ch = (4 * nt + 2 * blk + (p >> 1)) ^ (((r >> 1) & 1) << 2);
        o[t] = trread(c.sm + ((8 * hi + r) * 128 + ch * 16 + 8 * (p & 1)) + (slot * SLICE + ks * 2048 + t * 512));
    }
    return (u4){o[0][0], o[0][1], o[1][0], o[1][1]};
}
#define W_SLP 4
#define P_ESLP 0
#define MID_K 20
#define PF_AHEAD 12
#define NSEG1 10
DI void set_flag(const SideCtx& c, u4 rhb, int off, unsigned val) {
    waitvm<0>(); bar();
    if (c.wave == 0) sst32(val, rhb, c.lane == 0 ? off : OOB, 0);
}
DI void wait_prog(u4 rhb, int voff_or_oob, unsigned want) {
    unsigned sp = 0;
#pragma nounroll
    for (;;) {
        unsigned v = sld32(rhb, voff_or_oob, 0); WAITV(0, "+v"(v));
        if (__all((v - want < 256u) | (voff_or_oob >= OOB))) break;
        asm volatile("s_sleep %0" :: "i"(W_SLP) : "memory");
        if (++sp > SPIN_MAX) __builtin_amdgcn_endpgm();
    }
}
#define MFW32(x, y) asm volatile("s_nop 11" : "+v"(x), "+v"(y))
DI int perm16(int r) { return 4 * ((r >> 2) & 1) + 8 * ((r >> 1) & 1) + 2 * ((r >> 3) & 1) + (r & 1); }
#define CHUNK(u) ((u) == 6 ? 3 : ((u) >> 1) + 4 * ((u) & 1))
DI void w_first(int wave, int type, int h, int sub, int part) {
    CArgs& a = get_args();
    const SideCtx c = make_ctx(wave);
    h = __builtin_amdgcn_readfirstlane(h); sub = __builtin_amdgcn_readfirstlane(sub); asm volatile("" : "+s"(h), "+s"(sub));
    const u4 rw1 = mksrd(type ? a.Wv : a.Wk, 448 * 8192), rw2 = mksrd(type ? a.Wo : a.Wq, 448 * 8192);
    const int c1 = sub == 3 ? 384 : sub * 128 + 64;
    if (part) { dma_rows(c, rw1, h, 64 * CHUNK(0), 0); dma_rows(c, rw1, h, 64 * CHUNK(1), 1); }
    else if (type) { dma_cols(c, rw2, h, sub * 128, 2); dma_cols(c, rw2, h, c1, 3); }
    else { dma_rows(c, rw2, h, sub * 128, 2); dma_rows(c, rw2, h, c1, 3); }
}
template <int N> struct IntC { static constexpr int value = N; };
template <int TYPE>
DI void wgu_task(unsigned tag, int vb, int wave, int h, int sub) {
    CArgs& a = get_args();
    const SideCtx c = make_ctx(wave);
    const int lane = c.lane, hi = lane >> 5, l31 = lane & 31, w = c.wave;
    h = __builtin_amdgcn_readfirstlane(h); sub = __builtin_amdgcn_readfirstlane(sub); asm volatile("" : "+s"(h), "+s"(sub));
    const u4 rw1 = mksrd(TYPE ? a.Wv : a.Wk, 448 * 8192), rx = mksrd(a.X, 2048 * 896), rhb = mksrd(a.HB, HB_SZ);
    const int o_fl = (w == 0 && lane == 0) ? O_FW + ((h * 2 + TYPE) * 4 + sub) * 128 : OOB;
    int xo[7];
#pragma unroll
    for (int k = 0; k < 7; ++k) { const int n = 64 * (7 * (w & 1) + k) + lane, row = (n * 1171) >> 16, pos = n - 56 * row; xo[k] = (row + 16 * (w >> 1)) * 896 + ((pos ^ ((row >> 1) & 7)) * 16); }
    auto xdma = [&](int s, int reg, int k) { dma_one(rx, xo[k], s * 28672, c.smb + reg + (7 * w + k) * 1024); };
    const int m = sub * 4 + w;
    u4 wk[28];
    f16v d[2][2];
    {
        u4 kf[16];
        const int rowp = 16 * (l31 >> 4) + perm16(l31 & 15);
#pragma unroll
        for (int ks = 0; ks < 16; ++ks)
            kf[ks] = TYPE ? tr_cols(c, 2 + (w >> 1), ks, w & 1) : *(const u4 LAS*)(c.sm + 2 * SLICE + w * 16384 + (rowp * 512 + ((2 * (ks & 7) + hi) ^ swzr(rowp)) * 16) + (ks >> 3) * 256);
        bar();
        unsigned pf[3];
#pragma unroll
        for (int i = 0; i < 3; ++i) asm volatile("buffer_load_dword %0, %1, %2, %3 offen" : "=v"(pf[i]) : "v"(lane < 7 ? (vb >> 3) * 896 + lane * 128 : OOB), "s"(rx), "s"((4 * i + w) * 28672) : "memory");
        dma_rows(c, rw1, h, 64 * CHUNK(2), 2); dma_rows(c, rw1, h, 64 * CHUNK(3), 3); dma_rows(c, rw1, h, 64 * CHUNK(4), 4);
        const u4 rout = mksrd(TYPE ? a.WvoF : a.WqkF, 28 * 16 * 14336);
        const int t16 = 2 * m + (l31 >> 4);
        const int o_st = m > 13 ? OOB : (TYPE ? t16 * 16 + h : h * 28 + t16) * 14336 + (hi * 16 + (l31 & 15)) * 16;
        u4 p[4], pn;
        auto epi = [&](int v, int e, int st) __attribute__((always_inline)) {
            const f16v& x = d[v & 1][e & 1]; const int o = 8 * (e >> 1) + 4 * st; const float sc = TYPE ? 1.f : CSCALE;
            if (st < 2) { p[e][2 * st] = pk2(x[o] * sc, x[o + 1] * sc); p[e][2 * st + 1] = pk2(x[o + 2] * sc, x[o + 3] * sc); asm volatile("" : "+v"(p[e][2 * st]), "+v"(p[e][2 * st + 1])); }
            else if (st == 2) st128(p[e], rout, o_st, (CHUNK(v) * 2 + (e >> 1)) * 1024 + (e & 1) * 512);
            else if (st == 3) { const auto s0 = __builtin_amdgcn_permlane32_swap(p[e][0], p[e][1], false, false), s1 = __builtin_amdgcn_permlane32_swap(p[e][2], p[e][3], false, false);
                                pn = (u4){s0[0], s1[0], s0[1], s1[1]}; PIN(pn); }
            else { wk[4 * CHUNK(v) + e] = pn; asm volatile("" : "+a"(wk[4 * CHUNK(v) + e])); }
        };
#pragma unroll
        for (int u = 0; u < 7; ++u) {
            if (u == 0) waitvm<35>(); else if (u == 1) waitvm<27>(); else if (u == 2) waitvm<28>(); else if (u == 3) waitvm<20>(); else if (u == 4) waitvm<13>(); else waitvm<7>();
            if (u == 2) asm volatile("" :: "v"(pf[0]), "v"(pf[1]), "v"(pf[2]));
            bar();
            if (u >= 3) st32(tag + (u < 5 ? u - 2 : u - 1), rhb, o_fl, 0);
            pipe<32, 4>([&](int j) { return rd_rows(c, u % 5, j >> 1, j & 1); },
                        [&](int j, u4 f) { if (j < 2) mV_vv0(d[u & 1][j & 1], f, kf[j >> 1]); else mV_vv(d[u & 1][j & 1], f, kf[j >> 1]); },
                        [&](int j) { if (u > 0 && j >= 2 && j < 14) { if (j == 2) asm volatile("s_nop 3" : "+v"(d[(u - 1) & 1][0]), "+v"(d[(u - 1) & 1][1])); epi(u - 1, (j - 2) / 3, (j - 2) % 3); }
                                     if (j < 14 || j >= 30) return;
                                     const int g = (j - 14) >> 1;
                                     if (j & 1) { if (u > 0) epi(u - 1, g >> 1, 3 + (g & 1)); }
                                     else if (u == 1 || u == 2) dma_one(rw1, (g & 1) ? c.vr1 + 1024 : c.vr0, (64 * CHUNK(u + 4) + 16 * (g >> 1)) * 8192 + h * 512, c.smb + (u - 1) * SLICE + (8 * (g >> 1) + 2 * w + (g & 1)) * 1024);
                                     else if (u >= 4 && g < 7) xdma(u - 4, (u - 2) * SLICE, g); });
        }
        MFW32(d[0][0], d[0][1]);
#pragma unroll
        for (int e = 0; e < 12; ++e) epi(6, e / 3, e % 3);
#pragma unroll
        for (int e = 0; e < 8; ++e) epi(6, e >> 1, 3 + (e & 1));
    }
    const u4 rout = mksrd(TYPE ? a.UF : a.GF, GU_SZ);
    auto o_out = [&](int mm) { return mm > 13 ? OOB : h * GU_H + (TYPE ? (2 * mm + (l31 >> 4)) * 1024 : mm * 1024 + (l31 >> 4) * 14336) + (hi * 16 + (l31 & 15)) * 16; };
    const int o_st = o_out(m);
    const char LAS* xb[4];
#pragma unroll
    for (int k = 0; k < 4; ++k) xb[k] = c.sm + l31 * 896 + ((2 * k + hi) ^ ((l31 >> 1) & 7)) * 16;
    const int o_pf = (w == 1 && lane < 56) ? (vb >> 3) * 896 + lane * 16 : OOB;
    unsigned ring = 0x10432;
    auto mm1 = [&](f16v& acc, const u4& wt, u4 f, bool first) __attribute__((always_inline)) {
        if (TYPE) { if (first) mV_va0(acc, f, wt); else mV_va(acc, f, wt); } else { if (first) mV_av0(acc, wt, f); else mV_av(acc, wt, f); } };
    u4 ph;
    auto half = [&](const f16v& x, int x8, int st, int vo, int so) __attribute__((always_inline)) {
        if (st == 0) { ph = pack8(x, x8); PIN(ph); } else st128(ph, rout, vo, so + x8 * 512); };
    auto seg = [&](auto first_, auto par_, int i) __attribute__((always_inline)) {
        constexpr int FI = decltype(first_)::value, PAR = decltype(par_)::value;
        if (FI == 0) waitvm<11>(); else waitvm<8>();
        bar();
        if (FI == 0) st32(tag + 6, rhb, o_fl, 0);
        dma_one(rx, o_pf, (i + PF_AHEAD) * 28672, c.smb + 4 * SLICE + 28672 + w * 1024);
        const int r0 = (ring & 7) * SLICE, r3 = ((ring >> 12) & 7) * SLICE;
        ring = (ring >> 4) | ((ring & 7) << 16);
        pipe<28, 4>([&](int k) { return *(const u4 LAS*)(xb[k & 3] + r0 + (k >> 2) * 128); },
                    [&](int k, u4 f) { mm1(d[PAR][0], wk[k], f, k == 0); },
                    [&](int k) { if (FI != 0 && k >= 1 && k < 5) { if (k == 1) asm volatile("s_nop 5" : "+v"(d[PAR ^ 1][0])); half(d[PAR ^ 1][0], (k - 1) >> 1, (k - 1) & 1, o_st, (i - 1) * 28672); }
                                 if (k == (FI ? MID_K : MID_K - 6)) { asm volatile("s_waitcnt vmcnt(%0)\n\ts_barrier" :: "i"(FI ? MID_K / 3 - 1 : MID_K / 3 - 1) : "memory"); st32(tag + 7 + i, rhb, o_fl, 0); }
                                 if (k >= 6 && k % 3 == 0 && k < 27) xdma(i + 3, r3, k / 3 - 2); });
    };
    seg(IntC<0>{}, IntC<0>{}, 0); seg(IntC<1>{}, IntC<1>{}, 1);
#pragma nounroll
    for (int i = 2; i < NSEG1; i += 2) { seg(IntC<2>{}, IntC<0>{}, i); seg(IntC<2>{}, IntC<1>{}, i + 1); }
    asm volatile("s_nop 11" : "+v"(d[1][0]));
    sst128(pack8(d[1][0], 0), rout, o_st, (NSEG1 - 1) * 28672); sst128(pack8(d[1][0], 1), rout, o_st, (NSEG1 - 1) * 28672 + 512);
    if (sub & 1) { set_flag(c, rhb, O_FW + ((h * 2 + TYPE) * 4 + sub) * 128, tag + 7 + NSEG); return; }
    wait_prog(rhb, lane < 2 ? O_FW + ((h * 2 + TYPE) * 4 + 1 + 2 * lane) * 128 : OOB, tag + 7);
    bar();
    if (w == 0) sst32(tag + 7 + NSEG1, rhb, o_fl, 0);
    const int pfo = lane < 56 ? ((h & 3) * 4 + TYPE * 2 + (sub >> 1)) * 1792 + lane * 16 : OOB;
    if (w == 3) {
        int xp[7];
#pragma unroll
        for (int k = 0; k < 7; ++k) { const int n = 64 * k + lane, row = (n * 1171) >> 16, pos = n - 56 * row; xp[k] = row * 896 + ((pos ^ ((row >> 1) & 7)) * 16) - (k & 1) * 1024; xo[k] -= 16 * 896 + (~k & 1) * 1024; }
        auto segl = [&](auto first_, auto par_, int i) __attribute__((always_inline)) {
            constexpr int FI = decltype(first_)::value, PAR = decltype(par_)::value;
            if (FI) { waitvm<62>(); bar(); }
            const int r0 = (ring & 7) * SLICE, r3 = ((ring >> 12) & 7) * SLICE;
            ring = (ring >> 4) | ((ring & 7) << 16);
            pipe<28, 4>([&](int k) { return *(const u4 LAS*)(xb[k & 3] + r0 + (k >> 2) * 128); },
                        [&](int k, u4 f) { mm1(d[PAR][0], wk[k], f, k == 0); },
                        [&](int k) { if (k == 0) { if (FI) { asm volatile("s_nop 9" : "+v"(d[PAR ^ 1][0])); st128(pack8(d[PAR ^ 1][0], 0), rout, o_st, (i - 1) * 28672); st128(pack8(d[PAR ^ 1][0], 1), rout, o_st, (i - 1) * 28672 + 512); }
                                                   dma_pair(rx, pfo, pfo - 128, (i + PF_AHEAD) * 28672, c.smb + 4 * SLICE + 28672); }
                                     if (k & 1) { const int k0 = (k - 1) % 14, k1 = k % 14; dma_pair(rx, k0 < 7 ? xp[k0] : xo[k0 - 7], k1 < 7 ? xp[k1] : xo[k1 - 7], (i + 3) * 28672 + (k / 14) * 14336, c.smb + r3 + (k - 1) * 1024); } });
        };
        segl(IntC<0>{}, IntC<0>{}, NSEG1); segl(IntC<1>{}, IntC<1>{}, NSEG1 + 1);
#pragma nounroll
        for (int i = NSEG1 + 2; i < NSEG; i += 2) { segl(IntC<1>{}, IntC<0>{}, i); segl(IntC<1>{}, IntC<1>{}, i + 1); }
        waitvm<30>(); bar();
        asm volatile("s_nop 11" : "+v"(d[1][0]));
    } else {
        const int m2 = sub == 0 ? 4 + w : w == 2 ? 7 : 12 + w, t16 = 2 * m2 + (l31 >> 4);
        const int o_s2 = o_out(m2);
        u4 w2[28];
        {
            const u4 rin = mksrd(TYPE ? a.WvoF : a.WqkF, 28 * 16 * 14336);
            const int o_in = (TYPE ? t16 * 16 + h : h * 28 + t16) * 14336 + (hi * 16 + (l31 & 15)) * 16;
#pragma unroll
            for (int k = 0; k < 28; ++k) w2[k] = sld128(rin, o_in, k * 512);
            waitvm<0>();
#pragma unroll
            for (int k = 0; k < 28; ++k) {
                PIN(w2[k]);
                const auto s0 = __builtin_amdgcn_permlane32_swap(w2[k][0], w2[k][1], false, false), s1 = __builtin_amdgcn_permlane32_swap(w2[k][2], w2[k][3], false, false);
                w2[k] = (u4){s0[0], s1[0], s0[1], s1[1]};
                asm volatile("" : "+a"(w2[k]));
            }
        }
        u4 fr[4];
#pragma unroll
        for (int k = 0; k < 4; ++k) fr[k] = *(const u4 LAS*)(xb[k] + (ring & 7) * SLICE);
        auto seg2 = [&](auto first_, auto par_, int i) __attribute__((always_inline)) {
            constexpr int FI = decltype(first_)::value, PAR = decltype(par_)::value;
            const int r0 = (ring & 7) * SLICE, r1 = ((ring >> 4) & 7) * SLICE;
            ring = (ring >> 4) | ((ring & 7) << 16);
#pragma unroll
            for (int k = 0; k < 28; ++k) {
                mm1(d[PAR][0], wk[k], fr[k & 3], k == 0);
                if (k == 21) { asm volatile("s_waitcnt vmcnt(5)\n\ts_barrier" ::: "memory"); st32(tag + (FI ? 6 + i : 7 + NSEG1), rhb, o_fl, 0); }
                if (FI && k >= 3 && k < 11) { if (k == 3) asm volatile("" : "+v"(d[PAR ^ 1][0]), "+v"(d[PAR ^ 1][1])); half(d[PAR ^ 1][k >= 7], ((k - 3) >> 1) & 1, (k - 3) & 1, k >= 7 ? o_s2 : o_st, (i - 1) * 28672); }
                SB();
                mm1(d[PAR][1], w2[k], fr[k & 3], k == 0);
                fr[k & 3] = *(const u4 LAS*)(xb[k & 3] + (k < 24 ? r0 + ((k + 4) >> 2) * 128 : r1));
                SB();
            }
        };
        seg2(IntC<0>{}, IntC<0>{}, NSEG1); seg2(IntC<1>{}, IntC<1>{}, NSEG1 + 1);
#pragma nounroll
        for (int i = NSEG1 + 2; i < NSEG; i += 2) { seg2(IntC<1>{}, IntC<0>{}, i); seg2(IntC<1>{}, IntC<1>{}, i + 1); }
        MFW32(d[1][0], d[1][1]);
        sst128(pack8(d[1][1], 0), rout, o_s2, 63 * 28672); sst128(pack8(d[1][1], 1), rout, o_s2, 63 * 28672 + 512);
    }
    sst128(pack8(d[1][0], 0), rout, o_st, 63 * 28672); sst128(pack8(d[1][0], 1), rout, o_st, 63 * 28672 + 512);
    set_flag(c, rhb, O_FW + ((h * 2 + TYPE) * 4 + sub) * 128, tag + 7 + NSEG);
}

DI void x_image(const SideCtx& c, u4 rx, int s, int at = 40960) {
#pragma unroll
    for (int k = 0; k < 7; ++k) {
        const int n = 64 * (7 * (c.wave & 1) + k) + c.lane, row = (n * 1171) >> 16, pos = n - 56 * row;
        dma_one(rx, row * 896 + ((pos ^ ((row >> 1) & 7)) * 16), (s * 32 + 16 * (c.wave >> 1)) * 896, c.smb + at + (7 * c.wave + k) * 1024);
    }
}
DI void e_task(unsigned tag, int wave, int s, int hg) {
    CArgs& a = get_args();
    const SideCtx c = make_ctx(wave);
    const int lane = c.lane, hi = lane >> 5, l31 = lane & 31;
    s = __builtin_amdgcn_readfirstlane(s); hg = __builtin_amdgcn_readfirstlane(hg); asm volatile("" : "+s"(s), "+s"(hg));
    const int sh = s * 16 + 4 * hg + c.wave;
    const u4 rhb = mksrd(a.HB, HB_SZ), rg = mksrd(a.GF, GU_SZ);
    bar();
    x_image(c, mksrd(a.X, 2048 * 896), s);
    wait_prog(rhb, lane < 4 ? O_FW + ((4 * hg + c.wave) * 8 + lane) * 128 : OOB, tag + 8 + s);
    u4 g[28];
    const int o_g = (l31 >> 4) * 14336 + (hi * 16 + (l31 & 15)) * 16;
#pragma unroll
    for (int k = 0; k < 28; ++k) {
        asm volatile("s_nop 4\n\tbuffer_load_dwordx4 %0, %1, %2, %3 offen sc1" : "=a"(g[k]) : "v"(o_g), "s"(rg), "s"((4 * hg + c.wave) * GU_H + s * 28672 + k * 512) : "memory");
        if (k % 7 == 6) asm volatile("s_sleep %0" :: "i"(P_ESLP));
    }
    bar();
    f16v sx;
    pipe<28, 4>([&](int k) { const char LAS* b = c.sm + 40960 + l31 * 896 + hi * 8 + (k >> 2) * 128;
                             const u2 c0 = *(const u2 LAS*)(b + ((2 * (k & 3)) ^ ((l31 >> 1) & 7)) * 16), c1 = *(const u2 LAS*)(b + ((2 * (k & 3) + 1) ^ ((l31 >> 1) & 7)) * 16);
                             return (u4){c0[0], c0[1], c1[0], c1[1]}; },
                [&](int k, u4 f) { if (k == 0) WAITV(21, "+a"(g[0]), "+a"(g[1]), "+a"(g[2]), "+a"(g[3]), "+a"(g[4]), "+a"(g[5]), "+a"(g[6]));
                                   if (k == 7) WAITV(14, "+a"(g[7]), "+a"(g[8]), "+a"(g[9]), "+a"(g[10]), "+a"(g[11]), "+a"(g[12]), "+a"(g[13]));
                                   if (k == 14) WAITV(7, "+a"(g[14]), "+a"(g[15]), "+a"(g[16]), "+a"(g[17]), "+a"(g[18]), "+a"(g[19]), "+a"(g[20]));
                                   if (k == 21) WAITV(0, "+a"(g[21]), "+a"(g[22]), "+a"(g[23]), "+a"(g[24]), "+a"(g[25]), "+a"(g[26]), "+a"(g[27]));
                                   u4 y = {f[0], f[2], f[1], f[3]}; asm volatile("s_nop 1" : "+v"(y));
                                   if (k == 0) mV_av0(sx, g[k], y); else mV_av(sx, g[k], y); },
                [&](int) {});
    asm volatile("s_nop 11" : "+v"(sx));
    float m = -3.0e38f;
#pragma unroll
    for (int j = 0; j < 16; ++j) { const int key = (j & 3) + 8 * (j >> 2) + 4 * hi; if (key > l31) sx[j] = -INFINITY; m = fmaxf(m, sx[j]); }
    m = x32max(m);
    float z = 0.f;
#pragma unroll
    for (int j = 0; j < 16; ++j) { sx[j] = __builtin_amdgcn_exp2f(sx[j] - m); z += sx[j]; }
    z = x32add(z);
    const u4 rex = mksrd(a.EX, 64 * 16 * 2048), rmz = mksrd(a.MZX, 64 * 16 * 256);
    sst128(pack8(sx, 0), rex, lane * 16, sh * 2048); sst128(pack8(sx, 1), rex, lane * 16, sh * 2048 + 1024);
    sst64((u2){__float_as_uint(m), __float_as_uint(z)}, rmz, lane < 32 ? lane * 8 : OOB, sh * 256);
    set_flag(c, rhb, O_FE + (s * 4 + hg) * 4, tag + 1);
}
#define L_SLP 0
#define P_XSLP 0
DI void slp(int n) {
#pragma nounroll
    for (int i = 0; i < n; i += 8) asm volatile("s_sleep 8");
}
DI void x_task(unsigned tag, int vb, int wave, int s, int cb) {
    CArgs& a = get_args();
    const SideCtx c = make_ctx(wave);
    const int lane = c.lane, hi = lane >> 5, l31 = lane & 31, w = c.wave;
    s = __builtin_amdgcn_readfirstlane(s); cb = __builtin_amdgcn_readfirstlane(cb); asm volatile("" : "+s"(s), "+s"(cb));
    const u4 rhb = mksrd(a.HB, HB_SZ);
    const u4 ru = mksrd(a.UF, GU_SZ), rex = mksrd(a.EX, 64 * 16 * 2048), rmz = mksrd(a.MZX, 64 * 16 * 256);
    const u4 rum = mksrd(a.UM, 28 * TU_P), rtf = mksrd(a.TF, 16 * TU_P), rx = mksrd(a.X, 2048 * 896);
    bar();
    x_image(c, rx, s);
    wait_prog(rhb, lane < 16 ? O_FW + (((4 * w + (lane >> 2)) * 2 + 1) * 4 + (lane & 3)) * 128 : lane == 16 ? O_FE + (s * 4 + w) * 4 : OOB, lane < 16 ? tag + 8 + s : tag + 1);
    u4 uf[4][2][2], ex[4][2]; u2 mz[4];
    const int o_u = (l31 >> 4) * 1024 + hi * 256 + (l31 & 15) * 16;
#pragma unroll
    for (int k = 0; k < 4; ++k) {
        const int sh = s * 16 + 4 * w + k;
#pragma unroll
        for (int sg = 0; sg < 2; ++sg) {
            ex[k][sg] = sld128(rex, lane * 16, sh * 2048 + sg * 1024);
#pragma unroll
            for (int nt = 0; nt < 2; ++nt) uf[k][sg][nt] = sld128(ru, o_u, (4 * w + k) * GU_H + (s * 28 + 4 * cb + 2 * nt) * 1024 + sg * 512);
        }
        mz[k] = sld64(rmz, l31 * 8, sh * 256);
        asm volatile("s_sleep %0" :: "i"(P_XSLP));
    }
    waitvm<0>();
    f16v rr[4][2];
#pragma unroll
    for (int k = 0; k < 4; ++k) {
        asm volatile("" : "+v"(mz[k]), "+v"(ex[k][0]), "+v"(ex[k][1]), "+v"(uf[k][0][0]), "+v"(uf[k][0][1]), "+v"(uf[k][1][0]), "+v"(uf[k][1][1]));
        mV_vv0(rr[k][0], uf[k][0][0], ex[k][0]); mV_vv0(rr[k][1], uf[k][0][1], ex[k][0]);
        mV_vv(rr[k][0], uf[k][1][0], ex[k][1]); mV_vv(rr[k][1], uf[k][1][1], ex[k][1]);
        MFW32(rr[k][0], rr[k][1]);
    }
    bar();
    const bool last = s >= 62;
    wait_prog(rhb, lane < 4 ? O_FT + (32 + 4 * cb + lane) * 128 : lane < 12 ? O_FT + (8 * w + lane - 4) * 128 : OOB, tag + (lane < 4 || last ? min(s, 58) : s) + 1);
    u4 um[2][2];
    const int o_um = (l31 >> 4) * TU_P + hi * 256 + (l31 & 15) * 16;
    auto mx4 = [](unsigned m, u4 x) { return max(max(max(m, x[0]), max(x[1], x[2])), x[3]); };
    auto umld = [&]() __attribute__((always_inline)) {
#pragma unroll
        for (int k = 0; k < 4; ++k) um[k >> 1][k & 1] = sld128(rum, o_um, (4 * cb + 2 * (k & 1)) * TU_P + (s + 1) * 4096 + w * 1024 + (k >> 1) * 512);
    };
    auto umwait = [&]() __attribute__((always_inline)) {
#pragma nounroll
        for (unsigned sp = 0; !__all(mx4(mx4(mx4(mx4(0u, um[0][0]), um[0][1]), um[1][0]), um[1][1]) != FF); ++sp) {
            umld(); WAITV(0, "+v"(um[0][0]), "+v"(um[0][1]), "+v"(um[1][0]), "+v"(um[1][1]));
            if (sp > SPIN_MAX) __builtin_amdgcn_endpgm();
        }
    };
    f16v sm_;
    if (last) {
        um[0][0] = um[0][1] = um[1][0] = um[1][1] = (u4){FF, FF, FF, FF};
        umwait();
        asm volatile("s_sleep %0" :: "i"(L_SLP));
        u4 p[8];
#pragma nounroll
        for (unsigned sp = 0;; ++sp) {
#pragma unroll
            for (int k = 0; k < 8; ++k) p[k] = sld128(rtf, l31 * 32 + hi * 16, (4 * w + (k >> 1)) * TU_P + TU_S + (s - 62) * 2048 + (k & 1) * 1024);
            WAITV(0, "+v"(p[0]), "+v"(p[1]), "+v"(p[2]), "+v"(p[3]), "+v"(p[4]), "+v"(p[5]), "+v"(p[6]), "+v"(p[7]));
            unsigned m = 0;
#pragma unroll
            for (int k = 0; k < 8; ++k) m = mx4(m, p[k]);
            if (__all(m != FF)) break;
            if (sp > SPIN_MAX) __builtin_amdgcn_endpgm();
        }
#pragma unroll
        for (int k = 0; k < 16; ++k) sm_[k] = __uint_as_float(p[k >> 2 << 1][k & 3]) + __uint_as_float(p[(k >> 2 << 1) + 1][k & 3]);
    } else {
        u4 tf[28];
        const int o_tf = (l31 >> 3) * TU_P + hi * 128 + (l31 & 7) * 16;
#pragma unroll
        for (int k = 0; k < 28; k += 4)
            asm volatile("s_nop 4\n\tbuffer_load_dwordx4 %0, %4, %5, %6 offen sc1\n\tbuffer_load_dwordx4 %1, %4, %5, %6 offen offset:256 sc1\n\tbuffer_load_dwordx4 %2, %4, %5, %6 offen offset:512 sc1\n\tbuffer_load_dwordx4 %3, %4, %5, %6 offen offset:768 sc1"
                         : "=&a"(tf[k]), "=&a"(tf[k + 1]), "=&a"(tf[k + 2]), "=&a"(tf[k + 3]) : "v"(o_tf), "s"(rtf), "s"(4 * w * TU_P + (s + 1) * 7168 + k * 256) : "memory");
        umld();
        pipe<28, 4>([&](int k) { return *(const u4 LAS*)(c.sm + 40960 + l31 * 896 + (((2 * (k & 3) + hi) ^ ((l31 >> 1) & 7)) * 16) + (k >> 2) * 128); },
                    [&](int k, u4 f) { if (k == 0) WAITV(25, "+a"(tf[0]), "+a"(tf[1]), "+a"(tf[2]), "+a"(tf[3]), "+a"(tf[4]), "+a"(tf[5]), "+a"(tf[6]));
                                       if (k == 7) WAITV(18, "+a"(tf[7]), "+a"(tf[8]), "+a"(tf[9]), "+a"(tf[10]), "+a"(tf[11]), "+a"(tf[12]), "+a"(tf[13]));
                                       if (k == 14) WAITV(11, "+a"(tf[14]), "+a"(tf[15]), "+a"(tf[16]), "+a"(tf[17]), "+a"(tf[18]), "+a"(tf[19]), "+a"(tf[20]));
                                       if (k == 21) WAITV(4, "+a"(tf[21]), "+a"(tf[22]), "+a"(tf[23]), "+a"(tf[24]), "+a"(tf[25]), "+a"(tf[26]), "+a"(tf[27]));
                                       if (k == 0) mV_av0(sm_, tf[k], f); else mV_av(sm_, tf[k], f); },
                    [&](int) {});
        asm volatile("s_waitcnt vmcnt(0)\n\ts_nop 10" : "+v"(sm_), "+v"(um[0][0]), "+v"(um[0][1]), "+v"(um[1][0]), "+v"(um[1][1]) :: "memory");
        umwait();
    }
    u2 pm[4]; float scx[4];
#pragma unroll
    for (int k = 0; k < 4; ++k) {
        const f4 sc = {sm_[4 * k], sm_[4 * k + 1], sm_[4 * k + 2], sm_[4 * k + 3]};
        const float mx = __uint_as_float(mz[k][0]);
        float m = fmaxf(x32max(fmaxf(fmaxf(sc[0], sc[1]), fmaxf(sc[2], sc[3]))), mx);
        float e[4];
#pragma unroll
        for (int j = 0; j < 4; ++j) e[j] = __builtin_amdgcn_exp2f(sc[j] - m);
        const float cx = __builtin_amdgcn_exp2f(mx - m);
        const float z = x32add((e[0] + e[1]) + (e[2] + e[3])) + cx * __uint_as_float(mz[k][1]);
        const float inv = __builtin_amdgcn_rcpf(z);
        scx[k] = cx * inv;
        pm[k] = (u2){pk2(e[0] * inv, e[1] * inv), pk2(e[2] * inv, e[3] * inv)};
    }
    f16v d0, d1;
    {
        u4 p0 = {pm[0][0], pm[0][1], pm[2][0], pm[2][1]}, p1 = {pm[1][0], pm[1][1], pm[3][0], pm[3][1]};
        asm volatile("s_nop 1\n\tv_mfma_f32_32x32x16_bf16 %0, %2, %6, 0\n\tv_mfma_f32_32x32x16_bf16 %1, %3, %6, 0\n\tv_mfma_f32_32x32x16_bf16 %0, %4, %7, %0\n\tv_mfma_f32_32x32x16_bf16 %1, %5, %7, %1"
                     : "=&v"(d0), "=&v"(d1) : "v"(um[0][0]), "v"(um[0][1]), "v"(um[1][0]), "v"(um[1][1]), "v"(p0), "v"(p1));
    }
    f16v a0 = rr[0][0] * scx[0] + (rr[1][0] * scx[1] + (rr[2][0] * scx[2] + rr[3][0] * scx[3]));
    asm volatile("s_nop 11" : "+v"(d0), "+v"(d1), "+v"(a0));
    a0 += d0;
#pragma unroll
    for (int g = 0; g < 4; ++g) *(f4 LAS*)(c.sm + w * 8704 + l31 * 272 + (8 * g + 4 * hi) * 4) = (f4){a0[4 * g], a0[4 * g + 1], a0[4 * g + 2], a0[4 * g + 3]};
    const f16v a1 = rr[0][1] * scx[0] + (rr[1][1] * scx[1] + (rr[2][1] * scx[2] + (rr[3][1] * scx[3] + d1)));
#pragma unroll
    for (int g = 0; g < 4; ++g) *(f4 LAS*)(c.sm + w * 8704 + l31 * 272 + 128 + (8 * g + 4 * hi) * 4) = (f4){a1[4 * g], a1[4 * g + 1], a1[4 * g + 2], a1[4 * g + 3]};
    bar();
    {
        const int t = w * 8 + (lane >> 3), c8 = lane & 7;
        f4 s0 = {0.f, 0.f, 0.f, 0.f}, s1 = s0;
#pragma unroll
        for (int p = 0; p < 4; ++p) { s0 += *(const f4 LAS*)(c.sm + p * 8704 + t * 272 + c8 * 32); s1 += *(const f4 LAS*)(c.sm + p * 8704 + t * 272 + c8 * 32 + 16); }
        sst128((u4){pk2(s0[0], s0[1]), pk2(s0[2], s0[3]), pk2(s1[0], s1[1]), pk2(s1[2], s1[3])}, mksrd(a.out, 2048 * 1792), t * 1792 + c8 * 16, s * 32 * 1792 + 896 + cb * 128);
    }
}

#define PA_LAG 300
#define P_ASLP 8
#define PA_UT 0
#define PA_TU 67584
#define PA_KH (PA_TU + 35456)
#define PA_KT (PA_KH + 7680)
#define PA_RED (PA_KT + 12288)
#define PA_SC (PA_RED + 36864)
typedef __attribute__((ext_vector_type(2))) _Float16 h2;
typedef __attribute__((ext_vector_type(8))) _Float16 h8;
DI unsigned pkh(f2 v) { return __builtin_bit_cast(unsigned, __builtin_convertvector(v, h2)); }
DI void mH(f16v& c, u4 x, u4 y) { asm volatile("v_mfma_f32_32x32x16_f16 %0, %1, %2, %0" : "+v"(c) : "v"(x), "v"(y)); }
DI void mH0(f16v& c, u4 x, u4 y) { asm volatile("v_mfma_f32_32x32x16_f16 %0, %1, %2, 0" : "=&v"(c) : "v"(x), "v"(y)); }
DI unsigned fold(unsigned m, unsigned x) { return max(max(m, x & 0x7f80u), (x >> 16) & 0x7f80u); }
DI int top_exp(unsigned m) {
#pragma unroll
    for (int sft = 32; sft >= 1; sft >>= 1) m = max(m, (unsigned)__shfl_xor(m, sft));
    return (int)(m >> 7) - 127;
}
DI float bf2f(u16 x) { return __uint_as_float((unsigned)x << 16); }
DI void parta_task(unsigned tag, int vb, int wave, int task, int nt) {
    CArgs& a = get_args();
    const SideCtx c = make_ctx(wave);
    char LAS* sm = c.sm;
    const int lane = c.lane, w = c.wave, tid = w * 64 + lane, hi = lane >> 5, l31 = lane & 31, l15 = lane & 15, kk = lane >> 4;
    task = __builtin_amdgcn_readfirstlane(task); asm volatile("" : "+s"(task));
    int d0 = task * 4;
    char LAS* ut = sm + PA_UT;
    const u4 rpa = mksrd(a.PA, 56 * 32768);
    bar();
    u2 pr[4][4], dsks[4]; unsigned ldts[4];
#pragma unroll
    for (int g = 0; g < 4; ++g) {
        const int d = d0 + (g < nt ? 4 * g : 0);
        ldts[g] = a.Alogdt[d + w]; dsks[g] = *(const u2*)(a.ADsk + d);
        const int n = (d + w) * 256 + 4 * lane;
        pr[g][0] = *(const u2*)(a.AlogA + n); pr[g][1] = *(const u2*)(a.Aim + n); pr[g][2] = *(const u2*)(a.ACr + n); pr[g][3] = *(const u2*)(a.ACi + n);
    }
    SB();
    for (int i = tid; i < 1024; i += 256) { const int ch = i >> 6, m = i & 63; *(u16 LAS*)(ut + ch * 4224 + (m == 0 ? 0 : (2048 + m) * 2)) = 0; }
    if (tid < 155) *(u4 LAS*)(sm + PA_KH + tid * 16) = (u4){0u, 0u, 0u, 0u};
    if (tid < 48) *(u4 LAS*)(sm + PA_TU + ((tid / 6) * 276 + (tid >= 6) + 256 + tid % 6) * 16) = (u4){0u, 0u, 0u, 0u};
    int toff[8];
#pragma unroll
    for (int j = 0; j < 8; ++j) {
        const int p = (int)bfr((float)(tid * 8 + j));
        int col, ar = (p & 255) >> 4;
        if (p < 256) col = p & 15; else if (p < 512) col = 16 + ((p & 15) >> 1); else if (p < 1024) col = 24 + ((p - 512) >> 8) * 4 + ((p & 15) >> 2);
        else if (p < 2048) col = 32 + ((p - 1024) >> 8) * 2 + ((p & 15) >> 3); else col = 40;
        toff[j] = (col * 16 + ar) * 4;
    }
    float qpos[3];
    qpos[0] = (float)l15;
    qpos[1] = l15 < 8 ? (float)(256 + 2 * l15) : (float)(512 + ((l15 - 8) >> 2) * 256 + (l15 & 3) * 4);
    qpos[2] = l15 < 8 ? (float)(1024 + (l15 >> 1) * 256 + (l15 & 1) * 8) : 2048.f;
    const float ppos = (float)(16 * l15);
#pragma nounroll
    for (int rnd = 0; rnd < nt; ++rnd) {
        const bool nld = rnd == 0; const u2 dsk4 = dsks[0];
        const float dt = bfr(expf(bf2f(ldts[0])));
        const int ec = top_exp(fold(fold(fold(fold(0u, pr[0][2][0]), pr[0][2][1]), pr[0][3][0]), pr[0][3][1]));
        {
            char LAS* pt = sm + PA_RED + w * 9216;
#pragma unroll
            for (int i = 0; i < 4; ++i) {
                auto el = [&](int k) { const unsigned x = pr[0][k][i >> 1]; return __uint_as_float((i & 1) ? x & 0xffff0000u : x << 16); };
                const float ar = -bfr(expf(el(0)));
                *(f4 LAS*)(pt + (lane + 64 * i) * 16) = (f4){ar * dt * LOG2E, el(1) * dt * 0.15915494309189535f, ldexpf(el(2), 3 - ec), ldexpf(el(3), 3 - ec)};
            }
            f4 acc[3] = {{0.f, 0.f, 0.f, 0.f}, {0.f, 0.f, 0.f, 0.f}, {0.f, 0.f, 0.f, 0.f}};
            auto cexp = [](f2 z, float p) __attribute__((always_inline)) {
                const f2 x = z * p + (f2){10.f, 0.f}; const float ex = __builtin_amdgcn_exp2f(x[0]), rv = __builtin_amdgcn_fractf(x[1]);
                return (f2){__builtin_amdgcn_cosf(rv), __builtin_amdgcn_sinf(rv)} * ex; };
            f4 zc[4];
#pragma unroll
            for (int j = 0; j < 4; ++j) zc[j] = *(const f4 LAS*)(pt + (4 * j + kk) * 16);
            const __amdgpu_buffer_rsrc_t ru = __builtin_amdgcn_make_buffer_rsrc((void*)a.Au, 0, 2048 * 896, 0x00020000);
#pragma nounroll
            for (int s2 = 0; s2 < 8; ++s2) {
                u4 uv[2];
                if (nld) { uv[0] = __builtin_amdgcn_raw_buffer_load_b128(ru, tid * 896, s2 * 256 * 896 + d0 * 2, 0); uv[1] = __builtin_amdgcn_raw_buffer_load_b128(ru, tid * 896 + 16, s2 * 256 * 896 + d0 * 2, 0); }
#pragma unroll
                for (int hf = 0; hf < 2; ++hf) {
                    u4 fa, fb; f2 zz[4];
#pragma unroll
                    for (int j = 0; j < 4; ++j) {
                        const f4 z = zc[j]; zz[j] = (f2){z[0], z[1]};
                        const f2 e = cexp(zz[j], ppos);
                        fa[j] = pkh((f2){z[2] * e[0] - z[3] * e[1], -(z[2] * e[1] + z[3] * e[0])});
                        zc[j] = *(const f4 LAS*)(pt + (32 * s2 + 16 * hf + 16 + 4 * j + kk) * 16);
                    }
#pragma unroll
                    for (int t = 0; t < 3; ++t) {
#pragma unroll
                        for (int j = 0; j < 4; ++j) fb[j] = pkh(cexp(zz[j], qpos[t]));
                        acc[t] = __builtin_amdgcn_mfma_f32_16x16x32_f16(__builtin_bit_cast(h8, fa), __builtin_bit_cast(h8, fb), acc[t], 0, 0, 0);
                        asm volatile("" : "+v"(acc[t]));
                    }
                }
                if (nld) {
#pragma unroll
                    for (int ch = 0; ch < 16; ++ch) *(u16 LAS*)(ut + ch * 4224 + (2048 - tid - 256 * s2) * 2) = (u16)(uv[ch >> 3][(ch >> 1) & 3] >> (16 * (ch & 1)));
                }
            }
#pragma unroll
            for (int t = 0; t < 3; ++t) *(f4 LAS*)(sm + PA_KT + w * 3072 + ((t * 16 + l15) * 16 + 4 * kk) * 4) = acc[t] * 0x1p-18f;
        }
        if (nld && nt == 3) slp(PA_LAG);
        bar();
        {
            unsigned m = 0;
#pragma unroll
            for (int i = 0; i < 5; ++i) { const u4 x = *(const u4 LAS*)(ut + w * 4224 + min(lane + 64 * i, 256) * 16); m = fold(fold(fold(fold(m, x[0]), x[1]), x[2]), x[3]); }
            const int eu = top_exp(m);
            if (lane == 0) *(u4 LAS*)(sm + PA_SC + w * 16) = (u4){__float_as_uint(2.f * dt), (unsigned)(ec - 5 + eu - 14), (unsigned)(14 - eu), 0u};
        }
        bar();
        auto emit = [&](int ch) __attribute__((always_inline)) {
            f4 s0 = {0.f, 0.f, 0.f, 0.f}, s1 = s0;
#pragma unroll
            for (int p = 0; p < 4; ++p) { const char LAS* q_ = sm + PA_RED + p * 9216 + (tid >> 2) * 144 + (tid & 3) * 32; s0 += *(const f4 LAS*)q_; s1 += *(const f4 LAS*)(q_ + 16); }
            const u4 x0 = *(const u4 LAS*)(ut + ch * 4224 + (255 - tid) * 16); const unsigned x1 = *(const unsigned LAS*)(ut + ch * 4224 + (256 - tid) * 16);
            const float dsk = __uint_as_float((ch & 2 ? dsk4[1] : dsk4[0]) >> (16 * (ch & 1)) << 16);
            const u2 sc = *(const u2 LAS*)(sm + PA_SC + ch * 16);
            float yv[8];
#pragma unroll
            for (int j = 0; j < 8; ++j) { const unsigned uw = j == 0 ? x1 : x0[(8 - j) >> 1]; yv[j] = bfr(ldexpf((j < 4 ? s0[j & 3] : s1[j & 3]) * __uint_as_float(sc[0]), (int)sc[1])) + bfr(__uint_as_float((j & 1) ? uw & 0xffff0000u : uw << 16) * dsk); }
            sst128((u4){pk2(yv[0], yv[1]), pk2(yv[2], yv[3]), pk2(yv[4], yv[5]), pk2(yv[6], yv[7])}, rpa, tid * 16, (d0 + ch) * 4096);
        };
#pragma nounroll
        for (int cc = 0; cc < 4; ++cc) {
            const int ch = cc;
            {
                float kv[8];
#pragma unroll
                for (int j = 0; j < 8; ++j) kv[j] = *(const float LAS*)(sm + PA_KT + cc * 3072 + toff[j]);
                *(u4 LAS*)(sm + PA_KH + (31 + (tid >> 2)) * 80 + (tid & 3) * 16) = (u4){pkh((f2){kv[0], kv[1]}), pkh((f2){kv[2], kv[3]}), pkh((f2){kv[4], kv[5]}), pkh((f2){kv[6], kv[7]})};
            }
            {
                const int i = tid;
                const u4 x0 = *(const u4 LAS*)(ut + ch * 4224 + i * 16), x1 = *(const u4 LAS*)(ut + ch * 4224 + i * 16 + 16);
                const int su = *(const int LAS*)(sm + PA_SC + ch * 16 + 8);
                unsigned r[8] = {x0[0], x0[1], x0[2], x0[3], x1[0], x1[1], x1[2], x1[3]};
#pragma unroll
                for (int j = 0; j < 8; ++j) r[j] = pkh((f2){ldexpf(__uint_as_float(r[j] << 16), su), ldexpf(__uint_as_float(r[j] & 0xffff0000u), su)});
#pragma unroll
                for (int cp = 0; cp < 8; ++cp) {
                    u4 o;
#pragma unroll
                    for (int j = 0; j < 4; ++j) o[j] = (cp & 1) ? __builtin_amdgcn_alignbit(r[(cp >> 1) + j + 1], r[(cp >> 1) + j], 16) : r[(cp >> 1) + j];
                    *(u4 LAS*)(sm + PA_TU + (cp * 276 + (cp ? 1 : 0) + i) * 16) = o;
                }
                if (tid == 255) *(u4 LAS*)(sm + PA_TU + 256 * 16) = (u4){r[4], r[5], r[6], r[7]};
            }
            if (cc) emit(cc - 1);
            bar();
            {
                f16v y0, y1;
                const int m00 = 2048 - l31 + 8 * hi, cp = m00 & 7;
                const char LAS* ab = sm + PA_TU + (cp * 276 + (cp ? 1 : 0) + (m00 >> 3)) * 16 - 63 * 64;
                const char LAS* bb = sm + PA_KH + l31 * 80 + hi * 16;
                const char LAS* a1w = ab - w * 64; const char LAS* b1w = bb - w * 80;
                const char LAS* a2w = ab + w * 64; const char LAS* b2w = bb + w * 80;
                u4 fr[3][3];
                auto rdg = [&](int g, int sl) __attribute__((always_inline)) {
                    const int i = (g & 15) >> 1, ks = g & 1;
                    if (g < 16) { fr[sl][0] = *(const u4 LAS*)(b1w + (31 - 4 * i) * 80 + ks * 32); fr[sl][1] = *(const u4 LAS*)(a1w + (63 - 4 * i) * 64 + ks * 32); fr[sl][2] = *(const u4 LAS*)(a1w + (31 - 4 * i) * 64 + ks * 32); }
                    else { fr[sl][0] = *(const u4 LAS*)(b2w + (32 + 4 * i) * 80 + ks * 32); fr[sl][2] = *(const u4 LAS*)(a2w + (32 + 4 * i) * 64 + ks * 32); }
                };
                rdg(0, 0); rdg(1, 1); SB();
#pragma unroll
                for (int g = 0; g < 32; ++g) {
                    const int sl = g % 3;
                    if (g + 2 < 32) rdg(g + 2, (g + 2) % 3);
                    SB();
                    if (g == 0) { mH0(y0, fr[sl][1], fr[sl][0]); mH0(y1, fr[sl][2], fr[sl][0]); }
                    else { if (g < 16) mH(y0, fr[sl][1], fr[sl][0]); mH(y1, fr[sl][2], fr[sl][0]); }
                    SB();
                }
                MFW32(y0, y1);
#pragma unroll
                for (int g = 0; g < 4; ++g) {
                    *(f4 LAS*)(sm + PA_RED + w * 9216 + l31 * 144 + (8 * g + 4 * hi) * 4) = (f4){y0[4 * g], y0[4 * g + 1], y0[4 * g + 2], y0[4 * g + 3]};
                    *(f4 LAS*)(sm + PA_RED + w * 9216 + (32 + l31) * 144 + (8 * g + 4 * hi) * 4) = (f4){y1[4 * g], y1[4 * g + 1], y1[4 * g + 2], y1[4 * g + 3]};
                }
            }
            bar();
        }
        emit(3);
        bar();
        d0 += 4; ut += 16896;
#pragma unroll
        for (int g = 0; g < 3; ++g) {
            ldts[g] = ldts[g + 1]; dsks[g] = dsks[g + 1];
#pragma unroll
            for (int k = 0; k < 4; ++k) pr[g][k] = pr[g + 1][k];
        }
    }
    waitvm<0>(); bar();
    if (w == 0) sst32(tag + 1, mksrd(a.HB, HB_SZ), lane < nt ? O_FPA + (task + lane) * 4 : OOB, 0);
}
DI void pa_out(unsigned tag, int wave, int t) {
    CArgs& a = get_args();
    const SideCtx c = make_ctx(wave);
    const int lane = c.lane, tid = c.wave * 64 + lane;
    t = __builtin_amdgcn_readfirstlane(t); asm volatile("" : "+s"(t));
    const u4 rpa = mksrd(a.PA, 56 * 32768), ro = mksrd(a.out, 2048 * 1792);
    wait_prog(mksrd(a.HB, HB_SZ), lane < 16 ? O_FPA + (t & ~7) * 8 + lane * 4 : OOB, tag + 1);
    u4 v[8];
#pragma unroll
    for (int i = 0; i < 8; ++i) v[i] = sld128(rpa, (tid >> 5) * 4096 + (tid & 31) * 16, ((t >> 3) * 64 + i * 8) * 4096 + (t & 7) * 512);
    WAITV(0, "+v"(v[0]), "+v"(v[1]), "+v"(v[2]), "+v"(v[3]), "+v"(v[4]), "+v"(v[5]), "+v"(v[6]), "+v"(v[7]));
    bar();
#pragma unroll
    for (int i = 0; i < 8; ++i) *(u4 LAS*)(c.sm + (i * 8 + (tid >> 5)) * 528 + (tid & 31) * 16) = v[i];
    bar();
#pragma unroll
    for (int k = 0; k < 8; ++k) {
        const char LAS* p = c.sm + (tid & 7) * 8 * 528 + ((tid >> 3) + 32 * k) * 2;
        u4 o;
#pragma unroll
        for (int j = 0; j < 4; ++j) o[j] = (unsigned)*(const u16 LAS*)(p + (2 * j) * 528) | ((unsigned)*(const u16 LAS*)(p + (2 * j + 1) * 528) << 16);
        sst128(o, ro, (tid >> 3) * 1792 + (tid & 7) * 16, ((t & 7) * 256 + 32 * k) * 1792 + (t >> 3) * 128);
        asm volatile("s_sleep %0" :: "i"(P_ASLP));
    }
}
#define PROBE_N 3
#define L_TAB 0
#define L_CLK 32768
#define L_MEM 33792
#define L_WRK 49152
#define L_XIM 65536
DI void edge(unsigned& ce, unsigned& ne) {
    const unsigned n0 = rt_now(); u64 t, c; int n = 0;
#pragma nounroll
    do { asm volatile("s_memrealtime %0\n\ts_memtime %1\n\ts_waitcnt lgkmcnt(0)" : "=&s"(t), "=&s"(c) :: "memory"); } while ((unsigned)t == n0 && ++n < 64);
    ce = (unsigned)c - 30u; ne = (unsigned)t;
}
DI unsigned srdw(const void* p, unsigned bytes, int i) { const u64 x = (u64)p; return i == 0 ? (unsigned)x : i == 1 ? (unsigned)(x >> 32) & 0xffffu : i == 2 ? bytes : 0x00020000u; }
#define RUN(r) asm volatile("v_mov_b32 v255, %0\n\ts_getpc_b64 vcc\n.Lfk_from_" #r ":\n\ts_add_u32 vcc_lo, vcc_lo, fk_role_" #r "-.Lfk_from_" #r "\n\ts_addc_u32 vcc_hi, vcc_hi, 0\n\ts_setpc_b64 vcc" :: "v"(tab) : "memory", "vcc", "scc", "s99", "v255", "a255")
DI unsigned probe(u4 r, int id, int xh) {
    const int lane = fresh_lane(), o = xh * (2 << 20) + O_PROBE + id * 128; unsigned best = 1016;
#pragma nounroll
    for (int rep = 0; rep < PROBE_N; ++rep) {
        sst32(rep, r, lane < 4 ? o + lane * 8192 : OOB, 0); waitvm<0>();
#pragma nounroll
        for (int k = 0; k < 4; ++k) {
            const int kk = (k + id) & 3, ol = lane == 0 ? o + kk * 8192 : OOB;
            const unsigned t0 = now32(); unsigned v = sld32(r, ol, 0); WAITV(0, "+v"(v));
            const unsigned d = now32() - t0;
            if (lane == kk) best = min(best, d);
        }
    }
    return best;
}
DI void ring_init(unsigned tag, int W, int b, unsigned best) {
    CArgs& a = get_args();
    const int lane = fresh_lane();
    const u4 r = mksrd(a.HB, HB_SZ);
    sst32(tag + 1 + (best >> 2), r, lane < 4 ? O_PAR + (b * 4 + W) * 16 + lane * 4 : OOB, 0);
    u4 m0[2];
#pragma unroll
    for (int k = 0; k < 2; ++k) {
        m0[k] = *(const u4*)(a.mem0 + (lane & 7) * 448 + min(2 * W + k, 6) * 64 + (lane >> 3) * 8);
        m0[k] = (b & 1) ? (u4){FF, FF, FF, FF} : (u4){san(m0[k][0]), san(m0[k][1]), san(m0[k][2]), san(m0[k][3])};
    }
    if (b == 0)
        for (int k = 0; k < 3; ++k) sst128((u4){REC_A, REC_B, REC_A, REC_B} + (W + k + lane == 0 ? REC_0 : 0u), r, O_REC + lane * 16, (W * 3 + k) * 1024);
    wait_prog(r, O_PAR + lane * 4, tag + 1);
    unsigned la = sld32(r, O_PAR + lane * 4, 0); WAITV(0, "+v"(la));
    la = ((la - tag) << 2) | (lane & 3);
#pragma unroll
    for (int sft = 16; sft >= 1; sft >>= 1) la = min(la, (unsigned)__shfl_xor(la, sft));
    const unsigned pa = __builtin_amdgcn_readlane(la, 0) & 3, pb = __builtin_amdgcn_readlane(la, 32) & 3;
    const int base = (b >> 1) * (2 << 20) + (b & 1) * 524288 + ((((b & 2) ? pb : pa) ^ ((b & 1) * 2)) << 13) + W * 2048;
    if (b == 0 && W == 0) sst32(tag + 1 + pa + 4 * pb, r, lane == 0 ? O_WIN : OOB, 0);
#pragma nounroll
    for (int n = 0; n < 6; ++n) {
        const int o = base + (n < 4 ? n * 32768 : 16384 + (n - 4) * 32768);
#pragma unroll
        for (int k = 0; k < 2; ++k) { sst128((n & 2) ? (u4){FF, FF, FF, FF} : m0[k], r, lane * 16, o + k * 1024); sst128((u4){FF, FF, FF, FF}, r, lane * 16, o + k * 1024 + (1 << 20)); }
    }
    waitvm<0>(); bar();
    if (W == 0) sst32(tag + 1 + pa + 4 * pb, r, lane == 0 ? O_RDY + b * 128 : OOB, 0);
}
DI void chain_role(unsigned tag, int vb, int W, int role, int id, int xh) {
    CArgs& a = get_args();
    char LAS* sm = (char LAS*)smem_;
    const unsigned smb = (unsigned)(size_t)sm;
    const int lane = fresh_lane(), q = lane >> 4, l15 = lane & 15, i8 = lane & 7, hb = (lane >> 3) & 1;
    unsigned LAS* tb = (unsigned LAS*)(sm + L_TAB + W * 8192) + lane;
    const int wave = W;
    unsigned c1, n1;
    const int cp = xh * 32768;
    const void *p1, *p2 = a.UF; unsigned x1 = 0, x2 = 0, po = 0; int fw, wk;
    if (role == 0) {
        const int part = id & 1, h = id >> 1, rr = l15 >> 2, c = lane & 3;
        const int bq = q == 0 ? 0 : q == 1 ? 528 : q == 2 ? 1152 : 1680;
        tb[8 * 64] = O_MEM + cp + lane * 16 + part * 4096; tb[9 * 64] = O_MEM + cp + lane * 16 + (1 - part) * 4096; tb[10 * 64] = O_MEM + cp + lane * 16 + 3072;
        tb[11 * 64] = smb + L_WRK + bq + perm16(l15) * 8 + (W == 3 ? 3 * 2304 + part * 256 : 0); tb[14 * 64] = smb + L_WRK + bq + perm16(l15) * 8 + 3 * 2304 + (1 - part) * 256;
        tb[12 * 64] = smb + L_WRK + (c == 0 ? 0 : c == 2 ? 528 : c == 1 ? 1152 : 1680) + (16 * (q >> 1) + 4 * (q & 1) + 8 * (rr >> 1) + (rr & 1)) * 8;
        const int op = O_SMM + id * 256 + hb * 128 + (4 * (q & 1) + 2 * (q >> 1)) * 16 + i8 * 4;
        tb[13 * 64] = op + cp; tb[15 * 64] = op + (cp ^ 32768);
        int tile, chunk;
        if (W < 3) {
            const int i = lane < 28 ? lane : 0;
            const int pk = (int)((i < 12 ? SEQ_LO : i < 24 ? SEQ_MI : SEQ_HI) >> (5 * (i % 12))), p = pk & 7, k = (pk >> 3) & 3;
            const int n = 3 * (p & 3) + W;
            tile = 4 * (4 * (p < 4 ? part : 1 - part) + (n >> 2)) + (n & 3); chunk = k < 3 ? 4 * part + (W + k) % 3 : 3;
        } else {
            const int p = lane < 2 ? 2 + lane : (lane - 2) & 3, k = lane < 2 || lane >= 14 ? 3 : (lane - 2) >> 2;
            tile = 12 + (p < 2 ? 2 * part + p : 2 * (1 - part) + p - 2); chunk = k < 3 ? 4 * part + k : 3;
        }
        wk = ((h * 28 + tile) * 7 + chunk) * 2048; fw = h * 8 + (lane & 3);
        p1 = a.WqkF; x1 = part + 1;
    } else if (role == 1) {
        const int hc = 4 * W + ((q & 1) << 1) + (q >> 1), k = i8 - 4 * hb;
        const int ocp = W == 0 ? O_MEM : W == 3 ? O_MEM + 32768 : O_MEMX + (W - 1) * 32768;
        tb[8 * 64] = O_MEM + cp + lane * 16; tb[9 * 64] = O_MEM + cp + lane * 16 + 4096;
        tb[10 * 64] = O_SMM + cp + hc * 512 + l15 * 16; tb[11 * 64] = O_MZ + hc * 128 + l15 * 8;
        tb[12 * 64] = RXW(id >> 1) + (id & 1) * 4096 + hc * 256 + l15 * 16;
        tb[13 * 64] = ocp + id * 256 + lane * 4;
        tb[14 * 64] = k == 0 ? 0xffffu : k == 1 ? 0xffff0000u : 0u; tb[15 * 64] = k == 2 ? 0xffffu : k == 3 ? 0xffff0000u : 0u;
        tb[16 * 64] = smb + L_WRK + W * 512 + lane * 8;
        tb[17 * 64] = smb + L_WRK + ((lane & 32) + (lane & 2) * 8 + (lane & 1) * 8 + ((lane >> 2) & 7)) * 8;
        tb[18 * 64] = id * TU_P + W * 1024 + lane * 16;
        tb[20 * 64] = smb + L_MEM + lane * 16;
        tb[21 * 64] = lane == 0 ? O_FT + (32 + id) * 128 : OOB;
        tb[23 * 64] = (q == 0 ? O_MEM : q == 3 ? O_MEM + 32768 : O_MEMX + (q - 1) * 32768) + id * 256 + l15 * 16;
        for (int k = 60; k < 65; ++k) sst128((u4){FF, FF, FF, FF}, mksrd(a.UM, 28 * TU_P), lane * 16, id * TU_P + k * 4096 + W * 1024);
        const bool lead = id == 14 * xh;
        tb[22 * 64] = lane != 0 ? OOB : W == 2 ? (lead ? O_REC + xh * 8 : OOB) : id == 0 ? O_EPOCH : OOB;
        const int ord = ((W == 0 ? 0x6543210 : W == 1 ? 0x3106542 : W == 2 ? 0x4321065 : 0x6542103) >> (4 * (lane % 7))) & 7;
        wk = ((id * 16 + 4 * W + (lane / 7 & 3)) * 7 + ord) * 2048; fw = (4 * W + (lane & 3)) * 8 + 4 + (id >> 3);
        p1 = a.WvoF; p2 = a.UM; po = OFF_P;
    } else {
        const int h = id >> 1, half = id & 1, pt = 4 * W + q;
        tb[8 * 64] = O_MEMX + cp + lane * 16; tb[9 * 64] = O_MEMX + cp + lane * 16 + 4096;
        tb[10 * 64] = (W < 3 || (q & 1) == 0) ? RXW(half * 7 + 2 * W + (q & 1)) + (q >> 1) * 4096 + h * 256 + l15 * 16 : OOB;
        tb[11 * 64] = ((lane & 3) == 0 && half == 0) ? O_MZ + h * 128 + (lane >> 2) * 8 : OOB;
        tb[12 * 64] = smb + L_WRK + lane * 8;
        tb[13 * 64] = smb + L_WRK + ((q >> 1) * 32 + (lane & 1) * 16 + 4 * (q & 1) + (l15 >> 2)) * 8;
        tb[14 * 64] = pt < 14 ? RXW(half * 7 + (pt >> 1)) + (pt & 1) * 4096 + h * 256 + l15 * 16 : (pt == 14 && half == 0 && l15 < 8) ? O_MZ + h * 128 + l15 * 16 : OOB;
        tb[15 * 64] = O_FW + (h * 8 + i8) * 128;
        tb[16 * 64] = smb + L_MEM + lane * 16;
        tb[17 * 64] = smb + L_WRK + 1024 + W * 2048 + ((q >> 1) * 16 + perm16(l15)) * 16 + (q & 1) * 8;
        tb[18 * 64] = smb + L_WRK + 1024 + W * 2048 + (8 * q + (l15 >> 2) + 32 * ((lane >> 1) & 1)) * 16 + (lane & 1) * 8;
        const int otf = h * TU_P + half * 3584 + W * 1024 + (4 * hb + q) * 128 + i8 * 16;
        tb[19 * 64] = otf; tb[21 * 64] = hb ? OOB : otf;
        tb[20 * 64] = lane != 0 ? OOB : W == 3 ? O_RDY + (4 + id) * 128 : O_FT + id * 128;
        {
            const int xr = smb + L_XIM + (16 * W + l15) * 896, p0 = 28 * half + q, p1 = p0 + 4, sw = (l15 >> 1) & 7;
            tb[22 * 64] = smb + L_WRK + 1024 + (8 * q + (l15 >> 2)) * 16 + (lane & 1) * 8;
            tb[23 * 64] = xr + ((p0 & ~7) + ((p0 & 7) ^ sw)) * 16; tb[26 * 64] = xr + ((p1 & ~7) + ((p1 & 7) ^ sw)) * 16;
            tb[24 * 64] = q < 2 ? h * TU_P + TU_S + half * 1024 + (16 * W + l15) * 32 + q * 16 : OOB;
            if (W < 2) sst128((u4){FF, FF, FF, FF}, mksrd(a.TF, 16 * TU_P), lane * 16, h * TU_P + TU_S + half * 1024 + W * 2048);
            const SideCtx c = make_ctx(W);
            x_image(c, mksrd(a.X, 2048 * 896), 62, L_XIM); x_image(c, mksrd(a.X, 2048 * 896), 63, L_XIM + 28672);
        }
        if (W == 3)
            for (int k = 0; k < 3; ++k) tb[(17 + k) * 64] = RXW(half * 7 + 2 * k + (q >> 1)) + (q & 1) * 4096 + h * 256 + l15 * 16;
        wk = ((h * 28 + half * 14 + 4 * W) * 7 + (lane < 42 ? lane : 0)) * 2048; fw = h * 8 + (lane & 3);
        p1 = W == 3 ? a.GF : a.WqkF; x1 = h * GU_H; x2 = h * GU_H + (half * 14 + 4 * W) * 1024; po = OFF_X;
    }
    tb[0 * 64] = (lane & 8) ? 0xEEEE : 0x4444; tb[1 * 64] = lane * 16; tb[2 * 64] = wk; tb[3 * 64] = ((vb >> 3) * 16 + (vb & 3) * 4 + W) * 1280 + lane * 4;
    tb[4 * 64] = O_FW + fw * 128; tb[5 * 64] = O_RDY + (role == 1 && lane < 36 ? lane : lane & 3) * 128; tb[6 * 64] = role == 1 ? O_SIDE + (lane & 31) * 4 : O_WIN; tb[7 * 64] = O_REC;
    tb[27 * 64] = ((role * 32 + id) * 4 + W) * 8192 + lane * 128;
    edge(c1, n1);
    const unsigned f = 5900;
    const unsigned k0 = n1 * 16u / LAT_R;
    const unsigned sz1 = (role == 2 && W == 3) ? GU_SZ : 28 * 16 * 14336, sz2 = role == 1 ? 28 * TU_P : GU_SZ;
    unsigned sv;
    if (lane < 4) sv = srdw(a.HB, HB_SZ, lane); else if (lane < 8) sv = srdw(p1, sz1, lane - 4); else if (lane < 12) sv = srdw(p2, sz2, lane - 8); else if (lane < 16) sv = srdw(role == 2 ? a.TF : a.X, role == 2 ? 16 * TU_P : 2048 * 896, lane - 12);
    else if (lane < 20) sv = srdw(a.TS, 512 * 1280, lane - 16);
    else sv = lane == 20 ? k0 * LAT_R : lane == 21 ? LAT_R : lane == 24 ? po : lane == 26 ? tag : lane == 35 ? k0 & 1 : lane == 29 ? x1 : lane == 30 ? x2
            : lane == 33 ? (unsigned)TOL_C : lane == 34 ? smb + L_CLK : 0u;
    tb[31 * 64] = sv;
    if (W == 0 && lane < 2) *(u4 LAS*)(sm + L_CLK + lane * 16) = lane == 0 ? (u4){n1 * 16u, c1, f, 0u} : (u4){0u, 0u, 0u, 0u};
    bar();
    const unsigned tab = smb + L_TAB + W * 8192 + lane * 4;
    if (role == 0) { if (W == 0) RUN(S0); else if (W == 1) RUN(S1); else if (W == 2) RUN(S2); else RUN(S3); }
    else if (role == 1) { if (W == 0) RUN(P0); else if (W == 1) RUN(P1); else if (W == 2) RUN(P2); else RUN(P3); }
    else { if (W == 0) RUN(X0); else if (W == 1) RUN(X1); else if (W == 2) RUN(X2); else RUN(X3); }
}

#define XCC_XOR 1
#define HD(i) ((i) & 3)
#define SUB(i) ((i) >> 3)
#define PF_LO 0
#define PF_ROWS 864
#define PAO_AT 140
__global__ __launch_bounds__(256, 1) void fused_kernel(Args a_) {
    CArgs& a = get_args();
    const int wave = __builtin_amdgcn_readfirstlane(threadIdx.x >> 6);
    const u4 rhb = mksrd(a.HB, HB_SZ);
    unsigned ep = 0;
    if (wave == 0) ep = ld32(rhb, O_EPOCH, 0);
    else {
        u64 pc; unsigned len;
        asm volatile("s_getpc_b64 %0\nfk_code_lo:\n\ts_mov_b32 %1, fk_code_hi-fk_code_lo" : "=s"(pc), "=s"(len));
        const u4 rc = mksrd((const void*)pc, len & ~15u);
        const int vo = (((blockIdx.x >> 3) * 3 + wave - 1) * 2) * 1024 + (threadIdx.x & 63) * 16;
#pragma unroll
        for (int i = 0; i < 2; ++i) dma_one(rc, vo + i * 1024, 0, (unsigned)(size_t)(char LAS*)smem_ + 147456 + (wave * 3 + i) * 1024);
    }
    const int xcc = (__builtin_amdgcn_s_getreg((31 << 11) | 20) & 7) ^ XCC_XOR;
    int vb = xcc + (blockIdx.x & ~7);
    unsigned tag, best = 1016;
    if ((vb & 0xf5) == 0xf1 && wave) best = probe(rhb, ((vb >> 3) & 1) * 4 + wave, (vb >> 1) & 1);
    if ((vb & 0x85) == 0x80 && wave) {
#pragma nounroll
        for (int r = PF_LO + (((vb >> 3) & 15) * 2 + ((vb >> 1) & 1)) * 3 + wave - 1; r < PF_ROWS; r += 96) {
            const int u = 2 + (r >> 7);
            const u4 rs = r < 640 ? mksrd((r & 64) ? a.Wv : a.Wk, 448 * 8192) : mksrd(a.Au, 2048 * 896);
            asm volatile("s_mov_b32 m0, %3\n\ts_nop 4\n\tbuffer_load_dword %0, %1, %2 offen lds" :: "v"((threadIdx.x & 63) * 128), "s"(rs), "s"((r < 640 ? 64 * CHUNK(u) + (r & 63) : r - 640) * 8192), "s"((unsigned)(size_t)(char LAS*)smem_ + 140 * 1024) : "memory");
        }
    }
#pragma nounroll
    for (int pass = 0;; ++pass) {
        const bool far = vb & 4;
        if (far) w_first(wave, (vb >> 5) & 1, 4 * (vb & 3) + HD(vb >> 3), SUB(vb >> 3), 0); else waitvm<0>();
        unsigned slot = (unsigned)xcc * 32u + (blockIdx.x >> 3), old = 0;
        if (wave == 0 && !pass) {
            WAITV(16, "+v"(ep));
            ep += 0x9E3779B4u; old = ep;
            asm volatile("s_nop 4\n\tbuffer_atomic_swap %0, %1, %2, 0 offen sc0 sc1" : "+v"(old) : "v"(threadIdx.x == 0 ? O_CNT + slot * 128u : (unsigned)OOB), "s"(rhb) : "memory");
        }
        if (far) { w_first(wave, (vb >> 5) & 1, 4 * (vb & 3) + HD(vb >> 3), SUB(vb >> 3), 1); } else waitvm<0>();
        WAITV(16, "+v"(old));
        if (pass) { bar(); break; }
        if (threadIdx.x == 0) {
            for (int k = 0; k < 256 && old == ep; ++k) {
                slot = (slot + 1u) & 255u;
                old = __hip_atomic_exchange((unsigned*)(a.HB + O_CNT) + slot * 32u, ep, __ATOMIC_RELAXED, __HIP_MEMORY_SCOPE_AGENT);
            }
            *(u2 LAS*)(smem_ + 147456) = (u2){(slot >> 5) + 8u * (slot & 31u), ep};
        }
        bar();
        const int got = __builtin_amdgcn_readfirstlane(*(const unsigned LAS*)(smem_ + 147456));
        tag = __builtin_amdgcn_readfirstlane(*(const unsigned LAS*)(smem_ + 147460));
        if (got == vb) break;
        vb = got; best = 1016; waitvm<0>(); bar();
    }
    const int res = vb & 7, idx = vb >> 3;
    const int xq = res & 3;
    unsigned* const tick = (unsigned*)(a.HB + O_TICK) + xq * 32;
    if (res < 4) {
        const int xh = res >> 1;
        if (idx < 16 || ((res & 1) && idx < 30)) { chain_role(tag, vb, wave, (res & 1) ? idx >> 4 : 2, idx < 16 ? xh * 16 + idx : xh * 14 + idx - 16, xh); return; }
        if (res & 1) { ring_init(tag, wave, xh * 2 + (idx & 1), best); return; }
        if (wave == 0) sst32(tag + 1, rhb, fresh_lane() == 0 ? O_SIDE + (xh * 16 + idx - 16) * 4 : OOB, 0);
        const int sid = xh * 16 + idx - 16;
        parta_task(tag, vb, wave, (sid >> 1) * 7 + (sid & 1) * 4, 4 - (sid & 1));
        return;
    } else {
        if (idx == 0 && wave == 0) sst32(0u, rhb, fresh_lane() == 0 ? O_TICK + xq * 128 : OOB, 0);
        if (idx & 4) wgu_task<1>(tag, vb, wave, 4 * xq + HD(idx), SUB(idx)); else wgu_task<0>(tag, vb, wave, 4 * xq + HD(idx), SUB(idx));
        wait_prog(rhb, fresh_lane() == 0 ? O_FW + xq * 32 * 128 : OOB, tag + 1);
    }
    int round = 0;
#pragma nounroll
    for (; round < 256; ++round) {
        if (wave == 0 && fresh_lane() == 0) *(unsigned LAS*)(smem_ + 160 * 1024 - 16) = __hip_atomic_fetch_add(tick, 1u, __ATOMIC_RELAXED, __HIP_MEMORY_SCOPE_AGENT);
        bar();
        int k = __builtin_amdgcn_readfirstlane(*(const unsigned LAS*)(smem_ + 160 * 1024 - 16));
        if ((unsigned)k >= 190u) break;
        if (k >= PAO_AT && k < PAO_AT + 14) { pa_out(tag, wave, (k - PAO_AT) * 4 + xq); bar(); continue; }
        if (k >= PAO_AT) k -= 14;
        const int g = k < 162 ? (k + 14) / 11 : 16 + (k - 162) / 7, o = k < 162 ? (k + 14) % 11 : (k - 162) % 7;
        if (k < 8 || o >= 7) e_task(tag, wave, 4 * (k < 8 ? k >> 2 : g) + xq, k < 8 ? k & 3 : o - 7);
        else { x_task(tag, vb, wave, 4 * (g - 2) + xq, o); bar(); }
    }
}
__device__ __attribute__((used, noinline)) void fused_kernel_roles() { asm volatile("s_endpgm\n" ASM_ROLES "fk_code_hi:"); }

#define WS_SKIP 0
extern "C" void kernel_launch(void* const* d_in, const int* in_sizes, int n_in, void* d_out, int out_size, void* d_ws, size_t ws_size, hipStream_t stream) {
    Args a;
    a.Au = (const u16*)d_in[0]; a.AlogA = (const u16*)d_in[1]; a.Aim = (const u16*)d_in[2]; a.ACr = (const u16*)d_in[3]; a.ACi = (const u16*)d_in[4];
    a.Alogdt = (const u16*)d_in[5]; a.ADsk = (const u16*)d_in[6]; a.X = (const u16*)d_in[7]; a.mem0 = (const u16*)d_in[8];
    a.Wq = (const u16*)d_in[9]; a.Wk = (const u16*)d_in[10]; a.Wv = (const u16*)d_in[11]; a.Wo = (const u16*)d_in[12];
    a.out = (u16*)d_out;
    char* base = (char*)(((size_t)d_ws + WS_SKIP + (2u << 20) - 1) & ~(size_t)((2u << 20) - 1));
    a.HB = base;
    size_t off = 6u << 20;
    auto alloc = [&](size_t n) { char* p = base + off; off += (n + 4095) & ~(size_t)4095; return p; };
    a.GF = (u16*)alloc(GU_SZ); a.UF = (u16*)alloc(GU_SZ); a.TF = (u16*)alloc(16 * TU_P); a.UM = (u16*)alloc(28 * TU_P);
    a.WvoF = (u16*)alloc(28 * 16 * 14336); a.WqkF = (u16*)alloc(28 * 16 * 14336); a.EX = (u16*)alloc(64 * 16 * 2048); a.MZX = (float*)alloc(64 * 16 * 256); a.PA = (u16*)alloc(56 * 32768);
    a.TS = (unsigned*)alloc(768 * 1280);
    (void)hipFuncSetAttribute((const void*)fused_kernel, hipFuncAttributeMaxDynamicSharedMemorySize, 160 * 1024);
    fused_kernel<<<256, 256, 160 * 1024, stream>>>(a);
}
```
